```python
import torch

def _softmax(x, axis=-1):
    return torch.softmax(x, dim=axis)
def _gelu(x):
    return torch.nn.functional.gelu(x, approximate="tanh")
def _silu(x):
    return torch.nn.functional.silu(x)
def _sigmoid(x):
    return torch.sigmoid(x)
def _layernorm(x, axis=-1, eps=1e-5):
    m = x.mean(dim=axis, keepdim=True)
    v = x.var(dim=axis, keepdim=True, unbiased=False)
    return (x - m) / torch.sqrt(v + eps)
def _scatter_add(tgt, idx, src, axis=0):
    return tgt.clone().index_add_(axis, idx, src)
def _segment_sum(src, seg, n):
    out = torch.zeros((n,) + src.shape[1:], dtype=src.dtype)
    return out.index_add_(0, seg, src)
def _pad(x, pw):
    flat = tuple(v for pair in reversed(pw) for v in pair)
    return torch.nn.functional.pad(x, flat)

kernel_name = 'sequential_preLN__sliding_local_dilated__mixer_token_mlp__prompt'

def setup_inputs():

    _w = (lambda a: (lambda t: t.to(torch.bfloat16) if t.is_floating_point() else t)(torch.as_tensor(a)))
    def _A_setup():
        B, S, D = 1, 2048, 448
        return {
            "x": _w((lambda *s: torch.randn(*s, dtype=torch.bfloat16) * 0.05)(B, S, D)),
            "wqkv": _w((lambda *s: torch.randn(*s, dtype=torch.bfloat16) * 0.05)(D, 3 * D)),
            "wo": _w((lambda *s: torch.randn(*s, dtype=torch.bfloat16) * 0.05)(D, D)),
        }
    _A_in = _A_setup()

    def _B_setup():
        B, S, D, F = 1, 2048, 448, 1792
        return {
            "x": _w((lambda *s: torch.randn(*s, dtype=torch.bfloat16) * 0.05)(B, S, D)),
            "w1": _w((lambda *s: torch.randn(*s, dtype=torch.bfloat16) * 1.6)(S, F) * (S ** -0.5)),
            "b1": _w((lambda *s: torch.randn(*s, dtype=torch.bfloat16) * 12.8)(F) * 0.02),
            "w2": _w((lambda *s: torch.randn(*s, dtype=torch.bfloat16) * 0.4)(F, S) * (F ** -0.5)),
            "b2": _w((lambda *s: torch.randn(*s, dtype=torch.bfloat16) * 6.4)(S) * 0.02),
        }
    _B_in = _B_setup()

    return {
        "A_x": _A_in["x"],
        "A_wqkv": _A_in["wqkv"],
        "A_wo": _A_in["wo"],
        "B_x": _B_in["x"],
        "B_w1": _B_in["w1"],
        "B_b1": _B_in["b1"],
        "B_w2": _B_in["w2"],
        "B_b2": _B_in["b2"],
    }

def reference(A_x, A_wqkv, A_wo, B_x, B_w1, B_b1, B_w2, B_b2):
    def _A_ref(x, wqkv, wo):
        B, S, D = x.shape
        H = 16; Dh = D // H
        W, dil = 8, 2
        qkv = (lambda x, *d: x.permute(*d))((x @ wqkv).reshape(B, S, 3, H, Dh), 2, 0, 3, 1, 4)
        q, k, v = qkv[0], qkv[1], qkv[2]
        scale = Dh ** -0.5
        pos = torch.arange(S)
        off = torch.arange(W) * dil
        idx = pos[:, None] - off[None, :]
        mask = idx >= 0
        idx_c = torch.where(mask, idx, 0).reshape(-1)
        kg = k[:, :, idx_c, :].reshape(B, H, S, W, Dh)
        vg = v[:, :, idx_c, :].reshape(B, H, S, W, Dh)
        logits = torch.einsum("bhsd,bhswd->bhsw", q, kg) * scale
        logits = torch.where(mask[None, None, :, :], logits, -1e9)
        attn = _softmax(logits, axis=-1)
        o = torch.einsum("bhsw,bhswd->bhsd", attn, vg)
        return (lambda x, *d: x.permute(*d))(o, 0, 2, 1, 3).reshape(B, S, D) @ wo

    def _B_ref(x, w1, b1, w2, b2):
        B, S, D = x.shape
        h = _layernorm(x, axis=-1)
        u = torch.einsum("bsd,sf->bdf", h, w1) + b1
        u = _gelu(u)
        y = torch.einsum("bdf,fs->bds", u, w2) + b2
        return (lambda x, *d: x.permute(*d))(y, 0, 2, 1)

    x = A_x
    h = x + _A_ref(_layernorm(x, axis=-1), A_wqkv, A_wo)
    h2 = h + _B_ref(_layernorm(h, axis=-1), B_w1, B_b1, B_w2, B_b2)
    return h2
```

```cpp
#include <hip/hip_runtime.h>
#include <stdint.h>
typedef unsigned short u16;
__global__ __launch_bounds__(512) void fused_block(const u16* x, const u16* wqkv, const u16* wo, const u16* w1, const u16* b1,
                                                   const u16* w2, const u16* b2, u16* out, char* ws) {
#if defined(__HIP_DEVICE_COMPILE__)
  unsigned long long ka = (unsigned long long)__builtin_amdgcn_kernarg_segment_ptr();
  unsigned tid = threadIdx.x;
  asm volatile(
      "s_load_dwordx16 s[4:19], s[0:1], 0x0\n\t"
      "s_load_dwordx2 s[20:21], s[0:1], 0x40\n\t"
      "v_lshrrev_b32_e32 v199, 6, v0\n\t"
      "v_and_b32_e32 v192, 63, v0\n\t"
      "v_readfirstlane_b32 s24, v199\n\t"
      "s_getpc_b64 s[58:59]\n\t"
      "s_lshr_b32 s34, s2, 3\n\t"
      "v_min_u32_e32 v200, 8, v0\n\t"
      "v_lshl_add_u32 v200, v200, 5, s34\n\t"
      "s_mov_b32 m0, 64512\n\t"
      "v_min_u32_e32 v200, 277, v200\n\t"
      "v_lshlrev_b32_e32 v200, 7, v200\n\t"
      "s_cmp_lg_u32 s24, 0\n\t"
      "s_cbranch_scc1 NTC_%=\n\t"
      "global_load_lds_dword v200, s[58:59]\n\t"
      "NTC_%=:\n\t"
      "s_and_b32 s34, s2, 7\n\t"
      "s_lshr_b32 s35, s2, 6\n\t"
      "s_lshl2_add_u32 s25, s34, s35\n\t"
      "s_bfe_u32 s26, s2, 196611\n\t"
      "s_and_b32 s34, s25, 30\n\t"
      "s_lshl_b32 s34, s34, 6\n\t"
      "s_and_b32 s35, s25, 1\n\t"
      "s_add_u32 s38, s34, s35\n\t"
      "v_lshrrev_b32_e32 v197, 3, v192\n\t"
      "v_and_b32_e32 v198, 7, v192\n\t"
      "s_lshr_b32 s3, s2, 1\n\t"
      "s_and_b32 s3, s3, 0\n\t"
      "s_xor_b32 s3, s3, s24\n\t"
      "s_lshl4_add_u32 s34, s3, s38\n\t"
      "s_mul_i32 s34, s34, 896\n\t"
      "s_movk_i32 s35, 1792\n\t"
      "v_lshl_add_u32 v199, v198, 4, s34\n\t"
      "v_mad_u32_u24 v120, v197, s35, v199\n\t"
      "s_min_u32 s74, s26, 6\n\t"
      "s_lshl_b32 s34, s74, 7\n\t"
      "s_add_u32 s35, s34, 768\n\t"
      "s_sub_u32 s36, s35, 896\n\t"
      "s_min_u32 s35, s35, s36\n\t"
      "v_add_u32_e32 v126, s35, v120\n\t"
      "s_add_u32 s35, s34, 640\n\t"
      "s_sub_u32 s36, s35, 896\n\t"
      "s_min_u32 s35, s35, s36\n\t"
      "v_add_u32_e32 v125, s35, v120\n\t"
      "s_add_u32 s35, s34, 512\n\t"
      "s_sub_u32 s36, s35, 896\n\t"
      "s_min_u32 s35, s35, s36\n\t"
      "v_add_u32_e32 v124, s35, v120\n\t"
      "s_add_u32 s35, s34, 384\n\t"
      "s_sub_u32 s36, s35, 896\n\t"
      "s_min_u32 s35, s35, s36\n\t"
      "v_add_u32_e32 v123, s35, v120\n\t"
      "s_add_u32 s35, s34, 256\n\t"
      "s_sub_u32 s36, s35, 896\n\t"
      "s_min_u32 s35, s35, s36\n\t"
      "v_add_u32_e32 v122, s35, v120\n\t"
      "s_add_u32 s35, s34, 128\n\t"
      "s_sub_u32 s36, s35, 896\n\t"
      "s_min_u32 s35, s35, s36\n\t"
      "v_add_u32_e32 v121, s35, v120\n\t"
      "s_add_u32 s35, s34, 0\n\t"
      "s_sub_u32 s36, s35, 896\n\t"
      "s_min_u32 s35, s35, s36\n\t"
      "v_add_u32_e32 v120, s35, v120\n\t"
      "s_waitcnt lgkmcnt(0)\n\t"
      "global_load_dwordx4 v[0:3], v120, s[4:5]\n\t"
      "global_load_dwordx4 v[4:7], v121, s[4:5]\n\t"
      "global_load_dwordx4 v[8:11], v122, s[4:5]\n\t"
      "global_load_dwordx4 v[12:15], v123, s[4:5]\n\t"
      "global_load_dwordx4 v[16:19], v124, s[4:5]\n\t"
      "global_load_dwordx4 v[20:23], v125, s[4:5]\n\t"
      "s_cmp_lt_u32 s25, 2\n\t"
      "s_cselect_b32 s34, 0, 16\n\t"
      "s_sub_u32 s34, s38, s34\n\t"
      "s_lshl1_add_u32 s34, s24, s34\n\t"
      "s_mul_i32 s34, s34, 896\n\t"
      "s_and_b32 s33, s25, 3\n\t"
      "s_lshl_b32 s33, s33, 1\n\t"
      "v_min_u32_e32 v200, 55, v192\n\t"
      "s_lshl_b32 s35, s33, 3\n\t"
      "v_add_u32_e32 v201, s35, v200\n\t"
      "v_subrev_u32_e32 v202, 56, v201\n\t"
      "v_min_u32_e32 v201, v201, v202\n\t"
      "v_lshl_add_u32 v127, v201, 4, s34\n\t"
      "s_min_u32 s28, s26, 6\n\t"
      "s_and_b32 s39, s24, 3\n\t"
      "s_lshl_b32 s39, s39, 12\n\t"
      "s_cmp_lt_u32 s24, 4\n\t"
      "s_cselect_b32 s27, 4, 0\n\t"
      "v_lshrrev_b32_e32 v193, 4, v192\n\t"
      "v_and_b32_e32 v194, 15, v192\n\t"
      "v_bfe_u32 v195, v192, 2, 2\n\t"
      "v_and_b32_e32 v196, 3, v192\n\t"
      "v_lshlrev_b32_e32 v240, 4, v192\n\t"
      "s_lshr_b32 s34, s3, 1\n\t"
      "s_mul_i32 s34, s34, 14336\n\t"
      "s_and_b32 s35, s3, 1\n\t"
      "s_lshl_b32 s35, s35, 7\n\t"
      "s_add_u32 s34, s34, s35\n\t"
      "v_xor_b32_e32 v199, v197, v198\n\t"
      "v_lshl_add_u32 v199, v199, 4, s34\n\t"
      "v_lshl_add_u32 v199, v198, 8, v199\n\t"
      "s_sub_u32 s35, s74, s33\n\t"
      "s_cselect_b32 s36, 7, 0\n\t"
      "s_add_u32 s35, s35, s36\n\t"
      "s_lshl_b32 s35, s35, 11\n\t"
      "s_add_u32 s34, s35, 0\n\t"
      "s_sub_u32 s36, s34, 14336\n\t"
      "s_min_u32 s34, s34, s36\n\t"
      "v_add_u32_e32 v112, s34, v199\n\t"
      "s_add_u32 s34, s35, 2048\n\t"
      "s_sub_u32 s36, s34, 14336\n\t"
      "s_min_u32 s34, s34, s36\n\t"
      "v_add_u32_e32 v113, s34, v199\n\t"
      "s_add_u32 s34, s35, 4096\n\t"
      "s_sub_u32 s36, s34, 14336\n\t"
      "s_min_u32 s34, s34, s36\n\t"
      "v_add_u32_e32 v114, s34, v199\n\t"
      "s_add_u32 s34, s35, 6144\n\t"
      "s_sub_u32 s36, s34, 14336\n\t"
      "s_min_u32 s34, s34, s36\n\t"
      "v_add_u32_e32 v115, s34, v199\n\t"
      "s_add_u32 s34, s35, 8192\n\t"
      "s_sub_u32 s36, s34, 14336\n\t"
      "s_min_u32 s34, s34, s36\n\t"
      "v_add_u32_e32 v116, s34, v199\n\t"
      "s_add_u32 s34, s35, 10240\n\t"
      "s_sub_u32 s36, s34, 14336\n\t"
      "s_min_u32 s34, s34, s36\n\t"
      "v_add_u32_e32 v117, s34, v199\n\t"
      "s_add_u32 s34, s35, 12288\n\t"
      "s_sub_u32 s36, s34, 14336\n\t"
      "s_min_u32 s34, s34, s36\n\t"
      "v_add_u32_e32 v118, s34, v199\n\t"
      "v_min_u32_e32 v200, 55, v192\n\t"
      "v_lshrrev_b32_e32 v201, 2, v200\n\t"
      "v_lshlrev_b32_e32 v119, 10, v201\n\t"
      "v_cmp_lt_u32_e32 vcc, 6, v201\n\t"
      "v_mov_b32_e32 v202, 57344\n\t"
      "v_mov_b32_e32 v201, 50688\n\t"
      "v_cndmask_b32_e32 v202, v202, v201, vcc\n\t"
      "v_and_b32_e32 v201, 3, v200\n\t"
      "v_lshl_add_u32 v202, v201, 7, v202\n\t"
      "v_and_b32_e32 v200, 7, v200\n\t"
      "v_xor_b32_e32 v200, s24, v200\n\t"
      "v_lshl_add_u32 v202, v200, 4, v202\n\t"
      "v_add_u32_e32 v119, v119, v202\n\t"
      "s_bitcmp1_b32 s27, 2\n\t"
      "s_cbranch_scc1 LSET_%=\n\t"
      "v_xor_b32_e32 v199, v194, v193\n\t"
      "v_lshlrev_b32_e32 v199, 4, v199\n\t"
      "v_lshl_or_b32 v200, v193, 8, v199\n\t"
      "s_and_b32 s34, s24, 1\n\t"
      "s_mul_i32 s35, s34, 28672\n\t"
      "v_add_u32_e32 v160, s35, v200\n\t"
      "v_xor_b32_e32 v161, 64, v160\n\t"
      "v_and_b32_e32 v199, 0x70, v199\n\t"
      "v_lshl_or_b32 v199, v193, 7, v199\n\t"
      "v_add_u32_e32 v199, 57344, v199\n\t"
      "v_add_u32_e32 v200, 14336, v200\n\t"
      "s_cmp_eq_u32 s34, 1\n\t"
      "s_cselect_b64 vcc, -1, 0\n\t"
      "s_cselect_b32 s35, 0, 6656\n\t"
      "v_cndmask_b32_e32 v162, v199, v200, vcc\n\t"
      "v_xor_b32_e32 v163, 64, v162\n\t"
      "v_subrev_u32_e32 v164, s35, v162\n\t"
      "v_subrev_u32_e32 v165, s35, v163\n\t"
      "s_bfe_u32 s35, s24, 65537\n\t"
      "s_mul_i32 s35, s35, 7\n\t"
      "v_and_b32_e32 v201, 24, v192\n\t"
      "v_lshlrev_b32_e32 v201, 2, v201\n\t"
      "v_lshlrev_b32_e32 v202, 7, v195\n\t"
      "v_lshl_or_b32 v202, v193, 12, v202\n\t"
      "v_add_u32_e32 v202, 65536, v202\n\t"
      "v_add_lshl_u32 v199, v196, s35, 3\n\t"
      "v_xor_b32_e32 v199, v199, v201\n\t"
      "v_add_u32_e32 v166, v199, v202\n\t"
      "v_min_u32_e32 v199, 2, v196\n\t"
      "s_add_u32 s35, s35, 4\n\t"
      "v_add_lshl_u32 v199, v199, s35, 3\n\t"
      "v_xor_b32_e32 v199, v199, v201\n\t"
      "v_add_u32_e32 v167, v199, v202\n\t"
      "v_add_u32_e32 v168, 0x10000, v166\n\t"
      "v_add_u32_e32 v169, 0x10000, v167\n\t"
      "v_lshlrev_b32_e32 v199, 11, v193\n\t"
      "v_sub_u32_e32 v170, v166, v199\n\t"
      "v_sub_u32_e32 v171, v167, v199\n\t"
      "s_branch NORM_%=\n\t"
      "LSET_%=:\n\t"
      "s_add_u32 s60, s20, 16777216\n\t"
      "s_addc_u32 s61, s21, 0\n\t"
      "s_load_dwordx2 s[22:23], s[60:61], 0x0\n\t"
      "s_lshl_b32 s34, s24, 3\n\t"
      "v_add_u32_e32 v199, s34, v197\n\t"
      "s_and_b32 s34, s24, 1\n\t"
      "s_lshl_b32 s34, s34, 2\n\t"
      "v_and_b32_e32 v200, 2, v197\n\t"
      "v_or_b32_e32 v200, s34, v200\n\t"
      "v_xor_b32_e32 v200, v200, v198\n\t"
      "v_min_u32_e32 v200, 6, v200\n\t"
      "v_lshlrev_b32_e32 v200, 4, v200\n\t"
      "s_movk_i32 s34, 2688\n\t"
      "v_mad_u32_u24 v149, v199, s34, v200\n\t"
      "v_add_u32_e32 v148, 128, v149\n\t"
      "v_add_u32_e32 v150, 84096, v149\n\t"
      "v_add_u32_e32 v151, 83968, v149\n\t"
      "v_add_u32_e32 v152, 85120, v149\n\t"
      "s_sub_u32 s6, s6, 128\n\t"
      "s_subb_u32 s7, s7, 0\n\t"
      "s_mul_i32 s37, s26, 112\n\t"
      "s_mov_b32 s81, s33\n\t"
      "s_lshr_b32 s35, s2, 6\n\t"
      "s_lshl2_add_u32 s31, s26, s35\n\t"
      "s_and_b32 s35, s2, 7\n\t"
      "s_mul_i32 s34, s35, 14\n\t"
      "s_lshl4_add_u32 s35, s35, -14\n\t"
      "s_cmp_lt_u32 s31, 14\n\t"
      "s_cselect_b32 s34, s34, s35\n\t"
      "s_cselect_b32 s36, 1, 0\n\t"
      "s_or_b32 s27, s27, s36\n\t"
      "s_add_u32 s34, s34, s31\n\t"
      "s_lshr_b32 s29, s34, 2\n\t"
      "s_and_b32 s30, s34, 3\n\t"
      "s_mul_i32 s30, s30, 7\n\t"
      "v_and_b32_e32 v199, 60, v192\n\t"
      "v_and_b32_e32 v200, 24, v192\n\t"
      "v_lshlrev_b32_e32 v202, 3, v196\n\t"
      "v_lshl_or_b32 v202, v200, 2, v202\n\t"
      "v_lshl_or_b32 v202, v195, 7, v202\n\t"
      "v_lshl_or_b32 v202, v193, 10, v202\n\t"
      "v_add_u32_e32 v224, s39, v202\n\t"
      "v_xor_b32_e32 v225, 32, v224\n\t"
      "v_xor_b32_e32 v226, 64, v224\n\t"
      "v_xor_b32_e32 v227, 96, v224\n\t"
      "s_lshl_b32 s34, s24, 12\n\t"
      "s_add_u32 s34, s34, 98304\n\t"
      "v_and_b32_e32 v200, 16, v192\n\t"
      "v_lshlrev_b32_e32 v200, 3, v200\n\t"
      "v_xor_b32_e32 v241, v240, v200\n\t"
      "v_add_u32_e32 v241, s34, v241\n\t"
      "s_and_b32 s35, s24, 1\n\t"
      "s_lshl_b32 s35, s35, 7\n\t"
      "v_lshlrev_b32_e32 v199, 2, v199\n\t"
      "s_lshl_b32 s34, s24, 8\n\t"
      "v_add_u32_e32 v243, s34, v199\n\t"
      "v_xor_b32_e32 v199, s35, v199\n\t"
      "v_and_b32_e32 v200, 2, v192\n\t"
      "v_lshl_or_b32 v199, v200, 9, v199\n\t"
      "v_and_b32_e32 v202, 1, v192\n\t"
      "v_lshl_or_b32 v199, v202, 3, v199\n\t"
      "s_and_b32 s35, s34, 0x400\n\t"
      "s_add_u32 s34, s34, s35\n\t"
      "s_add_u32 s34, s34, 98304\n\t"
      "v_add_u32_e32 v242, s34, v199\n\t"
      "v_lshlrev_b32_e32 v199, 2, v202\n\t"
      "v_lshl_or_b32 v199, v200, 4, v199\n\t"
      "s_min_u32 s34, s29, 27\n\t"
      "s_lshl_b32 s34, s34, 7\n\t"
      "s_lshr_b32 s35, s24, 2\n\t"
      "s_lshl_b32 s35, s35, 6\n\t"
      "s_add_u32 s34, s34, s35\n\t"
      "s_lshr_b32 s35, s39, 9\n\t"
      "s_add_u32 s34, s34, s35\n\t"
      "v_add_u32_e32 v244, s34, v199\n\t"
      "s_add_u32 s48, s20, 8388608\n\t"
      "s_addc_u32 s49, s21, 0\n\t"
      "v_and_b32_e32 v199, 2, v197\n\t"
      "v_xor_b32_e32 v199, v199, v198\n\t"
      "v_lshlrev_b32_e32 v199, 4, v199\n\t"
      "s_movk_i32 s34, 7168\n\t"
      "v_mad_u32_u24 v144, v197, s34, v199\n\t"
      "v_xor_b32_e32 v199, 64, v144\n\t"
      "v_add_u32_e32 v145, 56320, v199\n\t"
      "v_add_u32_e32 v146, 112640, v144\n\t"
      "v_add_u32_e32 v147, 168960, v199\n\t"
      "v_and_b32_e32 v199, 6, v197\n\t"
      "v_xor_b32_e32 v199, v199, v198\n\t"
      "v_lshlrev_b32_e32 v199, 4, v199\n\t"
      "s_movk_i32 s35, 896\n\t"
      "v_mad_u32_u24 v154, v197, s35, v199\n\t"
      "v_add_u32_e32 v155, 6144, v154\n\t"
      "v_add_u32_e32 v156, 12288, v154\n\t"
      "v_add_u32_e32 v157, 18432, v154\n\t"
      "s_mul_i32 s34, s24, 917504\n\t"
      "s_lshl_b32 s35, s29, 7\n\t"
      "s_add_u32 s34, s34, s35\n\t"
      "s_add_u32 s52, s10, s34\n\t"
      "s_addc_u32 s53, s11, 0\n\t"
      "s_lshl_b32 s34, s28, 7\n\t"
      "s_add_u32 s56, s8, s34\n\t"
      "s_addc_u32 s57, s9, 0\n\t"
      "s_mov_b32 s34, 0\n\t"
      "s_and_b32 s35, s34, 1\n\t"
      "s_and_b32 s34, s34, 2\n\t"
      "s_lshl_b32 s34, s34, 6\n\t"
      "s_add_u32 s34, s34, s35\n\t"
      "s_mul_i32 s76, s34, 3584\n\t"
      "s_mov_b32 s34, 1\n\t"
      "s_and_b32 s35, s34, 1\n\t"
      "s_and_b32 s34, s34, 2\n\t"
      "s_lshl_b32 s34, s34, 6\n\t"
      "s_add_u32 s34, s34, s35\n\t"
      "s_mul_i32 s77, s34, 3584\n\t"
      "s_mov_b32 s34, 2\n\t"
      "s_and_b32 s35, s34, 1\n\t"
      "s_and_b32 s34, s34, 2\n\t"
      "s_lshl_b32 s34, s34, 6\n\t"
      "s_add_u32 s34, s34, s35\n\t"
      "s_mul_i32 s78, s34, 3584\n\t"
      "s_mov_b32 s34, 3\n\t"
      "s_and_b32 s35, s34, 1\n\t"
      "NORM_%=:\n\t"
      "global_load_dwordx4 v[24:27], v126, s[4:5]\n\t"
      "global_load_dwordx4 v[28:31], v127, s[4:5]\n\t"
      "v_mov_b32_e32 v199, 0x3f803f80\n\t"
      "s_waitcnt vmcnt(7)\n\t"
      "v_dot2_f32_bf16 v88, v0, v199, 0\n\t"
      "v_dot2_f32_bf16 v92, v0, v0, 0\n\t"
      "v_lshlrev_b32_e32 v32, 16, v0\n\t"
      "v_and_b32_e32 v33, 0xffff0000, v0\n\t"
      "v_dot2_f32_bf16 v89, v1, v199, 0\n\t"
      "v_dot2_f32_bf16 v93, v1, v1, 0\n\t"
      "v_lshlrev_b32_e32 v34, 16, v1\n\t"
      "v_and_b32_e32 v35, 0xffff0000, v1\n\t"
      "v_dot2_f32_bf16 v90, v2, v199, 0\n\t"
      "v_dot2_f32_bf16 v94, v2, v2, 0\n\t"
      "v_lshlrev_b32_e32 v36, 16, v2\n\t"
      "v_and_b32_e32 v37, 0xffff0000, v2\n\t"
      "v_dot2_f32_bf16 v91, v3, v199, 0\n\t"
      "v_dot2_f32_bf16 v95, v3, v3, 0\n\t"
      "v_lshlrev_b32_e32 v38, 16, v3\n\t"
      "v_and_b32_e32 v39, 0xffff0000, v3\n\t"
      "s_waitcnt vmcnt(6)\n\t"
      "v_dot2_f32_bf16 v88, v4, v199, v88\n\t"
      "v_dot2_f32_bf16 v92, v4, v4, v92\n\t"
      "v_lshlrev_b32_e32 v40, 16, v4\n\t"
      "v_and_b32_e32 v41, 0xffff0000, v4\n\t"
      "v_dot2_f32_bf16 v89, v5, v199, v89\n\t"
      "v_dot2_f32_bf16 v93, v5, v5, v93\n\t"
      "v_lshlrev_b32_e32 v42, 16, v5\n\t"
      "v_and_b32_e32 v43, 0xffff0000, v5\n\t"
      "v_dot2_f32_bf16 v90, v6, v199, v90\n\t"
      "v_dot2_f32_bf16 v94, v6, v6, v94\n\t"
      "v_lshlrev_b32_e32 v44, 16, v6\n\t"
      "v_and_b32_e32 v45, 0xffff0000, v6\n\t"
      "v_dot2_f32_bf16 v91, v7, v199, v91\n\t"
      "v_dot2_f32_bf16 v95, v7, v7, v95\n\t"
      "v_lshlrev_b32_e32 v46, 16, v7\n\t"
      "v_and_b32_e32 v47, 0xffff0000, v7\n\t"
      "s_waitcnt vmcnt(5)\n\t"
      "v_dot2_f32_bf16 v88, v8, v199, v88\n\t"
      "v_dot2_f32_bf16 v92, v8, v8, v92\n\t"
      "v_lshlrev_b32_e32 v48, 16, v8\n\t"
      "v_and_b32_e32 v49, 0xffff0000, v8\n\t"
      "v_dot2_f32_bf16 v89, v9, v199, v89\n\t"
      "v_dot2_f32_bf16 v93, v9, v9, v93\n\t"
      "v_lshlrev_b32_e32 v50, 16, v9\n\t"
      "v_and_b32_e32 v51, 0xffff0000, v9\n\t"
      "v_dot2_f32_bf16 v90, v10, v199, v90\n\t"
      "v_dot2_f32_bf16 v94, v10, v10, v94\n\t"
      "v_lshlrev_b32_e32 v52, 16, v10\n\t"
      "v_and_b32_e32 v53, 0xffff0000, v10\n\t"
      "v_dot2_f32_bf16 v91, v11, v199, v91\n\t"
      "v_dot2_f32_bf16 v95, v11, v11, v95\n\t"
      "v_lshlrev_b32_e32 v54, 16, v11\n\t"
      "v_and_b32_e32 v55, 0xffff0000, v11\n\t"
      "s_waitcnt vmcnt(4)\n\t"
      "v_dot2_f32_bf16 v88, v12, v199, v88\n\t"
      "v_dot2_f32_bf16 v92, v12, v12, v92\n\t"
      "v_lshlrev_b32_e32 v56, 16, v12\n\t"
      "v_and_b32_e32 v57, 0xffff0000, v12\n\t"
      "v_dot2_f32_bf16 v89, v13, v199, v89\n\t"
      "v_dot2_f32_bf16 v93, v13, v13, v93\n\t"
      "v_lshlrev_b32_e32 v58, 16, v13\n\t"
      "v_and_b32_e32 v59, 0xffff0000, v13\n\t"
      "v_dot2_f32_bf16 v90, v14, v199, v90\n\t"
      "v_dot2_f32_bf16 v94, v14, v14, v94\n\t"
      "v_lshlrev_b32_e32 v60, 16, v14\n\t"
      "v_and_b32_e32 v61, 0xffff0000, v14\n\t"
      "v_dot2_f32_bf16 v91, v15, v199, v91\n\t"
      "v_dot2_f32_bf16 v95, v15, v15, v95\n\t"
      "v_lshlrev_b32_e32 v62, 16, v15\n\t"
      "v_and_b32_e32 v63, 0xffff0000, v15\n\t"
      "s_waitcnt vmcnt(3)\n\t"
      "v_dot2_f32_bf16 v88, v16, v199, v88\n\t"
      "v_dot2_f32_bf16 v92, v16, v16, v92\n\t"
      "v_lshlrev_b32_e32 v64, 16, v16\n\t"
      "v_and_b32_e32 v65, 0xffff0000, v16\n\t"
      "v_dot2_f32_bf16 v89, v17, v199, v89\n\t"
      "v_dot2_f32_bf16 v93, v17, v17, v93\n\t"
      "v_lshlrev_b32_e32 v66, 16, v17\n\t"
      "v_and_b32_e32 v67, 0xffff0000, v17\n\t"
      "v_dot2_f32_bf16 v90, v18, v199, v90\n\t"
      "v_dot2_f32_bf16 v94, v18, v18, v94\n\t"
      "v_lshlrev_b32_e32 v68, 16, v18\n\t"
      "v_and_b32_e32 v69, 0xffff0000, v18\n\t"
      "v_dot2_f32_bf16 v91, v19, v199, v91\n\t"
      "v_dot2_f32_bf16 v95, v19, v19, v95\n\t"
      "v_lshlrev_b32_e32 v70, 16, v19\n\t"
      "v_and_b32_e32 v71, 0xffff0000, v19\n\t"
      "s_waitcnt vmcnt(2)\n\t"
      "v_dot2_f32_bf16 v88, v20, v199, v88\n\t"
      "v_dot2_f32_bf16 v92, v20, v20, v92\n\t"
      "v_lshlrev_b32_e32 v72, 16, v20\n\t"
      "v_and_b32_e32 v73, 0xffff0000, v20\n\t"
      "v_dot2_f32_bf16 v89, v21, v199, v89\n\t"
      "v_dot2_f32_bf16 v93, v21, v21, v93\n\t"
      "v_lshlrev_b32_e32 v74, 16, v21\n\t"
      "v_and_b32_e32 v75, 0xffff0000, v21\n\t"
      "v_dot2_f32_bf16 v90, v22, v199, v90\n\t"
      "v_dot2_f32_bf16 v94, v22, v22, v94\n\t"
      "v_lshlrev_b32_e32 v76, 16, v22\n\t"
      "v_and_b32_e32 v77, 0xffff0000, v22\n\t"
      "v_dot2_f32_bf16 v91, v23, v199, v91\n\t"
      "v_dot2_f32_bf16 v95, v23, v23, v95\n\t"
      "v_lshlrev_b32_e32 v78, 16, v23\n\t"
      "v_and_b32_e32 v79, 0xffff0000, v23\n\t"
      "s_waitcnt vmcnt(1)\n\t"
      "v_dot2_f32_bf16 v88, v24, v199, v88\n\t"
      "v_dot2_f32_bf16 v92, v24, v24, v92\n\t"
      "v_lshlrev_b32_e32 v80, 16, v24\n\t"
      "v_and_b32_e32 v81, 0xffff0000, v24\n\t"
      "v_dot2_f32_bf16 v89, v25, v199, v89\n\t"
      "v_dot2_f32_bf16 v93, v25, v25, v93\n\t"
      "v_lshlrev_b32_e32 v82, 16, v25\n\t"
      "v_and_b32_e32 v83, 0xffff0000, v25\n\t"
      "v_dot2_f32_bf16 v90, v26, v199, v90\n\t"
      "v_dot2_f32_bf16 v94, v26, v26, v94\n\t"
      "v_lshlrev_b32_e32 v84, 16, v26\n\t"
      "v_and_b32_e32 v85, 0xffff0000, v26\n\t"
      "v_dot2_f32_bf16 v91, v27, v199, v91\n\t"
      "v_dot2_f32_bf16 v95, v27, v27, v95\n\t"
      "v_lshlrev_b32_e32 v86, 16, v27\n\t"
      "v_and_b32_e32 v87, 0xffff0000, v27\n\t"
      "s_waitcnt vmcnt(0)\n\t"
      "s_bitcmp0_b32 s27, 2\n\t"
      "s_cbranch_scc1 SK1_%=\n\t"
      "s_mul_i32 s72, s81, 172032\n\t"
      "s_add_u32 s72, s72, s37\n\t"
      "s_add_u32 s40, s6, s72\n\t"
      "s_addc_u32 s41, s7, 0\n\t"
      "s_add_u32 s81, s81, 1\n\t"
      "s_cmp_eq_u32 s81, 7\n\t"
      "s_cselect_b32 s81, 0, s81\n\t"
      "s_add_u32 m0, s39, 131072\n\t"
      "s_nop 0\n\t"
      "global_load_lds_dwordx4 v148, s[40:41]\n\t"
      "global_load_lds_dwordx4 v149, s[40:41] offset:1024\n\t"
      "global_load_lds_dwordx4 v150, s[40:41] offset:2048\n\t"
      "global_load_lds_dwordx4 v151, s[40:41] offset:3072\n\t"
      "SK1_%=:\n\t"
      "v_dot2_f32_bf16 v96, v28, v199, 0\n\t"
      "v_dot2_f32_bf16 v97, v28, v28, 0\n\t"
      "v_dot2_f32_bf16 v96, v29, v199, v96\n\t"
      "v_dot2_f32_bf16 v97, v29, v29, v97\n\t"
      "v_dot2_f32_bf16 v96, v30, v199, v96\n\t"
      "v_dot2_f32_bf16 v97, v30, v30, v97\n\t"
      "v_dot2_f32_bf16 v96, v31, v199, v96\n\t"
      "v_dot2_f32_bf16 v97, v31, v31, v97\n\t"
      "v_cmp_gt_u32_e32 vcc, 56, v192\n\t"
      "v_pk_add_f32 v[88:89], v[88:89], v[90:91]\n\t"
      "v_pk_add_f32 v[92:93], v[92:93], v[94:95]\n\t"
      "v_cndmask_b32_e32 v96, 0, v96, vcc\n\t"
      "v_cndmask_b32_e32 v97, 0, v97, vcc\n\t"
      "v_add_f32_e32 v88, v88, v89\n\t"
      "v_add_f32_e32 v92, v92, v93\n\t"
      "v_add_f32_dpp v96, v96, v96 quad_perm:[1,0,3,2] row_mask:0xf bank_mask:0xf\n\t"
      "v_add_f32_dpp v97, v97, v97 quad_perm:[1,0,3,2] row_mask:0xf bank_mask:0xf\n\t"
      "v_add_f32_dpp v88, v88, v88 quad_perm:[1,0,3,2] row_mask:0xf bank_mask:0xf\n\t"
      "v_add_f32_dpp v92, v92, v92 quad_perm:[1,0,3,2] row_mask:0xf bank_mask:0xf\n\t"
      "v_add_f32_dpp v96, v96, v96 quad_perm:[2,3,0,1] row_mask:0xf bank_mask:0xf\n\t"
      "v_add_f32_dpp v97, v97, v97 quad_perm:[2,3,0,1] row_mask:0xf bank_mask:0xf\n\t"
      "v_add_f32_dpp v88, v88, v88 quad_perm:[2,3,0,1] row_mask:0xf bank_mask:0xf\n\t"
      "v_add_f32_dpp v92, v92, v92 quad_perm:[2,3,0,1] row_mask:0xf bank_mask:0xf\n\t"
      "v_add_f32_dpp v96, v96, v96 row_half_mirror row_mask:0xf bank_mask:0xf\n\t"
      "v_add_f32_dpp v97, v97, v97 row_half_mirror row_mask:0xf bank_mask:0xf\n\t"
      "v_add_f32_dpp v88, v88, v88 row_half_mirror row_mask:0xf bank_mask:0xf\n\t"
      "v_add_f32_dpp v92, v92, v92 row_half_mirror row_mask:0xf bank_mask:0xf\n\t"
      "v_mul_f32_e32 v88, 0xbb124925, v88\n\t"
      "v_add_f32_dpp v96, v96, v96 row_mirror row_mask:0xf bank_mask:0xf\n\t"
      "v_mul_f32_e32 v92, 0x3b124925, v92\n\t"
      "s_bitcmp0_b32 s27, 2\n\t"
      "s_cbranch_scc1 SK2_%=\n\t"
      "s_mul_i32 s72, s81, 172032\n\t"
      "s_add_u32 s72, s72, s37\n\t"
      "s_add_u32 s40, s6, s72\n\t"
      "s_addc_u32 s41, s7, 0\n\t"
      "s_add_u32 s81, s81, 1\n\t"
      "s_cmp_eq_u32 s81, 7\n\t"
      "s_cselect_b32 s81, 0, s81\n\t"
      "s_add_u32 m0, s39, 147456\n\t"
      "s_nop 0\n\t"
      "global_load_lds_dwordx4 v148, s[40:41]\n\t"
      "global_load_lds_dwordx4 v149, s[40:41] offset:1024\n\t"
      "SK2_%=:\n\t"
      "v_add_f32_dpp v97, v97, v97 row_mirror row_mask:0xf bank_mask:0xf\n\t"
      "v_fma_f32 v92, -v88, v88, v92\n\t"
      "s_nop 0\n\t"
      "v_permlane16_swap_b32_e32 v96, v97\n\t"
      "v_max_f32_e32 v92, 0, v92\n\t"
      "v_add_f32_e32 v96, v96, v97\n\t"
      "v_add_f32_e32 v92, 0x3727c5ac, v92\n\t"
      "v_mov_b32_e32 v97, v96\n\t"
      "v_rsq_f32_e32 v92, v92\n\t"
      "s_nop 0\n\t"
      "v_permlane32_swap_b32_e32 v96, v97\n\t"
      "v_add_f32_e32 v96, v96, v97\n\t"
      "v_mul_f32_e32 v88, v88, v92\n\t"
      "v_mov_b32_e32 v97, v96\n\t"
      "v_pk_fma_f32 v[32:33], v[32:33], v[92:93], v[88:89] op_sel_hi:[1,0,0]\n\t"
      "v_pk_fma_f32 v[34:35], v[34:35], v[92:93], v[88:89] op_sel_hi:[1,0,0]\n\t"
      "v_permlane16_swap_b32_e32 v96, v97\n\t"
      "v_pk_fma_f32 v[36:37], v[36:37], v[92:93], v[88:89] op_sel_hi:[1,0,0]\n\t"
      "v_pk_fma_f32 v[38:39], v[38:39], v[92:93], v[88:89] op_sel_hi:[1,0,0]\n\t"
      "v_mul_f32_e32 v96, 0xbb124925, v96\n\t"
      "v_cvt_pk_bf16_f32 v0, v32, v33\n\t"
      "v_cvt_pk_bf16_f32 v1, v34, v35\n\t"
      "v_cvt_pk_bf16_f32 v2, v36, v37\n\t"
      "v_cvt_pk_bf16_f32 v3, v38, v39\n\t"
      "ds_write_b128 v112, v[0:3]\n\t"
      "v_pk_fma_f32 v[40:41], v[40:41], v[92:93], v[88:89] op_sel_hi:[1,0,0]\n\t"
      "v_pk_fma_f32 v[42:43], v[42:43], v[92:93], v[88:89] op_sel_hi:[1,0,0]\n\t"
      "v_mul_f32_e32 v97, 0x3b124925, v97\n\t"
      "v_pk_fma_f32 v[44:45], v[44:45], v[92:93], v[88:89] op_sel_hi:[1,0,0]\n\t"
      "v_pk_fma_f32 v[46:47], v[46:47], v[92:93], v[88:89] op_sel_hi:[1,0,0]\n\t"
      "v_fma_f32 v97, -v96, v96, v97\n\t"
      "v_cvt_pk_bf16_f32 v4, v40, v41\n\t"
      "s_bitcmp0_b32 s27, 2\n\t"
      "s_cbranch_scc1 SK3_%=\n\t"
      "global_load_lds_dwordx4 v150, s[40:41] offset:2048\n\t"
      "global_load_lds_dwordx4 v151, s[40:41] offset:3072\n\t"
      "SK3_%=:\n\t"
      "v_cvt_pk_bf16_f32 v5, v42, v43\n\t"
      "v_cvt_pk_bf16_f32 v6, v44, v45\n\t"
      "v_cvt_pk_bf16_f32 v7, v46, v47\n\t"
      "ds_write_b128 v113, v[4:7]\n\t"
      "v_pk_fma_f32 v[48:49], v[48:49], v[92:93], v[88:89] op_sel_hi:[1,0,0]\n\t"
      "v_pk_fma_f32 v[50:51], v[50:51], v[92:93], v[88:89] op_sel_hi:[1,0,0]\n\t"
      "v_max_f32_e32 v97, 0, v97\n\t"
      "v_pk_fma_f32 v[52:53], v[52:53], v[92:93], v[88:89] op_sel_hi:[1,0,0]\n\t"
      "v_pk_fma_f32 v[54:55], v[54:55], v[92:93], v[88:89] op_sel_hi:[1,0,0]\n\t"
      "v_add_f32_e32 v97, 0x3727c5ac, v97\n\t"
      "v_cvt_pk_bf16_f32 v8, v48, v49\n\t"
      "v_cvt_pk_bf16_f32 v9, v50, v51\n\t"
      "v_cvt_pk_bf16_f32 v10, v52, v53\n\t"
      "v_cvt_pk_bf16_f32 v11, v54, v55\n\t"
      "ds_write_b128 v114, v[8:11]\n\t"
      "v_pk_fma_f32 v[56:57], v[56:57], v[92:93], v[88:89] op_sel_hi:[1,0,0]\n\t"
      "v_pk_fma_f32 v[58:59], v[58:59], v[92:93], v[88:89] op_sel_hi:[1,0,0]\n\t"
      "v_rsq_f32_e32 v97, v97\n\t"
      "v_pk_fma_f32 v[60:61], v[60:61], v[92:93], v[88:89] op_sel_hi:[1,0,0]\n\t"
      "v_pk_fma_f32 v[62:63], v[62:63], v[92:93], v[88:89] op_sel_hi:[1,0,0]\n\t"
      "v_cvt_pk_bf16_f32 v12, v56, v57\n\t"
      "v_cvt_pk_bf16_f32 v13, v58, v59\n\t"
      "v_cvt_pk_bf16_f32 v14, v60, v61\n\t"
      "v_cvt_pk_bf16_f32 v15, v62, v63\n\t"
      "ds_write_b128 v115, v[12:15]\n\t"
      "v_pk_fma_f32 v[64:65], v[64:65], v[92:93], v[88:89] op_sel_hi:[1,0,0]\n\t"
      "v_pk_fma_f32 v[66:67], v[66:67], v[92:93], v[88:89] op_sel_hi:[1,0,0]\n\t"
      "v_mul_f32_e32 v96, v96, v97\n\t"
      "v_pk_fma_f32 v[68:69], v[68:69], v[92:93], v[88:89] op_sel_hi:[1,0,0]\n\t"
      "v_pk_fma_f32 v[70:71], v[70:71], v[92:93], v[88:89] op_sel_hi:[1,0,0]\n\t"
      "s_bitcmp0_b32 s27, 2\n\t"
      "s_cbranch_scc1 SK4_%=\n\t"
      "s_mul_i32 s72, s81, 172032\n\t"
      "s_add_u32 s72, s72, s37\n\t"
      "s_add_u32 s40, s6, s72\n\t"
      "s_addc_u32 s41, s7, 0\n\t"
      "s_add_u32 s81, s81, 1\n\t"
      "s_cmp_eq_u32 s81, 7\n\t"
      "s_cselect_b32 s81, 0, s81\n\t"
      "s_add_u32 m0, s39, 65536\n\t"
      "s_nop 0\n\t"
      "global_load_lds_dwordx4 v148, s[40:41]\n\t"
      "global_load_lds_dwordx4 v149, s[40:41] offset:1024\n\t"
      "SK4_%=:\n\t"
      "v_cvt_pk_bf16_f32 v16, v64, v65\n\t"
      "v_cvt_pk_bf16_f32 v17, v66, v67\n\t"
      "v_cvt_pk_bf16_f32 v18, v68, v69\n\t"
      "v_cvt_pk_bf16_f32 v19, v70, v71\n\t"
      "ds_write_b128 v116, v[16:19]\n\t"
      "v_pk_fma_f32 v[72:73], v[72:73], v[92:93], v[88:89] op_sel_hi:[1,0,0]\n\t"
      "v_pk_fma_f32 v[74:75], v[74:75], v[92:93], v[88:89] op_sel_hi:[1,0,0]\n\t"
      "v_pk_fma_f32 v[76:77], v[76:77], v[92:93], v[88:89] op_sel_hi:[1,0,0]\n\t"
      "v_pk_fma_f32 v[78:79], v[78:79], v[92:93], v[88:89] op_sel_hi:[1,0,0]\n\t"
      "v_cvt_pk_bf16_f32 v20, v72, v73\n\t"
      "v_cvt_pk_bf16_f32 v21, v74, v75\n\t"
      "v_cvt_pk_bf16_f32 v22, v76, v77\n\t"
      "v_cvt_pk_bf16_f32 v23, v78, v79\n\t"
      "ds_write_b128 v117, v[20:23]\n\t"
      "v_pk_fma_f32 v[80:81], v[80:81], v[92:93], v[88:89] op_sel_hi:[1,0,0]\n\t"
      "v_pk_fma_f32 v[82:83], v[82:83], v[92:93], v[88:89] op_sel_hi:[1,0,0]\n\t"
      "v_pk_fma_f32 v[84:85], v[84:85], v[92:93], v[88:89] op_sel_hi:[1,0,0]\n\t"
      "v_pk_fma_f32 v[86:87], v[86:87], v[92:93], v[88:89] op_sel_hi:[1,0,0]\n\t"
      "v_cvt_pk_bf16_f32 v24, v80, v81\n\t"
      "v_cvt_pk_bf16_f32 v25, v82, v83\n\t"
      "v_cvt_pk_bf16_f32 v26, v84, v85\n\t"
      "v_cvt_pk_bf16_f32 v27, v86, v87\n\t"
      "ds_write_b128 v118, v[24:27]\n\t"
      "v_lshlrev_b32_e32 v32, 16, v28\n\t"
      "v_and_b32_e32 v33, 0xffff0000, v28\n\t"
      "v_lshlrev_b32_e32 v34, 16, v29\n\t"
      "v_and_b32_e32 v35, 0xffff0000, v29\n\t"
      "v_lshlrev_b32_e32 v36, 16, v30\n\t"
      "v_and_b32_e32 v37, 0xffff0000, v30\n\t"
      "v_lshlrev_b32_e32 v38, 16, v31\n\t"
      "s_bitcmp0_b32 s27, 2\n\t"
      "s_cbranch_scc1 SK5_%=\n\t"
      "global_load_lds_dwordx4 v150, s[40:41] offset:2048\n\t"
      "global_load_lds_dwordx4 v151, s[40:41] offset:3072\n\t"
      "SK5_%=:\n\t"
      "v_and_b32_e32 v39, 0xffff0000, v31\n\t"
      "v_pk_fma_f32 v[32:33], v[32:33], v[96:97], v[96:97] op_sel:[0,1,0] op_sel_hi:[1,1,0]\n\t"
      "v_pk_fma_f32 v[34:35], v[34:35], v[96:97], v[96:97] op_sel:[0,1,0] op_sel_hi:[1,1,0]\n\t"
      "v_pk_fma_f32 v[36:37], v[36:37], v[96:97], v[96:97] op_sel:[0,1,0] op_sel_hi:[1,1,0]\n\t"
      "v_pk_fma_f32 v[38:39], v[38:39], v[96:97], v[96:97] op_sel:[0,1,0] op_sel_hi:[1,1,0]\n\t"
      "v_cvt_pk_bf16_f32 v28, v32, v33\n\t"
      "v_cvt_pk_bf16_f32 v29, v34, v35\n\t"
      "v_cvt_pk_bf16_f32 v30, v36, v37\n\t"
      "v_cvt_pk_bf16_f32 v31, v38, v39\n\t"
      "ds_write_b128 v119, v[28:31]\n\t"
      "s_waitcnt lgkmcnt(0)\n\t"
      "s_bitcmp1_b32 s27, 2\n\t"
      "s_cbranch_scc1 LPATH_%=\n\t"
      "s_setprio 2\n\t"
      "s_barrier\n\t"
      "ds_read_b64_tr_b16 v[76:77], v168\n\t"
      "ds_read_b64_tr_b16 v[78:79], v168 offset:512\n\t"
      "ds_read_b128 v[68:71], v160\n\t"
      "ds_read_b128 v[72:75], v160 offset:14336\n\t"
      "ds_read_b64_tr_b16 v[80:81], v169\n\t"
      "ds_read_b64_tr_b16 v[82:83], v169 offset:512\n\t"
      "ds_read_b64_tr_b16 v[84:85], v168 offset:1024\n\t"
      "ds_read_b64_tr_b16 v[86:87], v168 offset:1536\n\t"
      "ds_read_b128 v[64:67], v162\n\t"
      "ds_read_b64_tr_b16 v[88:89], v169 offset:1024\n\t"
      "ds_read_b64_tr_b16 v[90:91], v169 offset:1536\n\t"
      "ds_read_b64_tr_b16 v[104:105], v168 offset:2048\n\t"
      "ds_read_b64_tr_b16 v[106:107], v168 offset:2560\n\t"
      "ds_read_b128 v[96:99], v161 offset:1024\n\t"
      "ds_read_b128 v[100:103], v161 offset:15360\n\t"
      "ds_read_b64_tr_b16 v[108:109], v169 offset:2048\n\t"
      "ds_read_b64_tr_b16 v[110:111], v169 offset:2560\n\t"
      "ds_read_b64_tr_b16 v[112:113], v168 offset:3072\n\t"
      "ds_read_b64_tr_b16 v[114:115], v168 offset:3584\n\t"
      "ds_read_b128 v[92:95], v163 offset:1024\n\t"
      "ds_read_b64_tr_b16 v[116:117], v169 offset:3072\n\t"
      "ds_read_b64_tr_b16 v[118:119], v169 offset:3584\n\t"
      "s_barrier\n\t"
      "s_waitcnt lgkmcnt(14)\n\t"
      "v_mfma_f32_16x16x32_bf16 v[0:3], v[76:79], v[68:71], 0\n\t"
      "ds_read_b64_tr_b16 v[132:133], v168 offset:16384\n\t"
      "ds_read_b64_tr_b16 v[134:135], v168 offset:16896\n\t"
      "v_mfma_f32_16x16x32_bf16 v[8:11], v[76:79], v[72:75], 0\n\t"
      "ds_read_b128 v[124:127], v160 offset:2048\n\t"
      "v_mfma_f32_16x16x32_bf16 v[4:7], v[80:83], v[68:71], 0\n\t"
      "ds_read_b128 v[128:131], v160 offset:16384\n\t"
      "v_mfma_f32_16x16x32_bf16 v[12:15], v[80:83], v[72:75], 0\n\t"
      "ds_read_b64_tr_b16 v[136:137], v169 offset:16384\n\t"
      "v_mfma_f32_16x16x32_bf16 v[24:27], v[84:87], v[68:71], 0\n\t"
      "ds_read_b64_tr_b16 v[138:139], v169 offset:16896\n\t"
      "v_mfma_f32_16x16x32_bf16 v[32:35], v[84:87], v[72:75], 0\n\t"
      "ds_read_b64_tr_b16 v[140:141], v168 offset:17408\n\t"
      "s_waitcnt lgkmcnt(14)\n\t"
      "v_mfma_f32_16x16x32_bf16 v[16:19], v[84:87], v[64:67], 0\n\t"
      "ds_read_b64_tr_b16 v[142:143], v168 offset:17920\n\t"
      "v_mfma_f32_16x16x32_bf16 v[28:31], v[88:91], v[68:71], 0\n\t"
      "ds_read_b128 v[120:123], v162 offset:2048\n\t"
      "v_mfma_f32_16x16x32_bf16 v[36:39], v[88:91], v[72:75], 0\n\t"
      "ds_read_b64_tr_b16 v[144:145], v169 offset:17408\n\t"
      "v_mfma_f32_16x16x32_bf16 v[20:23], v[88:91], v[64:67], 0\n\t"
      "ds_read_b64_tr_b16 v[146:147], v169 offset:17920\n\t"
      "s_waitcnt lgkmcnt(11)\n\t"
      "v_mfma_f32_16x16x32_bf16 v[0:3], v[104:107], v[96:99], v[0:3]\n\t"
      "ds_read_b64_tr_b16 v[76:77], v168 offset:18432\n\t"
      "ds_read_b64_tr_b16 v[78:79], v168 offset:18944\n\t"
      "v_mfma_f32_16x16x32_bf16 v[8:11], v[104:107], v[100:103], v[8:11]\n\t"
      "ds_read_b128 v[68:71], v161 offset:3072\n\t"
      "v_mfma_f32_16x16x32_bf16 v[4:7], v[108:111], v[96:99], v[4:7]\n\t"
      "ds_read_b128 v[72:75], v161 offset:17408\n\t"
      "v_mfma_f32_16x16x32_bf16 v[12:15], v[108:111], v[100:103], v[12:15]\n\t"
      "ds_read_b64_tr_b16 v[80:81], v169 offset:18432\n\t"
      "v_mfma_f32_16x16x32_bf16 v[24:27], v[112:115], v[96:99], v[24:27]\n\t"
      "ds_read_b64_tr_b16 v[82:83], v169 offset:18944\n\t"
      "v_mfma_f32_16x16x32_bf16 v[32:35], v[112:115], v[100:103], v[32:35]\n\t"
      "ds_read_b64_tr_b16 v[84:85], v168 offset:19456\n\t"
      "v_mfma_f32_16x16x32_bf16 v[16:19], v[112:115], v[92:95], v[16:19]\n\t"
      "ds_read_b64_tr_b16 v[86:87], v168 offset:19968\n\t"
      "v_mfma_f32_16x16x32_bf16 v[28:31], v[116:119], v[96:99], v[28:31]\n\t"
      "ds_read_b128 v[64:67], v163 offset:3072\n\t"
      "v_mfma_f32_16x16x32_bf16 v[36:39], v[116:119], v[100:103], v[36:39]\n\t"
      "ds_read_b64_tr_b16 v[88:89], v169 offset:19456\n\t"
      "v_mfma_f32_16x16x32_bf16 v[20:23], v[116:119], v[92:95], v[20:23]\n\t"
      "ds_read_b64_tr_b16 v[90:91], v169 offset:19968\n\t"
      "s_barrier\n\t"
      "s_waitcnt lgkmcnt(11)\n\t"
      "v_mfma_f32_16x16x32_bf16 v[0:3], v[132:135], v[124:127], v[0:3]\n\t"
      "ds_read_b64_tr_b16 v[104:105], v166\n\t"
      "ds_read_b64_tr_b16 v[106:107], v166 offset:512\n\t"
      "v_lshlrev_b32_e32 v199, 2, v193\n\t"
      "v_mfma_f32_16x16x32_bf16 v[8:11], v[132:135], v[128:131], v[8:11]\n\t"
      "ds_read_b128 v[96:99], v160 offset:4096\n\t"
      "v_sub_u32_e32 v199, v194, v199\n\t"
      "v_mfma_f32_16x16x32_bf16 v[4:7], v[136:139], v[124:127], v[4:7]\n\t"
      "ds_read_b128 v[100:103], v160 offset:18432\n\t"
      "v_mov_b32_e32 v200, 0xff800000\n\t"
      "v_mfma_f32_16x16x32_bf16 v[12:15], v[136:139], v[128:131], v[12:15]\n\t"
      "ds_read_b64_tr_b16 v[108:109], v167\n\t"
      "s_cmp_lt_u32 s25, 2\n\t"
      "v_mfma_f32_16x16x32_bf16 v[24:27], v[140:143], v[124:127], v[24:27]\n\t"
      "ds_read_b64_tr_b16 v[110:111], v167 offset:512\n\t"
      "s_cselect_b32 s35, 0xff800000, 0\n\t"
      "v_mfma_f32_16x16x32_bf16 v[32:35], v[140:143], v[128:131], v[32:35]\n\t"
      "ds_read_b64_tr_b16 v[112:113], v166 offset:1024\n\t"
      "s_bitcmp0_b32 s24, 0\n\t"
      "v_mfma_f32_16x16x32_bf16 v[16:19], v[140:143], v[120:123], v[16:19]\n\t"
      "ds_read_b64_tr_b16 v[114:115], v166 offset:1536\n\t"
      "s_cselect_b32 s35, s35, 0\n\t"
      "v_mfma_f32_16x16x32_bf16 v[28:31], v[144:147], v[124:127], v[28:31]\n\t"
      "ds_read_b128 v[92:95], v162 offset:4096\n\t"
      "v_cmp_ge_i32_e32 vcc, -9, v199\n\t"
      "v_mfma_f32_16x16x32_bf16 v[36:39], v[144:147], v[128:131], v[36:39]\n\t"
      "ds_read_b64_tr_b16 v[116:117], v167 offset:1024\n\t"
      "v_subrev_u32_e32 v201, 0, v199\n\t"
      "v_mfma_f32_16x16x32_bf16 v[20:23], v[144:147], v[120:123], v[20:23]\n\t"
      "ds_read_b64_tr_b16 v[118:119], v167 offset:1536\n\t"
      "v_cmp_gt_u32_e64 s[64:65], 8, v201\n\t"
      "s_waitcnt lgkmcnt(11)\n\t"
      "v_mfma_f32_16x16x32_bf16 v[0:3], v[76:79], v[68:71], v[0:3]\n\t"
      "ds_read_b64_tr_b16 v[132:133], v166 offset:2048\n\t"
      "ds_read_b64_tr_b16 v[134:135], v166 offset:2560\n\t"
      "v_cndmask_b32_e64 v172, v200, 0, vcc\n\t"
      "v_mfma_f32_16x16x32_bf16 v[8:11], v[76:79], v[72:75], v[8:11]\n\t"
      "ds_read_b128 v[124:127], v161 offset:5120\n\t"
      "v_cndmask_b32_e64 v176, v200, 0, s[64:65]\n\t"
      "v_mfma_f32_16x16x32_bf16 v[4:7], v[80:83], v[68:71], v[4:7]\n\t"
      "ds_read_b128 v[128:131], v161 offset:19456\n\t"
      "v_or_b32_e32 v180, s35, v172\n\t"
      "v_mfma_f32_16x16x32_bf16 v[12:15], v[80:83], v[72:75], v[12:15]\n\t"
      "ds_read_b64_tr_b16 v[136:137], v167 offset:2048\n\t"
      "v_cmp_ge_i32_e32 vcc, -8, v199\n\t"
      "v_mfma_f32_16x16x32_bf16 v[24:27], v[84:87], v[68:71], v[24:27]\n\t"
      "ds_read_b64_tr_b16 v[138:139], v167 offset:2560\n\t"
      "v_subrev_u32_e32 v201, 1, v199\n\t"
      "v_mfma_f32_16x16x32_bf16 v[32:35], v[84:87], v[72:75], v[32:35]\n\t"
      "ds_read_b64_tr_b16 v[140:141], v166 offset:3072\n\t"
      "v_cmp_gt_u32_e64 s[64:65], 8, v201\n\t"
      "v_mfma_f32_16x16x32_bf16 v[16:19], v[84:87], v[64:67], v[16:19]\n\t"
      "ds_read_b64_tr_b16 v[142:143], v166 offset:3584\n\t"
      "v_cndmask_b32_e64 v173, v200, 0, vcc\n\t"
      "v_mfma_f32_16x16x32_bf16 v[28:31], v[88:91], v[68:71], v[28:31]\n\t"
      "ds_read_b128 v[120:123], v163 offset:5120\n\t"
      "v_cndmask_b32_e64 v177, v200, 0, s[64:65]\n\t"
      "v_mfma_f32_16x16x32_bf16 v[36:39], v[88:91], v[72:75], v[36:39]\n\t"
      "ds_read_b64_tr_b16 v[144:145], v167 offset:3072\n\t"
      "v_or_b32_e32 v181, s35, v173\n\t"
      "v_mfma_f32_16x16x32_bf16 v[20:23], v[88:91], v[64:67], v[20:23]\n\t"
      "ds_read_b64_tr_b16 v[146:147], v167 offset:3584\n\t"
      "v_cmp_ge_i32_e32 vcc, -7, v199\n\t"
      "s_barrier\n\t"
      "s_waitcnt lgkmcnt(11)\n\t"
      "v_mfma_f32_16x16x32_bf16 v[0:3], v[104:107], v[96:99], v[0:3]\n\t"
      "ds_read_b64_tr_b16 v[76:77], v166 offset:16384\n\t"
      "ds_read_b64_tr_b16 v[78:79], v166 offset:16896\n\t"
      "v_subrev_u32_e32 v201, 2, v199\n\t"
      "v_mfma_f32_16x16x32_bf16 v[8:11], v[104:107], v[100:103], v[8:11]\n\t"
      "ds_read_b128 v[68:71], v160 offset:6144\n\t"
      "v_cmp_gt_u32_e64 s[64:65], 8, v201\n\t"
      "v_mfma_f32_16x16x32_bf16 v[4:7], v[108:111], v[96:99], v[4:7]\n\t"
      "ds_read_b128 v[72:75], v160 offset:20480\n\t"
      "v_cndmask_b32_e64 v174, v200, 0, vcc\n\t"
      "v_mfma_f32_16x16x32_bf16 v[12:15], v[108:111], v[100:103], v[12:15]\n\t"
      "ds_read_b64_tr_b16 v[80:81], v167 offset:16384\n\t"
      "v_cndmask_b32_e64 v178, v200, 0, s[64:65]\n\t"
      "v_mfma_f32_16x16x32_bf16 v[24:27], v[112:115], v[96:99], v[24:27]\n\t"
      "ds_read_b64_tr_b16 v[82:83], v167 offset:16896\n\t"
      "v_or_b32_e32 v182, s35, v174\n\t"
      "v_mfma_f32_16x16x32_bf16 v[32:35], v[112:115], v[100:103], v[32:35]\n\t"
      "ds_read_b64_tr_b16 v[84:85], v166 offset:17408\n\t"
      "v_cmp_ge_i32_e32 vcc, -6, v199\n\t"
      "v_mfma_f32_16x16x32_bf16 v[16:19], v[112:115], v[92:95], v[16:19]\n\t"
      "ds_read_b64_tr_b16 v[86:87], v166 offset:17920\n\t"
      "v_subrev_u32_e32 v201, 3, v199\n\t"
      "v_mfma_f32_16x16x32_bf16 v[28:31], v[116:119], v[96:99], v[28:31]\n\t"
      "ds_read_b128 v[64:67], v162 offset:6144\n\t"
      "v_cmp_gt_u32_e64 s[64:65], 8, v201\n\t"
      "v_mfma_f32_16x16x32_bf16 v[36:39], v[116:119], v[100:103], v[36:39]\n\t"
      "ds_read_b64_tr_b16 v[88:89], v167 offset:17408\n\t"
      "v_cndmask_b32_e64 v175, v200, 0, vcc\n\t"
      "v_mfma_f32_16x16x32_bf16 v[20:23], v[116:119], v[92:95], v[20:23]\n\t"
      "ds_read_b64_tr_b16 v[90:91], v167 offset:17920\n\t"
      "v_cndmask_b32_e64 v179, v200, 0, s[64:65]\n\t"
      "s_waitcnt lgkmcnt(11)\n\t"
      "v_mfma_f32_16x16x32_bf16 v[0:3], v[132:135], v[124:127], v[0:3]\n\t"
      "ds_read_b64_tr_b16 v[104:105], v166 offset:18432\n\t"
      "ds_read_b64_tr_b16 v[106:107], v166 offset:18944\n\t"
      "v_or_b32_e32 v183, s35, v175\n\t"
      "v_mfma_f32_16x16x32_bf16 v[8:11], v[132:135], v[128:131], v[8:11]\n\t"
      "ds_read_b128 v[96:99], v161 offset:7168\n\t"
      "v_lshrrev_b32_e32 v201, 31, v172\n\t"
      "v_mfma_f32_16x16x32_bf16 v[4:7], v[136:139], v[124:127], v[4:7]\n\t"
      "ds_read_b128 v[100:103], v161 offset:21504\n\t"
      "v_lshrrev_b32_e32 v202, 31, v173\n\t"
      "v_mfma_f32_16x16x32_bf16 v[12:15], v[136:139], v[128:131], v[12:15]\n\t"
      "ds_read_b64_tr_b16 v[108:109], v167 offset:18432\n\t"
      "v_lshl_or_b32 v201, v202, 16, v201\n\t"
      "v_mfma_f32_16x16x32_bf16 v[24:27], v[140:143], v[124:127], v[24:27]\n\t"
      "ds_read_b64_tr_b16 v[110:111], v167 offset:18944\n\t"
      "v_mul_u32_u24_e32 v184, 0xffff, v201\n\t"
      "v_mfma_f32_16x16x32_bf16 v[32:35], v[140:143], v[128:131], v[32:35]\n\t"
      "ds_read_b64_tr_b16 v[112:113], v166 offset:19456\n\t"
      "v_lshrrev_b32_e32 v201, 31, v174\n\t"
      "v_mfma_f32_16x16x32_bf16 v[16:19], v[140:143], v[120:123], v[16:19]\n\t"
      "ds_read_b64_tr_b16 v[114:115], v166 offset:19968\n\t"
      "v_lshrrev_b32_e32 v202, 31, v175\n\t"
      "v_mfma_f32_16x16x32_bf16 v[28:31], v[144:147], v[124:127], v[28:31]\n\t"
      "ds_read_b128 v[92:95], v165 offset:7168\n\t"
      "v_lshl_or_b32 v201, v202, 16, v201\n\t"
      "v_mfma_f32_16x16x32_bf16 v[36:39], v[144:147], v[128:131], v[36:39]\n\t"
      "ds_read_b64_tr_b16 v[116:117], v167 offset:19456\n\t"
      "v_mul_u32_u24_e32 v185, 0xffff, v201\n\t"
      "v_mfma_f32_16x16x32_bf16 v[20:23], v[144:147], v[120:123], v[20:23]\n\t"
      "ds_read_b64_tr_b16 v[118:119], v167 offset:19968\n\t"
      "s_mov_b32 s68, 0x0\n\t"
      "s_barrier\n\t"
      "s_waitcnt lgkmcnt(11)\n\t"
      "v_mfma_f32_16x16x32_bf16 v[0:3], v[76:79], v[68:71], v[0:3]\n\t"
      "ds_read_b64_tr_b16 v[132:133], v166 offset:32768\n\t"
      "ds_read_b64_tr_b16 v[134:135], v166 offset:33280\n\t"
      "s_mov_b32 s69, 0xffff0000\n\t"
      "v_mfma_f32_16x16x32_bf16 v[8:11], v[76:79], v[72:75], v[8:11]\n\t"
      "ds_read_b128 v[124:127], v160 offset:8192\n\t"
      "s_mov_b32 s70, 0xffff\n\t"
      "v_mfma_f32_16x16x32_bf16 v[4:7], v[80:83], v[68:71], v[4:7]\n\t"
      "ds_read_b128 v[128:131], v160 offset:22528\n\t"
      "s_mov_b32 s71, 0x0\n\t"
      "v_mfma_f32_16x16x32_bf16 v[12:15], v[80:83], v[72:75], v[12:15]\n\t"
      "ds_read_b64_tr_b16 v[136:137], v167 offset:32768\n\t"
      "v_lshlrev_b32_e32 v231, 3, v192\n\t"
      "v_mfma_f32_16x16x32_bf16 v[24:27], v[84:87], v[68:71], v[24:27]\n\t"
      "ds_read_b64_tr_b16 v[138:139], v167 offset:33280\n\t"
      "s_lshl_b32 s34, s25, 12\n\t"
      "v_mfma_f32_16x16x32_bf16 v[32:35], v[84:87], v[72:75], v[32:35]\n\t"
      "ds_read_b64_tr_b16 v[140:141], v166 offset:33792\n\t"
      "s_lshr_b32 s35, s39, 2\n\t"
      "v_mfma_f32_16x16x32_bf16 v[16:19], v[84:87], v[64:67], v[16:19]\n\t"
      "ds_read_b64_tr_b16 v[142:143], v166 offset:34304\n\t"
      "s_add_u32 s34, s34, s35\n\t"
      "v_mfma_f32_16x16x32_bf16 v[28:31], v[88:91], v[68:71], v[28:31]\n\t"
      "ds_read_b128 v[120:123], v164 offset:8192\n\t"
      "s_add_u32 s34, s34, 12582912\n\t"
      "v_mfma_f32_16x16x32_bf16 v[36:39], v[88:91], v[72:75], v[36:39]\n\t"
      "ds_read_b64_tr_b16 v[144:145], v167 offset:33792\n\t"
      "s_add_u32 s46, s20, s34\n\t"
      "v_mfma_f32_16x16x32_bf16 v[20:23], v[88:91], v[64:67], v[20:23]\n\t"
      "ds_read_b64_tr_b16 v[146:147], v167 offset:34304\n\t"
      "s_addc_u32 s47, s21, 0\n\t"
      "s_waitcnt lgkmcnt(11)\n\t"
      "v_mfma_f32_16x16x32_bf16 v[0:3], v[104:107], v[96:99], v[0:3]\n\t"
      "ds_read_b64_tr_b16 v[76:77], v166 offset:34816\n\t"
      "ds_read_b64_tr_b16 v[78:79], v166 offset:35328\n\t"
      "s_lshl_b32 s34, s28, 7\n\t"
      "v_mfma_f32_16x16x32_bf16 v[8:11], v[104:107], v[100:103], v[8:11]\n\t"
      "ds_read_b128 v[68:71], v161 offset:9216\n\t"
      "s_add_u32 s58, s46, s34\n\t"
      "v_mfma_f32_16x16x32_bf16 v[4:7], v[108:111], v[96:99], v[4:7]\n\t"
      "ds_read_b128 v[72:75], v161 offset:23552\n\t"
      "s_addc_u32 s59, s47, 0\n\t"
      "v_mfma_f32_16x16x32_bf16 v[12:15], v[108:111], v[100:103], v[12:15]\n\t"
      "ds_read_b64_tr_b16 v[80:81], v167 offset:34816\n\t"
      "s_and_b32 s34, s24, 1\n\t"
      "v_mfma_f32_16x16x32_bf16 v[24:27], v[112:115], v[96:99], v[24:27]\n\t"
      "ds_read_b64_tr_b16 v[82:83], v167 offset:35328\n\t"
      "s_lshl1_add_u32 s34, s25, s34\n\t"
      "v_mfma_f32_16x16x32_bf16 v[32:35], v[112:115], v[100:103], v[32:35]\n\t"
      "ds_read_b64_tr_b16 v[84:85], v166 offset:35840\n\t"
      "s_lshl_b32 s34, s34, 5\n\t"
      "v_mfma_f32_16x16x32_bf16 v[16:19], v[112:115], v[92:95], v[16:19]\n\t"
      "ds_read_b64_tr_b16 v[86:87], v166 offset:36352\n\t"
      "s_lshl1_add_u32 s34, s26, s34\n\t"
      "v_mfma_f32_16x16x32_bf16 v[28:31], v[116:119], v[96:99], v[28:31]\n\t"
      "ds_read_b128 v[64:67], v165 offset:9216\n\t"
      "s_bfe_u32 s35, s24, 65537\n\t"
      "v_mfma_f32_16x16x32_bf16 v[36:39], v[116:119], v[100:103], v[36:39]\n\t"
      "ds_read_b64_tr_b16 v[88:89], v167 offset:35840\n\t"
      "s_add_u32 s34, s34, s35\n\t"
      "v_mfma_f32_16x16x32_bf16 v[20:23], v[116:119], v[92:95], v[20:23]\n\t"
      "ds_read_b64_tr_b16 v[90:91], v167 offset:36352\n\t"
      "s_lshl_b32 s34, s34, 10\n\t"
      "s_barrier\n\t"
      "s_waitcnt lgkmcnt(11)\n\t"
      "v_mfma_f32_16x16x32_bf16 v[0:3], v[132:135], v[124:127], v[0:3]\n\t"
      "ds_read_b64_tr_b16 v[104:105], v166 offset:49152\n\t"
      "ds_read_b64_tr_b16 v[106:107], v166 offset:49664\n\t"
      "s_add_u32 s42, s20, s34\n\t"
      "v_mfma_f32_16x16x32_bf16 v[8:11], v[132:135], v[128:131], v[8:11]\n\t"
      "ds_read_b128 v[96:99], v160 offset:10240\n\t"
      "s_addc_u32 s43, s21, 0\n\t"
      "v_mfma_f32_16x16x32_bf16 v[4:7], v[136:139], v[124:127], v[4:7]\n\t"
      "ds_read_b128 v[100:103], v160 offset:24576\n\t"
      "s_add_u32 s44, s42, 16384\n\t"
      "v_mfma_f32_16x16x32_bf16 v[12:15], v[136:139], v[128:131], v[12:15]\n\t"
      "ds_read_b64_tr_b16 v[108:109], v167 offset:49152\n\t"
      "s_addc_u32 s45, s43, 0\n\t"
      "v_mfma_f32_16x16x32_bf16 v[24:27], v[140:143], v[124:127], v[24:27]\n\t"
      "ds_read_b64_tr_b16 v[110:111], v167 offset:49664\n\t"
      "v_and_b32_e32 v199, 60, v192\n\t"
      "v_mfma_f32_16x16x32_bf16 v[32:35], v[140:143], v[128:131], v[32:35]\n\t"
      "ds_read_b64_tr_b16 v[112:113], v166 offset:50176\n\t"
      "v_and_b32_e32 v200, 24, v192\n\t"
      "v_mfma_f32_16x16x32_bf16 v[16:19], v[140:143], v[120:123], v[16:19]\n\t"
      "ds_read_b64_tr_b16 v[114:115], v166 offset:50688\n\t"
      "v_lshlrev_b32_e32 v202, 3, v196\n\t"
      "v_mfma_f32_16x16x32_bf16 v[28:31], v[144:147], v[124:127], v[28:31]\n\t"
      "ds_read_b128 v[92:95], v164 offset:10240\n\t"
      "v_lshl_or_b32 v202, v200, 2, v202\n\t"
      "v_mfma_f32_16x16x32_bf16 v[36:39], v[144:147], v[128:131], v[36:39]\n\t"
      "ds_read_b64_tr_b16 v[116:117], v167 offset:50176\n\t"
      "v_lshl_or_b32 v201, v199, 5, v202\n\t"
      "v_mfma_f32_16x16x32_bf16 v[20:23], v[144:147], v[120:123], v[20:23]\n\t"
      "ds_read_b64_tr_b16 v[118:119], v167 offset:50688\n\t"
      "v_add_u32_e32 v216, 106496, v201\n\t"
      "s_waitcnt lgkmcnt(11)\n\t"
      "v_mfma_f32_16x16x32_bf16 v[0:3], v[76:79], v[68:71], v[0:3]\n\t"
      "ds_read_b64_tr_b16 v[132:133], v166 offset:51200\n\t"
      "ds_read_b64_tr_b16 v[134:135], v166 offset:51712\n\t"
      "v_mov_b32_e32 v199, 2048\n\t"
      "v_mfma_f32_16x16x32_bf16 v[8:11], v[76:79], v[72:75], v[8:11]\n\t"
      "ds_read_b128 v[124:127], v161 offset:11264\n\t"
      "v_cndmask_b32_e64 v199, v199, 0, s[68:69]\n\t"
      "v_mfma_f32_16x16x32_bf16 v[4:7], v[80:83], v[68:71], v[4:7]\n\t"
      "ds_read_b128 v[128:131], v161 offset:25600\n\t"
      "v_add_u32_e32 v220, v216, v199\n\t"
      "v_mfma_f32_16x16x32_bf16 v[12:15], v[80:83], v[72:75], v[12:15]\n\t"
      "ds_read_b64_tr_b16 v[136:137], v167 offset:51200\n\t"
      "v_xor_b32_e32 v217, 32, v216\n\t"
      "v_mfma_f32_16x16x32_bf16 v[24:27], v[84:87], v[68:71], v[24:27]\n\t"
      "ds_read_b64_tr_b16 v[138:139], v167 offset:51712\n\t"
      "v_xor_b32_e32 v218, 64, v216\n\t"
      "v_mfma_f32_16x16x32_bf16 v[32:35], v[84:87], v[72:75], v[32:35]\n\t"
      "ds_read_b64_tr_b16 v[140:141], v166 offset:52224\n\t"
      "v_xor_b32_e32 v219, 96, v216\n\t"
      "v_mfma_f32_16x16x32_bf16 v[16:19], v[84:87], v[64:67], v[16:19]\n\t"
      "ds_read_b64_tr_b16 v[142:143], v166 offset:52736\n\t"
      "v_xor_b32_e32 v221, 32, v220\n\t"
      "v_mfma_f32_16x16x32_bf16 v[28:31], v[88:91], v[68:71], v[28:31]\n\t"
      "ds_read_b128 v[120:123], v165 offset:11264\n\t"
      "v_xor_b32_e32 v222, 64, v220\n\t"
      "v_mfma_f32_16x16x32_bf16 v[36:39], v[88:91], v[72:75], v[36:39]\n\t"
      "ds_read_b64_tr_b16 v[144:145], v167 offset:52224\n\t"
      "v_xor_b32_e32 v223, 96, v220\n\t"
      "v_mfma_f32_16x16x32_bf16 v[20:23], v[88:91], v[64:67], v[20:23]\n\t"
      "ds_read_b64_tr_b16 v[146:147], v167 offset:52736\n\t"
      "s_lshr_b32 s34, s39, 7\n\t"
      "s_barrier\n\t"
      "s_waitcnt lgkmcnt(11)\n\t"
      "v_mfma_f32_16x16x32_bf16 v[0:3], v[104:107], v[96:99], v[0:3]\n\t"
      "ds_read_b64_tr_b16 v[76:77], v168\n\t"
      "ds_read_b64_tr_b16 v[78:79], v168 offset:512\n\t"
      "s_add_u32 s34, s34, s38\n\t"
      "v_mfma_f32_16x16x32_bf16 v[8:11], v[104:107], v[100:103], v[8:11]\n\t"
      "ds_read_b128 v[68:71], v160 offset:12288\n\t"
      "s_mul_i32 s34, s34, 896\n\t"
      "v_mfma_f32_16x16x32_bf16 v[4:7], v[108:111], v[96:99], v[4:7]\n\t"
      "ds_read_b128 v[72:75], v160 offset:26624\n\t"
      "s_lshl_b32 s35, s28, 7\n\t"
      "v_mfma_f32_16x16x32_bf16 v[12:15], v[108:111], v[100:103], v[12:15]\n\t"
      "ds_read_b64_tr_b16 v[80:81], v169\n\t"
      "s_add_u32 s34, s34, s35\n\t"
      "v_mfma_f32_16x16x32_bf16 v[24:27], v[112:115], v[96:99], v[24:27]\n\t"
      "ds_read_b64_tr_b16 v[82:83], v169 offset:512\n\t"
      "v_and_b32_e32 v199, 16, v192\n\t"
      "v_mfma_f32_16x16x32_bf16 v[32:35], v[112:115], v[100:103], v[32:35]\n\t"
      "ds_read_b64_tr_b16 v[84:85], v168 offset:1024\n\t"
      "v_lshlrev_b32_e32 v199, 1, v199\n\t"
      "v_mfma_f32_16x16x32_bf16 v[16:19], v[112:115], v[92:95], v[16:19]\n\t"
      "ds_read_b64_tr_b16 v[86:87], v168 offset:1536\n\t"
      "v_and_b32_e32 v200, 32, v192\n\t"
      "v_mfma_f32_16x16x32_bf16 v[28:31], v[116:119], v[96:99], v[28:31]\n\t"
      "ds_read_b128 v[64:67], v164 offset:12288\n\t"
      "v_lshrrev_b32_e32 v201, 1, v200\n\t"
      "v_mfma_f32_16x16x32_bf16 v[36:39], v[116:119], v[100:103], v[36:39]\n\t"
      "ds_read_b64_tr_b16 v[88:89], v169 offset:1024\n\t"
      "v_add3_u32 v228, v199, v201, s34\n\t"
      "v_mfma_f32_16x16x32_bf16 v[20:23], v[116:119], v[92:95], v[20:23]\n\t"
      "ds_read_b64_tr_b16 v[90:91], v169 offset:1536\n\t"
      "s_movk_i32 s35, 1792\n\t"
      "s_waitcnt lgkmcnt(11)\n\t"
      "v_mfma_f32_16x16x32_bf16 v[0:3], v[132:135], v[124:127], v[0:3]\n\t"
      "ds_read_b64_tr_b16 v[104:105], v168 offset:2048\n\t"
      "ds_read_b64_tr_b16 v[106:107], v168 offset:2560\n\t"
      "v_mad_u32_u24 v228, v194, s35, v228\n\t"
      "v_mfma_f32_16x16x32_bf16 v[8:11], v[132:135], v[128:131], v[8:11]\n\t"
      "ds_read_b128 v[96:99], v161 offset:13312\n\t"
      "v_mfma_f32_16x16x32_bf16 v[4:7], v[136:139], v[124:127], v[4:7]\n\t"
      "ds_read_b128 v[100:103], v161 offset:27648\n\t"
      "v_mfma_f32_16x16x32_bf16 v[12:15], v[136:139], v[128:131], v[12:15]\n\t"
      "ds_read_b64_tr_b16 v[108:109], v169 offset:2048\n\t"
      "v_mfma_f32_16x16x32_bf16 v[24:27], v[140:143], v[124:127], v[24:27]\n\t"
      "ds_read_b64_tr_b16 v[110:111], v169 offset:2560\n\t"
      "v_mfma_f32_16x16x32_bf16 v[32:35], v[140:143], v[128:131], v[32:35]\n\t"
      "ds_read_b64_tr_b16 v[112:113], v168 offset:3072\n\t"
      "v_mfma_f32_16x16x32_bf16 v[16:19], v[140:143], v[120:123], v[16:19]\n\t"
      "ds_read_b64_tr_b16 v[114:115], v168 offset:3584\n\t"
      "v_mfma_f32_16x16x32_bf16 v[28:31], v[144:147], v[124:127], v[28:31]\n\t"
      "ds_read_b128 v[92:95], v165 offset:13312\n\t"
      "v_mfma_f32_16x16x32_bf16 v[36:39], v[144:147], v[128:131], v[36:39]\n\t"
      "ds_read_b64_tr_b16 v[116:117], v169 offset:3072\n\t"
      "v_mfma_f32_16x16x32_bf16 v[20:23], v[144:147], v[120:123], v[20:23]\n\t"
      "ds_read_b64_tr_b16 v[118:119], v169 offset:3584\n\t"
      "s_barrier\n\t"
      "s_waitcnt lgkmcnt(11)\n\t"
      "v_mfma_f32_16x16x32_bf16 v[0:3], v[76:79], v[68:71], v[0:3]\n\t"
      "ds_read_b64_tr_b16 v[132:133], v168 offset:16384\n\t"
      "v_mfma_f32_16x16x32_bf16 v[8:11], v[76:79], v[72:75], v[8:11]\n\t"
      "ds_read_b64_tr_b16 v[134:135], v168 offset:16896\n\t"
      "v_mfma_f32_16x16x32_bf16 v[4:7], v[80:83], v[68:71], v[4:7]\n\t"
      "ds_read_b128 v[124:127], v160\n\t"
      "v_mfma_f32_16x16x32_bf16 v[12:15], v[80:83], v[72:75], v[12:15]\n\t"
      "ds_read_b128 v[128:131], v160 offset:14336\n\t"
      "v_mfma_f32_16x16x32_bf16 v[24:27], v[84:87], v[68:71], v[24:27]\n\t"
      "ds_read_b128 v[120:123], v162\n\t"
      "v_mfma_f32_16x16x32_bf16 v[32:35], v[84:87], v[72:75], v[32:35]\n\t"
      "ds_read_b64_tr_b16 v[136:137], v169 offset:16384\n\t"
      "v_mfma_f32_16x16x32_bf16 v[16:19], v[84:87], v[64:67], v[16:19]\n\t"
      "ds_read_b64_tr_b16 v[138:139], v169 offset:16896\n\t"
      "v_mfma_f32_16x16x32_bf16 v[28:31], v[88:91], v[68:71], v[28:31]\n\t"
      "v_mfma_f32_16x16x32_bf16 v[36:39], v[88:91], v[72:75], v[36:39]\n\t"
      "v_mfma_f32_16x16x32_bf16 v[20:23], v[88:91], v[64:67], v[20:23]\n\t"
      "s_waitcnt lgkmcnt(7)\n\t"
      "v_mfma_f32_16x16x32_bf16 v[0:3], v[104:107], v[96:99], v[0:3]\n\t"
      "ds_read_b64_tr_b16 v[76:77], v168 offset:17408\n\t"
      "v_mfma_f32_16x16x32_bf16 v[8:11], v[104:107], v[100:103], v[8:11]\n\t"
      "ds_read_b64_tr_b16 v[78:79], v168 offset:17920\n\t"
      "v_mfma_f32_16x16x32_bf16 v[4:7], v[108:111], v[96:99], v[4:7]\n\t"
      "ds_read_b128 v[68:71], v161 offset:1024\n\t"
      "v_mfma_f32_16x16x32_bf16 v[12:15], v[108:111], v[100:103], v[12:15]\n\t"
      "ds_read_b128 v[72:75], v161 offset:15360\n\t"
      "v_mfma_f32_16x16x32_bf16 v[24:27], v[112:115], v[96:99], v[24:27]\n\t"
      "ds_read_b128 v[64:67], v163 offset:1024\n\t"
      "v_mfma_f32_16x16x32_bf16 v[32:35], v[112:115], v[100:103], v[32:35]\n\t"
      "ds_read_b64_tr_b16 v[80:81], v169 offset:17408\n\t"
      "v_mfma_f32_16x16x32_bf16 v[16:19], v[112:115], v[92:95], v[16:19]\n\t"
      "ds_read_b64_tr_b16 v[82:83], v169 offset:17920\n\t"
      "v_mfma_f32_16x16x32_bf16 v[28:31], v[116:119], v[96:99], v[28:31]\n\t"
      "v_mfma_f32_16x16x32_bf16 v[36:39], v[116:119], v[100:103], v[36:39]\n\t"
      "v_mfma_f32_16x16x32_bf16 v[20:23], v[116:119], v[92:95], v[20:23]\n\t"
      "s_waitcnt lgkmcnt(7)\n\t"
      "v_mfma_f32_16x16x32_bf16 v[48:51], v[124:127], v[132:135], 0\n\t"
      "ds_read_b64_tr_b16 v[104:105], v168 offset:18432\n\t"
      "ds_read_b64_tr_b16 v[106:107], v168 offset:18944\n\t"
      "v_mfma_f32_16x16x32_bf16 v[56:59], v[128:131], v[132:135], 0\n\t"
      "ds_read_b128 v[96:99], v160 offset:2048\n\t"
      "v_mfma_f32_16x16x32_bf16 v[40:43], v[120:123], v[132:135], 0\n\t"
      "ds_read_b128 v[100:103], v160 offset:16384\n\t"
      "v_mfma_f32_16x16x32_bf16 v[52:55], v[124:127], v[136:139], 0\n\t"
      "ds_read_b128 v[92:95], v162 offset:2048\n\t"
      "v_cvt_pk_bf16_f32 v84, v0, v1\n\t"
      "v_cvt_pk_bf16_f32 v85, v2, v3\n\t"
      "v_mfma_f32_16x16x32_bf16 v[60:63], v[128:131], v[136:139], 0\n\t"
      "ds_read_b64_tr_b16 v[108:109], v169 offset:18432\n\t"
      "v_cvt_pk_bf16_f32 v86, v4, v5\n\t"
      "v_cvt_pk_bf16_f32 v87, v6, v7\n\t"
      "v_mfma_f32_16x16x32_bf16 v[44:47], v[120:123], v[136:139], 0\n\t"
      "ds_read_b64_tr_b16 v[110:111], v169 offset:18944\n\t"
      "v_cvt_pk_bf16_f32 v88, v8, v9\n\t"
      "v_cvt_pk_bf16_f32 v89, v10, v11\n\t"
      "s_waitcnt lgkmcnt(7)\n\t"
      "v_mfma_f32_16x16x32_bf16 v[48:51], v[68:71], v[76:79], v[48:51]\n\t"
      "ds_read_b64_tr_b16 v[132:133], v168 offset:19456\n\t"
      "ds_read_b64_tr_b16 v[134:135], v168 offset:19968\n\t"
      "v_cvt_pk_bf16_f32 v90, v12, v13\n\t"
      "v_cvt_pk_bf16_f32 v91, v14, v15\n\t"
      "v_mfma_f32_16x16x32_bf16 v[56:59], v[72:75], v[76:79], v[56:59]\n\t"
      "ds_read_b128 v[124:127], v161 offset:3072\n\t"
      "v_cvt_pk_bf16_f32 v152, v24, v25\n\t"
      "v_cvt_pk_bf16_f32 v153, v26, v27\n\t"
      "v_mfma_f32_16x16x32_bf16 v[40:43], v[64:67], v[76:79], v[40:43]\n\t"
      "ds_read_b128 v[128:131], v161 offset:17408\n\t"
      "v_cvt_pk_bf16_f32 v154, v28, v29\n\t"
      "v_cvt_pk_bf16_f32 v155, v30, v31\n\t"
      "v_mfma_f32_16x16x32_bf16 v[52:55], v[68:71], v[80:83], v[52:55]\n\t"
      "ds_read_b128 v[120:123], v163 offset:3072\n\t"
      "v_cndmask_b32_e64 v86, v86, 0, s[68:69]\n\t"
      "v_cndmask_b32_e64 v87, v87, 0, s[68:69]\n\t"
      "v_mfma_f32_16x16x32_bf16 v[60:63], v[72:75], v[80:83], v[60:63]\n\t"
      "ds_read_b64_tr_b16 v[136:137], v169 offset:19456\n\t"
      "v_cvt_pk_bf16_f32 v156, v32, v33\n\t"
      "v_cvt_pk_bf16_f32 v157, v34, v35\n\t"
      "v_mfma_f32_16x16x32_bf16 v[44:47], v[64:67], v[80:83], v[44:47]\n\t"
      "ds_read_b64_tr_b16 v[138:139], v169 offset:19968\n\t"
      "v_cvt_pk_bf16_f32 v158, v36, v37\n\t"
      "v_cvt_pk_bf16_f32 v159, v38, v39\n\t"
      "s_barrier\n\t"
      "s_waitcnt lgkmcnt(7)\n\t"
      "v_mfma_f32_16x16x32_bf16 v[48:51], v[96:99], v[104:107], v[48:51]\n\t"
      "ds_read_b64_tr_b16 v[76:77], v166\n\t"
      "ds_read_b64_tr_b16 v[78:79], v166 offset:512\n\t"
      "v_cndmask_b32_e64 v90, v90, 0, s[68:69]\n\t"
      "v_cndmask_b32_e64 v91, v91, 0, s[68:69]\n\t"
      "v_mfma_f32_16x16x32_bf16 v[56:59], v[100:103], v[104:107], v[56:59]\n\t"
      "ds_read_b128 v[68:71], v160 offset:4096\n\t"
      "v_cvt_pk_bf16_f32 v148, v16, v17\n\t"
      "v_cvt_pk_bf16_f32 v149, v18, v19\n\t"
      "v_mfma_f32_16x16x32_bf16 v[40:43], v[92:95], v[104:107], v[40:43]\n\t"
      "ds_read_b128 v[72:75], v160 offset:18432\n\t"
      "v_cvt_pk_bf16_f32 v150, v20, v21\n\t"
      "v_cvt_pk_bf16_f32 v151, v22, v23\n\t"
      "v_mfma_f32_16x16x32_bf16 v[52:55], v[96:99], v[108:111], v[52:55]\n\t"
      "ds_read_b128 v[64:67], v162 offset:4096\n\t"
      "v_mov_b32_e32 v34, 0x3e8b97f6\n\t"
      "v_mfma_f32_16x16x32_bf16 v[28:31], v[156:159], v[88:91], v[176:179]\n\t"
      "v_mfma_f32_16x16x32_bf16 v[60:63], v[100:103], v[108:111], v[60:63]\n\t"
      "ds_read_b64_tr_b16 v[80:81], v167\n\t"
      "v_mfma_f32_16x16x32_bf16 v[24:27], v[152:155], v[88:91], v[172:175]\n\t"
      "v_mfma_f32_16x16x32_bf16 v[20:23], v[152:155], v[84:87], v[176:179]\n\t"
      "v_mfma_f32_16x16x32_bf16 v[44:47], v[92:95], v[108:111], v[44:47]\n\t"
      "ds_read_b64_tr_b16 v[82:83], v167 offset:512\n\t"
      "v_mfma_f32_16x16x32_bf16 v[16:19], v[148:151], v[84:87], v[180:183]\n\t"
      "s_waitcnt lgkmcnt(7)\n\t"
      "v_mfma_f32_16x16x32_bf16 v[48:51], v[124:127], v[132:135], v[48:51]\n\t"
      "ds_read_b64_tr_b16 v[104:105], v166 offset:1024\n\t"
      "ds_read_b64_tr_b16 v[106:107], v166 offset:1536\n\t"
      "v_mfma_f32_16x16x32_bf16 v[56:59], v[128:131], v[132:135], v[56:59]\n\t"
      "ds_read_b128 v[96:99], v161 offset:5120\n\t"
      "v_mfma_f32_16x16x32_bf16 v[40:43], v[120:123], v[132:135], v[40:43]\n\t"
      "ds_read_b128 v[100:103], v161 offset:19456\n\t"
      "v_max_f32_e32 v24, v24, v28\n\t"
      "v_mfma_f32_16x16x32_bf16 v[52:55], v[124:127], v[136:139], v[52:55]\n\t"
      "ds_read_b128 v[92:95], v163 offset:5120\n\t"
      "v_max_f32_e32 v25, v25, v29\n\t"
      "v_max_f32_e32 v26, v26, v30\n\t"
      "v_mfma_f32_16x16x32_bf16 v[60:63], v[128:131], v[136:139], v[60:63]\n\t"
      "ds_read_b64_tr_b16 v[108:109], v167 offset:1024\n\t"
      "v_max_f32_e32 v27, v27, v31\n\t"
      "v_max_f32_e32 v16, v16, v20\n\t"
      "v_mfma_f32_16x16x32_bf16 v[44:47], v[120:123], v[136:139], v[44:47]\n\t"
      "ds_read_b64_tr_b16 v[110:111], v167 offset:1536\n\t"
      "v_max_f32_e32 v17, v17, v21\n\t"
      "v_max_f32_e32 v18, v18, v22\n\t"
      "s_waitcnt lgkmcnt(7)\n\t"
      "v_mfma_f32_16x16x32_bf16 v[48:51], v[68:71], v[76:79], v[48:51]\n\t"
      "ds_read_b64_tr_b16 v[132:133], v166 offset:2048\n\t"
      "ds_read_b64_tr_b16 v[134:135], v166 offset:2560\n\t"
      "v_max_f32_e32 v19, v19, v23\n\t"
      "v_max3_f32 v33, v24, v25, v26\n\t"
      "v_mfma_f32_16x16x32_bf16 v[56:59], v[72:75], v[76:79], v[56:59]\n\t"
      "ds_read_b128 v[124:127], v160 offset:6144\n\t"
      "v_max3_f32 v32, v16, v17, v18\n\t"
      "v_max_f32_e32 v33, v33, v27\n\t"
      "v_mfma_f32_16x16x32_bf16 v[40:43], v[64:67], v[76:79], v[40:43]\n\t"
      "ds_read_b128 v[128:131], v160 offset:20480\n\t"
      "v_max_f32_e32 v32, v32, v19\n\t"
      "v_mfma_f32_16x16x32_bf16 v[52:55], v[68:71], v[80:83], v[52:55]\n\t"
      "ds_read_b128 v[120:123], v162 offset:6144\n\t"
      "v_permlane16_swap_b32_e32 v32, v33\n\t"
      "v_max_f32_e32 v32, v32, v33\n\t"
      "v_mfma_f32_16x16x32_bf16 v[60:63], v[72:75], v[80:83], v[60:63]\n\t"
      "ds_read_b64_tr_b16 v[136:137], v167 offset:2048\n\t"
      "v_mov_b32_e32 v33, v32\n\t"
      "v_mfma_f32_16x16x32_bf16 v[44:47], v[64:67], v[80:83], v[44:47]\n\t"
      "ds_read_b64_tr_b16 v[138:139], v167 offset:2560\n\t"
      "v_permlane32_swap_b32_e32 v32, v33\n\t"
      "v_max_f32_e32 v32, v32, v33\n\t"
      "s_waitcnt lgkmcnt(7)\n\t"
      "v_mfma_f32_16x16x32_bf16 v[48:51], v[96:99], v[104:107], v[48:51]\n\t"
      "ds_read_b64_tr_b16 v[76:77], v166 offset:3072\n\t"
      "ds_read_b64_tr_b16 v[78:79], v166 offset:3584\n\t"
      "v_mov_b32_e32 v33, v32\n\t"
      "v_mfma_f32_16x16x32_bf16 v[56:59], v[100:103], v[104:107], v[56:59]\n\t"
      "ds_read_b128 v[68:71], v161 offset:7168\n\t"
      "v_permlane16_swap_b32_e32 v32, v33\n\t"
      "v_mul_f32_e64 v33, -v33, v34\n\t"
      "v_mfma_f32_16x16x32_bf16 v[40:43], v[92:95], v[104:107], v[40:43]\n\t"
      "ds_read_b128 v[72:75], v161 offset:21504\n\t"
      "v_mul_f32_e64 v32, -v32, v34\n\t"
      "v_fma_f32 v24, v24, v34, v33\n\t"
      "v_mfma_f32_16x16x32_bf16 v[52:55], v[96:99], v[108:111], v[52:55]\n\t"
      "ds_read_b128 v[64:67], v165 offset:7168\n\t"
      "v_fma_f32 v25, v25, v34, v33\n\t"
      "v_fma_f32 v26, v26, v34, v33\n\t"
      "v_mfma_f32_16x16x32_bf16 v[60:63], v[100:103], v[108:111], v[60:63]\n\t"
      "ds_read_b64_tr_b16 v[80:81], v167 offset:3072\n\t"
      "v_fma_f32 v27, v27, v34, v33\n\t"
      "v_fma_f32 v16, v16, v34, v32\n\t"
      "v_mfma_f32_16x16x32_bf16 v[44:47], v[92:95], v[108:111], v[44:47]\n\t"
      "ds_read_b64_tr_b16 v[82:83], v167 offset:3584\n\t"
      "v_fma_f32 v17, v17, v34, v32\n\t"
      "v_fma_f32 v18, v18, v34, v32\n\t"
      "s_barrier\n\t"
      "s_waitcnt lgkmcnt(7)\n\t"
      "v_mfma_f32_16x16x32_bf16 v[48:51], v[124:127], v[132:135], v[48:51]\n\t"
      "ds_read_b64_tr_b16 v[104:105], v166 offset:16384\n\t"
      "ds_read_b64_tr_b16 v[106:107], v166 offset:16896\n\t"
      "v_fma_f32 v19, v19, v34, v32\n\t"
      "v_exp_f32_e32 v24, v24\n\t"
      "v_mfma_f32_16x16x32_bf16 v[56:59], v[128:131], v[132:135], v[56:59]\n\t"
      "ds_read_b128 v[96:99], v160 offset:8192\n\t"
      "v_exp_f32_e32 v25, v25\n\t"
      "v_mfma_f32_16x16x32_bf16 v[40:43], v[120:123], v[132:135], v[40:43]\n\t"
      "ds_read_b128 v[100:103], v160 offset:22528\n\t"
      "v_exp_f32_e32 v26, v26\n\t"
      "v_mfma_f32_16x16x32_bf16 v[52:55], v[124:127], v[136:139], v[52:55]\n\t"
      "ds_read_b128 v[92:95], v164 offset:8192\n\t"
      "v_exp_f32_e32 v27, v27\n\t"
      "v_mfma_f32_16x16x32_bf16 v[60:63], v[128:131], v[136:139], v[60:63]\n\t"
      "ds_read_b64_tr_b16 v[108:109], v167 offset:16384\n\t"
      "v_exp_f32_e32 v16, v16\n\t"
      "v_mfma_f32_16x16x32_bf16 v[44:47], v[120:123], v[136:139], v[44:47]\n\t"
      "ds_read_b64_tr_b16 v[110:111], v167 offset:16896\n\t"
      "v_exp_f32_e32 v17, v17\n\t"
      "s_waitcnt lgkmcnt(7)\n\t"
      "v_mfma_f32_16x16x32_bf16 v[48:51], v[68:71], v[76:79], v[48:51]\n\t"
      "ds_read_b64_tr_b16 v[132:133], v166 offset:17408\n\t"
      "ds_read_b64_tr_b16 v[134:135], v166 offset:17920\n\t"
      "v_exp_f32_e32 v18, v18\n\t"
      "v_mfma_f32_16x16x32_bf16 v[56:59], v[72:75], v[76:79], v[56:59]\n\t"
      "ds_read_b128 v[124:127], v161 offset:9216\n\t"
      "v_exp_f32_e32 v19, v19\n\t"
      "v_mfma_f32_16x16x32_bf16 v[40:43], v[64:67], v[76:79], v[40:43]\n\t"
      "ds_read_b128 v[128:131], v161 offset:23552\n\t"
      "v_add_f32_e32 v33, v24, v25\n\t"
      "v_mfma_f32_16x16x32_bf16 v[52:55], v[68:71], v[80:83], v[52:55]\n\t"
      "ds_read_b128 v[120:123], v165 offset:9216\n\t"
      "v_add_f32_e32 v28, v26, v27\n\t"
      "v_add_f32_e32 v32, v16, v17\n\t"
      "v_mfma_f32_16x16x32_bf16 v[60:63], v[72:75], v[80:83], v[60:63]\n\t"
      "ds_read_b64_tr_b16 v[136:137], v167 offset:17408\n\t"
      "v_add_f32_e32 v20, v18, v19\n\t"
      "v_add_f32_e32 v33, v33, v28\n\t"
      "v_mfma_f32_16x16x32_bf16 v[44:47], v[64:67], v[80:83], v[44:47]\n\t"
      "ds_read_b64_tr_b16 v[138:139], v167 offset:17920\n\t"
      "v_add_f32_e32 v32, v32, v20\n\t"
      "s_waitcnt lgkmcnt(7)\n\t"
      "v_mfma_f32_16x16x32_bf16 v[48:51], v[96:99], v[104:107], v[48:51]\n\t"
      "ds_read_b64_tr_b16 v[76:77], v166 offset:18432\n\t"
      "ds_read_b64_tr_b16 v[78:79], v166 offset:18944\n\t"
      "v_permlane16_swap_b32_e32 v32, v33\n\t"
      "v_add_f32_e32 v32, v32, v33\n\t"
      "v_mfma_f32_16x16x32_bf16 v[56:59], v[100:103], v[104:107], v[56:59]\n\t"
      "ds_read_b128 v[68:71], v160 offset:10240\n\t"
      "v_mov_b32_e32 v33, v32\n\t"
      "v_mfma_f32_16x16x32_bf16 v[40:43], v[92:95], v[104:107], v[40:43]\n\t"
      "ds_read_b128 v[72:75], v160 offset:24576\n\t"
      "v_permlane32_swap_b32_e32 v32, v33\n\t"
      "v_add_f32_e32 v32, v32, v33\n\t"
      "v_mfma_f32_16x16x32_bf16 v[52:55], v[96:99], v[108:111], v[52:55]\n\t"
      "ds_read_b128 v[64:67], v164 offset:10240\n\t"
      "v_mov_b32_e32 v33, v32\n\t"
      "v_mfma_f32_16x16x32_bf16 v[60:63], v[100:103], v[108:111], v[60:63]\n\t"
      "ds_read_b64_tr_b16 v[80:81], v167 offset:18432\n\t"
      "v_permlane16_swap_b32_e32 v32, v33\n\t"
      "v_rcp_f32_e32 v33, v33\n\t"
      "v_mfma_f32_16x16x32_bf16 v[44:47], v[92:95], v[108:111], v[44:47]\n\t"
      "ds_read_b64_tr_b16 v[82:83], v167 offset:18944\n\t"
      "v_rcp_f32_e32 v32, v32\n\t"
      "s_waitcnt lgkmcnt(7)\n\t"
      "v_mfma_f32_16x16x32_bf16 v[48:51], v[124:127], v[132:135], v[48:51]\n\t"
      "ds_read_b64_tr_b16 v[104:105], v166 offset:19456\n\t"
      "ds_read_b64_tr_b16 v[106:107], v166 offset:19968\n\t"
      "v_mul_f32_e32 v24, v24, v33\n\t"
      "v_mfma_f32_16x16x32_bf16 v[56:59], v[128:131], v[132:135], v[56:59]\n\t"
      "ds_read_b128 v[96:99], v161 offset:11264\n\t"
      "v_mul_f32_e32 v25, v25, v33\n\t"
      "v_mul_f32_e32 v26, v26, v33\n\t"
      "v_mfma_f32_16x16x32_bf16 v[40:43], v[120:123], v[132:135], v[40:43]\n\t"
      "ds_read_b128 v[100:103], v161 offset:25600\n\t"
      "v_mul_f32_e32 v27, v27, v33\n\t"
      "v_mul_f32_e32 v16, v16, v32\n\t"
      "v_mfma_f32_16x16x32_bf16 v[52:55], v[124:127], v[136:139], v[52:55]\n\t"
      "ds_read_b128 v[92:95], v165 offset:11264\n\t"
      "v_mul_f32_e32 v17, v17, v32\n\t"
      "v_mul_f32_e32 v18, v18, v32\n\t"
      "v_mfma_f32_16x16x32_bf16 v[60:63], v[128:131], v[136:139], v[60:63]\n\t"
      "ds_read_b64_tr_b16 v[108:109], v167 offset:19456\n\t"
      "v_mul_f32_e32 v19, v19, v32\n\t"
      "v_cvt_pk_bf16_f32 v4, v24, v25\n\t"
      "v_mfma_f32_16x16x32_bf16 v[44:47], v[120:123], v[136:139], v[44:47]\n\t"
      "ds_read_b64_tr_b16 v[110:111], v167 offset:19968\n\t"
      "v_cvt_pk_bf16_f32 v5, v26, v27\n\t"
      "v_cvt_pk_bf16_f32 v0, v16, v17\n\t"
      "s_barrier\n\t"
      "s_waitcnt lgkmcnt(7)\n\t"
      "v_mfma_f32_16x16x32_bf16 v[48:51], v[68:71], v[76:79], v[48:51]\n\t"
      "ds_read_b64_tr_b16 v[132:133], v170 offset:32768\n\t"
      "ds_read_b64_tr_b16 v[134:135], v170 offset:33280\n\t"
      "v_cvt_pk_bf16_f32 v1, v18, v19\n\t"
      "v_and_b32_e32 v6, v184, v4\n\t"
      "v_mfma_f32_16x16x32_bf16 v[56:59], v[72:75], v[76:79], v[56:59]\n\t"
      "ds_read_b128 v[124:127], v160 offset:12288\n\t"
      "v_and_b32_e32 v7, v185, v5\n\t"
      "v_and_b32_e32 v2, v184, v0\n\t"
      "v_mfma_f32_16x16x32_bf16 v[40:43], v[64:67], v[76:79], v[40:43]\n\t"
      "ds_read_b128 v[128:131], v160 offset:26624\n\t"
      "v_and_b32_e32 v3, v185, v1\n\t"
      "v_xor_b32_e32 v4, v4, v6\n\t"
      "v_mfma_f32_16x16x32_bf16 v[52:55], v[68:71], v[80:83], v[52:55]\n\t"
      "ds_read_b128 v[120:123], v164 offset:12288\n\t"
      "v_xor_b32_e32 v5, v5, v7\n\t"
      "v_xor_b32_e32 v0, v0, v2\n\t"
      "v_mfma_f32_16x16x32_bf16 v[60:63], v[72:75], v[80:83], v[60:63]\n\t"
      "ds_read_b64_tr_b16 v[136:137], v171 offset:32768\n\t"
      "v_xor_b32_e32 v1, v1, v3\n\t"
      "v_mfma_f32_16x16x32_bf16 v[44:47], v[64:67], v[80:83], v[44:47]\n\t"
      "ds_read_b64_tr_b16 v[138:139], v171 offset:33280\n\t"
      "s_waitcnt lgkmcnt(7)\n\t"
      "v_mfma_f32_16x16x32_bf16 v[48:51], v[96:99], v[104:107], v[48:51]\n\t"
      "ds_read_b64_tr_b16 v[76:77], v170 offset:33792\n\t"
      "ds_read_b64_tr_b16 v[78:79], v170 offset:34304\n\t"
      "v_mfma_f32_16x16x32_bf16 v[56:59], v[100:103], v[104:107], v[56:59]\n\t"
      "ds_read_b128 v[68:71], v161 offset:13312\n\t"
      "v_mfma_f32_16x16x32_bf16 v[40:43], v[92:95], v[104:107], v[40:43]\n\t"
      "ds_read_b128 v[72:75], v161 offset:27648\n\t"
      "v_mfma_f32_16x16x32_bf16 v[52:55], v[96:99], v[108:111], v[52:55]\n\t"
      "ds_read_b128 v[64:67], v165 offset:13312\n\t"
      "v_mfma_f32_16x16x32_bf16 v[60:63], v[100:103], v[108:111], v[60:63]\n\t"
      "ds_read_b64_tr_b16 v[80:81], v171 offset:33792\n\t"
      "v_mfma_f32_16x16x32_bf16 v[44:47], v[92:95], v[108:111], v[44:47]\n\t"
      "ds_read_b64_tr_b16 v[82:83], v171 offset:34304\n\t"
      "s_waitcnt lgkmcnt(7)\n\t"
      "v_mfma_f32_16x16x32_bf16 v[48:51], v[124:127], v[132:135], v[48:51]\n\t"
      "v_mfma_f32_16x16x32_bf16 v[56:59], v[128:131], v[132:135], v[56:59]\n\t"
      "v_mfma_f32_16x16x32_bf16 v[40:43], v[120:123], v[132:135], v[40:43]\n\t"
      "v_mfma_f32_16x16x32_bf16 v[52:55], v[124:127], v[136:139], v[52:55]\n\t"
      "v_mfma_f32_16x16x32_bf16 v[60:63], v[128:131], v[136:139], v[60:63]\n\t"
      "v_mfma_f32_16x16x32_bf16 v[44:47], v[120:123], v[136:139], v[44:47]\n\t"
      "s_waitcnt lgkmcnt(0)\n\t"
      "v_mfma_f32_16x16x32_bf16 v[48:51], v[68:71], v[76:79], v[48:51]\n\t"
      "v_mfma_f32_16x16x32_bf16 v[56:59], v[72:75], v[76:79], v[56:59]\n\t"
      "v_mfma_f32_16x16x32_bf16 v[40:43], v[64:67], v[76:79], v[40:43]\n\t"
      "v_mfma_f32_16x16x32_bf16 v[52:55], v[68:71], v[80:83], v[52:55]\n\t"
      "v_mfma_f32_16x16x32_bf16 v[60:63], v[72:75], v[80:83], v[60:63]\n\t"
      "v_mfma_f32_16x16x32_bf16 v[44:47], v[64:67], v[80:83], v[44:47]\n\t"
      "s_nop 2\n\t"
      "v_cvt_pk_bf16_f32 v66, v48, v49\n\t"
      "v_cvt_pk_bf16_f32 v67, v50, v51\n\t"
      "v_cvt_pk_bf16_f32 v68, v56, v57\n\t"
      "v_cvt_pk_bf16_f32 v69, v58, v59\n\t"
      "v_cvt_pk_bf16_f32 v64, v40, v41\n\t"
      "v_cvt_pk_bf16_f32 v65, v42, v43\n\t"
      "v_mfma_f32_16x16x32_bf16 v[88:91], v[66:69], v[4:7], 0\n\t"
      "s_nop 0\n\t"
      "v_mfma_f32_16x16x32_bf16 v[80:83], v[64:67], v[0:3], 0\n\t"
      "v_cvt_pk_bf16_f32 v72, v52, v53\n\t"
      "v_cvt_pk_bf16_f32 v73, v54, v55\n\t"
      "v_cvt_pk_bf16_f32 v74, v60, v61\n\t"
      "v_cvt_pk_bf16_f32 v75, v62, v63\n\t"
      "v_cvt_pk_bf16_f32 v70, v44, v45\n\t"
      "v_cvt_pk_bf16_f32 v71, v46, v47\n\t"
      "v_mfma_f32_16x16x32_bf16 v[92:95], v[72:75], v[4:7], 0\n\t"
      "s_nop 0\n\t"
      "v_mfma_f32_16x16x32_bf16 v[84:87], v[70:73], v[0:3], 0\n\t"
      "v_cvt_pk_bf16_f32 v152, v88, v89\n\t"
      "v_cvt_pk_bf16_f32 v153, v90, v91\n\t"
      "v_min_u32_e32 v152, -2, v152\n\t"
      "v_min_u32_e32 v153, -2, v153\n\t"
      "v_cvt_pk_bf16_f32 v148, v80, v81\n\t"
      "v_cvt_pk_bf16_f32 v149, v82, v83\n\t"
      "v_min_u32_e32 v148, -2, v148\n\t"
      "v_min_u32_e32 v149, -2, v149\n\t"
      "v_cvt_pk_bf16_f32 v154, v92, v93\n\t"
      "v_cvt_pk_bf16_f32 v155, v94, v95\n\t"
      "v_cndmask_b32_e64 v154, v154, 0, s[68:69]\n\t"
      "v_min_u32_e32 v154, -2, v154\n\t"
      "v_cndmask_b32_e64 v155, v155, 0, s[68:69]\n\t"
      "v_min_u32_e32 v155, -2, v155\n\t"
      "global_store_dwordx4 v240, v[152:155], s[44:45] sc1\n\t"
      "v_cvt_pk_bf16_f32 v150, v84, v85\n\t"
      "v_cvt_pk_bf16_f32 v151, v86, v87\n\t"
      "v_cndmask_b32_e64 v150, v150, 0, s[68:69]\n\t"
      "v_min_u32_e32 v150, -2, v150\n\t"
      "v_cndmask_b32_e64 v151, v151, 0, s[68:69]\n\t"
      "v_min_u32_e32 v151, -2, v151\n\t"
      "global_store_dwordx4 v240, v[148:151], s[42:43] sc1\n\t"
      "s_barrier\n\t"
      "s_cmp_eq_u32 s26, 7\n\t"
      "s_cbranch_scc1 SPARE_%=\n\t"
      "s_lshl_b32 s34, s25, 16\n\t"
      "s_lshl2_add_u32 s34, s39, s34\n\t"
      "s_add_u32 s34, s34, 4096\n\t"
      "s_add_u32 s42, s20, s34\n\t"
      "s_addc_u32 s43, s21, 0\n\t"
      "s_add_u32 s44, s42, 8192\n\t"
      "s_addc_u32 s45, s43, 0\n\t"
      "global_load_dwordx4 v[208:211], v228, s[4:5]\n\t"
      "global_load_dwordx4 v[212:215], v228, s[4:5] offset:64\n\t"
      "s_lshr_b32 s35, s2, 6\n\t"
      "s_lshl2_add_u32 s31, s26, s35\n\t"
      "s_and_b32 s35, s2, 7\n\t"
      "s_mul_i32 s34, s35, 14\n\t"
      "s_lshl4_add_u32 s35, s35, -14\n\t"
      "s_cmp_lt_u32 s31, 14\n\t"
      "s_cselect_b32 s34, s34, s35\n\t"
      "s_cselect_b32 s36, 1, 0\n\t"
      "s_or_b32 s27, s27, s36\n\t"
      "s_add_u32 s34, s34, s31\n\t"
      "s_lshr_b32 s29, s34, 2\n\t"
      "s_and_b32 s30, s34, 3\n\t"
      "s_mul_i32 s30, s30, 7\n\t"
      "v_and_b32_e32 v199, 60, v192\n\t"
      "v_and_b32_e32 v200, 24, v192\n\t"
      "v_lshlrev_b32_e32 v202, 3, v196\n\t"
      "v_lshl_or_b32 v202, v200, 2, v202\n\t"
      "v_lshl_or_b32 v202, v195, 7, v202\n\t"
      "v_lshl_or_b32 v202, v193, 10, v202\n\t"
      "v_add_u32_e32 v224, s39, v202\n\t"
      "v_xor_b32_e32 v225, 32, v224\n\t"
      "v_xor_b32_e32 v226, 64, v224\n\t"
      "v_xor_b32_e32 v227, 96, v224\n\t"
      "s_lshl_b32 s34, s24, 12\n\t"
      "s_add_u32 s34, s34, 98304\n\t"
      "v_and_b32_e32 v200, 16, v192\n\t"
      "v_lshlrev_b32_e32 v200, 3, v200\n\t"
      "v_xor_b32_e32 v241, v240, v200\n\t"
      "v_add_u32_e32 v241, s34, v241\n\t"
      "s_and_b32 s35, s24, 1\n\t"
      "s_lshl_b32 s35, s35, 7\n\t"
      "v_lshlrev_b32_e32 v199, 2, v199\n\t"
      "s_lshl_b32 s34, s24, 8\n\t"
      "v_add_u32_e32 v243, s34, v199\n\t"
      "v_xor_b32_e32 v199, s35, v199\n\t"
      "v_and_b32_e32 v200, 2, v192\n\t"
      "v_lshl_or_b32 v199, v200, 9, v199\n\t"
      "v_and_b32_e32 v202, 1, v192\n\t"
      "v_lshl_or_b32 v199, v202, 3, v199\n\t"
      "s_and_b32 s35, s34, 0x400\n\t"
      "s_barrier\n\t"
      "ds_read_b64_tr_b16 v[0:1], v216\n\t"
      "ds_read_b64_tr_b16 v[2:3], v220\n\t"
      "ds_read_b64_tr_b16 v[4:5], v217\n\t"
      "ds_read_b64_tr_b16 v[6:7], v221\n\t"
      "ds_read_b64_tr_b16 v[8:9], v218\n\t"
      "ds_read_b64_tr_b16 v[10:11], v222\n\t"
      "ds_read_b64_tr_b16 v[12:13], v219\n\t"
      "ds_read_b64_tr_b16 v[14:15], v223\n\t"
      "ds_read_b64_tr_b16 v[16:17], v218 offset:3584\n\t"
      "ds_read_b64_tr_b16 v[18:19], v222 offset:3584\n\t"
      "ds_read_b64_tr_b16 v[20:21], v219 offset:3584\n\t"
      "ds_read_b64_tr_b16 v[22:23], v223 offset:3584\n\t"
      "ds_read_b64_tr_b16 v[24:25], v216 offset:3584\n\t"
      "ds_read_b64_tr_b16 v[26:27], v220 offset:3584\n\t"
      "ds_read_b64_tr_b16 v[28:29], v217 offset:3584\n\t"
      "ds_read_b64_tr_b16 v[30:31], v221 offset:3584\n\t"
      "ds_read_b64_tr_b16 v[32:33], v216 offset:7168\n\t"
      "ds_read_b64_tr_b16 v[34:35], v220 offset:7168\n\t"
      "ds_read_b64_tr_b16 v[36:37], v217 offset:7168\n\t"
      "ds_read_b64_tr_b16 v[38:39], v221 offset:7168\n\t"
      "ds_read_b64_tr_b16 v[40:41], v218 offset:7168\n\t"
      "ds_read_b64_tr_b16 v[42:43], v222 offset:7168\n\t"
      "ds_read_b64_tr_b16 v[44:45], v219 offset:7168\n\t"
      "ds_read_b64_tr_b16 v[46:47], v223 offset:7168\n\t"
      "ds_read_b64_tr_b16 v[48:49], v218 offset:10752\n\t"
      "ds_read_b64_tr_b16 v[50:51], v222 offset:10752\n\t"
      "ds_read_b64_tr_b16 v[52:53], v219 offset:10752\n\t"
      "ds_read_b64_tr_b16 v[54:55], v223 offset:10752\n\t"
      "ds_read_b64_tr_b16 v[56:57], v216 offset:10752\n\t"
      "ds_read_b64_tr_b16 v[58:59], v220 offset:10752\n\t"
      "ds_read_b64_tr_b16 v[60:61], v217 offset:10752\n\t"
      "ds_read_b64_tr_b16 v[62:63], v221 offset:10752\n\t"
      "ds_read_b64_tr_b16 v[64:65], v216 offset:14336\n\t"
      "ds_read_b64_tr_b16 v[66:67], v220 offset:14336\n\t"
      "ds_read_b64_tr_b16 v[68:69], v217 offset:14336\n\t"
      "ds_read_b64_tr_b16 v[70:71], v221 offset:14336\n\t"
      "ds_read_b64_tr_b16 v[72:73], v218 offset:14336\n\t"
      "ds_read_b64_tr_b16 v[74:75], v222 offset:14336\n\t"
      "ds_read_b64_tr_b16 v[76:77], v219 offset:14336\n\t"
      "ds_read_b64_tr_b16 v[78:79], v223 offset:14336\n\t"
      "ds_read_b64_tr_b16 v[80:81], v218 offset:17920\n\t"
      "ds_read_b64_tr_b16 v[82:83], v222 offset:17920\n\t"
      "ds_read_b64_tr_b16 v[84:85], v219 offset:17920\n\t"
      "ds_read_b64_tr_b16 v[86:87], v223 offset:17920\n\t"
      "ds_read_b64_tr_b16 v[88:89], v216 offset:17920\n\t"
      "ds_read_b64_tr_b16 v[90:91], v220 offset:17920\n\t"
      "ds_read_b64_tr_b16 v[92:93], v217 offset:17920\n\t"
      "ds_read_b64_tr_b16 v[94:95], v221 offset:17920\n\t"
      "ds_read_b64_tr_b16 v[96:97], v216 offset:21504\n\t"
      "ds_read_b64_tr_b16 v[98:99], v220 offset:21504\n\t"
      "ds_read_b64_tr_b16 v[100:101], v217 offset:21504\n\t"
      "ds_read_b64_tr_b16 v[102:103], v221 offset:21504\n\t"
      "ds_read_b64_tr_b16 v[104:105], v218 offset:21504\n\t"
      "ds_read_b64_tr_b16 v[106:107], v222 offset:21504\n\t"
      "ds_read_b64_tr_b16 v[108:109], v219 offset:21504\n\t"
      "ds_read_b64_tr_b16 v[110:111], v223 offset:21504\n\t"
      "ds_read_b64_tr_b16 v[112:113], v218 offset:25088\n\t"
      "ds_read_b64_tr_b16 v[114:115], v222 offset:25088\n\t"
      "ds_read_b64_tr_b16 v[116:117], v219 offset:25088\n\t"
      "ds_read_b64_tr_b16 v[118:119], v223 offset:25088\n\t"
      "ds_read_b64_tr_b16 v[120:121], v216 offset:25088\n\t"
      "ds_read_b64_tr_b16 v[122:123], v220 offset:25088\n\t"
      "ds_read_b64_tr_b16 v[124:125], v217 offset:25088\n\t"
      "ds_read_b64_tr_b16 v[126:127], v221 offset:25088\n\t"
      "global_load_dwordx4 v[128:131], v240, s[42:43] offset:-4096 sc1\n\t"
      "global_load_dwordx4 v[132:135], v240, s[42:43] offset:-3072 sc1\n\t"
      "global_load_dwordx4 v[136:139], v240, s[42:43] offset:-2048 sc1\n\t"
      "global_load_dwordx4 v[140:143], v240, s[42:43] offset:-1024 sc1\n\t"
      "global_load_dwordx4 v[144:147], v240, s[42:43] sc1\n\t"
      "global_load_dwordx4 v[148:151], v240, s[42:43] offset:1024 sc1\n\t"
      "global_load_dwordx4 v[152:155], v240, s[42:43] offset:2048 sc1\n\t"
      "global_load_dwordx4 v[156:159], v240, s[42:43] offset:3072 sc1\n\t"
      "global_load_dwordx4 v[160:163], v240, s[44:45] offset:-4096 sc1\n\t"
      "global_load_dwordx4 v[164:167], v240, s[44:45] offset:-3072 sc1\n\t"
      "s_add_u32 s34, s34, s35\n\t"
      "s_add_u32 s34, s34, 98304\n\t"
      "v_add_u32_e32 v242, s34, v199\n\t"
      "v_lshlrev_b32_e32 v199, 2, v202\n\t"
      "v_lshl_or_b32 v199, v200, 4, v199\n\t"
      "s_min_u32 s34, s29, 27\n\t"
      "s_lshl_b32 s34, s34, 7\n\t"
      "s_lshr_b32 s35, s24, 2\n\t"
      "s_lshl_b32 s35, s35, 6\n\t"
      "s_add_u32 s34, s34, s35\n\t"
      "s_lshr_b32 s35, s39, 9\n\t"
      "s_add_u32 s34, s34, s35\n\t"
      "v_add_u32_e32 v244, s34, v199\n\t"
      "s_add_u32 s48, s20, 8388608\n\t"
      "s_addc_u32 s49, s21, 0\n\t"
      "v_and_b32_e32 v199, 16, v192\n\t"
      "v_lshlrev_b32_e32 v199, 1, v199\n\t"
      "v_and_b32_e32 v200, 32, v192\n\t"
      "s_lshl_b32 s34, s28, 18\n\t"
      "s_lshl_b32 s35, s25, 11\n\t"
      "s_add_u32 s34, s34, s35\n\t"
      "s_lshr_b32 s35, s39, 3\n\t"
      "s_add_u32 s34, s34, s35\n\t"
      "v_and_b32_e32 v201, 0x1f0, v240\n\t"
      "v_lshl_or_b32 v201, v200, 11, v201\n\t"
      "v_add_u32_e32 v229, s34, v201\n\t"
      "v_add_u32_e32 v230, 131072, v229\n\t"
      "s_and_b32 s34, s25, 30\n\t"
      "s_lshr_b32 s35, s39, 13\n\t"
      "s_add_u32 s34, s34, s35\n\t"
      "s_mul_i32 s34, s34, 7\n\t"
      "s_add_u32 s34, s34, s28\n\t"
      "s_lshl_b32 s34, s34, 3\n\t"
      "s_and_b32 s35, s25, 1\n\t"
      "s_add_u32 s34, s34, s35\n\t"
      "s_lshl_b32 s34, s34, 10\n\t"
      "s_and_b32 s35, s24, 1\n\t"
      "s_lshl_b32 s35, s35, 9\n\t"
      "s_add_u32 s34, s34, s35\n\t"
      "s_add_u32 s34, s34, 6293504\n\t"
      "v_lshrrev_b32_e32 v201, 1, v200\n\t"
      "v_lshl_or_b32 v201, v199, 6, v201\n\t"
      "v_lshl_or_b32 v201, v194, 5, v201\n\t"
      "v_add_u32_e32 v250, s34, v201\n\t"
      "s_lshl_b32 s34, s24, 12\n\t"
      "s_add_u32 s34, s34, 131072\n\t"
      "v_xor_b32_e32 v201, v193, v194\n\t"
      "v_lshlrev_b32_e32 v201, 3, v201\n\t"
      "v_lshl_or_b32 v201, v194, 7, v201\n\t"
      "v_add_u32_e32 v233, s34, v201\n\t"
      "v_lshrrev_b32_e32 v199, 2, v199\n\t"
      "v_or_b32_e32 v199, v199, v195\n\t"
      "v_lshrrev_b32_e32 v200, 3, v200\n\t"
      "v_or_b32_e32 v200, v196, v200\n\t"
      "v_xor_b32_e32 v200, v200, v199\n\t"
      "v_lshlrev_b32_e32 v200, 3, v200\n\t"
      "v_lshl_or_b32 v200, v199, 7, v200\n\t"
      "v_add_u32_e32 v246, s34, v200\n\t"
      "v_xor_b32_e32 v234, 32, v233\n\t"
      "v_xor_b32_e32 v247, 32, v246\n\t"
      "v_xor_b32_e32 v235, 64, v233\n\t"
      "v_xor_b32_e32 v248, 64, v246\n\t"
      "v_xor_b32_e32 v236, 96, v233\n\t"
      "v_xor_b32_e32 v249, 96, v246\n\t"
      "s_waitcnt vmcnt(10)\n\t"
      "v_permlane16_swap_b32_e32 v208, v210\n\t"
      "v_permlane16_swap_b32_e32 v209, v211\n\t"
      "v_permlane16_swap_b32_e32 v212, v214\n\t"
      "v_permlane16_swap_b32_e32 v213, v215\n\t"
      "v_lshlrev_b32_e32 v192, 16, v208\n\t"
      "v_and_b32_e32 v193, 0xffff0000, v208\n\t"
      "v_lshlrev_b32_e32 v194, 16, v209\n\t"
      "v_and_b32_e32 v195, 0xffff0000, v209\n\t"
      "v_lshlrev_b32_e32 v196, 16, v210\n\t"
      "v_and_b32_e32 v197, 0xffff0000, v210\n\t"
      "v_lshlrev_b32_e32 v198, 16, v211\n\t"
      "v_and_b32_e32 v199, 0xffff0000, v211\n\t"
      "v_lshlrev_b32_e32 v200, 16, v212\n\t"
      "v_and_b32_e32 v201, 0xffff0000, v212\n\t"
      "v_lshlrev_b32_e32 v202, 16, v213\n\t"
      "v_and_b32_e32 v203, 0xffff0000, v213\n\t"
      "v_lshlrev_b32_e32 v204, 16, v214\n\t"
      "v_and_b32_e32 v205, 0xffff0000, v214\n\t"
      "v_lshlrev_b32_e32 v206, 16, v215\n\t"
      "v_and_b32_e32 v207, 0xffff0000, v215\n\t"
      "global_load_dwordx4 v[168:171], v240, s[44:45] offset:-2048 sc1\n\t"
      "global_load_dwordx4 v[172:175], v240, s[44:45] offset:-1024 sc1\n\t"
      "global_load_dwordx4 v[176:179], v240, s[44:45] sc1\n\t"
      "global_load_dwordx4 v[180:183], v240, s[44:45] offset:1024 sc1\n\t"
      "global_load_dwordx4 v[184:187], v240, s[44:45] offset:2048 sc1\n\t"
      "global_load_dwordx4 v[188:191], v240, s[44:45] offset:3072 sc1\n\t"
      "global_load_dword v245, v244, s[12:13]\n\t"
      "s_barrier\n\t"
      "s_waitcnt lgkmcnt(0)\n\t"
      "s_waitcnt vmcnt(15)\n\t"
      "v_max3_u32 v232, v128, v129, v130\n\t"
      "v_max3_u32 v232, v232, v131, v132\n\t"
      "v_max3_u32 v232, v232, v133, v134\n\t"
      "v_max_u32_e32 v232, v232, v135\n\t"
      "v_cmp_eq_u32_e32 vcc, -1, v232\n\t"
      "s_cbranch_vccnz LO0R_%=\n\t"
      "LO0B_%=:\n\t"
      "v_mfma_f32_16x16x32_bf16 v[192:195], v[0:3], v[128:131], v[192:195]\n\t"
      "ds_read_b64_tr_b16 v[0:1], v216 offset:28672\n\t"
      "ds_read_b64_tr_b16 v[2:3], v220 offset:28672\n\t"
      "v_mfma_f32_16x16x32_bf16 v[196:199], v[4:7], v[128:131], v[196:199]\n\t"
      "ds_read_b64_tr_b16 v[4:5], v217 offset:28672\n\t"
      "v_mfma_f32_16x16x32_bf16 v[200:203], v[8:11], v[128:131], v[200:203]\n\t"
      "ds_read_b64_tr_b16 v[6:7], v221 offset:28672\n\t"
      "v_mfma_f32_16x16x32_bf16 v[204:207], v[12:15], v[128:131], v[204:207]\n\t"
      "ds_read_b64_tr_b16 v[8:9], v218 offset:28672\n\t"
      "ds_read_b64_tr_b16 v[10:11], v222 offset:28672\n\t"
      "v_mfma_f32_16x16x32_bf16 v[192:195], v[16:19], v[132:135], v[192:195]\n\t"
      "ds_read_b64_tr_b16 v[12:13], v219 offset:28672\n\t"
      "v_mfma_f32_16x16x32_bf16 v[196:199], v[20:23], v[132:135], v[196:199]\n\t"
      "ds_read_b64_tr_b16 v[14:15], v223 offset:28672\n\t"
      "v_mfma_f32_16x16x32_bf16 v[200:203], v[24:27], v[132:135], v[200:203]\n\t"
      "ds_read_b64_tr_b16 v[16:17], v218 offset:32256\n\t"
      "ds_read_b64_tr_b16 v[18:19], v222 offset:32256\n\t"
      "v_mfma_f32_16x16x32_bf16 v[204:207], v[28:31], v[132:135], v[204:207]\n\t"
      "ds_read_b64_tr_b16 v[20:21], v219 offset:32256\n\t"
      "s_waitcnt vmcnt(13)\n\t"
      "v_max3_u32 v232, v136, v137, v138\n\t"
      "v_max3_u32 v232, v232, v139, v140\n\t"
      "v_max3_u32 v232, v232, v141, v142\n\t"
      "v_max_u32_e32 v232, v232, v143\n\t"
      "v_cmp_eq_u32_e32 vcc, -1, v232\n\t"
      "s_cbranch_vccnz LO2R_%=\n\t"
      "LO2B_%=:\n\t"
      "v_mfma_f32_16x16x32_bf16 v[192:195], v[32:35], v[136:139], v[192:195]\n\t"
      "ds_read_b64_tr_b16 v[22:23], v223 offset:32256\n\t"
      "v_mfma_f32_16x16x32_bf16 v[196:199], v[36:39], v[136:139], v[196:199]\n\t"
      "ds_read_b64_tr_b16 v[24:25], v216 offset:32256\n\t"
      "ds_read_b64_tr_b16 v[26:27], v220 offset:32256\n\t"
      "v_mfma_f32_16x16x32_bf16 v[200:203], v[40:43], v[136:139], v[200:203]\n\t"
      "ds_read_b64_tr_b16 v[28:29], v217 offset:32256\n\t"
      "v_mfma_f32_16x16x32_bf16 v[204:207], v[44:47], v[136:139], v[204:207]\n\t"
      "ds_read_b64_tr_b16 v[30:31], v221 offset:32256\n\t"
      "v_mfma_f32_16x16x32_bf16 v[192:195], v[48:51], v[140:143], v[192:195]\n\t"
      "ds_read_b64_tr_b16 v[32:33], v216 offset:35840\n\t"
      "ds_read_b64_tr_b16 v[34:35], v220 offset:35840\n\t"
      "v_mfma_f32_16x16x32_bf16 v[196:199], v[52:55], v[140:143], v[196:199]\n\t"
      "ds_read_b64_tr_b16 v[36:37], v217 offset:35840\n\t"
      "v_mfma_f32_16x16x32_bf16 v[200:203], v[56:59], v[140:143], v[200:203]\n\t"
      "ds_read_b64_tr_b16 v[38:39], v221 offset:35840\n\t"
      "v_mfma_f32_16x16x32_bf16 v[204:207], v[60:63], v[140:143], v[204:207]\n\t"
      "ds_read_b64_tr_b16 v[40:41], v218 offset:35840\n\t"
      "ds_read_b64_tr_b16 v[42:43], v222 offset:35840\n\t"
      "s_waitcnt vmcnt(11)\n\t"
      "v_max3_u32 v232, v144, v145, v146\n\t"
      "v_max3_u32 v232, v232, v147, v148\n\t"
      "v_max3_u32 v232, v232, v149, v150\n\t"
      "v_max_u32_e32 v232, v232, v151\n\t"
      "v_cmp_eq_u32_e32 vcc, -1, v232\n\t"
      "s_cbranch_vccnz LO4R_%=\n\t"
      "LO4B_%=:\n\t"
      "v_mfma_f32_16x16x32_bf16 v[192:195], v[64:67], v[144:147], v[192:195]\n\t"
      "ds_read_b64_tr_b16 v[44:45], v219 offset:35840\n\t"
      "v_mfma_f32_16x16x32_bf16 v[196:199], v[68:71], v[144:147], v[196:199]\n\t"
      "ds_read_b64_tr_b16 v[46:47], v223 offset:35840\n\t"
      "v_mfma_f32_16x16x32_bf16 v[200:203], v[72:75], v[144:147], v[200:203]\n\t"
      "ds_read_b64_tr_b16 v[48:49], v218 offset:39424\n\t"
      "ds_read_b64_tr_b16 v[50:51], v222 offset:39424\n\t"
      "v_mfma_f32_16x16x32_bf16 v[204:207], v[76:79], v[144:147], v[204:207]\n\t"
      "ds_read_b64_tr_b16 v[52:53], v219 offset:39424\n\t"
      "v_mfma_f32_16x16x32_bf16 v[192:195], v[80:83], v[148:151], v[192:195]\n\t"
      "ds_read_b64_tr_b16 v[54:55], v223 offset:39424\n\t"
      "v_mfma_f32_16x16x32_bf16 v[196:199], v[84:87], v[148:151], v[196:199]\n\t"
      "ds_read_b64_tr_b16 v[56:57], v216 offset:39424\n\t"
      "ds_read_b64_tr_b16 v[58:59], v220 offset:39424\n\t"
      "v_mfma_f32_16x16x32_bf16 v[200:203], v[88:91], v[148:151], v[200:203]\n\t"
      "ds_read_b64_tr_b16 v[60:61], v217 offset:39424\n\t"
      "v_mfma_f32_16x16x32_bf16 v[204:207], v[92:95], v[148:151], v[204:207]\n\t"
      "ds_read_b64_tr_b16 v[62:63], v221 offset:39424\n\t"
      "s_waitcnt vmcnt(9)\n\t"
      "v_max3_u32 v232, v152, v153, v154\n\t"
      "v_max3_u32 v232, v232, v155, v156\n\t"
      "v_max3_u32 v232, v232, v157, v158\n\t"
      "v_max_u32_e32 v232, v232, v159\n\t"
      "v_cmp_eq_u32_e32 vcc, -1, v232\n\t"
      "s_cbranch_vccnz LO6R_%=\n\t"
      "LO6B_%=:\n\t"
      "v_mfma_f32_16x16x32_bf16 v[192:195], v[96:99], v[152:155], v[192:195]\n\t"
      "ds_read_b64_tr_b16 v[64:65], v216 offset:43008\n\t"
      "ds_read_b64_tr_b16 v[66:67], v220 offset:43008\n\t"
      "v_mfma_f32_16x16x32_bf16 v[196:199], v[100:103], v[152:155], v[196:199]\n\t"
      "ds_read_b64_tr_b16 v[68:69], v217 offset:43008\n\t"
      "v_mfma_f32_16x16x32_bf16 v[200:203], v[104:107], v[152:155], v[200:203]\n\t"
      "ds_read_b64_tr_b16 v[70:71], v221 offset:43008\n\t"
      "v_mfma_f32_16x16x32_bf16 v[204:207], v[108:111], v[152:155], v[204:207]\n\t"
      "ds_read_b64_tr_b16 v[72:73], v218 offset:43008\n\t"
      "ds_read_b64_tr_b16 v[74:75], v222 offset:43008\n\t"
      "v_mfma_f32_16x16x32_bf16 v[192:195], v[112:115], v[156:159], v[192:195]\n\t"
      "ds_read_b64_tr_b16 v[76:77], v219 offset:43008\n\t"
      "v_mfma_f32_16x16x32_bf16 v[196:199], v[116:119], v[156:159], v[196:199]\n\t"
      "ds_read_b64_tr_b16 v[78:79], v223 offset:43008\n\t"
      "v_mfma_f32_16x16x32_bf16 v[200:203], v[120:123], v[156:159], v[200:203]\n\t"
      "ds_read_b64_tr_b16 v[80:81], v218 offset:46592\n\t"
      "ds_read_b64_tr_b16 v[82:83], v222 offset:46592\n\t"
      "v_mfma_f32_16x16x32_bf16 v[204:207], v[124:127], v[156:159], v[204:207]\n\t"
      "ds_read_b64_tr_b16 v[84:85], v219 offset:46592\n\t"
      "s_waitcnt vmcnt(7)\n\t"
      "v_max3_u32 v232, v160, v161, v162\n\t"
      "v_max3_u32 v232, v232, v163, v164\n\t"
      "v_max3_u32 v232, v232, v165, v166\n\t"
      "v_max_u32_e32 v232, v232, v167\n\t"
      "v_cmp_eq_u32_e32 vcc, -1, v232\n\t"
      "s_cbranch_vccnz LO8R_%=\n\t"
      "LO8B_%=:\n\t"
      "s_waitcnt lgkmcnt(14)\n\t"
      "v_mfma_f32_16x16x32_bf16 v[192:195], v[0:3], v[160:163], v[192:195]\n\t"
      "ds_read_b64_tr_b16 v[86:87], v223 offset:46592\n\t"
      "v_mfma_f32_16x16x32_bf16 v[196:199], v[4:7], v[160:163], v[196:199]\n\t"
      "ds_read_b64_tr_b16 v[88:89], v216 offset:46592\n\t"
      "ds_read_b64_tr_b16 v[90:91], v220 offset:46592\n\t"
      "v_mfma_f32_16x16x32_bf16 v[200:203], v[8:11], v[160:163], v[200:203]\n\t"
      "ds_read_b64_tr_b16 v[92:93], v217 offset:46592\n\t"
      "v_mfma_f32_16x16x32_bf16 v[204:207], v[12:15], v[160:163], v[204:207]\n\t"
      "ds_read_b64_tr_b16 v[94:95], v221 offset:46592\n\t"
      "v_mfma_f32_16x16x32_bf16 v[192:195], v[16:19], v[164:167], v[192:195]\n\t"
      "ds_read_b64_tr_b16 v[96:97], v216 offset:50176\n\t"
      "ds_read_b64_tr_b16 v[98:99], v220 offset:50176\n\t"
      "v_mfma_f32_16x16x32_bf16 v[196:199], v[20:23], v[164:167], v[196:199]\n\t"
      "ds_read_b64_tr_b16 v[100:101], v217 offset:50176\n\t"
      "v_mfma_f32_16x16x32_bf16 v[200:203], v[24:27], v[164:167], v[200:203]\n\t"
      "ds_read_b64_tr_b16 v[102:103], v221 offset:50176\n\t"
      "v_mfma_f32_16x16x32_bf16 v[204:207], v[28:31], v[164:167], v[204:207]\n\t"
      "ds_read_b64_tr_b16 v[104:105], v218 offset:50176\n\t"
      "ds_read_b64_tr_b16 v[106:107], v222 offset:50176\n\t"
      "s_waitcnt vmcnt(5)\n\t"
      "v_max3_u32 v232, v168, v169, v170\n\t"
      "v_max3_u32 v232, v232, v171, v172\n\t"
      "v_max3_u32 v232, v232, v173, v174\n\t"
      "v_max_u32_e32 v232, v232, v175\n\t"
      "v_cmp_eq_u32_e32 vcc, -1, v232\n\t"
      "s_cbranch_vccnz LO10R_%=\n\t"
      "LO10B_%=:\n\t"
      "v_mfma_f32_16x16x32_bf16 v[192:195], v[32:35], v[168:171], v[192:195]\n\t"
      "ds_read_b64_tr_b16 v[108:109], v219 offset:50176\n\t"
      "v_mfma_f32_16x16x32_bf16 v[196:199], v[36:39], v[168:171], v[196:199]\n\t"
      "ds_read_b64_tr_b16 v[110:111], v223 offset:50176\n\t"
      "v_mfma_f32_16x16x32_bf16 v[200:203], v[40:43], v[168:171], v[200:203]\n\t"
      "ds_read_b64_tr_b16 v[112:113], v218 offset:53760\n\t"
      "ds_read_b64_tr_b16 v[114:115], v222 offset:53760\n\t"
      "v_mfma_f32_16x16x32_bf16 v[204:207], v[44:47], v[168:171], v[204:207]\n\t"
      "ds_read_b64_tr_b16 v[116:117], v219 offset:53760\n\t"
      "s_waitcnt lgkmcnt(14)\n\t"
      "v_mfma_f32_16x16x32_bf16 v[192:195], v[48:51], v[172:175], v[192:195]\n\t"
      "ds_read_b64_tr_b16 v[118:119], v223 offset:53760\n\t"
      "v_mfma_f32_16x16x32_bf16 v[196:199], v[52:55], v[172:175], v[196:199]\n\t"
      "ds_read_b64_tr_b16 v[120:121], v216 offset:53760\n\t"
      "ds_read_b64_tr_b16 v[122:123], v220 offset:53760\n\t"
      "v_mfma_f32_16x16x32_bf16 v[200:203], v[56:59], v[172:175], v[200:203]\n\t"
      "ds_read_b64_tr_b16 v[124:125], v217 offset:53760\n\t"
      "v_mfma_f32_16x16x32_bf16 v[204:207], v[60:63], v[172:175], v[204:207]\n\t"
      "ds_read_b64_tr_b16 v[126:127], v221 offset:53760\n\t"
      "s_waitcnt vmcnt(3)\n\t"
      "v_max3_u32 v232, v176, v177, v178\n\t"
      "v_max3_u32 v232, v232, v179, v180\n\t"
      "v_max3_u32 v232, v232, v181, v182\n\t"
      "v_max_u32_e32 v232, v232, v183\n\t"
      "v_cmp_eq_u32_e32 vcc, -1, v232\n\t"
      "s_cbranch_vccnz LO12R_%=\n\t"
      "LO12B_%=:\n\t"
      "v_mfma_f32_16x16x32_bf16 v[192:195], v[64:67], v[176:179], v[192:195]\n\t"
      "v_mfma_f32_16x16x32_bf16 v[196:199], v[68:71], v[176:179], v[196:199]\n\t"
      "v_mfma_f32_16x16x32_bf16 v[200:203], v[72:75], v[176:179], v[200:203]\n\t"
      "v_mfma_f32_16x16x32_bf16 v[204:207], v[76:79], v[176:179], v[204:207]\n\t"
      "s_waitcnt lgkmcnt(14)\n\t"
      "v_mfma_f32_16x16x32_bf16 v[192:195], v[80:83], v[180:183], v[192:195]\n\t"
      "v_mfma_f32_16x16x32_bf16 v[196:199], v[84:87], v[180:183], v[196:199]\n\t"
      "v_mfma_f32_16x16x32_bf16 v[200:203], v[88:91], v[180:183], v[200:203]\n\t"
      "v_mfma_f32_16x16x32_bf16 v[204:207], v[92:95], v[180:183], v[204:207]\n\t"
      "s_waitcnt vmcnt(1)\n\t"
      "v_max3_u32 v232, v184, v185, v186\n\t"
      "v_max3_u32 v232, v232, v187, v188\n\t"
      "v_max3_u32 v232, v232, v189, v190\n\t"
      "v_max_u32_e32 v232, v232, v191\n\t"
      "v_cmp_eq_u32_e32 vcc, -1, v232\n\t"
      "s_cbranch_vccnz LO14R_%=\n\t"
      "LO14B_%=:\n\t"
      "s_waitcnt lgkmcnt(8)\n\t"
      "v_mfma_f32_16x16x32_bf16 v[192:195], v[96:99], v[184:187], v[192:195]\n\t"
      "v_mfma_f32_16x16x32_bf16 v[196:199], v[100:103], v[184:187], v[196:199]\n\t"
      "v_mfma_f32_16x16x32_bf16 v[200:203], v[104:107], v[184:187], v[200:203]\n\t"
      "v_mfma_f32_16x16x32_bf16 v[204:207], v[108:111], v[184:187], v[204:207]\n\t"
      "s_waitcnt lgkmcnt(0)\n\t"
      "v_mfma_f32_16x16x32_bf16 v[192:195], v[112:115], v[188:191], v[192:195]\n\t"
      "v_mfma_f32_16x16x32_bf16 v[196:199], v[116:119], v[188:191], v[196:199]\n\t"
      "v_mfma_f32_16x16x32_bf16 v[200:203], v[120:123], v[188:191], v[200:203]\n\t"
      "v_mfma_f32_16x16x32_bf16 v[204:207], v[124:127], v[188:191], v[204:207]\n\t"
      "s_nop 4\n\t"
      "v_cvt_pk_bf16_f32 v208, v192, v193\n\t"
      "v_cvt_pk_bf16_f32 v209, v194, v195\n\t"
      "v_cvt_pk_bf16_f32 v210, v196, v197\n\t"
      "v_cvt_pk_bf16_f32 v211, v198, v199\n\t"
      "v_cvt_pk_bf16_f32 v212, v200, v201\n\t"
      "v_cvt_pk_bf16_f32 v213, v202, v203\n\t"
      "v_cvt_pk_bf16_f32 v214, v204, v205\n\t"
      "v_cvt_pk_bf16_f32 v215, v206, v207\n\t"
      "v_mov_b32_e32 v148, 0x3f803f80\n\t"
      "v_dot2_f32_bf16 v144, v208, v148, 0\n\t"
      "v_dot2_f32_bf16 v145, v208, v208, 0\n\t"
      "v_dot2_f32_bf16 v146, v209, v148, 0\n\t"
      "v_dot2_f32_bf16 v147, v209, v209, 0\n\t"
      "v_dot2_f32_bf16 v144, v210, v148, v144\n\t"
      "v_dot2_f32_bf16 v145, v210, v210, v145\n\t"
      "v_dot2_f32_bf16 v146, v211, v148, v146\n\t"
      "v_dot2_f32_bf16 v147, v211, v211, v147\n\t"
      "v_dot2_f32_bf16 v144, v212, v148, v144\n\t"
      "v_dot2_f32_bf16 v145, v212, v212, v145\n\t"
      "v_dot2_f32_bf16 v146, v213, v148, v146\n\t"
      "v_dot2_f32_bf16 v147, v213, v213, v147\n\t"
      "v_dot2_f32_bf16 v144, v214, v148, v144\n\t"
      "v_dot2_f32_bf16 v145, v214, v214, v145\n\t"
      "v_dot2_f32_bf16 v146, v215, v148, v146\n\t"
      "v_dot2_f32_bf16 v147, v215, v215, v147\n\t"
      "v_lshlrev_b32_e32 v128, 16, v208\n\t"
      "v_and_b32_e32 v129, 0xffff0000, v208\n\t"
      "v_lshlrev_b32_e32 v130, 16, v209\n\t"
      "v_pk_add_f32 v[144:145], v[144:145], v[146:147]\n\t"
      "v_and_b32_e32 v131, 0xffff0000, v209\n\t"
      "v_lshlrev_b32_e32 v132, 16, v210\n\t"
      "v_permlane16_swap_b32_e32 v144, v145\n\t"
      "v_add_f32_e32 v144, v144, v145\n\t"
      "v_mov_b32_e32 v145, v144\n\t"
      "v_and_b32_e32 v133, 0xffff0000, v210\n\t"
      "v_lshlrev_b32_e32 v134, 16, v211\n\t"
      "v_permlane32_swap_b32_e32 v144, v145\n\t"
      "v_add_f32_e32 v144, v144, v145\n\t"
      "v_mov_b32_e32 v145, v144\n\t"
      "v_and_b32_e32 v135, 0xffff0000, v211\n\t"
      "v_lshlrev_b32_e32 v136, 16, v212\n\t"
      "v_permlane16_swap_b32_e32 v144, v145\n\t"
      "s_mov_b64 exec, s[70:71]\n\t"
      "v_min_u32_e32 v144, -2, v144\n\t"
      "v_min_u32_e32 v145, -2, v145\n\t"
      "global_store_dwordx2 v231, v[144:145], s[58:59] sc1\n\t"
      "s_mov_b64 exec, -1\n\t"
      "s_barrier\n\t"
      "v_and_b32_e32 v137, 0xffff0000, v212\n\t"
      "v_lshlrev_b32_e32 v138, 16, v213\n\t"
      "v_and_b32_e32 v139, 0xffff0000, v213\n\t"
      "v_lshlrev_b32_e32 v140, 16, v214\n\t"
      "v_and_b32_e32 v141, 0xffff0000, v214\n\t"
      "v_lshlrev_b32_e32 v142, 16, v215\n\t"
      "v_and_b32_e32 v143, 0xffff0000, v215\n\t"
      "v_permlane16_swap_b32_e32 v208, v210\n\t"
      "v_permlane16_swap_b32_e32 v209, v211\n\t"
      "v_permlane16_swap_b32_e32 v212, v214\n\t"
      "v_permlane16_swap_b32_e32 v213, v215\n\t"
      "global_store_dwordx4 v250, v[208:211], s[20:21] offset:-2048 sc1\n\t"
      "global_store_dwordx4 v250, v[212:215], s[20:21] offset:2048 sc1\n\t"
      "s_bitcmp0_b32 s27, 0\n\t"
      "s_cbranch_scc1 NTK0_%=\n\t"
      "ds_read_b64_tr_b16 v[0:1], v224 offset:16384\n\t"
      "ds_read_b64_tr_b16 v[2:3], v224 offset:16896\n\t"
      "ds_read_b64_tr_b16 v[4:5], v225 offset:16384\n\t"
      "ds_read_b64_tr_b16 v[6:7], v225 offset:16896\n\t"
      "ds_read_b64_tr_b16 v[8:9], v226 offset:16384\n\t"
      "ds_read_b64_tr_b16 v[10:11], v226 offset:16896\n\t"
      "ds_read_b64_tr_b16 v[12:13], v227 offset:16384\n\t"
      "ds_read_b64_tr_b16 v[14:15], v227 offset:16896\n\t"
      "ds_read_b64_tr_b16 v[16:17], v224 offset:49152\n\t"
      "ds_read_b64_tr_b16 v[18:19], v224 offset:49664\n\t"
      "ds_read_b64_tr_b16 v[20:21], v225 offset:49152\n\t"
      "ds_read_b64_tr_b16 v[22:23], v225 offset:49664\n\t"
      "ds_read_b64_tr_b16 v[24:25], v226 offset:49152\n\t"
      "ds_read_b64_tr_b16 v[26:27], v226 offset:49664\n\t"
      "ds_read_b64_tr_b16 v[28:29], v227 offset:49152\n\t"
      "ds_read_b64_tr_b16 v[30:31], v227 offset:49664\n\t"
      "ds_read_b64_tr_b16 v[32:33], v224\n\t"
      "ds_read_b64_tr_b16 v[34:35], v224 offset:512\n\t"
      "ds_read_b64_tr_b16 v[36:37], v225\n\t"
      "ds_read_b64_tr_b16 v[38:39], v225 offset:512\n\t"
      "ds_read_b64_tr_b16 v[40:41], v226\n\t"
      "ds_read_b64_tr_b16 v[42:43], v226 offset:512\n\t"
      "ds_read_b64_tr_b16 v[44:45], v227\n\t"
      "ds_read_b64_tr_b16 v[46:47], v227 offset:512\n\t"
      "ds_read_b64_tr_b16 v[48:49], v224 offset:32768\n\t"
      "ds_read_b64_tr_b16 v[50:51], v224 offset:33280\n\t"
      "ds_read_b64_tr_b16 v[52:53], v225 offset:32768\n\t"
      "ds_read_b64_tr_b16 v[54:55], v225 offset:33280\n\t"
      "ds_read_b64_tr_b16 v[56:57], v226 offset:32768\n\t"
      "ds_read_b64_tr_b16 v[58:59], v226 offset:33280\n\t"
      "ds_read_b64_tr_b16 v[60:61], v227 offset:32768\n\t"
      "ds_read_b64_tr_b16 v[62:63], v227 offset:33280\n\t"
      "NTK0_%=:\n\t"
      "global_load_dwordx2 v[148:149], v231, s[46:47] sc1\n\t"
      "global_load_dwordx2 v[150:151], v231, s[46:47] offset:384 sc1\n\t"
      "s_waitcnt lgkmcnt(0)\n\t"
      "s_barrier\n\t"
      "s_waitcnt vmcnt(0)\n\t"
      "v_max3_u32 v232, v148, v149, v150\n\t"
      "v_max_u32_e32 v232, v232, v151\n\t"
      "v_cmp_eq_u32_e32 vcc, -1, v232\n\t"
      "s_cbranch_vccnz LSR_%=\n\t"
      "LSB_%=:\n\t"
      "v_cndmask_b32_e64 v150, v150, 0, s[70:71]\n\t"
      "v_cndmask_b32_e64 v151, v151, 0, s[70:71]\n\t"
      "v_pk_add_f32 v[144:145], v[148:149], v[150:151]\n\t"
      "s_nop 1\n\t"
      "v_permlane16_swap_b32_e32 v144, v145\n\t"
      "v_add_f32_e32 v144, v144, v145\n\t"
      "v_mov_b32_e32 v145, v144\n\t"
      "s_nop 1\n\t"
      "v_permlane32_swap_b32_e32 v144, v145\n\t"
      "v_add_f32_e32 v144, v144, v145\n\t"
      "v_mov_b32_e32 v145, v144\n\t"
      "s_nop 1\n\t"
      "v_permlane16_swap_b32_e32 v144, v145\n\t"
      "v_mul_f32_e32 v144, 0xbb124925, v144\n\t"
      "v_mul_f32_e32 v145, 0x3b124925, v145\n\t"
      "v_fma_f32 v145, -v144, v144, v145\n\t"
      "v_max_f32_e32 v145, 0, v145\n\t"
      "v_add_f32_e32 v146, 0x3727c5ac, v145\n\t"
      "v_fmac_f32_e32 v145, 0x3727c5ac, v146\n\t"
      "v_rsq_f32_e32 v145, v145\n\t"
      "s_nop 0\n\t"
      "v_mul_f32_e32 v144, v144, v145\n\t"
      "v_pk_fma_f32 v[128:129], v[128:129], v[144:145], v[144:145] op_sel:[0,1,0] op_sel_hi:[1,1,0]\n\t"
      "v_pk_fma_f32 v[130:131], v[130:131], v[144:145], v[144:145] op_sel:[0,1,0] op_sel_hi:[1,1,0]\n\t"
      "v_cvt_pk_bf16_f32 v152, v128, v129\n\t"
      "v_cvt_pk_bf16_f32 v153, v130, v131\n\t"
      "ds_write_b64 v233, v[152:153]\n\t"
      "v_pk_fma_f32 v[132:133], v[132:133], v[144:145], v[144:145] op_sel:[0,1,0] op_sel_hi:[1,1,0]\n\t"
      "v_pk_fma_f32 v[134:135], v[134:135], v[144:145], v[144:145] op_sel:[0,1,0] op_sel_hi:[1,1,0]\n\t"
      "v_cvt_pk_bf16_f32 v154, v132, v133\n\t"
      "v_cvt_pk_bf16_f32 v155, v134, v135\n\t"
      "ds_write_b64 v234, v[154:155]\n\t"
      "ds_read_b64_tr_b16 v[160:161], v246\n\t"
      "ds_read_b64_tr_b16 v[162:163], v247 offset:512\n\t"
      "v_pk_fma_f32 v[136:137], v[136:137], v[144:145], v[144:145] op_sel:[0,1,0] op_sel_hi:[1,1,0]\n\t"
      "v_pk_fma_f32 v[138:139], v[138:139], v[144:145], v[144:145] op_sel:[0,1,0] op_sel_hi:[1,1,0]\n\t"
      "v_cvt_pk_bf16_f32 v156, v136, v137\n\t"
      "v_cvt_pk_bf16_f32 v157, v138, v139\n\t"
      "ds_write_b64 v235, v[156:157]\n\t"
      "v_pk_fma_f32 v[140:141], v[140:141], v[144:145], v[144:145] op_sel:[0,1,0] op_sel_hi:[1,1,0]\n\t"
      "v_pk_fma_f32 v[142:143], v[142:143], v[144:145], v[144:145] op_sel:[0,1,0] op_sel_hi:[1,1,0]\n\t"
      "v_cvt_pk_bf16_f32 v158, v140, v141\n\t"
      "v_cvt_pk_bf16_f32 v159, v142, v143\n\t"
      "ds_write_b64 v236, v[158:159]\n\t"
      "ds_read_b64_tr_b16 v[164:165], v248\n\t"
      "ds_read_b64_tr_b16 v[166:167], v249 offset:512\n\t"
      "s_waitcnt lgkmcnt(4)\n\t"
      "v_min_u32_e32 v160, -2, v160\n\t"
      "v_min_u32_e32 v161, -2, v161\n\t"
      "v_min_u32_e32 v162, -2, v162\n\t"
      "v_min_u32_e32 v163, -2, v163\n\t"
      "global_store_dwordx4 v229, v[160:163], s[48:49] sc1\n\t"
      "s_waitcnt lgkmcnt(0)\n\t"
      "v_min_u32_e32 v164, -2, v164\n\t"
      "v_min_u32_e32 v165, -2, v165\n\t"
      "v_min_u32_e32 v166, -2, v166\n\t"
      "v_min_u32_e32 v167, -2, v167\n\t"
      "global_store_dwordx4 v230, v[164:167], s[48:49] sc1\n\t"
      "s_barrier\n\t"
      "s_bitcmp0_b32 s27, 0\n\t"
      "s_cbranch_scc1 NTK4_%=\n\t"
      "ds_read_b64_tr_b16 v[64:65], v224 offset:16384\n\t"
      "ds_read_b64_tr_b16 v[66:67], v224 offset:16896\n\t"
      "ds_read_b64_tr_b16 v[68:69], v225 offset:16384\n\t"
      "ds_read_b64_tr_b16 v[70:71], v225 offset:16896\n\t"
      "ds_read_b64_tr_b16 v[72:73], v226 offset:16384\n\t"
      "ds_read_b64_tr_b16 v[74:75], v226 offset:16896\n\t"
      "ds_read_b64_tr_b16 v[76:77], v227 offset:16384\n\t"
      "ds_read_b64_tr_b16 v[78:79], v227 offset:16896\n\t"
      "ds_read_b64_tr_b16 v[80:81], v224 offset:49152\n\t"
      "ds_read_b64_tr_b16 v[82:83], v224 offset:49664\n\t"
      "ds_read_b64_tr_b16 v[84:85], v225 offset:49152\n\t"
      "ds_read_b64_tr_b16 v[86:87], v225 offset:49664\n\t"
      "ds_read_b64_tr_b16 v[88:89], v226 offset:49152\n\t"
      "ds_read_b64_tr_b16 v[90:91], v226 offset:49664\n\t"
      "ds_read_b64_tr_b16 v[92:93], v227 offset:49152\n\t"
      "ds_read_b64_tr_b16 v[94:95], v227 offset:49664\n\t"
      "ds_read_b64_tr_b16 v[96:97], v224\n\t"
      "ds_read_b64_tr_b16 v[98:99], v224 offset:512\n\t"
      "ds_read_b64_tr_b16 v[100:101], v225\n\t"
      "ds_read_b64_tr_b16 v[102:103], v225 offset:512\n\t"
      "ds_read_b64_tr_b16 v[104:105], v226\n\t"
      "ds_read_b64_tr_b16 v[106:107], v226 offset:512\n\t"
      "ds_read_b64_tr_b16 v[108:109], v227\n\t"
      "ds_read_b64_tr_b16 v[110:111], v227 offset:512\n\t"
      "ds_read_b64_tr_b16 v[112:113], v224 offset:32768\n\t"
      "ds_read_b64_tr_b16 v[114:115], v224 offset:33280\n\t"
      "ds_read_b64_tr_b16 v[116:117], v225 offset:32768\n\t"
      "ds_read_b64_tr_b16 v[118:119], v225 offset:33280\n\t"
      "ds_read_b64_tr_b16 v[120:121], v226 offset:32768\n\t"
      "ds_read_b64_tr_b16 v[122:123], v226 offset:33280\n\t"
      "ds_read_b64_tr_b16 v[124:125], v227 offset:32768\n\t"
      "ds_read_b64_tr_b16 v[126:127], v227 offset:33280\n\t"
      "NTK4_%=:\n\t"
      "s_waitcnt lgkmcnt(0)\n\t"
      "s_setprio 0\n\t"
      "GST_%=:\n\t"
      "s_barrier\n\t"
      "s_bitcmp0_b32 s27, 0\n\t"
      "s_cbranch_scc1 G4_%=\n\t"
      "s_mul_i32 s34, s30, 65536\n\t"
      "s_mul_i32 s35, s24, 8192\n\t"
      "s_add_u32 s34, s34, s35\n\t"
      "s_add_u32 s34, s34, 0\n\t"
      "s_add_u32 s64, s48, s34\n\t"
      "s_addc_u32 s65, s49, 0\n\t"
      "s_and_b32 s34, s29, 0\n\t"
      "s_lshl_b32 s34, s34, 11\n\t"
      "s_add_u32 s40, s34, 0\n\t"
      "s_sub_u32 s35, s40, 8192\n\t"
      "s_min_u32 s40, s40, s35\n\t"
      "s_add_u32 s42, s34, 2048\n\t"
      "s_sub_u32 s35, s42, 8192\n\t"
      "s_min_u32 s42, s42, s35\n\t"
      "s_add_u32 s44, s34, 4096\n\t"
      "s_sub_u32 s35, s44, 8192\n\t"
      "s_min_u32 s44, s44, s35\n\t"
      "s_add_u32 s46, s34, 6144\n\t"
      "s_sub_u32 s35, s46, 8192\n\t"
      "s_min_u32 s46, s46, s35\n\t"
      "s_add_u32 s58, s64, s40\n\t"
      "s_addc_u32 s59, s65, 0\n\t"
      "global_load_dwordx4 v[128:131], v240, s[58:59] sc1\n\t"
      "global_load_dwordx4 v[132:135], v240, s[58:59] offset:1024 sc1\n\t"
      "s_add_u32 s58, s64, s42\n\t"
      "s_addc_u32 s59, s65, 0\n\t"
      "global_load_dwordx4 v[136:139], v240, s[58:59] sc1\n\t"
      "global_load_dwordx4 v[140:143], v240, s[58:59] offset:1024 sc1\n\t"
      "s_add_u32 s58, s64, s44\n\t"
      "s_addc_u32 s59, s65, 0\n\t"
      "global_load_dwordx4 v[144:147], v240, s[58:59] sc1\n\t"
      "global_load_dwordx4 v[148:151], v240, s[58:59] offset:1024 sc1\n\t"
      "s_add_u32 s58, s64, s46\n\t"
      "s_addc_u32 s59, s65, 0\n\t"
      "global_load_dwordx4 v[152:155], v240, s[58:59] sc1\n\t"
      "global_load_dwordx4 v[156:159], v240, s[58:59] offset:1024 sc1\n\t"
      "s_add_u32 s66, s64, 65536\n\t"
      "s_addc_u32 s67, s65, 0\n\t"
      "s_add_u32 s68, s66, 65536\n\t"
      "s_addc_u32 s69, s67, 0\n\t"
      "s_add_u32 s70, s68, 65536\n\t"
      "s_addc_u32 s71, s69, 0\n\t"
      "s_add_u32 s72, s70, 65536\n\t"
      "s_addc_u32 s73, s71, 0\n\t"
      "s_add_u32 s74, s72, 65536\n\t"
      "s_addc_u32 s75, s73, 0\n\t"
      "s_add_u32 s76, s74, 65536\n\t"
      "s_addc_u32 s77, s75, 0\n\t"
      "s_mov_b32 s82, 0x11111111\n\t"
      "s_mov_b32 s83, 0x11111111\n\t"
      "s_mul_i32 s34, s30, 28\n\t"
      "s_add_u32 s34, s34, s29\n\t"
      "s_lshl_b32 s34, s34, 11\n\t"
      "s_add_u32 s34, s34, 10223616\n\t"
      "s_add_u32 s62, s20, s34\n\t"
      "s_addc_u32 s63, s21, 0\n\t"
      "v_lshlrev_b32_e32 v244, 16, v245\n\t"
      "v_and_b32_e32 v245, 0xffff0000, v245\n\t"
      "v_mov_b32_e32 v233, 0xbdd2d3e8\n\t"
      "s_waitcnt vmcnt(0)\n\t"
      "v_max3_u32 v232, v128, v129, v130\n\t"
      "v_max3_u32 v232, v232, v131, v132\n\t"
      "v_max3_u32 v232, v232, v133, v134\n\t"
      "v_max3_u32 v232, v232, v135, v136\n\t"
      "v_max3_u32 v232, v232, v137, v138\n\t"
      "v_max3_u32 v232, v232, v139, v140\n\t"
      "v_max3_u32 v232, v232, v141, v142\n\t"
      "v_max3_u32 v232, v232, v143, v144\n\t"
      "v_max3_u32 v232, v232, v145, v146\n\t"
      "v_max3_u32 v232, v232, v147, v148\n\t"
      "v_max3_u32 v232, v232, v149, v150\n\t"
      "v_max3_u32 v232, v232, v151, v152\n\t"
      "v_max3_u32 v232, v232, v153, v154\n\t"
      "v_max3_u32 v232, v232, v155, v156\n\t"
      "v_max3_u32 v232, v232, v157, v158\n\t"
      "v_max_u32_e32 v232, v232, v159\n\t"
      "v_cmp_eq_u32_e32 vcc, -1, v232\n\t"
      "s_cbranch_vccnz LA30R_%=\n\t"
      "LA30B_%=:\n\t"
      "v_mfma_f32_16x16x32_bf16 v[192:195], v[0:3], v[128:131], 0\n\t"
      "s_add_u32 s58, s66, s40\n\t"
      "s_addc_u32 s59, s67, 0\n\t"
      "global_load_dwordx4 v[160:163], v240, s[58:59] sc1\n\t"
      "v_mfma_f32_16x16x32_bf16 v[192:195], v[16:19], v[132:135], v[192:195]\n\t"
      "v_mfma_f32_16x16x32_bf16 v[192:195], v[32:35], v[136:139], v[192:195]\n\t"
      "s_add_u32 s58, s66, s40\n\t"
      "s_addc_u32 s59, s67, 0\n\t"
      "global_load_dwordx4 v[164:167], v240, s[58:59] offset:1024 sc1\n\t"
      "v_mfma_f32_16x16x32_bf16 v[192:195], v[48:51], v[140:143], v[192:195]\n\t"
      "v_mfma_f32_16x16x32_bf16 v[192:195], v[64:67], v[144:147], v[192:195]\n\t"
      "s_add_u32 s58, s66, s42\n\t"
      "s_addc_u32 s59, s67, 0\n\t"
      "global_load_dwordx4 v[168:171], v240, s[58:59] sc1\n\t"
      "v_mfma_f32_16x16x32_bf16 v[192:195], v[80:83], v[148:151], v[192:195]\n\t"
      "v_mfma_f32_16x16x32_bf16 v[192:195], v[96:99], v[152:155], v[192:195]\n\t"
      "s_add_u32 s58, s66, s42\n\t"
      "s_addc_u32 s59, s67, 0\n\t"
      "global_load_dwordx4 v[172:175], v240, s[58:59] offset:1024 sc1\n\t"
      "v_mfma_f32_16x16x32_bf16 v[192:195], v[112:115], v[156:159], v[192:195]\n\t"
      "v_mfma_f32_16x16x32_bf16 v[196:199], v[4:7], v[128:131], 0\n\t"
      "s_add_u32 s58, s66, s44\n\t"
      "s_addc_u32 s59, s67, 0\n\t"
      "global_load_dwordx4 v[176:179], v240, s[58:59] sc1\n\t"
      "v_mfma_f32_16x16x32_bf16 v[196:199], v[20:23], v[132:135], v[196:199]\n\t"
      "v_mfma_f32_16x16x32_bf16 v[196:199], v[36:39], v[136:139], v[196:199]\n\t"
      "s_nop 1\n\t"
      "ds_write_b128 v241, v[192:195]\n\t"
      "s_add_u32 s58, s66, s44\n\t"
      "s_addc_u32 s59, s67, 0\n\t"
      "global_load_dwordx4 v[180:183], v240, s[58:59] offset:1024 sc1\n\t"
      "v_mfma_f32_16x16x32_bf16 v[196:199], v[52:55], v[140:143], v[196:199]\n\t"
      "v_mfma_f32_16x16x32_bf16 v[196:199], v[68:71], v[144:147], v[196:199]\n\t"
      "s_add_u32 s58, s66, s46\n\t"
      "s_addc_u32 s59, s67, 0\n\t"
      "global_load_dwordx4 v[184:187], v240, s[58:59] sc1\n\t"
      "v_mfma_f32_16x16x32_bf16 v[196:199], v[84:87], v[148:151], v[196:199]\n\t"
      "v_mfma_f32_16x16x32_bf16 v[196:199], v[100:103], v[152:155], v[196:199]\n\t"
      "s_add_u32 s58, s66, s46\n\t"
      "s_addc_u32 s59, s67, 0\n\t"
      "global_load_dwordx4 v[188:191], v240, s[58:59] offset:1024 sc1\n\t"
      "v_mfma_f32_16x16x32_bf16 v[196:199], v[116:119], v[156:159], v[196:199]\n\t"
      "v_mfma_f32_16x16x32_bf16 v[200:203], v[8:11], v[128:131], 0\n\t"
      "v_mfma_f32_16x16x32_bf16 v[200:203], v[24:27], v[132:135], v[200:203]\n\t"
      "v_mfma_f32_16x16x32_bf16 v[200:203], v[40:43], v[136:139], v[200:203]\n\t"
      "s_nop 4\n\t"
      "ds_write_b128 v241, v[196:199] offset:1024\n\t"
      "v_mfma_f32_16x16x32_bf16 v[200:203], v[56:59], v[140:143], v[200:203]\n\t"
      "v_mfma_f32_16x16x32_bf16 v[200:203], v[72:75], v[144:147], v[200:203]\n\t"
      "v_mfma_f32_16x16x32_bf16 v[200:203], v[88:91], v[148:151], v[200:203]\n\t"
      "v_mfma_f32_16x16x32_bf16 v[200:203], v[104:107], v[152:155], v[200:203]\n\t"
      "v_mfma_f32_16x16x32_bf16 v[200:203], v[120:123], v[156:159], v[200:203]\n\t"
      "v_mfma_f32_16x16x32_bf16 v[204:207], v[12:15], v[128:131], 0\n\t"
      "s_add_u32 s58, s68, s40\n\t"
      "s_addc_u32 s59, s69, 0\n\t"
      "global_load_dwordx4 v[128:131], v240, s[58:59] sc1\n\t"
      "v_mfma_f32_16x16x32_bf16 v[204:207], v[28:31], v[132:135], v[204:207]\n\t"
      "v_mfma_f32_16x16x32_bf16 v[204:207], v[44:47], v[136:139], v[204:207]\n\t"
      "s_nop 1\n\t"
      "ds_write_b128 v241, v[200:203] offset:2048\n\t"
      "v_mfma_f32_16x16x32_bf16 v[204:207], v[60:63], v[140:143], v[204:207]\n\t"
      "v_mfma_f32_16x16x32_bf16 v[204:207], v[76:79], v[144:147], v[204:207]\n\t"
      "s_add_u32 s58, s68, s40\n\t"
      "s_addc_u32 s59, s69, 0\n\t"
      "global_load_dwordx4 v[132:135], v240, s[58:59] offset:1024 sc1\n\t"
      "v_mfma_f32_16x16x32_bf16 v[204:207], v[92:95], v[148:151], v[204:207]\n\t"
      "v_mfma_f32_16x16x32_bf16 v[204:207], v[108:111], v[152:155], v[204:207]\n\t"
      "v_mfma_f32_16x16x32_bf16 v[204:207], v[124:127], v[156:159], v[204:207]\n\t"
      "s_waitcnt vmcnt(9)\n\t"
      "v_mfma_f32_16x16x32_bf16 v[208:211], v[0:3], v[160:163], 0\n\t"
      "s_add_u32 s58, s68, s42\n\t"
      "s_addc_u32 s59, s69, 0\n\t"
      "global_load_dwordx4 v[136:139], v240, s[58:59] sc1\n\t"
      "s_waitcnt vmcnt(9)\n\t"
      "v_mfma_f32_16x16x32_bf16 v[208:211], v[16:19], v[164:167], v[208:211]\n\t"
      "s_nop 0\n\t"
      "ds_write_b128 v241, v[204:207] offset:3072\n\t"
      "s_waitcnt vmcnt(8)\n\t"
      "v_mfma_f32_16x16x32_bf16 v[208:211], v[32:35], v[168:171], v[208:211]\n\t"
      "s_waitcnt vmcnt(7)\n\t"
      "v_mfma_f32_16x16x32_bf16 v[208:211], v[48:51], v[172:175], v[208:211]\n\t"
      "s_waitcnt vmcnt(6)\n\t"
      "v_mfma_f32_16x16x32_bf16 v[208:211], v[64:67], v[176:179], v[208:211]\n\t"
      "s_add_u32 s58, s68, s42\n\t"
      "s_addc_u32 s59, s69, 0\n\t"
      "global_load_dwordx4 v[140:143], v240, s[58:59] offset:1024 sc1\n\t"
      "s_waitcnt vmcnt(6)\n\t"
      "v_mfma_f32_16x16x32_bf16 v[208:211], v[80:83], v[180:183], v[208:211]\n\t"
      "s_waitcnt lgkmcnt(0)\n\t"
      "s_barrier\n\t"
      "ds_read_b64 v[212:213], v242\n\t"
      "ds_read_b64 v[214:215], v242 offset:4096\n\t"
      "s_waitcnt vmcnt(5)\n\t"
      "v_mfma_f32_16x16x32_bf16 v[208:211], v[96:99], v[184:187], v[208:211]\n\t"
      "ds_read_b64 v[216:217], v242 offset:8192\n\t"
      "ds_read_b64 v[218:219], v242 offset:12288\n\t"
      "s_waitcnt vmcnt(4)\n\t"
      "v_mfma_f32_16x16x32_bf16 v[208:211], v[112:115], v[188:191], v[208:211]\n\t"
      "ds_read_b64 v[220:221], v242 offset:16384\n\t"
      "ds_read_b64 v[222:223], v242 offset:20480\n\t"
      "v_mfma_f32_16x16x32_bf16 v[192:195], v[4:7], v[160:163], 0\n\t"
      "s_add_u32 s58, s68, s44\n\t"
      "s_addc_u32 s59, s69, 0\n\t"
      "global_load_dwordx4 v[144:147], v240, s[58:59] sc1\n\t"
      "ds_read_b64 v[224:225], v242 offset:24576\n\t"
      "ds_read_b64 v[226:227], v242 offset:28672\n\t"
      "v_mfma_f32_16x16x32_bf16 v[192:195], v[20:23], v[164:167], v[192:195]\n\t"
      "v_mfma_f32_16x16x32_bf16 v[192:195], v[36:39], v[168:171], v[192:195]\n\t"
      "s_waitcnt lgkmcnt(4)\n\t"
      "v_add_f32_e32 v212, v212, v214\n\t"
      "v_cmp_u_f32_e32 vcc, v208, v208\n\t"
      "s_cbranch_vccnz LA31R_%=\n\t"
      "LA31B_%=:\n\t"
      "ds_write_b128 v241, v[208:211] offset:32768\n\t"
      "v_mfma_f32_16x16x32_bf16 v[192:195], v[52:55], v[172:175], v[192:195]\n\t"
      "v_add_f32_e32 v216, v216, v218\n\t"
      "v_add_f32_e32 v213, v213, v215\n\t"
      "v_mfma_f32_16x16x32_bf16 v[192:195], v[68:71], v[176:179], v[192:195]\n\t"
      "s_add_u32 s58, s68, s44\n\t"
      "s_addc_u32 s59, s69, 0\n\t"
      "global_load_dwordx4 v[148:151], v240, s[58:59] offset:1024 sc1\n\t"
      "v_add_f32_e32 v217, v217, v219\n\t"
      "s_waitcnt lgkmcnt(1)\n\t"
      "v_mfma_f32_16x16x32_bf16 v[192:195], v[84:87], v[180:183], v[192:195]\n\t"
      "v_add_f32_e32 v220, v220, v222\n\t"
      "v_add_f32_e32 v224, v224, v226\n\t"
      "v_mfma_f32_16x16x32_bf16 v[192:195], v[100:103], v[184:187], v[192:195]\n\t"
      "v_add_f32_e32 v221, v221, v223\n\t"
      "v_add_f32_e32 v225, v225, v227\n\t"
      "v_mfma_f32_16x16x32_bf16 v[192:195], v[116:119], v[188:191], v[192:195]\n\t"
      "v_add_f32_e32 v212, v212, v216\n\t"
      "v_add_f32_e32 v220, v220, v224\n\t"
      "v_mfma_f32_16x16x32_bf16 v[196:199], v[8:11], v[160:163], 0\n\t"
      "s_add_u32 s58, s68, s46\n\t"
      "s_addc_u32 s59, s69, 0\n\t"
      "global_load_dwordx4 v[152:155], v240, s[58:59] sc1\n\t"
      "v_add_f32_e32 v213, v213, v217\n\t"
      "v_add_f32_e32 v221, v221, v225\n\t"
      "v_mfma_f32_16x16x32_bf16 v[196:199], v[24:27], v[164:167], v[196:199]\n\t"
      "v_add_f32_e32 v212, v212, v244\n\t"
      "v_add_f32_e32 v213, v213, v245\n\t"
      "v_mfma_f32_16x16x32_bf16 v[196:199], v[40:43], v[168:171], v[196:199]\n\t"
      "v_add_f32_e32 v212, v212, v220\n\t"
      "v_add_f32_e32 v213, v213, v221\n\t"
      "ds_write_b128 v241, v[192:195] offset:33792\n\t"
      "v_mfma_f32_16x16x32_bf16 v[196:199], v[56:59], v[172:175], v[196:199]\n\t"
      "v_mul_f32_e32 v214, v212, v212\n\t"
      "v_mfma_f32_16x16x32_bf16 v[196:199], v[72:75], v[176:179], v[196:199]\n\t"
      "s_add_u32 s58, s68, s46\n\t"
      "s_addc_u32 s59, s69, 0\n\t"
      "global_load_dwordx4 v[156:159], v240, s[58:59] offset:1024 sc1\n\t"
      "v_mul_f32_e32 v215, v213, v213\n\t"
      "v_fmaak_f32 v214, v233, v214, 0xc0135761\n\t"
      "v_mfma_f32_16x16x32_bf16 v[196:199], v[88:91], v[180:183], v[196:199]\n\t"
      "v_fmaak_f32 v215, v233, v215, 0xc0135761\n\t"
      "v_mul_f32_e32 v214, v212, v214\n\t"
      "v_mfma_f32_16x16x32_bf16 v[196:199], v[104:107], v[184:187], v[196:199]\n\t"
      "v_mul_f32_e32 v215, v213, v215\n\t"
      "v_exp_f32_e32 v214, v214\n\t"
      "v_mfma_f32_16x16x32_bf16 v[196:199], v[120:123], v[188:191], v[196:199]\n\t"
      "v_exp_f32_e32 v215, v215\n\t"
      "v_add_f32_e32 v214, 1.0, v214\n\t"
      "v_mfma_f32_16x16x32_bf16 v[200:203], v[12:15], v[160:163], 0\n\t"
      "v_add_f32_e32 v215, 1.0, v215\n\t"
      "v_rcp_f32_e32 v214, v214\n\t"
      "s_add_u32 s58, s70, s40\n\t"
      "s_addc_u32 s59, s71, 0\n\t"
      "global_load_dwordx4 v[160:163], v240, s[58:59] sc1\n\t"
      "v_mfma_f32_16x16x32_bf16 v[200:203], v[28:31], v[164:167], v[200:203]\n\t"
      "v_rcp_f32_e32 v215, v215\n\t"
      "v_mul_f32_e32 v212, v212, v214\n\t"
      "v_mfma_f32_16x16x32_bf16 v[200:203], v[44:47], v[168:171], v[200:203]\n\t"
      "v_mul_f32_e32 v213, v213, v215\n\t"
      "v_cvt_pk_bf16_f32 v216, v212, v213\n\t"
      "ds_write_b128 v241, v[196:199] offset:34816\n\t"
      "v_mfma_f32_16x16x32_bf16 v[200:203], v[60:63], v[172:175], v[200:203]\n\t"
      "v_min_u32_e32 v216, -2, v216\n\t"
      "v_mfma_f32_16x16x32_bf16 v[200:203], v[76:79], v[176:179], v[200:203]\n\t"
      "s_nop 0\n\t"
      "v_mov_b32_dpp v217, v216 quad_perm:[1,1,1,1] row_mask:0xf bank_mask:0xf\n\t"
      "v_mov_b32_dpp v218, v216 quad_perm:[2,2,2,2] row_mask:0xf bank_mask:0xf\n\t"
      "s_add_u32 s58, s70, s40\n\t"
      "s_addc_u32 s59, s71, 0\n\t"
      "global_load_dwordx4 v[164:167], v240, s[58:59] offset:1024 sc1\n\t"
      "v_mfma_f32_16x16x32_bf16 v[200:203], v[92:95], v[180:183], v[200:203]\n\t"
      "v_mov_b32_dpp v219, v216 quad_perm:[3,3,3,3] row_mask:0xf bank_mask:0xf\n\t"
      "v_mfma_f32_16x16x32_bf16 v[200:203], v[108:111], v[184:187], v[200:203]\n\t"
      "s_mov_b64 exec, s[82:83]\n\t"
      "global_store_dwordx4 v243, v[216:219], s[62:63] sc1\n\t"
      "s_mov_b64 exec, -1\n\t"
      "v_mfma_f32_16x16x32_bf16 v[200:203], v[124:127], v[188:191], v[200:203]\n\t"
      "s_waitcnt vmcnt(10)\n\t"
      "v_mfma_f32_16x16x32_bf16 v[204:207], v[0:3], v[128:131], 0\n\t"
      "s_add_u32 s58, s70, s42\n\t"
      "s_addc_u32 s59, s71, 0\n\t"
      "global_load_dwordx4 v[168:171], v240, s[58:59] sc1\n\t"
      "s_waitcnt vmcnt(10)\n\t"
      "v_mfma_f32_16x16x32_bf16 v[204:207], v[16:19], v[132:135], v[204:207]\n\t"
      "s_nop 0\n\t"
      "ds_write_b128 v241, v[200:203] offset:35840\n\t"
      "s_waitcnt vmcnt(9)\n\t"
      "v_mfma_f32_16x16x32_bf16 v[204:207], v[32:35], v[136:139], v[204:207]\n\t"
      "s_waitcnt vmcnt(8)\n\t"
      "v_mfma_f32_16x16x32_bf16 v[204:207], v[48:51], v[140:143], v[204:207]\n\t"
      "s_waitcnt vmcnt(7)\n\t"
      "v_mfma_f32_16x16x32_bf16 v[204:207], v[64:67], v[144:147], v[204:207]\n\t"
      "s_add_u32 s58, s70, s42\n\t"
      "s_addc_u32 s59, s71, 0\n\t"
      "global_load_dwordx4 v[172:175], v240, s[58:59] offset:1024 sc1\n\t"
      "s_waitcnt vmcnt(7)\n\t"
      "v_mfma_f32_16x16x32_bf16 v[204:207], v[80:83], v[148:151], v[204:207]\n\t"
      "s_waitcnt lgkmcnt(0)\n\t"
      "s_barrier\n\t"
      "ds_read_b64 v[212:213], v242 offset:32768\n\t"
      "ds_read_b64 v[214:215], v242 offset:36864\n\t"
      "s_waitcnt vmcnt(6)\n\t"
      "v_mfma_f32_16x16x32_bf16 v[204:207], v[96:99], v[152:155], v[204:207]\n\t"
      "ds_read_b64 v[216:217], v242 offset:40960\n\t"
      "ds_read_b64 v[218:219], v242 offset:45056\n\t"
      "s_waitcnt vmcnt(5)\n\t"
      "v_mfma_f32_16x16x32_bf16 v[204:207], v[112:115], v[156:159], v[204:207]\n\t"
      "ds_read_b64 v[220:221], v242 offset:49152\n\t"
      "ds_read_b64 v[222:223], v242 offset:53248\n\t"
      "v_mfma_f32_16x16x32_bf16 v[208:211], v[4:7], v[128:131], 0\n\t"
      "s_add_u32 s58, s70, s44\n\t"
      "s_addc_u32 s59, s71, 0\n\t"
      "global_load_dwordx4 v[176:179], v240, s[58:59] sc1\n\t"
      "ds_read_b64 v[224:225], v242 offset:57344\n\t"
      "ds_read_b64 v[226:227], v242 offset:61440\n\t"
      "v_mfma_f32_16x16x32_bf16 v[208:211], v[20:23], v[132:135], v[208:211]\n\t"
      "v_mfma_f32_16x16x32_bf16 v[208:211], v[36:39], v[136:139], v[208:211]\n\t"
      "s_waitcnt lgkmcnt(4)\n\t"
      "v_add_f32_e32 v212, v212, v214\n\t"
      "v_cmp_u_f32_e32 vcc, v204, v204\n\t"
      "s_cbranch_vccnz LA32R_%=\n\t"
      "LA32B_%=:\n\t"
      "ds_write_b128 v241, v[204:207]\n\t"
      "v_mfma_f32_16x16x32_bf16 v[208:211], v[52:55], v[140:143], v[208:211]\n\t"
      "v_add_f32_e32 v216, v216, v218\n\t"
      "v_add_f32_e32 v213, v213, v215\n\t"
      "v_mfma_f32_16x16x32_bf16 v[208:211], v[68:71], v[144:147], v[208:211]\n\t"
      "s_add_u32 s58, s70, s44\n\t"
      "s_addc_u32 s59, s71, 0\n\t"
      "global_load_dwordx4 v[180:183], v240, s[58:59] offset:1024 sc1\n\t"
      "v_add_f32_e32 v217, v217, v219\n\t"
      "s_waitcnt lgkmcnt(1)\n\t"
      "v_mfma_f32_16x16x32_bf16 v[208:211], v[84:87], v[148:151], v[208:211]\n\t"
      "v_add_f32_e32 v220, v220, v222\n\t"
      "v_add_f32_e32 v224, v224, v226\n\t"
      "v_mfma_f32_16x16x32_bf16 v[208:211], v[100:103], v[152:155], v[208:211]\n\t"
      "v_add_f32_e32 v221, v221, v223\n\t"
      "v_add_f32_e32 v225, v225, v227\n\t"
      "v_mfma_f32_16x16x32_bf16 v[208:211], v[116:119], v[156:159], v[208:211]\n\t"
      "v_add_f32_e32 v212, v212, v216\n\t"
      "v_add_f32_e32 v220, v220, v224\n\t"
      "v_mfma_f32_16x16x32_bf16 v[192:195], v[8:11], v[128:131], 0\n\t"
      "s_add_u32 s58, s70, s46\n\t"
      "s_addc_u32 s59, s71, 0\n\t"
      "global_load_dwordx4 v[184:187], v240, s[58:59] sc1\n\t"
      "v_add_f32_e32 v213, v213, v217\n\t"
      "v_add_f32_e32 v221, v221, v225\n\t"
      "v_mfma_f32_16x16x32_bf16 v[192:195], v[24:27], v[132:135], v[192:195]\n\t"
      "v_add_f32_e32 v212, v212, v244\n\t"
      "v_add_f32_e32 v213, v213, v245\n\t"
      "v_mfma_f32_16x16x32_bf16 v[192:195], v[40:43], v[136:139], v[192:195]\n\t"
      "v_add_f32_e32 v212, v212, v220\n\t"
      "v_add_f32_e32 v213, v213, v221\n\t"
      "ds_write_b128 v241, v[208:211] offset:1024\n\t"
      "v_mfma_f32_16x16x32_bf16 v[192:195], v[56:59], v[140:143], v[192:195]\n\t"
      "v_mul_f32_e32 v214, v212, v212\n\t"
      "v_mfma_f32_16x16x32_bf16 v[192:195], v[72:75], v[144:147], v[192:195]\n\t"
      "s_add_u32 s58, s70, s46\n\t"
      "s_addc_u32 s59, s71, 0\n\t"
      "global_load_dwordx4 v[188:191], v240, s[58:59] offset:1024 sc1\n\t"
      "v_mul_f32_e32 v215, v213, v213\n\t"
      "v_fmaak_f32 v214, v233, v214, 0xc0135761\n\t"
      "v_mfma_f32_16x16x32_bf16 v[192:195], v[88:91], v[148:151], v[192:195]\n\t"
      "v_fmaak_f32 v215, v233, v215, 0xc0135761\n\t"
      "v_mul_f32_e32 v214, v212, v214\n\t"
      "v_mfma_f32_16x16x32_bf16 v[192:195], v[104:107], v[152:155], v[192:195]\n\t"
      "v_mul_f32_e32 v215, v213, v215\n\t"
      "v_exp_f32_e32 v214, v214\n\t"
      "v_mfma_f32_16x16x32_bf16 v[192:195], v[120:123], v[156:159], v[192:195]\n\t"
      "v_exp_f32_e32 v215, v215\n\t"
      "v_add_f32_e32 v214, 1.0, v214\n\t"
      "v_mfma_f32_16x16x32_bf16 v[196:199], v[12:15], v[128:131], 0\n\t"
      "v_add_f32_e32 v215, 1.0, v215\n\t"
      "v_rcp_f32_e32 v214, v214\n\t"
      "s_add_u32 s58, s72, s40\n\t"
      "s_addc_u32 s59, s73, 0\n\t"
      "global_load_dwordx4 v[128:131], v240, s[58:59] sc1\n\t"
      "v_mfma_f32_16x16x32_bf16 v[196:199], v[28:31], v[132:135], v[196:199]\n\t"
      "v_rcp_f32_e32 v215, v215\n\t"
      "v_mul_f32_e32 v212, v212, v214\n\t"
      "v_mfma_f32_16x16x32_bf16 v[196:199], v[44:47], v[136:139], v[196:199]\n\t"
      "v_mul_f32_e32 v213, v213, v215\n\t"
      "v_cvt_pk_bf16_f32 v216, v212, v213\n\t"
      "ds_write_b128 v241, v[192:195] offset:2048\n\t"
      "v_mfma_f32_16x16x32_bf16 v[196:199], v[60:63], v[140:143], v[196:199]\n\t"
      "v_min_u32_e32 v216, -2, v216\n\t"
      "s_add_u32 s62, s62, 57344\n\t"
      "s_addc_u32 s63, s63, 0\n\t"
      "v_mfma_f32_16x16x32_bf16 v[196:199], v[76:79], v[144:147], v[196:199]\n\t"
      "v_mov_b32_dpp v217, v216 quad_perm:[1,1,1,1] row_mask:0xf bank_mask:0xf\n\t"
      "v_mov_b32_dpp v218, v216 quad_perm:[2,2,2,2] row_mask:0xf bank_mask:0xf\n\t"
      "s_add_u32 s58, s72, s40\n\t"
      "s_addc_u32 s59, s73, 0\n\t"
      "global_load_dwordx4 v[132:135], v240, s[58:59] offset:1024 sc1\n\t"
      "v_mfma_f32_16x16x32_bf16 v[196:199], v[92:95], v[148:151], v[196:199]\n\t"
      "v_mov_b32_dpp v219, v216 quad_perm:[3,3,3,3] row_mask:0xf bank_mask:0xf\n\t"
      "v_mfma_f32_16x16x32_bf16 v[196:199], v[108:111], v[152:155], v[196:199]\n\t"
      "s_mov_b64 exec, s[82:83]\n\t"
      "global_store_dwordx4 v243, v[216:219], s[62:63] sc1\n\t"
      "s_mov_b64 exec, -1\n\t"
      "v_mfma_f32_16x16x32_bf16 v[196:199], v[124:127], v[156:159], v[196:199]\n\t"
      "s_waitcnt vmcnt(11)\n\t"
      "v_mfma_f32_16x16x32_bf16 v[200:203], v[0:3], v[160:163], 0\n\t"
      "s_add_u32 s58, s72, s42\n\t"
      "s_addc_u32 s59, s73, 0\n\t"
      "global_load_dwordx4 v[136:139], v240, s[58:59] sc1\n\t"
      "s_waitcnt vmcnt(11)\n\t"
      "v_mfma_f32_16x16x32_bf16 v[200:203], v[16:19], v[164:167], v[200:203]\n\t"
      "s_nop 0\n\t"
      "ds_write_b128 v241, v[196:199] offset:3072\n\t"
      "s_waitcnt vmcnt(9)\n\t"
      "v_mfma_f32_16x16x32_bf16 v[200:203], v[32:35], v[168:171], v[200:203]\n\t"
      "s_waitcnt vmcnt(8)\n\t"
      "v_mfma_f32_16x16x32_bf16 v[200:203], v[48:51], v[172:175], v[200:203]\n\t"
      "s_waitcnt vmcnt(7)\n\t"
      "v_mfma_f32_16x16x32_bf16 v[200:203], v[64:67], v[176:179], v[200:203]\n\t"
      "s_add_u32 s58, s72, s42\n\t"
      "s_addc_u32 s59, s73, 0\n\t"
      "global_load_dwordx4 v[140:143], v240, s[58:59] offset:1024 sc1\n\t"
      "s_waitcnt vmcnt(7)\n\t"
      "v_mfma_f32_16x16x32_bf16 v[200:203], v[80:83], v[180:183], v[200:203]\n\t"
      "s_waitcnt lgkmcnt(0)\n\t"
      "s_barrier\n\t"
      "ds_read_b64 v[212:213], v242\n\t"
      "ds_read_b64 v[214:215], v242 offset:4096\n\t"
      "s_waitcnt vmcnt(6)\n\t"
      "v_mfma_f32_16x16x32_bf16 v[200:203], v[96:99], v[184:187], v[200:203]\n\t"
      "ds_read_b64 v[216:217], v242 offset:8192\n\t"
      "ds_read_b64 v[218:219], v242 offset:12288\n\t"
      "s_waitcnt vmcnt(5)\n\t"
      "v_mfma_f32_16x16x32_bf16 v[200:203], v[112:115], v[188:191], v[200:203]\n\t"
      "ds_read_b64 v[220:221], v242 offset:16384\n\t"
      "ds_read_b64 v[222:223], v242 offset:20480\n\t"
      "v_mfma_f32_16x16x32_bf16 v[204:207], v[4:7], v[160:163], 0\n\t"
      "s_add_u32 s58, s72, s44\n\t"
      "s_addc_u32 s59, s73, 0\n\t"
      "global_load_dwordx4 v[144:147], v240, s[58:59] sc1\n\t"
      "ds_read_b64 v[224:225], v242 offset:24576\n\t"
      "ds_read_b64 v[226:227], v242 offset:28672\n\t"
      "v_mfma_f32_16x16x32_bf16 v[204:207], v[20:23], v[164:167], v[204:207]\n\t"
      "v_mfma_f32_16x16x32_bf16 v[204:207], v[36:39], v[168:171], v[204:207]\n\t"
      "s_waitcnt lgkmcnt(4)\n\t"
      "v_add_f32_e32 v212, v212, v214\n\t"
      "v_cmp_u_f32_e32 vcc, v200, v200\n\t"
      "s_cbranch_vccnz LA33R_%=\n\t"
      "LA33B_%=:\n\t"
      "ds_write_b128 v241, v[200:203] offset:32768\n\t"
      "v_mfma_f32_16x16x32_bf16 v[204:207], v[52:55], v[172:175], v[204:207]\n\t"
      "v_add_f32_e32 v216, v216, v218\n\t"
      "v_add_f32_e32 v213, v213, v215\n\t"
      "v_mfma_f32_16x16x32_bf16 v[204:207], v[68:71], v[176:179], v[204:207]\n\t"
      "s_add_u32 s58, s72, s44\n\t"
      "s_addc_u32 s59, s73, 0\n\t"
      "global_load_dwordx4 v[148:151], v240, s[58:59] offset:1024 sc1\n\t"
      "v_add_f32_e32 v217, v217, v219\n\t"
      "s_waitcnt lgkmcnt(1)\n\t"
      "v_mfma_f32_16x16x32_bf16 v[204:207], v[84:87], v[180:183], v[204:207]\n\t"
      "v_add_f32_e32 v220, v220, v222\n\t"
      "v_add_f32_e32 v224, v224, v226\n\t"
      "v_mfma_f32_16x16x32_bf16 v[204:207], v[100:103], v[184:187], v[204:207]\n\t"
      "v_add_f32_e32 v221, v221, v223\n\t"
      "v_add_f32_e32 v225, v225, v227\n\t"
      "v_mfma_f32_16x16x32_bf16 v[204:207], v[116:119], v[188:191], v[204:207]\n\t"
      "v_add_f32_e32 v212, v212, v216\n\t"
      "v_add_f32_e32 v220, v220, v224\n\t"
      "v_mfma_f32_16x16x32_bf16 v[208:211], v[8:11], v[160:163], 0\n\t"
      "s_add_u32 s58, s72, s46\n\t"
      "s_addc_u32 s59, s73, 0\n\t"
      "global_load_dwordx4 v[152:155], v240, s[58:59] sc1\n\t"
      "v_add_f32_e32 v213, v213, v217\n\t"
      "v_add_f32_e32 v221, v221, v225\n\t"
      "v_mfma_f32_16x16x32_bf16 v[208:211], v[24:27], v[164:167], v[208:211]\n\t"
      "v_add_f32_e32 v212, v212, v244\n\t"
      "v_add_f32_e32 v213, v213, v245\n\t"
      "v_mfma_f32_16x16x32_bf16 v[208:211], v[40:43], v[168:171], v[208:211]\n\t"
      "v_add_f32_e32 v212, v212, v220\n\t"
      "v_add_f32_e32 v213, v213, v221\n\t"
      "ds_write_b128 v241, v[204:207] offset:33792\n\t"
      "v_mfma_f32_16x16x32_bf16 v[208:211], v[56:59], v[172:175], v[208:211]\n\t"
      "v_mul_f32_e32 v214, v212, v212\n\t"
      "v_mfma_f32_16x16x32_bf16 v[208:211], v[72:75], v[176:179], v[208:211]\n\t"
      "s_add_u32 s58, s72, s46\n\t"
      "s_addc_u32 s59, s73, 0\n\t"
      "global_load_dwordx4 v[156:159], v240, s[58:59] offset:1024 sc1\n\t"
      "v_mul_f32_e32 v215, v213, v213\n\t"
      "v_fmaak_f32 v214, v233, v214, 0xc0135761\n\t"
      "v_mfma_f32_16x16x32_bf16 v[208:211], v[88:91], v[180:183], v[208:211]\n\t"
      "v_fmaak_f32 v215, v233, v215, 0xc0135761\n\t"
      "v_mul_f32_e32 v214, v212, v214\n\t"
      "v_mfma_f32_16x16x32_bf16 v[208:211], v[104:107], v[184:187], v[208:211]\n\t"
      "v_mul_f32_e32 v215, v213, v215\n\t"
      "v_exp_f32_e32 v214, v214\n\t"
      "v_mfma_f32_16x16x32_bf16 v[208:211], v[120:123], v[188:191], v[208:211]\n\t"
      "v_exp_f32_e32 v215, v215\n\t"
      "v_add_f32_e32 v214, 1.0, v214\n\t"
      "v_mfma_f32_16x16x32_bf16 v[192:195], v[12:15], v[160:163], 0\n\t"
      "v_add_f32_e32 v215, 1.0, v215\n\t"
      "v_rcp_f32_e32 v214, v214\n\t"
      "s_add_u32 s58, s74, s40\n\t"
      "s_addc_u32 s59, s75, 0\n\t"
      "global_load_dwordx4 v[160:163], v240, s[58:59] sc1\n\t"
      "v_mfma_f32_16x16x32_bf16 v[192:195], v[28:31], v[164:167], v[192:195]\n\t"
      "v_rcp_f32_e32 v215, v215\n\t"
      "v_mul_f32_e32 v212, v212, v214\n\t"
      "v_mfma_f32_16x16x32_bf16 v[192:195], v[44:47], v[168:171], v[192:195]\n\t"
      "v_mul_f32_e32 v213, v213, v215\n\t"
      "v_cvt_pk_bf16_f32 v216, v212, v213\n\t"
      "ds_write_b128 v241, v[208:211] offset:34816\n\t"
      "v_mfma_f32_16x16x32_bf16 v[192:195], v[60:63], v[172:175], v[192:195]\n\t"
      "v_min_u32_e32 v216, -2, v216\n\t"
      "s_add_u32 s62, s62, 57344\n\t"
      "s_addc_u32 s63, s63, 0\n\t"
      "v_mfma_f32_16x16x32_bf16 v[192:195], v[76:79], v[176:179], v[192:195]\n\t"
      "v_mov_b32_dpp v217, v216 quad_perm:[1,1,1,1] row_mask:0xf bank_mask:0xf\n\t"
      "v_mov_b32_dpp v218, v216 quad_perm:[2,2,2,2] row_mask:0xf bank_mask:0xf\n\t"
      "s_add_u32 s58, s74, s40\n\t"
      "s_addc_u32 s59, s75, 0\n\t"
      "global_load_dwordx4 v[164:167], v240, s[58:59] offset:1024 sc1\n\t"
      "v_mfma_f32_16x16x32_bf16 v[192:195], v[92:95], v[180:183], v[192:195]\n\t"
      "v_mov_b32_dpp v219, v216 quad_perm:[3,3,3,3] row_mask:0xf bank_mask:0xf\n\t"
      "v_mfma_f32_16x16x32_bf16 v[192:195], v[108:111], v[184:187], v[192:195]\n\t"
      "s_mov_b64 exec, s[82:83]\n\t"
      "global_store_dwordx4 v243, v[216:219], s[62:63] sc1\n\t"
      "s_mov_b64 exec, -1\n\t"
      "v_mfma_f32_16x16x32_bf16 v[192:195], v[124:127], v[188:191], v[192:195]\n\t"
      "s_waitcnt vmcnt(11)\n\t"
      "v_mfma_f32_16x16x32_bf16 v[196:199], v[0:3], v[128:131], 0\n\t"
      "s_add_u32 s58, s74, s42\n\t"
      "s_addc_u32 s59, s75, 0\n\t"
      "global_load_dwordx4 v[168:171], v240, s[58:59] sc1\n\t"
      "s_waitcnt vmcnt(11)\n\t"
      "v_mfma_f32_16x16x32_bf16 v[196:199], v[16:19], v[132:135], v[196:199]\n\t"
      "s_nop 0\n\t"
      "ds_write_b128 v241, v[192:195] offset:35840\n\t"
      "s_waitcnt vmcnt(9)\n\t"
      "v_mfma_f32_16x16x32_bf16 v[196:199], v[32:35], v[136:139], v[196:199]\n\t"
      "s_waitcnt vmcnt(8)\n\t"
      "v_mfma_f32_16x16x32_bf16 v[196:199], v[48:51], v[140:143], v[196:199]\n\t"
      "s_waitcnt vmcnt(7)\n\t"
      "v_mfma_f32_16x16x32_bf16 v[196:199], v[64:67], v[144:147], v[196:199]\n\t"
      "s_add_u32 s58, s74, s42\n\t"
      "s_addc_u32 s59, s75, 0\n\t"
      "global_load_dwordx4 v[172:175], v240, s[58:59] offset:1024 sc1\n\t"
      "s_waitcnt vmcnt(7)\n\t"
      "v_mfma_f32_16x16x32_bf16 v[196:199], v[80:83], v[148:151], v[196:199]\n\t"
      "s_waitcnt lgkmcnt(0)\n\t"
      "s_barrier\n\t"
      "ds_read_b64 v[212:213], v242 offset:32768\n\t"
      "ds_read_b64 v[214:215], v242 offset:36864\n\t"
      "s_waitcnt vmcnt(6)\n\t"
      "v_mfma_f32_16x16x32_bf16 v[196:199], v[96:99], v[152:155], v[196:199]\n\t"
      "ds_read_b64 v[216:217], v242 offset:40960\n\t"
      "ds_read_b64 v[218:219], v242 offset:45056\n\t"
      "s_waitcnt vmcnt(5)\n\t"
      "v_mfma_f32_16x16x32_bf16 v[196:199], v[112:115], v[156:159], v[196:199]\n\t"
      "ds_read_b64 v[220:221], v242 offset:49152\n\t"
      "ds_read_b64 v[222:223], v242 offset:53248\n\t"
      "v_mfma_f32_16x16x32_bf16 v[200:203], v[4:7], v[128:131], 0\n\t"
      "s_add_u32 s58, s74, s44\n\t"
      "s_addc_u32 s59, s75, 0\n\t"
      "global_load_dwordx4 v[176:179], v240, s[58:59] sc1\n\t"
      "ds_read_b64 v[224:225], v242 offset:57344\n\t"
      "ds_read_b64 v[226:227], v242 offset:61440\n\t"
      "v_mfma_f32_16x16x32_bf16 v[200:203], v[20:23], v[132:135], v[200:203]\n\t"
      "v_mfma_f32_16x16x32_bf16 v[200:203], v[36:39], v[136:139], v[200:203]\n\t"
      "s_waitcnt lgkmcnt(4)\n\t"
      "v_add_f32_e32 v212, v212, v214\n\t"
      "v_cmp_u_f32_e32 vcc, v196, v196\n\t"
      "s_cbranch_vccnz LA34R_%=\n\t"
      "LA34B_%=:\n\t"
      "ds_write_b128 v241, v[196:199]\n\t"
      "v_mfma_f32_16x16x32_bf16 v[200:203], v[52:55], v[140:143], v[200:203]\n\t"
      "v_add_f32_e32 v216, v216, v218\n\t"
      "v_add_f32_e32 v213, v213, v215\n\t"
      "v_mfma_f32_16x16x32_bf16 v[200:203], v[68:71], v[144:147], v[200:203]\n\t"
      "s_add_u32 s58, s74, s44\n\t"
      "s_addc_u32 s59, s75, 0\n\t"
      "global_load_dwordx4 v[180:183], v240, s[58:59] offset:1024 sc1\n\t"
      "v_add_f32_e32 v217, v217, v219\n\t"
      "s_waitcnt lgkmcnt(1)\n\t"
      "v_mfma_f32_16x16x32_bf16 v[200:203], v[84:87], v[148:151], v[200:203]\n\t"
      "v_add_f32_e32 v220, v220, v222\n\t"
      "v_add_f32_e32 v224, v224, v226\n\t"
      "v_mfma_f32_16x16x32_bf16 v[200:203], v[100:103], v[152:155], v[200:203]\n\t"
      "v_add_f32_e32 v221, v221, v223\n\t"
      "v_add_f32_e32 v225, v225, v227\n\t"
      "v_mfma_f32_16x16x32_bf16 v[200:203], v[116:119], v[156:159], v[200:203]\n\t"
      "v_add_f32_e32 v212, v212, v216\n\t"
      "v_add_f32_e32 v220, v220, v224\n\t"
      "v_mfma_f32_16x16x32_bf16 v[204:207], v[8:11], v[128:131], 0\n\t"
      "s_add_u32 s58, s74, s46\n\t"
      "s_addc_u32 s59, s75, 0\n\t"
      "global_load_dwordx4 v[184:187], v240, s[58:59] sc1\n\t"
      "v_add_f32_e32 v213, v213, v217\n\t"
      "v_add_f32_e32 v221, v221, v225\n\t"
      "v_mfma_f32_16x16x32_bf16 v[204:207], v[24:27], v[132:135], v[204:207]\n\t"
      "v_add_f32_e32 v212, v212, v244\n\t"
      "v_add_f32_e32 v213, v213, v245\n\t"
      "v_mfma_f32_16x16x32_bf16 v[204:207], v[40:43], v[136:139], v[204:207]\n\t"
      "v_add_f32_e32 v212, v212, v220\n\t"
      "v_add_f32_e32 v213, v213, v221\n\t"
      "ds_write_b128 v241, v[200:203] offset:1024\n\t"
      "v_mfma_f32_16x16x32_bf16 v[204:207], v[56:59], v[140:143], v[204:207]\n\t"
      "v_mul_f32_e32 v214, v212, v212\n\t"
      "v_mfma_f32_16x16x32_bf16 v[204:207], v[72:75], v[144:147], v[204:207]\n\t"
      "s_add_u32 s58, s74, s46\n\t"
      "s_addc_u32 s59, s75, 0\n\t"
      "global_load_dwordx4 v[188:191], v240, s[58:59] offset:1024 sc1\n\t"
      "v_mul_f32_e32 v215, v213, v213\n\t"
      "v_fmaak_f32 v214, v233, v214, 0xc0135761\n\t"
      "v_mfma_f32_16x16x32_bf16 v[204:207], v[88:91], v[148:151], v[204:207]\n\t"
      "v_fmaak_f32 v215, v233, v215, 0xc0135761\n\t"
      "v_mul_f32_e32 v214, v212, v214\n\t"
      "v_mfma_f32_16x16x32_bf16 v[204:207], v[104:107], v[152:155], v[204:207]\n\t"
      "v_mul_f32_e32 v215, v213, v215\n\t"
      "v_exp_f32_e32 v214, v214\n\t"
      "v_mfma_f32_16x16x32_bf16 v[204:207], v[120:123], v[156:159], v[204:207]\n\t"
      "v_exp_f32_e32 v215, v215\n\t"
      "v_add_f32_e32 v214, 1.0, v214\n\t"
      "v_mfma_f32_16x16x32_bf16 v[208:211], v[12:15], v[128:131], 0\n\t"
      "v_add_f32_e32 v215, 1.0, v215\n\t"
      "v_rcp_f32_e32 v214, v214\n\t"
      "s_add_u32 s58, s76, s40\n\t"
      "s_addc_u32 s59, s77, 0\n\t"
      "global_load_dwordx4 v[128:131], v240, s[58:59] sc1\n\t"
      "v_mfma_f32_16x16x32_bf16 v[208:211], v[28:31], v[132:135], v[208:211]\n\t"
      "v_rcp_f32_e32 v215, v215\n\t"
      "v_mul_f32_e32 v212, v212, v214\n\t"
      "v_mfma_f32_16x16x32_bf16 v[208:211], v[44:47], v[136:139], v[208:211]\n\t"
      "v_mul_f32_e32 v213, v213, v215\n\t"
      "v_cvt_pk_bf16_f32 v216, v212, v213\n\t"
      "ds_write_b128 v241, v[204:207] offset:2048\n\t"
      "v_mfma_f32_16x16x32_bf16 v[208:211], v[60:63], v[140:143], v[208:211]\n\t"
      "v_min_u32_e32 v216, -2, v216\n\t"
      "s_add_u32 s62, s62, 57344\n\t"
      "s_addc_u32 s63, s63, 0\n\t"
      "v_mfma_f32_16x16x32_bf16 v[208:211], v[76:79], v[144:147], v[208:211]\n\t"
      "v_mov_b32_dpp v217, v216 quad_perm:[1,1,1,1] row_mask:0xf bank_mask:0xf\n\t"
      "v_mov_b32_dpp v218, v216 quad_perm:[2,2,2,2] row_mask:0xf bank_mask:0xf\n\t"
      "s_add_u32 s58, s76, s40\n\t"
      "s_addc_u32 s59, s77, 0\n\t"
      "global_load_dwordx4 v[132:135], v240, s[58:59] offset:1024 sc1\n\t"
      "v_mfma_f32_16x16x32_bf16 v[208:211], v[92:95], v[148:151], v[208:211]\n\t"
      "v_mov_b32_dpp v219, v216 quad_perm:[3,3,3,3] row_mask:0xf bank_mask:0xf\n\t"
      "v_mfma_f32_16x16x32_bf16 v[208:211], v[108:111], v[152:155], v[208:211]\n\t"
      "s_mov_b64 exec, s[82:83]\n\t"
      "global_store_dwordx4 v243, v[216:219], s[62:63] sc1\n\t"
      "s_mov_b64 exec, -1\n\t"
      "v_mfma_f32_16x16x32_bf16 v[208:211], v[124:127], v[156:159], v[208:211]\n\t"
      "s_waitcnt vmcnt(11)\n\t"
      "v_mfma_f32_16x16x32_bf16 v[192:195], v[0:3], v[160:163], 0\n\t"
      "s_add_u32 s58, s76, s42\n\t"
      "s_addc_u32 s59, s77, 0\n\t"
      "global_load_dwordx4 v[136:139], v240, s[58:59] sc1\n\t"
      "s_waitcnt vmcnt(11)\n\t"
      "v_mfma_f32_16x16x32_bf16 v[192:195], v[16:19], v[164:167], v[192:195]\n\t"
      "s_nop 0\n\t"
      "ds_write_b128 v241, v[208:211] offset:3072\n\t"
      "s_waitcnt vmcnt(9)\n\t"
      "v_mfma_f32_16x16x32_bf16 v[192:195], v[32:35], v[168:171], v[192:195]\n\t"
      "s_waitcnt vmcnt(8)\n\t"
      "v_mfma_f32_16x16x32_bf16 v[192:195], v[48:51], v[172:175], v[192:195]\n\t"
      "s_waitcnt vmcnt(7)\n\t"
      "v_mfma_f32_16x16x32_bf16 v[192:195], v[64:67], v[176:179], v[192:195]\n\t"
      "s_add_u32 s58, s76, s42\n\t"
      "s_addc_u32 s59, s77, 0\n\t"
      "global_load_dwordx4 v[140:143], v240, s[58:59] offset:1024 sc1\n\t"
      "s_waitcnt vmcnt(7)\n\t"
      "v_mfma_f32_16x16x32_bf16 v[192:195], v[80:83], v[180:183], v[192:195]\n\t"
      "s_waitcnt lgkmcnt(0)\n\t"
      "s_barrier\n\t"
      "ds_read_b64 v[212:213], v242\n\t"
      "ds_read_b64 v[214:215], v242 offset:4096\n\t"
      "s_waitcnt vmcnt(6)\n\t"
      "v_mfma_f32_16x16x32_bf16 v[192:195], v[96:99], v[184:187], v[192:195]\n\t"
      "ds_read_b64 v[216:217], v242 offset:8192\n\t"
      "ds_read_b64 v[218:219], v242 offset:12288\n\t"
      "s_waitcnt vmcnt(5)\n\t"
      "v_mfma_f32_16x16x32_bf16 v[192:195], v[112:115], v[188:191], v[192:195]\n\t"
      "ds_read_b64 v[220:221], v242 offset:16384\n\t"
      "ds_read_b64 v[222:223], v242 offset:20480\n\t"
      "v_mfma_f32_16x16x32_bf16 v[196:199], v[4:7], v[160:163], 0\n\t"
      "s_add_u32 s58, s76, s44\n\t"
      "s_addc_u32 s59, s77, 0\n\t"
      "global_load_dwordx4 v[144:147], v240, s[58:59] sc1\n\t"
      "ds_read_b64 v[224:225], v242 offset:24576\n\t"
      "ds_read_b64 v[226:227], v242 offset:28672\n\t"
      "v_mfma_f32_16x16x32_bf16 v[196:199], v[20:23], v[164:167], v[196:199]\n\t"
      "v_mfma_f32_16x16x32_bf16 v[196:199], v[36:39], v[168:171], v[196:199]\n\t"
      "s_waitcnt lgkmcnt(4)\n\t"
      "v_add_f32_e32 v212, v212, v214\n\t"
      "v_cmp_u_f32_e32 vcc, v192, v192\n\t"
      "s_cbranch_vccnz LA35R_%=\n\t"
      "LA35B_%=:\n\t"
      "ds_write_b128 v241, v[192:195] offset:32768\n\t"
      "v_mfma_f32_16x16x32_bf16 v[196:199], v[52:55], v[172:175], v[196:199]\n\t"
      "v_add_f32_e32 v216, v216, v218\n\t"
      "v_add_f32_e32 v213, v213, v215\n\t"
      "v_mfma_f32_16x16x32_bf16 v[196:199], v[68:71], v[176:179], v[196:199]\n\t"
      "s_add_u32 s58, s76, s44\n\t"
      "s_addc_u32 s59, s77, 0\n\t"
      "global_load_dwordx4 v[148:151], v240, s[58:59] offset:1024 sc1\n\t"
      "v_add_f32_e32 v217, v217, v219\n\t"
      "s_waitcnt lgkmcnt(1)\n\t"
      "v_mfma_f32_16x16x32_bf16 v[196:199], v[84:87], v[180:183], v[196:199]\n\t"
      "v_add_f32_e32 v220, v220, v222\n\t"
      "v_add_f32_e32 v224, v224, v226\n\t"
      "v_mfma_f32_16x16x32_bf16 v[196:199], v[100:103], v[184:187], v[196:199]\n\t"
      "v_add_f32_e32 v221, v221, v223\n\t"
      "v_add_f32_e32 v225, v225, v227\n\t"
      "v_mfma_f32_16x16x32_bf16 v[196:199], v[116:119], v[188:191], v[196:199]\n\t"
      "v_add_f32_e32 v212, v212, v216\n\t"
      "v_add_f32_e32 v220, v220, v224\n\t"
      "v_mfma_f32_16x16x32_bf16 v[200:203], v[8:11], v[160:163], 0\n\t"
      "s_add_u32 s58, s76, s46\n\t"
      "s_addc_u32 s59, s77, 0\n\t"
      "global_load_dwordx4 v[152:155], v240, s[58:59] sc1\n\t"
      "v_add_f32_e32 v213, v213, v217\n\t"
      "v_add_f32_e32 v221, v221, v225\n\t"
      "v_mfma_f32_16x16x32_bf16 v[200:203], v[24:27], v[164:167], v[200:203]\n\t"
      "v_add_f32_e32 v212, v212, v244\n\t"
      "v_add_f32_e32 v213, v213, v245\n\t"
      "v_mfma_f32_16x16x32_bf16 v[200:203], v[40:43], v[168:171], v[200:203]\n\t"
      "v_add_f32_e32 v212, v212, v220\n\t"
      "v_add_f32_e32 v213, v213, v221\n\t"
      "ds_write_b128 v241, v[196:199] offset:33792\n\t"
      "v_mfma_f32_16x16x32_bf16 v[200:203], v[56:59], v[172:175], v[200:203]\n\t"
      "v_mul_f32_e32 v214, v212, v212\n\t"
      "v_mfma_f32_16x16x32_bf16 v[200:203], v[72:75], v[176:179], v[200:203]\n\t"
      "s_add_u32 s58, s76, s46\n\t"
      "s_addc_u32 s59, s77, 0\n\t"
      "global_load_dwordx4 v[156:159], v240, s[58:59] offset:1024 sc1\n\t"
      "v_mul_f32_e32 v215, v213, v213\n\t"
      "v_fmaak_f32 v214, v233, v214, 0xc0135761\n\t"
      "v_mfma_f32_16x16x32_bf16 v[200:203], v[88:91], v[180:183], v[200:203]\n\t"
      "v_fmaak_f32 v215, v233, v215, 0xc0135761\n\t"
      "v_mul_f32_e32 v214, v212, v214\n\t"
      "v_mfma_f32_16x16x32_bf16 v[200:203], v[104:107], v[184:187], v[200:203]\n\t"
      "v_mul_f32_e32 v215, v213, v215\n\t"
      "v_exp_f32_e32 v214, v214\n\t"
      "v_mfma_f32_16x16x32_bf16 v[200:203], v[120:123], v[188:191], v[200:203]\n\t"
      "v_exp_f32_e32 v215, v215\n\t"
      "v_add_f32_e32 v214, 1.0, v214\n\t"
      "v_mfma_f32_16x16x32_bf16 v[204:207], v[12:15], v[160:163], 0\n\t"
      "v_add_f32_e32 v215, 1.0, v215\n\t"
      "v_rcp_f32_e32 v214, v214\n\t"
      "v_mfma_f32_16x16x32_bf16 v[204:207], v[28:31], v[164:167], v[204:207]\n\t"
      "v_rcp_f32_e32 v215, v215\n\t"
      "v_mul_f32_e32 v212, v212, v214\n\t"
      "v_mfma_f32_16x16x32_bf16 v[204:207], v[44:47], v[168:171], v[204:207]\n\t"
      "v_mul_f32_e32 v213, v213, v215\n\t"
      "v_cvt_pk_bf16_f32 v216, v212, v213\n\t"
      "ds_write_b128 v241, v[200:203] offset:34816\n\t"
      "v_mfma_f32_16x16x32_bf16 v[204:207], v[60:63], v[172:175], v[204:207]\n\t"
      "v_min_u32_e32 v216, -2, v216\n\t"
      "s_add_u32 s62, s62, 57344\n\t"
      "s_addc_u32 s63, s63, 0\n\t"
      "v_mfma_f32_16x16x32_bf16 v[204:207], v[76:79], v[176:179], v[204:207]\n\t"
      "v_mov_b32_dpp v217, v216 quad_perm:[1,1,1,1] row_mask:0xf bank_mask:0xf\n\t"
      "v_mov_b32_dpp v218, v216 quad_perm:[2,2,2,2] row_mask:0xf bank_mask:0xf\n\t"
      "v_mfma_f32_16x16x32_bf16 v[204:207], v[92:95], v[180:183], v[204:207]\n\t"
      "v_mov_b32_dpp v219, v216 quad_perm:[3,3,3,3] row_mask:0xf bank_mask:0xf\n\t"
      "v_mfma_f32_16x16x32_bf16 v[204:207], v[108:111], v[184:187], v[204:207]\n\t"
      "s_mov_b64 exec, s[82:83]\n\t"
      "global_store_dwordx4 v243, v[216:219], s[62:63] sc1\n\t"
      "s_mov_b64 exec, -1\n\t"
      "v_mfma_f32_16x16x32_bf16 v[204:207], v[124:127], v[188:191], v[204:207]\n\t"
      "s_waitcnt vmcnt(9)\n\t"
      "v_mfma_f32_16x16x32_bf16 v[208:211], v[0:3], v[128:131], 0\n\t"
      "s_waitcnt vmcnt(8)\n\t"
      "v_mfma_f32_16x16x32_bf16 v[208:211], v[16:19], v[132:135], v[208:211]\n\t"
      "s_nop 3\n\t"
      "ds_write_b128 v241, v[204:207] offset:35840\n\t"
      "s_waitcnt vmcnt(6)\n\t"
      "v_mfma_f32_16x16x32_bf16 v[208:211], v[32:35], v[136:139], v[208:211]\n\t"
      "s_waitcnt vmcnt(5)\n\t"
      "v_mfma_f32_16x16x32_bf16 v[208:211], v[48:51], v[140:143], v[208:211]\n\t"
      "s_waitcnt vmcnt(4)\n\t"
      "v_mfma_f32_16x16x32_bf16 v[208:211], v[64:67], v[144:147], v[208:211]\n\t"
      "s_waitcnt vmcnt(3)\n\t"
      "v_mfma_f32_16x16x32_bf16 v[208:211], v[80:83], v[148:151], v[208:211]\n\t"
      "s_waitcnt lgkmcnt(0)\n\t"
      "s_barrier\n\t"
      "ds_read_b64 v[212:213], v242 offset:32768\n\t"
      "ds_read_b64 v[214:215], v242 offset:36864\n\t"
      "s_waitcnt vmcnt(2)\n\t"
      "v_mfma_f32_16x16x32_bf16 v[208:211], v[96:99], v[152:155], v[208:211]\n\t"
      "ds_read_b64 v[216:217], v242 offset:40960\n\t"
      "ds_read_b64 v[218:219], v242 offset:45056\n\t"
      "s_waitcnt vmcnt(1)\n\t"
      "v_mfma_f32_16x16x32_bf16 v[208:211], v[112:115], v[156:159], v[208:211]\n\t"
      "ds_read_b64 v[220:221], v242 offset:49152\n\t"
      "ds_read_b64 v[222:223], v242 offset:53248\n\t"
      "v_mfma_f32_16x16x32_bf16 v[192:195], v[4:7], v[128:131], 0\n\t"
      "ds_read_b64 v[224:225], v242 offset:57344\n\t"
      "ds_read_b64 v[226:227], v242 offset:61440\n\t"
      "v_mfma_f32_16x16x32_bf16 v[192:195], v[20:23], v[132:135], v[192:195]\n\t"
      "v_mfma_f32_16x16x32_bf16 v[192:195], v[36:39], v[136:139], v[192:195]\n\t"
      "s_waitcnt lgkmcnt(4)\n\t"
      "v_add_f32_e32 v212, v212, v214\n\t"
      "v_cmp_u_f32_e32 vcc, v208, v208\n\t"
      "s_cbranch_vccnz LA36R_%=\n\t"
      "LA36B_%=:\n\t"
      "ds_write_b128 v241, v[208:211]\n\t"
      "v_mfma_f32_16x16x32_bf16 v[192:195], v[52:55], v[140:143], v[192:195]\n\t"
      "v_add_f32_e32 v216, v216, v218\n\t"
      "v_add_f32_e32 v213, v213, v215\n\t"
      "v_mfma_f32_16x16x32_bf16 v[192:195], v[68:71], v[144:147], v[192:195]\n\t"
      "v_add_f32_e32 v217, v217, v219\n\t"
      "s_waitcnt lgkmcnt(1)\n\t"
      "v_mfma_f32_16x16x32_bf16 v[192:195], v[84:87], v[148:151], v[192:195]\n\t"
      "v_add_f32_e32 v220, v220, v222\n\t"
      "v_add_f32_e32 v224, v224, v226\n\t"
      "v_mfma_f32_16x16x32_bf16 v[192:195], v[100:103], v[152:155], v[192:195]\n\t"
      "v_add_f32_e32 v221, v221, v223\n\t"
      "v_add_f32_e32 v225, v225, v227\n\t"
      "v_mfma_f32_16x16x32_bf16 v[192:195], v[116:119], v[156:159], v[192:195]\n\t"
      "v_add_f32_e32 v212, v212, v216\n\t"
      "v_add_f32_e32 v220, v220, v224\n\t"
      "v_mfma_f32_16x16x32_bf16 v[196:199], v[8:11], v[128:131], 0\n\t"
      "v_add_f32_e32 v213, v213, v217\n\t"
      "v_add_f32_e32 v221, v221, v225\n\t"
      "v_mfma_f32_16x16x32_bf16 v[196:199], v[24:27], v[132:135], v[196:199]\n\t"
      "v_add_f32_e32 v212, v212, v244\n\t"
      "v_add_f32_e32 v213, v213, v245\n\t"
      "v_mfma_f32_16x16x32_bf16 v[196:199], v[40:43], v[136:139], v[196:199]\n\t"
      "v_add_f32_e32 v212, v212, v220\n\t"
      "v_add_f32_e32 v213, v213, v221\n\t"
      "ds_write_b128 v241, v[192:195] offset:1024\n\t"
      "v_mfma_f32_16x16x32_bf16 v[196:199], v[56:59], v[140:143], v[196:199]\n\t"
      "v_mul_f32_e32 v214, v212, v212\n\t"
      "v_mfma_f32_16x16x32_bf16 v[196:199], v[72:75], v[144:147], v[196:199]\n\t"
      "v_mul_f32_e32 v215, v213, v213\n\t"
      "v_fmaak_f32 v214, v233, v214, 0xc0135761\n\t"
      "v_mfma_f32_16x16x32_bf16 v[196:199], v[88:91], v[148:151], v[196:199]\n\t"
      "v_fmaak_f32 v215, v233, v215, 0xc0135761\n\t"
      "v_mul_f32_e32 v214, v212, v214\n\t"
      "v_mfma_f32_16x16x32_bf16 v[196:199], v[104:107], v[152:155], v[196:199]\n\t"
      "v_mul_f32_e32 v215, v213, v215\n\t"
      "v_exp_f32_e32 v214, v214\n\t"
      "v_mfma_f32_16x16x32_bf16 v[196:199], v[120:123], v[156:159], v[196:199]\n\t"
      "v_exp_f32_e32 v215, v215\n\t"
      "v_add_f32_e32 v214, 1.0, v214\n\t"
      "v_mfma_f32_16x16x32_bf16 v[200:203], v[12:15], v[128:131], 0\n\t"
      "v_add_f32_e32 v215, 1.0, v215\n\t"
      "v_rcp_f32_e32 v214, v214\n\t"
      "v_mfma_f32_16x16x32_bf16 v[200:203], v[28:31], v[132:135], v[200:203]\n\t"
      "v_rcp_f32_e32 v215, v215\n\t"
      "v_mul_f32_e32 v212, v212, v214\n\t"
      "v_mfma_f32_16x16x32_bf16 v[200:203], v[44:47], v[136:139], v[200:203]\n\t"
      "v_mul_f32_e32 v213, v213, v215\n\t"
      "v_cvt_pk_bf16_f32 v216, v212, v213\n\t"
      "ds_write_b128 v241, v[196:199] offset:2048\n\t"
      "v_mfma_f32_16x16x32_bf16 v[200:203], v[60:63], v[140:143], v[200:203]\n\t"
      "v_min_u32_e32 v216, -2, v216\n\t"
      "s_add_u32 s62, s62, 57344\n\t"
      "s_addc_u32 s63, s63, 0\n\t"
      "v_mfma_f32_16x16x32_bf16 v[200:203], v[76:79], v[144:147], v[200:203]\n\t"
      "v_mov_b32_dpp v217, v216 quad_perm:[1,1,1,1] row_mask:0xf bank_mask:0xf\n\t"
      "v_mov_b32_dpp v218, v216 quad_perm:[2,2,2,2] row_mask:0xf bank_mask:0xf\n\t"
      "v_mfma_f32_16x16x32_bf16 v[200:203], v[92:95], v[148:151], v[200:203]\n\t"
      "v_mov_b32_dpp v219, v216 quad_perm:[3,3,3,3] row_mask:0xf bank_mask:0xf\n\t"
      "v_mfma_f32_16x16x32_bf16 v[200:203], v[108:111], v[152:155], v[200:203]\n\t"
      "s_mov_b64 exec, s[82:83]\n\t"
      "global_store_dwordx4 v243, v[216:219], s[62:63] sc1\n\t"
      "s_mov_b64 exec, -1\n\t"
      "v_mfma_f32_16x16x32_bf16 v[200:203], v[124:127], v[156:159], v[200:203]\n\t"
      "s_nop 7\n\t"
      "ds_write_b128 v241, v[200:203] offset:3072\n\t"
      "s_waitcnt lgkmcnt(0)\n\t"
      "s_barrier\n\t"
      "ds_read_b64 v[212:213], v242\n\t"
      "ds_read_b64 v[214:215], v242 offset:4096\n\t"
      "ds_read_b64 v[216:217], v242 offset:8192\n\t"
      "ds_read_b64 v[218:219], v242 offset:12288\n\t"
      "ds_read_b64 v[220:221], v242 offset:16384\n\t"
      "ds_read_b64 v[222:223], v242 offset:20480\n\t"
      "ds_read_b64 v[224:225], v242 offset:24576\n\t"
      "ds_read_b64 v[226:227], v242 offset:28672\n\t"
      "s_waitcnt lgkmcnt(4)\n\t"
      "v_add_f32_e32 v212, v212, v214\n\t"
      "v_add_f32_e32 v216, v216, v218\n\t"
      "v_add_f32_e32 v213, v213, v215\n\t"
      "v_add_f32_e32 v217, v217, v219\n\t"
      "s_waitcnt lgkmcnt(0)\n\t"
      "v_add_f32_e32 v220, v220, v222\n\t"
      "v_add_f32_e32 v224, v224, v226\n\t"
      "v_add_f32_e32 v221, v221, v223\n\t"
      "v_add_f32_e32 v225, v225, v227\n\t"
      "v_add_f32_e32 v212, v212, v216\n\t"
      "v_add_f32_e32 v220, v220, v224\n\t"
      "v_add_f32_e32 v213, v213, v217\n\t"
      "v_add_f32_e32 v221, v221, v225\n\t"
      "v_add_f32_e32 v212, v212, v244\n\t"
      "v_add_f32_e32 v213, v213, v245\n\t"
      "v_add_f32_e32 v212, v212, v220\n\t"
      "v_add_f32_e32 v213, v213, v221\n\t"
      "v_mul_f32_e32 v214, v212, v212\n\t"
      "v_mul_f32_e32 v215, v213, v213\n\t"
      "v_fmaak_f32 v214, v233, v214, 0xc0135761\n\t"
      "v_fmaak_f32 v215, v233, v215, 0xc0135761\n\t"
      "v_mul_f32_e32 v214, v212, v214\n\t"
      "v_mul_f32_e32 v215, v213, v215\n\t"
      "v_exp_f32_e32 v214, v214\n\t"
      "v_exp_f32_e32 v215, v215\n\t"
      "v_add_f32_e32 v214, 1.0, v214\n\t"
      "v_add_f32_e32 v215, 1.0, v215\n\t"
      "v_rcp_f32_e32 v214, v214\n\t"
      "v_rcp_f32_e32 v215, v215\n\t"
      "v_mul_f32_e32 v212, v212, v214\n\t"
      "v_mul_f32_e32 v213, v213, v215\n\t"
      "v_cvt_pk_bf16_f32 v216, v212, v213\n\t"
      "v_min_u32_e32 v216, -2, v216\n\t"
      "s_add_u32 s62, s62, 57344\n\t"
      "s_addc_u32 s63, s63, 0\n\t"
      "v_mov_b32_dpp v217, v216 quad_perm:[1,1,1,1] row_mask:0xf bank_mask:0xf\n\t"
      "v_mov_b32_dpp v218, v216 quad_perm:[2,2,2,2] row_mask:0xf bank_mask:0xf\n\t"
      "v_mov_b32_dpp v219, v216 quad_perm:[3,3,3,3] row_mask:0xf bank_mask:0xf\n\t"
      "s_mov_b64 exec, s[82:83]\n\t"
      "global_store_dwordx4 v243, v[216:219], s[62:63] sc1\n\t"
      "s_mov_b64 exec, -1\n\t"
      "s_or_b32 s34, s2, s24\n\t"
      "s_cbranch_scc1 EPB_%=\n\t"
      "v_mov_b32_e32 v212, s22\n\t"
      "v_mov_b32_e32 v213, s23\n\t"
      "v_mov_b32_e32 v214, 0\n\t"
      "s_mov_b64 exec, 1\n\t"
      "global_store_dwordx2 v214, v[212:213], s[60:61] sc1\n\t"
      "s_mov_b64 exec, -1\n\t"
      "EPB_%=:\n\t"
      "s_endpgm\n\t"
      "LPATH_%=:\n\t"
      "s_add_u32 s22, s22, 1\n\t"
      "s_addc_u32 s23, s23, 0\n\t"
      "s_add_u32 s72, s26, 1\n\t"
      "s_and_b32 s72, s72, 6\n\t"
      "s_cselect_b32 s72, 0, 2\n\t"
      "s_or_b32 s27, s27, s72\n\t"
      "s_mul_i32 s72, s81, 172032\n\t"
      "s_add_u32 s72, s72, s37\n\t"
      "s_add_u32 s40, s6, s72\n\t"
      "s_addc_u32 s41, s7, 0\n\t"
      "s_add_u32 s81, s81, 1\n\t"
      "s_cmp_eq_u32 s81, 7\n\t"
      "s_cselect_b32 s81, 0, s81\n\t"
      "s_add_u32 m0, s39, 81920\n\t"
      "s_nop 0\n\t"
      "global_load_lds_dwordx4 v148, s[40:41]\n\t"
      "global_load_lds_dwordx4 v149, s[40:41] offset:1024\n\t"
      "global_load_lds_dwordx4 v150, s[40:41] offset:2048\n\t"
      "global_load_lds_dwordx4 v151, s[40:41] offset:3072\n\t"
      "s_mul_i32 s72, s81, 172032\n\t"
      "s_add_u32 s72, s72, s37\n\t"
      "s_add_u32 s40, s6, s72\n\t"
      "s_addc_u32 s41, s7, 0\n\t"
      "s_add_u32 s81, s81, 1\n\t"
      "s_cmp_eq_u32 s81, 7\n\t"
      "s_cselect_b32 s81, 0, s81\n\t"
      "s_add_u32 m0, s39, 98304\n\t"
      "s_nop 0\n\t"
      "global_load_lds_dwordx4 v148, s[40:41]\n\t"
      "global_load_lds_dwordx4 v149, s[40:41] offset:1024\n\t"
      "s_waitcnt vmcnt(14)\n\t"
      "s_barrier\n\t"
      "s_and_b32 s34, s34, 2\n\t"
      "s_lshl_b32 s34, s34, 6\n\t"
      "s_add_u32 s34, s34, s35\n\t"
      "s_mul_i32 s79, s34, 3584\n\t"
      "v_add_u32_e32 v158, 0x10000, v224\n\t"
      "v_add_u32_e32 v159, 0x10000, v225\n\t"
      "v_add_u32_e32 v160, 0x10000, v226\n\t"
      "v_add_u32_e32 v161, 0x10000, v227\n\t"
      "s_lshl_b32 s34, s25, 8\n\t"
      "s_add_u32 s34, s34, 4096\n\t"
      "v_and_b32_e32 v136, 1, v192\n\t"
      "v_lshl_add_u32 v136, v136, 7, s34\n\t"
      "s_lshl_b32 s34, s25, 7\n\t"
      "s_add_u32 s34, s34, 16384\n\t"
      "v_mov_b32_e32 v137, s34\n\t"
      "s_lshl_b32 s34, s2, 7\n\t"
      "s_add_u32 s34, s34, 32768\n\t"
      "v_mov_b32_e32 v143, s34\n\t"
      "s_bitcmp0_b32 s27, 1\n\t"
      "s_cbranch_scc1 SK6_%=\n\t"
      "s_lshr_b32 s73, s26, 2\n\t"
      "s_lshl1_add_u32 s73, s25, s73\n\t"
      "s_lshl_b32 s72, s73, 7\n\t"
      "s_add_u32 s72, s72, 4096\n\t"
      "v_mov_b32_e32 v130, s72\n\t"
      "s_lshl2_add_u32 s75, s73, s24\n\t"
      "v_mov_b32_e32 v128, s22\n\t"
      "v_mov_b32_e32 v129, s23\n\t"
      "v_lshrrev_b32_e32 v140, 1, v240\n\t"
      "s_lshl_b32 s72, s75, 9\n\t"
      "s_add_u32 s72, s72, 12582912\n\t"
      "v_mov_b32_e32 v132, -1\n\t"
      "v_mov_b32_e32 v133, -1\n\t"
      "v_mov_b32_e32 v134, -1\n\t"
      "v_mov_b32_e32 v135, -1\n\t"
      "s_add_u32 s42, s20, s72\n\t"
      "s_addc_u32 s43, s21, 0\n\t"
      "global_store_dwordx2 v140, v[132:133], s[42:43] sc1\n\t"
      "s_lshl_b32 s72, s75, 13\n\t"
      "s_add_u32 s72, s72, 4096\n\t"
      "s_add_u32 s42, s20, s72\n\t"
      "s_addc_u32 s43, s21, 0\n\t"
      "global_store_dwordx4 v240, v[132:135], s[42:43] offset:-4096 sc1\n\t"
      "global_store_dwordx4 v240, v[132:135], s[42:43] offset:-3072 sc1\n\t"
      "global_store_dwordx4 v240, v[132:135], s[42:43] offset:-2048 sc1\n\t"
      "SK6_%=:\n\t"
      "s_waitcnt vmcnt(10)\n\t"
      "s_barrier\n\t"
      "global_load_lds_dwordx4 v150, s[40:41] offset:2048\n\t"
      "global_load_lds_dwordx4 v151, s[40:41] offset:3072\n\t"
      "s_bitcmp0_b32 s27, 1\n\t"
      "s_cbranch_scc1 SK7_%=\n\t"
      "global_store_dwordx4 v240, v[132:135], s[42:43] offset:-1024 sc1\n\t"
      "global_store_dwordx4 v240, v[132:135], s[42:43] sc1\n\t"
      "global_store_dwordx4 v240, v[132:135], s[42:43] offset:1024 sc1\n\t"
      "SK7_%=:\n\t"
      "s_waitcnt vmcnt(8)\n\t"
      "s_barrier\n\t"
      "s_mul_i32 s72, s81, 172032\n\t"
      "s_add_u32 s72, s72, s37\n\t"
      "s_add_u32 s40, s6, s72\n\t"
      "s_addc_u32 s41, s7, 0\n\t"
      "s_add_u32 s81, s81, 1\n\t"
      "s_cmp_eq_u32 s81, 7\n\t"
      "s_cselect_b32 s81, 0, s81\n\t"
      "s_add_u32 m0, s39, 114688\n\t"
      "s_nop 0\n\t"
      "global_load_lds_dwordx4 v148, s[40:41]\n\t"
      "global_load_lds_dwordx4 v149, s[40:41] offset:1024\n\t"
      "global_load_lds_dwordx4 v150, s[40:41] offset:2048\n\t"
      "global_load_lds_dwordx4 v151, s[40:41] offset:3072\n\t"
      "s_bitcmp0_b32 s27, 1\n\t"
      "s_cbranch_scc1 SK8_%=\n\t"
      "global_store_dwordx4 v240, v[132:135], s[42:43] offset:2048 sc1\n\t"
      "global_store_dwordx4 v240, v[132:135], s[42:43] offset:3072 sc1\n\t"
      "SK8_%=:\n\t"
      "s_waitcnt vmcnt(8)\n\t"
      "s_barrier\n\t"
      "s_mul_i32 s72, s81, 172032\n\t"
      "s_add_u32 s72, s72, s37\n\t"
      "s_add_u32 s40, s6, s72\n\t"
      "s_addc_u32 s41, s7, 0\n\t"
      "s_add_u32 s81, s81, 1\n\t"
      "s_cmp_eq_u32 s81, 7\n\t"
      "s_cselect_b32 s81, 0, s81\n\t"
      "s_add_u32 m0, s39, 131072\n\t"
      "s_nop 0\n\t"
      "global_load_lds_dwordx4 v148, s[40:41]\n\t"
      "global_load_lds_dwordx4 v149, s[40:41] offset:1024\n\t"
      "global_load_lds_dwordx4 v150, s[40:41] offset:2048\n\t"
      "global_load_lds_dwordx4 v151, s[40:41] offset:3072\n\t"
      "s_waitcnt vmcnt(8)\n\t"
      "s_bitcmp0_b32 s27, 1\n\t"
      "s_cbranch_scc1 SK9_%=\n\t"
      "s_waitcnt vmcnt(4)\n\t"
      "SK9_%=:\n\t"
      "s_barrier\n\t"
      "s_mul_i32 s72, s81, 172032\n\t"
      "s_add_u32 s72, s72, s37\n\t"
      "s_add_u32 s72, s72, 1792\n\t"
      "s_add_u32 s40, s6, s72\n\t"
      "s_addc_u32 s41, s7, 0\n\t"
      "s_add_u32 s81, s81, 1\n\t"
      "s_cmp_eq_u32 s81, 7\n\t"
      "s_cselect_b32 s81, 0, s81\n\t"
      "s_add_u32 m0, s39, 147456\n\t"
      "s_nop 0\n\t"
      "global_load_lds_dwordx4 v148, s[40:41]\n\t"
      "global_load_lds_dwordx4 v152, s[40:41] offset:1024\n\t"
      "s_mul_i32 s72, s81, 172032\n\t"
      "s_add_u32 s72, s72, s37\n\t"
      "s_add_u32 s72, s72, 1792\n\t"
      "s_add_u32 s40, s6, s72\n\t"
      "s_addc_u32 s41, s7, 0\n\t"
      "s_add_u32 s81, s81, 1\n\t"
      "s_cmp_eq_u32 s81, 7\n\t"
      "s_cselect_b32 s81, 0, s81\n\t"
      "s_add_u32 m0, s39, 149504\n\t"
      "s_nop 0\n\t"
      "global_load_lds_dwordx4 v148, s[40:41]\n\t"
      "global_load_lds_dwordx4 v152, s[40:41] offset:1024\n\t"
      "s_bitcmp0_b32 s27, 1\n\t"
      "s_cbranch_scc1 SK10_%=\n\t"
      "s_cmp_eq_u32 s24, 0\n\t"
      "s_cselect_b64 exec, 1, 0\n\t"
      "global_store_dwordx2 v130, v[128:129], s[60:61] sc1\n\t"
      "s_mov_b64 exec, -1\n\t"
      "SK10_%=:\n\t"
      "s_waitcnt vmcnt(8)\n\t"
      "s_barrier\n\t"
      "s_mul_i32 s72, s81, 172032\n\t"
      "s_add_u32 s72, s72, s37\n\t"
      "s_add_u32 s72, s72, 1792\n\t"
      "s_add_u32 s40, s6, s72\n\t"
      "s_addc_u32 s41, s7, 0\n\t"
      "s_add_u32 s81, s81, 1\n\t"
      "s_cmp_eq_u32 s81, 7\n\t"
      "s_cselect_b32 s81, 0, s81\n\t"
      "s_add_u32 m0, s39, 65536\n\t"
      "s_nop 0\n\t"
      "global_load_lds_dwordx4 v148, s[40:41]\n\t"
      "global_load_lds_dwordx4 v152, s[40:41] offset:1024\n\t"
      "s_mul_i32 s72, s81, 172032\n\t"
      "s_add_u32 s72, s72, s37\n\t"
      "s_add_u32 s72, s72, 1792\n\t"
      "s_add_u32 s40, s6, s72\n\t"
      "s_addc_u32 s41, s7, 0\n\t"
      "s_add_u32 s81, s81, 1\n\t"
      "s_cmp_eq_u32 s81, 7\n\t"
      "s_cselect_b32 s81, 0, s81\n\t"
      "s_add_u32 m0, s39, 67584\n\t"
      "s_nop 0\n\t"
      "global_load_lds_dwordx4 v148, s[40:41]\n\t"
      "global_load_lds_dwordx4 v152, s[40:41] offset:1024\n\t"
      "s_waitcnt vmcnt(8)\n\t"
      "s_barrier\n\t"
      "s_mul_i32 s72, s81, 172032\n\t"
      "s_add_u32 s72, s72, s37\n\t"
      "s_add_u32 s72, s72, 1792\n\t"
      "s_add_u32 s40, s6, s72\n\t"
      "s_addc_u32 s41, s7, 0\n\t"
      "s_add_u32 s81, s81, 1\n\t"
      "s_cmp_eq_u32 s81, 7\n\t"
      "s_cselect_b32 s81, 0, s81\n\t"
      "s_add_u32 m0, s39, 81920\n\t"
      "s_nop 0\n\t"
      "global_load_lds_dwordx4 v148, s[40:41]\n\t"
      "global_load_lds_dwordx4 v152, s[40:41] offset:1024\n\t"
      "s_mul_i32 s72, s81, 172032\n\t"
      "s_add_u32 s72, s72, s37\n\t"
      "s_add_u32 s72, s72, 1792\n\t"
      "s_add_u32 s40, s6, s72\n\t"
      "s_addc_u32 s41, s7, 0\n\t"
      "s_add_u32 s81, s81, 1\n\t"
      "s_cmp_eq_u32 s81, 7\n\t"
      "s_cselect_b32 s81, 0, s81\n\t"
      "s_add_u32 m0, s39, 83968\n\t"
      "s_nop 0\n\t"
      "global_load_lds_dwordx4 v148, s[40:41]\n\t"
      "global_load_lds_dwordx4 v152, s[40:41] offset:1024\n\t"
      "s_waitcnt vmcnt(8)\n\t"
      "s_barrier\n\t"
      "s_cmp_lg_u32 s24, 3\n\t"
      "s_cbranch_scc1 SK11_%=\n\t"
      "global_load_dwordx2 v[138:139], v136, s[60:61] sc1\n\t"
      "SK11_%=:\n\t"
      "s_mul_i32 s72, s81, 172032\n\t"
      "s_add_u32 s72, s72, s37\n\t"
      "s_add_u32 s72, s72, 1792\n\t"
      "s_add_u32 s40, s6, s72\n\t"
      "s_addc_u32 s41, s7, 0\n\t"
      "s_add_u32 s81, s81, 1\n\t"
      "s_cmp_eq_u32 s81, 7\n\t"
      "s_cselect_b32 s81, 0, s81\n\t"
      "s_lshr_b32 s72, s39, 1\n\t"
      "s_add_u32 m0, s72, 98304\n\t"
      "s_nop 0\n\t"
      "global_load_lds_dwordx4 v148, s[40:41]\n\t"
      "global_load_lds_dwordx4 v152, s[40:41] offset:1024\n\t"
      "s_mul_i32 s72, s24, 14336\n\t"
      "s_add_u32 s40, s56, s72\n\t"
      "s_addc_u32 s41, s57, 0\n\t"
      "s_lshr_b32 s72, s39, 1\n\t"
      "s_add_u32 m0, s72, 106496\n\t"
      "s_nop 0\n\t"
      "global_load_lds_dwordx4 v154, s[40:41]\n\t"
      "global_load_lds_dwordx4 v155, s[40:41] offset:1024\n\t"
      "s_mul_i32 s72, s24, 28672\n\t"
      "s_add_u32 s72, s72, 57344\n\t"
      "s_add_u32 s40, s56, s72\n\t"
      "s_addc_u32 s41, s57, 0\n\t"
      "s_lshr_b32 s72, s39, 0\n\t"
      "s_add_u32 m0, s72, 114688\n\t"
      "s_nop 0\n\t"
      "global_load_lds_dwordx4 v154, s[40:41]\n\t"
      "global_load_lds_dwordx4 v155, s[40:41] offset:1024\n\t"
      "global_load_lds_dwordx4 v156, s[40:41] offset:2048\n\t"
      "global_load_lds_dwordx4 v157, s[40:41] offset:3072\n\t"
      "global_load_dword v245, v244, s[12:13]\n\t"
      "s_cmp_lt_u32 s31, 14\n\t"
      "s_cbranch_scc1 NTA_%=\n\t"
      "s_lshl2_add_u32 s72, s31, s24\n\t"
      "s_sub_u32 s72, s72, 56\n\t"
      "s_mov_b32 m0, 64512\n\t"
      "s_cmp_ge_u32 s72, 64\n\t"
      "s_cselect_b32 s73, 8, 0\n\t"
      "s_sub_u32 s73, s72, s73\n\t"
      "s_and_b32 s73, s73, 0x38\n\t"
      "s_add_u32 s72, s72, s73\n\t"
      "s_mul_i32 s72, s72, 57344\n\t"
      "s_and_b32 s73, s2, 7\n\t"
      "s_mul_i32 s73, s73, 448\n\t"
      "s_and_b32 s73, s73, 0xffffff80\n\t"
      "s_add_u32 s72, s72, s73\n\t"
      "v_lshrrev_b32_e32 v141, 2, v192\n\t"
      "v_mul_u32_u24_e32 v141, 3584, v141\n\t"
      "v_lshl_add_u32 v141, v196, 7, v141\n\t"
      "v_add_u32_e32 v141, s72, v141\n\t"
      "global_load_lds_dword v141, s[10:11]\n\t"
      "NTA_%=:\n\t"
      "s_waitcnt vmcnt(13)\n\t"
      "s_barrier\n\t"
      "s_waitcnt vmcnt(9)\n\t"
      "s_barrier\n\t"
      "s_mul_i32 s72, s24, 28672\n\t"
      "s_add_u32 s72, s72, 172032\n\t"
      "s_add_u32 s40, s56, s72\n\t"
      "s_addc_u32 s41, s57, 0\n\t"
      "s_lshr_b32 s72, s39, 0\n\t"
      "s_add_u32 m0, s72, 131072\n\t"
      "s_nop 0\n\t"
      "global_load_lds_dwordx4 v154, s[40:41]\n\t"
      "global_load_lds_dwordx4 v155, s[40:41] offset:1024\n\t"
      "global_load_lds_dwordx4 v156, s[40:41] offset:2048\n\t"
      "global_load_lds_dwordx4 v157, s[40:41] offset:3072\n\t"
      "s_waitcnt vmcnt(11)\n\t"
      "s_cmp_lg_u32 s24, 3\n\t"
      "s_cbranch_scc1 LF1B_%=\n\t"
      "v_cmp_ne_u64_e32 vcc, s[22:23], v[138:139]\n\t"
      "s_cbranch_vccnz LF1R_%=\n\t"
      "LF1B_%=:\n\t"
      "s_barrier\n\t"
      "s_mul_i32 s72, s24, 28672\n\t"
      "s_add_u32 s72, s72, 286720\n\t"
      "s_add_u32 s40, s56, s72\n\t"
      "s_addc_u32 s41, s57, 0\n\t"
      "s_lshr_b32 s72, s39, 0\n\t"
      "s_add_u32 m0, s72, 147456\n\t"
      "s_nop 0\n\t"
      "global_load_lds_dwordx4 v154, s[40:41]\n\t"
      "global_load_lds_dwordx4 v155, s[40:41] offset:1024\n\t"
      "global_load_lds_dwordx4 v156, s[40:41] offset:2048\n\t"
      "global_load_lds_dwordx4 v157, s[40:41] offset:3072\n\t"
      "s_barrier\n\t"
      "s_cmp_eq_u32 s26, 7\n\t"
      "s_cbranch_scc1 SPARE_%=\n\t"
      "s_waitcnt vmcnt(0)\n\t"
      "s_barrier\n\t"
      "s_barrier\n\t"
      "s_bitcmp0_b32 s27, 0\n\t"
      "s_cbranch_scc1 L4_%=\n\t"
      "s_add_u32 s72, s76, 3670016\n\t"
      "s_add_u32 s40, s52, s72\n\t"
      "s_addc_u32 s41, s53, 0\n\t"
      "s_add_u32 m0, s39, 16384\n\t"
      "s_nop 0\n\t"
      "global_load_lds_dwordx4 v144, s[40:41]\n\t"
      "global_load_lds_dwordx4 v145, s[40:41] offset:1024\n\t"
      "global_load_lds_dwordx4 v146, s[40:41] offset:2048\n\t"
      "global_load_lds_dwordx4 v147, s[40:41] offset:3072\n\t"
      "s_add_u32 s72, s76, 3899392\n\t"
      "s_add_u32 s40, s52, s72\n\t"
      "s_addc_u32 s41, s53, 0\n\t"
      "s_add_u32 m0, s39, 49152\n\t"
      "s_nop 0\n\t"
      "global_load_lds_dwordx4 v144, s[40:41]\n\t"
      "global_load_lds_dwordx4 v145, s[40:41] offset:1024\n\t"
      "global_load_lds_dwordx4 v146, s[40:41] offset:2048\n\t"
      "global_load_lds_dwordx4 v147, s[40:41] offset:3072\n\t"
      "s_add_u32 s72, s77, 3670016\n\t"
      "s_add_u32 s40, s52, s72\n\t"
      "s_addc_u32 s41, s53, 0\n\t"
      "s_add_u32 m0, s39, 0\n\t"
      "s_nop 0\n\t"
      "global_load_lds_dwordx4 v144, s[40:41]\n\t"
      "global_load_lds_dwordx4 v145, s[40:41] offset:1024\n\t"
      "global_load_lds_dwordx4 v146, s[40:41] offset:2048\n\t"
      "global_load_lds_dwordx4 v147, s[40:41] offset:3072\n\t"
      "s_add_u32 s72, s77, 3899392\n\t"
      "s_add_u32 s40, s52, s72\n\t"
      "s_addc_u32 s41, s53, 0\n\t"
      "s_add_u32 m0, s39, 32768\n\t"
      "s_nop 0\n\t"
      "global_load_lds_dwordx4 v144, s[40:41]\n\t"
      "global_load_lds_dwordx4 v145, s[40:41] offset:1024\n\t"
      "global_load_lds_dwordx4 v146, s[40:41] offset:2048\n\t"
      "global_load_lds_dwordx4 v147, s[40:41] offset:3072\n\t"
      "s_add_u32 s72, s76, 0\n\t"
      "s_add_u32 s40, s52, s72\n\t"
      "s_addc_u32 s41, s53, 0\n\t"
      "s_add_u32 m0, s39, 65536\n\t"
      "s_nop 0\n\t"
      "global_load_lds_dwordx4 v144, s[40:41]\n\t"
      "global_load_lds_dwordx4 v145, s[40:41] offset:1024\n\t"
      "global_load_lds_dwordx4 v146, s[40:41] offset:2048\n\t"
      "global_load_lds_dwordx4 v147, s[40:41] offset:3072\n\t"
      "s_add_u32 s72, s76, 229376\n\t"
      "s_add_u32 s40, s52, s72\n\t"
      "s_addc_u32 s41, s53, 0\n\t"
      "s_add_u32 m0, s39, 81920\n\t"
      "s_nop 0\n\t"
      "global_load_lds_dwordx4 v144, s[40:41]\n\t"
      "global_load_lds_dwordx4 v145, s[40:41] offset:1024\n\t"
      "global_load_lds_dwordx4 v146, s[40:41] offset:2048\n\t"
      "global_load_lds_dwordx4 v147, s[40:41] offset:3072\n\t"
      "s_waitcnt vmcnt(8)\n\t"
      "s_barrier\n\t"
      "s_cmp_lg_u32 s24, 3\n\t"
      "s_cbranch_scc1 SK13_%=\n\t"
      "global_load_dwordx2 v[138:139], v137, s[60:61] sc1\n\t"
      "SK13_%=:\n\t"
      "s_add_u32 s72, s77, 0\n\t"
      "s_add_u32 s40, s52, s72\n\t"
      "s_addc_u32 s41, s53, 0\n\t"
      "s_add_u32 m0, s39, 98304\n\t"
      "s_nop 0\n\t"
      "global_load_lds_dwordx4 v144, s[40:41]\n\t"
      "global_load_lds_dwordx4 v145, s[40:41] offset:1024\n\t"
      "global_load_lds_dwordx4 v146, s[40:41] offset:2048\n\t"
      "global_load_lds_dwordx4 v147, s[40:41] offset:3072\n\t"
      "s_add_u32 s72, s77, 229376\n\t"
      "s_add_u32 s40, s52, s72\n\t"
      "s_addc_u32 s41, s53, 0\n\t"
      "s_add_u32 m0, s39, 114688\n\t"
      "s_nop 0\n\t"
      "global_load_lds_dwordx4 v144, s[40:41]\n\t"
      "global_load_lds_dwordx4 v145, s[40:41] offset:1024\n\t"
      "global_load_lds_dwordx4 v146, s[40:41] offset:2048\n\t"
      "global_load_lds_dwordx4 v147, s[40:41] offset:3072\n\t"
      "s_waitcnt vmcnt(12)\n\t"
      "ds_read_b64_tr_b16 v[0:1], v158\n\t"
      "ds_read_b64_tr_b16 v[2:3], v158 offset:512\n\t"
      "ds_read_b64_tr_b16 v[4:5], v159\n\t"
      "ds_read_b64_tr_b16 v[6:7], v159 offset:512\n\t"
      "ds_read_b64_tr_b16 v[8:9], v160\n\t"
      "ds_read_b64_tr_b16 v[10:11], v160 offset:512\n\t"
      "ds_read_b64_tr_b16 v[12:13], v161\n\t"
      "ds_read_b64_tr_b16 v[14:15], v161 offset:512\n\t"
      "s_waitcnt vmcnt(8)\n\t"
      "ds_read_b64_tr_b16 v[16:17], v158 offset:16384\n\t"
      "ds_read_b64_tr_b16 v[18:19], v158 offset:16896\n\t"
      "ds_read_b64_tr_b16 v[20:21], v159 offset:16384\n\t"
      "ds_read_b64_tr_b16 v[22:23], v159 offset:16896\n\t"
      "ds_read_b64_tr_b16 v[24:25], v160 offset:16384\n\t"
      "ds_read_b64_tr_b16 v[26:27], v160 offset:16896\n\t"
      "ds_read_b64_tr_b16 v[28:29], v161 offset:16384\n\t"
      "ds_read_b64_tr_b16 v[30:31], v161 offset:16896\n\t"
      "s_cmp_lg_u32 s24, 3\n\t"
      "s_cbranch_scc1 LF2B_%=\n\t"
      "s_waitcnt vmcnt(8)\n\t"
      "v_cmp_ne_u64_e32 vcc, s[22:23], v[138:139]\n\t"
      "s_cbranch_vccnz LF2R_%=\n\t"
      "LF2B_%=:\n\t"
      "s_barrier\n\t"
      "s_waitcnt lgkmcnt(0)\n\t"
      "s_add_u32 s72, s78, 3670016\n\t"
      "s_add_u32 s40, s52, s72\n\t"
      "s_addc_u32 s41, s53, 0\n\t"
      "s_add_u32 m0, s39, 16384\n\t"
      "s_nop 0\n\t"
      "global_load_lds_dwordx4 v144, s[40:41]\n\t"
      "global_load_lds_dwordx4 v145, s[40:41] offset:1024\n\t"
      "global_load_lds_dwordx4 v146, s[40:41] offset:2048\n\t"
      "global_load_lds_dwordx4 v147, s[40:41] offset:3072\n\t"
      "s_add_u32 s72, s78, 3899392\n\t"
      "s_add_u32 s40, s52, s72\n\t"
      "s_addc_u32 s41, s53, 0\n\t"
      "s_add_u32 m0, s39, 49152\n\t"
      "s_nop 0\n\t"
      "global_load_lds_dwordx4 v144, s[40:41]\n\t"
      "global_load_lds_dwordx4 v145, s[40:41] offset:1024\n\t"
      "global_load_lds_dwordx4 v146, s[40:41] offset:2048\n\t"
      "global_load_lds_dwordx4 v147, s[40:41] offset:3072\n\t"
      "s_add_u32 s72, s79, 3670016\n\t"
      "s_add_u32 s40, s52, s72\n\t"
      "s_addc_u32 s41, s53, 0\n\t"
      "s_add_u32 m0, s39, 0\n\t"
      "s_nop 0\n\t"
      "global_load_lds_dwordx4 v144, s[40:41]\n\t"
      "global_load_lds_dwordx4 v145, s[40:41] offset:1024\n\t"
      "global_load_lds_dwordx4 v146, s[40:41] offset:2048\n\t"
      "global_load_lds_dwordx4 v147, s[40:41] offset:3072\n\t"
      "s_add_u32 s72, s79, 3899392\n\t"
      "s_add_u32 s40, s52, s72\n\t"
      "s_addc_u32 s41, s53, 0\n\t"
      "s_add_u32 m0, s39, 32768\n\t"
      "s_nop 0\n\t"
      "global_load_lds_dwordx4 v144, s[40:41]\n\t"
      "global_load_lds_dwordx4 v145, s[40:41] offset:1024\n\t"
      "global_load_lds_dwordx4 v146, s[40:41] offset:2048\n\t"
      "global_load_lds_dwordx4 v147, s[40:41] offset:3072\n\t"
      "s_add_u32 s72, s78, 0\n\t"
      "s_add_u32 s40, s52, s72\n\t"
      "s_addc_u32 s41, s53, 0\n\t"
      "s_add_u32 m0, s39, 65536\n\t"
      "s_nop 0\n\t"
      "global_load_lds_dwordx4 v144, s[40:41]\n\t"
      "global_load_lds_dwordx4 v145, s[40:41] offset:1024\n\t"
      "global_load_lds_dwordx4 v146, s[40:41] offset:2048\n\t"
      "global_load_lds_dwordx4 v147, s[40:41] offset:3072\n\t"
      "s_add_u32 s72, s78, 229376\n\t"
      "s_add_u32 s40, s52, s72\n\t"
      "s_addc_u32 s41, s53, 0\n\t"
      "s_add_u32 m0, s39, 81920\n\t"
      "s_nop 0\n\t"
      "global_load_lds_dwordx4 v144, s[40:41]\n\t"
      "global_load_lds_dwordx4 v145, s[40:41] offset:1024\n\t"
      "global_load_lds_dwordx4 v146, s[40:41] offset:2048\n\t"
      "global_load_lds_dwordx4 v147, s[40:41] offset:3072\n\t"
      "s_waitcnt vmcnt(28)\n\t"
      "ds_read_b64_tr_b16 v[32:33], v158 offset:32768\n\t"
      "ds_read_b64_tr_b16 v[34:35], v158 offset:33280\n\t"
      "ds_read_b64_tr_b16 v[36:37], v159 offset:32768\n\t"
      "ds_read_b64_tr_b16 v[38:39], v159 offset:33280\n\t"
      "ds_read_b64_tr_b16 v[40:41], v160 offset:32768\n\t"
      "ds_read_b64_tr_b16 v[42:43], v160 offset:33280\n\t"
      "ds_read_b64_tr_b16 v[44:45], v161 offset:32768\n\t"
      "ds_read_b64_tr_b16 v[46:47], v161 offset:33280\n\t"
      "s_waitcnt lgkmcnt(0)\n\t"
      "s_add_u32 s72, s79, 0\n\t"
      "s_add_u32 s40, s52, s72\n\t"
      "s_addc_u32 s41, s53, 0\n\t"
      "s_add_u32 m0, s39, 98304\n\t"
      "s_nop 0\n\t"
      "global_load_lds_dwordx4 v144, s[40:41]\n\t"
      "global_load_lds_dwordx4 v145, s[40:41] offset:1024\n\t"
      "global_load_lds_dwordx4 v146, s[40:41] offset:2048\n\t"
      "global_load_lds_dwordx4 v147, s[40:41] offset:3072\n\t"
      "s_waitcnt vmcnt(28)\n\t"
      "ds_read_b64_tr_b16 v[48:49], v158 offset:49152\n\t"
      "ds_read_b64_tr_b16 v[50:51], v158 offset:49664\n\t"
      "ds_read_b64_tr_b16 v[52:53], v159 offset:49152\n\t"
      "ds_read_b64_tr_b16 v[54:55], v159 offset:49664\n\t"
      "ds_read_b64_tr_b16 v[56:57], v160 offset:49152\n\t"
      "ds_read_b64_tr_b16 v[58:59], v160 offset:49664\n\t"
      "ds_read_b64_tr_b16 v[60:61], v161 offset:49152\n\t"
      "ds_read_b64_tr_b16 v[62:63], v161 offset:49664\n\t"
      "s_waitcnt lgkmcnt(0)\n\t"
      "s_add_u32 s72, s79, 229376\n\t"
      "s_add_u32 s40, s52, s72\n\t"
      "s_addc_u32 s41, s53, 0\n\t"
      "s_add_u32 m0, s39, 114688\n\t"
      "s_nop 0\n\t"
      "global_load_lds_dwordx4 v144, s[40:41]\n\t"
      "global_load_lds_dwordx4 v145, s[40:41] offset:1024\n\t"
      "global_load_lds_dwordx4 v146, s[40:41] offset:2048\n\t"
      "global_load_lds_dwordx4 v147, s[40:41] offset:3072\n\t"
      "s_waitcnt vmcnt(16)\n\t"
      "s_barrier\n\t"
      "s_cmp_lg_u32 s24, 3\n\t"
      "s_cbranch_scc1 SK14_%=\n\t"
      "global_load_dwordx2 v[138:139], v143, s[60:61] sc1\n\t"
      "SK14_%=:\n\t"
      "s_waitcnt vmcnt(12)\n\t"
      "ds_read_b64_tr_b16 v[64:65], v158\n\t"
      "ds_read_b64_tr_b16 v[66:67], v158 offset:512\n\t"
      "ds_read_b64_tr_b16 v[68:69], v159\n\t"
      "ds_read_b64_tr_b16 v[70:71], v159 offset:512\n\t"
      "ds_read_b64_tr_b16 v[72:73], v160\n\t"
      "ds_read_b64_tr_b16 v[74:75], v160 offset:512\n\t"
      "ds_read_b64_tr_b16 v[76:77], v161\n\t"
      "ds_read_b64_tr_b16 v[78:79], v161 offset:512\n\t"
      "s_waitcnt vmcnt(8)\n\t"
      "ds_read_b64_tr_b16 v[80:81], v158 offset:16384\n\t"
      "ds_read_b64_tr_b16 v[82:83], v158 offset:16896\n\t"
      "ds_read_b64_tr_b16 v[84:85], v159 offset:16384\n\t"
      "ds_read_b64_tr_b16 v[86:87], v159 offset:16896\n\t"
      "ds_read_b64_tr_b16 v[88:89], v160 offset:16384\n\t"
      "ds_read_b64_tr_b16 v[90:91], v160 offset:16896\n\t"
      "ds_read_b64_tr_b16 v[92:93], v161 offset:16384\n\t"
      "ds_read_b64_tr_b16 v[94:95], v161 offset:16896\n\t"
      "s_waitcnt vmcnt(4)\n\t"
      "ds_read_b64_tr_b16 v[96:97], v158 offset:32768\n\t"
      "ds_read_b64_tr_b16 v[98:99], v158 offset:33280\n\t"
      "ds_read_b64_tr_b16 v[100:101], v159 offset:32768\n\t"
      "ds_read_b64_tr_b16 v[102:103], v159 offset:33280\n\t"
      "ds_read_b64_tr_b16 v[104:105], v160 offset:32768\n\t"
      "ds_read_b64_tr_b16 v[106:107], v160 offset:33280\n\t"
      "ds_read_b64_tr_b16 v[108:109], v161 offset:32768\n\t"
      "ds_read_b64_tr_b16 v[110:111], v161 offset:33280\n\t"
      "s_waitcnt vmcnt(0)\n\t"
      "ds_read_b64_tr_b16 v[112:113], v158 offset:49152\n\t"
      "ds_read_b64_tr_b16 v[114:115], v158 offset:49664\n\t"
      "ds_read_b64_tr_b16 v[116:117], v159 offset:49152\n\t"
      "ds_read_b64_tr_b16 v[118:119], v159 offset:49664\n\t"
      "ds_read_b64_tr_b16 v[120:121], v160 offset:49152\n\t"
      "ds_read_b64_tr_b16 v[122:123], v160 offset:49664\n\t"
      "ds_read_b64_tr_b16 v[124:125], v161 offset:49152\n\t"
      "ds_read_b64_tr_b16 v[126:127], v161 offset:49664\n\t"
      "s_cmp_lg_u32 s24, 3\n\t"
      "s_cbranch_scc1 LF3B_%=\n\t"
      "s_waitcnt vmcnt(0)\n\t"
      "v_cmp_ne_u64_e32 vcc, s[22:23], v[138:139]\n\t"
      "s_cbranch_vccnz LF3R_%=\n\t"
      "LF3B_%=:\n\t"
      "s_waitcnt lgkmcnt(0)\n\t"
      "s_branch GST_%=\n\t"
      "G4_%=:\n\t"
      "s_mul_i32 s34, s30, 57344\n\t"
      "s_mul_i32 s35, s24, 7168\n\t"
      "s_add_u32 s34, s34, s35\n\t"
      "s_add_u32 s34, s34, 1835008\n\t"
      "s_add_u32 s64, s48, s34\n\t"
      "s_addc_u32 s65, s49, 0\n\t"
      "s_and_b32 s34, s30, 3\n\t"
      "s_lshl_b32 s34, s34, 11\n\t"
      "s_add_u32 s40, s34, 0\n\t"
      "s_sub_u32 s35, s40, 7168\n\t"
      "s_min_u32 s40, s40, s35\n\t"
      "s_add_u32 s41, s34, 1024\n\t"
      "s_sub_u32 s35, s41, 7168\n\t"
      "s_min_u32 s41, s41, s35\n\t"
      "s_add_u32 s42, s34, 2048\n\t"
      "s_sub_u32 s35, s42, 7168\n\t"
      "s_min_u32 s42, s42, s35\n\t"
      "s_add_u32 s43, s34, 3072\n\t"
      "s_sub_u32 s35, s43, 7168\n\t"
      "s_min_u32 s43, s43, s35\n\t"
      "s_add_u32 s44, s34, 4096\n\t"
      "s_sub_u32 s35, s44, 7168\n\t"
      "s_min_u32 s44, s44, s35\n\t"
      "s_add_u32 s45, s34, 5120\n\t"
      "s_sub_u32 s35, s45, 7168\n\t"
      "s_min_u32 s45, s45, s35\n\t"
      "s_add_u32 s46, s34, 6144\n\t"
      "s_sub_u32 s35, s46, 7168\n\t"
      "s_min_u32 s46, s46, s35\n\t"
      "s_add_u32 s66, s64, 57344\n\t"
      "s_addc_u32 s67, s65, 0\n\t"
      "s_add_u32 s68, s66, 57344\n\t"
      "s_addc_u32 s69, s67, 0\n\t"
      "s_add_u32 s70, s68, 57344\n\t"
      "s_addc_u32 s71, s69, 0\n\t"
      "s_add_u32 s72, s70, 57344\n\t"
      "s_addc_u32 s73, s71, 0\n\t"
      "s_add_u32 s74, s72, 57344\n\t"
      "s_addc_u32 s75, s73, 0\n\t"
      "s_add_u32 s76, s74, 57344\n\t"
      "s_addc_u32 s77, s75, 0\n\t"
      "s_mov_b32 s82, 0x11111111\n\t"
      "s_mov_b32 s83, 0x11111111\n\t"
      "v_lshrrev_b32_e32 v224, 7, v240\n\t"
      "v_and_b32_e32 v225, 0x70, v240\n\t"
      "v_and_b32_e32 v226, 6, v224\n\t"
      "v_lshlrev_b32_e32 v226, 4, v226\n\t"
      "v_xor_b32_e32 v226, v226, v225\n\t"
      "v_lshl_or_b32 v212, v224, 12, v226\n\t"
      "s_mul_i32 s34, s24, 917504\n\t"
      "s_lshl_b32 s35, s29, 7\n\t"
      "s_add_u32 s34, s34, s35\n\t"
      "s_lshl_b32 s39, s24, 12\n\t"
      "s_add_u32 s54, s14, s34\n\t"
      "s_addc_u32 s55, s15, 0\n\t"
      "v_add_u32_e32 v213, 31744, v212\n\t"
      "v_add_u32_e32 v214, 63488, v212\n\t"
      "v_add_u32_e32 v215, 95232, v212\n\t"
      "s_lshl_b32 s37, s40, 7\n\t"
      "s_add_u32 s58, s54, s37\n\t"
      "s_addc_u32 s59, s55, 0\n\t"
      "s_add_u32 m0, s39, 0\n\t"
      "s_nop 0\n\t"
      "global_load_lds_dwordx4 v212, s[58:59]\n\t"
      "global_load_lds_dwordx4 v213, s[58:59] offset:1024\n\t"
      "global_load_lds_dwordx4 v214, s[58:59] offset:2048\n\t"
      "global_load_lds_dwordx4 v215, s[58:59] offset:3072\n\t"
      "s_lshl_b32 s37, s41, 7\n\t"
      "s_add_u32 s58, s54, s37\n\t"
      "s_addc_u32 s59, s55, 0\n\t"
      "s_add_u32 m0, s39, 32768\n\t"
      "s_nop 0\n\t"
      "global_load_lds_dwordx4 v212, s[58:59]\n\t"
      "global_load_lds_dwordx4 v213, s[58:59] offset:1024\n\t"
      "global_load_lds_dwordx4 v214, s[58:59] offset:2048\n\t"
      "global_load_lds_dwordx4 v215, s[58:59] offset:3072\n\t"
      "s_lshl_b32 s37, s42, 7\n\t"
      "s_add_u32 s58, s54, s37\n\t"
      "s_addc_u32 s59, s55, 0\n\t"
      "s_add_u32 m0, s39, 65536\n\t"
      "s_nop 0\n\t"
      "global_load_lds_dwordx4 v212, s[58:59]\n\t"
      "global_load_lds_dwordx4 v213, s[58:59] offset:1024\n\t"
      "global_load_lds_dwordx4 v214, s[58:59] offset:2048\n\t"
      "global_load_lds_dwordx4 v215, s[58:59] offset:3072\n\t"
      "s_lshl_b32 s37, s43, 7\n\t"
      "s_add_u32 s58, s54, s37\n\t"
      "s_addc_u32 s59, s55, 0\n\t"
      "s_add_u32 m0, s39, 98304\n\t"
      "s_nop 0\n\t"
      "global_load_lds_dwordx4 v212, s[58:59]\n\t"
      "global_load_lds_dwordx4 v213, s[58:59] offset:1024\n\t"
      "global_load_lds_dwordx4 v214, s[58:59] offset:2048\n\t"
      "global_load_lds_dwordx4 v215, s[58:59] offset:3072\n\t"
      "s_lshr_b32 s34, s24, 1\n\t"
      "s_lshl2_add_u32 s35, s29, s34\n\t"
      "s_and_b32 s36, s24, 1\n\t"
      "s_lshl4_add_u32 s35, s35, s36\n\t"
      "v_lshl_add_u32 v226, v224, 1, s35\n\t"
      "v_lshlrev_b32_e32 v244, 1, v226\n\t"
      "global_load_ushort v245, v244, s[16:17]\n\t"
      "s_movk_i32 s35, 896\n\t"
      "v_and_b32_e32 v243, 64, v240\n\t"
      "v_lshrrev_b32_e32 v243, 2, v243\n\t"
      "v_mad_u32_u24 v243, v226, s35, v243\n\t"
      "v_and_b32_e32 v226, 0x30, v240\n\t"
      "v_lshl_add_u32 v243, v226, 1, v243\n\t"
      "s_mov_b32 s82, 0x77777777\n\t"
      "s_mov_b32 s83, 0x77777777\n\t"
      "s_lshl_b32 s35, s30, 5\n\t"
      "s_add_u32 s62, s18, s35\n\t"
      "s_addc_u32 s63, s19, 0\n\t"
      "s_mul_i32 s35, s29, 28\n\t"
      "s_add_u32 s35, s35, s30\n\t"
      "s_lshl1_add_u32 s35, s35, s36\n\t"
      "s_lshl2_add_u32 s35, s35, s34\n\t"
      "s_lshl_b32 s35, s35, 8\n\t"
      "s_add_u32 s35, s35, 6291456\n\t"
      "s_add_u32 s50, s20, s35\n\t"
      "s_addc_u32 s51, s21, 0\n\t"
      "v_lshrrev_b32_e32 v244, 2, v240\n\t"
      "v_bfe_u32 v226, v240, 8, 1\n\t"
      "v_lshlrev_b32_e32 v226, 4, v226\n\t"
      "v_xor_b32_e32 v241, v240, v226\n\t"
      "v_bfe_u32 v226, v240, 9, 1\n\t"
      "v_lshlrev_b32_e32 v226, 7, v226\n\t"
      "v_xor_b32_e32 v241, v241, v226\n\t"
      "s_add_u32 s35, s39, 98304\n\t"
      "v_add_u32_e32 v241, s35, v241\n\t"
      "v_lshl_or_b32 v242, v224, 1, s36\n\t"
      "v_bfe_u32 v226, v240, 5, 1\n\t"
      "v_xor_b32_e32 v242, v242, v226\n\t"
      "v_bfe_u32 v226, v240, 6, 1\n\t"
      "v_lshlrev_b32_e32 v226, 3, v226\n\t"
      "v_xor_b32_e32 v242, v242, v226\n\t"
      "v_lshlrev_b32_e32 v242, 4, v242\n\t"
      "v_and_b32_e32 v226, 0x60, v240\n\t"
      "v_lshl_or_b32 v242, v226, 3, v242\n\t"
      "v_bfe_u32 v226, v240, 4, 1\n\t"
      "v_lshl_or_b32 v242, v226, 3, v242\n\t"
      "s_lshl_b32 s34, s34, 10\n\t"
      "s_add_u32 s34, s34, 98304\n\t"
      "v_add_u32_e32 v242, s34, v242\n\t"
      "v_and_b32_e32 v224, 0x3c0, v240\n\t"
      "v_lshlrev_b32_e32 v224, 1, v224\n\t"
      "v_and_b32_e32 v225, 0x180, v240\n\t"
      "v_lshrrev_b32_e32 v225, 2, v225\n\t"
      "v_and_b32_e32 v226, 0x30, v240\n\t"
      "v_lshrrev_b32_e32 v226, 1, v226\n\t"
      "v_or3_b32 v216, v224, v225, v226\n\t"
      "v_add_u32_e32 v216, s39, v216\n\t"
      "v_min_u32_e32 v233, 432, v240\n\t"
      "v_lshlrev_b32_e32 v233, 4, v233\n\t"
      "v_xor_b32_e32 v217, 32, v216\n\t"
      "v_xor_b32_e32 v218, 64, v216\n\t"
      "v_xor_b32_e32 v219, 96, v216\n\t"
      "v_xor_b32_e32 v220, 65536, v216\n\t"
      "v_xor_b32_e32 v221, 65568, v216\n\t"
      "v_xor_b32_e32 v222, 65600, v216\n\t"
      "v_xor_b32_e32 v223, 65632, v216\n\t"
      "s_waitcnt vmcnt(13)\n\t"
      "ds_read_b64_tr_b16 v[0:1], v216\n\t"
      "ds_read_b64_tr_b16 v[2:3], v216 offset:2048\n\t"
      "ds_read_b64_tr_b16 v[4:5], v217\n\t"
      "ds_read_b64_tr_b16 v[6:7], v217 offset:2048\n\t"
      "ds_read_b64_tr_b16 v[8:9], v218\n\t"
      "ds_read_b64_tr_b16 v[10:11], v218 offset:2048\n\t"
      "ds_read_b64_tr_b16 v[12:13], v219\n\t"
      "ds_read_b64_tr_b16 v[14:15], v219 offset:2048\n\t"
      "s_waitcnt lgkmcnt(0)\n\t"
      "s_lshl_b32 s37, s44, 7\n\t"
      "s_add_u32 s58, s54, s37\n\t"
      "s_addc_u32 s59, s55, 0\n\t"
      "s_add_u32 m0, s39, 0\n\t"
      "s_nop 0\n\t"
      "global_load_lds_dwordx4 v212, s[58:59]\n\t"
      "global_load_lds_dwordx4 v213, s[58:59] offset:1024\n\t"
      "global_load_lds_dwordx4 v214, s[58:59] offset:2048\n\t"
      "global_load_lds_dwordx4 v215, s[58:59] offset:3072\n\t"
      "s_waitcnt vmcnt(13)\n\t"
      "ds_read_b64_tr_b16 v[16:17], v216 offset:32768\n\t"
      "ds_read_b64_tr_b16 v[18:19], v216 offset:34816\n\t"
      "ds_read_b64_tr_b16 v[20:21], v217 offset:32768\n\t"
      "ds_read_b64_tr_b16 v[22:23], v217 offset:34816\n\t"
      "ds_read_b64_tr_b16 v[24:25], v218 offset:32768\n\t"
      "ds_read_b64_tr_b16 v[26:27], v218 offset:34816\n\t"
      "ds_read_b64_tr_b16 v[28:29], v219 offset:32768\n\t"
      "ds_read_b64_tr_b16 v[30:31], v219 offset:34816\n\t"
      "s_waitcnt lgkmcnt(0)\n\t"
      "s_lshl_b32 s37, s45, 7\n\t"
      "s_add_u32 s58, s54, s37\n\t"
      "s_addc_u32 s59, s55, 0\n\t"
      "s_add_u32 m0, s39, 32768\n\t"
      "s_nop 0\n\t"
      "global_load_lds_dwordx4 v212, s[58:59]\n\t"
      "global_load_lds_dwordx4 v213, s[58:59] offset:1024\n\t"
      "global_load_lds_dwordx4 v214, s[58:59] offset:2048\n\t"
      "global_load_lds_dwordx4 v215, s[58:59] offset:3072\n\t"
      "s_waitcnt vmcnt(13)\n\t"
      "ds_read_b64_tr_b16 v[32:33], v220\n\t"
      "ds_read_b64_tr_b16 v[34:35], v220 offset:2048\n\t"
      "ds_read_b64_tr_b16 v[36:37], v221\n\t"
      "ds_read_b64_tr_b16 v[38:39], v221 offset:2048\n\t"
      "ds_read_b64_tr_b16 v[40:41], v222\n\t"
      "ds_read_b64_tr_b16 v[42:43], v222 offset:2048\n\t"
      "ds_read_b64_tr_b16 v[44:45], v223\n\t"
      "ds_read_b64_tr_b16 v[46:47], v223 offset:2048\n\t"
      "s_waitcnt lgkmcnt(0)\n\t"
      "s_lshl_b32 s37, s46, 7\n\t"
      "s_add_u32 s58, s54, s37\n\t"
      "s_addc_u32 s59, s55, 0\n\t"
      "s_add_u32 m0, s39, 65536\n\t"
      "s_nop 0\n\t"
      "global_load_lds_dwordx4 v212, s[58:59]\n\t"
      "global_load_lds_dwordx4 v213, s[58:59] offset:1024\n\t"
      "global_load_lds_dwordx4 v214, s[58:59] offset:2048\n\t"
      "global_load_lds_dwordx4 v215, s[58:59] offset:3072\n\t"
      "s_waitcnt vmcnt(13)\n\t"
      "ds_read_b64_tr_b16 v[48:49], v220 offset:32768\n\t"
      "ds_read_b64_tr_b16 v[50:51], v220 offset:34816\n\t"
      "ds_read_b64_tr_b16 v[52:53], v221 offset:32768\n\t"
      "ds_read_b64_tr_b16 v[54:55], v221 offset:34816\n\t"
      "ds_read_b64_tr_b16 v[56:57], v222 offset:32768\n\t"
      "ds_read_b64_tr_b16 v[58:59], v222 offset:34816\n\t"
      "ds_read_b64_tr_b16 v[60:61], v223 offset:32768\n\t"
      "ds_read_b64_tr_b16 v[62:63], v223 offset:34816\n\t"
      "s_waitcnt vmcnt(8)\n\t"
      "ds_read_b64_tr_b16 v[64:65], v216\n\t"
      "ds_read_b64_tr_b16 v[66:67], v216 offset:2048\n\t"
      "ds_read_b64_tr_b16 v[68:69], v217\n\t"
      "ds_read_b64_tr_b16 v[70:71], v217 offset:2048\n\t"
      "ds_read_b64_tr_b16 v[72:73], v218\n\t"
      "ds_read_b64_tr_b16 v[74:75], v218 offset:2048\n\t"
      "ds_read_b64_tr_b16 v[76:77], v219\n\t"
      "ds_read_b64_tr_b16 v[78:79], v219 offset:2048\n\t"
      "s_waitcnt vmcnt(4)\n\t"
      "ds_read_b64_tr_b16 v[80:81], v216 offset:32768\n\t"
      "ds_read_b64_tr_b16 v[82:83], v216 offset:34816\n\t"
      "ds_read_b64_tr_b16 v[84:85], v217 offset:32768\n\t"
      "ds_read_b64_tr_b16 v[86:87], v217 offset:34816\n\t"
      "ds_read_b64_tr_b16 v[88:89], v218 offset:32768\n\t"
      "ds_read_b64_tr_b16 v[90:91], v218 offset:34816\n\t"
      "ds_read_b64_tr_b16 v[92:93], v219 offset:32768\n\t"
      "ds_read_b64_tr_b16 v[94:95], v219 offset:34816\n\t"
      "s_waitcnt vmcnt(0)\n\t"
      "ds_read_b64_tr_b16 v[96:97], v220\n\t"
      "ds_read_b64_tr_b16 v[98:99], v220 offset:2048\n\t"
      "ds_read_b64_tr_b16 v[100:101], v221\n\t"
      "ds_read_b64_tr_b16 v[102:103], v221 offset:2048\n\t"
      "ds_read_b64_tr_b16 v[104:105], v222\n\t"
      "ds_read_b64_tr_b16 v[106:107], v222 offset:2048\n\t"
      "ds_read_b64_tr_b16 v[108:109], v223\n\t"
      "ds_read_b64_tr_b16 v[110:111], v223 offset:2048\n\t"
      "v_lshlrev_b32_e32 v245, 16, v245\n\t"
      "s_waitcnt lgkmcnt(0)\n\t"
      "s_sub_u32 s34, s29, 28\n\t"
      "s_min_u32 s34, s34, s29\n\t"
      "s_lshl_b32 s34, s34, 11\n\t"
      "s_add_u32 s34, s34, 1792\n\t"
      "v_mov_b32_e32 v234, s34\n\t"
      "s_cmp_lg_u32 s24, 3\n\t"
      "s_cbranch_scc1 SK16_%=\n\t"
      "global_load_dwordx2 v[138:139], v143, s[60:61] sc1\n\t"
      "SK16_%=:\n\t"
      "s_cmp_lg_u32 s24, 3\n\t"
      "s_cbranch_scc1 LF5B_%=\n\t"
      "s_waitcnt vmcnt(0)\n\t"
      "v_cmp_ne_u64_e32 vcc, s[22:23], v[138:139]\n\t"
      "s_cbranch_vccnz LF5R_%=\n\t"
      "LF5B_%=:\n\t"
      "s_barrier\n\t"
      "s_cmp_eq_u32 s24, 0\n\t"
      "s_cbranch_scc1 PG2R_%=\n\t"
      "PG2B_%=:\n\t"
      "s_barrier\n\t"
      "s_add_u32 s58, s64, s40\n\t"
      "s_addc_u32 s59, s65, 0\n\t"
      "global_load_dwordx4 v[128:131], v240, s[58:59] sc1\n\t"
      "s_add_u32 s58, s64, s41\n\t"
      "s_addc_u32 s59, s65, 0\n\t"
      "global_load_dwordx4 v[132:135], v240, s[58:59] sc1\n\t"
      "s_add_u32 s58, s64, s42\n\t"
      "s_addc_u32 s59, s65, 0\n\t"
      "global_load_dwordx4 v[136:139], v240, s[58:59] sc1\n\t"
      "s_add_u32 s58, s64, s43\n\t"
      "s_addc_u32 s59, s65, 0\n\t"
      "global_load_dwordx4 v[140:143], v240, s[58:59] sc1\n\t"
      "s_add_u32 s58, s64, s44\n\t"
      "s_addc_u32 s59, s65, 0\n\t"
      "global_load_dwordx4 v[144:147], v240, s[58:59] sc1\n\t"
      "s_add_u32 s58, s64, s45\n\t"
      "s_addc_u32 s59, s65, 0\n\t"
      "global_load_dwordx4 v[148:151], v240, s[58:59] sc1\n\t"
      "s_add_u32 s58, s64, s46\n\t"
      "s_addc_u32 s59, s65, 0\n\t"
      "global_load_dwordx4 v[152:155], v240, s[58:59] sc1\n\t"
      "s_waitcnt vmcnt(6)\n\t"
      "v_mfma_f32_16x16x32_bf16 v[192:195], v[128:131], v[0:3], 0\n\t"
      "s_add_u32 s58, s66, s40\n\t"
      "s_addc_u32 s59, s67, 0\n\t"
      "global_load_dwordx4 v[160:163], v240, s[58:59] sc1\n\t"
      "s_waitcnt vmcnt(6)\n\t"
      "v_mfma_f32_16x16x32_bf16 v[192:195], v[132:135], v[16:19], v[192:195]\n\t"
      "s_waitcnt vmcnt(5)\n\t"
      "v_mfma_f32_16x16x32_bf16 v[192:195], v[136:139], v[32:35], v[192:195]\n\t"
      "s_add_u32 s58, s66, s41\n\t"
      "s_addc_u32 s59, s67, 0\n\t"
      "global_load_dwordx4 v[164:167], v240, s[58:59] sc1\n\t"
      "s_waitcnt vmcnt(5)\n\t"
      "v_mfma_f32_16x16x32_bf16 v[192:195], v[140:143], v[48:51], v[192:195]\n\t"
      "s_waitcnt vmcnt(4)\n\t"
      "v_mfma_f32_16x16x32_bf16 v[192:195], v[144:147], v[64:67], v[192:195]\n\t"
      "s_add_u32 s58, s66, s42\n\t"
      "s_addc_u32 s59, s67, 0\n\t"
      "global_load_dwordx4 v[168:171], v240, s[58:59] sc1\n\t"
      "s_waitcnt vmcnt(4)\n\t"
      "v_mfma_f32_16x16x32_bf16 v[192:195], v[148:151], v[80:83], v[192:195]\n\t"
      "s_waitcnt vmcnt(3)\n\t"
      "v_mfma_f32_16x16x32_bf16 v[192:195], v[152:155], v[96:99], v[192:195]\n\t"
      "s_add_u32 s58, s66, s43\n\t"
      "s_addc_u32 s59, s67, 0\n\t"
      "global_load_dwordx4 v[172:175], v240, s[58:59] sc1\n\t"
      "v_mfma_f32_16x16x32_bf16 v[196:199], v[128:131], v[4:7], 0\n\t"
      "v_mfma_f32_16x16x32_bf16 v[196:199], v[132:135], v[20:23], v[196:199]\n\t"
      "s_add_u32 s58, s66, s44\n\t"
      "s_addc_u32 s59, s67, 0\n\t"
      "global_load_dwordx4 v[176:179], v240, s[58:59] sc1\n\t"
      "v_mfma_f32_16x16x32_bf16 v[196:199], v[136:139], v[36:39], v[196:199]\n\t"
      "v_add_f32_e32 v232, v192, v193\n\t"
      "v_add_f32_e32 v232, v232, v194\n\t"
      "v_cmp_u_f32_e32 vcc, v232, v195\n\t"
      "s_cbranch_vccnz LA40R_%=\n\t"
      "LA40B_%=:\n\t"
      "ds_write_b128 v241, v[192:195]\n\t"
      "v_mfma_f32_16x16x32_bf16 v[196:199], v[140:143], v[52:55], v[196:199]\n\t"
      "s_add_u32 s58, s66, s45\n\t"
      "s_addc_u32 s59, s67, 0\n\t"
      "global_load_dwordx4 v[180:183], v240, s[58:59] sc1\n\t"
      "v_mfma_f32_16x16x32_bf16 v[196:199], v[144:147], v[68:71], v[196:199]\n\t"
      "v_mfma_f32_16x16x32_bf16 v[196:199], v[148:151], v[84:87], v[196:199]\n\t"
      "global_load_dword v236, v244, s[50:51] sc1\n\t"
      "s_add_u32 s50, s50, 2048\n\t"
      "s_addc_u32 s51, s51, 0\n\t"
      "s_add_u32 s58, s66, s46\n\t"
      "s_addc_u32 s59, s67, 0\n\t"
      "global_load_dwordx4 v[184:187], v240, s[58:59] sc1\n\t"
      "v_mfma_f32_16x16x32_bf16 v[196:199], v[152:155], v[100:103], v[196:199]\n\t"
      "v_mfma_f32_16x16x32_bf16 v[200:203], v[128:131], v[8:11], 0\n\t"
      "v_mfma_f32_16x16x32_bf16 v[200:203], v[132:135], v[24:27], v[200:203]\n\t"
      "v_mfma_f32_16x16x32_bf16 v[200:203], v[136:139], v[40:43], v[200:203]\n\t"
      "s_nop 4\n\t"
      "ds_write_b128 v241, v[196:199] offset:1024\n\t"
      "v_mfma_f32_16x16x32_bf16 v[200:203], v[140:143], v[56:59], v[200:203]\n\t"
      "v_mfma_f32_16x16x32_bf16 v[200:203], v[144:147], v[72:75], v[200:203]\n\t"
      "v_mfma_f32_16x16x32_bf16 v[200:203], v[148:151], v[88:91], v[200:203]\n\t"
      "v_mfma_f32_16x16x32_bf16 v[200:203], v[152:155], v[104:107], v[200:203]\n\t"
      "v_mfma_f32_16x16x32_bf16 v[204:207], v[128:131], v[12:15], 0\n\t"
      "s_add_u32 s58, s68, s40\n\t"
      "s_addc_u32 s59, s69, 0\n\t"
      "global_load_dwordx4 v[128:131], v240, s[58:59] sc1\n\t"
      "v_mfma_f32_16x16x32_bf16 v[204:207], v[132:135], v[28:31], v[204:207]\n\t"
      "v_mfma_f32_16x16x32_bf16 v[204:207], v[136:139], v[44:47], v[204:207]\n\t"
      "s_nop 1\n\t"
      "ds_write_b128 v241, v[200:203] offset:2048\n\t"
      "v_mfma_f32_16x16x32_bf16 v[204:207], v[140:143], v[60:63], v[204:207]\n\t"
      "v_mfma_f32_16x16x32_bf16 v[204:207], v[144:147], v[76:79], v[204:207]\n\t"
      "s_add_u32 s58, s68, s41\n\t"
      "s_addc_u32 s59, s69, 0\n\t"
      "global_load_dwordx4 v[132:135], v240, s[58:59] sc1\n\t"
      "v_mfma_f32_16x16x32_bf16 v[204:207], v[148:151], v[92:95], v[204:207]\n\t"
      "v_mfma_f32_16x16x32_bf16 v[204:207], v[152:155], v[108:111], v[204:207]\n\t"
      "s_waitcnt vmcnt(9)\n\t"
      "v_mfma_f32_16x16x32_bf16 v[208:211], v[160:163], v[0:3], 0\n\t"
      "s_waitcnt vmcnt(8)\n\t"
      "v_mfma_f32_16x16x32_bf16 v[208:211], v[164:167], v[16:19], v[208:211]\n\t"
      "s_add_u32 s58, s68, s42\n\t"
      "s_addc_u32 s59, s69, 0\n\t"
      "global_load_dwordx4 v[136:139], v240, s[58:59] sc1\n\t"
      "s_nop 0\n\t"
      "ds_write_b128 v241, v[204:207] offset:3072\n\t"
      "s_waitcnt vmcnt(8)\n\t"
      "v_mfma_f32_16x16x32_bf16 v[208:211], v[168:171], v[32:35], v[208:211]\n\t"
      "s_waitcnt vmcnt(7)\n\t"
      "v_mfma_f32_16x16x32_bf16 v[208:211], v[172:175], v[48:51], v[208:211]\n\t"
      "s_waitcnt vmcnt(6)\n\t"
      "v_mfma_f32_16x16x32_bf16 v[208:211], v[176:179], v[64:67], v[208:211]\n\t"
      "s_waitcnt vmcnt(5)\n\t"
      "v_mfma_f32_16x16x32_bf16 v[208:211], v[180:183], v[80:83], v[208:211]\n\t"
      "s_add_u32 s58, s68, s43\n\t"
      "s_addc_u32 s59, s69, 0\n\t"
      "global_load_dwordx4 v[140:143], v240, s[58:59] sc1\n\t"
      "s_waitcnt lgkmcnt(0)\n\t"
      "s_barrier\n\t"
      "ds_read_b64 v[212:213], v242\n\t"
      "ds_read_b64 v[214:215], v242 offset:4096\n\t"
      "s_waitcnt vmcnt(4)\n\t"
      "v_mfma_f32_16x16x32_bf16 v[208:211], v[184:187], v[96:99], v[208:211]\n\t"
      "ds_read_b64 v[216:217], v242 offset:8192\n\t"
      "ds_read_b64 v[218:219], v242 offset:12288\n\t"
      "v_mfma_f32_16x16x32_bf16 v[192:195], v[160:163], v[4:7], 0\n\t"
      "ds_read_b64 v[220:221], v242 offset:16384\n\t"
      "ds_read_b64 v[222:223], v242 offset:20480\n\t"
      "v_mfma_f32_16x16x32_bf16 v[192:195], v[164:167], v[20:23], v[192:195]\n\t"
      "ds_read_b64 v[224:225], v242 offset:24576\n\t"
      "ds_read_b64 v[226:227], v242 offset:28672\n\t"
      "v_lshlrev_b32_e32 v237, 16, v236\n\t"
      "v_mfma_f32_16x16x32_bf16 v[192:195], v[168:171], v[36:39], v[192:195]\n\t"
      "s_add_u32 s58, s68, s44\n\t"
      "s_addc_u32 s59, s69, 0\n\t"
      "global_load_dwordx4 v[144:147], v240, s[58:59] sc1\n\t"
      "v_and_b32_e32 v238, 0xffff0000, v236\n\t"
      "v_add_f32_e32 v237, v245, v237\n\t"
      "v_add_f32_e32 v232, v208, v209\n\t"
      "v_add_f32_e32 v232, v232, v210\n\t"
      "v_cmp_u_f32_e32 vcc, v232, v211\n\t"
      "s_cbranch_vccnz LA41R_%=\n\t"
      "LA41B_%=:\n\t"
      "ds_write_b128 v241, v[208:211] offset:32768\n\t"
      "v_mfma_f32_16x16x32_bf16 v[192:195], v[172:175], v[52:55], v[192:195]\n\t"
      "v_add_f32_e32 v238, v245, v238\n\t"
      "s_waitcnt lgkmcnt(5)\n\t"
      "v_mfma_f32_16x16x32_bf16 v[192:195], v[176:179], v[68:71], v[192:195]\n\t"
      "v_add_f32_e32 v212, v212, v214\n\t"
      "v_mfma_f32_16x16x32_bf16 v[192:195], v[180:183], v[84:87], v[192:195]\n\t"
      "v_add_f32_e32 v216, v216, v218\n\t"
      "v_add_f32_e32 v213, v213, v215\n\t"
      "global_load_dword v236, v244, s[50:51] sc1\n\t"
      "s_add_u32 s50, s50, 2048\n\t"
      "s_addc_u32 s51, s51, 0\n\t"
      "v_mfma_f32_16x16x32_bf16 v[192:195], v[184:187], v[100:103], v[192:195]\n\t"
      "s_add_u32 s58, s68, s45\n\t"
      "s_addc_u32 s59, s69, 0\n\t"
      "global_load_dwordx4 v[148:151], v240, s[58:59] sc1\n\t"
      "v_add_f32_e32 v217, v217, v219\n\t"
      "s_waitcnt lgkmcnt(1)\n\t"
      "v_mfma_f32_16x16x32_bf16 v[196:199], v[160:163], v[8:11], 0\n\t"
      "v_add_f32_e32 v220, v220, v222\n\t"
      "v_mfma_f32_16x16x32_bf16 v[196:199], v[164:167], v[24:27], v[196:199]\n\t"
      "v_add_f32_e32 v224, v224, v226\n\t"
      "v_add_f32_e32 v221, v221, v223\n\t"
      "v_mfma_f32_16x16x32_bf16 v[196:199], v[168:171], v[40:43], v[196:199]\n\t"
      "v_add_f32_e32 v225, v225, v227\n\t"
      "v_add_f32_e32 v212, v212, v216\n\t"
      "ds_write_b128 v241, v[192:195] offset:33792\n\t"
      "v_mfma_f32_16x16x32_bf16 v[196:199], v[172:175], v[56:59], v[196:199]\n\t"
      "s_add_u32 s58, s68, s46\n\t"
      "s_addc_u32 s59, s69, 0\n\t"
      "global_load_dwordx4 v[152:155], v240, s[58:59] sc1\n\t"
      "v_add_f32_e32 v220, v220, v224\n\t"
      "v_mfma_f32_16x16x32_bf16 v[196:199], v[176:179], v[72:75], v[196:199]\n\t"
      "v_add_f32_e32 v213, v213, v217\n\t"
      "v_add_f32_e32 v221, v221, v225\n\t"
      "v_mfma_f32_16x16x32_bf16 v[196:199], v[180:183], v[88:91], v[196:199]\n\t"
      "v_add_f32_e32 v212, v212, v237\n\t"
      "v_add_f32_e32 v213, v213, v238\n\t"
      "v_mfma_f32_16x16x32_bf16 v[196:199], v[184:187], v[104:107], v[196:199]\n\t"
      "v_add_f32_e32 v212, v212, v220\n\t"
      "v_add_f32_e32 v213, v213, v221\n\t"
      "v_mfma_f32_16x16x32_bf16 v[200:203], v[160:163], v[12:15], 0\n\t"
      "v_cvt_pk_bf16_f32 v216, v212, v213\n\t"
      "s_add_u32 s58, s70, s40\n\t"
      "s_addc_u32 s59, s71, 0\n\t"
      "global_load_dwordx4 v[160:163], v240, s[58:59] sc1\n\t"
      "v_mfma_f32_16x16x32_bf16 v[200:203], v[164:167], v[28:31], v[200:203]\n\t"
      "s_mov_b32 vcc_lo, 0xeeeeeeee\n\t"
      "s_mov_b32 vcc_hi, 0xeeeeeeee\n\t"
      "v_cndmask_b32_dpp v228, v216, v228, vcc quad_perm:[0,0,0,0] row_mask:0xf bank_mask:0xf\n\t"
      "v_cndmask_b32_dpp v229, v216, v229, vcc quad_perm:[1,1,1,1] row_mask:0xf bank_mask:0xf\n\t"
      "v_cndmask_b32_dpp v230, v216, v230, vcc quad_perm:[2,2,2,2] row_mask:0xf bank_mask:0xf\n\t"
      "v_cndmask_b32_dpp v231, v216, v231, vcc quad_perm:[3,3,3,3] row_mask:0xf bank_mask:0xf\n\t"
      "v_mfma_f32_16x16x32_bf16 v[200:203], v[168:171], v[44:47], v[200:203]\n\t"
      "ds_write_b128 v241, v[196:199] offset:34816\n\t"
      "v_mfma_f32_16x16x32_bf16 v[200:203], v[172:175], v[60:63], v[200:203]\n\t"
      "v_mfma_f32_16x16x32_bf16 v[200:203], v[176:179], v[76:79], v[200:203]\n\t"
      "s_add_u32 s58, s70, s41\n\t"
      "s_addc_u32 s59, s71, 0\n\t"
      "global_load_dwordx4 v[164:167], v240, s[58:59] sc1\n\t"
      "v_mfma_f32_16x16x32_bf16 v[200:203], v[180:183], v[92:95], v[200:203]\n\t"
      "v_mfma_f32_16x16x32_bf16 v[200:203], v[184:187], v[108:111], v[200:203]\n\t"
      "s_waitcnt vmcnt(9)\n\t"
      "v_mfma_f32_16x16x32_bf16 v[204:207], v[128:131], v[0:3], 0\n\t"
      "s_waitcnt vmcnt(8)\n\t"
      "v_mfma_f32_16x16x32_bf16 v[204:207], v[132:135], v[16:19], v[204:207]\n\t"
      "s_add_u32 s58, s70, s42\n\t"
      "s_addc_u32 s59, s71, 0\n\t"
      "global_load_dwordx4 v[168:171], v240, s[58:59] sc1\n\t"
      "s_nop 0\n\t"
      "ds_write_b128 v241, v[200:203] offset:35840\n\t"
      "s_waitcnt vmcnt(8)\n\t"
      "v_mfma_f32_16x16x32_bf16 v[204:207], v[136:139], v[32:35], v[204:207]\n\t"
      "s_waitcnt vmcnt(7)\n\t"
      "v_mfma_f32_16x16x32_bf16 v[204:207], v[140:143], v[48:51], v[204:207]\n\t"
      "s_waitcnt vmcnt(6)\n\t"
      "v_mfma_f32_16x16x32_bf16 v[204:207], v[144:147], v[64:67], v[204:207]\n\t"
      "s_waitcnt vmcnt(4)\n\t"
      "v_mfma_f32_16x16x32_bf16 v[204:207], v[148:151], v[80:83], v[204:207]\n\t"
      "s_add_u32 s58, s70, s43\n\t"
      "s_addc_u32 s59, s71, 0\n\t"
      "global_load_dwordx4 v[172:175], v240, s[58:59] sc1\n\t"
      "s_waitcnt lgkmcnt(0)\n\t"
      "s_barrier\n\t"
      "ds_read_b64 v[212:213], v242 offset:32768\n\t"
      "ds_read_b64 v[214:215], v242 offset:36864\n\t"
      "s_waitcnt vmcnt(4)\n\t"
      "v_mfma_f32_16x16x32_bf16 v[204:207], v[152:155], v[96:99], v[204:207]\n\t"
      "ds_read_b64 v[216:217], v242 offset:40960\n\t"
      "ds_read_b64 v[218:219], v242 offset:45056\n\t"
      "v_mfma_f32_16x16x32_bf16 v[208:211], v[128:131], v[4:7], 0\n\t"
      "ds_read_b64 v[220:221], v242 offset:49152\n\t"
      "ds_read_b64 v[222:223], v242 offset:53248\n\t"
      "v_mfma_f32_16x16x32_bf16 v[208:211], v[132:135], v[20:23], v[208:211]\n\t"
      "ds_read_b64 v[224:225], v242 offset:57344\n\t"
      "ds_read_b64 v[226:227], v242 offset:61440\n\t"
      "v_lshlrev_b32_e32 v237, 16, v236\n\t"
      "v_mfma_f32_16x16x32_bf16 v[208:211], v[136:139], v[36:39], v[208:211]\n\t"
      "s_add_u32 s58, s70, s44\n\t"
      "s_addc_u32 s59, s71, 0\n\t"
      "global_load_dwordx4 v[176:179], v240, s[58:59] sc1\n\t"
      "v_and_b32_e32 v238, 0xffff0000, v236\n\t"
      "v_add_f32_e32 v237, v245, v237\n\t"
      "v_add_f32_e32 v232, v204, v205\n\t"
      "v_add_f32_e32 v232, v232, v206\n\t"
      "v_cmp_u_f32_e32 vcc, v232, v207\n\t"
      "s_cbranch_vccnz LA42R_%=\n\t"
      "LA42B_%=:\n\t"
      "ds_write_b128 v241, v[204:207]\n\t"
      "v_mfma_f32_16x16x32_bf16 v[208:211], v[140:143], v[52:55], v[208:211]\n\t"
      "v_add_f32_e32 v238, v245, v238\n\t"
      "s_waitcnt lgkmcnt(5)\n\t"
      "v_mfma_f32_16x16x32_bf16 v[208:211], v[144:147], v[68:71], v[208:211]\n\t"
      "v_add_f32_e32 v212, v212, v214\n\t"
      "v_mfma_f32_16x16x32_bf16 v[208:211], v[148:151], v[84:87], v[208:211]\n\t"
      "v_add_f32_e32 v216, v216, v218\n\t"
      "v_add_f32_e32 v213, v213, v215\n\t"
      "global_load_dword v236, v244, s[50:51] sc1\n\t"
      "s_add_u32 s50, s50, 2048\n\t"
      "s_addc_u32 s51, s51, 0\n\t"
      "v_mfma_f32_16x16x32_bf16 v[208:211], v[152:155], v[100:103], v[208:211]\n\t"
      "s_add_u32 s58, s70, s45\n\t"
      "s_addc_u32 s59, s71, 0\n\t"
      "global_load_dwordx4 v[180:183], v240, s[58:59] sc1\n\t"
      "v_add_f32_e32 v217, v217, v219\n\t"
      "s_waitcnt lgkmcnt(1)\n\t"
      "v_mfma_f32_16x16x32_bf16 v[192:195], v[128:131], v[8:11], 0\n\t"
      "v_add_f32_e32 v220, v220, v222\n\t"
      "v_mfma_f32_16x16x32_bf16 v[192:195], v[132:135], v[24:27], v[192:195]\n\t"
      "v_add_f32_e32 v224, v224, v226\n\t"
      "v_add_f32_e32 v221, v221, v223\n\t"
      "v_mfma_f32_16x16x32_bf16 v[192:195], v[136:139], v[40:43], v[192:195]\n\t"
      "v_add_f32_e32 v225, v225, v227\n\t"
      "v_add_f32_e32 v212, v212, v216\n\t"
      "ds_write_b128 v241, v[208:211] offset:1024\n\t"
      "v_mfma_f32_16x16x32_bf16 v[192:195], v[140:143], v[56:59], v[192:195]\n\t"
      "s_add_u32 s58, s70, s46\n\t"
      "s_addc_u32 s59, s71, 0\n\t"
      "global_load_dwordx4 v[184:187], v240, s[58:59] sc1\n\t"
      "v_add_f32_e32 v220, v220, v224\n\t"
      "v_mfma_f32_16x16x32_bf16 v[192:195], v[144:147], v[72:75], v[192:195]\n\t"
      "v_add_f32_e32 v213, v213, v217\n\t"
      "v_add_f32_e32 v221, v221, v225\n\t"
      "v_mfma_f32_16x16x32_bf16 v[192:195], v[148:151], v[88:91], v[192:195]\n\t"
      "v_add_f32_e32 v212, v212, v237\n\t"
      "v_add_f32_e32 v213, v213, v238\n\t"
      "v_mfma_f32_16x16x32_bf16 v[192:195], v[152:155], v[104:107], v[192:195]\n\t"
      "v_add_f32_e32 v212, v212, v220\n\t"
      "v_add_f32_e32 v213, v213, v221\n\t"
      "v_mfma_f32_16x16x32_bf16 v[196:199], v[128:131], v[12:15], 0\n\t"
      "v_cvt_pk_bf16_f32 v216, v212, v213\n\t"
      "s_add_u32 s58, s72, s40\n\t"
      "s_addc_u32 s59, s73, 0\n\t"
      "global_load_dwordx4 v[128:131], v240, s[58:59] sc1\n\t"
      "v_mfma_f32_16x16x32_bf16 v[196:199], v[132:135], v[28:31], v[196:199]\n\t"
      "s_mov_b32 vcc_lo, 0xdddddddd\n\t"
      "s_mov_b32 vcc_hi, 0xdddddddd\n\t"
      "v_cndmask_b32_dpp v228, v216, v228, vcc quad_perm:[0,0,0,0] row_mask:0xf bank_mask:0xf\n\t"
      "v_cndmask_b32_dpp v229, v216, v229, vcc quad_perm:[1,1,1,1] row_mask:0xf bank_mask:0xf\n\t"
      "v_cndmask_b32_dpp v230, v216, v230, vcc quad_perm:[2,2,2,2] row_mask:0xf bank_mask:0xf\n\t"
      "v_cndmask_b32_dpp v231, v216, v231, vcc quad_perm:[3,3,3,3] row_mask:0xf bank_mask:0xf\n\t"
      "v_mfma_f32_16x16x32_bf16 v[196:199], v[136:139], v[44:47], v[196:199]\n\t"
      "ds_write_b128 v241, v[192:195] offset:2048\n\t"
      "v_mfma_f32_16x16x32_bf16 v[196:199], v[140:143], v[60:63], v[196:199]\n\t"
      "v_mfma_f32_16x16x32_bf16 v[196:199], v[144:147], v[76:79], v[196:199]\n\t"
      "s_add_u32 s58, s72, s41\n\t"
      "s_addc_u32 s59, s73, 0\n\t"
      "global_load_dwordx4 v[132:135], v240, s[58:59] sc1\n\t"
      "v_mfma_f32_16x16x32_bf16 v[196:199], v[148:151], v[92:95], v[196:199]\n\t"
      "v_mfma_f32_16x16x32_bf16 v[196:199], v[152:155], v[108:111], v[196:199]\n\t"
      "s_waitcnt vmcnt(9)\n\t"
      "v_mfma_f32_16x16x32_bf16 v[200:203], v[160:163], v[0:3], 0\n\t"
      "s_waitcnt vmcnt(8)\n\t"
      "v_mfma_f32_16x16x32_bf16 v[200:203], v[164:167], v[16:19], v[200:203]\n\t"
      "s_add_u32 s58, s72, s42\n\t"
      "s_addc_u32 s59, s73, 0\n\t"
      "global_load_dwordx4 v[136:139], v240, s[58:59] sc1\n\t"
      "s_nop 0\n\t"
      "ds_write_b128 v241, v[196:199] offset:3072\n\t"
      "s_waitcnt vmcnt(8)\n\t"
      "v_mfma_f32_16x16x32_bf16 v[200:203], v[168:171], v[32:35], v[200:203]\n\t"
      "s_waitcnt vmcnt(7)\n\t"
      "v_mfma_f32_16x16x32_bf16 v[200:203], v[172:175], v[48:51], v[200:203]\n\t"
      "s_waitcnt vmcnt(6)\n\t"
      "v_mfma_f32_16x16x32_bf16 v[200:203], v[176:179], v[64:67], v[200:203]\n\t"
      "s_waitcnt vmcnt(4)\n\t"
      "v_mfma_f32_16x16x32_bf16 v[200:203], v[180:183], v[80:83], v[200:203]\n\t"
      "s_add_u32 s58, s72, s43\n\t"
      "s_addc_u32 s59, s73, 0\n\t"
      "global_load_dwordx4 v[140:143], v240, s[58:59] sc1\n\t"
      "s_waitcnt lgkmcnt(0)\n\t"
      "s_barrier\n\t"
      "ds_read_b64 v[212:213], v242\n\t"
      "ds_read_b64 v[214:215], v242 offset:4096\n\t"
      "s_waitcnt vmcnt(4)\n\t"
      "v_mfma_f32_16x16x32_bf16 v[200:203], v[184:187], v[96:99], v[200:203]\n\t"
      "ds_read_b64 v[216:217], v242 offset:8192\n\t"
      "ds_read_b64 v[218:219], v242 offset:12288\n\t"
      "v_mfma_f32_16x16x32_bf16 v[204:207], v[160:163], v[4:7], 0\n\t"
      "ds_read_b64 v[220:221], v242 offset:16384\n\t"
      "ds_read_b64 v[222:223], v242 offset:20480\n\t"
      "v_mfma_f32_16x16x32_bf16 v[204:207], v[164:167], v[20:23], v[204:207]\n\t"
      "ds_read_b64 v[224:225], v242 offset:24576\n\t"
      "ds_read_b64 v[226:227], v242 offset:28672\n\t"
      "v_lshlrev_b32_e32 v237, 16, v236\n\t"
      "v_mfma_f32_16x16x32_bf16 v[204:207], v[168:171], v[36:39], v[204:207]\n\t"
      "s_add_u32 s58, s72, s44\n\t"
      "s_addc_u32 s59, s73, 0\n\t"
      "global_load_dwordx4 v[144:147], v240, s[58:59] sc1\n\t"
      "v_and_b32_e32 v238, 0xffff0000, v236\n\t"
      "v_add_f32_e32 v237, v245, v237\n\t"
      "v_add_f32_e32 v232, v200, v201\n\t"
      "v_add_f32_e32 v232, v232, v202\n\t"
      "v_cmp_u_f32_e32 vcc, v232, v203\n\t"
      "s_cbranch_vccnz LA43R_%=\n\t"
      "LA43B_%=:\n\t"
      "ds_write_b128 v241, v[200:203] offset:32768\n\t"
      "v_mfma_f32_16x16x32_bf16 v[204:207], v[172:175], v[52:55], v[204:207]\n\t"
      "v_add_f32_e32 v238, v245, v238\n\t"
      "s_waitcnt lgkmcnt(5)\n\t"
      "v_mfma_f32_16x16x32_bf16 v[204:207], v[176:179], v[68:71], v[204:207]\n\t"
      "v_add_f32_e32 v212, v212, v214\n\t"
      "v_mfma_f32_16x16x32_bf16 v[204:207], v[180:183], v[84:87], v[204:207]\n\t"
      "v_add_f32_e32 v216, v216, v218\n\t"
      "v_add_f32_e32 v213, v213, v215\n\t"
      "global_load_dword v236, v244, s[50:51] sc1\n\t"
      "s_add_u32 s50, s50, 2048\n\t"
      "s_addc_u32 s51, s51, 0\n\t"
      "v_mfma_f32_16x16x32_bf16 v[204:207], v[184:187], v[100:103], v[204:207]\n\t"
      "s_add_u32 s58, s72, s45\n\t"
      "s_addc_u32 s59, s73, 0\n\t"
      "global_load_dwordx4 v[148:151], v240, s[58:59] sc1\n\t"
      "v_add_f32_e32 v217, v217, v219\n\t"
      "s_waitcnt lgkmcnt(1)\n\t"
      "v_mfma_f32_16x16x32_bf16 v[208:211], v[160:163], v[8:11], 0\n\t"
      "v_add_f32_e32 v220, v220, v222\n\t"
      "v_mfma_f32_16x16x32_bf16 v[208:211], v[164:167], v[24:27], v[208:211]\n\t"
      "v_add_f32_e32 v224, v224, v226\n\t"
      "v_add_f32_e32 v221, v221, v223\n\t"
      "v_mfma_f32_16x16x32_bf16 v[208:211], v[168:171], v[40:43], v[208:211]\n\t"
      "v_add_f32_e32 v225, v225, v227\n\t"
      "v_add_f32_e32 v212, v212, v216\n\t"
      "ds_write_b128 v241, v[204:207] offset:33792\n\t"
      "v_mfma_f32_16x16x32_bf16 v[208:211], v[172:175], v[56:59], v[208:211]\n\t"
      "s_add_u32 s58, s72, s46\n\t"
      "s_addc_u32 s59, s73, 0\n\t"
      "global_load_dwordx4 v[152:155], v240, s[58:59] sc1\n\t"
      "v_add_f32_e32 v220, v220, v224\n\t"
      "v_mfma_f32_16x16x32_bf16 v[208:211], v[176:179], v[72:75], v[208:211]\n\t"
      "v_add_f32_e32 v213, v213, v217\n\t"
      "v_add_f32_e32 v221, v221, v225\n\t"
      "v_mfma_f32_16x16x32_bf16 v[208:211], v[180:183], v[88:91], v[208:211]\n\t"
      "v_add_f32_e32 v212, v212, v237\n\t"
      "v_add_f32_e32 v213, v213, v238\n\t"
      "v_mfma_f32_16x16x32_bf16 v[208:211], v[184:187], v[104:107], v[208:211]\n\t"
      "v_add_f32_e32 v212, v212, v220\n\t"
      "v_add_f32_e32 v213, v213, v221\n\t"
      "v_mfma_f32_16x16x32_bf16 v[192:195], v[160:163], v[12:15], 0\n\t"
      "v_cvt_pk_bf16_f32 v216, v212, v213\n\t"
      "s_add_u32 s58, s74, s40\n\t"
      "s_addc_u32 s59, s75, 0\n\t"
      "global_load_dwordx4 v[160:163], v240, s[58:59] sc1\n\t"
      "v_mfma_f32_16x16x32_bf16 v[192:195], v[164:167], v[28:31], v[192:195]\n\t"
      "s_mov_b32 vcc_lo, 0xbbbbbbbb\n\t"
      "s_mov_b32 vcc_hi, 0xbbbbbbbb\n\t"
      "v_cndmask_b32_dpp v228, v216, v228, vcc quad_perm:[0,0,0,0] row_mask:0xf bank_mask:0xf\n\t"
      "v_cndmask_b32_dpp v229, v216, v229, vcc quad_perm:[1,1,1,1] row_mask:0xf bank_mask:0xf\n\t"
      "v_cndmask_b32_dpp v230, v216, v230, vcc quad_perm:[2,2,2,2] row_mask:0xf bank_mask:0xf\n\t"
      "v_cndmask_b32_dpp v231, v216, v231, vcc quad_perm:[3,3,3,3] row_mask:0xf bank_mask:0xf\n\t"
      "v_mfma_f32_16x16x32_bf16 v[192:195], v[168:171], v[44:47], v[192:195]\n\t"
      "ds_write_b128 v241, v[208:211] offset:34816\n\t"
      "v_mfma_f32_16x16x32_bf16 v[192:195], v[172:175], v[60:63], v[192:195]\n\t"
      "v_mfma_f32_16x16x32_bf16 v[192:195], v[176:179], v[76:79], v[192:195]\n\t"
      "s_add_u32 s58, s74, s41\n\t"
      "s_addc_u32 s59, s75, 0\n\t"
      "global_load_dwordx4 v[164:167], v240, s[58:59] sc1\n\t"
      "v_mfma_f32_16x16x32_bf16 v[192:195], v[180:183], v[92:95], v[192:195]\n\t"
      "v_mfma_f32_16x16x32_bf16 v[192:195], v[184:187], v[108:111], v[192:195]\n\t"
      "s_waitcnt vmcnt(9)\n\t"
      "v_mfma_f32_16x16x32_bf16 v[196:199], v[128:131], v[0:3], 0\n\t"
      "s_waitcnt vmcnt(8)\n\t"
      "v_mfma_f32_16x16x32_bf16 v[196:199], v[132:135], v[16:19], v[196:199]\n\t"
      "s_add_u32 s58, s74, s42\n\t"
      "s_addc_u32 s59, s75, 0\n\t"
      "global_load_dwordx4 v[168:171], v240, s[58:59] sc1\n\t"
      "s_nop 0\n\t"
      "ds_write_b128 v241, v[192:195] offset:35840\n\t"
      "s_waitcnt vmcnt(8)\n\t"
      "v_mfma_f32_16x16x32_bf16 v[196:199], v[136:139], v[32:35], v[196:199]\n\t"
      "s_waitcnt vmcnt(7)\n\t"
      "v_mfma_f32_16x16x32_bf16 v[196:199], v[140:143], v[48:51], v[196:199]\n\t"
      "s_waitcnt vmcnt(6)\n\t"
      "v_mfma_f32_16x16x32_bf16 v[196:199], v[144:147], v[64:67], v[196:199]\n\t"
      "s_waitcnt vmcnt(4)\n\t"
      "v_mfma_f32_16x16x32_bf16 v[196:199], v[148:151], v[80:83], v[196:199]\n\t"
      "s_add_u32 s58, s74, s43\n\t"
      "s_addc_u32 s59, s75, 0\n\t"
      "global_load_dwordx4 v[172:175], v240, s[58:59] sc1\n\t"
      "s_waitcnt lgkmcnt(0)\n\t"
      "s_barrier\n\t"
      "ds_read_b64 v[212:213], v242 offset:32768\n\t"
      "ds_read_b64 v[214:215], v242 offset:36864\n\t"
      "s_waitcnt vmcnt(4)\n\t"
      "v_mfma_f32_16x16x32_bf16 v[196:199], v[152:155], v[96:99], v[196:199]\n\t"
      "ds_read_b64 v[216:217], v242 offset:40960\n\t"
      "ds_read_b64 v[218:219], v242 offset:45056\n\t"
      "v_mfma_f32_16x16x32_bf16 v[200:203], v[128:131], v[4:7], 0\n\t"
      "ds_read_b64 v[220:221], v242 offset:49152\n\t"
      "ds_read_b64 v[222:223], v242 offset:53248\n\t"
      "v_mfma_f32_16x16x32_bf16 v[200:203], v[132:135], v[20:23], v[200:203]\n\t"
      "ds_read_b64 v[224:225], v242 offset:57344\n\t"
      "ds_read_b64 v[226:227], v242 offset:61440\n\t"
      "v_lshlrev_b32_e32 v237, 16, v236\n\t"
      "v_mfma_f32_16x16x32_bf16 v[200:203], v[136:139], v[36:39], v[200:203]\n\t"
      "s_add_u32 s58, s74, s44\n\t"
      "s_addc_u32 s59, s75, 0\n\t"
      "global_load_dwordx4 v[176:179], v240, s[58:59] sc1\n\t"
      "v_and_b32_e32 v238, 0xffff0000, v236\n\t"
      "v_add_f32_e32 v237, v245, v237\n\t"
      "v_add_f32_e32 v232, v196, v197\n\t"
      "v_add_f32_e32 v232, v232, v198\n\t"
      "v_cmp_u_f32_e32 vcc, v232, v199\n\t"
      "s_cbranch_vccnz LA44R_%=\n\t"
      "LA44B_%=:\n\t"
      "ds_write_b128 v241, v[196:199]\n\t"
      "v_mfma_f32_16x16x32_bf16 v[200:203], v[140:143], v[52:55], v[200:203]\n\t"
      "v_add_f32_e32 v238, v245, v238\n\t"
      "s_waitcnt lgkmcnt(5)\n\t"
      "v_mfma_f32_16x16x32_bf16 v[200:203], v[144:147], v[68:71], v[200:203]\n\t"
      "v_add_f32_e32 v212, v212, v214\n\t"
      "v_mfma_f32_16x16x32_bf16 v[200:203], v[148:151], v[84:87], v[200:203]\n\t"
      "v_add_f32_e32 v216, v216, v218\n\t"
      "v_add_f32_e32 v213, v213, v215\n\t"
      "global_load_dword v236, v244, s[50:51] sc1\n\t"
      "s_add_u32 s50, s50, 2048\n\t"
      "s_addc_u32 s51, s51, 0\n\t"
      "v_mfma_f32_16x16x32_bf16 v[200:203], v[152:155], v[100:103], v[200:203]\n\t"
      "s_add_u32 s58, s74, s45\n\t"
      "s_addc_u32 s59, s75, 0\n\t"
      "global_load_dwordx4 v[180:183], v240, s[58:59] sc1\n\t"
      "v_add_f32_e32 v217, v217, v219\n\t"
      "s_waitcnt lgkmcnt(1)\n\t"
      "v_mfma_f32_16x16x32_bf16 v[204:207], v[128:131], v[8:11], 0\n\t"
      "v_add_f32_e32 v220, v220, v222\n\t"
      "v_mfma_f32_16x16x32_bf16 v[204:207], v[132:135], v[24:27], v[204:207]\n\t"
      "v_add_f32_e32 v224, v224, v226\n\t"
      "v_add_f32_e32 v221, v221, v223\n\t"
      "v_mfma_f32_16x16x32_bf16 v[204:207], v[136:139], v[40:43], v[204:207]\n\t"
      "v_add_f32_e32 v225, v225, v227\n\t"
      "v_add_f32_e32 v212, v212, v216\n\t"
      "ds_write_b128 v241, v[200:203] offset:1024\n\t"
      "v_mfma_f32_16x16x32_bf16 v[204:207], v[140:143], v[56:59], v[204:207]\n\t"
      "s_add_u32 s58, s74, s46\n\t"
      "s_addc_u32 s59, s75, 0\n\t"
      "global_load_dwordx4 v[184:187], v240, s[58:59] sc1\n\t"
      "v_add_f32_e32 v220, v220, v224\n\t"
      "v_mfma_f32_16x16x32_bf16 v[204:207], v[144:147], v[72:75], v[204:207]\n\t"
      "v_add_f32_e32 v213, v213, v217\n\t"
      "v_add_f32_e32 v221, v221, v225\n\t"
      "v_mfma_f32_16x16x32_bf16 v[204:207], v[148:151], v[88:91], v[204:207]\n\t"
      "v_add_f32_e32 v212, v212, v237\n\t"
      "v_add_f32_e32 v213, v213, v238\n\t"
      "v_mfma_f32_16x16x32_bf16 v[204:207], v[152:155], v[104:107], v[204:207]\n\t"
      "v_add_f32_e32 v212, v212, v220\n\t"
      "v_add_f32_e32 v213, v213, v221\n\t"
      "v_mfma_f32_16x16x32_bf16 v[208:211], v[128:131], v[12:15], 0\n\t"
      "v_cvt_pk_bf16_f32 v216, v212, v213\n\t"
      "s_add_u32 s58, s76, s40\n\t"
      "s_addc_u32 s59, s77, 0\n\t"
      "global_load_dwordx4 v[128:131], v240, s[58:59] sc1\n\t"
      "v_mfma_f32_16x16x32_bf16 v[208:211], v[132:135], v[28:31], v[208:211]\n\t"
      "s_mov_b32 vcc_lo, 0x77777777\n\t"
      "s_mov_b32 vcc_hi, 0x77777777\n\t"
      "v_cndmask_b32_dpp v228, v216, v228, vcc quad_perm:[0,0,0,0] row_mask:0xf bank_mask:0xf\n\t"
      "v_cndmask_b32_dpp v229, v216, v229, vcc quad_perm:[1,1,1,1] row_mask:0xf bank_mask:0xf\n\t"
      "v_cndmask_b32_dpp v230, v216, v230, vcc quad_perm:[2,2,2,2] row_mask:0xf bank_mask:0xf\n\t"
      "v_cndmask_b32_dpp v231, v216, v231, vcc quad_perm:[3,3,3,3] row_mask:0xf bank_mask:0xf\n\t"
      "global_store_dwordx4 v243, v[228:231], s[62:63] sc1\n\t"
      "v_mfma_f32_16x16x32_bf16 v[208:211], v[136:139], v[44:47], v[208:211]\n\t"
      "ds_write_b128 v241, v[204:207] offset:2048\n\t"
      "v_mfma_f32_16x16x32_bf16 v[208:211], v[140:143], v[60:63], v[208:211]\n\t"
      "v_mfma_f32_16x16x32_bf16 v[208:211], v[144:147], v[76:79], v[208:211]\n\t"
      "s_add_u32 s58, s76, s41\n\t"
      "s_addc_u32 s59, s77, 0\n\t"
      "global_load_dwordx4 v[132:135], v240, s[58:59] sc1\n\t"
      "v_mfma_f32_16x16x32_bf16 v[208:211], v[148:151], v[92:95], v[208:211]\n\t"
      "v_mfma_f32_16x16x32_bf16 v[208:211], v[152:155], v[108:111], v[208:211]\n\t"
      "s_waitcnt vmcnt(10)\n\t"
      "v_mfma_f32_16x16x32_bf16 v[192:195], v[160:163], v[0:3], 0\n\t"
      "s_waitcnt vmcnt(9)\n\t"
      "v_mfma_f32_16x16x32_bf16 v[192:195], v[164:167], v[16:19], v[192:195]\n\t"
      "s_add_u32 s58, s76, s42\n\t"
      "s_addc_u32 s59, s77, 0\n\t"
      "global_load_dwordx4 v[136:139], v240, s[58:59] sc1\n\t"
      "s_nop 0\n\t"
      "ds_write_b128 v241, v[208:211] offset:3072\n\t"
      "s_waitcnt vmcnt(9)\n\t"
      "v_mfma_f32_16x16x32_bf16 v[192:195], v[168:171], v[32:35], v[192:195]\n\t"
      "s_waitcnt vmcnt(8)\n\t"
      "v_mfma_f32_16x16x32_bf16 v[192:195], v[172:175], v[48:51], v[192:195]\n\t"
      "s_waitcnt vmcnt(7)\n\t"
      "v_mfma_f32_16x16x32_bf16 v[192:195], v[176:179], v[64:67], v[192:195]\n\t"
      "s_waitcnt vmcnt(5)\n\t"
      "v_mfma_f32_16x16x32_bf16 v[192:195], v[180:183], v[80:83], v[192:195]\n\t"
      "s_add_u32 s58, s76, s43\n\t"
      "s_addc_u32 s59, s77, 0\n\t"
      "global_load_dwordx4 v[140:143], v240, s[58:59] sc1\n\t"
      "s_waitcnt lgkmcnt(0)\n\t"
      "s_barrier\n\t"
      "ds_read_b64 v[212:213], v242\n\t"
      "ds_read_b64 v[214:215], v242 offset:4096\n\t"
      "s_waitcnt vmcnt(5)\n\t"
      "v_mfma_f32_16x16x32_bf16 v[192:195], v[184:187], v[96:99], v[192:195]\n\t"
      "ds_read_b64 v[216:217], v242 offset:8192\n\t"
      "ds_read_b64 v[218:219], v242 offset:12288\n\t"
      "v_mfma_f32_16x16x32_bf16 v[196:199], v[160:163], v[4:7], 0\n\t"
      "ds_read_b64 v[220:221], v242 offset:16384\n\t"
      "ds_read_b64 v[222:223], v242 offset:20480\n\t"
      "v_mfma_f32_16x16x32_bf16 v[196:199], v[164:167], v[20:23], v[196:199]\n\t"
      "ds_read_b64 v[224:225], v242 offset:24576\n\t"
      "ds_read_b64 v[226:227], v242 offset:28672\n\t"
      "v_lshlrev_b32_e32 v237, 16, v236\n\t"
      "v_mfma_f32_16x16x32_bf16 v[196:199], v[168:171], v[36:39], v[196:199]\n\t"
      "s_add_u32 s58, s76, s44\n\t"
      "s_addc_u32 s59, s77, 0\n\t"
      "global_load_dwordx4 v[144:147], v240, s[58:59] sc1\n\t"
      "v_and_b32_e32 v238, 0xffff0000, v236\n\t"
      "v_add_f32_e32 v237, v245, v237\n\t"
      "v_add_f32_e32 v232, v192, v193\n\t"
      "v_add_f32_e32 v232, v232, v194\n\t"
      "v_cmp_u_f32_e32 vcc, v232, v195\n\t"
      "s_cbranch_vccnz LA45R_%=\n\t"
      "LA45B_%=:\n\t"
      "ds_write_b128 v241, v[192:195] offset:32768\n\t"
      "v_mfma_f32_16x16x32_bf16 v[196:199], v[172:175], v[52:55], v[196:199]\n\t"
      "v_add_f32_e32 v238, v245, v238\n\t"
      "s_waitcnt lgkmcnt(5)\n\t"
      "v_mfma_f32_16x16x32_bf16 v[196:199], v[176:179], v[68:71], v[196:199]\n\t"
      "v_add_f32_e32 v212, v212, v214\n\t"
      "v_mfma_f32_16x16x32_bf16 v[196:199], v[180:183], v[84:87], v[196:199]\n\t"
      "v_add_f32_e32 v216, v216, v218\n\t"
      "v_add_f32_e32 v213, v213, v215\n\t"
      "global_load_dword v236, v244, s[50:51] sc1\n\t"
      "s_add_u32 s50, s50, 2048\n\t"
      "s_addc_u32 s51, s51, 0\n\t"
      "v_mfma_f32_16x16x32_bf16 v[196:199], v[184:187], v[100:103], v[196:199]\n\t"
      "s_add_u32 s58, s76, s45\n\t"
      "s_addc_u32 s59, s77, 0\n\t"
      "global_load_dwordx4 v[148:151], v240, s[58:59] sc1\n\t"
      "v_add_f32_e32 v217, v217, v219\n\t"
      "s_waitcnt lgkmcnt(1)\n\t"
      "v_mfma_f32_16x16x32_bf16 v[200:203], v[160:163], v[8:11], 0\n\t"
      "v_add_f32_e32 v220, v220, v222\n\t"
      "v_mfma_f32_16x16x32_bf16 v[200:203], v[164:167], v[24:27], v[200:203]\n\t"
      "v_add_f32_e32 v224, v224, v226\n\t"
      "v_add_f32_e32 v221, v221, v223\n\t"
      "v_mfma_f32_16x16x32_bf16 v[200:203], v[168:171], v[40:43], v[200:203]\n\t"
      "v_add_f32_e32 v225, v225, v227\n\t"
      "v_add_f32_e32 v212, v212, v216\n\t"
      "ds_write_b128 v241, v[196:199] offset:33792\n\t"
      "v_mfma_f32_16x16x32_bf16 v[200:203], v[172:175], v[56:59], v[200:203]\n\t"
      "s_add_u32 s58, s76, s46\n\t"
      "s_addc_u32 s59, s77, 0\n\t"
      "global_load_dwordx4 v[152:155], v240, s[58:59] sc1\n\t"
      "v_add_f32_e32 v220, v220, v224\n\t"
      "v_mfma_f32_16x16x32_bf16 v[200:203], v[176:179], v[72:75], v[200:203]\n\t"
      "v_add_f32_e32 v213, v213, v217\n\t"
      "v_add_f32_e32 v221, v221, v225\n\t"
      "v_mfma_f32_16x16x32_bf16 v[200:203], v[180:183], v[88:91], v[200:203]\n\t"
      "v_add_f32_e32 v212, v212, v237\n\t"
      "v_add_f32_e32 v213, v213, v238\n\t"
      "v_mfma_f32_16x16x32_bf16 v[200:203], v[184:187], v[104:107], v[200:203]\n\t"
      "v_add_f32_e32 v212, v212, v220\n\t"
      "v_add_f32_e32 v213, v213, v221\n\t"
      "v_mfma_f32_16x16x32_bf16 v[204:207], v[160:163], v[12:15], 0\n\t"
      "v_cvt_pk_bf16_f32 v216, v212, v213\n\t"
      "v_mfma_f32_16x16x32_bf16 v[204:207], v[164:167], v[28:31], v[204:207]\n\t"
      "s_mov_b32 vcc_lo, 0xeeeeeeee\n\t"
      "s_mov_b32 vcc_hi, 0xeeeeeeee\n\t"
      "v_cndmask_b32_dpp v228, v216, v228, vcc quad_perm:[0,0,0,0] row_mask:0xf bank_mask:0xf\n\t"
      "v_cndmask_b32_dpp v229, v216, v229, vcc quad_perm:[1,1,1,1] row_mask:0xf bank_mask:0xf\n\t"
      "v_cndmask_b32_dpp v230, v216, v230, vcc quad_perm:[2,2,2,2] row_mask:0xf bank_mask:0xf\n\t"
      "v_cndmask_b32_dpp v231, v216, v231, vcc quad_perm:[3,3,3,3] row_mask:0xf bank_mask:0xf\n\t"
      "v_mfma_f32_16x16x32_bf16 v[204:207], v[168:171], v[44:47], v[204:207]\n\t"
      "ds_write_b128 v241, v[200:203] offset:34816\n\t"
      "v_mfma_f32_16x16x32_bf16 v[204:207], v[172:175], v[60:63], v[204:207]\n\t"
      "v_mfma_f32_16x16x32_bf16 v[204:207], v[176:179], v[76:79], v[204:207]\n\t"
      "v_mfma_f32_16x16x32_bf16 v[204:207], v[180:183], v[92:95], v[204:207]\n\t"
      "v_mfma_f32_16x16x32_bf16 v[204:207], v[184:187], v[108:111], v[204:207]\n\t"
      "s_waitcnt vmcnt(8)\n\t"
      "v_mfma_f32_16x16x32_bf16 v[208:211], v[128:131], v[0:3], 0\n\t"
      "s_waitcnt vmcnt(6)\n\t"
      "v_mfma_f32_16x16x32_bf16 v[208:211], v[132:135], v[16:19], v[208:211]\n\t"
      "s_nop 3\n\t"
      "ds_write_b128 v241, v[204:207] offset:35840\n\t"
      "s_waitcnt vmcnt(5)\n\t"
      "v_mfma_f32_16x16x32_bf16 v[208:211], v[136:139], v[32:35], v[208:211]\n\t"
      "s_waitcnt vmcnt(4)\n\t"
      "v_mfma_f32_16x16x32_bf16 v[208:211], v[140:143], v[48:51], v[208:211]\n\t"
      "s_waitcnt vmcnt(3)\n\t"
      "v_mfma_f32_16x16x32_bf16 v[208:211], v[144:147], v[64:67], v[208:211]\n\t"
      "s_waitcnt vmcnt(1)\n\t"
      "v_mfma_f32_16x16x32_bf16 v[208:211], v[148:151], v[80:83], v[208:211]\n\t"
      "s_waitcnt lgkmcnt(0)\n\t"
      "s_barrier\n\t"
      "ds_read_b64 v[212:213], v242 offset:32768\n\t"
      "ds_read_b64 v[214:215], v242 offset:36864\n\t"
      "s_waitcnt vmcnt(0)\n\t"
      "v_mfma_f32_16x16x32_bf16 v[208:211], v[152:155], v[96:99], v[208:211]\n\t"
      "ds_read_b64 v[216:217], v242 offset:40960\n\t"
      "ds_read_b64 v[218:219], v242 offset:45056\n\t"
      "v_mfma_f32_16x16x32_bf16 v[192:195], v[128:131], v[4:7], 0\n\t"
      "ds_read_b64 v[220:221], v242 offset:49152\n\t"
      "ds_read_b64 v[222:223], v242 offset:53248\n\t"
      "v_mfma_f32_16x16x32_bf16 v[192:195], v[132:135], v[20:23], v[192:195]\n\t"
      "ds_read_b64 v[224:225], v242 offset:57344\n\t"
      "ds_read_b64 v[226:227], v242 offset:61440\n\t"
      "v_lshlrev_b32_e32 v237, 16, v236\n\t"
      "v_mfma_f32_16x16x32_bf16 v[192:195], v[136:139], v[36:39], v[192:195]\n\t"
      "v_and_b32_e32 v238, 0xffff0000, v236\n\t"
      "v_add_f32_e32 v237, v245, v237\n\t"
      "v_add_f32_e32 v232, v208, v209\n\t"
      "v_add_f32_e32 v232, v232, v210\n\t"
      "v_cmp_u_f32_e32 vcc, v232, v211\n\t"
      "s_cbranch_vccnz LA46R_%=\n\t"
      "LA46B_%=:\n\t"
      "ds_write_b128 v241, v[208:211]\n\t"
      "v_mfma_f32_16x16x32_bf16 v[192:195], v[140:143], v[52:55], v[192:195]\n\t"
      "v_add_f32_e32 v238, v245, v238\n\t"
      "s_waitcnt lgkmcnt(5)\n\t"
      "v_mfma_f32_16x16x32_bf16 v[192:195], v[144:147], v[68:71], v[192:195]\n\t"
      "v_add_f32_e32 v212, v212, v214\n\t"
      "v_mfma_f32_16x16x32_bf16 v[192:195], v[148:151], v[84:87], v[192:195]\n\t"
      "v_add_f32_e32 v216, v216, v218\n\t"
      "v_add_f32_e32 v213, v213, v215\n\t"
      "global_load_dword v236, v244, s[50:51] sc1\n\t"
      "s_add_u32 s50, s50, 2048\n\t"
      "s_addc_u32 s51, s51, 0\n\t"
      "v_mfma_f32_16x16x32_bf16 v[192:195], v[152:155], v[100:103], v[192:195]\n\t"
      "v_add_f32_e32 v217, v217, v219\n\t"
      "s_waitcnt lgkmcnt(1)\n\t"
      "v_mfma_f32_16x16x32_bf16 v[196:199], v[128:131], v[8:11], 0\n\t"
      "v_add_f32_e32 v220, v220, v222\n\t"
      "v_mfma_f32_16x16x32_bf16 v[196:199], v[132:135], v[24:27], v[196:199]\n\t"
      "v_add_f32_e32 v224, v224, v226\n\t"
      "v_add_f32_e32 v221, v221, v223\n\t"
      "v_mfma_f32_16x16x32_bf16 v[196:199], v[136:139], v[40:43], v[196:199]\n\t"
      "v_add_f32_e32 v225, v225, v227\n\t"
      "v_add_f32_e32 v212, v212, v216\n\t"
      "ds_write_b128 v241, v[192:195] offset:1024\n\t"
      "v_mfma_f32_16x16x32_bf16 v[196:199], v[140:143], v[56:59], v[196:199]\n\t"
      "v_add_f32_e32 v220, v220, v224\n\t"
      "v_mfma_f32_16x16x32_bf16 v[196:199], v[144:147], v[72:75], v[196:199]\n\t"
      "v_add_f32_e32 v213, v213, v217\n\t"
      "v_add_f32_e32 v221, v221, v225\n\t"
      "v_mfma_f32_16x16x32_bf16 v[196:199], v[148:151], v[88:91], v[196:199]\n\t"
      "v_add_f32_e32 v212, v212, v237\n\t"
      "v_add_f32_e32 v213, v213, v238\n\t"
      "v_mfma_f32_16x16x32_bf16 v[196:199], v[152:155], v[104:107], v[196:199]\n\t"
      "v_add_f32_e32 v212, v212, v220\n\t"
      "v_add_f32_e32 v213, v213, v221\n\t"
      "v_mfma_f32_16x16x32_bf16 v[200:203], v[128:131], v[12:15], 0\n\t"
      "v_cvt_pk_bf16_f32 v216, v212, v213\n\t"
      "v_mfma_f32_16x16x32_bf16 v[200:203], v[132:135], v[28:31], v[200:203]\n\t"
      "s_mov_b32 vcc_lo, 0xdddddddd\n\t"
      "s_mov_b32 vcc_hi, 0xdddddddd\n\t"
      "v_cndmask_b32_dpp v228, v216, v228, vcc quad_perm:[0,0,0,0] row_mask:0xf bank_mask:0xf\n\t"
      "v_cndmask_b32_dpp v229, v216, v229, vcc quad_perm:[1,1,1,1] row_mask:0xf bank_mask:0xf\n\t"
      "v_cndmask_b32_dpp v230, v216, v230, vcc quad_perm:[2,2,2,2] row_mask:0xf bank_mask:0xf\n\t"
      "v_cndmask_b32_dpp v231, v216, v231, vcc quad_perm:[3,3,3,3] row_mask:0xf bank_mask:0xf\n\t"
      "v_mfma_f32_16x16x32_bf16 v[200:203], v[136:139], v[44:47], v[200:203]\n\t"
      "ds_write_b128 v241, v[196:199] offset:2048\n\t"
      "v_mfma_f32_16x16x32_bf16 v[200:203], v[140:143], v[60:63], v[200:203]\n\t"
      "v_mfma_f32_16x16x32_bf16 v[200:203], v[144:147], v[76:79], v[200:203]\n\t"
      "v_mfma_f32_16x16x32_bf16 v[200:203], v[148:151], v[92:95], v[200:203]\n\t"
      "v_mfma_f32_16x16x32_bf16 v[200:203], v[152:155], v[108:111], v[200:203]\n\t"
      "s_nop 7\n\t"
      "ds_write_b128 v241, v[200:203] offset:3072\n\t"
      "s_waitcnt lgkmcnt(0)\n\t"
      "s_barrier\n\t"
      "ds_read_b64 v[212:213], v242\n\t"
      "ds_read_b64 v[214:215], v242 offset:4096\n\t"
      "ds_read_b64 v[216:217], v242 offset:8192\n\t"
      "ds_read_b64 v[218:219], v242 offset:12288\n\t"
      "ds_read_b64 v[220:221], v242 offset:16384\n\t"
      "ds_read_b64 v[222:223], v242 offset:20480\n\t"
      "ds_read_b64 v[224:225], v242 offset:24576\n\t"
      "ds_read_b64 v[226:227], v242 offset:28672\n\t"
      "s_waitcnt vmcnt(0)\n\t"
      "v_lshlrev_b32_e32 v237, 16, v236\n\t"
      "v_and_b32_e32 v238, 0xffff0000, v236\n\t"
      "v_add_f32_e32 v237, v245, v237\n\t"
      "v_add_f32_e32 v238, v245, v238\n\t"
      "s_waitcnt lgkmcnt(4)\n\t"
      "v_add_f32_e32 v212, v212, v214\n\t"
      "v_add_f32_e32 v216, v216, v218\n\t"
      "v_add_f32_e32 v213, v213, v215\n\t"
      "v_add_f32_e32 v217, v217, v219\n\t"
      "s_waitcnt lgkmcnt(0)\n\t"
      "v_add_f32_e32 v220, v220, v222\n\t"
      "v_add_f32_e32 v224, v224, v226\n\t"
      "v_add_f32_e32 v221, v221, v223\n\t"
      "v_add_f32_e32 v225, v225, v227\n\t"
      "v_add_f32_e32 v212, v212, v216\n\t"
      "v_add_f32_e32 v220, v220, v224\n\t"
      "v_add_f32_e32 v213, v213, v217\n\t"
      "v_add_f32_e32 v221, v221, v225\n\t"
      "v_add_f32_e32 v212, v212, v237\n\t"
      "v_add_f32_e32 v213, v213, v238\n\t"
      "v_add_f32_e32 v212, v212, v220\n\t"
      "v_add_f32_e32 v213, v213, v221\n\t"
      "v_cvt_pk_bf16_f32 v216, v212, v213\n\t"
      "s_mov_b32 vcc_lo, 0xbbbbbbbb\n\t"
      "s_mov_b32 vcc_hi, 0xbbbbbbbb\n\t"
      "v_cndmask_b32_dpp v228, v216, v228, vcc quad_perm:[0,0,0,0] row_mask:0xf bank_mask:0xf\n\t"
      "v_cndmask_b32_dpp v229, v216, v229, vcc quad_perm:[1,1,1,1] row_mask:0xf bank_mask:0xf\n\t"
      "v_cndmask_b32_dpp v230, v216, v230, vcc quad_perm:[2,2,2,2] row_mask:0xf bank_mask:0xf\n\t"
      "v_cndmask_b32_dpp v231, v216, v231, vcc quad_perm:[3,3,3,3] row_mask:0xf bank_mask:0xf\n\t"
      "s_mov_b64 exec, s[82:83]\n\t"
      "global_store_dwordx4 v243, v[228:231], s[62:63] offset:128 sc1\n\t"
      "s_mov_b64 exec, -1\n\t"
      "s_endpgm\n\t"
      "LO0R_%=:\n\t"
      "s_mov_b32 s80, 0\n\t"
      "LO0L_%=:\n\t"
      "s_add_u32 s80, s80, 1\n\t"
      "s_cmp_lt_u32 s80, 262144\n\t"
      "s_cbranch_scc0 LO0X_%=\n\t"
      "s_waitcnt vmcnt(0)\n\t"
      "s_sleep 1\n\t"
      "global_load_dwordx4 v[128:131], v240, s[42:43] offset:-4096 sc1\n\t"
      "global_load_dwordx4 v[132:135], v240, s[42:43] offset:-3072 sc1\n\t"
      "global_load_dwordx4 v[136:139], v240, s[42:43] offset:-2048 sc1\n\t"
      "global_load_dwordx4 v[140:143], v240, s[42:43] offset:-1024 sc1\n\t"
      "global_load_dwordx4 v[144:147], v240, s[42:43] sc1\n\t"
      "global_load_dwordx4 v[148:151], v240, s[42:43] offset:1024 sc1\n\t"
      "global_load_dwordx4 v[152:155], v240, s[42:43] offset:2048 sc1\n\t"
      "global_load_dwordx4 v[156:159], v240, s[42:43] offset:3072 sc1\n\t"
      "global_load_dwordx4 v[160:163], v240, s[44:45] offset:-4096 sc1\n\t"
      "global_load_dwordx4 v[164:167], v240, s[44:45] offset:-3072 sc1\n\t"
      "global_load_dwordx4 v[168:171], v240, s[44:45] offset:-2048 sc1\n\t"
      "global_load_dwordx4 v[172:175], v240, s[44:45] offset:-1024 sc1\n\t"
      "global_load_dwordx4 v[176:179], v240, s[44:45] sc1\n\t"
      "global_load_dwordx4 v[180:183], v240, s[44:45] offset:1024 sc1\n\t"
      "global_load_dwordx4 v[184:187], v240, s[44:45] offset:2048 sc1\n\t"
      "global_load_dwordx4 v[188:191], v240, s[44:45] offset:3072 sc1\n\t"
      "s_waitcnt vmcnt(0)\n\t"
      "v_max3_u32 v232, v128, v129, v130\n\t"
      "v_max3_u32 v232, v232, v131, v132\n\t"
      "v_max3_u32 v232, v232, v133, v134\n\t"
      "v_max_u32_e32 v232, v232, v135\n\t"
      "v_cmp_eq_u32_e32 vcc, -1, v232\n\t"
      "s_cbranch_vccnz LO0L_%=\n\t"
      "LO0X_%=:\n\t"
      "s_branch LO0B_%=\n\t"
      "LO2R_%=:\n\t"
      "s_mov_b32 s80, 0\n\t"
      "LO2L_%=:\n\t"
      "s_add_u32 s80, s80, 1\n\t"
      "s_cmp_lt_u32 s80, 262144\n\t"
      "s_cbranch_scc0 LO2X_%=\n\t"
      "s_waitcnt vmcnt(0)\n\t"
      "s_sleep 1\n\t"
      "global_load_dwordx4 v[136:139], v240, s[42:43] offset:-2048 sc1\n\t"
      "global_load_dwordx4 v[140:143], v240, s[42:43] offset:-1024 sc1\n\t"
      "global_load_dwordx4 v[144:147], v240, s[42:43] sc1\n\t"
      "global_load_dwordx4 v[148:151], v240, s[42:43] offset:1024 sc1\n\t"
      "global_load_dwordx4 v[152:155], v240, s[42:43] offset:2048 sc1\n\t"
      "global_load_dwordx4 v[156:159], v240, s[42:43] offset:3072 sc1\n\t"
      "global_load_dwordx4 v[160:163], v240, s[44:45] offset:-4096 sc1\n\t"
      "global_load_dwordx4 v[164:167], v240, s[44:45] offset:-3072 sc1\n\t"
      "global_load_dwordx4 v[168:171], v240, s[44:45] offset:-2048 sc1\n\t"
      "global_load_dwordx4 v[172:175], v240, s[44:45] offset:-1024 sc1\n\t"
      "global_load_dwordx4 v[176:179], v240, s[44:45] sc1\n\t"
      "global_load_dwordx4 v[180:183], v240, s[44:45] offset:1024 sc1\n\t"
      "global_load_dwordx4 v[184:187], v240, s[44:45] offset:2048 sc1\n\t"
      "global_load_dwordx4 v[188:191], v240, s[44:45] offset:3072 sc1\n\t"
      "s_waitcnt vmcnt(0)\n\t"
      "v_max3_u32 v232, v136, v137, v138\n\t"
      "v_max3_u32 v232, v232, v139, v140\n\t"
      "v_max3_u32 v232, v232, v141, v142\n\t"
      "v_max_u32_e32 v232, v232, v143\n\t"
      "v_cmp_eq_u32_e32 vcc, -1, v232\n\t"
      "s_cbranch_vccnz LO2L_%=\n\t"
      "LO2X_%=:\n\t"
      "s_branch LO2B_%=\n\t"
      "LO4R_%=:\n\t"
      "s_mov_b32 s80, 0\n\t"
      "LO4L_%=:\n\t"
      "s_add_u32 s80, s80, 1\n\t"
      "s_cmp_lt_u32 s80, 262144\n\t"
      "s_cbranch_scc0 LO4X_%=\n\t"
      "s_waitcnt vmcnt(0)\n\t"
      "s_sleep 1\n\t"
      "global_load_dwordx4 v[144:147], v240, s[42:43] sc1\n\t"
      "global_load_dwordx4 v[148:151], v240, s[42:43] offset:1024 sc1\n\t"
      "global_load_dwordx4 v[152:155], v240, s[42:43] offset:2048 sc1\n\t"
      "global_load_dwordx4 v[156:159], v240, s[42:43] offset:3072 sc1\n\t"
      "global_load_dwordx4 v[160:163], v240, s[44:45] offset:-4096 sc1\n\t"
      "global_load_dwordx4 v[164:167], v240, s[44:45] offset:-3072 sc1\n\t"
      "global_load_dwordx4 v[168:171], v240, s[44:45] offset:-2048 sc1\n\t"
      "global_load_dwordx4 v[172:175], v240, s[44:45] offset:-1024 sc1\n\t"
      "global_load_dwordx4 v[176:179], v240, s[44:45] sc1\n\t"
      "global_load_dwordx4 v[180:183], v240, s[44:45] offset:1024 sc1\n\t"
      "global_load_dwordx4 v[184:187], v240, s[44:45] offset:2048 sc1\n\t"
      "global_load_dwordx4 v[188:191], v240, s[44:45] offset:3072 sc1\n\t"
      "s_waitcnt vmcnt(0)\n\t"
      "v_max3_u32 v232, v144, v145, v146\n\t"
      "v_max3_u32 v232, v232, v147, v148\n\t"
      "v_max3_u32 v232, v232, v149, v150\n\t"
      "v_max_u32_e32 v232, v232, v151\n\t"
      "v_cmp_eq_u32_e32 vcc, -1, v232\n\t"
      "s_cbranch_vccnz LO4L_%=\n\t"
      "LO4X_%=:\n\t"
      "s_branch LO4B_%=\n\t"
      "LO6R_%=:\n\t"
      "s_mov_b32 s80, 0\n\t"
      "LO6L_%=:\n\t"
      "s_add_u32 s80, s80, 1\n\t"
      "s_cmp_lt_u32 s80, 262144\n\t"
      "s_cbranch_scc0 LO6X_%=\n\t"
      "s_waitcnt vmcnt(0)\n\t"
      "s_sleep 1\n\t"
      "global_load_dwordx4 v[152:155], v240, s[42:43] offset:2048 sc1\n\t"
      "global_load_dwordx4 v[156:159], v240, s[42:43] offset:3072 sc1\n\t"
      "global_load_dwordx4 v[160:163], v240, s[44:45] offset:-4096 sc1\n\t"
      "global_load_dwordx4 v[164:167], v240, s[44:45] offset:-3072 sc1\n\t"
      "global_load_dwordx4 v[168:171], v240, s[44:45] offset:-2048 sc1\n\t"
      "global_load_dwordx4 v[172:175], v240, s[44:45] offset:-1024 sc1\n\t"
      "global_load_dwordx4 v[176:179], v240, s[44:45] sc1\n\t"
      "global_load_dwordx4 v[180:183], v240, s[44:45] offset:1024 sc1\n\t"
      "global_load_dwordx4 v[184:187], v240, s[44:45] offset:2048 sc1\n\t"
      "global_load_dwordx4 v[188:191], v240, s[44:45] offset:3072 sc1\n\t"
      "s_waitcnt vmcnt(0)\n\t"
      "v_max3_u32 v232, v152, v153, v154\n\t"
      "v_max3_u32 v232, v232, v155, v156\n\t"
      "v_max3_u32 v232, v232, v157, v158\n\t"
      "v_max_u32_e32 v232, v232, v159\n\t"
      "v_cmp_eq_u32_e32 vcc, -1, v232\n\t"
      "s_cbranch_vccnz LO6L_%=\n\t"
      "LO6X_%=:\n\t"
      "s_branch LO6B_%=\n\t"
      "LO8R_%=:\n\t"
      "s_mov_b32 s80, 0\n\t"
      "LO8L_%=:\n\t"
      "s_add_u32 s80, s80, 1\n\t"
      "s_cmp_lt_u32 s80, 262144\n\t"
      "s_cbranch_scc0 LO8X_%=\n\t"
      "s_waitcnt vmcnt(0)\n\t"
      "s_sleep 1\n\t"
      "global_load_dwordx4 v[160:163], v240, s[44:45] offset:-4096 sc1\n\t"
      "global_load_dwordx4 v[164:167], v240, s[44:45] offset:-3072 sc1\n\t"
      "global_load_dwordx4 v[168:171], v240, s[44:45] offset:-2048 sc1\n\t"
      "global_load_dwordx4 v[172:175], v240, s[44:45] offset:-1024 sc1\n\t"
      "global_load_dwordx4 v[176:179], v240, s[44:45] sc1\n\t"
      "global_load_dwordx4 v[180:183], v240, s[44:45] offset:1024 sc1\n\t"
      "global_load_dwordx4 v[184:187], v240, s[44:45] offset:2048 sc1\n\t"
      "global_load_dwordx4 v[188:191], v240, s[44:45] offset:3072 sc1\n\t"
      "s_waitcnt vmcnt(0)\n\t"
      "v_max3_u32 v232, v160, v161, v162\n\t"
      "v_max3_u32 v232, v232, v163, v164\n\t"
      "v_max3_u32 v232, v232, v165, v166\n\t"
      "v_max_u32_e32 v232, v232, v167\n\t"
      "v_cmp_eq_u32_e32 vcc, -1, v232\n\t"
      "s_cbranch_vccnz LO8L_%=\n\t"
      "LO8X_%=:\n\t"
      "s_branch LO8B_%=\n\t"
      "LO10R_%=:\n\t"
      "s_mov_b32 s80, 0\n\t"
      "LO10L_%=:\n\t"
      "s_add_u32 s80, s80, 1\n\t"
      "s_cmp_lt_u32 s80, 262144\n\t"
      "s_cbranch_scc0 LO10X_%=\n\t"
      "s_waitcnt vmcnt(0)\n\t"
      "s_sleep 1\n\t"
      "global_load_dwordx4 v[168:171], v240, s[44:45] offset:-2048 sc1\n\t"
      "global_load_dwordx4 v[172:175], v240, s[44:45] offset:-1024 sc1\n\t"
      "global_load_dwordx4 v[176:179], v240, s[44:45] sc1\n\t"
      "global_load_dwordx4 v[180:183], v240, s[44:45] offset:1024 sc1\n\t"
      "global_load_dwordx4 v[184:187], v240, s[44:45] offset:2048 sc1\n\t"
      "global_load_dwordx4 v[188:191], v240, s[44:45] offset:3072 sc1\n\t"
      "s_waitcnt vmcnt(0)\n\t"
      "v_max3_u32 v232, v168, v169, v170\n\t"
      "v_max3_u32 v232, v232, v171, v172\n\t"
      "v_max3_u32 v232, v232, v173, v174\n\t"
      "v_max_u32_e32 v232, v232, v175\n\t"
      "v_cmp_eq_u32_e32 vcc, -1, v232\n\t"
      "s_cbranch_vccnz LO10L_%=\n\t"
      "LO10X_%=:\n\t"
      "s_branch LO10B_%=\n\t"
      "LO12R_%=:\n\t"
      "s_mov_b32 s80, 0\n\t"
      "LO12L_%=:\n\t"
      "s_add_u32 s80, s80, 1\n\t"
      "s_cmp_lt_u32 s80, 262144\n\t"
      "s_cbranch_scc0 LO12X_%=\n\t"
      "s_waitcnt vmcnt(0)\n\t"
      "s_sleep 1\n\t"
      "global_load_dwordx4 v[176:179], v240, s[44:45] sc1\n\t"
      "global_load_dwordx4 v[180:183], v240, s[44:45] offset:1024 sc1\n\t"
      "global_load_dwordx4 v[184:187], v240, s[44:45] offset:2048 sc1\n\t"
      "global_load_dwordx4 v[188:191], v240, s[44:45] offset:3072 sc1\n\t"
      "s_waitcnt vmcnt(0)\n\t"
      "v_max3_u32 v232, v176, v177, v178\n\t"
      "v_max3_u32 v232, v232, v179, v180\n\t"
      "v_max3_u32 v232, v232, v181, v182\n\t"
      "v_max_u32_e32 v232, v232, v183\n\t"
      "v_cmp_eq_u32_e32 vcc, -1, v232\n\t"
      "s_cbranch_vccnz LO12L_%=\n\t"
      "LO12X_%=:\n\t"
      "s_branch LO12B_%=\n\t"
      "LO14R_%=:\n\t"
      "s_mov_b32 s80, 0\n\t"
      "LO14L_%=:\n\t"
      "s_add_u32 s80, s80, 1\n\t"
      "s_cmp_lt_u32 s80, 262144\n\t"
      "s_cbranch_scc0 LO14X_%=\n\t"
      "s_waitcnt vmcnt(0)\n\t"
      "s_sleep 1\n\t"
      "global_load_dwordx4 v[184:187], v240, s[44:45] offset:2048 sc1\n\t"
      "global_load_dwordx4 v[188:191], v240, s[44:45] offset:3072 sc1\n\t"
      "s_waitcnt vmcnt(0)\n\t"
      "v_max3_u32 v232, v184, v185, v186\n\t"
      "v_max3_u32 v232, v232, v187, v188\n\t"
      "v_max3_u32 v232, v232, v189, v190\n\t"
      "v_max_u32_e32 v232, v232, v191\n\t"
      "v_cmp_eq_u32_e32 vcc, -1, v232\n\t"
      "s_cbranch_vccnz LO14L_%=\n\t"
      "LO14X_%=:\n\t"
      "s_branch LO14B_%=\n\t"
      "LSR_%=:\n\t"
      "s_mov_b32 s80, 0\n\t"
      "LSL_%=:\n\t"
      "s_add_u32 s80, s80, 1\n\t"
      "s_cmp_lt_u32 s80, 262144\n\t"
      "s_cbranch_scc0 LSX_%=\n\t"
      "s_sleep 1\n\t"
      "global_load_dwordx2 v[148:149], v231, s[46:47] sc1\n\t"
      "global_load_dwordx2 v[150:151], v231, s[46:47] offset:384 sc1\n\t"
      "s_waitcnt vmcnt(0)\n\t"
      "v_max3_u32 v232, v148, v149, v150\n\t"
      "v_max_u32_e32 v232, v232, v151\n\t"
      "v_cmp_eq_u32_e32 vcc, -1, v232\n\t"
      "s_cbranch_vccnz LSL_%=\n\t"
      "LSX_%=:\n\t"
      "s_branch LSB_%=\n\t"
      "LF1R_%=:\n\t"
      "s_mov_b32 s80, 0\n\t"
      "LF1L_%=:\n\t"
      "s_add_u32 s80, s80, 1\n\t"
      "s_cmp_lt_u32 s80, 262144\n\t"
      "s_cbranch_scc0 LF1X_%=\n\t"
      "s_sleep 2\n\t"
      "global_load_dwordx2 v[138:139], v136, s[60:61] sc1\n\t"
      "s_waitcnt vmcnt(0)\n\t"
      "v_cmp_ne_u64_e32 vcc, s[22:23], v[138:139]\n\t"
      "s_cbranch_vccnz LF1L_%=\n\t"
      "LF1X_%=:\n\t"
      "s_branch LF1B_%=\n\t"
      "LF4R_%=:\n\t"
      "s_mov_b32 s80, 0\n\t"
      "LF4L_%=:\n\t"
      "s_add_u32 s80, s80, 1\n\t"
      "s_cmp_lt_u32 s80, 262144\n\t"
      "s_cbranch_scc0 LF4X_%=\n\t"
      "s_sleep 2\n\t"
      "global_load_dwordx2 v[138:139], v137, s[60:61] sc1\n\t"
      "s_waitcnt vmcnt(0)\n\t"
      "v_cmp_ne_u64_e32 vcc, s[22:23], v[138:139]\n\t"
      "s_cbranch_vccnz LF4L_%=\n\t"
      "LF4X_%=:\n\t"
      "s_branch LF4B_%=\n\t"
      "L4_%=:\n\t"
      "s_lshl2_add_u32 s72, s31, s24\n\t"
      "s_sub_u32 s72, s72, 56\n\t"
      "s_mov_b32 m0, 64512\n\t"
      "s_cmp_ge_u32 s72, 64\n\t"
      "s_cselect_b32 s73, 8, 0\n\t"
      "s_sub_u32 s73, s72, s73\n\t"
      "s_and_b32 s73, s73, 0x38\n\t"
      "s_add_u32 s72, s72, s73\n\t"
      "s_add_u32 s72, s72, 8\n\t"
      "s_mul_i32 s72, s72, 57344\n\t"
      "s_and_b32 s73, s2, 7\n\t"
      "s_mul_i32 s73, s73, 448\n\t"
      "s_and_b32 s73, s73, 0xffffff80\n\t"
      "s_add_u32 s72, s72, s73\n\t"
      "v_lshrrev_b32_e32 v141, 2, v192\n\t"
      "v_mul_u32_u24_e32 v141, 3584, v141\n\t"
      "v_lshl_add_u32 v141, v196, 7, v141\n\t"
      "v_add_u32_e32 v141, s72, v141\n\t"
      "global_load_lds_dword v141, s[10:11]\n\t"
      "s_barrier\n\t"
      "s_cmp_lg_u32 s24, 3\n\t"
      "s_cbranch_scc1 SK12_%=\n\t"
      "global_load_dwordx2 v[138:139], v137, s[60:61] sc1\n\t"
      "SK12_%=:\n\t"
      "s_cmp_lg_u32 s24, 3\n\t"
      "s_cbranch_scc1 LF4B_%=\n\t"
      "s_waitcnt vmcnt(0)\n\t"
      "v_cmp_ne_u64_e32 vcc, s[22:23], v[138:139]\n\t"
      "s_cbranch_vccnz LF4R_%=\n\t"
      "LF4B_%=:\n\t"
      "s_barrier\n\t"
      "s_lshl2_add_u32 s72, s31, s24\n\t"
      "s_sub_u32 s72, s72, 56\n\t"
      "s_mov_b32 m0, 64512\n\t"
      "s_lshl1_add_u32 s72, s72, 0\n\t"
      "s_mul_i32 s72, s72, 65536\n\t"
      "s_and_b32 s73, s2, 7\n\t"
      "s_mul_i32 s73, s73, 512\n\t"
      "s_and_b32 s73, s73, 0xffffff80\n\t"
      "s_add_u32 s72, s72, s73\n\t"
      "v_lshrrev_b32_e32 v141, 2, v192\n\t"
      "v_mul_u32_u24_e32 v141, 4096, v141\n\t"
      "v_lshl_add_u32 v141, v196, 7, v141\n\t"
      "v_add_u32_e32 v141, s72, v141\n\t"
      "global_load_lds_dword v141, s[14:15]\n\t"
      "s_barrier\n\t"
      "s_waitcnt vmcnt(0)\n\t"
      "s_branch GST_%=\n\t"
      "LF2R_%=:\n\t"
      "s_mov_b32 s80, 0\n\t"
      "LF2L_%=:\n\t"
      "s_add_u32 s80, s80, 1\n\t"
      "s_cmp_lt_u32 s80, 262144\n\t"
      "s_cbranch_scc0 LF2X_%=\n\t"
      "s_sleep 2\n\t"
      "global_load_dwordx2 v[138:139], v137, s[60:61] sc1\n\t"
      "s_waitcnt vmcnt(0)\n\t"
      "v_cmp_ne_u64_e32 vcc, s[22:23], v[138:139]\n\t"
      "s_cbranch_vccnz LF2L_%=\n\t"
      "LF2X_%=:\n\t"
      "s_branch LF2B_%=\n\t"
      "LF3R_%=:\n\t"
      "s_mov_b32 s80, 0\n\t"
      "LF3L_%=:\n\t"
      "s_add_u32 s80, s80, 1\n\t"
      "s_cmp_lt_u32 s80, 262144\n\t"
      "s_cbranch_scc0 LF3X_%=\n\t"
      "s_sleep 2\n\t"
      "global_load_dwordx2 v[138:139], v143, s[60:61] sc1\n\t"
      "s_waitcnt vmcnt(0)\n\t"
      "v_cmp_ne_u64_e32 vcc, s[22:23], v[138:139]\n\t"
      "s_cbranch_vccnz LF3L_%=\n\t"
      "LF3X_%=:\n\t"
      "s_branch LF3B_%=\n\t"
      "LF7R_%=:\n\t"
      "s_mov_b32 s80, 0\n\t"
      "LF7L_%=:\n\t"
      "s_add_u32 s80, s80, 1\n\t"
      "s_cmp_lt_u32 s80, 262144\n\t"
      "s_cbranch_scc0 LF7X_%=\n\t"
      "s_sleep 4\n\t"
      "global_load_dwordx2 v[138:139], v136, s[60:61] sc1\n\t"
      "s_waitcnt vmcnt(0)\n\t"
      "v_cmp_ne_u64_e32 vcc, s[22:23], v[138:139]\n\t"
      "s_cbranch_vccnz LF7L_%=\n\t"
      "LF7X_%=:\n\t"
      "s_branch LF7B_%=\n\t"
      "SPARE_%=:\n\t"
      "v_mov_b32_e32 v132, -1\n\t"
      "v_mov_b32_e32 v133, -1\n\t"
      "v_mov_b32_e32 v134, -1\n\t"
      "v_mov_b32_e32 v135, -1\n\t"
      "s_and_b32 s34, s25, 3\n\t"
      "s_lshl_b32 s34, s34, 15\n\t"
      "s_mul_i32 s35, s24, 917504\n\t"
      "s_add_u32 s34, s34, s35\n\t"
      "s_and_b32 s35, s2, 7\n\t"
      "s_lshl_b32 s35, s35, 9\n\t"
      "s_add_u32 s34, s34, s35\n\t"
      "v_and_b32_e32 v141, 0x1c0, v240\n\t"
      "v_lshlrev_b32_e32 v141, 6, v141\n\t"
      "v_and_b32_e32 v142, 0x30, v240\n\t"
      "v_lshl_add_u32 v141, v142, 3, v141\n\t"
      "v_add_u32_e32 v141, s34, v141\n\t"
      "s_mov_b32 m0, 64512\n\t"
      "s_lshr_b32 s34, s24, 1\n\t"
      "s_lshl_b32 s34, s34, 5\n\t"
      "s_add_u32 s34, s34, s25\n\t"
      "s_and_b32 s35, s24, 1\n\t"
      "s_lshl1_add_u32 s34, s34, s35\n\t"
      "s_lshl_b32 s34, s34, 10\n\t"
      "s_add_u32 s34, s34, 8388608\n\t"
      "s_add_u32 s40, s20, s34\n\t"
      "s_addc_u32 s41, s21, 0\n\t"
      "global_store_dwordx4 v240, v[132:135], s[40:41] sc1\n\t"
      "s_add_u32 s40, s40, 262144\n\t"
      "s_addc_u32 s41, s41, 0\n\t"
      "global_store_dwordx4 v240, v[132:135], s[40:41] sc1\n\t"
      "s_add_u32 s40, s40, 262144\n\t"
      "s_addc_u32 s41, s41, 0\n\t"
      "global_store_dwordx4 v240, v[132:135], s[40:41] sc1\n\t"
      "s_add_u32 s40, s40, 262144\n\t"
      "s_addc_u32 s41, s41, 0\n\t"
      "global_store_dwordx4 v240, v[132:135], s[40:41] sc1\n\t"
      "s_add_u32 s40, s40, 262144\n\t"
      "s_addc_u32 s41, s41, 0\n\t"
      "global_store_dwordx4 v240, v[132:135], s[40:41] sc1\n\t"
      "s_add_u32 s40, s40, 262144\n\t"
      "s_addc_u32 s41, s41, 0\n\t"
      "global_store_dwordx4 v240, v[132:135], s[40:41] sc1\n\t"
      "s_add_u32 s40, s40, 262144\n\t"
      "s_addc_u32 s41, s41, 0\n\t"
      "global_store_dwordx4 v240, v[132:135], s[40:41] sc1\n\t"
      "s_add_u32 s40, s40, 262144\n\t"
      "s_addc_u32 s41, s41, 0\n\t"
      "s_lshl_b32 s34, s25, 7\n\t"
      "s_add_u32 s34, s34, 16384\n\t"
      "v_mov_b32_e32 v131, s34\n\t"
      "s_waitcnt vmcnt(0)\n\t"
      "s_barrier\n\t"
      "s_cmp_lg_u32 s24, 0\n\t"
      "s_cbranch_scc1 SP0_%=\n\t"
      "s_mov_b64 exec, 1\n\t"
      "global_store_dwordx2 v131, v[128:129], s[60:61] sc1\n\t"
      "s_mov_b64 exec, -1\n\t"
      "SP0_%=:\n\t"
      "s_lshl3_add_u32 s34, s25, s24\n\t"
      "s_mul_i32 s34, s34, 7168\n\t"
      "s_add_u32 s34, s34, 10227712\n\t"
      "s_add_u32 s40, s20, s34\n\t"
      "s_addc_u32 s41, s21, 0\n\t"
      "global_store_dwordx4 v240, v[132:135], s[40:41] offset:-4096 sc1\n\t"
      "global_store_dwordx4 v240, v[132:135], s[40:41] offset:-3072 sc1\n\t"
      "global_store_dwordx4 v240, v[132:135], s[40:41] offset:-2048 sc1\n\t"
      "global_store_dwordx4 v240, v[132:135], s[40:41] offset:-1024 sc1\n\t"
      "global_store_dwordx4 v240, v[132:135], s[40:41] sc1\n\t"
      "global_store_dwordx4 v240, v[132:135], s[40:41] offset:1024 sc1\n\t"
      "global_store_dwordx4 v240, v[132:135], s[40:41] offset:2048 sc1\n\t"
      "s_lshl_b32 s34, s25, 7\n\t"
      "s_add_u32 s34, s34, 24576\n\t"
      "v_mov_b32_e32 v131, s34\n\t"
      "s_waitcnt vmcnt(0)\n\t"
      "s_barrier\n\t"
      "s_cmp_lg_u32 s24, 0\n\t"
      "s_cbranch_scc1 SP1_%=\n\t"
      "s_mov_b64 exec, 1\n\t"
      "global_store_dwordx2 v131, v[128:129], s[60:61] sc1\n\t"
      "s_mov_b64 exec, -1\n\t"
      "SP1_%=:\n\t"
      "s_and_b32 s34, s25, 3\n\t"
      "s_lshl3_add_u32 s34, s34, s24\n\t"
      "s_cmp_lg_u32 s34, 27\n\t"
      "s_cbranch_scc1 SP3_%=\n\t"
      "v_and_b32_e32 v136, 0x1f0, v240\n\t"
      "v_lshlrev_b32_e32 v131, 6, v136\n\t"
      "v_lshlrev_b32_e32 v136, 3, v136\n\t"
      "v_add_u32_e32 v136, 24576, v136\n\t"
      "s_and_b32 s34, s2, 7\n\t"
      "s_lshl_b32 s34, s34, 7\n\t"
      "s_add_u32 s34, s34, 32768\n\t"
      "v_add_u32_e32 v131, s34, v131\n\t"
      "s_cmp_lg_u32 s24, 3\n\t"
      "s_cbranch_scc1 SK15_%=\n\t"
      "global_load_dwordx2 v[138:139], v136, s[60:61] sc1\n\t"
      "SK15_%=:\n\t"
      "s_cmp_lg_u32 s24, 3\n\t"
      "s_cbranch_scc1 LF7B_%=\n\t"
      "s_waitcnt vmcnt(0)\n\t"
      "v_cmp_ne_u64_e32 vcc, s[22:23], v[138:139]\n\t"
      "s_cbranch_vccnz LF7R_%=\n\t"
      "LF7B_%=:\n\t"
      "global_store_dwordx2 v131, v[128:129], s[60:61] sc1\n\t"
      "SP3_%=:\n\t"
      "s_barrier\n\t"
      "v_add_u32_e32 v142, 0, v141\n\t"
      "global_load_lds_dword v142, s[14:15]\n\t"
      "v_add_u32_e32 v142, 131072, v141\n\t"
      "global_load_lds_dword v142, s[14:15]\n\t"
      "v_add_u32_e32 v142, 262144, v141\n\t"
      "global_load_lds_dword v142, s[14:15]\n\t"
      "v_add_u32_e32 v142, 393216, v141\n\t"
      "global_load_lds_dword v142, s[14:15]\n\t"
      "v_add_u32_e32 v142, 524288, v141\n\t"
      "global_load_lds_dword v142, s[14:15]\n\t"
      "v_add_u32_e32 v142, 655360, v141\n\t"
      "global_load_lds_dword v142, s[14:15]\n\t"
      "v_add_u32_e32 v142, 786432, v141\n\t"
      "global_load_lds_dword v142, s[14:15]\n\t"
      "s_lshr_b32 s35, s2, 6\n\t"
      "s_lshl2_add_u32 s31, s26, s35\n\t"
      "s_and_b32 s35, s2, 7\n\t"
      "s_mul_i32 s34, s35, 14\n\t"
      "s_lshl4_add_u32 s35, s35, -14\n\t"
      "s_cmp_lt_u32 s31, 14\n\t"
      "s_cselect_b32 s34, s34, s35\n\t"
      "s_cselect_b32 s36, 1, 0\n\t"
      "s_or_b32 s27, s27, s36\n\t"
      "s_add_u32 s34, s34, s31\n\t"
      "s_lshr_b32 s29, s34, 2\n\t"
      "s_and_b32 s30, s34, 3\n\t"
      "s_mul_i32 s30, s30, 7\n\t"
      "v_and_b32_e32 v199, 60, v192\n\t"
      "v_and_b32_e32 v200, 24, v192\n\t"
      "v_lshlrev_b32_e32 v202, 3, v196\n\t"
      "v_lshl_or_b32 v202, v200, 2, v202\n\t"
      "v_lshl_or_b32 v202, v195, 7, v202\n\t"
      "v_lshl_or_b32 v202, v193, 10, v202\n\t"
      "v_add_u32_e32 v224, s39, v202\n\t"
      "v_xor_b32_e32 v225, 32, v224\n\t"
      "v_xor_b32_e32 v226, 64, v224\n\t"
      "v_xor_b32_e32 v227, 96, v224\n\t"
      "s_lshl_b32 s34, s24, 12\n\t"
      "s_add_u32 s34, s34, 98304\n\t"
      "v_and_b32_e32 v200, 16, v192\n\t"
      "v_lshlrev_b32_e32 v200, 3, v200\n\t"
      "v_xor_b32_e32 v241, v240, v200\n\t"
      "v_add_u32_e32 v241, s34, v241\n\t"
      "s_and_b32 s35, s24, 1\n\t"
      "s_lshl_b32 s35, s35, 7\n\t"
      "v_lshlrev_b32_e32 v199, 2, v199\n\t"
      "s_lshl_b32 s34, s24, 8\n\t"
      "v_add_u32_e32 v243, s34, v199\n\t"
      "v_xor_b32_e32 v199, s35, v199\n\t"
      "v_and_b32_e32 v200, 2, v192\n\t"
      "v_lshl_or_b32 v199, v200, 9, v199\n\t"
      "v_and_b32_e32 v202, 1, v192\n\t"
      "v_lshl_or_b32 v199, v202, 3, v199\n\t"
      "s_and_b32 s35, s34, 0x400\n\t"
      "s_add_u32 s34, s34, s35\n\t"
      "s_add_u32 s34, s34, 98304\n\t"
      "v_add_u32_e32 v242, s34, v199\n\t"
      "v_lshlrev_b32_e32 v199, 2, v202\n\t"
      "v_lshl_or_b32 v199, v200, 4, v199\n\t"
      "s_min_u32 s34, s29, 27\n\t"
      "s_lshl_b32 s34, s34, 7\n\t"
      "s_lshr_b32 s35, s24, 2\n\t"
      "s_lshl_b32 s35, s35, 6\n\t"
      "s_add_u32 s34, s34, s35\n\t"
      "s_lshr_b32 s35, s39, 9\n\t"
      "s_add_u32 s34, s34, s35\n\t"
      "v_add_u32_e32 v244, s34, v199\n\t"
      "s_add_u32 s48, s20, 8388608\n\t"
      "s_addc_u32 s49, s21, 0\n\t"
      "s_cmp_ge_u32 s31, 30\n\t"
      "s_cbranch_scc1 BYE_%=\n\t"
      "s_setprio 0\n\t"
      "s_waitcnt vmcnt(0)\n\t"
      "s_branch GST_%=\n\t"
      "BYE_%=:\n\t"
      "s_endpgm\n\t"
      "LA30R_%=:\n\t"
      "s_mov_b32 s80, 0\n\t"
      "LA30L_%=:\n\t"
      "s_add_u32 s80, s80, 1\n\t"
      "s_cmp_lt_u32 s80, 262144\n\t"
      "s_cbranch_scc0 LA30X_%=\n\t"
      "s_waitcnt vmcnt(0)\n\t"
      "s_sleep 1\n\t"
      "s_add_u32 s58, s64, s40\n\t"
      "s_addc_u32 s59, s65, 0\n\t"
      "global_load_dwordx4 v[128:131], v240, s[58:59] sc1\n\t"
      "global_load_dwordx4 v[132:135], v240, s[58:59] offset:1024 sc1\n\t"
      "s_add_u32 s58, s64, s42\n\t"
      "s_addc_u32 s59, s65, 0\n\t"
      "global_load_dwordx4 v[136:139], v240, s[58:59] sc1\n\t"
      "global_load_dwordx4 v[140:143], v240, s[58:59] offset:1024 sc1\n\t"
      "s_add_u32 s58, s64, s44\n\t"
      "s_addc_u32 s59, s65, 0\n\t"
      "global_load_dwordx4 v[144:147], v240, s[58:59] sc1\n\t"
      "global_load_dwordx4 v[148:151], v240, s[58:59] offset:1024 sc1\n\t"
      "s_add_u32 s58, s64, s46\n\t"
      "s_addc_u32 s59, s65, 0\n\t"
      "global_load_dwordx4 v[152:155], v240, s[58:59] sc1\n\t"
      "global_load_dwordx4 v[156:159], v240, s[58:59] offset:1024 sc1\n\t"
      "s_waitcnt vmcnt(0)\n\t"
      "v_max3_u32 v232, v128, v129, v130\n\t"
      "v_max3_u32 v232, v232, v131, v132\n\t"
      "v_max3_u32 v232, v232, v133, v134\n\t"
      "v_max3_u32 v232, v232, v135, v136\n\t"
      "v_max3_u32 v232, v232, v137, v138\n\t"
      "v_max3_u32 v232, v232, v139, v140\n\t"
      "v_max3_u32 v232, v232, v141, v142\n\t"
      "v_max3_u32 v232, v232, v143, v144\n\t"
      "v_max3_u32 v232, v232, v145, v146\n\t"
      "v_max3_u32 v232, v232, v147, v148\n\t"
      "v_max3_u32 v232, v232, v149, v150\n\t"
      "v_max3_u32 v232, v232, v151, v152\n\t"
      "v_max3_u32 v232, v232, v153, v154\n\t"
      "v_max3_u32 v232, v232, v155, v156\n\t"
      "v_max3_u32 v232, v232, v157, v158\n\t"
      "v_max_u32_e32 v232, v232, v159\n\t"
      "v_cmp_eq_u32_e32 vcc, -1, v232\n\t"
      "s_cbranch_vccnz LA30L_%=\n\t"
      "LA30X_%=:\n\t"
      "s_branch LA30B_%=\n\t"
      "LA31R_%=:\n\t"
      "s_mov_b32 s80, 0\n\t"
      "LA31L_%=:\n\t"
      "s_add_u32 s80, s80, 1\n\t"
      "s_cmp_lt_u32 s80, 262144\n\t"
      "s_cbranch_scc0 LA31X_%=\n\t"
      "s_waitcnt vmcnt(0)\n\t"
      "s_sleep 1\n\t"
      "s_add_u32 s58, s66, s40\n\t"
      "s_addc_u32 s59, s67, 0\n\t"
      "global_load_dwordx4 v[160:163], v240, s[58:59] sc1\n\t"
      "global_load_dwordx4 v[164:167], v240, s[58:59] offset:1024 sc1\n\t"
      "s_add_u32 s58, s66, s42\n\t"
      "s_addc_u32 s59, s67, 0\n\t"
      "global_load_dwordx4 v[168:171], v240, s[58:59] sc1\n\t"
      "global_load_dwordx4 v[172:175], v240, s[58:59] offset:1024 sc1\n\t"
      "s_add_u32 s58, s66, s44\n\t"
      "s_addc_u32 s59, s67, 0\n\t"
      "global_load_dwordx4 v[176:179], v240, s[58:59] sc1\n\t"
      "global_load_dwordx4 v[180:183], v240, s[58:59] offset:1024 sc1\n\t"
      "s_add_u32 s58, s66, s46\n\t"
      "s_addc_u32 s59, s67, 0\n\t"
      "global_load_dwordx4 v[184:187], v240, s[58:59] sc1\n\t"
      "global_load_dwordx4 v[188:191], v240, s[58:59] offset:1024 sc1\n\t"
      "s_waitcnt vmcnt(0)\n\t"
      "v_max3_u32 v232, v160, v161, v162\n\t"
      "v_max3_u32 v232, v232, v163, v164\n\t"
      "v_max3_u32 v232, v232, v165, v166\n\t"
      "v_max3_u32 v232, v232, v167, v168\n\t"
      "v_max3_u32 v232, v232, v169, v170\n\t"
      "v_max3_u32 v232, v232, v171, v172\n\t"
      "v_max3_u32 v232, v232, v173, v174\n\t"
      "v_max3_u32 v232, v232, v175, v176\n\t"
      "v_max3_u32 v232, v232, v177, v178\n\t"
      "v_max3_u32 v232, v232, v179, v180\n\t"
      "v_max3_u32 v232, v232, v181, v182\n\t"
      "v_max3_u32 v232, v232, v183, v184\n\t"
      "v_max3_u32 v232, v232, v185, v186\n\t"
      "v_max3_u32 v232, v232, v187, v188\n\t"
      "v_max3_u32 v232, v232, v189, v190\n\t"
      "v_max_u32_e32 v232, v232, v191\n\t"
      "v_cmp_eq_u32_e32 vcc, -1, v232\n\t"
      "s_cbranch_vccnz LA31L_%=\n\t"
      "LA31X_%=:\n\t"
      "v_mfma_f32_16x16x32_bf16 v[208:211], v[0:3], v[160:163], 0\n\t"
      "v_mfma_f32_16x16x32_bf16 v[208:211], v[16:19], v[164:167], v[208:211]\n\t"
      "v_mfma_f32_16x16x32_bf16 v[208:211], v[32:35], v[168:171], v[208:211]\n\t"
      "v_mfma_f32_16x16x32_bf16 v[208:211], v[48:51], v[172:175], v[208:211]\n\t"
      "v_mfma_f32_16x16x32_bf16 v[208:211], v[64:67], v[176:179], v[208:211]\n\t"
      "v_mfma_f32_16x16x32_bf16 v[208:211], v[80:83], v[180:183], v[208:211]\n\t"
      "v_mfma_f32_16x16x32_bf16 v[208:211], v[96:99], v[184:187], v[208:211]\n\t"
      "v_mfma_f32_16x16x32_bf16 v[208:211], v[112:115], v[188:191], v[208:211]\n\t"
      "v_mfma_f32_16x16x32_bf16 v[192:195], v[4:7], v[160:163], 0\n\t"
      "v_mfma_f32_16x16x32_bf16 v[192:195], v[20:23], v[164:167], v[192:195]\n\t"
      "v_mfma_f32_16x16x32_bf16 v[192:195], v[36:39], v[168:171], v[192:195]\n\t"
      "s_nop 7\n\t"
      "s_branch LA31B_%=\n\t"
      "LA32R_%=:\n\t"
      "s_mov_b32 s80, 0\n\t"
      "LA32L_%=:\n\t"
      "s_add_u32 s80, s80, 1\n\t"
      "s_cmp_lt_u32 s80, 262144\n\t"
      "s_cbranch_scc0 LA32X_%=\n\t"
      "s_waitcnt vmcnt(0)\n\t"
      "s_sleep 1\n\t"
      "s_add_u32 s58, s68, s40\n\t"
      "s_addc_u32 s59, s69, 0\n\t"
      "global_load_dwordx4 v[128:131], v240, s[58:59] sc1\n\t"
      "global_load_dwordx4 v[132:135], v240, s[58:59] offset:1024 sc1\n\t"
      "s_add_u32 s58, s68, s42\n\t"
      "s_addc_u32 s59, s69, 0\n\t"
      "global_load_dwordx4 v[136:139], v240, s[58:59] sc1\n\t"
      "global_load_dwordx4 v[140:143], v240, s[58:59] offset:1024 sc1\n\t"
      "s_add_u32 s58, s68, s44\n\t"
      "s_addc_u32 s59, s69, 0\n\t"
      "global_load_dwordx4 v[144:147], v240, s[58:59] sc1\n\t"
      "global_load_dwordx4 v[148:151], v240, s[58:59] offset:1024 sc1\n\t"
      "s_add_u32 s58, s68, s46\n\t"
      "s_addc_u32 s59, s69, 0\n\t"
      "global_load_dwordx4 v[152:155], v240, s[58:59] sc1\n\t"
      "global_load_dwordx4 v[156:159], v240, s[58:59] offset:1024 sc1\n\t"
      "s_waitcnt vmcnt(0)\n\t"
      "v_max3_u32 v232, v128, v129, v130\n\t"
      "v_max3_u32 v232, v232, v131, v132\n\t"
      "v_max3_u32 v232, v232, v133, v134\n\t"
      "v_max3_u32 v232, v232, v135, v136\n\t"
      "v_max3_u32 v232, v232, v137, v138\n\t"
      "v_max3_u32 v232, v232, v139, v140\n\t"
      "v_max3_u32 v232, v232, v141, v142\n\t"
      "v_max3_u32 v232, v232, v143, v144\n\t"
      "v_max3_u32 v232, v232, v145, v146\n\t"
      "v_max3_u32 v232, v232, v147, v148\n\t"
      "v_max3_u32 v232, v232, v149, v150\n\t"
      "v_max3_u32 v232, v232, v151, v152\n\t"
      "v_max3_u32 v232, v232, v153, v154\n\t"
      "v_max3_u32 v232, v232, v155, v156\n\t"
      "v_max3_u32 v232, v232, v157, v158\n\t"
      "v_max_u32_e32 v232, v232, v159\n\t"
      "v_cmp_eq_u32_e32 vcc, -1, v232\n\t"
      "s_cbranch_vccnz LA32L_%=\n\t"
      "LA32X_%=:\n\t"
      "v_mfma_f32_16x16x32_bf16 v[204:207], v[0:3], v[128:131], 0\n\t"
      "v_mfma_f32_16x16x32_bf16 v[204:207], v[16:19], v[132:135], v[204:207]\n\t"
      "v_mfma_f32_16x16x32_bf16 v[204:207], v[32:35], v[136:139], v[204:207]\n\t"
      "v_mfma_f32_16x16x32_bf16 v[204:207], v[48:51], v[140:143], v[204:207]\n\t"
      "v_mfma_f32_16x16x32_bf16 v[204:207], v[64:67], v[144:147], v[204:207]\n\t"
      "v_mfma_f32_16x16x32_bf16 v[204:207], v[80:83], v[148:151], v[204:207]\n\t"
      "v_mfma_f32_16x16x32_bf16 v[204:207], v[96:99], v[152:155], v[204:207]\n\t"
      "v_mfma_f32_16x16x32_bf16 v[204:207], v[112:115], v[156:159], v[204:207]\n\t"
      "v_mfma_f32_16x16x32_bf16 v[208:211], v[4:7], v[128:131], 0\n\t"
      "v_mfma_f32_16x16x32_bf16 v[208:211], v[20:23], v[132:135], v[208:211]\n\t"
      "v_mfma_f32_16x16x32_bf16 v[208:211], v[36:39], v[136:139], v[208:211]\n\t"
      "s_nop 7\n\t"
      "s_branch LA32B_%=\n\t"
      "LA33R_%=:\n\t"
      "s_mov_b32 s80, 0\n\t"
      "LA33L_%=:\n\t"
      "s_add_u32 s80, s80, 1\n\t"
      "s_cmp_lt_u32 s80, 262144\n\t"
      "s_cbranch_scc0 LA33X_%=\n\t"
      "s_waitcnt vmcnt(0)\n\t"
      "s_sleep 1\n\t"
      "s_add_u32 s58, s70, s40\n\t"
      "s_addc_u32 s59, s71, 0\n\t"
      "global_load_dwordx4 v[160:163], v240, s[58:59] sc1\n\t"
      "global_load_dwordx4 v[164:167], v240, s[58:59] offset:1024 sc1\n\t"
      "s_add_u32 s58, s70, s42\n\t"
      "s_addc_u32 s59, s71, 0\n\t"
      "global_load_dwordx4 v[168:171], v240, s[58:59] sc1\n\t"
      "global_load_dwordx4 v[172:175], v240, s[58:59] offset:1024 sc1\n\t"
      "s_add_u32 s58, s70, s44\n\t"
      "s_addc_u32 s59, s71, 0\n\t"
      "global_load_dwordx4 v[176:179], v240, s[58:59] sc1\n\t"
      "global_load_dwordx4 v[180:183], v240, s[58:59] offset:1024 sc1\n\t"
      "s_add_u32 s58, s70, s46\n\t"
      "s_addc_u32 s59, s71, 0\n\t"
      "global_load_dwordx4 v[184:187], v240, s[58:59] sc1\n\t"
      "global_load_dwordx4 v[188:191], v240, s[58:59] offset:1024 sc1\n\t"
      "s_waitcnt vmcnt(0)\n\t"
      "v_max3_u32 v232, v160, v161, v162\n\t"
      "v_max3_u32 v232, v232, v163, v164\n\t"
      "v_max3_u32 v232, v232, v165, v166\n\t"
      "v_max3_u32 v232, v232, v167, v168\n\t"
      "v_max3_u32 v232, v232, v169, v170\n\t"
      "v_max3_u32 v232, v232, v171, v172\n\t"
      "v_max3_u32 v232, v232, v173, v174\n\t"
      "v_max3_u32 v232, v232, v175, v176\n\t"
      "v_max3_u32 v232, v232, v177, v178\n\t"
      "v_max3_u32 v232, v232, v179, v180\n\t"
      "v_max3_u32 v232, v232, v181, v182\n\t"
      "v_max3_u32 v232, v232, v183, v184\n\t"
      "v_max3_u32 v232, v232, v185, v186\n\t"
      "v_max3_u32 v232, v232, v187, v188\n\t"
      "v_max3_u32 v232, v232, v189, v190\n\t"
      "v_max_u32_e32 v232, v232, v191\n\t"
      "v_cmp_eq_u32_e32 vcc, -1, v232\n\t"
      "s_cbranch_vccnz LA33L_%=\n\t"
      "LA33X_%=:\n\t"
      "v_mfma_f32_16x16x32_bf16 v[200:203], v[0:3], v[160:163], 0\n\t"
      "v_mfma_f32_16x16x32_bf16 v[200:203], v[16:19], v[164:167], v[200:203]\n\t"
      "v_mfma_f32_16x16x32_bf16 v[200:203], v[32:35], v[168:171], v[200:203]\n\t"
      "v_mfma_f32_16x16x32_bf16 v[200:203], v[48:51], v[172:175], v[200:203]\n\t"
      "v_mfma_f32_16x16x32_bf16 v[200:203], v[64:67], v[176:179], v[200:203]\n\t"
      "v_mfma_f32_16x16x32_bf16 v[200:203], v[80:83], v[180:183], v[200:203]\n\t"
      "v_mfma_f32_16x16x32_bf16 v[200:203], v[96:99], v[184:187], v[200:203]\n\t"
      "v_mfma_f32_16x16x32_bf16 v[200:203], v[112:115], v[188:191], v[200:203]\n\t"
      "v_mfma_f32_16x16x32_bf16 v[204:207], v[4:7], v[160:163], 0\n\t"
      "v_mfma_f32_16x16x32_bf16 v[204:207], v[20:23], v[164:167], v[204:207]\n\t"
      "v_mfma_f32_16x16x32_bf16 v[204:207], v[36:39], v[168:171], v[204:207]\n\t"
      "s_nop 7\n\t"
      "s_branch LA33B_%=\n\t"
      "LA34R_%=:\n\t"
      "s_mov_b32 s80, 0\n\t"
      "LA34L_%=:\n\t"
      "s_add_u32 s80, s80, 1\n\t"
      "s_cmp_lt_u32 s80, 262144\n\t"
      "s_cbranch_scc0 LA34X_%=\n\t"
      "s_waitcnt vmcnt(0)\n\t"
      "s_sleep 1\n\t"
      "s_add_u32 s58, s72, s40\n\t"
      "s_addc_u32 s59, s73, 0\n\t"
      "global_load_dwordx4 v[128:131], v240, s[58:59] sc1\n\t"
      "global_load_dwordx4 v[132:135], v240, s[58:59] offset:1024 sc1\n\t"
      "s_add_u32 s58, s72, s42\n\t"
      "s_addc_u32 s59, s73, 0\n\t"
      "global_load_dwordx4 v[136:139], v240, s[58:59] sc1\n\t"
      "global_load_dwordx4 v[140:143], v240, s[58:59] offset:1024 sc1\n\t"
      "s_add_u32 s58, s72, s44\n\t"
      "s_addc_u32 s59, s73, 0\n\t"
      "global_load_dwordx4 v[144:147], v240, s[58:59] sc1\n\t"
      "global_load_dwordx4 v[148:151], v240, s[58:59] offset:1024 sc1\n\t"
      "s_add_u32 s58, s72, s46\n\t"
      "s_addc_u32 s59, s73, 0\n\t"
      "global_load_dwordx4 v[152:155], v240, s[58:59] sc1\n\t"
      "global_load_dwordx4 v[156:159], v240, s[58:59] offset:1024 sc1\n\t"
      "s_waitcnt vmcnt(0)\n\t"
      "v_max3_u32 v232, v128, v129, v130\n\t"
      "v_max3_u32 v232, v232, v131, v132\n\t"
      "v_max3_u32 v232, v232, v133, v134\n\t"
      "v_max3_u32 v232, v232, v135, v136\n\t"
      "v_max3_u32 v232, v232, v137, v138\n\t"
      "v_max3_u32 v232, v232, v139, v140\n\t"
      "v_max3_u32 v232, v232, v141, v142\n\t"
      "v_max3_u32 v232, v232, v143, v144\n\t"
      "v_max3_u32 v232, v232, v145, v146\n\t"
      "v_max3_u32 v232, v232, v147, v148\n\t"
      "v_max3_u32 v232, v232, v149, v150\n\t"
      "v_max3_u32 v232, v232, v151, v152\n\t"
      "v_max3_u32 v232, v232, v153, v154\n\t"
      "v_max3_u32 v232, v232, v155, v156\n\t"
      "v_max3_u32 v232, v232, v157, v158\n\t"
      "v_max_u32_e32 v232, v232, v159\n\t"
      "v_cmp_eq_u32_e32 vcc, -1, v232\n\t"
      "s_cbranch_vccnz LA34L_%=\n\t"
      "LA34X_%=:\n\t"
      "v_mfma_f32_16x16x32_bf16 v[196:199], v[0:3], v[128:131], 0\n\t"
      "v_mfma_f32_16x16x32_bf16 v[196:199], v[16:19], v[132:135], v[196:199]\n\t"
      "v_mfma_f32_16x16x32_bf16 v[196:199], v[32:35], v[136:139], v[196:199]\n\t"
      "v_mfma_f32_16x16x32_bf16 v[196:199], v[48:51], v[140:143], v[196:199]\n\t"
      "v_mfma_f32_16x16x32_bf16 v[196:199], v[64:67], v[144:147], v[196:199]\n\t"
      "v_mfma_f32_16x16x32_bf16 v[196:199], v[80:83], v[148:151], v[196:199]\n\t"
      "v_mfma_f32_16x16x32_bf16 v[196:199], v[96:99], v[152:155], v[196:199]\n\t"
      "v_mfma_f32_16x16x32_bf16 v[196:199], v[112:115], v[156:159], v[196:199]\n\t"
      "v_mfma_f32_16x16x32_bf16 v[200:203], v[4:7], v[128:131], 0\n\t"
      "v_mfma_f32_16x16x32_bf16 v[200:203], v[20:23], v[132:135], v[200:203]\n\t"
      "v_mfma_f32_16x16x32_bf16 v[200:203], v[36:39], v[136:139], v[200:203]\n\t"
      "s_nop 7\n\t"
      "s_branch LA34B_%=\n\t"
      "LA35R_%=:\n\t"
      "s_mov_b32 s80, 0\n\t"
      "LA35L_%=:\n\t"
      "s_add_u32 s80, s80, 1\n\t"
      "s_cmp_lt_u32 s80, 262144\n\t"
      "s_cbranch_scc0 LA35X_%=\n\t"
      "s_waitcnt vmcnt(0)\n\t"
      "s_sleep 1\n\t"
      "s_add_u32 s58, s74, s40\n\t"
      "s_addc_u32 s59, s75, 0\n\t"
      "global_load_dwordx4 v[160:163], v240, s[58:59] sc1\n\t"
      "global_load_dwordx4 v[164:167], v240, s[58:59] offset:1024 sc1\n\t"
      "s_add_u32 s58, s74, s42\n\t"
      "s_addc_u32 s59, s75, 0\n\t"
      "global_load_dwordx4 v[168:171], v240, s[58:59] sc1\n\t"
      "global_load_dwordx4 v[172:175], v240, s[58:59] offset:1024 sc1\n\t"
      "s_add_u32 s58, s74, s44\n\t"
      "s_addc_u32 s59, s75, 0\n\t"
      "global_load_dwordx4 v[176:179], v240, s[58:59] sc1\n\t"
      "global_load_dwordx4 v[180:183], v240, s[58:59] offset:1024 sc1\n\t"
      "s_add_u32 s58, s74, s46\n\t"
      "s_addc_u32 s59, s75, 0\n\t"
      "global_load_dwordx4 v[184:187], v240, s[58:59] sc1\n\t"
      "global_load_dwordx4 v[188:191], v240, s[58:59] offset:1024 sc1\n\t"
      "s_waitcnt vmcnt(0)\n\t"
      "v_max3_u32 v232, v160, v161, v162\n\t"
      "v_max3_u32 v232, v232, v163, v164\n\t"
      "v_max3_u32 v232, v232, v165, v166\n\t"
      "v_max3_u32 v232, v232, v167, v168\n\t"
      "v_max3_u32 v232, v232, v169, v170\n\t"
      "v_max3_u32 v232, v232, v171, v172\n\t"
      "v_max3_u32 v232, v232, v173, v174\n\t"
      "v_max3_u32 v232, v232, v175, v176\n\t"
      "v_max3_u32 v232, v232, v177, v178\n\t"
      "v_max3_u32 v232, v232, v179, v180\n\t"
      "v_max3_u32 v232, v232, v181, v182\n\t"
      "v_max3_u32 v232, v232, v183, v184\n\t"
      "v_max3_u32 v232, v232, v185, v186\n\t"
      "v_max3_u32 v232, v232, v187, v188\n\t"
      "v_max3_u32 v232, v232, v189, v190\n\t"
      "v_max_u32_e32 v232, v232, v191\n\t"
      "v_cmp_eq_u32_e32 vcc, -1, v232\n\t"
      "s_cbranch_vccnz LA35L_%=\n\t"
      "LA35X_%=:\n\t"
      "v_mfma_f32_16x16x32_bf16 v[192:195], v[0:3], v[160:163], 0\n\t"
      "v_mfma_f32_16x16x32_bf16 v[192:195], v[16:19], v[164:167], v[192:195]\n\t"
      "v_mfma_f32_16x16x32_bf16 v[192:195], v[32:35], v[168:171], v[192:195]\n\t"
      "v_mfma_f32_16x16x32_bf16 v[192:195], v[48:51], v[172:175], v[192:195]\n\t"
      "v_mfma_f32_16x16x32_bf16 v[192:195], v[64:67], v[176:179], v[192:195]\n\t"
      "v_mfma_f32_16x16x32_bf16 v[192:195], v[80:83], v[180:183], v[192:195]\n\t"
      "v_mfma_f32_16x16x32_bf16 v[192:195], v[96:99], v[184:187], v[192:195]\n\t"
      "v_mfma_f32_16x16x32_bf16 v[192:195], v[112:115], v[188:191], v[192:195]\n\t"
      "v_mfma_f32_16x16x32_bf16 v[196:199], v[4:7], v[160:163], 0\n\t"
      "v_mfma_f32_16x16x32_bf16 v[196:199], v[20:23], v[164:167], v[196:199]\n\t"
      "v_mfma_f32_16x16x32_bf16 v[196:199], v[36:39], v[168:171], v[196:199]\n\t"
      "s_nop 7\n\t"
      "s_branch LA35B_%=\n\t"
      "LA36R_%=:\n\t"
      "s_mov_b32 s80, 0\n\t"
      "LA36L_%=:\n\t"
      "s_add_u32 s80, s80, 1\n\t"
      "s_cmp_lt_u32 s80, 262144\n\t"
      "s_cbranch_scc0 LA36X_%=\n\t"
      "s_waitcnt vmcnt(0)\n\t"
      "s_sleep 1\n\t"
      "s_add_u32 s58, s76, s40\n\t"
      "s_addc_u32 s59, s77, 0\n\t"
      "global_load_dwordx4 v[128:131], v240, s[58:59] sc1\n\t"
      "global_load_dwordx4 v[132:135], v240, s[58:59] offset:1024 sc1\n\t"
      "s_add_u32 s58, s76, s42\n\t"
      "s_addc_u32 s59, s77, 0\n\t"
      "global_load_dwordx4 v[136:139], v240, s[58:59] sc1\n\t"
      "global_load_dwordx4 v[140:143], v240, s[58:59] offset:1024 sc1\n\t"
      "s_add_u32 s58, s76, s44\n\t"
      "s_addc_u32 s59, s77, 0\n\t"
      "global_load_dwordx4 v[144:147], v240, s[58:59] sc1\n\t"
      "global_load_dwordx4 v[148:151], v240, s[58:59] offset:1024 sc1\n\t"
      "s_add_u32 s58, s76, s46\n\t"
      "s_addc_u32 s59, s77, 0\n\t"
      "global_load_dwordx4 v[152:155], v240, s[58:59] sc1\n\t"
      "global_load_dwordx4 v[156:159], v240, s[58:59] offset:1024 sc1\n\t"
      "s_waitcnt vmcnt(0)\n\t"
      "v_max3_u32 v232, v128, v129, v130\n\t"
      "v_max3_u32 v232, v232, v131, v132\n\t"
      "v_max3_u32 v232, v232, v133, v134\n\t"
      "v_max3_u32 v232, v232, v135, v136\n\t"
      "v_max3_u32 v232, v232, v137, v138\n\t"
      "v_max3_u32 v232, v232, v139, v140\n\t"
      "v_max3_u32 v232, v232, v141, v142\n\t"
      "v_max3_u32 v232, v232, v143, v144\n\t"
      "v_max3_u32 v232, v232, v145, v146\n\t"
      "v_max3_u32 v232, v232, v147, v148\n\t"
      "v_max3_u32 v232, v232, v149, v150\n\t"
      "v_max3_u32 v232, v232, v151, v152\n\t"
      "v_max3_u32 v232, v232, v153, v154\n\t"
      "v_max3_u32 v232, v232, v155, v156\n\t"
      "v_max3_u32 v232, v232, v157, v158\n\t"
      "v_max_u32_e32 v232, v232, v159\n\t"
      "v_cmp_eq_u32_e32 vcc, -1, v232\n\t"
      "s_cbranch_vccnz LA36L_%=\n\t"
      "LA36X_%=:\n\t"
      "v_mfma_f32_16x16x32_bf16 v[208:211], v[0:3], v[128:131], 0\n\t"
      "v_mfma_f32_16x16x32_bf16 v[208:211], v[16:19], v[132:135], v[208:211]\n\t"
      "v_mfma_f32_16x16x32_bf16 v[208:211], v[32:35], v[136:139], v[208:211]\n\t"
      "v_mfma_f32_16x16x32_bf16 v[208:211], v[48:51], v[140:143], v[208:211]\n\t"
      "v_mfma_f32_16x16x32_bf16 v[208:211], v[64:67], v[144:147], v[208:211]\n\t"
      "v_mfma_f32_16x16x32_bf16 v[208:211], v[80:83], v[148:151], v[208:211]\n\t"
      "v_mfma_f32_16x16x32_bf16 v[208:211], v[96:99], v[152:155], v[208:211]\n\t"
      "v_mfma_f32_16x16x32_bf16 v[208:211], v[112:115], v[156:159], v[208:211]\n\t"
      "v_mfma_f32_16x16x32_bf16 v[192:195], v[4:7], v[128:131], 0\n\t"
      "v_mfma_f32_16x16x32_bf16 v[192:195], v[20:23], v[132:135], v[192:195]\n\t"
      "v_mfma_f32_16x16x32_bf16 v[192:195], v[36:39], v[136:139], v[192:195]\n\t"
      "s_nop 7\n\t"
      "s_branch LA36B_%=\n\t"
      "LF5R_%=:\n\t"
      "s_mov_b32 s80, 0\n\t"
      "LF5L_%=:\n\t"
      "s_add_u32 s80, s80, 1\n\t"
      "s_cmp_lt_u32 s80, 262144\n\t"
      "s_cbranch_scc0 LF5X_%=\n\t"
      "s_sleep 2\n\t"
      "global_load_dwordx2 v[138:139], v143, s[60:61] sc1\n\t"
      "s_waitcnt vmcnt(0)\n\t"
      "v_cmp_ne_u64_e32 vcc, s[22:23], v[138:139]\n\t"
      "s_cbranch_vccnz LF5L_%=\n\t"
      "LF5X_%=:\n\t"
      "s_branch LF5B_%=\n\t"
      "PG2R_%=:\n\t"
      "s_mov_b32 s80, 0\n\t"
      "PG2L_%=:\n\t"
      "s_add_u32 s80, s80, 1\n\t"
      "s_cmp_lt_u32 s80, 262144\n\t"
      "s_cbranch_scc0 PG2X_%=\n\t"
      "s_sleep 2\n\t"
      "global_load_dword v232, v234, s[68:69] offset:252 sc1\n\t"
      "s_waitcnt vmcnt(0)\n\t"
      "v_cmp_eq_u32_e32 vcc, -1, v232\n\t"
      "s_cbranch_vccnz PG2L_%=\n\t"
      "PG2X_%=:\n\t"
      "s_branch PG2B_%=\n\t"
      "LA40R_%=:\n\t"
      "s_mov_b32 s80, 0\n\t"
      "LA40L_%=:\n\t"
      "s_add_u32 s80, s80, 1\n\t"
      "s_cmp_lt_u32 s80, 262144\n\t"
      "s_cbranch_scc0 LA40X_%=\n\t"
      "s_waitcnt vmcnt(0)\n\t"
      "s_mov_b32 s81, s80\n\t"
      "PQ0R_%=:\n\t"
      "s_mov_b32 s80, 0\n\t"
      "PQ0L_%=:\n\t"
      "s_add_u32 s80, s80, 1\n\t"
      "s_cmp_lt_u32 s80, 262144\n\t"
      "s_cbranch_scc0 PQ0X_%=\n\t"
      "s_sleep 1\n\t"
      "global_load_dword v232, v233, s[64:65] offset:252 sc1\n\t"
      "s_waitcnt vmcnt(0)\n\t"
      "v_cmp_eq_u32_e32 vcc, -1, v232\n\t"
      "s_cbranch_vccnz PQ0L_%=\n\t"
      "PQ0X_%=:\n\t"
      "s_mov_b32 s80, s81\n\t"
      "s_add_u32 s58, s64, s40\n\t"
      "s_addc_u32 s59, s65, 0\n\t"
      "global_load_dwordx4 v[128:131], v240, s[58:59] sc1\n\t"
      "s_add_u32 s58, s64, s41\n\t"
      "s_addc_u32 s59, s65, 0\n\t"
      "global_load_dwordx4 v[132:135], v240, s[58:59] sc1\n\t"
      "s_add_u32 s58, s64, s42\n\t"
      "s_addc_u32 s59, s65, 0\n\t"
      "global_load_dwordx4 v[136:139], v240, s[58:59] sc1\n\t"
      "s_add_u32 s58, s64, s43\n\t"
      "s_addc_u32 s59, s65, 0\n\t"
      "global_load_dwordx4 v[140:143], v240, s[58:59] sc1\n\t"
      "s_add_u32 s58, s64, s44\n\t"
      "s_addc_u32 s59, s65, 0\n\t"
      "global_load_dwordx4 v[144:147], v240, s[58:59] sc1\n\t"
      "s_add_u32 s58, s64, s45\n\t"
      "s_addc_u32 s59, s65, 0\n\t"
      "global_load_dwordx4 v[148:151], v240, s[58:59] sc1\n\t"
      "s_add_u32 s58, s64, s46\n\t"
      "s_addc_u32 s59, s65, 0\n\t"
      "global_load_dwordx4 v[152:155], v240, s[58:59] sc1\n\t"
      "s_waitcnt vmcnt(0)\n\t"
      "v_max3_u32 v232, v128, v129, v130\n\t"
      "v_max3_u32 v232, v232, v131, v132\n\t"
      "v_max3_u32 v232, v232, v133, v134\n\t"
      "v_max3_u32 v232, v232, v135, v136\n\t"
      "v_max3_u32 v232, v232, v137, v138\n\t"
      "v_max3_u32 v232, v232, v139, v140\n\t"
      "v_max3_u32 v232, v232, v141, v142\n\t"
      "v_max3_u32 v232, v232, v143, v144\n\t"
      "v_max3_u32 v232, v232, v145, v146\n\t"
      "v_max3_u32 v232, v232, v147, v148\n\t"
      "v_max3_u32 v232, v232, v149, v150\n\t"
      "v_max3_u32 v232, v232, v151, v152\n\t"
      "v_max3_u32 v232, v232, v153, v154\n\t"
      "v_max_u32_e32 v232, v232, v155\n\t"
      "v_cmp_eq_u32_e32 vcc, -1, v232\n\t"
      "s_cbranch_vccnz LA40L_%=\n\t"
      "LA40X_%=:\n\t"
      "v_mfma_f32_16x16x32_bf16 v[192:195], v[128:131], v[0:3], 0\n\t"
      "v_mfma_f32_16x16x32_bf16 v[192:195], v[132:135], v[16:19], v[192:195]\n\t"
      "v_mfma_f32_16x16x32_bf16 v[192:195], v[136:139], v[32:35], v[192:195]\n\t"
      "v_mfma_f32_16x16x32_bf16 v[192:195], v[140:143], v[48:51], v[192:195]\n\t"
      "v_mfma_f32_16x16x32_bf16 v[192:195], v[144:147], v[64:67], v[192:195]\n\t"
      "v_mfma_f32_16x16x32_bf16 v[192:195], v[148:151], v[80:83], v[192:195]\n\t"
      "v_mfma_f32_16x16x32_bf16 v[192:195], v[152:155], v[96:99], v[192:195]\n\t"
      "v_mfma_f32_16x16x32_bf16 v[196:199], v[128:131], v[4:7], 0\n\t"
      "v_mfma_f32_16x16x32_bf16 v[196:199], v[132:135], v[20:23], v[196:199]\n\t"
      "v_mfma_f32_16x16x32_bf16 v[196:199], v[136:139], v[36:39], v[196:199]\n\t"
      "s_nop 7\n\t"
      "s_branch LA40B_%=\n\t"
      "LA41R_%=:\n\t"
      "s_mov_b32 s80, 0\n\t"
      "LA41L_%=:\n\t"
      "s_add_u32 s80, s80, 1\n\t"
      "s_cmp_lt_u32 s80, 262144\n\t"
      "s_cbranch_scc0 LA41X_%=\n\t"
      "s_waitcnt vmcnt(0)\n\t"
      "s_mov_b32 s81, s80\n\t"
      "PQ1R_%=:\n\t"
      "s_mov_b32 s80, 0\n\t"
      "PQ1L_%=:\n\t"
      "s_add_u32 s80, s80, 1\n\t"
      "s_cmp_lt_u32 s80, 262144\n\t"
      "s_cbranch_scc0 PQ1X_%=\n\t"
      "s_sleep 1\n\t"
      "global_load_dword v232, v233, s[66:67] offset:252 sc1\n\t"
      "s_waitcnt vmcnt(0)\n\t"
      "v_cmp_eq_u32_e32 vcc, -1, v232\n\t"
      "s_cbranch_vccnz PQ1L_%=\n\t"
      "PQ1X_%=:\n\t"
      "s_mov_b32 s80, s81\n\t"
      "s_add_u32 s58, s66, s40\n\t"
      "s_addc_u32 s59, s67, 0\n\t"
      "global_load_dwordx4 v[160:163], v240, s[58:59] sc1\n\t"
      "s_add_u32 s58, s66, s41\n\t"
      "s_addc_u32 s59, s67, 0\n\t"
      "global_load_dwordx4 v[164:167], v240, s[58:59] sc1\n\t"
      "s_add_u32 s58, s66, s42\n\t"
      "s_addc_u32 s59, s67, 0\n\t"
      "global_load_dwordx4 v[168:171], v240, s[58:59] sc1\n\t"
      "s_add_u32 s58, s66, s43\n\t"
      "s_addc_u32 s59, s67, 0\n\t"
      "global_load_dwordx4 v[172:175], v240, s[58:59] sc1\n\t"
      "s_add_u32 s58, s66, s44\n\t"
      "s_addc_u32 s59, s67, 0\n\t"
      "global_load_dwordx4 v[176:179], v240, s[58:59] sc1\n\t"
      "s_add_u32 s58, s66, s45\n\t"
      "s_addc_u32 s59, s67, 0\n\t"
      "global_load_dwordx4 v[180:183], v240, s[58:59] sc1\n\t"
      "s_add_u32 s58, s66, s46\n\t"
      "s_addc_u32 s59, s67, 0\n\t"
      "global_load_dwordx4 v[184:187], v240, s[58:59] sc1\n\t"
      "s_waitcnt vmcnt(0)\n\t"
      "v_max3_u32 v232, v160, v161, v162\n\t"
      "v_max3_u32 v232, v232, v163, v164\n\t"
      "v_max3_u32 v232, v232, v165, v166\n\t"
      "v_max3_u32 v232, v232, v167, v168\n\t"
      "v_max3_u32 v232, v232, v169, v170\n\t"
      "v_max3_u32 v232, v232, v171, v172\n\t"
      "v_max3_u32 v232, v232, v173, v174\n\t"
      "v_max3_u32 v232, v232, v175, v176\n\t"
      "v_max3_u32 v232, v232, v177, v178\n\t"
      "v_max3_u32 v232, v232, v179, v180\n\t"
      "v_max3_u32 v232, v232, v181, v182\n\t"
      "v_max3_u32 v232, v232, v183, v184\n\t"
      "v_max3_u32 v232, v232, v185, v186\n\t"
      "v_max_u32_e32 v232, v232, v187\n\t"
      "v_cmp_eq_u32_e32 vcc, -1, v232\n\t"
      "s_cbranch_vccnz LA41L_%=\n\t"
      "LA41X_%=:\n\t"
      "v_mfma_f32_16x16x32_bf16 v[208:211], v[160:163], v[0:3], 0\n\t"
      "v_mfma_f32_16x16x32_bf16 v[208:211], v[164:167], v[16:19], v[208:211]\n\t"
      "v_mfma_f32_16x16x32_bf16 v[208:211], v[168:171], v[32:35], v[208:211]\n\t"
      "v_mfma_f32_16x16x32_bf16 v[208:211], v[172:175], v[48:51], v[208:211]\n\t"
      "v_mfma_f32_16x16x32_bf16 v[208:211], v[176:179], v[64:67], v[208:211]\n\t"
      "v_mfma_f32_16x16x32_bf16 v[208:211], v[180:183], v[80:83], v[208:211]\n\t"
      "v_mfma_f32_16x16x32_bf16 v[208:211], v[184:187], v[96:99], v[208:211]\n\t"
      "v_mfma_f32_16x16x32_bf16 v[192:195], v[160:163], v[4:7], 0\n\t"
      "v_mfma_f32_16x16x32_bf16 v[192:195], v[164:167], v[20:23], v[192:195]\n\t"
      "v_mfma_f32_16x16x32_bf16 v[192:195], v[168:171], v[36:39], v[192:195]\n\t"
      "s_nop 7\n\t"
      "s_branch LA41B_%=\n\t"
      "LA42R_%=:\n\t"
      "s_mov_b32 s80, 0\n\t"
      "LA42L_%=:\n\t"
      "s_add_u32 s80, s80, 1\n\t"
      "s_cmp_lt_u32 s80, 262144\n\t"
      "s_cbranch_scc0 LA42X_%=\n\t"
      "s_waitcnt vmcnt(0)\n\t"
      "s_mov_b32 s81, s80\n\t"
      "PQ2R_%=:\n\t"
      "s_mov_b32 s80, 0\n\t"
      "PQ2L_%=:\n\t"
      "s_add_u32 s80, s80, 1\n\t"
      "s_cmp_lt_u32 s80, 262144\n\t"
      "s_cbranch_scc0 PQ2X_%=\n\t"
      "s_sleep 1\n\t"
      "global_load_dword v232, v233, s[68:69] offset:252 sc1\n\t"
      "s_waitcnt vmcnt(0)\n\t"
      "v_cmp_eq_u32_e32 vcc, -1, v232\n\t"
      "s_cbranch_vccnz PQ2L_%=\n\t"
      "PQ2X_%=:\n\t"
      "s_mov_b32 s80, s81\n\t"
      "s_add_u32 s58, s68, s40\n\t"
      "s_addc_u32 s59, s69, 0\n\t"
      "global_load_dwordx4 v[128:131], v240, s[58:59] sc1\n\t"
      "s_add_u32 s58, s68, s41\n\t"
      "s_addc_u32 s59, s69, 0\n\t"
      "global_load_dwordx4 v[132:135], v240, s[58:59] sc1\n\t"
      "s_add_u32 s58, s68, s42\n\t"
      "s_addc_u32 s59, s69, 0\n\t"
      "global_load_dwordx4 v[136:139], v240, s[58:59] sc1\n\t"
      "s_add_u32 s58, s68, s43\n\t"
      "s_addc_u32 s59, s69, 0\n\t"
      "global_load_dwordx4 v[140:143], v240, s[58:59] sc1\n\t"
      "s_add_u32 s58, s68, s44\n\t"
      "s_addc_u32 s59, s69, 0\n\t"
      "global_load_dwordx4 v[144:147], v240, s[58:59] sc1\n\t"
      "s_add_u32 s58, s68, s45\n\t"
      "s_addc_u32 s59, s69, 0\n\t"
      "global_load_dwordx4 v[148:151], v240, s[58:59] sc1\n\t"
      "s_add_u32 s58, s68, s46\n\t"
      "s_addc_u32 s59, s69, 0\n\t"
      "global_load_dwordx4 v[152:155], v240, s[58:59] sc1\n\t"
      "s_waitcnt vmcnt(0)\n\t"
      "v_max3_u32 v232, v128, v129, v130\n\t"
      "v_max3_u32 v232, v232, v131, v132\n\t"
      "v_max3_u32 v232, v232, v133, v134\n\t"
      "v_max3_u32 v232, v232, v135, v136\n\t"
      "v_max3_u32 v232, v232, v137, v138\n\t"
      "v_max3_u32 v232, v232, v139, v140\n\t"
      "v_max3_u32 v232, v232, v141, v142\n\t"
      "v_max3_u32 v232, v232, v143, v144\n\t"
      "v_max3_u32 v232, v232, v145, v146\n\t"
      "v_max3_u32 v232, v232, v147, v148\n\t"
      "v_max3_u32 v232, v232, v149, v150\n\t"
      "v_max3_u32 v232, v232, v151, v152\n\t"
      "v_max3_u32 v232, v232, v153, v154\n\t"
      "v_max_u32_e32 v232, v232, v155\n\t"
      "v_cmp_eq_u32_e32 vcc, -1, v232\n\t"
      "s_cbranch_vccnz LA42L_%=\n\t"
      "LA42X_%=:\n\t"
      "v_mfma_f32_16x16x32_bf16 v[204:207], v[128:131], v[0:3], 0\n\t"
      "v_mfma_f32_16x16x32_bf16 v[204:207], v[132:135], v[16:19], v[204:207]\n\t"
      "v_mfma_f32_16x16x32_bf16 v[204:207], v[136:139], v[32:35], v[204:207]\n\t"
      "v_mfma_f32_16x16x32_bf16 v[204:207], v[140:143], v[48:51], v[204:207]\n\t"
      "v_mfma_f32_16x16x32_bf16 v[204:207], v[144:147], v[64:67], v[204:207]\n\t"
      "v_mfma_f32_16x16x32_bf16 v[204:207], v[148:151], v[80:83], v[204:207]\n\t"
      "v_mfma_f32_16x16x32_bf16 v[204:207], v[152:155], v[96:99], v[204:207]\n\t"
      "v_mfma_f32_16x16x32_bf16 v[208:211], v[128:131], v[4:7], 0\n\t"
      "v_mfma_f32_16x16x32_bf16 v[208:211], v[132:135], v[20:23], v[208:211]\n\t"
      "v_mfma_f32_16x16x32_bf16 v[208:211], v[136:139], v[36:39], v[208:211]\n\t"
      "s_nop 7\n\t"
      "s_branch LA42B_%=\n\t"
      "LA43R_%=:\n\t"
      "s_mov_b32 s80, 0\n\t"
      "LA43L_%=:\n\t"
      "s_add_u32 s80, s80, 1\n\t"
      "s_cmp_lt_u32 s80, 262144\n\t"
      "s_cbranch_scc0 LA43X_%=\n\t"
      "s_waitcnt vmcnt(0)\n\t"
      "s_mov_b32 s81, s80\n\t"
      "PQ3R_%=:\n\t"
      "s_mov_b32 s80, 0\n\t"
      "PQ3L_%=:\n\t"
      "s_add_u32 s80, s80, 1\n\t"
      "s_cmp_lt_u32 s80, 262144\n\t"
      "s_cbranch_scc0 PQ3X_%=\n\t"
      "s_sleep 1\n\t"
      "global_load_dword v232, v233, s[70:71] offset:252 sc1\n\t"
      "s_waitcnt vmcnt(0)\n\t"
      "v_cmp_eq_u32_e32 vcc, -1, v232\n\t"
      "s_cbranch_vccnz PQ3L_%=\n\t"
      "PQ3X_%=:\n\t"
      "s_mov_b32 s80, s81\n\t"
      "s_add_u32 s58, s70, s40\n\t"
      "s_addc_u32 s59, s71, 0\n\t"
      "global_load_dwordx4 v[160:163], v240, s[58:59] sc1\n\t"
      "s_add_u32 s58, s70, s41\n\t"
      "s_addc_u32 s59, s71, 0\n\t"
      "global_load_dwordx4 v[164:167], v240, s[58:59] sc1\n\t"
      "s_add_u32 s58, s70, s42\n\t"
      "s_addc_u32 s59, s71, 0\n\t"
      "global_load_dwordx4 v[168:171], v240, s[58:59] sc1\n\t"
      "s_add_u32 s58, s70, s43\n\t"
      "s_addc_u32 s59, s71, 0\n\t"
      "global_load_dwordx4 v[172:175], v240, s[58:59] sc1\n\t"
      "s_add_u32 s58, s70, s44\n\t"
      "s_addc_u32 s59, s71, 0\n\t"
      "global_load_dwordx4 v[176:179], v240, s[58:59] sc1\n\t"
      "s_add_u32 s58, s70, s45\n\t"
      "s_addc_u32 s59, s71, 0\n\t"
      "global_load_dwordx4 v[180:183], v240, s[58:59] sc1\n\t"
      "s_add_u32 s58, s70, s46\n\t"
      "s_addc_u32 s59, s71, 0\n\t"
      "global_load_dwordx4 v[184:187], v240, s[58:59] sc1\n\t"
      "s_waitcnt vmcnt(0)\n\t"
      "v_max3_u32 v232, v160, v161, v162\n\t"
      "v_max3_u32 v232, v232, v163, v164\n\t"
      "v_max3_u32 v232, v232, v165, v166\n\t"
      "v_max3_u32 v232, v232, v167, v168\n\t"
      "v_max3_u32 v232, v232, v169, v170\n\t"
      "v_max3_u32 v232, v232, v171, v172\n\t"
      "v_max3_u32 v232, v232, v173, v174\n\t"
      "v_max3_u32 v232, v232, v175, v176\n\t"
      "v_max3_u32 v232, v232, v177, v178\n\t"
      "v_max3_u32 v232, v232, v179, v180\n\t"
      "v_max3_u32 v232, v232, v181, v182\n\t"
      "v_max3_u32 v232, v232, v183, v184\n\t"
      "v_max3_u32 v232, v232, v185, v186\n\t"
      "v_max_u32_e32 v232, v232, v187\n\t"
      "v_cmp_eq_u32_e32 vcc, -1, v232\n\t"
      "s_cbranch_vccnz LA43L_%=\n\t"
      "LA43X_%=:\n\t"
      "v_mfma_f32_16x16x32_bf16 v[200:203], v[160:163], v[0:3], 0\n\t"
      "v_mfma_f32_16x16x32_bf16 v[200:203], v[164:167], v[16:19], v[200:203]\n\t"
      "v_mfma_f32_16x16x32_bf16 v[200:203], v[168:171], v[32:35], v[200:203]\n\t"
      "v_mfma_f32_16x16x32_bf16 v[200:203], v[172:175], v[48:51], v[200:203]\n\t"
      "v_mfma_f32_16x16x32_bf16 v[200:203], v[176:179], v[64:67], v[200:203]\n\t"
      "v_mfma_f32_16x16x32_bf16 v[200:203], v[180:183], v[80:83], v[200:203]\n\t"
      "v_mfma_f32_16x16x32_bf16 v[200:203], v[184:187], v[96:99], v[200:203]\n\t"
      "v_mfma_f32_16x16x32_bf16 v[204:207], v[160:163], v[4:7], 0\n\t"
      "v_mfma_f32_16x16x32_bf16 v[204:207], v[164:167], v[20:23], v[204:207]\n\t"
      "v_mfma_f32_16x16x32_bf16 v[204:207], v[168:171], v[36:39], v[204:207]\n\t"
      "s_nop 7\n\t"
      "s_branch LA43B_%=\n\t"
      "LA44R_%=:\n\t"
      "s_mov_b32 s80, 0\n\t"
      "LA44L_%=:\n\t"
      "s_add_u32 s80, s80, 1\n\t"
      "s_cmp_lt_u32 s80, 262144\n\t"
      "s_cbranch_scc0 LA44X_%=\n\t"
      "s_waitcnt vmcnt(0)\n\t"
      "s_mov_b32 s81, s80\n\t"
      "PQ4R_%=:\n\t"
      "s_mov_b32 s80, 0\n\t"
      "PQ4L_%=:\n\t"
      "s_add_u32 s80, s80, 1\n\t"
      "s_cmp_lt_u32 s80, 262144\n\t"
      "s_cbranch_scc0 PQ4X_%=\n\t"
      "s_sleep 1\n\t"
      "global_load_dword v232, v233, s[72:73] offset:252 sc1\n\t"
      "s_waitcnt vmcnt(0)\n\t"
      "v_cmp_eq_u32_e32 vcc, -1, v232\n\t"
      "s_cbranch_vccnz PQ4L_%=\n\t"
      "PQ4X_%=:\n\t"
      "s_mov_b32 s80, s81\n\t"
      "s_add_u32 s58, s72, s40\n\t"
      "s_addc_u32 s59, s73, 0\n\t"
      "global_load_dwordx4 v[128:131], v240, s[58:59] sc1\n\t"
      "s_add_u32 s58, s72, s41\n\t"
      "s_addc_u32 s59, s73, 0\n\t"
      "global_load_dwordx4 v[132:135], v240, s[58:59] sc1\n\t"
      "s_add_u32 s58, s72, s42\n\t"
      "s_addc_u32 s59, s73, 0\n\t"
      "global_load_dwordx4 v[136:139], v240, s[58:59] sc1\n\t"
      "s_add_u32 s58, s72, s43\n\t"
      "s_addc_u32 s59, s73, 0\n\t"
      "global_load_dwordx4 v[140:143], v240, s[58:59] sc1\n\t"
      "s_add_u32 s58, s72, s44\n\t"
      "s_addc_u32 s59, s73, 0\n\t"
      "global_load_dwordx4 v[144:147], v240, s[58:59] sc1\n\t"
      "s_add_u32 s58, s72, s45\n\t"
      "s_addc_u32 s59, s73, 0\n\t"
      "global_load_dwordx4 v[148:151], v240, s[58:59] sc1\n\t"
      "s_add_u32 s58, s72, s46\n\t"
      "s_addc_u32 s59, s73, 0\n\t"
      "global_load_dwordx4 v[152:155], v240, s[58:59] sc1\n\t"
      "s_waitcnt vmcnt(0)\n\t"
      "v_max3_u32 v232, v128, v129, v130\n\t"
      "v_max3_u32 v232, v232, v131, v132\n\t"
      "v_max3_u32 v232, v232, v133, v134\n\t"
      "v_max3_u32 v232, v232, v135, v136\n\t"
      "v_max3_u32 v232, v232, v137, v138\n\t"
      "v_max3_u32 v232, v232, v139, v140\n\t"
      "v_max3_u32 v232, v232, v141, v142\n\t"
      "v_max3_u32 v232, v232, v143, v144\n\t"
      "v_max3_u32 v232, v232, v145, v146\n\t"
      "v_max3_u32 v232, v232, v147, v148\n\t"
      "v_max3_u32 v232, v232, v149, v150\n\t"
      "v_max3_u32 v232, v232, v151, v152\n\t"
      "v_max3_u32 v232, v232, v153, v154\n\t"
      "v_max_u32_e32 v232, v232, v155\n\t"
      "v_cmp_eq_u32_e32 vcc, -1, v232\n\t"
      "s_cbranch_vccnz LA44L_%=\n\t"
      "LA44X_%=:\n\t"
      "v_mfma_f32_16x16x32_bf16 v[196:199], v[128:131], v[0:3], 0\n\t"
      "v_mfma_f32_16x16x32_bf16 v[196:199], v[132:135], v[16:19], v[196:199]\n\t"
      "v_mfma_f32_16x16x32_bf16 v[196:199], v[136:139], v[32:35], v[196:199]\n\t"
      "v_mfma_f32_16x16x32_bf16 v[196:199], v[140:143], v[48:51], v[196:199]\n\t"
      "v_mfma_f32_16x16x32_bf16 v[196:199], v[144:147], v[64:67], v[196:199]\n\t"
      "v_mfma_f32_16x16x32_bf16 v[196:199], v[148:151], v[80:83], v[196:199]\n\t"
      "v_mfma_f32_16x16x32_bf16 v[196:199], v[152:155], v[96:99], v[196:199]\n\t"
      "v_mfma_f32_16x16x32_bf16 v[200:203], v[128:131], v[4:7], 0\n\t"
      "v_mfma_f32_16x16x32_bf16 v[200:203], v[132:135], v[20:23], v[200:203]\n\t"
      "v_mfma_f32_16x16x32_bf16 v[200:203], v[136:139], v[36:39], v[200:203]\n\t"
      "s_nop 7\n\t"
      "s_branch LA44B_%=\n\t"
      "LA45R_%=:\n\t"
      "s_mov_b32 s80, 0\n\t"
      "LA45L_%=:\n\t"
      "s_add_u32 s80, s80, 1\n\t"
      "s_cmp_lt_u32 s80, 262144\n\t"
      "s_cbranch_scc0 LA45X_%=\n\t"
      "s_waitcnt vmcnt(0)\n\t"
      "s_mov_b32 s81, s80\n\t"
      "PQ5R_%=:\n\t"
      "s_mov_b32 s80, 0\n\t"
      "PQ5L_%=:\n\t"
      "s_add_u32 s80, s80, 1\n\t"
      "s_cmp_lt_u32 s80, 262144\n\t"
      "s_cbranch_scc0 PQ5X_%=\n\t"
      "s_sleep 1\n\t"
      "global_load_dword v232, v233, s[74:75] offset:252 sc1\n\t"
      "s_waitcnt vmcnt(0)\n\t"
      "v_cmp_eq_u32_e32 vcc, -1, v232\n\t"
      "s_cbranch_vccnz PQ5L_%=\n\t"
      "PQ5X_%=:\n\t"
      "s_mov_b32 s80, s81\n\t"
      "s_add_u32 s58, s74, s40\n\t"
      "s_addc_u32 s59, s75, 0\n\t"
      "global_load_dwordx4 v[160:163], v240, s[58:59] sc1\n\t"
      "s_add_u32 s58, s74, s41\n\t"
      "s_addc_u32 s59, s75, 0\n\t"
      "global_load_dwordx4 v[164:167], v240, s[58:59] sc1\n\t"
      "s_add_u32 s58, s74, s42\n\t"
      "s_addc_u32 s59, s75, 0\n\t"
      "global_load_dwordx4 v[168:171], v240, s[58:59] sc1\n\t"
      "s_add_u32 s58, s74, s43\n\t"
      "s_addc_u32 s59, s75, 0\n\t"
      "global_load_dwordx4 v[172:175], v240, s[58:59] sc1\n\t"
      "s_add_u32 s58, s74, s44\n\t"
      "s_addc_u32 s59, s75, 0\n\t"
      "global_load_dwordx4 v[176:179], v240, s[58:59] sc1\n\t"
      "s_add_u32 s58, s74, s45\n\t"
      "s_addc_u32 s59, s75, 0\n\t"
      "global_load_dwordx4 v[180:183], v240, s[58:59] sc1\n\t"
      "s_add_u32 s58, s74, s46\n\t"
      "s_addc_u32 s59, s75, 0\n\t"
      "global_load_dwordx4 v[184:187], v240, s[58:59] sc1\n\t"
      "s_waitcnt vmcnt(0)\n\t"
      "v_max3_u32 v232, v160, v161, v162\n\t"
      "v_max3_u32 v232, v232, v163, v164\n\t"
      "v_max3_u32 v232, v232, v165, v166\n\t"
      "v_max3_u32 v232, v232, v167, v168\n\t"
      "v_max3_u32 v232, v232, v169, v170\n\t"
      "v_max3_u32 v232, v232, v171, v172\n\t"
      "v_max3_u32 v232, v232, v173, v174\n\t"
      "v_max3_u32 v232, v232, v175, v176\n\t"
      "v_max3_u32 v232, v232, v177, v178\n\t"
      "v_max3_u32 v232, v232, v179, v180\n\t"
      "v_max3_u32 v232, v232, v181, v182\n\t"
      "v_max3_u32 v232, v232, v183, v184\n\t"
      "v_max3_u32 v232, v232, v185, v186\n\t"
      "v_max_u32_e32 v232, v232, v187\n\t"
      "v_cmp_eq_u32_e32 vcc, -1, v232\n\t"
      "s_cbranch_vccnz LA45L_%=\n\t"
      "LA45X_%=:\n\t"
      "v_mfma_f32_16x16x32_bf16 v[192:195], v[160:163], v[0:3], 0\n\t"
      "v_mfma_f32_16x16x32_bf16 v[192:195], v[164:167], v[16:19], v[192:195]\n\t"
      "v_mfma_f32_16x16x32_bf16 v[192:195], v[168:171], v[32:35], v[192:195]\n\t"
      "v_mfma_f32_16x16x32_bf16 v[192:195], v[172:175], v[48:51], v[192:195]\n\t"
      "v_mfma_f32_16x16x32_bf16 v[192:195], v[176:179], v[64:67], v[192:195]\n\t"
      "v_mfma_f32_16x16x32_bf16 v[192:195], v[180:183], v[80:83], v[192:195]\n\t"
      "v_mfma_f32_16x16x32_bf16 v[192:195], v[184:187], v[96:99], v[192:195]\n\t"
      "v_mfma_f32_16x16x32_bf16 v[196:199], v[160:163], v[4:7], 0\n\t"
      "v_mfma_f32_16x16x32_bf16 v[196:199], v[164:167], v[20:23], v[196:199]\n\t"
      "v_mfma_f32_16x16x32_bf16 v[196:199], v[168:171], v[36:39], v[196:199]\n\t"
      "s_nop 7\n\t"
      "s_branch LA45B_%=\n\t"
      "LA46R_%=:\n\t"
      "s_mov_b32 s80, 0\n\t"
      "LA46L_%=:\n\t"
      "s_add_u32 s80, s80, 1\n\t"
      "s_cmp_lt_u32 s80, 262144\n\t"
      "s_cbranch_scc0 LA46X_%=\n\t"
      "s_waitcnt vmcnt(0)\n\t"
      "s_mov_b32 s81, s80\n\t"
      "PQ6R_%=:\n\t"
      "s_mov_b32 s80, 0\n\t"
      "PQ6L_%=:\n\t"
      "s_add_u32 s80, s80, 1\n\t"
      "s_cmp_lt_u32 s80, 262144\n\t"
      "s_cbranch_scc0 PQ6X_%=\n\t"
      "s_sleep 1\n\t"
      "global_load_dword v232, v233, s[76:77] offset:252 sc1\n\t"
      "s_waitcnt vmcnt(0)\n\t"
      "v_cmp_eq_u32_e32 vcc, -1, v232\n\t"
      "s_cbranch_vccnz PQ6L_%=\n\t"
      "PQ6X_%=:\n\t"
      "s_mov_b32 s80, s81\n\t"
      "s_add_u32 s58, s76, s40\n\t"
      "s_addc_u32 s59, s77, 0\n\t"
      "global_load_dwordx4 v[128:131], v240, s[58:59] sc1\n\t"
      "s_add_u32 s58, s76, s41\n\t"
      "s_addc_u32 s59, s77, 0\n\t"
      "global_load_dwordx4 v[132:135], v240, s[58:59] sc1\n\t"
      "s_add_u32 s58, s76, s42\n\t"
      "s_addc_u32 s59, s77, 0\n\t"
      "global_load_dwordx4 v[136:139], v240, s[58:59] sc1\n\t"
      "s_add_u32 s58, s76, s43\n\t"
      "s_addc_u32 s59, s77, 0\n\t"
      "global_load_dwordx4 v[140:143], v240, s[58:59] sc1\n\t"
      "s_add_u32 s58, s76, s44\n\t"
      "s_addc_u32 s59, s77, 0\n\t"
      "global_load_dwordx4 v[144:147], v240, s[58:59] sc1\n\t"
      "s_add_u32 s58, s76, s45\n\t"
      "s_addc_u32 s59, s77, 0\n\t"
      "global_load_dwordx4 v[148:151], v240, s[58:59] sc1\n\t"
      "s_add_u32 s58, s76, s46\n\t"
      "s_addc_u32 s59, s77, 0\n\t"
      "global_load_dwordx4 v[152:155], v240, s[58:59] sc1\n\t"
      "s_waitcnt vmcnt(0)\n\t"
      "v_max3_u32 v232, v128, v129, v130\n\t"
      "v_max3_u32 v232, v232, v131, v132\n\t"
      "v_max3_u32 v232, v232, v133, v134\n\t"
      "v_max3_u32 v232, v232, v135, v136\n\t"
      "v_max3_u32 v232, v232, v137, v138\n\t"
      "v_max3_u32 v232, v232, v139, v140\n\t"
      "v_max3_u32 v232, v232, v141, v142\n\t"
      "v_max3_u32 v232, v232, v143, v144\n\t"
      "v_max3_u32 v232, v232, v145, v146\n\t"
      "v_max3_u32 v232, v232, v147, v148\n\t"
      "v_max3_u32 v232, v232, v149, v150\n\t"
      "v_max3_u32 v232, v232, v151, v152\n\t"
      "v_max3_u32 v232, v232, v153, v154\n\t"
      "v_max_u32_e32 v232, v232, v155\n\t"
      "v_cmp_eq_u32_e32 vcc, -1, v232\n\t"
      "s_cbranch_vccnz LA46L_%=\n\t"
      "LA46X_%=:\n\t"
      "v_mfma_f32_16x16x32_bf16 v[208:211], v[128:131], v[0:3], 0\n\t"
      "v_mfma_f32_16x16x32_bf16 v[208:211], v[132:135], v[16:19], v[208:211]\n\t"
      "v_mfma_f32_16x16x32_bf16 v[208:211], v[136:139], v[32:35], v[208:211]\n\t"
      "v_mfma_f32_16x16x32_bf16 v[208:211], v[140:143], v[48:51], v[208:211]\n\t"
      "v_mfma_f32_16x16x32_bf16 v[208:211], v[144:147], v[64:67], v[208:211]\n\t"
      "v_mfma_f32_16x16x32_bf16 v[208:211], v[148:151], v[80:83], v[208:211]\n\t"
      "v_mfma_f32_16x16x32_bf16 v[208:211], v[152:155], v[96:99], v[208:211]\n\t"
      "v_mfma_f32_16x16x32_bf16 v[192:195], v[128:131], v[4:7], 0\n\t"
      "v_mfma_f32_16x16x32_bf16 v[192:195], v[132:135], v[20:23], v[192:195]\n\t"
      "v_mfma_f32_16x16x32_bf16 v[192:195], v[136:139], v[36:39], v[192:195]\n\t"
      "s_nop 7\n\t"
      "s_branch LA46B_%=\n\t"
      : "+{s[0:1]}"(ka), "+{v0}"(tid)
      : "{s2}"(blockIdx.x)
      : "memory", "vcc", "scc", "m0",
        "v1", "v2", "v3", "v4", "v5", "v6", "v7", "v8", "v9", "v10", "v11", "v12", "v13", "v14", "v15", "v16", "v17",
        "v18", "v19", "v20", "v21", "v22", "v23", "v24", "v25", "v26", "v27", "v28", "v29", "v30", "v31", "v32", "v33",
        "v34", "v35", "v36", "v37", "v38", "v39", "v40", "v41", "v42", "v43", "v44", "v45", "v46", "v47", "v48", "v49",
        "v50", "v51", "v52", "v53", "v54", "v55", "v56", "v57", "v58", "v59", "v60", "v61", "v62", "v63", "v64", "v65",
        "v66", "v67", "v68", "v69", "v70", "v71", "v72", "v73", "v74", "v75", "v76", "v77", "v78", "v79", "v80", "v81",
        "v82", "v83", "v84", "v85", "v86", "v87", "v88", "v89", "v90", "v91", "v92", "v93", "v94", "v95", "v96", "v97",
        "v98", "v99", "v100", "v101", "v102", "v103", "v104", "v105", "v106", "v107", "v108", "v109", "v110", "v111",
        "v112", "v113", "v114", "v115", "v116", "v117", "v118", "v119", "v120", "v121", "v122", "v123", "v124", "v125",
        "v126", "v127", "v128", "v129", "v130", "v131", "v132", "v133", "v134", "v135", "v136", "v137", "v138", "v139",
        "v140", "v141", "v142", "v143", "v144", "v145", "v146", "v147", "v148", "v149", "v150", "v151", "v152", "v153",
        "v154", "v155", "v156", "v157", "v158", "v159", "v160", "v161", "v162", "v163", "v164", "v165", "v166", "v167",
        "v168", "v169", "v170", "v171", "v172", "v173", "v174", "v175", "v176", "v177", "v178", "v179", "v180", "v181",
        "v182", "v183", "v184", "v185", "v186", "v187", "v188", "v189", "v190", "v191", "v192", "v193", "v194", "v195",
        "v196", "v197", "v198", "v199", "v200", "v201", "v202", "v203", "v204", "v205", "v206", "v207", "v208", "v209",
        "v210", "v211", "v212", "v213", "v214", "v215", "v216", "v217", "v218", "v219", "v220", "v221", "v222", "v223",
        "v224", "v225", "v226", "v227", "v228", "v229", "v230", "v231", "v232", "v233", "v234", "v235", "v236", "v237",
        "v238", "v239", "v240", "v241", "v242", "v243", "v244", "v245", "v246", "v247", "v248", "v249", "v250", "v251",
        "v252", "v253", "v254", "v255", "s3", "s4", "s5", "s6", "s7", "s8", "s9", "s10", "s11", "s12", "s13", "s14",
        "s15", "s16", "s17", "s18", "s19", "s20", "s21", "s22", "s23", "s24", "s25", "s26", "s27", "s28", "s29", "s30",
        "s31", "s32", "s33", "s34", "s35", "s36", "s37", "s38", "s39", "s40", "s41", "s42", "s43", "s44", "s45", "s46",
        "s47", "s48", "s49", "s50", "s51", "s52", "s53", "s54", "s55", "s56", "s57", "s58", "s59", "s60", "s61", "s62",
        "s63", "s64", "s65", "s66", "s67", "s68", "s69", "s70", "s71", "s72", "s73", "s74", "s75", "s76", "s77", "s78",
        "s79", "s80", "s81", "s82", "s83"
  );
#endif
}

extern "C" void kernel_launch(void* const* d_in, const int* in_sizes, int n_in, void* d_out, int out_size,
                              void* d_ws, size_t ws_size, hipStream_t stream) {
  (void)hipFuncSetAttribute((const void*)fused_block, hipFuncAttributeMaxDynamicSharedMemorySize, 163840);
  fused_block<<<256, 512, 163840, stream>>>((const u16*)d_in[0], (const u16*)d_in[1], (const u16*)d_in[2],
                                            (const u16*)d_in[4], (const u16*)d_in[5], (const u16*)d_in[6],
                                            (const u16*)d_in[7], (u16*)d_out, (char*)d_ws);
}
```

```cpp
#include <hip/hip_runtime.h>
#include <stdint.h>
typedef unsigned short u16;
__global__ __launch_bounds__(512) void fused_block(const u16* x, const u16* wqkv, const u16* wo, const u16* w1, const u16* b1,
                                                   const u16* w2, const u16* b2, u16* out, char* ws) {
#if defined(__HIP_DEVICE_COMPILE__)
  unsigned long long ka = (unsigned long long)__builtin_amdgcn_kernarg_segment_ptr();
  unsigned tid = threadIdx.x;
  asm volatile(
      "s_load_dwordx16 s[4:19], s[0:1], 0x0\n\t"
      "s_load_dwordx2 s[20:21], s[0:1], 0x40\n\t"
      "v_lshrrev_b32_e32 v199, 6, v0\n\t"
      "v_and_b32_e32 v192, 63, v0\n\t"
      "v_readfirstlane_b32 s24, v199\n\t"
      "s_getpc_b64 s[58:59]\n\t"
      "s_lshr_b32 s34, s2, 3\n\t"
      "v_min_u32_e32 v200, 8, v0\n\t"
      "v_lshl_add_u32 v200, v200, 5, s34\n\t"
      "s_mov_b32 m0, 64512\n\t"
      "v_min_u32_e32 v200, 267, v200\n\t"
      "v_lshlrev_b32_e32 v200, 7, v200\n\t"
      "s_cmp_lg_u32 s24, 0\n\t"
      "s_cbranch_scc1 NTC_%=\n\t"
      "global_load_lds_dword v200, s[58:59]\n\t"
      "NTC_%=:\n\t"
      "s_and_b32 s34, s2, 7\n\t"
      "s_lshr_b32 s35, s2, 6\n\t"
      "s_lshl2_add_u32 s25, s34, s35\n\t"
      "s_bfe_u32 s26, s2, 196611\n\t"
      "s_and_b32 s34, s25, 30\n\t"
      "s_lshl_b32 s34, s34, 6\n\t"
      "s_and_b32 s35, s25, 1\n\t"
      "s_add_u32 s38, s34, s35\n\t"
      "v_lshrrev_b32_e32 v197, 3, v192\n\t"
      "v_and_b32_e32 v198, 7, v192\n\t"
      "s_lshr_b32 s3, s2, 1\n\t"
      "s_and_b32 s3, s3, 0\n\t"
      "s_xor_b32 s3, s3, s24\n\t"
      "s_lshl4_add_u32 s34, s3, s38\n\t"
      "s_mul_i32 s34, s34, 896\n\t"
      "s_movk_i32 s35, 1792\n\t"
      "v_lshl_add_u32 v199, v198, 4, s34\n\t"
      "v_mad_u32_u24 v120, v197, s35, v199\n\t"
      "s_min_u32 s74, s26, 6\n\t"
      "s_lshl_b32 s34, s74, 7\n\t"
      "s_add_u32 s35, s34, 768\n\t"
      "s_sub_u32 s36, s35, 896\n\t"
      "s_min_u32 s35, s35, s36\n\t"
      "v_add_u32_e32 v126, s35, v120\n\t"
      "s_add_u32 s35, s34, 640\n\t"
      "s_sub_u32 s36, s35, 896\n\t"
      "s_min_u32 s35, s35, s36\n\t"
      "v_add_u32_e32 v125, s35, v120\n\t"
      "s_add_u32 s35, s34, 512\n\t"
      "s_sub_u32 s36, s35, 896\n\t"
      "s_min_u32 s35, s35, s36\n\t"
      "v_add_u32_e32 v124, s35, v120\n\t"
      "s_add_u32 s35, s34, 384\n\t"
      "s_sub_u32 s36, s35, 896\n\t"
      "s_min_u32 s35, s35, s36\n\t"
      "v_add_u32_e32 v123, s35, v120\n\t"
      "s_add_u32 s35, s34, 256\n\t"
      "s_sub_u32 s36, s35, 896\n\t"
      "s_min_u32 s35, s35, s36\n\t"
      "v_add_u32_e32 v122, s35, v120\n\t"
      "s_add_u32 s35, s34, 128\n\t"
      "s_sub_u32 s36, s35, 896\n\t"
      "s_min_u32 s35, s35, s36\n\t"
      "v_add_u32_e32 v121, s35, v120\n\t"
      "s_add_u32 s35, s34, 0\n\t"
      "s_sub_u32 s36, s35, 896\n\t"
      "s_min_u32 s35, s35, s36\n\t"
      "v_add_u32_e32 v120, s35, v120\n\t"
      "s_waitcnt lgkmcnt(0)\n\t"
      "global_load_dwordx4 v[0:3], v120, s[4:5]\n\t"
      "global_load_dwordx4 v[4:7], v121, s[4:5]\n\t"
      "global_load_dwordx4 v[8:11], v122, s[4:5]\n\t"
      "global_load_dwordx4 v[12:15], v123, s[4:5]\n\t"
      "global_load_dwordx4 v[16:19], v124, s[4:5]\n\t"
      "global_load_dwordx4 v[20:23], v125, s[4:5]\n\t"
      "s_cmp_lt_u32 s25, 2\n\t"
      "s_cselect_b32 s34, 0, 16\n\t"
      "s_sub_u32 s34, s38, s34\n\t"
      "s_lshl1_add_u32 s34, s24, s34\n\t"
      "s_mul_i32 s34, s34, 896\n\t"
      "s_and_b32 s33, s25, 3\n\t"
      "s_lshl_b32 s33, s33, 1\n\t"
      "v_min_u32_e32 v200, 55, v192\n\t"
      "s_lshl_b32 s35, s33, 3\n\t"
      "v_add_u32_e32 v201, s35, v200\n\t"
      "v_subrev_u32_e32 v202, 56, v201\n\t"
      "v_min_u32_e32 v201, v201, v202\n\t"
      "v_lshl_add_u32 v127, v201, 4, s34\n\t"
      "s_min_u32 s28, s26, 6\n\t"
      "s_and_b32 s39, s24, 3\n\t"
      "s_lshl_b32 s39, s39, 12\n\t"
      "s_cmp_lt_u32 s24, 4\n\t"
      "s_cselect_b32 s27, 4, 0\n\t"
      "v_lshrrev_b32_e32 v193, 4, v192\n\t"
      "v_and_b32_e32 v194, 15, v192\n\t"
      "v_bfe_u32 v195, v192, 2, 2\n\t"
      "v_and_b32_e32 v196, 3, v192\n\t"
      "v_lshlrev_b32_e32 v244, 4, v192\n\t"
      "s_lshr_b32 s34, s3, 1\n\t"
      "s_mul_i32 s34, s34, 14336\n\t"
      "s_and_b32 s35, s3, 1\n\t"
      "s_lshl_b32 s35, s35, 7\n\t"
      "s_add_u32 s34, s34, s35\n\t"
      "v_xor_b32_e32 v199, v197, v198\n\t"
      "v_lshl_add_u32 v199, v199, 4, s34\n\t"
      "v_lshl_add_u32 v199, v198, 8, v199\n\t"
      "s_sub_u32 s35, s74, s33\n\t"
      "s_cselect_b32 s36, 7, 0\n\t"
      "s_add_u32 s35, s35, s36\n\t"
      "s_lshl_b32 s35, s35, 11\n\t"
      "s_add_u32 s34, s35, 0\n\t"
      "s_sub_u32 s36, s34, 14336\n\t"
      "s_min_u32 s34, s34, s36\n\t"
      "v_add_u32_e32 v112, s34, v199\n\t"
      "s_add_u32 s34, s35, 2048\n\t"
      "s_sub_u32 s36, s34, 14336\n\t"
      "s_min_u32 s34, s34, s36\n\t"
      "v_add_u32_e32 v113, s34, v199\n\t"
      "s_add_u32 s34, s35, 4096\n\t"
      "s_sub_u32 s36, s34, 14336\n\t"
      "s_min_u32 s34, s34, s36\n\t"
      "v_add_u32_e32 v114, s34, v199\n\t"
      "s_add_u32 s34, s35, 6144\n\t"
      "s_sub_u32 s36, s34, 14336\n\t"
      "s_min_u32 s34, s34, s36\n\t"
      "v_add_u32_e32 v115, s34, v199\n\t"
      "s_add_u32 s34, s35, 8192\n\t"
      "s_sub_u32 s36, s34, 14336\n\t"
      "s_min_u32 s34, s34, s36\n\t"
      "v_add_u32_e32 v116, s34, v199\n\t"
      "s_add_u32 s34, s35, 10240\n\t"
      "s_sub_u32 s36, s34, 14336\n\t"
      "s_min_u32 s34, s34, s36\n\t"
      "v_add_u32_e32 v117, s34, v199\n\t"
      "s_add_u32 s34, s35, 12288\n\t"
      "s_sub_u32 s36, s34, 14336\n\t"
      "s_min_u32 s34, s34, s36\n\t"
      "v_add_u32_e32 v118, s34, v199\n\t"
      "v_min_u32_e32 v200, 55, v192\n\t"
      "v_lshrrev_b32_e32 v201, 2, v200\n\t"
      "v_lshlrev_b32_e32 v119, 10, v201\n\t"
      "v_cmp_lt_u32_e32 vcc, 6, v201\n\t"
      "v_mov_b32_e32 v202, 57344\n\t"
      "v_mov_b32_e32 v201, 50688\n\t"
      "v_cndmask_b32_e32 v202, v202, v201, vcc\n\t"
      "v_and_b32_e32 v201, 3, v200\n\t"
      "v_lshl_add_u32 v202, v201, 7, v202\n\t"
      "v_and_b32_e32 v200, 7, v200\n\t"
      "v_xor_b32_e32 v200, s24, v200\n\t"
      "v_lshl_add_u32 v202, v200, 4, v202\n\t"
      "v_add_u32_e32 v119, v119, v202\n\t"
      "s_bitcmp1_b32 s27, 2\n\t"
      "s_cbranch_scc1 LSET_%=\n\t"
      "v_xor_b32_e32 v199, v194, v193\n\t"
      "v_lshlrev_b32_e32 v199, 4, v199\n\t"
      "v_lshl_or_b32 v200, v193, 8, v199\n\t"
      "s_and_b32 s34, s24, 1\n\t"
      "s_mul_i32 s35, s34, 28672\n\t"
      "v_add_u32_e32 v160, s35, v200\n\t"
      "v_xor_b32_e32 v161, 64, v160\n\t"
      "v_and_b32_e32 v199, 0x70, v199\n\t"
      "v_lshl_or_b32 v199, v193, 7, v199\n\t"
      "v_add_u32_e32 v199, 57344, v199\n\t"
      "v_add_u32_e32 v200, 14336, v200\n\t"
      "s_cmp_eq_u32 s34, 1\n\t"
      "s_cselect_b64 vcc, -1, 0\n\t"
      "s_cselect_b32 s35, 0, 6656\n\t"
      "v_cndmask_b32_e32 v162, v199, v200, vcc\n\t"
      "v_xor_b32_e32 v163, 64, v162\n\t"
      "v_subrev_u32_e32 v164, s35, v162\n\t"
      "v_subrev_u32_e32 v165, s35, v163\n\t"
      "s_bfe_u32 s35, s24, 65537\n\t"
      "s_mul_i32 s35, s35, 7\n\t"
      "v_and_b32_e32 v201, 24, v192\n\t"
      "v_lshlrev_b32_e32 v201, 2, v201\n\t"
      "v_lshlrev_b32_e32 v202, 7, v195\n\t"
      "v_lshl_or_b32 v202, v193, 12, v202\n\t"
      "v_add_u32_e32 v202, 65536, v202\n\t"
      "v_add_lshl_u32 v199, v196, s35, 3\n\t"
      "v_xor_b32_e32 v199, v199, v201\n\t"
      "v_add_u32_e32 v166, v199, v202\n\t"
      "v_min_u32_e32 v199, 2, v196\n\t"
      "s_add_u32 s35, s35, 4\n\t"
      "v_add_lshl_u32 v199, v199, s35, 3\n\t"
      "v_xor_b32_e32 v199, v199, v201\n\t"
      "v_add_u32_e32 v167, v199, v202\n\t"
      "v_add_u32_e32 v168, 0x10000, v166\n\t"
      "v_add_u32_e32 v169, 0x10000, v167\n\t"
      "v_lshlrev_b32_e32 v199, 11, v193\n\t"
      "v_sub_u32_e32 v170, v166, v199\n\t"
      "v_sub_u32_e32 v171, v167, v199\n\t"
      "s_branch NORM_%=\n\t"
      "LSET_%=:\n\t"
      "s_add_u32 s60, s20, 16777216\n\t"
      "s_addc_u32 s61, s21, 0\n\t"
      "s_load_dwordx2 s[22:23], s[60:61], 0x0\n\t"
      "s_lshl_b32 s34, s24, 3\n\t"
      "v_add_u32_e32 v199, s34, v197\n\t"
      "s_and_b32 s34, s24, 1\n\t"
      "s_lshl_b32 s34, s34, 2\n\t"
      "v_and_b32_e32 v200, 2, v197\n\t"
      "v_or_b32_e32 v200, s34, v200\n\t"
      "v_xor_b32_e32 v200, v200, v198\n\t"
      "v_min_u32_e32 v200, 6, v200\n\t"
      "v_lshlrev_b32_e32 v200, 4, v200\n\t"
      "s_movk_i32 s34, 2688\n\t"
      "v_mad_u32_u24 v149, v199, s34, v200\n\t"
      "v_add_u32_e32 v148, 128, v149\n\t"
      "v_add_u32_e32 v150, 84096, v149\n\t"
      "v_add_u32_e32 v151, 83968, v149\n\t"
      "v_add_u32_e32 v152, 85120, v149\n\t"
      "s_sub_u32 s6, s6, 128\n\t"
      "s_subb_u32 s7, s7, 0\n\t"
      "s_mul_i32 s37, s26, 112\n\t"
      "s_mov_b32 s81, s33\n\t"
      "s_mov_b32 s82, s33\n\t"
      "s_lshr_b32 s35, s2, 6\n\t"
      "s_lshl2_add_u32 s31, s26, s35\n\t"
      "s_and_b32 s35, s2, 7\n\t"
      "s_mul_i32 s34, s35, 14\n\t"
      "s_lshl4_add_u32 s35, s35, -14\n\t"
      "s_cmp_lt_u32 s31, 14\n\t"
      "s_cselect_b32 s34, s34, s35\n\t"
      "s_cselect_b32 s36, 1, 0\n\t"
      "s_or_b32 s27, s27, s36\n\t"
      "s_add_u32 s34, s34, s31\n\t"
      "s_lshr_b32 s29, s34, 2\n\t"
      "s_and_b32 s30, s34, 3\n\t"
      "s_mul_i32 s30, s30, 7\n\t"
      "v_and_b32_e32 v199, 60, v192\n\t"
      "v_and_b32_e32 v200, 24, v192\n\t"
      "v_lshlrev_b32_e32 v202, 3, v196\n\t"
      "v_lshl_or_b32 v202, v200, 2, v202\n\t"
      "v_lshl_or_b32 v202, v195, 7, v202\n\t"
      "v_lshl_or_b32 v202, v193, 10, v202\n\t"
      "v_add_u32_e32 v224, s39, v202\n\t"
      "v_xor_b32_e32 v225, 32, v224\n\t"
      "v_xor_b32_e32 v226, 64, v224\n\t"
      "v_xor_b32_e32 v227, 96, v224\n\t"
      "s_lshl_b32 s34, s24, 12\n\t"
      "s_add_u32 s34, s34, 98304\n\t"
      "v_add_u32_e32 v245, s34, v244\n\t"
      "v_lshlrev_b32_e32 v199, 2, v199\n\t"
      "s_lshl_b32 s34, s24, 8\n\t"
      "v_lshl_add_u32 v247, v192, 2, s34\n\t"
      "v_and_b32_e32 v200, 2, v192\n\t"
      "v_mul_u32_u24_e32 v202, 576, v200\n\t"
      "v_xor_b32_e32 v199, v202, v199\n\t"
      "v_and_b32_e32 v202, 1, v192\n\t"
      "v_lshl_or_b32 v199, v202, 3, v199\n\t"
      "s_and_b32 s35, s34, 0x400\n\t"
      "s_add_u32 s34, s34, s35\n\t"
      "s_add_u32 s34, s34, 98304\n\t"
      "v_add_u32_e32 v246, s34, v199\n\t"
      "v_lshlrev_b32_e32 v199, 2, v202\n\t"
      "v_lshl_or_b32 v199, v200, 4, v199\n\t"
      "s_min_u32 s34, s29, 27\n\t"
      "s_lshl_b32 s34, s34, 7\n\t"
      "s_lshr_b32 s35, s24, 2\n\t"
      "s_lshl_b32 s35, s35, 6\n\t"
      "s_add_u32 s34, s34, s35\n\t"
      "s_lshr_b32 s35, s39, 9\n\t"
      "s_add_u32 s34, s34, s35\n\t"
      "v_add_u32_e32 v248, s34, v199\n\t"
      "s_add_u32 s48, s20, 8388608\n\t"
      "s_addc_u32 s49, s21, 0\n\t"
      "v_and_b32_e32 v199, 2, v197\n\t"
      "v_xor_b32_e32 v199, v199, v198\n\t"
      "v_lshlrev_b32_e32 v199, 4, v199\n\t"
      "s_movk_i32 s34, 7168\n\t"
      "v_mad_u32_u24 v144, v197, s34, v199\n\t"
      "v_xor_b32_e32 v199, 64, v144\n\t"
      "v_add_u32_e32 v145, 56320, v199\n\t"
      "v_add_u32_e32 v146, 112640, v144\n\t"
      "v_add_u32_e32 v147, 168960, v199\n\t"
      "v_and_b32_e32 v199, 6, v197\n\t"
      "v_xor_b32_e32 v199, v199, v198\n\t"
      "v_lshlrev_b32_e32 v199, 4, v199\n\t"
      "s_movk_i32 s35, 896\n\t"
      "v_mad_u32_u24 v154, v197, s35, v199\n\t"
      "v_add_u32_e32 v155, 6144, v154\n\t"
      "v_add_u32_e32 v156, 12288, v154\n\t"
      "v_add_u32_e32 v157, 18432, v154\n\t"
      "s_mul_i32 s34, s24, 917504\n\t"
      "s_lshl_b32 s35, s29, 7\n\t"
      "s_add_u32 s34, s34, s35\n\t"
      "s_add_u32 s52, s10, s34\n\t"
      "s_addc_u32 s53, s11, 0\n\t"
      "s_lshl_b32 s34, s28, 7\n\t"
      "s_add_u32 s56, s8, s34\n\t"
      "s_addc_u32 s57, s9, 0\n\t"
      "s_mov_b32 s34, 0\n\t"
      "s_and_b32 s35, s34, 1\n\t"
      "s_and_b32 s34, s34, 2\n\t"
      "s_lshl_b32 s34, s34, 6\n\t"
      "s_add_u32 s34, s34, s35\n\t"
      "s_mul_i32 s76, s34, 3584\n\t"
      "s_mov_b32 s34, 1\n\t"
      "s_and_b32 s35, s34, 1\n\t"
      "s_and_b32 s34, s34, 2\n\t"
      "s_lshl_b32 s34, s34, 6\n\t"
      "s_add_u32 s34, s34, s35\n\t"
      "s_mul_i32 s77, s34, 3584\n\t"
      "s_mov_b32 s34, 2\n\t"
      "s_and_b32 s35, s34, 1\n\t"
      "s_and_b32 s34, s34, 2\n\t"
      "s_lshl_b32 s34, s34, 6\n\t"
      "s_add_u32 s34, s34, s35\n\t"
      "s_mul_i32 s78, s34, 3584\n\t"
      "s_mov_b32 s34, 3\n\t"
      "s_and_b32 s35, s34, 1\n\t"
      "s_and_b32 s34, s34, 2\n\t"
      "s_lshl_b32 s34, s34, 6\n\t"
      "s_add_u32 s34, s34, s35\n\t"
      "s_mul_i32 s79, s34, 3584\n\t"
      "NORM_%=:\n\t"
      "global_load_dwordx4 v[24:27], v126, s[4:5]\n\t"
      "global_load_dwordx4 v[28:31], v127, s[4:5]\n\t"
      "v_mov_b32_e32 v199, 0x3f803f80\n\t"
      "s_waitcnt vmcnt(7)\n\t"
      "v_dot2_f32_bf16 v88, v0, v199, 0\n\t"
      "v_dot2_f32_bf16 v92, v0, v0, 0\n\t"
      "v_lshlrev_b32_e32 v32, 16, v0\n\t"
      "v_and_b32_e32 v33, 0xffff0000, v0\n\t"
      "v_dot2_f32_bf16 v89, v1, v199, 0\n\t"
      "v_dot2_f32_bf16 v93, v1, v1, 0\n\t"
      "v_lshlrev_b32_e32 v34, 16, v1\n\t"
      "v_and_b32_e32 v35, 0xffff0000, v1\n\t"
      "v_dot2_f32_bf16 v90, v2, v199, 0\n\t"
      "v_dot2_f32_bf16 v94, v2, v2, 0\n\t"
      "v_lshlrev_b32_e32 v36, 16, v2\n\t"
      "v_and_b32_e32 v37, 0xffff0000, v2\n\t"
      "v_dot2_f32_bf16 v91, v3, v199, 0\n\t"
      "v_dot2_f32_bf16 v95, v3, v3, 0\n\t"
      "v_lshlrev_b32_e32 v38, 16, v3\n\t"
      "v_and_b32_e32 v39, 0xffff0000, v3\n\t"
      "s_waitcnt vmcnt(6)\n\t"
      "v_dot2_f32_bf16 v88, v4, v199, v88\n\t"
      "v_dot2_f32_bf16 v92, v4, v4, v92\n\t"
      "v_lshlrev_b32_e32 v40, 16, v4\n\t"
      "v_and_b32_e32 v41, 0xffff0000, v4\n\t"
      "v_dot2_f32_bf16 v89, v5, v199, v89\n\t"
      "v_dot2_f32_bf16 v93, v5, v5, v93\n\t"
      "v_lshlrev_b32_e32 v42, 16, v5\n\t"
      "v_and_b32_e32 v43, 0xffff0000, v5\n\t"
      "v_dot2_f32_bf16 v90, v6, v199, v90\n\t"
      "v_dot2_f32_bf16 v94, v6, v6, v94\n\t"
      "v_lshlrev_b32_e32 v44, 16, v6\n\t"
      "v_and_b32_e32 v45, 0xffff0000, v6\n\t"
      "v_dot2_f32_bf16 v91, v7, v199, v91\n\t"
      "v_dot2_f32_bf16 v95, v7, v7, v95\n\t"
      "v_lshlrev_b32_e32 v46, 16, v7\n\t"
      "v_and_b32_e32 v47, 0xffff0000, v7\n\t"
      "s_waitcnt vmcnt(5)\n\t"
      "v_dot2_f32_bf16 v88, v8, v199, v88\n\t"
      "v_dot2_f32_bf16 v92, v8, v8, v92\n\t"
      "v_lshlrev_b32_e32 v48, 16, v8\n\t"
      "v_and_b32_e32 v49, 0xffff0000, v8\n\t"
      "v_dot2_f32_bf16 v89, v9, v199, v89\n\t"
      "v_dot2_f32_bf16 v93, v9, v9, v93\n\t"
      "v_lshlrev_b32_e32 v50, 16, v9\n\t"
      "v_and_b32_e32 v51, 0xffff0000, v9\n\t"
      "v_dot2_f32_bf16 v90, v10, v199, v90\n\t"
      "v_dot2_f32_bf16 v94, v10, v10, v94\n\t"
      "v_lshlrev_b32_e32 v52, 16, v10\n\t"
      "v_and_b32_e32 v53, 0xffff0000, v10\n\t"
      "v_dot2_f32_bf16 v91, v11, v199, v91\n\t"
      "v_dot2_f32_bf16 v95, v11, v11, v95\n\t"
      "v_lshlrev_b32_e32 v54, 16, v11\n\t"
      "v_and_b32_e32 v55, 0xffff0000, v11\n\t"
      "s_waitcnt vmcnt(4)\n\t"
      "v_dot2_f32_bf16 v88, v12, v199, v88\n\t"
      "v_dot2_f32_bf16 v92, v12, v12, v92\n\t"
      "v_lshlrev_b32_e32 v56, 16, v12\n\t"
      "v_and_b32_e32 v57, 0xffff0000, v12\n\t"
      "v_dot2_f32_bf16 v89, v13, v199, v89\n\t"
      "v_dot2_f32_bf16 v93, v13, v13, v93\n\t"
      "v_lshlrev_b32_e32 v58, 16, v13\n\t"
      "v_and_b32_e32 v59, 0xffff0000, v13\n\t"
      "v_dot2_f32_bf16 v90, v14, v199, v90\n\t"
      "v_dot2_f32_bf16 v94, v14, v14, v94\n\t"
      "v_lshlrev_b32_e32 v60, 16, v14\n\t"
      "v_and_b32_e32 v61, 0xffff0000, v14\n\t"
      "v_dot2_f32_bf16 v91, v15, v199, v91\n\t"
      "v_dot2_f32_bf16 v95, v15, v15, v95\n\t"
      "v_lshlrev_b32_e32 v62, 16, v15\n\t"
      "v_and_b32_e32 v63, 0xffff0000, v15\n\t"
      "s_waitcnt vmcnt(3)\n\t"
      "v_dot2_f32_bf16 v88, v16, v199, v88\n\t"
      "v_dot2_f32_bf16 v92, v16, v16, v92\n\t"
      "v_lshlrev_b32_e32 v64, 16, v16\n\t"
      "v_and_b32_e32 v65, 0xffff0000, v16\n\t"
      "v_dot2_f32_bf16 v89, v17, v199, v89\n\t"
      "v_dot2_f32_bf16 v93, v17, v17, v93\n\t"
      "v_lshlrev_b32_e32 v66, 16, v17\n\t"
      "v_and_b32_e32 v67, 0xffff0000, v17\n\t"
      "v_dot2_f32_bf16 v90, v18, v199, v90\n\t"
      "v_dot2_f32_bf16 v94, v18, v18, v94\n\t"
      "v_lshlrev_b32_e32 v68, 16, v18\n\t"
      "v_and_b32_e32 v69, 0xffff0000, v18\n\t"
      "v_dot2_f32_bf16 v91, v19, v199, v91\n\t"
      "v_dot2_f32_bf16 v95, v19, v19, v95\n\t"
      "v_lshlrev_b32_e32 v70, 16, v19\n\t"
      "v_and_b32_e32 v71, 0xffff0000, v19\n\t"
      "s_waitcnt vmcnt(2)\n\t"
      "v_dot2_f32_bf16 v88, v20, v199, v88\n\t"
      "v_dot2_f32_bf16 v92, v20, v20, v92\n\t"
      "v_lshlrev_b32_e32 v72, 16, v20\n\t"
      "v_and_b32_e32 v73, 0xffff0000, v20\n\t"
      "v_dot2_f32_bf16 v89, v21, v199, v89\n\t"
      "v_dot2_f32_bf16 v93, v21, v21, v93\n\t"
      "v_lshlrev_b32_e32 v74, 16, v21\n\t"
      "v_and_b32_e32 v75, 0xffff0000, v21\n\t"
      "v_dot2_f32_bf16 v90, v22, v199, v90\n\t"
      "v_dot2_f32_bf16 v94, v22, v22, v94\n\t"
      "v_lshlrev_b32_e32 v76, 16, v22\n\t"
      "v_and_b32_e32 v77, 0xffff0000, v22\n\t"
      "v_dot2_f32_bf16 v91, v23, v199, v91\n\t"
      "v_dot2_f32_bf16 v95, v23, v23, v95\n\t"
      "v_lshlrev_b32_e32 v78, 16, v23\n\t"
      "v_and_b32_e32 v79, 0xffff0000, v23\n\t"
      "s_waitcnt vmcnt(1)\n\t"
      "v_dot2_f32_bf16 v88, v24, v199, v88\n\t"
      "v_dot2_f32_bf16 v92, v24, v24, v92\n\t"
      "v_lshlrev_b32_e32 v80, 16, v24\n\t"
      "v_and_b32_e32 v81, 0xffff0000, v24\n\t"
      "v_dot2_f32_bf16 v89, v25, v199, v89\n\t"
      "v_dot2_f32_bf16 v93, v25, v25, v93\n\t"
      "v_lshlrev_b32_e32 v82, 16, v25\n\t"
      "v_and_b32_e32 v83, 0xffff0000, v25\n\t"
      "v_dot2_f32_bf16 v90, v26, v199, v90\n\t"
      "v_dot2_f32_bf16 v94, v26, v26, v94\n\t"
      "v_lshlrev_b32_e32 v84, 16, v26\n\t"
      "v_and_b32_e32 v85, 0xffff0000, v26\n\t"
      "v_dot2_f32_bf16 v91, v27, v199, v91\n\t"
      "v_dot2_f32_bf16 v95, v27, v27, v95\n\t"
      "v_lshlrev_b32_e32 v86, 16, v27\n\t"
      "v_and_b32_e32 v87, 0xffff0000, v27\n\t"
      "s_waitcnt vmcnt(0)\n\t"
      "s_bitcmp0_b32 s27, 2\n\t"
      "s_cbranch_scc1 SK1_%=\n\t"
      "s_mul_i32 s72, s81, 172032\n\t"
      "s_add_u32 s72, s72, s37\n\t"
      "s_add_u32 s40, s6, s72\n\t"
      "s_addc_u32 s41, s7, 0\n\t"
      "s_add_u32 s81, s81, 1\n\t"
      "s_cmp_eq_u32 s81, 7\n\t"
      "s_cselect_b32 s81, 0, s81\n\t"
      "s_add_u32 m0, s39, 131072\n\t"
      "s_nop 0\n\t"
      "global_load_lds_dwordx4 v148, s[40:41]\n\t"
      "global_load_lds_dwordx4 v149, s[40:41] offset:1024\n\t"
      "global_load_lds_dwordx4 v150, s[40:41] offset:2048\n\t"
      "global_load_lds_dwordx4 v151, s[40:41] offset:3072\n\t"
      "SK1_%=:\n\t"
      "v_dot2_f32_bf16 v96, v28, v199, 0\n\t"
      "v_dot2_f32_bf16 v97, v28, v28, 0\n\t"
      "v_dot2_f32_bf16 v96, v29, v199, v96\n\t"
      "v_dot2_f32_bf16 v97, v29, v29, v97\n\t"
      "v_dot2_f32_bf16 v96, v30, v199, v96\n\t"
      "v_dot2_f32_bf16 v97, v30, v30, v97\n\t"
      "v_dot2_f32_bf16 v96, v31, v199, v96\n\t"
      "v_dot2_f32_bf16 v97, v31, v31, v97\n\t"
      "v_cmp_gt_u32_e32 vcc, 56, v192\n\t"
      "v_pk_add_f32 v[88:89], v[88:89], v[90:91]\n\t"
      "v_pk_add_f32 v[92:93], v[92:93], v[94:95]\n\t"
      "v_cndmask_b32_e32 v96, 0, v96, vcc\n\t"
      "v_cndmask_b32_e32 v97, 0, v97, vcc\n\t"
      "v_add_f32_e32 v88, v88, v89\n\t"
      "v_add_f32_e32 v92, v92, v93\n\t"
      "v_add_f32_dpp v96, v96, v96 quad_perm:[1,0,3,2] row_mask:0xf bank_mask:0xf\n\t"
      "v_add_f32_dpp v97, v97, v97 quad_perm:[1,0,3,2] row_mask:0xf bank_mask:0xf\n\t"
      "v_add_f32_dpp v88, v88, v88 quad_perm:[1,0,3,2] row_mask:0xf bank_mask:0xf\n\t"
      "v_add_f32_dpp v92, v92, v92 quad_perm:[1,0,3,2] row_mask:0xf bank_mask:0xf\n\t"
      "v_add_f32_dpp v96, v96, v96 quad_perm:[2,3,0,1] row_mask:0xf bank_mask:0xf\n\t"
      "v_add_f32_dpp v97, v97, v97 quad_perm:[2,3,0,1] row_mask:0xf bank_mask:0xf\n\t"
      "v_add_f32_dpp v88, v88, v88 quad_perm:[2,3,0,1] row_mask:0xf bank_mask:0xf\n\t"
      "v_add_f32_dpp v92, v92, v92 quad_perm:[2,3,0,1] row_mask:0xf bank_mask:0xf\n\t"
      "v_add_f32_dpp v96, v96, v96 row_half_mirror row_mask:0xf bank_mask:0xf\n\t"
      "v_add_f32_dpp v97, v97, v97 row_half_mirror row_mask:0xf bank_mask:0xf\n\t"
      "v_add_f32_dpp v88, v88, v88 row_half_mirror row_mask:0xf bank_mask:0xf\n\t"
      "v_add_f32_dpp v92, v92, v92 row_half_mirror row_mask:0xf bank_mask:0xf\n\t"
      "v_mul_f32_e32 v88, 0xbb124925, v88\n\t"
      "v_add_f32_dpp v96, v96, v96 row_mirror row_mask:0xf bank_mask:0xf\n\t"
      "v_mul_f32_e32 v92, 0x3b124925, v92\n\t"
      "s_bitcmp0_b32 s27, 2\n\t"
      "s_cbranch_scc1 SK2_%=\n\t"
      "s_mul_i32 s72, s81, 172032\n\t"
      "s_add_u32 s72, s72, s37\n\t"
      "s_add_u32 s40, s6, s72\n\t"
      "s_addc_u32 s41, s7, 0\n\t"
      "s_add_u32 s81, s81, 1\n\t"
      "s_cmp_eq_u32 s81, 7\n\t"
      "s_cselect_b32 s81, 0, s81\n\t"
      "s_add_u32 m0, s39, 147456\n\t"
      "s_nop 0\n\t"
      "global_load_lds_dwordx4 v148, s[40:41]\n\t"
      "global_load_lds_dwordx4 v149, s[40:41] offset:1024\n\t"
      "SK2_%=:\n\t"
      "v_add_f32_dpp v97, v97, v97 row_mirror row_mask:0xf bank_mask:0xf\n\t"
      "v_fma_f32 v92, -v88, v88, v92\n\t"
      "s_nop 0\n\t"
      "v_permlane16_swap_b32_e32 v96, v97\n\t"
      "v_max_f32_e32 v92, 0, v92\n\t"
      "v_add_f32_e32 v96, v96, v97\n\t"
      "v_add_f32_e32 v92, 0x3727c5ac, v92\n\t"
      "v_mov_b32_e32 v97, v96\n\t"
      "v_rsq_f32_e32 v92, v92\n\t"
      "s_nop 0\n\t"
      "v_permlane32_swap_b32_e32 v96, v97\n\t"
      "v_add_f32_e32 v96, v96, v97\n\t"
      "v_mul_f32_e32 v88, v88, v92\n\t"
      "v_mov_b32_e32 v97, v96\n\t"
      "v_pk_fma_f32 v[32:33], v[32:33], v[92:93], v[88:89] op_sel_hi:[1,0,0]\n\t"
      "v_pk_fma_f32 v[34:35], v[34:35], v[92:93], v[88:89] op_sel_hi:[1,0,0]\n\t"
      "v_permlane16_swap_b32_e32 v96, v97\n\t"
      "v_pk_fma_f32 v[36:37], v[36:37], v[92:93], v[88:89] op_sel_hi:[1,0,0]\n\t"
      "v_pk_fma_f32 v[38:39], v[38:39], v[92:93], v[88:89] op_sel_hi:[1,0,0]\n\t"
      "v_mul_f32_e32 v96, 0xbb124925, v96\n\t"
      "v_cvt_pk_bf16_f32 v0, v32, v33\n\t"
      "v_cvt_pk_bf16_f32 v1, v34, v35\n\t"
      "v_cvt_pk_bf16_f32 v2, v36, v37\n\t"
      "v_cvt_pk_bf16_f32 v3, v38, v39\n\t"
      "ds_write_b128 v112, v[0:3]\n\t"
      "v_pk_fma_f32 v[40:41], v[40:41], v[92:93], v[88:89] op_sel_hi:[1,0,0]\n\t"
      "v_pk_fma_f32 v[42:43], v[42:43], v[92:93], v[88:89] op_sel_hi:[1,0,0]\n\t"
      "v_mul_f32_e32 v97, 0x3b124925, v97\n\t"
      "v_pk_fma_f32 v[44:45], v[44:45], v[92:93], v[88:89] op_sel_hi:[1,0,0]\n\t"
      "v_pk_fma_f32 v[46:47], v[46:47], v[92:93], v[88:89] op_sel_hi:[1,0,0]\n\t"
      "v_fma_f32 v97, -v96, v96, v97\n\t"
      "v_cvt_pk_bf16_f32 v4, v40, v41\n\t"
      "s_bitcmp0_b32 s27, 2\n\t"
      "s_cbranch_scc1 SK3_%=\n\t"
      "global_load_lds_dwordx4 v150, s[40:41] offset:2048\n\t"
      "global_load_lds_dwordx4 v151, s[40:41] offset:3072\n\t"
      "SK3_%=:\n\t"
      "v_cvt_pk_bf16_f32 v5, v42, v43\n\t"
      "v_cvt_pk_bf16_f32 v6, v44, v45\n\t"
      "v_cvt_pk_bf16_f32 v7, v46, v47\n\t"
      "ds_write_b128 v113, v[4:7]\n\t"
      "v_pk_fma_f32 v[48:49], v[48:49], v[92:93], v[88:89] op_sel_hi:[1,0,0]\n\t"
      "v_pk_fma_f32 v[50:51], v[50:51], v[92:93], v[88:89] op_sel_hi:[1,0,0]\n\t"
      "v_max_f32_e32 v97, 0, v97\n\t"
      "v_pk_fma_f32 v[52:53], v[52:53], v[92:93], v[88:89] op_sel_hi:[1,0,0]\n\t"
      "v_pk_fma_f32 v[54:55], v[54:55], v[92:93], v[88:89] op_sel_hi:[1,0,0]\n\t"
      "v_add_f32_e32 v97, 0x3727c5ac, v97\n\t"
      "v_cvt_pk_bf16_f32 v8, v48, v49\n\t"
      "v_cvt_pk_bf16_f32 v9, v50, v51\n\t"
      "v_cvt_pk_bf16_f32 v10, v52, v53\n\t"
      "v_cvt_pk_bf16_f32 v11, v54, v55\n\t"
      "ds_write_b128 v114, v[8:11]\n\t"
      "v_pk_fma_f32 v[56:57], v[56:57], v[92:93], v[88:89] op_sel_hi:[1,0,0]\n\t"
      "v_pk_fma_f32 v[58:59], v[58:59], v[92:93], v[88:89] op_sel_hi:[1,0,0]\n\t"
      "v_rsq_f32_e32 v97, v97\n\t"
      "v_pk_fma_f32 v[60:61], v[60:61], v[92:93], v[88:89] op_sel_hi:[1,0,0]\n\t"
      "v_pk_fma_f32 v[62:63], v[62:63], v[92:93], v[88:89] op_sel_hi:[1,0,0]\n\t"
      "v_cvt_pk_bf16_f32 v12, v56, v57\n\t"
      "v_cvt_pk_bf16_f32 v13, v58, v59\n\t"
      "v_cvt_pk_bf16_f32 v14, v60, v61\n\t"
      "v_cvt_pk_bf16_f32 v15, v62, v63\n\t"
      "ds_write_b128 v115, v[12:15]\n\t"
      "v_pk_fma_f32 v[64:65], v[64:65], v[92:93], v[88:89] op_sel_hi:[1,0,0]\n\t"
      "v_pk_fma_f32 v[66:67], v[66:67], v[92:93], v[88:89] op_sel_hi:[1,0,0]\n\t"
      "v_mul_f32_e32 v96, v96, v97\n\t"
      "v_pk_fma_f32 v[68:69], v[68:69], v[92:93], v[88:89] op_sel_hi:[1,0,0]\n\t"
      "v_pk_fma_f32 v[70:71], v[70:71], v[92:93], v[88:89] op_sel_hi:[1,0,0]\n\t"
      "s_bitcmp0_b32 s27, 2\n\t"
      "s_cbranch_scc1 SK4_%=\n\t"
      "s_mul_i32 s72, s82, 172032\n\t"
      "s_add_u32 s72, s72, s37\n\t"
      "s_add_u32 s72, s72, 1792\n\t"
      "s_add_u32 s40, s6, s72\n\t"
      "s_addc_u32 s41, s7, 0\n\t"
      "s_add_u32 s82, s82, 1\n\t"
      "s_cmp_eq_u32 s82, 7\n\t"
      "s_cselect_b32 s82, 0, s82\n\t"
      "s_add_u32 m0, s39, 65536\n\t"
      "s_nop 0\n\t"
      "global_load_lds_dwordx4 v148, s[40:41]\n\t"
      "global_load_lds_dwordx4 v152, s[40:41] offset:1024\n\t"
      "SK4_%=:\n\t"
      "v_cvt_pk_bf16_f32 v16, v64, v65\n\t"
      "v_cvt_pk_bf16_f32 v17, v66, v67\n\t"
      "v_cvt_pk_bf16_f32 v18, v68, v69\n\t"
      "v_cvt_pk_bf16_f32 v19, v70, v71\n\t"
      "ds_write_b128 v116, v[16:19]\n\t"
      "v_pk_fma_f32 v[72:73], v[72:73], v[92:93], v[88:89] op_sel_hi:[1,0,0]\n\t"
      "v_pk_fma_f32 v[74:75], v[74:75], v[92:93], v[88:89] op_sel_hi:[1,0,0]\n\t"
      "v_pk_fma_f32 v[76:77], v[76:77], v[92:93], v[88:89] op_sel_hi:[1,0,0]\n\t"
      "v_pk_fma_f32 v[78:79], v[78:79], v[92:93], v[88:89] op_sel_hi:[1,0,0]\n\t"
      "v_cvt_pk_bf16_f32 v20, v72, v73\n\t"
      "v_cvt_pk_bf16_f32 v21, v74, v75\n\t"
      "v_cvt_pk_bf16_f32 v22, v76, v77\n\t"
      "v_cvt_pk_bf16_f32 v23, v78, v79\n\t"
      "ds_write_b128 v117, v[20:23]\n\t"
      "v_pk_fma_f32 v[80:81], v[80:81], v[92:93], v[88:89] op_sel_hi:[1,0,0]\n\t"
      "v_pk_fma_f32 v[82:83], v[82:83], v[92:93], v[88:89] op_sel_hi:[1,0,0]\n\t"
      "v_pk_fma_f32 v[84:85], v[84:85], v[92:93], v[88:89] op_sel_hi:[1,0,0]\n\t"
      "v_pk_fma_f32 v[86:87], v[86:87], v[92:93], v[88:89] op_sel_hi:[1,0,0]\n\t"
      "v_cvt_pk_bf16_f32 v24, v80, v81\n\t"
      "v_cvt_pk_bf16_f32 v25, v82, v83\n\t"
      "v_cvt_pk_bf16_f32 v26, v84, v85\n\t"
      "v_cvt_pk_bf16_f32 v27, v86, v87\n\t"
      "ds_write_b128 v118, v[24:27]\n\t"
      "v_lshlrev_b32_e32 v32, 16, v28\n\t"
      "v_and_b32_e32 v33, 0xffff0000, v28\n\t"
      "v_lshlrev_b32_e32 v34, 16, v29\n\t"
      "v_and_b32_e32 v35, 0xffff0000, v29\n\t"
      "v_lshlrev_b32_e32 v36, 16, v30\n\t"
      "v_and_b32_e32 v37, 0xffff0000, v30\n\t"
      "v_lshlrev_b32_e32 v38, 16, v31\n\t"
      "s_bitcmp0_b32 s27, 2\n\t"
      "s_cbranch_scc1 SK5_%=\n\t"
      "s_mul_i32 s72, s82, 172032\n\t"
      "s_add_u32 s72, s72, s37\n\t"
      "s_add_u32 s72, s72, 1792\n\t"
      "s_add_u32 s40, s6, s72\n\t"
      "s_addc_u32 s41, s7, 0\n\t"
      "s_add_u32 s82, s82, 1\n\t"
      "s_cmp_eq_u32 s82, 7\n\t"
      "s_cselect_b32 s82, 0, s82\n\t"
      "s_add_u32 m0, s39, 67584\n\t"
      "s_nop 0\n\t"
      "global_load_lds_dwordx4 v148, s[40:41]\n\t"
      "global_load_lds_dwordx4 v152, s[40:41] offset:1024\n\t"
      "SK5_%=:\n\t"
      "v_and_b32_e32 v39, 0xffff0000, v31\n\t"
      "v_pk_fma_f32 v[32:33], v[32:33], v[96:97], v[96:97] op_sel:[0,1,0] op_sel_hi:[1,1,0]\n\t"
      "v_pk_fma_f32 v[34:35], v[34:35], v[96:97], v[96:97] op_sel:[0,1,0] op_sel_hi:[1,1,0]\n\t"
      "v_pk_fma_f32 v[36:37], v[36:37], v[96:97], v[96:97] op_sel:[0,1,0] op_sel_hi:[1,1,0]\n\t"
      "v_pk_fma_f32 v[38:39], v[38:39], v[96:97], v[96:97] op_sel:[0,1,0] op_sel_hi:[1,1,0]\n\t"
      "v_cvt_pk_bf16_f32 v28, v32, v33\n\t"
      "v_cvt_pk_bf16_f32 v29, v34, v35\n\t"
      "v_cvt_pk_bf16_f32 v30, v36, v37\n\t"
      "v_cvt_pk_bf16_f32 v31, v38, v39\n\t"
      "ds_write_b128 v119, v[28:31]\n\t"
      "s_waitcnt lgkmcnt(0)\n\t"
      "s_bitcmp1_b32 s27, 2\n\t"
      "s_cbranch_scc1 LPATH_%=\n\t"
      "s_setprio 2\n\t"
      "s_barrier\n\t"
      "ds_read_b64_tr_b16 v[76:77], v168\n\t"
      "ds_read_b64_tr_b16 v[78:79], v168 offset:512\n\t"
      "ds_read_b128 v[68:71], v160\n\t"
      "ds_read_b128 v[72:75], v160 offset:14336\n\t"
      "ds_read_b64_tr_b16 v[80:81], v169\n\t"
      "ds_read_b64_tr_b16 v[82:83], v169 offset:512\n\t"
      "ds_read_b64_tr_b16 v[84:85], v168 offset:1024\n\t"
      "ds_read_b64_tr_b16 v[86:87], v168 offset:1536\n\t"
      "ds_read_b128 v[64:67], v162\n\t"
      "ds_read_b64_tr_b16 v[88:89], v169 offset:1024\n\t"
      "ds_read_b64_tr_b16 v[90:91], v169 offset:1536\n\t"
      "ds_read_b64_tr_b16 v[104:105], v168 offset:2048\n\t"
      "ds_read_b64_tr_b16 v[106:107], v168 offset:2560\n\t"
      "ds_read_b128 v[96:99], v161 offset:1024\n\t"
      "ds_read_b128 v[100:103], v161 offset:15360\n\t"
      "ds_read_b64_tr_b16 v[108:109], v169 offset:2048\n\t"
      "ds_read_b64_tr_b16 v[110:111], v169 offset:2560\n\t"
      "ds_read_b64_tr_b16 v[112:113], v168 offset:3072\n\t"
      "ds_read_b64_tr_b16 v[114:115], v168 offset:3584\n\t"
      "ds_read_b128 v[92:95], v163 offset:1024\n\t"
      "ds_read_b64_tr_b16 v[116:117], v169 offset:3072\n\t"
      "ds_read_b64_tr_b16 v[118:119], v169 offset:3584\n\t"
      "s_barrier\n\t"
      "s_waitcnt lgkmcnt(14)\n\t"
      "v_mfma_f32_16x16x32_bf16 v[0:3], v[76:79], v[68:71], 0\n\t"
      "ds_read_b64_tr_b16 v[132:133], v168 offset:16384\n\t"
      "ds_read_b64_tr_b16 v[134:135], v168 offset:16896\n\t"
      "v_mfma_f32_16x16x32_bf16 v[8:11], v[76:79], v[72:75], 0\n\t"
      "ds_read_b128 v[124:127], v160 offset:2048\n\t"
      "v_mfma_f32_16x16x32_bf16 v[4:7], v[80:83], v[68:71], 0\n\t"
      "ds_read_b128 v[128:131], v160 offset:16384\n\t"
      "v_mfma_f32_16x16x32_bf16 v[12:15], v[80:83], v[72:75], 0\n\t"
      "ds_read_b64_tr_b16 v[136:137], v169 offset:16384\n\t"
      "v_mfma_f32_16x16x32_bf16 v[24:27], v[84:87], v[68:71], 0\n\t"
      "ds_read_b64_tr_b16 v[138:139], v169 offset:16896\n\t"
      "v_mfma_f32_16x16x32_bf16 v[32:35], v[84:87], v[72:75], 0\n\t"
      "ds_read_b64_tr_b16 v[140:141], v168 offset:17408\n\t"
      "s_waitcnt lgkmcnt(14)\n\t"
      "v_mfma_f32_16x16x32_bf16 v[16:19], v[84:87], v[64:67], 0\n\t"
      "ds_read_b64_tr_b16 v[142:143], v168 offset:17920\n\t"
      "v_mfma_f32_16x16x32_bf16 v[28:31], v[88:91], v[68:71], 0\n\t"
      "ds_read_b128 v[120:123], v162 offset:2048\n\t"
      "v_mfma_f32_16x16x32_bf16 v[36:39], v[88:91], v[72:75], 0\n\t"
      "ds_read_b64_tr_b16 v[144:145], v169 offset:17408\n\t"
      "v_mfma_f32_16x16x32_bf16 v[20:23], v[88:91], v[64:67], 0\n\t"
      "ds_read_b64_tr_b16 v[146:147], v169 offset:17920\n\t"
      "s_waitcnt lgkmcnt(11)\n\t"
      "v_mfma_f32_16x16x32_bf16 v[0:3], v[104:107], v[96:99], v[0:3]\n\t"
      "ds_read_b64_tr_b16 v[76:77], v168 offset:18432\n\t"
      "ds_read_b64_tr_b16 v[78:79], v168 offset:18944\n\t"
      "v_mfma_f32_16x16x32_bf16 v[8:11], v[104:107], v[100:103], v[8:11]\n\t"
      "ds_read_b128 v[68:71], v161 offset:3072\n\t"
      "v_mfma_f32_16x16x32_bf16 v[4:7], v[108:111], v[96:99], v[4:7]\n\t"
      "ds_read_b128 v[72:75], v161 offset:17408\n\t"
      "v_mfma_f32_16x16x32_bf16 v[12:15], v[108:111], v[100:103], v[12:15]\n\t"
      "ds_read_b64_tr_b16 v[80:81], v169 offset:18432\n\t"
      "v_mfma_f32_16x16x32_bf16 v[24:27], v[112:115], v[96:99], v[24:27]\n\t"
      "ds_read_b64_tr_b16 v[82:83], v169 offset:18944\n\t"
      "v_mfma_f32_16x16x32_bf16 v[32:35], v[112:115], v[100:103], v[32:35]\n\t"
      "ds_read_b64_tr_b16 v[84:85], v168 offset:19456\n\t"
      "v_mfma_f32_16x16x32_bf16 v[16:19], v[112:115], v[92:95], v[16:19]\n\t"
      "ds_read_b64_tr_b16 v[86:87], v168 offset:19968\n\t"
      "v_mfma_f32_16x16x32_bf16 v[28:31], v[116:119], v[96:99], v[28:31]\n\t"
      "ds_read_b128 v[64:67], v163 offset:3072\n\t"
      "v_mfma_f32_16x16x32_bf16 v[36:39], v[116:119], v[100:103], v[36:39]\n\t"
      "ds_read_b64_tr_b16 v[88:89], v169 offset:19456\n\t"
      "v_mfma_f32_16x16x32_bf16 v[20:23], v[116:119], v[92:95], v[20:23]\n\t"
      "ds_read_b64_tr_b16 v[90:91], v169 offset:19968\n\t"
      "s_barrier\n\t"
      "s_waitcnt lgkmcnt(11)\n\t"
      "v_mfma_f32_16x16x32_bf16 v[0:3], v[132:135], v[124:127], v[0:3]\n\t"
      "ds_read_b64_tr_b16 v[104:105], v166\n\t"
      "v_lshlrev_b32_e32 v199, 2, v193\n\t"
      "v_mfma_f32_16x16x32_bf16 v[8:11], v[132:135], v[128:131], v[8:11]\n\t"
      "ds_read_b64_tr_b16 v[106:107], v166 offset:512\n\t"
      "v_sub_u32_e32 v199, v194, v199\n\t"
      "v_mfma_f32_16x16x32_bf16 v[4:7], v[136:139], v[124:127], v[4:7]\n\t"
      "ds_read_b128 v[96:99], v160\n\t"
      "v_mov_b32_e32 v200, 0xff800000\n\t"
      "v_mfma_f32_16x16x32_bf16 v[12:15], v[136:139], v[128:131], v[12:15]\n\t"
      "ds_read_b128 v[100:103], v160 offset:14336\n\t"
      "s_cmp_lt_u32 s25, 2\n\t"
      "v_mfma_f32_16x16x32_bf16 v[24:27], v[140:143], v[124:127], v[24:27]\n\t"
      "ds_read_b128 v[92:95], v162\n\t"
      "s_cselect_b32 s35, 0xff800000, 0\n\t"
      "v_mfma_f32_16x16x32_bf16 v[32:35], v[140:143], v[128:131], v[32:35]\n\t"
      "ds_read_b64_tr_b16 v[108:109], v167\n\t"
      "s_bitcmp0_b32 s24, 0\n\t"
      "v_mfma_f32_16x16x32_bf16 v[16:19], v[140:143], v[120:123], v[16:19]\n\t"
      "ds_read_b64_tr_b16 v[110:111], v167 offset:512\n\t"
      "s_cselect_b32 s35, s35, 0\n\t"
      "v_mfma_f32_16x16x32_bf16 v[28:31], v[144:147], v[124:127], v[28:31]\n\t"
      "v_cmp_ge_i32_e32 vcc, -9, v199\n\t"
      "v_mfma_f32_16x16x32_bf16 v[36:39], v[144:147], v[128:131], v[36:39]\n\t"
      "v_subrev_u32_e32 v201, 0, v199\n\t"
      "v_mfma_f32_16x16x32_bf16 v[20:23], v[144:147], v[120:123], v[20:23]\n\t"
      "v_cmp_gt_u32_e64 s[64:65], 8, v201\n\t"
      "s_waitcnt lgkmcnt(7)\n\t"
      "v_mfma_f32_16x16x32_bf16 v[0:3], v[76:79], v[68:71], v[0:3]\n\t"
      "ds_read_b64_tr_b16 v[132:133], v166 offset:1024\n\t"
      "v_cndmask_b32_e64 v172, v200, 0, vcc\n\t"
      "v_mfma_f32_16x16x32_bf16 v[8:11], v[76:79], v[72:75], v[8:11]\n\t"
      "ds_read_b64_tr_b16 v[134:135], v166 offset:1536\n\t"
      "v_cndmask_b32_e64 v176, v200, 0, s[64:65]\n\t"
      "v_mfma_f32_16x16x32_bf16 v[4:7], v[80:83], v[68:71], v[4:7]\n\t"
      "ds_read_b128 v[124:127], v161 offset:1024\n\t"
      "v_or_b32_e32 v180, s35, v172\n\t"
      "v_mfma_f32_16x16x32_bf16 v[12:15], v[80:83], v[72:75], v[12:15]\n\t"
      "ds_read_b128 v[128:131], v161 offset:15360\n\t"
      "v_cmp_ge_i32_e32 vcc, -8, v199\n\t"
      "v_mfma_f32_16x16x32_bf16 v[24:27], v[84:87], v[68:71], v[24:27]\n\t"
      "ds_read_b128 v[120:123], v163 offset:1024\n\t"
      "v_subrev_u32_e32 v201, 1, v199\n\t"
      "v_mfma_f32_16x16x32_bf16 v[32:35], v[84:87], v[72:75], v[32:35]\n\t"
      "ds_read_b64_tr_b16 v[136:137], v167 offset:1024\n\t"
      "v_cmp_gt_u32_e64 s[64:65], 8, v201\n\t"
      "v_mfma_f32_16x16x32_bf16 v[16:19], v[84:87], v[64:67], v[16:19]\n\t"
      "ds_read_b64_tr_b16 v[138:139], v167 offset:1536\n\t"
      "v_cndmask_b32_e64 v173, v200, 0, vcc\n\t"
      "v_mfma_f32_16x16x32_bf16 v[28:31], v[88:91], v[68:71], v[28:31]\n\t"
      "v_cndmask_b32_e64 v177, v200, 0, s[64:65]\n\t"
      "v_mfma_f32_16x16x32_bf16 v[36:39], v[88:91], v[72:75], v[36:39]\n\t"
      "v_or_b32_e32 v181, s35, v173\n\t"
      "v_mfma_f32_16x16x32_bf16 v[20:23], v[88:91], v[64:67], v[20:23]\n\t"
      "v_cmp_ge_i32_e32 vcc, -7, v199\n\t"
      "s_waitcnt lgkmcnt(7)\n\t"
      "v_mfma_f32_16x16x32_bf16 v[48:51], v[96:99], v[104:107], 0\n\t"
      "ds_read_b64_tr_b16 v[76:77], v166 offset:2048\n\t"
      "ds_read_b64_tr_b16 v[78:79], v166 offset:2560\n\t"
      "v_subrev_u32_e32 v201, 2, v199\n\t"
      "v_mfma_f32_16x16x32_bf16 v[56:59], v[100:103], v[104:107], 0\n\t"
      "ds_read_b128 v[68:71], v160 offset:2048\n\t"
      "v_cmp_gt_u32_e64 s[64:65], 8, v201\n\t"
      "v_mfma_f32_16x16x32_bf16 v[40:43], v[92:95], v[104:107], 0\n\t"
      "ds_read_b128 v[72:75], v160 offset:16384\n\t"
      "v_cndmask_b32_e64 v174, v200, 0, vcc\n\t"
      "v_mfma_f32_16x16x32_bf16 v[52:55], v[96:99], v[108:111], 0\n\t"
      "ds_read_b128 v[64:67], v162 offset:2048\n\t"
      "v_cndmask_b32_e64 v178, v200, 0, s[64:65]\n\t"
      "v_mfma_f32_16x16x32_bf16 v[60:63], v[100:103], v[108:111], 0\n\t"
      "ds_read_b64_tr_b16 v[80:81], v167 offset:2048\n\t"
      "v_or_b32_e32 v182, s35, v174\n\t"
      "v_mfma_f32_16x16x32_bf16 v[44:47], v[92:95], v[108:111], 0\n\t"
      "ds_read_b64_tr_b16 v[82:83], v167 offset:2560\n\t"
      "v_cmp_ge_i32_e32 vcc, -6, v199\n\t"
      "s_waitcnt lgkmcnt(7)\n\t"
      "v_mfma_f32_16x16x32_bf16 v[48:51], v[124:127], v[132:135], v[48:51]\n\t"
      "ds_read_b64_tr_b16 v[104:105], v166 offset:3072\n\t"
      "ds_read_b64_tr_b16 v[106:107], v166 offset:3584\n\t"
      "v_subrev_u32_e32 v201, 3, v199\n\t"
      "v_mfma_f32_16x16x32_bf16 v[56:59], v[128:131], v[132:135], v[56:59]\n\t"
      "ds_read_b128 v[96:99], v161 offset:3072\n\t"
      "v_cmp_gt_u32_e64 s[64:65], 8, v201\n\t"
      "v_mfma_f32_16x16x32_bf16 v[40:43], v[120:123], v[132:135], v[40:43]\n\t"
      "ds_read_b128 v[100:103], v161 offset:17408\n\t"
      "v_cndmask_b32_e64 v175, v200, 0, vcc\n\t"
      "v_mfma_f32_16x16x32_bf16 v[52:55], v[124:127], v[136:139], v[52:55]\n\t"
      "ds_read_b128 v[92:95], v163 offset:3072\n\t"
      "v_cndmask_b32_e64 v179, v200, 0, s[64:65]\n\t"
      "v_mfma_f32_16x16x32_bf16 v[60:63], v[128:131], v[136:139], v[60:63]\n\t"
      "ds_read_b64_tr_b16 v[108:109], v167 offset:3072\n\t"
      "v_or_b32_e32 v183, s35, v175\n\t"
      "v_mfma_f32_16x16x32_bf16 v[44:47], v[120:123], v[136:139], v[44:47]\n\t"
      "ds_read_b64_tr_b16 v[110:111], v167 offset:3584\n\t"
      "v_lshrrev_b32_e32 v201, 31, v172\n\t"
      "s_barrier\n\t"
      "s_waitcnt lgkmcnt(7)\n\t"
      "v_mfma_f32_16x16x32_bf16 v[48:51], v[68:71], v[76:79], v[48:51]\n\t"
      "ds_read_b64_tr_b16 v[132:133], v166 offset:16384\n\t"
      "ds_read_b64_tr_b16 v[134:135], v166 offset:16896\n\t"
      "ds_read_b128 v[124:127], v160 offset:4096\n\t"
      "ds_read_b128 v[128:131], v160 offset:18432\n\t"
      "ds_read_b64_tr_b16 v[136:137], v167 offset:16384\n\t"
      "ds_read_b64_tr_b16 v[138:139], v167 offset:16896\n\t"
      "v_lshrrev_b32_e32 v202, 31, v173\n\t"
      "v_mfma_f32_16x16x32_bf16 v[56:59], v[72:75], v[76:79], v[56:59]\n\t"
      "ds_read_b64_tr_b16 v[140:141], v166 offset:17408\n\t"
      "v_lshl_or_b32 v201, v202, 16, v201\n\t"
      "v_mfma_f32_16x16x32_bf16 v[40:43], v[64:67], v[76:79], v[40:43]\n\t"
      "ds_read_b64_tr_b16 v[142:143], v166 offset:17920\n\t"
      "v_mul_u32_u24_e32 v184, 0xffff, v201\n\t"
      "v_mfma_f32_16x16x32_bf16 v[52:55], v[68:71], v[80:83], v[52:55]\n\t"
      "ds_read_b128 v[120:123], v162 offset:4096\n\t"
      "v_lshrrev_b32_e32 v201, 31, v174\n\t"
      "v_mfma_f32_16x16x32_bf16 v[60:63], v[72:75], v[80:83], v[60:63]\n\t"
      "ds_read_b64_tr_b16 v[144:145], v167 offset:17408\n\t"
      "v_lshrrev_b32_e32 v202, 31, v175\n\t"
      "v_mfma_f32_16x16x32_bf16 v[44:47], v[64:67], v[80:83], v[44:47]\n\t"
      "ds_read_b64_tr_b16 v[146:147], v167 offset:17920\n\t"
      "v_lshl_or_b32 v201, v202, 16, v201\n\t"
      "s_waitcnt lgkmcnt(11)\n\t"
      "v_mfma_f32_16x16x32_bf16 v[48:51], v[96:99], v[104:107], v[48:51]\n\t"
      "ds_read_b64_tr_b16 v[76:77], v166 offset:18432\n\t"
      "ds_read_b64_tr_b16 v[78:79], v166 offset:18944\n\t"
      "ds_read_b128 v[68:71], v161 offset:5120\n\t"
      "ds_read_b128 v[72:75], v161 offset:19456\n\t"
      "ds_read_b64_tr_b16 v[80:81], v167 offset:18432\n\t"
      "ds_read_b64_tr_b16 v[82:83], v167 offset:18944\n\t"
      "v_mul_u32_u24_e32 v185, 0xffff, v201\n\t"
      "v_mfma_f32_16x16x32_bf16 v[56:59], v[100:103], v[104:107], v[56:59]\n\t"
      "ds_read_b64_tr_b16 v[84:85], v166 offset:19456\n\t"
      "s_mov_b32 s68, 0x0\n\t"
      "v_mfma_f32_16x16x32_bf16 v[40:43], v[92:95], v[104:107], v[40:43]\n\t"
      "ds_read_b64_tr_b16 v[86:87], v166 offset:19968\n\t"
      "s_mov_b32 s69, 0xffff0000\n\t"
      "v_mfma_f32_16x16x32_bf16 v[52:55], v[96:99], v[108:111], v[52:55]\n\t"
      "ds_read_b128 v[64:67], v163 offset:5120\n\t"
      "s_mov_b32 s70, 0xffff\n\t"
      "v_mfma_f32_16x16x32_bf16 v[60:63], v[100:103], v[108:111], v[60:63]\n\t"
      "ds_read_b64_tr_b16 v[88:89], v167 offset:19456\n\t"
      "s_mov_b32 s71, 0x0\n\t"
      "v_mfma_f32_16x16x32_bf16 v[44:47], v[92:95], v[108:111], v[44:47]\n\t"
      "ds_read_b64_tr_b16 v[90:91], v167 offset:19968\n\t"
      "v_lshlrev_b32_e32 v231, 3, v192\n\t"
      "s_barrier\n\t"
      "s_waitcnt lgkmcnt(11)\n\t"
      "v_mfma_f32_16x16x32_bf16 v[0:3], v[132:135], v[124:127], v[0:3]\n\t"
      "ds_read_b64_tr_b16 v[104:105], v166 offset:32768\n\t"
      "ds_read_b64_tr_b16 v[106:107], v166 offset:33280\n\t"
      "s_lshl_b32 s34, s25, 12\n\t"
      "v_mfma_f32_16x16x32_bf16 v[8:11], v[132:135], v[128:131], v[8:11]\n\t"
      "ds_read_b128 v[96:99], v160 offset:6144\n\t"
      "s_lshr_b32 s35, s39, 2\n\t"
      "v_mfma_f32_16x16x32_bf16 v[4:7], v[136:139], v[124:127], v[4:7]\n\t"
      "ds_read_b128 v[100:103], v160 offset:20480\n\t"
      "s_add_u32 s34, s34, s35\n\t"
      "v_mfma_f32_16x16x32_bf16 v[12:15], v[136:139], v[128:131], v[12:15]\n\t"
      "ds_read_b64_tr_b16 v[108:109], v167 offset:32768\n\t"
      "s_add_u32 s34, s34, 12582912\n\t"
      "v_mfma_f32_16x16x32_bf16 v[24:27], v[140:143], v[124:127], v[24:27]\n\t"
      "ds_read_b64_tr_b16 v[110:111], v167 offset:33280\n\t"
      "s_add_u32 s46, s20, s34\n\t"
      "v_mfma_f32_16x16x32_bf16 v[32:35], v[140:143], v[128:131], v[32:35]\n\t"
      "ds_read_b64_tr_b16 v[112:113], v166 offset:33792\n\t"
      "s_addc_u32 s47, s21, 0\n\t"
      "v_mfma_f32_16x16x32_bf16 v[16:19], v[140:143], v[120:123], v[16:19]\n\t"
      "ds_read_b64_tr_b16 v[114:115], v166 offset:34304\n\t"
      "s_lshl_b32 s34, s28, 7\n\t"
      "v_mfma_f32_16x16x32_bf16 v[28:31], v[144:147], v[124:127], v[28:31]\n\t"
      "ds_read_b128 v[92:95], v162 offset:6144\n\t"
      "s_add_u32 s58, s46, s34\n\t"
      "v_mfma_f32_16x16x32_bf16 v[36:39], v[144:147], v[128:131], v[36:39]\n\t"
      "ds_read_b64_tr_b16 v[116:117], v167 offset:33792\n\t"
      "s_addc_u32 s59, s47, 0\n\t"
      "v_mfma_f32_16x16x32_bf16 v[20:23], v[144:147], v[120:123], v[20:23]\n\t"
      "ds_read_b64_tr_b16 v[118:119], v167 offset:34304\n\t"
      "s_and_b32 s34, s24, 1\n\t"
      "s_waitcnt lgkmcnt(11)\n\t"
      "v_mfma_f32_16x16x32_bf16 v[0:3], v[76:79], v[68:71], v[0:3]\n\t"
      "ds_read_b64_tr_b16 v[132:133], v166 offset:34816\n\t"
      "ds_read_b64_tr_b16 v[134:135], v166 offset:35328\n\t"
      "s_lshl1_add_u32 s34, s25, s34\n\t"
      "v_mfma_f32_16x16x32_bf16 v[8:11], v[76:79], v[72:75], v[8:11]\n\t"
      "ds_read_b128 v[124:127], v161 offset:7168\n\t"
      "s_lshl_b32 s34, s34, 5\n\t"
      "v_mfma_f32_16x16x32_bf16 v[4:7], v[80:83], v[68:71], v[4:7]\n\t"
      "ds_read_b128 v[128:131], v161 offset:21504\n\t"
      "s_lshl1_add_u32 s34, s26, s34\n\t"
      "v_mfma_f32_16x16x32_bf16 v[12:15], v[80:83], v[72:75], v[12:15]\n\t"
      "ds_read_b64_tr_b16 v[136:137], v167 offset:34816\n\t"
      "s_bfe_u32 s35, s24, 65537\n\t"
      "v_mfma_f32_16x16x32_bf16 v[24:27], v[84:87], v[68:71], v[24:27]\n\t"
      "ds_read_b64_tr_b16 v[138:139], v167 offset:35328\n\t"
      "s_add_u32 s34, s34, s35\n\t"
      "v_mfma_f32_16x16x32_bf16 v[32:35], v[84:87], v[72:75], v[32:35]\n\t"
      "ds_read_b64_tr_b16 v[140:141], v166 offset:35840\n\t"
      "s_lshl_b32 s34, s34, 10\n\t"
      "v_mfma_f32_16x16x32_bf16 v[16:19], v[84:87], v[64:67], v[16:19]\n\t"
      "ds_read_b64_tr_b16 v[142:143], v166 offset:36352\n\t"
      "s_add_u32 s42, s20, s34\n\t"
      "v_mfma_f32_16x16x32_bf16 v[28:31], v[88:91], v[68:71], v[28:31]\n\t"
      "ds_read_b128 v[120:123], v165 offset:7168\n\t"
      "s_addc_u32 s43, s21, 0\n\t"
      "v_mfma_f32_16x16x32_bf16 v[36:39], v[88:91], v[72:75], v[36:39]\n\t"
      "ds_read_b64_tr_b16 v[144:145], v167 offset:35840\n\t"
      "s_add_u32 s44, s42, 16384\n\t"
      "v_mfma_f32_16x16x32_bf16 v[20:23], v[88:91], v[64:67], v[20:23]\n\t"
      "ds_read_b64_tr_b16 v[146:147], v167 offset:36352\n\t"
      "s_addc_u32 s45, s43, 0\n\t"
      "s_barrier\n\t"
      "s_waitcnt lgkmcnt(11)\n\t"
      "v_mfma_f32_16x16x32_bf16 v[0:3], v[104:107], v[96:99], v[0:3]\n\t"
      "ds_read_b64_tr_b16 v[76:77], v166 offset:49152\n\t"
      "ds_read_b64_tr_b16 v[78:79], v166 offset:49664\n\t"
      "v_and_b32_e32 v199, 60, v192\n\t"
      "v_mfma_f32_16x16x32_bf16 v[8:11], v[104:107], v[100:103], v[8:11]\n\t"
      "ds_read_b128 v[68:71], v160 offset:8192\n\t"
      "v_and_b32_e32 v200, 24, v192\n\t"
      "v_mfma_f32_16x16x32_bf16 v[4:7], v[108:111], v[96:99], v[4:7]\n\t"
      "ds_read_b128 v[72:75], v160 offset:22528\n\t"
      "v_lshlrev_b32_e32 v202, 3, v196\n\t"
      "v_mfma_f32_16x16x32_bf16 v[12:15], v[108:111], v[100:103], v[12:15]\n\t"
      "ds_read_b64_tr_b16 v[80:81], v167 offset:49152\n\t"
      "v_lshl_or_b32 v202, v200, 2, v202\n\t"
      "v_mfma_f32_16x16x32_bf16 v[24:27], v[112:115], v[96:99], v[24:27]\n\t"
      "ds_read_b64_tr_b16 v[82:83], v167 offset:49664\n\t"
      "v_lshl_or_b32 v201, v199, 5, v202\n\t"
      "v_mfma_f32_16x16x32_bf16 v[32:35], v[112:115], v[100:103], v[32:35]\n\t"
      "ds_read_b64_tr_b16 v[84:85], v166 offset:50176\n\t"
      "v_add_u32_e32 v216, 106496, v201\n\t"
      "v_mfma_f32_16x16x32_bf16 v[16:19], v[112:115], v[92:95], v[16:19]\n\t"
      "ds_read_b64_tr_b16 v[86:87], v166 offset:50688\n\t"
      "v_mov_b32_e32 v199, 2048\n\t"
      "v_mfma_f32_16x16x32_bf16 v[28:31], v[116:119], v[96:99], v[28:31]\n\t"
      "ds_read_b128 v[64:67], v164 offset:8192\n\t"
      "v_cndmask_b32_e64 v199, v199, 0, s[68:69]\n\t"
      "v_mfma_f32_16x16x32_bf16 v[36:39], v[116:119], v[100:103], v[36:39]\n\t"
      "ds_read_b64_tr_b16 v[88:89], v167 offset:50176\n\t"
      "v_add_u32_e32 v220, v216, v199\n\t"
      "v_mfma_f32_16x16x32_bf16 v[20:23], v[116:119], v[92:95], v[20:23]\n\t"
      "ds_read_b64_tr_b16 v[90:91], v167 offset:50688\n\t"
      "v_xor_b32_e32 v217, 32, v216\n\t"
      "s_waitcnt lgkmcnt(11)\n\t"
      "v_mfma_f32_16x16x32_bf16 v[0:3], v[132:135], v[124:127], v[0:3]\n\t"
      "ds_read_b64_tr_b16 v[104:105], v166 offset:51200\n\t"
      "ds_read_b64_tr_b16 v[106:107], v166 offset:51712\n\t"
      "v_xor_b32_e32 v218, 64, v216\n\t"
      "v_mfma_f32_16x16x32_bf16 v[8:11], v[132:135], v[128:131], v[8:11]\n\t"
      "ds_read_b128 v[96:99], v161 offset:9216\n\t"
      "v_xor_b32_e32 v219, 96, v216\n\t"
      "v_mfma_f32_16x16x32_bf16 v[4:7], v[136:139], v[124:127], v[4:7]\n\t"
      "ds_read_b128 v[100:103], v161 offset:23552\n\t"
      "v_xor_b32_e32 v221, 32, v220\n\t"
      "v_mfma_f32_16x16x32_bf16 v[12:15], v[136:139], v[128:131], v[12:15]\n\t"
      "ds_read_b64_tr_b16 v[108:109], v167 offset:51200\n\t"
      "v_xor_b32_e32 v222, 64, v220\n\t"
      "v_mfma_f32_16x16x32_bf16 v[24:27], v[140:143], v[124:127], v[24:27]\n\t"
      "ds_read_b64_tr_b16 v[110:111], v167 offset:51712\n\t"
      "v_xor_b32_e32 v223, 96, v220\n\t"
      "v_mfma_f32_16x16x32_bf16 v[32:35], v[140:143], v[128:131], v[32:35]\n\t"
      "ds_read_b64_tr_b16 v[112:113], v166 offset:52224\n\t"
      "s_lshr_b32 s34, s39, 7\n\t"
      "v_mfma_f32_16x16x32_bf16 v[16:19], v[140:143], v[120:123], v[16:19]\n\t"
      "ds_read_b64_tr_b16 v[114:115], v166 offset:52736\n\t"
      "s_add_u32 s34, s34, s38\n\t"
      "v_mfma_f32_16x16x32_bf16 v[28:31], v[144:147], v[124:127], v[28:31]\n\t"
      "ds_read_b128 v[92:95], v165 offset:9216\n\t"
      "s_mul_i32 s34, s34, 896\n\t"
      "v_mfma_f32_16x16x32_bf16 v[36:39], v[144:147], v[128:131], v[36:39]\n\t"
      "ds_read_b64_tr_b16 v[116:117], v167 offset:52224\n\t"
      "s_lshl_b32 s35, s28, 7\n\t"
      "v_mfma_f32_16x16x32_bf16 v[20:23], v[144:147], v[120:123], v[20:23]\n\t"
      "ds_read_b64_tr_b16 v[118:119], v167 offset:52736\n\t"
      "s_add_u32 s34, s34, s35\n\t"
      "s_barrier\n\t"
      "s_waitcnt lgkmcnt(11)\n\t"
      "v_mfma_f32_16x16x32_bf16 v[0:3], v[76:79], v[68:71], v[0:3]\n\t"
      "ds_read_b64_tr_b16 v[132:133], v168\n\t"
      "ds_read_b64_tr_b16 v[134:135], v168 offset:512\n\t"
      "v_and_b32_e32 v199, 16, v192\n\t"
      "v_mfma_f32_16x16x32_bf16 v[8:11], v[76:79], v[72:75], v[8:11]\n\t"
      "ds_read_b128 v[124:127], v160 offset:10240\n\t"
      "v_lshlrev_b32_e32 v199, 1, v199\n\t"
      "v_mfma_f32_16x16x32_bf16 v[4:7], v[80:83], v[68:71], v[4:7]\n\t"
      "ds_read_b128 v[128:131], v160 offset:24576\n\t"
      "v_and_b32_e32 v200, 32, v192\n\t"
      "v_mfma_f32_16x16x32_bf16 v[12:15], v[80:83], v[72:75], v[12:15]\n\t"
      "ds_read_b64_tr_b16 v[136:137], v169\n\t"
      "v_lshrrev_b32_e32 v201, 1, v200\n\t"
      "v_mfma_f32_16x16x32_bf16 v[24:27], v[84:87], v[68:71], v[24:27]\n\t"
      "ds_read_b64_tr_b16 v[138:139], v169 offset:512\n\t"
      "v_add3_u32 v228, v199, v201, s34\n\t"
      "v_mfma_f32_16x16x32_bf16 v[32:35], v[84:87], v[72:75], v[32:35]\n\t"
      "ds_read_b64_tr_b16 v[140:141], v168 offset:1024\n\t"
      "s_movk_i32 s35, 1792\n\t"
      "v_mfma_f32_16x16x32_bf16 v[16:19], v[84:87], v[64:67], v[16:19]\n\t"
      "ds_read_b64_tr_b16 v[142:143], v168 offset:1536\n\t"
      "v_mad_u32_u24 v228, v194, s35, v228\n\t"
      "v_mfma_f32_16x16x32_bf16 v[28:31], v[88:91], v[68:71], v[28:31]\n\t"
      "ds_read_b128 v[120:123], v164 offset:10240\n\t"
      "v_mfma_f32_16x16x32_bf16 v[36:39], v[88:91], v[72:75], v[36:39]\n\t"
      "ds_read_b64_tr_b16 v[144:145], v169 offset:1024\n\t"
      "v_mfma_f32_16x16x32_bf16 v[20:23], v[88:91], v[64:67], v[20:23]\n\t"
      "ds_read_b64_tr_b16 v[146:147], v169 offset:1536\n\t"
      "s_waitcnt lgkmcnt(11)\n\t"
      "v_mfma_f32_16x16x32_bf16 v[0:3], v[104:107], v[96:99], v[0:3]\n\t"
      "ds_read_b64_tr_b16 v[76:77], v168 offset:2048\n\t"
      "ds_read_b64_tr_b16 v[78:79], v168 offset:2560\n\t"
      "v_mfma_f32_16x16x32_bf16 v[8:11], v[104:107], v[100:103], v[8:11]\n\t"
      "ds_read_b128 v[68:71], v161 offset:11264\n\t"
      "v_mfma_f32_16x16x32_bf16 v[4:7], v[108:111], v[96:99], v[4:7]\n\t"
      "ds_read_b128 v[72:75], v161 offset:25600\n\t"
      "v_mfma_f32_16x16x32_bf16 v[12:15], v[108:111], v[100:103], v[12:15]\n\t"
      "ds_read_b64_tr_b16 v[80:81], v169 offset:2048\n\t"
      "v_mfma_f32_16x16x32_bf16 v[24:27], v[112:115], v[96:99], v[24:27]\n\t"
      "ds_read_b64_tr_b16 v[82:83], v169 offset:2560\n\t"
      "v_mfma_f32_16x16x32_bf16 v[32:35], v[112:115], v[100:103], v[32:35]\n\t"
      "ds_read_b64_tr_b16 v[84:85], v168 offset:3072\n\t"
      "v_mfma_f32_16x16x32_bf16 v[16:19], v[112:115], v[92:95], v[16:19]\n\t"
      "ds_read_b64_tr_b16 v[86:87], v168 offset:3584\n\t"
      "v_mfma_f32_16x16x32_bf16 v[28:31], v[116:119], v[96:99], v[28:31]\n\t"
      "ds_read_b128 v[64:67], v165 offset:11264\n\t"
      "v_mfma_f32_16x16x32_bf16 v[36:39], v[116:119], v[100:103], v[36:39]\n\t"
      "ds_read_b64_tr_b16 v[88:89], v169 offset:3072\n\t"
      "v_mfma_f32_16x16x32_bf16 v[20:23], v[116:119], v[92:95], v[20:23]\n\t"
      "ds_read_b64_tr_b16 v[90:91], v169 offset:3584\n\t"
      "s_barrier\n\t"
      "s_waitcnt lgkmcnt(11)\n\t"
      "v_mfma_f32_16x16x32_bf16 v[0:3], v[132:135], v[124:127], v[0:3]\n\t"
      "ds_read_b64_tr_b16 v[104:105], v168 offset:16384\n\t"
      "ds_read_b64_tr_b16 v[106:107], v168 offset:16896\n\t"
      "v_mfma_f32_16x16x32_bf16 v[8:11], v[132:135], v[128:131], v[8:11]\n\t"
      "ds_read_b128 v[96:99], v160 offset:12288\n\t"
      "v_mfma_f32_16x16x32_bf16 v[4:7], v[136:139], v[124:127], v[4:7]\n\t"
      "ds_read_b128 v[100:103], v160 offset:26624\n\t"
      "v_mfma_f32_16x16x32_bf16 v[12:15], v[136:139], v[128:131], v[12:15]\n\t"
      "ds_read_b64_tr_b16 v[108:109], v169 offset:16384\n\t"
      "v_mfma_f32_16x16x32_bf16 v[24:27], v[140:143], v[124:127], v[24:27]\n\t"
      "ds_read_b64_tr_b16 v[110:111], v169 offset:16896\n\t"
      "v_mfma_f32_16x16x32_bf16 v[32:35], v[140:143], v[128:131], v[32:35]\n\t"
      "ds_read_b64_tr_b16 v[112:113], v168 offset:17408\n\t"
      "v_mfma_f32_16x16x32_bf16 v[16:19], v[140:143], v[120:123], v[16:19]\n\t"
      "ds_read_b64_tr_b16 v[114:115], v168 offset:17920\n\t"
      "v_mfma_f32_16x16x32_bf16 v[28:31], v[144:147], v[124:127], v[28:31]\n\t"
      "ds_read_b128 v[92:95], v164 offset:12288\n\t"
      "v_mfma_f32_16x16x32_bf16 v[36:39], v[144:147], v[128:131], v[36:39]\n\t"
      "ds_read_b64_tr_b16 v[116:117], v169 offset:17408\n\t"
      "v_mfma_f32_16x16x32_bf16 v[20:23], v[144:147], v[120:123], v[20:23]\n\t"
      "ds_read_b64_tr_b16 v[118:119], v169 offset:17920\n\t"
      "s_waitcnt lgkmcnt(11)\n\t"
      "v_mfma_f32_16x16x32_bf16 v[0:3], v[76:79], v[68:71], v[0:3]\n\t"
      "ds_read_b64_tr_b16 v[132:133], v168 offset:18432\n\t"
      "ds_read_b64_tr_b16 v[134:135], v168 offset:18944\n\t"
      "v_mfma_f32_16x16x32_bf16 v[8:11], v[76:79], v[72:75], v[8:11]\n\t"
      "ds_read_b128 v[124:127], v161 offset:13312\n\t"
      "v_mfma_f32_16x16x32_bf16 v[4:7], v[80:83], v[68:71], v[4:7]\n\t"
      "ds_read_b128 v[128:131], v161 offset:27648\n\t"
      "v_mfma_f32_16x16x32_bf16 v[12:15], v[80:83], v[72:75], v[12:15]\n\t"
      "ds_read_b64_tr_b16 v[136:137], v169 offset:18432\n\t"
      "v_mfma_f32_16x16x32_bf16 v[24:27], v[84:87], v[68:71], v[24:27]\n\t"
      "ds_read_b64_tr_b16 v[138:139], v169 offset:18944\n\t"
      "v_mfma_f32_16x16x32_bf16 v[32:35], v[84:87], v[72:75], v[32:35]\n\t"
      "ds_read_b64_tr_b16 v[140:141], v168 offset:19456\n\t"
      "v_mfma_f32_16x16x32_bf16 v[16:19], v[84:87], v[64:67], v[16:19]\n\t"
      "ds_read_b64_tr_b16 v[142:143], v168 offset:19968\n\t"
      "v_mfma_f32_16x16x32_bf16 v[28:31], v[88:91], v[68:71], v[28:31]\n\t"
      "ds_read_b128 v[120:123], v165 offset:13312\n\t"
      "v_mfma_f32_16x16x32_bf16 v[36:39], v[88:91], v[72:75], v[36:39]\n\t"
      "ds_read_b64_tr_b16 v[144:145], v169 offset:19456\n\t"
      "v_mfma_f32_16x16x32_bf16 v[20:23], v[88:91], v[64:67], v[20:23]\n\t"
      "ds_read_b64_tr_b16 v[146:147], v169 offset:19968\n\t"
      "s_barrier\n\t"
      "s_waitcnt lgkmcnt(11)\n\t"
      "v_mfma_f32_16x16x32_bf16 v[0:3], v[104:107], v[96:99], v[0:3]\n\t"
      "ds_read_b64_tr_b16 v[76:77], v166\n\t"
      "v_mfma_f32_16x16x32_bf16 v[8:11], v[104:107], v[100:103], v[8:11]\n\t"
      "ds_read_b64_tr_b16 v[78:79], v166 offset:512\n\t"
      "v_mfma_f32_16x16x32_bf16 v[4:7], v[108:111], v[96:99], v[4:7]\n\t"
      "ds_read_b128 v[68:71], v160 offset:4096\n\t"
      "v_mfma_f32_16x16x32_bf16 v[12:15], v[108:111], v[100:103], v[12:15]\n\t"
      "ds_read_b128 v[72:75], v160 offset:18432\n\t"
      "v_mfma_f32_16x16x32_bf16 v[24:27], v[112:115], v[96:99], v[24:27]\n\t"
      "ds_read_b128 v[64:67], v162 offset:4096\n\t"
      "v_mfma_f32_16x16x32_bf16 v[32:35], v[112:115], v[100:103], v[32:35]\n\t"
      "ds_read_b64_tr_b16 v[80:81], v167\n\t"
      "v_mfma_f32_16x16x32_bf16 v[16:19], v[112:115], v[92:95], v[16:19]\n\t"
      "ds_read_b64_tr_b16 v[82:83], v167 offset:512\n\t"
      "v_mfma_f32_16x16x32_bf16 v[28:31], v[116:119], v[96:99], v[28:31]\n\t"
      "v_mfma_f32_16x16x32_bf16 v[36:39], v[116:119], v[100:103], v[36:39]\n\t"
      "v_mfma_f32_16x16x32_bf16 v[20:23], v[116:119], v[92:95], v[20:23]\n\t"
      "s_waitcnt lgkmcnt(7)\n\t"
      "v_mfma_f32_16x16x32_bf16 v[0:3], v[132:135], v[124:127], v[0:3]\n\t"
      "ds_read_b64_tr_b16 v[104:105], v166 offset:1024\n\t"
      "v_mfma_f32_16x16x32_bf16 v[8:11], v[132:135], v[128:131], v[8:11]\n\t"
      "ds_read_b64_tr_b16 v[106:107], v166 offset:1536\n\t"
      "v_mfma_f32_16x16x32_bf16 v[4:7], v[136:139], v[124:127], v[4:7]\n\t"
      "ds_read_b128 v[96:99], v161 offset:5120\n\t"
      "v_mfma_f32_16x16x32_bf16 v[12:15], v[136:139], v[128:131], v[12:15]\n\t"
      "ds_read_b128 v[100:103], v161 offset:19456\n\t"
      "v_mfma_f32_16x16x32_bf16 v[24:27], v[140:143], v[124:127], v[24:27]\n\t"
      "ds_read_b128 v[92:95], v163 offset:5120\n\t"
      "v_mfma_f32_16x16x32_bf16 v[32:35], v[140:143], v[128:131], v[32:35]\n\t"
      "ds_read_b64_tr_b16 v[108:109], v167 offset:1024\n\t"
      "v_mfma_f32_16x16x32_bf16 v[16:19], v[140:143], v[120:123], v[16:19]\n\t"
      "ds_read_b64_tr_b16 v[110:111], v167 offset:1536\n\t"
      "v_mfma_f32_16x16x32_bf16 v[28:31], v[144:147], v[124:127], v[28:31]\n\t"
      "v_mfma_f32_16x16x32_bf16 v[36:39], v[144:147], v[128:131], v[36:39]\n\t"
      "v_mfma_f32_16x16x32_bf16 v[20:23], v[144:147], v[120:123], v[20:23]\n\t"
      "s_waitcnt lgkmcnt(7)\n\t"
      "v_mfma_f32_16x16x32_bf16 v[48:51], v[68:71], v[76:79], v[48:51]\n\t"
      "ds_read_b64_tr_b16 v[132:133], v166 offset:2048\n\t"
      "ds_read_b64_tr_b16 v[134:135], v166 offset:2560\n\t"
      "v_mfma_f32_16x16x32_bf16 v[56:59], v[72:75], v[76:79], v[56:59]\n\t"
      "ds_read_b128 v[124:127], v160 offset:6144\n\t"
      "v_mfma_f32_16x16x32_bf16 v[40:43], v[64:67], v[76:79], v[40:43]\n\t"
      "ds_read_b128 v[128:131], v160 offset:20480\n\t"
      "v_cvt_pk_bf16_f32 v84, v0, v1\n\t"
      "v_cvt_pk_bf16_f32 v85, v2, v3\n\t"
      "v_cvt_pk_bf16_f32 v86, v4, v5\n\t"
      "v_mfma_f32_16x16x32_bf16 v[52:55], v[68:71], v[80:83], v[52:55]\n\t"
      "ds_read_b128 v[120:123], v162 offset:6144\n\t"
      "v_cvt_pk_bf16_f32 v87, v6, v7\n\t"
      "v_cvt_pk_bf16_f32 v88, v8, v9\n\t"
      "v_cvt_pk_bf16_f32 v89, v10, v11\n\t"
      "v_mfma_f32_16x16x32_bf16 v[60:63], v[72:75], v[80:83], v[60:63]\n\t"
      "ds_read_b64_tr_b16 v[136:137], v167 offset:2048\n\t"
      "v_cvt_pk_bf16_f32 v90, v12, v13\n\t"
      "v_cvt_pk_bf16_f32 v91, v14, v15\n\t"
      "v_cvt_pk_bf16_f32 v152, v24, v25\n\t"
      "v_mfma_f32_16x16x32_bf16 v[44:47], v[64:67], v[80:83], v[44:47]\n\t"
      "ds_read_b64_tr_b16 v[138:139], v167 offset:2560\n\t"
      "v_cvt_pk_bf16_f32 v153, v26, v27\n\t"
      "v_cvt_pk_bf16_f32 v154, v28, v29\n\t"
      "v_cvt_pk_bf16_f32 v155, v30, v31\n\t"
      "s_waitcnt lgkmcnt(7)\n\t"
      "v_mfma_f32_16x16x32_bf16 v[48:51], v[96:99], v[104:107], v[48:51]\n\t"
      "ds_read_b64_tr_b16 v[76:77], v166 offset:3072\n\t"
      "ds_read_b64_tr_b16 v[78:79], v166 offset:3584\n\t"
      "v_cndmask_b32_e64 v86, v86, 0, s[68:69]\n\t"
      "v_cndmask_b32_e64 v87, v87, 0, s[68:69]\n\t"
      "v_cvt_pk_bf16_f32 v156, v32, v33\n\t"
      "v_mfma_f32_16x16x32_bf16 v[56:59], v[100:103], v[104:107], v[56:59]\n\t"
      "ds_read_b128 v[68:71], v161 offset:7168\n\t"
      "v_cvt_pk_bf16_f32 v157, v34, v35\n\t"
      "v_cvt_pk_bf16_f32 v158, v36, v37\n\t"
      "v_cvt_pk_bf16_f32 v159, v38, v39\n\t"
      "v_mfma_f32_16x16x32_bf16 v[40:43], v[92:95], v[104:107], v[40:43]\n\t"
      "ds_read_b128 v[72:75], v161 offset:21504\n\t"
      "v_cndmask_b32_e64 v90, v90, 0, s[68:69]\n\t"
      "v_cndmask_b32_e64 v91, v91, 0, s[68:69]\n\t"
      "v_cvt_pk_bf16_f32 v148, v16, v17\n\t"
      "v_mfma_f32_16x16x32_bf16 v[52:55], v[96:99], v[108:111], v[52:55]\n\t"
      "ds_read_b128 v[64:67], v165 offset:7168\n\t"
      "v_cvt_pk_bf16_f32 v149, v18, v19\n\t"
      "v_cvt_pk_bf16_f32 v150, v20, v21\n\t"
      "v_cvt_pk_bf16_f32 v151, v22, v23\n\t"
      "v_mfma_f32_16x16x32_bf16 v[60:63], v[100:103], v[108:111], v[60:63]\n\t"
      "ds_read_b64_tr_b16 v[80:81], v167 offset:3072\n\t"
      "v_mov_b32_e32 v34, 0x3e8b97f6\n\t"
      "v_mfma_f32_16x16x32_bf16 v[28:31], v[156:159], v[88:91], v[176:179]\n\t"
      "v_mfma_f32_16x16x32_bf16 v[24:27], v[152:155], v[88:91], v[172:175]\n\t"
      "v_mfma_f32_16x16x32_bf16 v[44:47], v[92:95], v[108:111], v[44:47]\n\t"
      "ds_read_b64_tr_b16 v[82:83], v167 offset:3584\n\t"
      "v_mfma_f32_16x16x32_bf16 v[20:23], v[152:155], v[84:87], v[176:179]\n\t"
      "v_mfma_f32_16x16x32_bf16 v[16:19], v[148:151], v[84:87], v[180:183]\n\t"
      "s_barrier\n\t"
      "s_waitcnt lgkmcnt(7)\n\t"
      "v_mfma_f32_16x16x32_bf16 v[48:51], v[124:127], v[132:135], v[48:51]\n\t"
      "ds_read_b64_tr_b16 v[104:105], v166 offset:16384\n\t"
      "ds_read_b64_tr_b16 v[106:107], v166 offset:16896\n\t"
      "v_mfma_f32_16x16x32_bf16 v[56:59], v[128:131], v[132:135], v[56:59]\n\t"
      "ds_read_b128 v[96:99], v160 offset:8192\n\t"
      "v_max_f32_e32 v24, v24, v28\n\t"
      "v_mfma_f32_16x16x32_bf16 v[40:43], v[120:123], v[132:135], v[40:43]\n\t"
      "ds_read_b128 v[100:103], v160 offset:22528\n\t"
      "v_max_f32_e32 v25, v25, v29\n\t"
      "v_max_f32_e32 v26, v26, v30\n\t"
      "v_max_f32_e32 v27, v27, v31\n\t"
      "v_mfma_f32_16x16x32_bf16 v[52:55], v[124:127], v[136:139], v[52:55]\n\t"
      "ds_read_b128 v[92:95], v164 offset:8192\n\t"
      "v_max_f32_e32 v16, v16, v20\n\t"
      "v_max_f32_e32 v17, v17, v21\n\t"
      "v_max_f32_e32 v18, v18, v22\n\t"
      "v_mfma_f32_16x16x32_bf16 v[60:63], v[128:131], v[136:139], v[60:63]\n\t"
      "ds_read_b64_tr_b16 v[108:109], v167 offset:16384\n\t"
      "v_max_f32_e32 v19, v19, v23\n\t"
      "v_max3_f32 v33, v24, v25, v26\n\t"
      "v_max3_f32 v32, v16, v17, v18\n\t"
      "v_mfma_f32_16x16x32_bf16 v[44:47], v[120:123], v[136:139], v[44:47]\n\t"
      "ds_read_b64_tr_b16 v[110:111], v167 offset:16896\n\t"
      "v_max_f32_e32 v33, v33, v27\n\t"
      "v_max_f32_e32 v32, v32, v19\n\t"
      "s_waitcnt lgkmcnt(7)\n\t"
      "v_mfma_f32_16x16x32_bf16 v[48:51], v[68:71], v[76:79], v[48:51]\n\t"
      "ds_read_b64_tr_b16 v[132:133], v166 offset:17408\n\t"
      "ds_read_b64_tr_b16 v[134:135], v166 offset:17920\n\t"
      "v_permlane16_swap_b32_e32 v32, v33\n\t"
      "v_max_f32_e32 v32, v32, v33\n\t"
      "v_mov_b32_e32 v33, v32\n\t"
      "v_mfma_f32_16x16x32_bf16 v[56:59], v[72:75], v[76:79], v[56:59]\n\t"
      "ds_read_b128 v[124:127], v161 offset:9216\n\t"
      "v_permlane32_swap_b32_e32 v32, v33\n\t"
      "v_max_f32_e32 v32, v32, v33\n\t"
      "v_mfma_f32_16x16x32_bf16 v[40:43], v[64:67], v[76:79], v[40:43]\n\t"
      "ds_read_b128 v[128:131], v161 offset:23552\n\t"
      "v_mov_b32_e32 v33, v32\n\t"
      "s_nop 1\n\t"
      "v_permlane16_swap_b32_e32 v32, v33\n\t"
      "v_mfma_f32_16x16x32_bf16 v[52:55], v[68:71], v[80:83], v[52:55]\n\t"
      "ds_read_b128 v[120:123], v165 offset:9216\n\t"
      "v_mul_f32_e64 v33, -v33, v34\n\t"
      "v_mul_f32_e64 v32, -v32, v34\n\t"
      "v_fma_f32 v24, v24, v34, v33\n\t"
      "v_mfma_f32_16x16x32_bf16 v[60:63], v[72:75], v[80:83], v[60:63]\n\t"
      "ds_read_b64_tr_b16 v[136:137], v167 offset:17408\n\t"
      "v_fma_f32 v25, v25, v34, v33\n\t"
      "v_fma_f32 v26, v26, v34, v33\n\t"
      "v_fma_f32 v27, v27, v34, v33\n\t"
      "v_mfma_f32_16x16x32_bf16 v[44:47], v[64:67], v[80:83], v[44:47]\n\t"
      "ds_read_b64_tr_b16 v[138:139], v167 offset:17920\n\t"
      "v_fma_f32 v16, v16, v34, v32\n\t"
      "v_fma_f32 v17, v17, v34, v32\n\t"
      "v_fma_f32 v18, v18, v34, v32\n\t"
      "s_waitcnt lgkmcnt(7)\n\t"
      "v_mfma_f32_16x16x32_bf16 v[48:51], v[96:99], v[104:107], v[48:51]\n\t"
      "ds_read_b64_tr_b16 v[76:77], v166 offset:18432\n\t"
      "ds_read_b64_tr_b16 v[78:79], v166 offset:18944\n\t"
      "v_fma_f32 v19, v19, v34, v32\n\t"
      "v_exp_f32_e32 v24, v24\n\t"
      "v_mfma_f32_16x16x32_bf16 v[56:59], v[100:103], v[104:107], v[56:59]\n\t"
      "ds_read_b128 v[68:71], v160 offset:10240\n\t"
      "v_exp_f32_e32 v25, v25\n\t"
      "v_exp_f32_e32 v26, v26\n\t"
      "v_mfma_f32_16x16x32_bf16 v[40:43], v[92:95], v[104:107], v[40:43]\n\t"
      "ds_read_b128 v[72:75], v160 offset:24576\n\t"
      "v_exp_f32_e32 v27, v27\n\t"
      "v_mfma_f32_16x16x32_bf16 v[52:55], v[96:99], v[108:111], v[52:55]\n\t"
      "ds_read_b128 v[64:67], v164 offset:10240\n\t"
      "v_exp_f32_e32 v16, v16\n\t"
      "v_exp_f32_e32 v17, v17\n\t"
      "v_mfma_f32_16x16x32_bf16 v[60:63], v[100:103], v[108:111], v[60:63]\n\t"
      "ds_read_b64_tr_b16 v[80:81], v167 offset:18432\n\t"
      "v_exp_f32_e32 v18, v18\n\t"
      "v_mfma_f32_16x16x32_bf16 v[44:47], v[92:95], v[108:111], v[44:47]\n\t"
      "ds_read_b64_tr_b16 v[82:83], v167 offset:18944\n\t"
      "v_exp_f32_e32 v19, v19\n\t"
      "v_add_f32_e32 v33, v24, v25\n\t"
      "s_waitcnt lgkmcnt(7)\n\t"
      "v_mfma_f32_16x16x32_bf16 v[48:51], v[124:127], v[132:135], v[48:51]\n\t"
      "ds_read_b64_tr_b16 v[104:105], v166 offset:19456\n\t"
      "ds_read_b64_tr_b16 v[106:107], v166 offset:19968\n\t"
      "v_add_f32_e32 v28, v26, v27\n\t"
      "v_add_f32_e32 v32, v16, v17\n\t"
      "v_add_f32_e32 v20, v18, v19\n\t"
      "v_mfma_f32_16x16x32_bf16 v[56:59], v[128:131], v[132:135], v[56:59]\n\t"
      "ds_read_b128 v[96:99], v161 offset:11264\n\t"
      "v_add_f32_e32 v33, v33, v28\n\t"
      "v_add_f32_e32 v32, v32, v20\n\t"
      "v_mfma_f32_16x16x32_bf16 v[40:43], v[120:123], v[132:135], v[40:43]\n\t"
      "ds_read_b128 v[100:103], v161 offset:25600\n\t"
      "v_permlane16_swap_b32_e32 v32, v33\n\t"
      "v_add_f32_e32 v32, v32, v33\n\t"
      "v_mov_b32_e32 v33, v32\n\t"
      "v_mfma_f32_16x16x32_bf16 v[52:55], v[124:127], v[136:139], v[52:55]\n\t"
      "ds_read_b128 v[92:95], v165 offset:11264\n\t"
      "v_permlane32_swap_b32_e32 v32, v33\n\t"
      "v_add_f32_e32 v32, v32, v33\n\t"
      "v_mfma_f32_16x16x32_bf16 v[60:63], v[128:131], v[136:139], v[60:63]\n\t"
      "ds_read_b64_tr_b16 v[108:109], v167 offset:19456\n\t"
      "v_mov_b32_e32 v33, v32\n\t"
      "s_nop 1\n\t"
      "v_permlane16_swap_b32_e32 v32, v33\n\t"
      "v_mfma_f32_16x16x32_bf16 v[44:47], v[120:123], v[136:139], v[44:47]\n\t"
      "ds_read_b64_tr_b16 v[110:111], v167 offset:19968\n\t"
      "v_rcp_f32_e32 v33, v33\n\t"
      "v_rcp_f32_e32 v32, v32\n\t"
      "s_barrier\n\t"
      "s_waitcnt lgkmcnt(7)\n\t"
      "v_mfma_f32_16x16x32_bf16 v[48:51], v[68:71], v[76:79], v[48:51]\n\t"
      "ds_read_b64_tr_b16 v[132:133], v170 offset:32768\n\t"
      "ds_read_b64_tr_b16 v[134:135], v170 offset:33280\n\t"
      "v_mul_f32_e32 v24, v24, v33\n\t"
      "v_mul_f32_e32 v25, v25, v33\n\t"
      "v_mfma_f32_16x16x32_bf16 v[56:59], v[72:75], v[76:79], v[56:59]\n\t"
      "ds_read_b128 v[124:127], v160 offset:12288\n\t"
      "v_mul_f32_e32 v26, v26, v33\n\t"
      "v_mul_f32_e32 v27, v27, v33\n\t"
      "v_mul_f32_e32 v16, v16, v32\n\t"
      "v_mfma_f32_16x16x32_bf16 v[40:43], v[64:67], v[76:79], v[40:43]\n\t"
      "ds_read_b128 v[128:131], v160 offset:26624\n\t"
      "v_mul_f32_e32 v17, v17, v32\n\t"
      "v_mul_f32_e32 v18, v18, v32\n\t"
      "v_mul_f32_e32 v19, v19, v32\n\t"
      "v_mfma_f32_16x16x32_bf16 v[52:55], v[68:71], v[80:83], v[52:55]\n\t"
      "ds_read_b128 v[120:123], v164 offset:12288\n\t"
      "v_cvt_pk_bf16_f32 v4, v24, v25\n\t"
      "v_cvt_pk_bf16_f32 v5, v26, v27\n\t"
      "v_cvt_pk_bf16_f32 v0, v16, v17\n\t"
      "v_mfma_f32_16x16x32_bf16 v[60:63], v[72:75], v[80:83], v[60:63]\n\t"
      "ds_read_b64_tr_b16 v[136:137], v171 offset:32768\n\t"
      "v_cvt_pk_bf16_f32 v1, v18, v19\n\t"
      "v_and_b32_e32 v6, v184, v4\n\t"
      "v_and_b32_e32 v7, v185, v5\n\t"
      "v_mfma_f32_16x16x32_bf16 v[44:47], v[64:67], v[80:83], v[44:47]\n\t"
      "ds_read_b64_tr_b16 v[138:139], v171 offset:33280\n\t"
      "v_and_b32_e32 v2, v184, v0\n\t"
      "v_and_b32_e32 v3, v185, v1\n\t"
      "v_xor_b32_e32 v4, v4, v6\n\t"
      "s_waitcnt lgkmcnt(7)\n\t"
      "v_mfma_f32_16x16x32_bf16 v[48:51], v[96:99], v[104:107], v[48:51]\n\t"
      "ds_read_b64_tr_b16 v[76:77], v170 offset:33792\n\t"
      "ds_read_b64_tr_b16 v[78:79], v170 offset:34304\n\t"
      "v_xor_b32_e32 v5, v5, v7\n\t"
      "v_xor_b32_e32 v0, v0, v2\n\t"
      "v_xor_b32_e32 v1, v1, v3\n\t"
      "v_mfma_f32_16x16x32_bf16 v[56:59], v[100:103], v[104:107], v[56:59]\n\t"
      "ds_read_b128 v[68:71], v161 offset:13312\n\t"
      "v_mfma_f32_16x16x32_bf16 v[40:43], v[92:95], v[104:107], v[40:43]\n\t"
      "ds_read_b128 v[72:75], v161 offset:27648\n\t"
      "v_mfma_f32_16x16x32_bf16 v[52:55], v[96:99], v[108:111], v[52:55]\n\t"
      "ds_read_b128 v[64:67], v165 offset:13312\n\t"
      "v_mfma_f32_16x16x32_bf16 v[60:63], v[100:103], v[108:111], v[60:63]\n\t"
      "ds_read_b64_tr_b16 v[80:81], v171 offset:33792\n\t"
      "v_mfma_f32_16x16x32_bf16 v[44:47], v[92:95], v[108:111], v[44:47]\n\t"
      "ds_read_b64_tr_b16 v[82:83], v171 offset:34304\n\t"
      "s_waitcnt lgkmcnt(7)\n\t"
      "v_mfma_f32_16x16x32_bf16 v[48:51], v[124:127], v[132:135], v[48:51]\n\t"
      "v_mfma_f32_16x16x32_bf16 v[56:59], v[128:131], v[132:135], v[56:59]\n\t"
      "v_mfma_f32_16x16x32_bf16 v[40:43], v[120:123], v[132:135], v[40:43]\n\t"
      "v_mfma_f32_16x16x32_bf16 v[52:55], v[124:127], v[136:139], v[52:55]\n\t"
      "v_mfma_f32_16x16x32_bf16 v[60:63], v[128:131], v[136:139], v[60:63]\n\t"
      "v_mfma_f32_16x16x32_bf16 v[44:47], v[120:123], v[136:139], v[44:47]\n\t"
      "s_waitcnt lgkmcnt(0)\n\t"
      "v_mfma_f32_16x16x32_bf16 v[48:51], v[68:71], v[76:79], v[48:51]\n\t"
      "v_mfma_f32_16x16x32_bf16 v[56:59], v[72:75], v[76:79], v[56:59]\n\t"
      "v_mfma_f32_16x16x32_bf16 v[40:43], v[64:67], v[76:79], v[40:43]\n\t"
      "v_mfma_f32_16x16x32_bf16 v[52:55], v[68:71], v[80:83], v[52:55]\n\t"
      "v_mfma_f32_16x16x32_bf16 v[60:63], v[72:75], v[80:83], v[60:63]\n\t"
      "v_mfma_f32_16x16x32_bf16 v[44:47], v[64:67], v[80:83], v[44:47]\n\t"
      "s_nop 2\n\t"
      "v_cvt_pk_bf16_f32 v66, v48, v49\n\t"
      "v_cvt_pk_bf16_f32 v67, v50, v51\n\t"
      "v_cvt_pk_bf16_f32 v68, v56, v57\n\t"
      "v_cvt_pk_bf16_f32 v69, v58, v59\n\t"
      "v_cvt_pk_bf16_f32 v64, v40, v41\n\t"
      "v_cvt_pk_bf16_f32 v65, v42, v43\n\t"
      "v_mfma_f32_16x16x32_bf16 v[88:91], v[66:69], v[4:7], 0\n\t"
      "s_nop 0\n\t"
      "v_mfma_f32_16x16x32_bf16 v[80:83], v[64:67], v[0:3], 0\n\t"
      "v_cvt_pk_bf16_f32 v72, v52, v53\n\t"
      "v_cvt_pk_bf16_f32 v73, v54, v55\n\t"
      "v_cvt_pk_bf16_f32 v74, v60, v61\n\t"
      "v_cvt_pk_bf16_f32 v75, v62, v63\n\t"
      "v_cvt_pk_bf16_f32 v70, v44, v45\n\t"
      "v_cvt_pk_bf16_f32 v71, v46, v47\n\t"
      "v_mfma_f32_16x16x32_bf16 v[92:95], v[72:75], v[4:7], 0\n\t"
      "s_nop 0\n\t"
      "v_mfma_f32_16x16x32_bf16 v[84:87], v[70:73], v[0:3], 0\n\t"
      "v_cvt_pk_bf16_f32 v152, v88, v89\n\t"
      "v_cvt_pk_bf16_f32 v153, v90, v91\n\t"
      "v_min_u32_e32 v152, -2, v152\n\t"
      "v_min_u32_e32 v153, -2, v153\n\t"
      "v_cvt_pk_bf16_f32 v148, v80, v81\n\t"
      "v_cvt_pk_bf16_f32 v149, v82, v83\n\t"
      "v_min_u32_e32 v148, -2, v148\n\t"
      "v_min_u32_e32 v149, -2, v149\n\t"
      "v_cvt_pk_bf16_f32 v154, v92, v93\n\t"
      "v_cvt_pk_bf16_f32 v155, v94, v95\n\t"
      "v_cndmask_b32_e64 v154, v154, 0, s[68:69]\n\t"
      "v_min_u32_e32 v154, -2, v154\n\t"
      "v_cndmask_b32_e64 v155, v155, 0, s[68:69]\n\t"
      "v_min_u32_e32 v155, -2, v155\n\t"
      "global_store_dwordx4 v244, v[152:155], s[44:45] sc1\n\t"
      "v_cvt_pk_bf16_f32 v150, v84, v85\n\t"
      "v_cvt_pk_bf16_f32 v151, v86, v87\n\t"
      "v_cndmask_b32_e64 v150, v150, 0, s[68:69]\n\t"
      "v_min_u32_e32 v150, -2, v150\n\t"
      "v_cndmask_b32_e64 v151, v151, 0, s[68:69]\n\t"
      "v_min_u32_e32 v151, -2, v151\n\t"
      "global_store_dwordx4 v244, v[148:151], s[42:43] sc1\n\t"
      "s_barrier\n\t"
      "s_cmp_eq_u32 s26, 7\n\t"
      "s_cbranch_scc1 SPARE_%=\n\t"
      "s_lshl_b32 s34, s25, 16\n\t"
      "s_lshl2_add_u32 s34, s39, s34\n\t"
      "s_add_u32 s34, s34, 4096\n\t"
      "s_add_u32 s42, s20, s34\n\t"
      "s_addc_u32 s43, s21, 0\n\t"
      "s_add_u32 s44, s42, 8192\n\t"
      "s_addc_u32 s45, s43, 0\n\t"
      "global_load_dwordx4 v[208:211], v228, s[4:5]\n\t"
      "global_load_dwordx4 v[212:215], v228, s[4:5] offset:64\n\t"
      "s_lshr_b32 s35, s2, 6\n\t"
      "s_lshl2_add_u32 s31, s26, s35\n\t"
      "s_and_b32 s35, s2, 7\n\t"
      "s_mul_i32 s34, s35, 14\n\t"
      "s_lshl4_add_u32 s35, s35, -14\n\t"
      "s_cmp_lt_u32 s31, 14\n\t"
      "s_cselect_b32 s34, s34, s35\n\t"
      "s_cselect_b32 s36, 1, 0\n\t"
      "s_or_b32 s27, s27, s36\n\t"
      "s_add_u32 s34, s34, s31\n\t"
      "s_lshr_b32 s29, s34, 2\n\t"
      "s_and_b32 s30, s34, 3\n\t"
      "s_mul_i32 s30, s30, 7\n\t"
      "v_and_b32_e32 v199, 60, v192\n\t"
      "v_and_b32_e32 v200, 24, v192\n\t"
      "v_lshlrev_b32_e32 v202, 3, v196\n\t"
      "v_lshl_or_b32 v202, v200, 2, v202\n\t"
      "v_lshl_or_b32 v202, v195, 7, v202\n\t"
      "v_lshl_or_b32 v202, v193, 10, v202\n\t"
      "v_add_u32_e32 v224, s39, v202\n\t"
      "v_xor_b32_e32 v225, 32, v224\n\t"
      "v_xor_b32_e32 v226, 64, v224\n\t"
      "v_xor_b32_e32 v227, 96, v224\n\t"
      "s_lshl_b32 s34, s24, 12\n\t"
      "s_add_u32 s34, s34, 98304\n\t"
      "v_add_u32_e32 v245, s34, v244\n\t"
      "v_lshlrev_b32_e32 v199, 2, v199\n\t"
      "s_lshl_b32 s34, s24, 8\n\t"
      "v_lshl_add_u32 v247, v192, 2, s34\n\t"
      "v_and_b32_e32 v200, 2, v192\n\t"
      "v_mul_u32_u24_e32 v202, 576, v200\n\t"
      "v_xor_b32_e32 v199, v202, v199\n\t"
      "v_and_b32_e32 v202, 1, v192\n\t"
      "v_lshl_or_b32 v199, v202, 3, v199\n\t"
      "s_and_b32 s35, s34, 0x400\n\t"
      "s_add_u32 s34, s34, s35\n\t"
      "s_add_u32 s34, s34, 98304\n\t"
      "v_add_u32_e32 v246, s34, v199\n\t"
      "v_lshlrev_b32_e32 v199, 2, v202\n\t"
      "v_lshl_or_b32 v199, v200, 4, v199\n\t"
      "s_barrier\n\t"
      "ds_read_b64_tr_b16 v[0:1], v216\n\t"
      "ds_read_b64_tr_b16 v[2:3], v220\n\t"
      "ds_read_b64_tr_b16 v[4:5], v217\n\t"
      "ds_read_b64_tr_b16 v[6:7], v221\n\t"
      "ds_read_b64_tr_b16 v[8:9], v218\n\t"
      "ds_read_b64_tr_b16 v[10:11], v222\n\t"
      "ds_read_b64_tr_b16 v[12:13], v219\n\t"
      "ds_read_b64_tr_b16 v[14:15], v223\n\t"
      "ds_read_b64_tr_b16 v[16:17], v218 offset:3584\n\t"
      "ds_read_b64_tr_b16 v[18:19], v222 offset:3584\n\t"
      "ds_read_b64_tr_b16 v[20:21], v219 offset:3584\n\t"
      "ds_read_b64_tr_b16 v[22:23], v223 offset:3584\n\t"
      "ds_read_b64_tr_b16 v[24:25], v216 offset:3584\n\t"
      "ds_read_b64_tr_b16 v[26:27], v220 offset:3584\n\t"
      "ds_read_b64_tr_b16 v[28:29], v217 offset:3584\n\t"
      "ds_read_b64_tr_b16 v[30:31], v221 offset:3584\n\t"
      "ds_read_b64_tr_b16 v[32:33], v216 offset:7168\n\t"
      "ds_read_b64_tr_b16 v[34:35], v220 offset:7168\n\t"
      "ds_read_b64_tr_b16 v[36:37], v217 offset:7168\n\t"
      "ds_read_b64_tr_b16 v[38:39], v221 offset:7168\n\t"
      "ds_read_b64_tr_b16 v[40:41], v218 offset:7168\n\t"
      "ds_read_b64_tr_b16 v[42:43], v222 offset:7168\n\t"
      "ds_read_b64_tr_b16 v[44:45], v219 offset:7168\n\t"
      "ds_read_b64_tr_b16 v[46:47], v223 offset:7168\n\t"
      "ds_read_b64_tr_b16 v[48:49], v218 offset:10752\n\t"
      "ds_read_b64_tr_b16 v[50:51], v222 offset:10752\n\t"
      "ds_read_b64_tr_b16 v[52:53], v219 offset:10752\n\t"
      "ds_read_b64_tr_b16 v[54:55], v223 offset:10752\n\t"
      "ds_read_b64_tr_b16 v[56:57], v216 offset:10752\n\t"
      "ds_read_b64_tr_b16 v[58:59], v220 offset:10752\n\t"
      "ds_read_b64_tr_b16 v[60:61], v217 offset:10752\n\t"
      "ds_read_b64_tr_b16 v[62:63], v221 offset:10752\n\t"
      "ds_read_b64_tr_b16 v[64:65], v216 offset:14336\n\t"
      "ds_read_b64_tr_b16 v[66:67], v220 offset:14336\n\t"
      "ds_read_b64_tr_b16 v[68:69], v217 offset:14336\n\t"
      "ds_read_b64_tr_b16 v[70:71], v221 offset:14336\n\t"
      "ds_read_b64_tr_b16 v[72:73], v218 offset:14336\n\t"
      "ds_read_b64_tr_b16 v[74:75], v222 offset:14336\n\t"
      "ds_read_b64_tr_b16 v[76:77], v219 offset:14336\n\t"
      "ds_read_b64_tr_b16 v[78:79], v223 offset:14336\n\t"
      "ds_read_b64_tr_b16 v[80:81], v218 offset:17920\n\t"
      "ds_read_b64_tr_b16 v[82:83], v222 offset:17920\n\t"
      "ds_read_b64_tr_b16 v[84:85], v219 offset:17920\n\t"
      "ds_read_b64_tr_b16 v[86:87], v223 offset:17920\n\t"
      "ds_read_b64_tr_b16 v[88:89], v216 offset:17920\n\t"
      "ds_read_b64_tr_b16 v[90:91], v220 offset:17920\n\t"
      "ds_read_b64_tr_b16 v[92:93], v217 offset:17920\n\t"
      "ds_read_b64_tr_b16 v[94:95], v221 offset:17920\n\t"
      "ds_read_b64_tr_b16 v[96:97], v216 offset:21504\n\t"
      "ds_read_b64_tr_b16 v[98:99], v220 offset:21504\n\t"
      "ds_read_b64_tr_b16 v[100:101], v217 offset:21504\n\t"
      "ds_read_b64_tr_b16 v[102:103], v221 offset:21504\n\t"
      "ds_read_b64_tr_b16 v[104:105], v218 offset:21504\n\t"
      "ds_read_b64_tr_b16 v[106:107], v222 offset:21504\n\t"
      "ds_read_b64_tr_b16 v[108:109], v219 offset:21504\n\t"
      "ds_read_b64_tr_b16 v[110:111], v223 offset:21504\n\t"
      "ds_read_b64_tr_b16 v[112:113], v218 offset:25088\n\t"
      "ds_read_b64_tr_b16 v[114:115], v222 offset:25088\n\t"
      "ds_read_b64_tr_b16 v[116:117], v219 offset:25088\n\t"
      "ds_read_b64_tr_b16 v[118:119], v223 offset:25088\n\t"
      "ds_read_b64_tr_b16 v[120:121], v216 offset:25088\n\t"
      "ds_read_b64_tr_b16 v[122:123], v220 offset:25088\n\t"
      "ds_read_b64_tr_b16 v[124:125], v217 offset:25088\n\t"
      "ds_read_b64_tr_b16 v[126:127], v221 offset:25088\n\t"
      "global_load_dwordx4 v[128:131], v244, s[42:43] offset:-4096 sc1\n\t"
      "global_load_dwordx4 v[132:135], v244, s[42:43] offset:-3072 sc1\n\t"
      "global_load_dwordx4 v[136:139], v244, s[42:43] offset:-2048 sc1\n\t"
      "global_load_dwordx4 v[140:143], v244, s[42:43] offset:-1024 sc1\n\t"
      "global_load_dwordx4 v[144:147], v244, s[42:43] sc1\n\t"
      "global_load_dwordx4 v[148:151], v244, s[42:43] offset:1024 sc1\n\t"
      "global_load_dwordx4 v[152:155], v244, s[42:43] offset:2048 sc1\n\t"
      "global_load_dwordx4 v[156:159], v244, s[42:43] offset:3072 sc1\n\t"
      "global_load_dwordx4 v[160:163], v244, s[44:45] offset:-4096 sc1\n\t"
      "global_load_dwordx4 v[164:167], v244, s[44:45] offset:-3072 sc1\n\t"
      "s_min_u32 s34, s29, 27\n\t"
      "s_lshl_b32 s34, s34, 7\n\t"
      "s_lshr_b32 s35, s24, 2\n\t"
      "s_lshl_b32 s35, s35, 6\n\t"
      "s_add_u32 s34, s34, s35\n\t"
      "s_lshr_b32 s35, s39, 9\n\t"
      "s_add_u32 s34, s34, s35\n\t"
      "v_add_u32_e32 v248, s34, v199\n\t"
      "s_add_u32 s48, s20, 8388608\n\t"
      "s_addc_u32 s49, s21, 0\n\t"
      "v_and_b32_e32 v199, 16, v192\n\t"
      "v_lshlrev_b32_e32 v199, 1, v199\n\t"
      "v_and_b32_e32 v200, 32, v192\n\t"
      "s_lshl_b32 s34, s28, 18\n\t"
      "s_lshl_b32 s35, s25, 11\n\t"
      "s_add_u32 s34, s34, s35\n\t"
      "s_lshr_b32 s35, s39, 3\n\t"
      "s_add_u32 s34, s34, s35\n\t"
      "v_and_b32_e32 v201, 0x1f0, v244\n\t"
      "v_lshl_or_b32 v201, v200, 11, v201\n\t"
      "v_add_u32_e32 v229, s34, v201\n\t"
      "v_add_u32_e32 v230, 131072, v229\n\t"
      "s_and_b32 s34, s25, 30\n\t"
      "s_lshr_b32 s35, s39, 13\n\t"
      "s_add_u32 s34, s34, s35\n\t"
      "s_mul_i32 s34, s34, 7\n\t"
      "s_add_u32 s34, s34, s28\n\t"
      "s_lshl_b32 s34, s34, 3\n\t"
      "s_and_b32 s35, s25, 1\n\t"
      "s_add_u32 s34, s34, s35\n\t"
      "s_lshl_b32 s34, s34, 10\n\t"
      "s_and_b32 s35, s24, 1\n\t"
      "s_lshl_b32 s35, s35, 9\n\t"
      "s_add_u32 s34, s34, s35\n\t"
      "s_add_u32 s34, s34, 6293504\n\t"
      "v_lshrrev_b32_e32 v201, 1, v200\n\t"
      "v_lshl_or_b32 v201, v199, 6, v201\n\t"
      "v_lshl_or_b32 v201, v194, 5, v201\n\t"
      "v_add_u32_e32 v241, s34, v201\n\t"
      "s_lshl_b32 s34, s24, 12\n\t"
      "s_add_u32 s34, s34, 131072\n\t"
      "v_xor_b32_e32 v201, v193, v194\n\t"
      "v_lshlrev_b32_e32 v201, 3, v201\n\t"
      "v_lshl_or_b32 v201, v194, 7, v201\n\t"
      "v_add_u32_e32 v233, s34, v201\n\t"
      "v_lshrrev_b32_e32 v199, 2, v199\n\t"
      "v_or_b32_e32 v199, v199, v195\n\t"
      "v_lshrrev_b32_e32 v200, 3, v200\n\t"
      "v_or_b32_e32 v200, v196, v200\n\t"
      "v_xor_b32_e32 v200, v200, v199\n\t"
      "v_lshlrev_b32_e32 v200, 3, v200\n\t"
      "v_lshl_or_b32 v200, v199, 7, v200\n\t"
      "v_add_u32_e32 v237, s34, v200\n\t"
      "v_xor_b32_e32 v234, 32, v233\n\t"
      "v_xor_b32_e32 v238, 32, v237\n\t"
      "v_xor_b32_e32 v235, 64, v233\n\t"
      "v_xor_b32_e32 v239, 64, v237\n\t"
      "v_xor_b32_e32 v236, 96, v233\n\t"
      "v_xor_b32_e32 v240, 96, v237\n\t"
      "s_waitcnt vmcnt(10)\n\t"
      "v_permlane16_swap_b32_e32 v208, v210\n\t"
      "v_permlane16_swap_b32_e32 v209, v211\n\t"
      "v_permlane16_swap_b32_e32 v212, v214\n\t"
      "v_permlane16_swap_b32_e32 v213, v215\n\t"
      "v_lshlrev_b32_e32 v192, 16, v208\n\t"
      "v_and_b32_e32 v193, 0xffff0000, v208\n\t"
      "v_lshlrev_b32_e32 v194, 16, v209\n\t"
      "v_and_b32_e32 v195, 0xffff0000, v209\n\t"
      "v_lshlrev_b32_e32 v196, 16, v210\n\t"
      "v_and_b32_e32 v197, 0xffff0000, v210\n\t"
      "v_lshlrev_b32_e32 v198, 16, v211\n\t"
      "v_and_b32_e32 v199, 0xffff0000, v211\n\t"
      "v_lshlrev_b32_e32 v200, 16, v212\n\t"
      "v_and_b32_e32 v201, 0xffff0000, v212\n\t"
      "v_lshlrev_b32_e32 v202, 16, v213\n\t"
      "v_and_b32_e32 v203, 0xffff0000, v213\n\t"
      "v_lshlrev_b32_e32 v204, 16, v214\n\t"
      "v_and_b32_e32 v205, 0xffff0000, v214\n\t"
      "v_lshlrev_b32_e32 v206, 16, v215\n\t"
      "v_and_b32_e32 v207, 0xffff0000, v215\n\t"
      "global_load_dwordx4 v[168:171], v244, s[44:45] offset:-2048 sc1\n\t"
      "global_load_dwordx4 v[172:175], v244, s[44:45] offset:-1024 sc1\n\t"
      "global_load_dwordx4 v[176:179], v244, s[44:45] sc1\n\t"
      "global_load_dwordx4 v[180:183], v244, s[44:45] offset:1024 sc1\n\t"
      "global_load_dwordx4 v[184:187], v244, s[44:45] offset:2048 sc1\n\t"
      "global_load_dwordx4 v[188:191], v244, s[44:45] offset:3072 sc1\n\t"
      "global_load_dword v249, v248, s[12:13]\n\t"
      "s_barrier\n\t"
      "s_waitcnt lgkmcnt(0)\n\t"
      "s_waitcnt vmcnt(15)\n\t"
      "v_max3_u32 v232, v128, v129, v130\n\t"
      "v_max3_u32 v232, v232, v131, v132\n\t"
      "v_max3_u32 v232, v232, v133, v134\n\t"
      "v_max_u32_e32 v232, v232, v135\n\t"
      "v_cmp_eq_u32_e32 vcc, -1, v232\n\t"
      "s_cbranch_vccnz LO0R_%=\n\t"
      "LO0B_%=:\n\t"
      "v_mfma_f32_16x16x32_bf16 v[192:195], v[0:3], v[128:131], v[192:195]\n\t"
      "ds_read_b64_tr_b16 v[0:1], v216 offset:28672\n\t"
      "ds_read_b64_tr_b16 v[2:3], v220 offset:28672\n\t"
      "v_mfma_f32_16x16x32_bf16 v[196:199], v[4:7], v[128:131], v[196:199]\n\t"
      "ds_read_b64_tr_b16 v[4:5], v217 offset:28672\n\t"
      "v_mfma_f32_16x16x32_bf16 v[200:203], v[8:11], v[128:131], v[200:203]\n\t"
      "ds_read_b64_tr_b16 v[6:7], v221 offset:28672\n\t"
      "v_mfma_f32_16x16x32_bf16 v[204:207], v[12:15], v[128:131], v[204:207]\n\t"
      "ds_read_b64_tr_b16 v[8:9], v218 offset:28672\n\t"
      "ds_read_b64_tr_b16 v[10:11], v222 offset:28672\n\t"
      "v_mfma_f32_16x16x32_bf16 v[192:195], v[16:19], v[132:135], v[192:195]\n\t"
      "ds_read_b64_tr_b16 v[12:13], v219 offset:28672\n\t"
      "v_mfma_f32_16x16x32_bf16 v[196:199], v[20:23], v[132:135], v[196:199]\n\t"
      "ds_read_b64_tr_b16 v[14:15], v223 offset:28672\n\t"
      "v_mfma_f32_16x16x32_bf16 v[200:203], v[24:27], v[132:135], v[200:203]\n\t"
      "ds_read_b64_tr_b16 v[16:17], v218 offset:32256\n\t"
      "ds_read_b64_tr_b16 v[18:19], v222 offset:32256\n\t"
      "v_mfma_f32_16x16x32_bf16 v[204:207], v[28:31], v[132:135], v[204:207]\n\t"
      "ds_read_b64_tr_b16 v[20:21], v219 offset:32256\n\t"
      "s_waitcnt vmcnt(13)\n\t"
      "v_max3_u32 v232, v136, v137, v138\n\t"
      "v_max3_u32 v232, v232, v139, v140\n\t"
      "v_max3_u32 v232, v232, v141, v142\n\t"
      "v_max_u32_e32 v232, v232, v143\n\t"
      "v_cmp_eq_u32_e32 vcc, -1, v232\n\t"
      "s_cbranch_vccnz LO2R_%=\n\t"
      "LO2B_%=:\n\t"
      "v_mfma_f32_16x16x32_bf16 v[192:195], v[32:35], v[136:139], v[192:195]\n\t"
      "ds_read_b64_tr_b16 v[22:23], v223 offset:32256\n\t"
      "v_mfma_f32_16x16x32_bf16 v[196:199], v[36:39], v[136:139], v[196:199]\n\t"
      "ds_read_b64_tr_b16 v[24:25], v216 offset:32256\n\t"
      "ds_read_b64_tr_b16 v[26:27], v220 offset:32256\n\t"
      "v_mfma_f32_16x16x32_bf16 v[200:203], v[40:43], v[136:139], v[200:203]\n\t"
      "ds_read_b64_tr_b16 v[28:29], v217 offset:32256\n\t"
      "v_mfma_f32_16x16x32_bf16 v[204:207], v[44:47], v[136:139], v[204:207]\n\t"
      "ds_read_b64_tr_b16 v[30:31], v221 offset:32256\n\t"
      "v_mfma_f32_16x16x32_bf16 v[192:195], v[48:51], v[140:143], v[192:195]\n\t"
      "ds_read_b64_tr_b16 v[32:33], v216 offset:35840\n\t"
      "ds_read_b64_tr_b16 v[34:35], v220 offset:35840\n\t"
      "v_mfma_f32_16x16x32_bf16 v[196:199], v[52:55], v[140:143], v[196:199]\n\t"
      "ds_read_b64_tr_b16 v[36:37], v217 offset:35840\n\t"
      "v_mfma_f32_16x16x32_bf16 v[200:203], v[56:59], v[140:143], v[200:203]\n\t"
      "ds_read_b64_tr_b16 v[38:39], v221 offset:35840\n\t"
      "v_mfma_f32_16x16x32_bf16 v[204:207], v[60:63], v[140:143], v[204:207]\n\t"
      "ds_read_b64_tr_b16 v[40:41], v218 offset:35840\n\t"
      "ds_read_b64_tr_b16 v[42:43], v222 offset:35840\n\t"
      "s_waitcnt vmcnt(11)\n\t"
      "v_max3_u32 v232, v144, v145, v146\n\t"
      "v_max3_u32 v232, v232, v147, v148\n\t"
      "v_max3_u32 v232, v232, v149, v150\n\t"
      "v_max_u32_e32 v232, v232, v151\n\t"
      "v_cmp_eq_u32_e32 vcc, -1, v232\n\t"
      "s_cbranch_vccnz LO4R_%=\n\t"
      "LO4B_%=:\n\t"
      "v_mfma_f32_16x16x32_bf16 v[192:195], v[64:67], v[144:147], v[192:195]\n\t"
      "ds_read_b64_tr_b16 v[44:45], v219 offset:35840\n\t"
      "v_mfma_f32_16x16x32_bf16 v[196:199], v[68:71], v[144:147], v[196:199]\n\t"
      "ds_read_b64_tr_b16 v[46:47], v223 offset:35840\n\t"
      "v_mfma_f32_16x16x32_bf16 v[200:203], v[72:75], v[144:147], v[200:203]\n\t"
      "ds_read_b64_tr_b16 v[48:49], v218 offset:39424\n\t"
      "ds_read_b64_tr_b16 v[50:51], v222 offset:39424\n\t"
      "v_mfma_f32_16x16x32_bf16 v[204:207], v[76:79], v[144:147], v[204:207]\n\t"
      "ds_read_b64_tr_b16 v[52:53], v219 offset:39424\n\t"
      "v_mfma_f32_16x16x32_bf16 v[192:195], v[80:83], v[148:151], v[192:195]\n\t"
      "ds_read_b64_tr_b16 v[54:55], v223 offset:39424\n\t"
      "v_mfma_f32_16x16x32_bf16 v[196:199], v[84:87], v[148:151], v[196:199]\n\t"
      "ds_read_b64_tr_b16 v[56:57], v216 offset:39424\n\t"
      "ds_read_b64_tr_b16 v[58:59], v220 offset:39424\n\t"
      "v_mfma_f32_16x16x32_bf16 v[200:203], v[88:91], v[148:151], v[200:203]\n\t"
      "ds_read_b64_tr_b16 v[60:61], v217 offset:39424\n\t"
      "v_mfma_f32_16x16x32_bf16 v[204:207], v[92:95], v[148:151], v[204:207]\n\t"
      "ds_read_b64_tr_b16 v[62:63], v221 offset:39424\n\t"
      "s_waitcnt vmcnt(9)\n\t"
      "v_max3_u32 v232, v152, v153, v154\n\t"
      "v_max3_u32 v232, v232, v155, v156\n\t"
      "v_max3_u32 v232, v232, v157, v158\n\t"
      "v_max_u32_e32 v232, v232, v159\n\t"
      "v_cmp_eq_u32_e32 vcc, -1, v232\n\t"
      "s_cbranch_vccnz LO6R_%=\n\t"
      "LO6B_%=:\n\t"
      "v_mfma_f32_16x16x32_bf16 v[192:195], v[96:99], v[152:155], v[192:195]\n\t"
      "ds_read_b64_tr_b16 v[64:65], v216 offset:43008\n\t"
      "ds_read_b64_tr_b16 v[66:67], v220 offset:43008\n\t"
      "v_mfma_f32_16x16x32_bf16 v[196:199], v[100:103], v[152:155], v[196:199]\n\t"
      "ds_read_b64_tr_b16 v[68:69], v217 offset:43008\n\t"
      "v_mfma_f32_16x16x32_bf16 v[200:203], v[104:107], v[152:155], v[200:203]\n\t"
      "ds_read_b64_tr_b16 v[70:71], v221 offset:43008\n\t"
      "v_mfma_f32_16x16x32_bf16 v[204:207], v[108:111], v[152:155], v[204:207]\n\t"
      "ds_read_b64_tr_b16 v[72:73], v218 offset:43008\n\t"
      "ds_read_b64_tr_b16 v[74:75], v222 offset:43008\n\t"
      "v_mfma_f32_16x16x32_bf16 v[192:195], v[112:115], v[156:159], v[192:195]\n\t"
      "ds_read_b64_tr_b16 v[76:77], v219 offset:43008\n\t"
      "v_mfma_f32_16x16x32_bf16 v[196:199], v[116:119], v[156:159], v[196:199]\n\t"
      "ds_read_b64_tr_b16 v[78:79], v223 offset:43008\n\t"
      "v_mfma_f32_16x16x32_bf16 v[200:203], v[120:123], v[156:159], v[200:203]\n\t"
      "ds_read_b64_tr_b16 v[80:81], v218 offset:46592\n\t"
      "ds_read_b64_tr_b16 v[82:83], v222 offset:46592\n\t"
      "v_mfma_f32_16x16x32_bf16 v[204:207], v[124:127], v[156:159], v[204:207]\n\t"
      "ds_read_b64_tr_b16 v[84:85], v219 offset:46592\n\t"
      "s_waitcnt vmcnt(7)\n\t"
      "v_max3_u32 v232, v160, v161, v162\n\t"
      "v_max3_u32 v232, v232, v163, v164\n\t"
      "v_max3_u32 v232, v232, v165, v166\n\t"
      "v_max_u32_e32 v232, v232, v167\n\t"
      "v_cmp_eq_u32_e32 vcc, -1, v232\n\t"
      "s_cbranch_vccnz LO8R_%=\n\t"
      "LO8B_%=:\n\t"
      "s_waitcnt lgkmcnt(14)\n\t"
      "v_mfma_f32_16x16x32_bf16 v[192:195], v[0:3], v[160:163], v[192:195]\n\t"
      "ds_read_b64_tr_b16 v[86:87], v223 offset:46592\n\t"
      "v_mfma_f32_16x16x32_bf16 v[196:199], v[4:7], v[160:163], v[196:199]\n\t"
      "ds_read_b64_tr_b16 v[88:89], v216 offset:46592\n\t"
      "ds_read_b64_tr_b16 v[90:91], v220 offset:46592\n\t"
      "v_mfma_f32_16x16x32_bf16 v[200:203], v[8:11], v[160:163], v[200:203]\n\t"
      "ds_read_b64_tr_b16 v[92:93], v217 offset:46592\n\t"
      "v_mfma_f32_16x16x32_bf16 v[204:207], v[12:15], v[160:163], v[204:207]\n\t"
      "ds_read_b64_tr_b16 v[94:95], v221 offset:46592\n\t"
      "v_mfma_f32_16x16x32_bf16 v[192:195], v[16:19], v[164:167], v[192:195]\n\t"
      "ds_read_b64_tr_b16 v[96:97], v216 offset:50176\n\t"
      "ds_read_b64_tr_b16 v[98:99], v220 offset:50176\n\t"
      "v_mfma_f32_16x16x32_bf16 v[196:199], v[20:23], v[164:167], v[196:199]\n\t"
      "ds_read_b64_tr_b16 v[100:101], v217 offset:50176\n\t"
      "v_mfma_f32_16x16x32_bf16 v[200:203], v[24:27], v[164:167], v[200:203]\n\t"
      "ds_read_b64_tr_b16 v[102:103], v221 offset:50176\n\t"
      "v_mfma_f32_16x16x32_bf16 v[204:207], v[28:31], v[164:167], v[204:207]\n\t"
      "ds_read_b64_tr_b16 v[104:105], v218 offset:50176\n\t"
      "ds_read_b64_tr_b16 v[106:107], v222 offset:50176\n\t"
      "s_waitcnt vmcnt(5)\n\t"
      "v_max3_u32 v232, v168, v169, v170\n\t"
      "v_max3_u32 v232, v232, v171, v172\n\t"
      "v_max3_u32 v232, v232, v173, v174\n\t"
      "v_max_u32_e32 v232, v232, v175\n\t"
      "v_cmp_eq_u32_e32 vcc, -1, v232\n\t"
      "s_cbranch_vccnz LO10R_%=\n\t"
      "LO10B_%=:\n\t"
      "v_mfma_f32_16x16x32_bf16 v[192:195], v[32:35], v[168:171], v[192:195]\n\t"
      "ds_read_b64_tr_b16 v[108:109], v219 offset:50176\n\t"
      "v_mfma_f32_16x16x32_bf16 v[196:199], v[36:39], v[168:171], v[196:199]\n\t"
      "ds_read_b64_tr_b16 v[110:111], v223 offset:50176\n\t"
      "v_mfma_f32_16x16x32_bf16 v[200:203], v[40:43], v[168:171], v[200:203]\n\t"
      "ds_read_b64_tr_b16 v[112:113], v218 offset:53760\n\t"
      "ds_read_b64_tr_b16 v[114:115], v222 offset:53760\n\t"
      "v_mfma_f32_16x16x32_bf16 v[204:207], v[44:47], v[168:171], v[204:207]\n\t"
      "ds_read_b64_tr_b16 v[116:117], v219 offset:53760\n\t"
      "s_waitcnt lgkmcnt(14)\n\t"
      "v_mfma_f32_16x16x32_bf16 v[192:195], v[48:51], v[172:175], v[192:195]\n\t"
      "ds_read_b64_tr_b16 v[118:119], v223 offset:53760\n\t"
      "v_mfma_f32_16x16x32_bf16 v[196:199], v[52:55], v[172:175], v[196:199]\n\t"
      "ds_read_b64_tr_b16 v[120:121], v216 offset:53760\n\t"
      "ds_read_b64_tr_b16 v[122:123], v220 offset:53760\n\t"
      "v_mfma_f32_16x16x32_bf16 v[200:203], v[56:59], v[172:175], v[200:203]\n\t"
      "ds_read_b64_tr_b16 v[124:125], v217 offset:53760\n\t"
      "v_mfma_f32_16x16x32_bf16 v[204:207], v[60:63], v[172:175], v[204:207]\n\t"
      "ds_read_b64_tr_b16 v[126:127], v221 offset:53760\n\t"
      "s_waitcnt vmcnt(3)\n\t"
      "v_max3_u32 v232, v176, v177, v178\n\t"
      "v_max3_u32 v232, v232, v179, v180\n\t"
      "v_max3_u32 v232, v232, v181, v182\n\t"
      "v_max_u32_e32 v232, v232, v183\n\t"
      "v_cmp_eq_u32_e32 vcc, -1, v232\n\t"
      "s_cbranch_vccnz LO12R_%=\n\t"
      "LO12B_%=:\n\t"
      "v_mfma_f32_16x16x32_bf16 v[192:195], v[64:67], v[176:179], v[192:195]\n\t"
      "v_mfma_f32_16x16x32_bf16 v[196:199], v[68:71], v[176:179], v[196:199]\n\t"
      "v_mfma_f32_16x16x32_bf16 v[200:203], v[72:75], v[176:179], v[200:203]\n\t"
      "v_mfma_f32_16x16x32_bf16 v[204:207], v[76:79], v[176:179], v[204:207]\n\t"
      "s_waitcnt lgkmcnt(14)\n\t"
      "v_mfma_f32_16x16x32_bf16 v[192:195], v[80:83], v[180:183], v[192:195]\n\t"
      "v_mfma_f32_16x16x32_bf16 v[196:199], v[84:87], v[180:183], v[196:199]\n\t"
      "v_mfma_f32_16x16x32_bf16 v[200:203], v[88:91], v[180:183], v[200:203]\n\t"
      "v_mfma_f32_16x16x32_bf16 v[204:207], v[92:95], v[180:183], v[204:207]\n\t"
      "s_waitcnt vmcnt(1)\n\t"
      "v_max3_u32 v232, v184, v185, v186\n\t"
      "v_max3_u32 v232, v232, v187, v188\n\t"
      "v_max3_u32 v232, v232, v189, v190\n\t"
      "v_max_u32_e32 v232, v232, v191\n\t"
      "v_cmp_eq_u32_e32 vcc, -1, v232\n\t"
      "s_cbranch_vccnz LO14R_%=\n\t"
      "LO14B_%=:\n\t"
      "s_waitcnt lgkmcnt(8)\n\t"
      "v_mfma_f32_16x16x32_bf16 v[192:195], v[96:99], v[184:187], v[192:195]\n\t"
      "v_mfma_f32_16x16x32_bf16 v[196:199], v[100:103], v[184:187], v[196:199]\n\t"
      "v_mfma_f32_16x16x32_bf16 v[200:203], v[104:107], v[184:187], v[200:203]\n\t"
      "v_mfma_f32_16x16x32_bf16 v[204:207], v[108:111], v[184:187], v[204:207]\n\t"
      "s_waitcnt lgkmcnt(0)\n\t"
      "v_mfma_f32_16x16x32_bf16 v[192:195], v[112:115], v[188:191], v[192:195]\n\t"
      "v_mfma_f32_16x16x32_bf16 v[196:199], v[116:119], v[188:191], v[196:199]\n\t"
      "v_mfma_f32_16x16x32_bf16 v[200:203], v[120:123], v[188:191], v[200:203]\n\t"
      "v_mfma_f32_16x16x32_bf16 v[204:207], v[124:127], v[188:191], v[204:207]\n\t"
      "s_nop 4\n\t"
      "v_cvt_pk_bf16_f32 v208, v192, v193\n\t"
      "v_cvt_pk_bf16_f32 v209, v194, v195\n\t"
      "v_cvt_pk_bf16_f32 v210, v196, v197\n\t"
      "v_cvt_pk_bf16_f32 v211, v198, v199\n\t"
      "v_cvt_pk_bf16_f32 v212, v200, v201\n\t"
      "v_cvt_pk_bf16_f32 v213, v202, v203\n\t"
      "v_cvt_pk_bf16_f32 v214, v204, v205\n\t"
      "v_cvt_pk_bf16_f32 v215, v206, v207\n\t"
      "v_mov_b32_e32 v148, 0x3f803f80\n\t"
      "v_dot2_f32_bf16 v144, v208, v148, 0\n\t"
      "v_dot2_f32_bf16 v145, v208, v208, 0\n\t"
      "v_dot2_f32_bf16 v146, v209, v148, 0\n\t"
      "v_dot2_f32_bf16 v147, v209, v209, 0\n\t"
      "v_dot2_f32_bf16 v144, v210, v148, v144\n\t"
      "v_dot2_f32_bf16 v145, v210, v210, v145\n\t"
      "v_dot2_f32_bf16 v146, v211, v148, v146\n\t"
      "v_dot2_f32_bf16 v147, v211, v211, v147\n\t"
      "v_dot2_f32_bf16 v144, v212, v148, v144\n\t"
      "v_dot2_f32_bf16 v145, v212, v212, v145\n\t"
      "v_dot2_f32_bf16 v146, v213, v148, v146\n\t"
      "v_dot2_f32_bf16 v147, v213, v213, v147\n\t"
      "v_dot2_f32_bf16 v144, v214, v148, v144\n\t"
      "v_dot2_f32_bf16 v145, v214, v214, v145\n\t"
      "v_dot2_f32_bf16 v146, v215, v148, v146\n\t"
      "v_dot2_f32_bf16 v147, v215, v215, v147\n\t"
      "v_lshlrev_b32_e32 v128, 16, v208\n\t"
      "v_and_b32_e32 v129, 0xffff0000, v208\n\t"
      "v_lshlrev_b32_e32 v130, 16, v209\n\t"
      "v_pk_add_f32 v[144:145], v[144:145], v[146:147]\n\t"
      "v_and_b32_e32 v131, 0xffff0000, v209\n\t"
      "v_lshlrev_b32_e32 v132, 16, v210\n\t"
      "v_permlane16_swap_b32_e32 v144, v145\n\t"
      "v_add_f32_e32 v144, v144, v145\n\t"
      "v_mov_b32_e32 v145, v144\n\t"
      "v_and_b32_e32 v133, 0xffff0000, v210\n\t"
      "v_lshlrev_b32_e32 v134, 16, v211\n\t"
      "v_permlane32_swap_b32_e32 v144, v145\n\t"
      "v_add_f32_e32 v144, v144, v145\n\t"
      "v_mov_b32_e32 v145, v144\n\t"
      "v_and_b32_e32 v135, 0xffff0000, v211\n\t"
      "v_lshlrev_b32_e32 v136, 16, v212\n\t"
      "v_permlane16_swap_b32_e32 v144, v145\n\t"
      "s_mov_b64 exec, s[70:71]\n\t"
      "v_min_u32_e32 v144, -2, v144\n\t"
      "v_min_u32_e32 v145, -2, v145\n\t"
      "global_store_dwordx2 v231, v[144:145], s[58:59] sc1\n\t"
      "s_mov_b64 exec, -1\n\t"
      "s_barrier\n\t"
      "v_and_b32_e32 v137, 0xffff0000, v212\n\t"
      "v_lshlrev_b32_e32 v138, 16, v213\n\t"
      "v_and_b32_e32 v139, 0xffff0000, v213\n\t"
      "v_lshlrev_b32_e32 v140, 16, v214\n\t"
      "v_and_b32_e32 v141, 0xffff0000, v214\n\t"
      "v_lshlrev_b32_e32 v142, 16, v215\n\t"
      "v_and_b32_e32 v143, 0xffff0000, v215\n\t"
      "v_permlane16_swap_b32_e32 v208, v210\n\t"
      "v_permlane16_swap_b32_e32 v209, v211\n\t"
      "v_permlane16_swap_b32_e32 v212, v214\n\t"
      "v_permlane16_swap_b32_e32 v213, v215\n\t"
      "global_store_dwordx4 v241, v[208:211], s[20:21] offset:-2048 sc1\n\t"
      "global_store_dwordx4 v241, v[212:215], s[20:21] offset:2048 sc1\n\t"
      "s_bitcmp0_b32 s27, 0\n\t"
      "s_cbranch_scc1 NTK0_%=\n\t"
      "ds_read_b64_tr_b16 v[0:1], v224 offset:16384\n\t"
      "ds_read_b64_tr_b16 v[2:3], v224 offset:16896\n\t"
      "ds_read_b64_tr_b16 v[4:5], v225 offset:16384\n\t"
      "ds_read_b64_tr_b16 v[6:7], v225 offset:16896\n\t"
      "ds_read_b64_tr_b16 v[8:9], v226 offset:16384\n\t"
      "ds_read_b64_tr_b16 v[10:11], v226 offset:16896\n\t"
      "ds_read_b64_tr_b16 v[12:13], v227 offset:16384\n\t"
      "ds_read_b64_tr_b16 v[14:15], v227 offset:16896\n\t"
      "ds_read_b64_tr_b16 v[16:17], v224 offset:49152\n\t"
      "ds_read_b64_tr_b16 v[18:19], v224 offset:49664\n\t"
      "ds_read_b64_tr_b16 v[20:21], v225 offset:49152\n\t"
      "ds_read_b64_tr_b16 v[22:23], v225 offset:49664\n\t"
      "ds_read_b64_tr_b16 v[24:25], v226 offset:49152\n\t"
      "ds_read_b64_tr_b16 v[26:27], v226 offset:49664\n\t"
      "ds_read_b64_tr_b16 v[28:29], v227 offset:49152\n\t"
      "ds_read_b64_tr_b16 v[30:31], v227 offset:49664\n\t"
      "ds_read_b64_tr_b16 v[32:33], v224\n\t"
      "ds_read_b64_tr_b16 v[34:35], v224 offset:512\n\t"
      "ds_read_b64_tr_b16 v[36:37], v225\n\t"
      "ds_read_b64_tr_b16 v[38:39], v225 offset:512\n\t"
      "ds_read_b64_tr_b16 v[40:41], v226\n\t"
      "ds_read_b64_tr_b16 v[42:43], v226 offset:512\n\t"
      "ds_read_b64_tr_b16 v[44:45], v227\n\t"
      "ds_read_b64_tr_b16 v[46:47], v227 offset:512\n\t"
      "ds_read_b64_tr_b16 v[48:49], v224 offset:32768\n\t"
      "ds_read_b64_tr_b16 v[50:51], v224 offset:33280\n\t"
      "ds_read_b64_tr_b16 v[52:53], v225 offset:32768\n\t"
      "ds_read_b64_tr_b16 v[54:55], v225 offset:33280\n\t"
      "ds_read_b64_tr_b16 v[56:57], v226 offset:32768\n\t"
      "ds_read_b64_tr_b16 v[58:59], v226 offset:33280\n\t"
      "ds_read_b64_tr_b16 v[60:61], v227 offset:32768\n\t"
      "ds_read_b64_tr_b16 v[62:63], v227 offset:33280\n\t"
      "NTK0_%=:\n\t"
      "global_load_dwordx2 v[148:149], v231, s[46:47] sc1\n\t"
      "global_load_dwordx2 v[150:151], v231, s[46:47] offset:384 sc1\n\t"
      "s_waitcnt lgkmcnt(0)\n\t"
      "s_barrier\n\t"
      "s_waitcnt vmcnt(0)\n\t"
      "v_max3_u32 v232, v148, v149, v150\n\t"
      "v_max_u32_e32 v232, v232, v151\n\t"
      "v_cmp_eq_u32_e32 vcc, -1, v232\n\t"
      "s_cbranch_vccnz LSR_%=\n\t"
      "LSB_%=:\n\t"
      "v_cndmask_b32_e64 v150, v150, 0, s[70:71]\n\t"
      "v_cndmask_b32_e64 v151, v151, 0, s[70:71]\n\t"
      "v_pk_add_f32 v[144:145], v[148:149], v[150:151]\n\t"
      "s_nop 1\n\t"
      "v_permlane16_swap_b32_e32 v144, v145\n\t"
      "v_add_f32_e32 v144, v144, v145\n\t"
      "v_mov_b32_e32 v145, v144\n\t"
      "s_nop 1\n\t"
      "v_permlane32_swap_b32_e32 v144, v145\n\t"
      "v_add_f32_e32 v144, v144, v145\n\t"
      "v_mov_b32_e32 v145, v144\n\t"
      "s_nop 1\n\t"
      "v_permlane16_swap_b32_e32 v144, v145\n\t"
      "v_mul_f32_e32 v144, 0xbb124925, v144\n\t"
      "v_mul_f32_e32 v145, 0x3b124925, v145\n\t"
      "v_fma_f32 v145, -v144, v144, v145\n\t"
      "v_max_f32_e32 v145, 0, v145\n\t"
      "v_add_f32_e32 v146, 0x3727c5ac, v145\n\t"
      "v_fmac_f32_e32 v145, 0x3727c5ac, v146\n\t"
      "v_rsq_f32_e32 v145, v145\n\t"
      "s_nop 0\n\t"
      "v_mul_f32_e32 v144, v144, v145\n\t"
      "v_pk_fma_f32 v[128:129], v[128:129], v[144:145], v[144:145] op_sel:[0,1,0] op_sel_hi:[1,1,0]\n\t"
      "v_pk_fma_f32 v[130:131], v[130:131], v[144:145], v[144:145] op_sel:[0,1,0] op_sel_hi:[1,1,0]\n\t"
      "v_cvt_pk_bf16_f32 v152, v128, v129\n\t"
      "v_cvt_pk_bf16_f32 v153, v130, v131\n\t"
      "ds_write_b64 v233, v[152:153]\n\t"
      "v_pk_fma_f32 v[132:133], v[132:133], v[144:145], v[144:145] op_sel:[0,1,0] op_sel_hi:[1,1,0]\n\t"
      "v_pk_fma_f32 v[134:135], v[134:135], v[144:145], v[144:145] op_sel:[0,1,0] op_sel_hi:[1,1,0]\n\t"
      "v_cvt_pk_bf16_f32 v154, v132, v133\n\t"
      "v_cvt_pk_bf16_f32 v155, v134, v135\n\t"
      "ds_write_b64 v234, v[154:155]\n\t"
      "ds_read_b64_tr_b16 v[160:161], v237\n\t"
      "ds_read_b64_tr_b16 v[162:163], v238 offset:512\n\t"
      "v_pk_fma_f32 v[136:137], v[136:137], v[144:145], v[144:145] op_sel:[0,1,0] op_sel_hi:[1,1,0]\n\t"
      "v_pk_fma_f32 v[138:139], v[138:139], v[144:145], v[144:145] op_sel:[0,1,0] op_sel_hi:[1,1,0]\n\t"
      "v_cvt_pk_bf16_f32 v156, v136, v137\n\t"
      "v_cvt_pk_bf16_f32 v157, v138, v139\n\t"
      "ds_write_b64 v235, v[156:157]\n\t"
      "v_pk_fma_f32 v[140:141], v[140:141], v[144:145], v[144:145] op_sel:[0,1,0] op_sel_hi:[1,1,0]\n\t"
      "v_pk_fma_f32 v[142:143], v[142:143], v[144:145], v[144:145] op_sel:[0,1,0] op_sel_hi:[1,1,0]\n\t"
      "v_cvt_pk_bf16_f32 v158, v140, v141\n\t"
      "v_cvt_pk_bf16_f32 v159, v142, v143\n\t"
      "ds_write_b64 v236, v[158:159]\n\t"
      "ds_read_b64_tr_b16 v[164:165], v239\n\t"
      "ds_read_b64_tr_b16 v[166:167], v240 offset:512\n\t"
      "s_waitcnt lgkmcnt(4)\n\t"
      "v_min_u32_e32 v160, -2, v160\n\t"
      "v_min_u32_e32 v161, -2, v161\n\t"
      "v_min_u32_e32 v162, -2, v162\n\t"
      "v_min_u32_e32 v163, -2, v163\n\t"
      "global_store_dwordx4 v229, v[160:163], s[48:49] sc1\n\t"
      "s_waitcnt lgkmcnt(0)\n\t"
      "v_min_u32_e32 v164, -2, v164\n\t"
      "v_min_u32_e32 v165, -2, v165\n\t"
      "v_min_u32_e32 v166, -2, v166\n\t"
      "v_min_u32_e32 v167, -2, v167\n\t"
      "global_store_dwordx4 v230, v[164:167], s[48:49] sc1\n\t"
      "s_setprio 0\n\t"
      "s_barrier\n\t"
      "s_bitcmp0_b32 s27, 0\n\t"
      "s_cbranch_scc1 G4_%=\n\t"
      "s_mul_i32 s34, s30, 65536\n\t"
      "s_mul_i32 s35, s24, 8192\n\t"
      "s_add_u32 s34, s34, s35\n\t"
      "s_add_u32 s64, s48, s34\n\t"
      "s_addc_u32 s65, s49, 0\n\t"
      "v_add_u32_e32 v250, 4096, v244\n\t"
      "global_load_dwordx4 v[128:131], v250, s[64:65] offset:-4096 sc1\n\t"
      "global_load_dwordx4 v[132:135], v250, s[64:65] offset:-3072 sc1\n\t"
      "global_load_dwordx4 v[136:139], v250, s[64:65] offset:-2048 sc1\n\t"
      "global_load_dwordx4 v[140:143], v250, s[64:65] offset:-1024 sc1\n\t"
      "global_load_dwordx4 v[144:147], v250, s[64:65] sc1\n\t"
      "global_load_dwordx4 v[148:151], v250, s[64:65] offset:1024 sc1\n\t"
      "global_load_dwordx4 v[152:155], v250, s[64:65] offset:2048 sc1\n\t"
      "global_load_dwordx4 v[156:159], v250, s[64:65] offset:3072 sc1\n\t"
      "ds_read_b64_tr_b16 v[64:65], v224 offset:16384\n\t"
      "ds_read_b64_tr_b16 v[66:67], v224 offset:16896\n\t"
      "ds_read_b64_tr_b16 v[68:69], v225 offset:16384\n\t"
      "ds_read_b64_tr_b16 v[70:71], v225 offset:16896\n\t"
      "ds_read_b64_tr_b16 v[72:73], v226 offset:16384\n\t"
      "ds_read_b64_tr_b16 v[74:75], v226 offset:16896\n\t"
      "ds_read_b64_tr_b16 v[76:77], v227 offset:16384\n\t"
      "ds_read_b64_tr_b16 v[78:79], v227 offset:16896\n\t"
      "ds_read_b64_tr_b16 v[80:81], v224 offset:49152\n\t"
      "ds_read_b64_tr_b16 v[82:83], v224 offset:49664\n\t"
      "ds_read_b64_tr_b16 v[84:85], v225 offset:49152\n\t"
      "ds_read_b64_tr_b16 v[86:87], v225 offset:49664\n\t"
      "ds_read_b64_tr_b16 v[88:89], v226 offset:49152\n\t"
      "ds_read_b64_tr_b16 v[90:91], v226 offset:49664\n\t"
      "ds_read_b64_tr_b16 v[92:93], v227 offset:49152\n\t"
      "ds_read_b64_tr_b16 v[94:95], v227 offset:49664\n\t"
      "ds_read_b64_tr_b16 v[96:97], v224\n\t"
      "ds_read_b64_tr_b16 v[98:99], v224 offset:512\n\t"
      "ds_read_b64_tr_b16 v[100:101], v225\n\t"
      "ds_read_b64_tr_b16 v[102:103], v225 offset:512\n\t"
      "ds_read_b64_tr_b16 v[104:105], v226\n\t"
      "ds_read_b64_tr_b16 v[106:107], v226 offset:512\n\t"
      "ds_read_b64_tr_b16 v[108:109], v227\n\t"
      "ds_read_b64_tr_b16 v[110:111], v227 offset:512\n\t"
      "ds_read_b64_tr_b16 v[112:113], v224 offset:32768\n\t"
      "ds_read_b64_tr_b16 v[114:115], v224 offset:33280\n\t"
      "ds_read_b64_tr_b16 v[116:117], v225 offset:32768\n\t"
      "ds_read_b64_tr_b16 v[118:119], v225 offset:33280\n\t"
      "ds_read_b64_tr_b16 v[120:121], v226 offset:32768\n\t"
      "ds_read_b64_tr_b16 v[122:123], v226 offset:33280\n\t"
      "ds_read_b64_tr_b16 v[124:125], v227 offset:32768\n\t"
      "ds_read_b64_tr_b16 v[126:127], v227 offset:33280\n\t"
      "s_waitcnt lgkmcnt(0)\n\t"
      "GST_%=:\n\t"
      "s_add_u32 s66, s64, 65536\n\t"
      "s_addc_u32 s67, s65, 0\n\t"
      "s_add_u32 s68, s66, 65536\n\t"
      "s_addc_u32 s69, s67, 0\n\t"
      "s_add_u32 s70, s68, 65536\n\t"
      "s_addc_u32 s71, s69, 0\n\t"
      "s_add_u32 s72, s70, 65536\n\t"
      "s_addc_u32 s73, s71, 0\n\t"
      "s_add_u32 s74, s72, 65536\n\t"
      "s_addc_u32 s75, s73, 0\n\t"
      "s_add_u32 s76, s74, 65536\n\t"
      "s_addc_u32 s77, s75, 0\n\t"
      "s_mul_i32 s34, s30, 28\n\t"
      "s_add_u32 s34, s34, s29\n\t"
      "s_lshl_b32 s34, s34, 11\n\t"
      "s_add_u32 s34, s34, 10223616\n\t"
      "s_add_u32 s62, s20, s34\n\t"
      "s_addc_u32 s63, s21, 0\n\t"
      "v_lshlrev_b32_e32 v248, 16, v249\n\t"
      "v_and_b32_e32 v249, 0xffff0000, v249\n\t"
      "v_mov_b32_e32 v252, 0xbdd2d3e8\n\t"
      "v_xor_b32_e32 v253, 128, v245\n\t"
      "s_waitcnt vmcnt(0)\n\t"
      "v_max3_u32 v251, v128, v129, v130\n\t"
      "v_max3_u32 v251, v251, v131, v132\n\t"
      "v_max3_u32 v251, v251, v133, v134\n\t"
      "v_max3_u32 v251, v251, v135, v136\n\t"
      "v_max3_u32 v251, v251, v137, v138\n\t"
      "v_max3_u32 v251, v251, v139, v140\n\t"
      "v_max3_u32 v251, v251, v141, v142\n\t"
      "v_max3_u32 v251, v251, v143, v144\n\t"
      "v_max3_u32 v251, v251, v145, v146\n\t"
      "v_max3_u32 v251, v251, v147, v148\n\t"
      "v_max3_u32 v251, v251, v149, v150\n\t"
      "v_max3_u32 v251, v251, v151, v152\n\t"
      "v_max3_u32 v251, v251, v153, v154\n\t"
      "v_max3_u32 v251, v251, v155, v156\n\t"
      "v_max3_u32 v251, v251, v157, v158\n\t"
      "v_max_u32_e32 v251, v251, v159\n\t"
      "v_cmp_eq_u32_e32 vcc, -1, v251\n\t"
      "s_cbranch_vccnz LA30R_%=\n\t"
      "LA30B_%=:\n\t"
      "v_mfma_f32_16x16x32_bf16 v[208:211], v[0:3], v[128:131], 0\n\t"
      "global_load_dwordx4 v[160:163], v250, s[66:67] offset:-4096 sc1\n\t"
      "v_mfma_f32_16x16x32_bf16 v[208:211], v[16:19], v[132:135], v[208:211]\n\t"
      "v_mfma_f32_16x16x32_bf16 v[208:211], v[32:35], v[136:139], v[208:211]\n\t"
      "global_load_dwordx4 v[164:167], v250, s[66:67] offset:-3072 sc1\n\t"
      "v_mfma_f32_16x16x32_bf16 v[208:211], v[48:51], v[140:143], v[208:211]\n\t"
      "v_mfma_f32_16x16x32_bf16 v[208:211], v[64:67], v[144:147], v[208:211]\n\t"
      "global_load_dwordx4 v[168:171], v250, s[66:67] offset:-2048 sc1\n\t"
      "v_mfma_f32_16x16x32_bf16 v[208:211], v[80:83], v[148:151], v[208:211]\n\t"
      "s_barrier\n\t"
      "v_mfma_f32_16x16x32_bf16 v[208:211], v[96:99], v[152:155], v[208:211]\n\t"
      "global_load_dwordx4 v[172:175], v250, s[66:67] offset:-1024 sc1\n\t"
      "v_mfma_f32_16x16x32_bf16 v[208:211], v[112:115], v[156:159], v[208:211]\n\t"
      "v_mfma_f32_16x16x32_bf16 v[212:215], v[4:7], v[128:131], 0\n\t"
      "global_load_dwordx4 v[176:179], v250, s[66:67] sc1\n\t"
      "v_mfma_f32_16x16x32_bf16 v[212:215], v[20:23], v[132:135], v[212:215]\n\t"
      "v_mfma_f32_16x16x32_bf16 v[212:215], v[36:39], v[136:139], v[212:215]\n\t"
      "global_load_dwordx4 v[180:183], v250, s[66:67] offset:1024 sc1\n\t"
      "v_mfma_f32_16x16x32_bf16 v[212:215], v[52:55], v[140:143], v[212:215]\n\t"
      "v_mfma_f32_16x16x32_bf16 v[212:215], v[68:71], v[144:147], v[212:215]\n\t"
      "global_load_dwordx4 v[184:187], v250, s[66:67] offset:2048 sc1\n\t"
      "v_mfma_f32_16x16x32_bf16 v[212:215], v[84:87], v[148:151], v[212:215]\n\t"
      "v_mfma_f32_16x16x32_bf16 v[212:215], v[100:103], v[152:155], v[212:215]\n\t"
      "ds_write_b128 v245, v[208:211]\n\t"
      "global_load_dwordx4 v[188:191], v250, s[66:67] offset:3072 sc1\n\t"
      "v_mfma_f32_16x16x32_bf16 v[212:215], v[116:119], v[156:159], v[212:215]\n\t"
      "v_mfma_f32_16x16x32_bf16 v[216:219], v[8:11], v[128:131], 0\n\t"
      "global_load_dwordx4 v[192:195], v250, s[68:69] offset:-4096 sc1\n\t"
      "v_mfma_f32_16x16x32_bf16 v[216:219], v[24:27], v[132:135], v[216:219]\n\t"
      "v_mfma_f32_16x16x32_bf16 v[216:219], v[40:43], v[136:139], v[216:219]\n\t"
      "global_load_dwordx4 v[196:199], v250, s[68:69] offset:-3072 sc1\n\t"
      "v_mfma_f32_16x16x32_bf16 v[216:219], v[56:59], v[140:143], v[216:219]\n\t"
      "v_mfma_f32_16x16x32_bf16 v[216:219], v[72:75], v[144:147], v[216:219]\n\t"
      "global_load_dwordx4 v[200:203], v250, s[68:69] offset:-2048 sc1\n\t"
      "v_mfma_f32_16x16x32_bf16 v[216:219], v[88:91], v[148:151], v[216:219]\n\t"
      "v_mfma_f32_16x16x32_bf16 v[216:219], v[104:107], v[152:155], v[216:219]\n\t"
      "ds_write_b128 v253, v[212:215] offset:1024\n\t"
      "global_load_dwordx4 v[204:207], v250, s[68:69] offset:-1024 sc1\n\t"
      "v_mfma_f32_16x16x32_bf16 v[216:219], v[120:123], v[156:159], v[216:219]\n\t"
      "v_mfma_f32_16x16x32_bf16 v[220:223], v[12:15], v[128:131], 0\n\t"
      "global_load_dwordx4 v[128:131], v250, s[68:69] sc1\n\t"
      "v_mfma_f32_16x16x32_bf16 v[220:223], v[28:31], v[132:135], v[220:223]\n\t"
      "v_mfma_f32_16x16x32_bf16 v[220:223], v[44:47], v[136:139], v[220:223]\n\t"
      "v_mfma_f32_16x16x32_bf16 v[220:223], v[60:63], v[140:143], v[220:223]\n\t"
      "v_mfma_f32_16x16x32_bf16 v[220:223], v[76:79], v[144:147], v[220:223]\n\t"
      "global_load_dwordx4 v[132:135], v250, s[68:69] offset:1024 sc1\n\t"
      "v_mfma_f32_16x16x32_bf16 v[220:223], v[92:95], v[148:151], v[220:223]\n\t"
      "v_mfma_f32_16x16x32_bf16 v[220:223], v[108:111], v[152:155], v[220:223]\n\t"
      "ds_write_b128 v245, v[216:219] offset:2048\n\t"
      "v_mfma_f32_16x16x32_bf16 v[220:223], v[124:127], v[156:159], v[220:223]\n\t"
      "s_waitcnt vmcnt(10)\n\t"
      "v_mfma_f32_16x16x32_bf16 v[224:227], v[0:3], v[160:163], 0\n\t"
      "global_load_dwordx4 v[136:139], v250, s[68:69] offset:2048 sc1\n\t"
      "v_mfma_f32_16x16x32_bf16 v[224:227], v[16:19], v[164:167], v[224:227]\n\t"
      "s_nop 3\n\t"
      "ds_write_b128 v253, v[220:223] offset:3072\n\t"
      "v_mfma_f32_16x16x32_bf16 v[224:227], v[32:35], v[168:171], v[224:227]\n\t"
      "v_mfma_f32_16x16x32_bf16 v[224:227], v[48:51], v[172:175], v[224:227]\n\t"
      "s_waitcnt vmcnt(7)\n\t"
      "v_mfma_f32_16x16x32_bf16 v[224:227], v[64:67], v[176:179], v[224:227]\n\t"
      "global_load_dwordx4 v[140:143], v250, s[68:69] offset:3072 sc1\n\t"
      "v_mfma_f32_16x16x32_bf16 v[224:227], v[80:83], v[180:183], v[224:227]\n\t"
      "s_waitcnt lgkmcnt(0)\n\t"
      "s_barrier\n\t"
      "ds_read_b64 v[228:229], v246\n\t"
      "ds_read_b64 v[230:231], v246 offset:4096\n\t"
      "v_mfma_f32_16x16x32_bf16 v[224:227], v[96:99], v[184:187], v[224:227]\n\t"
      "ds_read_b64 v[232:233], v246 offset:8192\n\t"
      "ds_read_b64 v[234:235], v246 offset:12288\n\t"
      "v_mfma_f32_16x16x32_bf16 v[224:227], v[112:115], v[188:191], v[224:227]\n\t"
      "ds_read_b64 v[236:237], v246 offset:16384\n\t"
      "ds_read_b64 v[238:239], v246 offset:20480\n\t"
      "v_mfma_f32_16x16x32_bf16 v[208:211], v[4:7], v[160:163], 0\n\t"
      "ds_read_b64 v[240:241], v246 offset:24576\n\t"
      "ds_read_b64 v[242:243], v246 offset:28672\n\t"
      "global_load_dwordx4 v[144:147], v250, s[70:71] offset:-4096 sc1\n\t"
      "v_mfma_f32_16x16x32_bf16 v[208:211], v[20:23], v[164:167], v[208:211]\n\t"
      "v_mfma_f32_16x16x32_bf16 v[208:211], v[36:39], v[168:171], v[208:211]\n\t"
      "s_waitcnt lgkmcnt(4)\n\t"
      "v_add_f32_e32 v228, v228, v230\n\t"
      "v_add_f32_e32 v232, v232, v234\n\t"
      "v_cmp_u_f32_e32 vcc, v224, v224\n\t"
      "s_cbranch_vccnz LA31R_%=\n\t"
      "LA31B_%=:\n\t"
      "v_mfma_f32_16x16x32_bf16 v[208:211], v[52:55], v[172:175], v[208:211]\n\t"
      "v_add_f32_e32 v229, v229, v231\n\t"
      "v_add_f32_e32 v233, v233, v235\n\t"
      "v_mfma_f32_16x16x32_bf16 v[208:211], v[68:71], v[176:179], v[208:211]\n\t"
      "s_waitcnt lgkmcnt(0)\n\t"
      "v_add_f32_e32 v236, v236, v238\n\t"
      "v_add_f32_e32 v240, v240, v242\n\t"
      "global_load_dwordx4 v[148:151], v250, s[70:71] offset:-3072 sc1\n\t"
      "v_mfma_f32_16x16x32_bf16 v[208:211], v[84:87], v[180:183], v[208:211]\n\t"
      "v_add_f32_e32 v237, v237, v239\n\t"
      "v_add_f32_e32 v241, v241, v243\n\t"
      "v_mfma_f32_16x16x32_bf16 v[208:211], v[100:103], v[184:187], v[208:211]\n\t"
      "v_add_f32_e32 v228, v228, v232\n\t"
      "v_add_f32_e32 v236, v236, v240\n\t"
      "v_add_f32_e32 v229, v229, v233\n\t"
      "ds_write_b128 v245, v[224:227] offset:32768\n\t"
      "v_mfma_f32_16x16x32_bf16 v[208:211], v[116:119], v[188:191], v[208:211]\n\t"
      "v_add_f32_e32 v237, v237, v241\n\t"
      "v_add_f32_e32 v228, v228, v248\n\t"
      "v_mfma_f32_16x16x32_bf16 v[212:215], v[8:11], v[160:163], 0\n\t"
      "v_add_f32_e32 v229, v229, v249\n\t"
      "v_add_f32_e32 v228, v228, v236\n\t"
      "v_add_f32_e32 v229, v229, v237\n\t"
      "global_load_dwordx4 v[152:155], v250, s[70:71] offset:-2048 sc1\n\t"
      "v_mfma_f32_16x16x32_bf16 v[212:215], v[24:27], v[164:167], v[212:215]\n\t"
      "v_mul_f32_e32 v230, v228, v228\n\t"
      "v_mul_f32_e32 v231, v229, v229\n\t"
      "v_mfma_f32_16x16x32_bf16 v[212:215], v[40:43], v[168:171], v[212:215]\n\t"
      "v_fmaak_f32 v230, v252, v230, 0xc0135761\n\t"
      "v_fmaak_f32 v231, v252, v231, 0xc0135761\n\t"
      "v_mul_f32_e32 v230, v228, v230\n\t"
      "v_mfma_f32_16x16x32_bf16 v[212:215], v[56:59], v[172:175], v[212:215]\n\t"
      "v_mul_f32_e32 v231, v229, v231\n\t"
      "v_exp_f32_e32 v230, v230\n\t"
      "v_mfma_f32_16x16x32_bf16 v[212:215], v[72:75], v[176:179], v[212:215]\n\t"
      "v_exp_f32_e32 v231, v231\n\t"
      "v_add_f32_e32 v230, 1.0, v230\n\t"
      "v_add_f32_e32 v231, 1.0, v231\n\t"
      "global_load_dwordx4 v[156:159], v250, s[70:71] offset:-1024 sc1\n\t"
      "v_mfma_f32_16x16x32_bf16 v[212:215], v[88:91], v[180:183], v[212:215]\n\t"
      "v_rcp_f32_e32 v230, v230\n\t"
      "v_rcp_f32_e32 v231, v231\n\t"
      "v_mfma_f32_16x16x32_bf16 v[212:215], v[104:107], v[184:187], v[212:215]\n\t"
      "v_mul_f32_e32 v228, v228, v230\n\t"
      "v_mul_f32_e32 v229, v229, v231\n\t"
      "v_cvt_pk_bf16_f32 v232, v228, v229\n\t"
      "ds_write_b128 v253, v[208:211] offset:33792\n\t"
      "v_mfma_f32_16x16x32_bf16 v[212:215], v[120:123], v[188:191], v[212:215]\n\t"
      "v_min_u32_e32 v254, -2, v232\n\t"
      "global_store_dword v247, v254, s[62:63] sc1\n\t"
      "s_add_u32 s62, s62, 57344\n\t"
      "s_addc_u32 s63, s63, 0\n\t"
      "v_mfma_f32_16x16x32_bf16 v[216:219], v[12:15], v[160:163], 0\n\t"
      "global_load_dwordx4 v[160:163], v250, s[70:71] sc1\n\t"
      "v_mfma_f32_16x16x32_bf16 v[216:219], v[28:31], v[164:167], v[216:219]\n\t"
      "v_mfma_f32_16x16x32_bf16 v[216:219], v[44:47], v[168:171], v[216:219]\n\t"
      "v_mfma_f32_16x16x32_bf16 v[216:219], v[60:63], v[172:175], v[216:219]\n\t"
      "v_mfma_f32_16x16x32_bf16 v[216:219], v[76:79], v[176:179], v[216:219]\n\t"
      "global_load_dwordx4 v[164:167], v250, s[70:71] offset:1024 sc1\n\t"
      "v_mfma_f32_16x16x32_bf16 v[216:219], v[92:95], v[180:183], v[216:219]\n\t"
      "v_mfma_f32_16x16x32_bf16 v[216:219], v[108:111], v[184:187], v[216:219]\n\t"
      "ds_write_b128 v245, v[212:215] offset:34816\n\t"
      "v_mfma_f32_16x16x32_bf16 v[216:219], v[124:127], v[188:191], v[216:219]\n\t"
      "s_waitcnt vmcnt(11)\n\t"
      "v_mfma_f32_16x16x32_bf16 v[220:223], v[0:3], v[192:195], 0\n\t"
      "global_load_dwordx4 v[168:171], v250, s[70:71] offset:2048 sc1\n\t"
      "v_mfma_f32_16x16x32_bf16 v[220:223], v[16:19], v[196:199], v[220:223]\n\t"
      "s_nop 3\n\t"
      "ds_write_b128 v253, v[216:219] offset:35840\n\t"
      "v_mfma_f32_16x16x32_bf16 v[220:223], v[32:35], v[200:203], v[220:223]\n\t"
      "v_mfma_f32_16x16x32_bf16 v[220:223], v[48:51], v[204:207], v[220:223]\n\t"
      "s_waitcnt vmcnt(8)\n\t"
      "v_mfma_f32_16x16x32_bf16 v[220:223], v[64:67], v[128:131], v[220:223]\n\t"
      "global_load_dwordx4 v[172:175], v250, s[70:71] offset:3072 sc1\n\t"
      "v_mfma_f32_16x16x32_bf16 v[220:223], v[80:83], v[132:135], v[220:223]\n\t"
      "s_waitcnt lgkmcnt(0)\n\t"
      "s_barrier\n\t"
      "ds_read_b64 v[228:229], v246 offset:32768\n\t"
      "ds_read_b64 v[230:231], v246 offset:36864\n\t"
      "v_mfma_f32_16x16x32_bf16 v[220:223], v[96:99], v[136:139], v[220:223]\n\t"
      "ds_read_b64 v[232:233], v246 offset:40960\n\t"
      "ds_read_b64 v[234:235], v246 offset:45056\n\t"
      "v_mfma_f32_16x16x32_bf16 v[220:223], v[112:115], v[140:143], v[220:223]\n\t"
      "ds_read_b64 v[236:237], v246 offset:49152\n\t"
      "ds_read_b64 v[238:239], v246 offset:53248\n\t"
      "v_mfma_f32_16x16x32_bf16 v[224:227], v[4:7], v[192:195], 0\n\t"
      "ds_read_b64 v[240:241], v246 offset:57344\n\t"
      "ds_read_b64 v[242:243], v246 offset:61440\n\t"
      "global_load_dwordx4 v[176:179], v250, s[72:73] offset:-4096 sc1\n\t"
      "v_mfma_f32_16x16x32_bf16 v[224:227], v[20:23], v[196:199], v[224:227]\n\t"
      "v_mfma_f32_16x16x32_bf16 v[224:227], v[36:39], v[200:203], v[224:227]\n\t"
      "s_waitcnt lgkmcnt(4)\n\t"
      "v_add_f32_e32 v228, v228, v230\n\t"
      "v_add_f32_e32 v232, v232, v234\n\t"
      "v_cmp_u_f32_e32 vcc, v220, v220\n\t"
      "s_cbranch_vccnz LA32R_%=\n\t"
      "LA32B_%=:\n\t"
      "v_mfma_f32_16x16x32_bf16 v[224:227], v[52:55], v[204:207], v[224:227]\n\t"
      "v_add_f32_e32 v229, v229, v231\n\t"
      "v_add_f32_e32 v233, v233, v235\n\t"
      "v_mfma_f32_16x16x32_bf16 v[224:227], v[68:71], v[128:131], v[224:227]\n\t"
      "s_waitcnt lgkmcnt(0)\n\t"
      "v_add_f32_e32 v236, v236, v238\n\t"
      "v_add_f32_e32 v240, v240, v242\n\t"
      "global_load_dwordx4 v[180:183], v250, s[72:73] offset:-3072 sc1\n\t"
      "v_mfma_f32_16x16x32_bf16 v[224:227], v[84:87], v[132:135], v[224:227]\n\t"
      "v_add_f32_e32 v237, v237, v239\n\t"
      "v_add_f32_e32 v241, v241, v243\n\t"
      "v_mfma_f32_16x16x32_bf16 v[224:227], v[100:103], v[136:139], v[224:227]\n\t"
      "v_add_f32_e32 v228, v228, v232\n\t"
      "v_add_f32_e32 v236, v236, v240\n\t"
      "v_add_f32_e32 v229, v229, v233\n\t"
      "ds_write_b128 v245, v[220:223]\n\t"
      "v_mfma_f32_16x16x32_bf16 v[224:227], v[116:119], v[140:143], v[224:227]\n\t"
      "v_add_f32_e32 v237, v237, v241\n\t"
      "v_add_f32_e32 v228, v228, v248\n\t"
      "v_mfma_f32_16x16x32_bf16 v[208:211], v[8:11], v[192:195], 0\n\t"
      "v_add_f32_e32 v229, v229, v249\n\t"
      "v_add_f32_e32 v228, v228, v236\n\t"
      "v_add_f32_e32 v229, v229, v237\n\t"
      "global_load_dwordx4 v[184:187], v250, s[72:73] offset:-2048 sc1\n\t"
      "v_mfma_f32_16x16x32_bf16 v[208:211], v[24:27], v[196:199], v[208:211]\n\t"
      "v_mul_f32_e32 v230, v228, v228\n\t"
      "v_mul_f32_e32 v231, v229, v229\n\t"
      "v_mfma_f32_16x16x32_bf16 v[208:211], v[40:43], v[200:203], v[208:211]\n\t"
      "v_fmaak_f32 v230, v252, v230, 0xc0135761\n\t"
      "v_fmaak_f32 v231, v252, v231, 0xc0135761\n\t"
      "v_mul_f32_e32 v230, v228, v230\n\t"
      "v_mfma_f32_16x16x32_bf16 v[208:211], v[56:59], v[204:207], v[208:211]\n\t"
      "v_mul_f32_e32 v231, v229, v231\n\t"
      "v_exp_f32_e32 v230, v230\n\t"
      "v_mfma_f32_16x16x32_bf16 v[208:211], v[72:75], v[128:131], v[208:211]\n\t"
      "v_exp_f32_e32 v231, v231\n\t"
      "v_add_f32_e32 v230, 1.0, v230\n\t"
      "v_add_f32_e32 v231, 1.0, v231\n\t"
      "global_load_dwordx4 v[188:191], v250, s[72:73] offset:-1024 sc1\n\t"
      "v_mfma_f32_16x16x32_bf16 v[208:211], v[88:91], v[132:135], v[208:211]\n\t"
      "v_rcp_f32_e32 v230, v230\n\t"
      "v_rcp_f32_e32 v231, v231\n\t"
      "v_mfma_f32_16x16x32_bf16 v[208:211], v[104:107], v[136:139], v[208:211]\n\t"
      "v_mul_f32_e32 v228, v228, v230\n\t"
      "v_mul_f32_e32 v229, v229, v231\n\t"
      "v_cvt_pk_bf16_f32 v232, v228, v229\n\t"
      "ds_write_b128 v253, v[224:227] offset:1024\n\t"
      "v_mfma_f32_16x16x32_bf16 v[208:211], v[120:123], v[140:143], v[208:211]\n\t"
      "v_min_u32_e32 v254, -2, v232\n\t"
      "global_store_dword v247, v254, s[62:63] sc1\n\t"
      "s_add_u32 s62, s62, 57344\n\t"
      "s_addc_u32 s63, s63, 0\n\t"
      "v_mfma_f32_16x16x32_bf16 v[212:215], v[12:15], v[192:195], 0\n\t"
      "global_load_dwordx4 v[192:195], v250, s[72:73] sc1\n\t"
      "v_mfma_f32_16x16x32_bf16 v[212:215], v[28:31], v[196:199], v[212:215]\n\t"
      "v_mfma_f32_16x16x32_bf16 v[212:215], v[44:47], v[200:203], v[212:215]\n\t"
      "v_mfma_f32_16x16x32_bf16 v[212:215], v[60:63], v[204:207], v[212:215]\n\t"
      "v_mfma_f32_16x16x32_bf16 v[212:215], v[76:79], v[128:131], v[212:215]\n\t"
      "global_load_dwordx4 v[196:199], v250, s[72:73] offset:1024 sc1\n\t"
      "v_mfma_f32_16x16x32_bf16 v[212:215], v[92:95], v[132:135], v[212:215]\n\t"
      "v_mfma_f32_16x16x32_bf16 v[212:215], v[108:111], v[136:139], v[212:215]\n\t"
      "ds_write_b128 v245, v[208:211] offset:2048\n\t"
      "v_mfma_f32_16x16x32_bf16 v[212:215], v[124:127], v[140:143], v[212:215]\n\t"
      "s_waitcnt vmcnt(12)\n\t"
      "v_mfma_f32_16x16x32_bf16 v[216:219], v[0:3], v[144:147], 0\n\t"
      "global_load_dwordx4 v[200:203], v250, s[72:73] offset:2048 sc1\n\t"
      "v_mfma_f32_16x16x32_bf16 v[216:219], v[16:19], v[148:151], v[216:219]\n\t"
      "s_nop 3\n\t"
      "ds_write_b128 v253, v[212:215] offset:3072\n\t"
      "v_mfma_f32_16x16x32_bf16 v[216:219], v[32:35], v[152:155], v[216:219]\n\t"
      "v_mfma_f32_16x16x32_bf16 v[216:219], v[48:51], v[156:159], v[216:219]\n\t"
      "s_waitcnt vmcnt(8)\n\t"
      "v_mfma_f32_16x16x32_bf16 v[216:219], v[64:67], v[160:163], v[216:219]\n\t"
      "global_load_dwordx4 v[204:207], v250, s[72:73] offset:3072 sc1\n\t"
      "v_mfma_f32_16x16x32_bf16 v[216:219], v[80:83], v[164:167], v[216:219]\n\t"
      "s_waitcnt lgkmcnt(0)\n\t"
      "s_barrier\n\t"
      "ds_read_b64 v[228:229], v246\n\t"
      "ds_read_b64 v[230:231], v246 offset:4096\n\t"
      "v_mfma_f32_16x16x32_bf16 v[216:219], v[96:99], v[168:171], v[216:219]\n\t"
      "ds_read_b64 v[232:233], v246 offset:8192\n\t"
      "ds_read_b64 v[234:235], v246 offset:12288\n\t"
      "v_mfma_f32_16x16x32_bf16 v[216:219], v[112:115], v[172:175], v[216:219]\n\t"
      "ds_read_b64 v[236:237], v246 offset:16384\n\t"
      "ds_read_b64 v[238:239], v246 offset:20480\n\t"
      "v_mfma_f32_16x16x32_bf16 v[220:223], v[4:7], v[144:147], 0\n\t"
      "ds_read_b64 v[240:241], v246 offset:24576\n\t"
      "ds_read_b64 v[242:243], v246 offset:28672\n\t"
      "global_load_dwordx4 v[128:131], v250, s[74:75] offset:-4096 sc1\n\t"
      "v_mfma_f32_16x16x32_bf16 v[220:223], v[20:23], v[148:151], v[220:223]\n\t"
      "v_mfma_f32_16x16x32_bf16 v[220:223], v[36:39], v[152:155], v[220:223]\n\t"
      "s_waitcnt lgkmcnt(4)\n\t"
      "v_add_f32_e32 v228, v228, v230\n\t"
      "v_add_f32_e32 v232, v232, v234\n\t"
      "v_cmp_u_f32_e32 vcc, v216, v216\n\t"
      "s_cbranch_vccnz LA33R_%=\n\t"
      "LA33B_%=:\n\t"
      "v_mfma_f32_16x16x32_bf16 v[220:223], v[52:55], v[156:159], v[220:223]\n\t"
      "v_add_f32_e32 v229, v229, v231\n\t"
      "v_add_f32_e32 v233, v233, v235\n\t"
      "v_mfma_f32_16x16x32_bf16 v[220:223], v[68:71], v[160:163], v[220:223]\n\t"
      "s_waitcnt lgkmcnt(0)\n\t"
      "v_add_f32_e32 v236, v236, v238\n\t"
      "v_add_f32_e32 v240, v240, v242\n\t"
      "global_load_dwordx4 v[132:135], v250, s[74:75] offset:-3072 sc1\n\t"
      "v_mfma_f32_16x16x32_bf16 v[220:223], v[84:87], v[164:167], v[220:223]\n\t"
      "v_add_f32_e32 v237, v237, v239\n\t"
      "v_add_f32_e32 v241, v241, v243\n\t"
      "v_mfma_f32_16x16x32_bf16 v[220:223], v[100:103], v[168:171], v[220:223]\n\t"
      "v_add_f32_e32 v228, v228, v232\n\t"
      "v_add_f32_e32 v236, v236, v240\n\t"
      "v_add_f32_e32 v229, v229, v233\n\t"
      "ds_write_b128 v245, v[216:219] offset:32768\n\t"
      "v_mfma_f32_16x16x32_bf16 v[220:223], v[116:119], v[172:175], v[220:223]\n\t"
      "v_add_f32_e32 v237, v237, v241\n\t"
      "v_add_f32_e32 v228, v228, v248\n\t"
      "v_mfma_f32_16x16x32_bf16 v[224:227], v[8:11], v[144:147], 0\n\t"
      "v_add_f32_e32 v229, v229, v249\n\t"
      "v_add_f32_e32 v228, v228, v236\n\t"
      "v_add_f32_e32 v229, v229, v237\n\t"
      "global_load_dwordx4 v[136:139], v250, s[74:75] offset:-2048 sc1\n\t"
      "v_mfma_f32_16x16x32_bf16 v[224:227], v[24:27], v[148:151], v[224:227]\n\t"
      "v_mul_f32_e32 v230, v228, v228\n\t"
      "v_mul_f32_e32 v231, v229, v229\n\t"
      "v_mfma_f32_16x16x32_bf16 v[224:227], v[40:43], v[152:155], v[224:227]\n\t"
      "v_fmaak_f32 v230, v252, v230, 0xc0135761\n\t"
      "v_fmaak_f32 v231, v252, v231, 0xc0135761\n\t"
      "v_mul_f32_e32 v230, v228, v230\n\t"
      "v_mfma_f32_16x16x32_bf16 v[224:227], v[56:59], v[156:159], v[224:227]\n\t"
      "v_mul_f32_e32 v231, v229, v231\n\t"
      "v_exp_f32_e32 v230, v230\n\t"
      "v_mfma_f32_16x16x32_bf16 v[224:227], v[72:75], v[160:163], v[224:227]\n\t"
      "v_exp_f32_e32 v231, v231\n\t"
      "v_add_f32_e32 v230, 1.0, v230\n\t"
      "v_add_f32_e32 v231, 1.0, v231\n\t"
      "global_load_dwordx4 v[140:143], v250, s[74:75] offset:-1024 sc1\n\t"
      "v_mfma_f32_16x16x32_bf16 v[224:227], v[88:91], v[164:167], v[224:227]\n\t"
      "v_rcp_f32_e32 v230, v230\n\t"
      "v_rcp_f32_e32 v231, v231\n\t"
      "v_mfma_f32_16x16x32_bf16 v[224:227], v[104:107], v[168:171], v[224:227]\n\t"
      "v_mul_f32_e32 v228, v228, v230\n\t"
      "v_mul_f32_e32 v229, v229, v231\n\t"
      "v_cvt_pk_bf16_f32 v232, v228, v229\n\t"
      "ds_write_b128 v253, v[220:223] offset:33792\n\t"
      "v_mfma_f32_16x16x32_bf16 v[224:227], v[120:123], v[172:175], v[224:227]\n\t"
      "v_min_u32_e32 v254, -2, v232\n\t"
      "global_store_dword v247, v254, s[62:63] sc1\n\t"
      "s_add_u32 s62, s62, 57344\n\t"
      "s_addc_u32 s63, s63, 0\n\t"
      "v_mfma_f32_16x16x32_bf16 v[208:211], v[12:15], v[144:147], 0\n\t"
      "global_load_dwordx4 v[144:147], v250, s[74:75] sc1\n\t"
      "v_mfma_f32_16x16x32_bf16 v[208:211], v[28:31], v[148:151], v[208:211]\n\t"
      "v_mfma_f32_16x16x32_bf16 v[208:211], v[44:47], v[152:155], v[208:211]\n\t"
      "v_mfma_f32_16x16x32_bf16 v[208:211], v[60:63], v[156:159], v[208:211]\n\t"
      "v_mfma_f32_16x16x32_bf16 v[208:211], v[76:79], v[160:163], v[208:211]\n\t"
      "global_load_dwordx4 v[148:151], v250, s[74:75] offset:1024 sc1\n\t"
      "v_mfma_f32_16x16x32_bf16 v[208:211], v[92:95], v[164:167], v[208:211]\n\t"
      "v_mfma_f32_16x16x32_bf16 v[208:211], v[108:111], v[168:171], v[208:211]\n\t"
      "ds_write_b128 v245, v[224:227] offset:34816\n\t"
      "v_mfma_f32_16x16x32_bf16 v[208:211], v[124:127], v[172:175], v[208:211]\n\t"
      "s_waitcnt vmcnt(12)\n\t"
      "v_mfma_f32_16x16x32_bf16 v[212:215], v[0:3], v[176:179], 0\n\t"
      "global_load_dwordx4 v[152:155], v250, s[74:75] offset:2048 sc1\n\t"
      "v_mfma_f32_16x16x32_bf16 v[212:215], v[16:19], v[180:183], v[212:215]\n\t"
      "s_nop 3\n\t"
      "ds_write_b128 v253, v[208:211] offset:35840\n\t"
      "v_mfma_f32_16x16x32_bf16 v[212:215], v[32:35], v[184:187], v[212:215]\n\t"
      "v_mfma_f32_16x16x32_bf16 v[212:215], v[48:51], v[188:191], v[212:215]\n\t"
      "s_waitcnt vmcnt(8)\n\t"
      "v_mfma_f32_16x16x32_bf16 v[212:215], v[64:67], v[192:195], v[212:215]\n\t"
      "global_load_dwordx4 v[156:159], v250, s[74:75] offset:3072 sc1\n\t"
      "v_mfma_f32_16x16x32_bf16 v[212:215], v[80:83], v[196:199], v[212:215]\n\t"
      "s_waitcnt lgkmcnt(0)\n\t"
      "s_barrier\n\t"
      "ds_read_b64 v[228:229], v246 offset:32768\n\t"
      "ds_read_b64 v[230:231], v246 offset:36864\n\t"
      "v_mfma_f32_16x16x32_bf16 v[212:215], v[96:99], v[200:203], v[212:215]\n\t"
      "ds_read_b64 v[232:233], v246 offset:40960\n\t"
      "ds_read_b64 v[234:235], v246 offset:45056\n\t"
      "v_mfma_f32_16x16x32_bf16 v[212:215], v[112:115], v[204:207], v[212:215]\n\t"
      "ds_read_b64 v[236:237], v246 offset:49152\n\t"
      "ds_read_b64 v[238:239], v246 offset:53248\n\t"
      "v_mfma_f32_16x16x32_bf16 v[216:219], v[4:7], v[176:179], 0\n\t"
      "ds_read_b64 v[240:241], v246 offset:57344\n\t"
      "ds_read_b64 v[242:243], v246 offset:61440\n\t"
      "global_load_dwordx4 v[160:163], v250, s[76:77] offset:-4096 sc1\n\t"
      "v_mfma_f32_16x16x32_bf16 v[216:219], v[20:23], v[180:183], v[216:219]\n\t"
      "v_mfma_f32_16x16x32_bf16 v[216:219], v[36:39], v[184:187], v[216:219]\n\t"
      "s_waitcnt lgkmcnt(4)\n\t"
      "v_add_f32_e32 v228, v228, v230\n\t"
      "v_add_f32_e32 v232, v232, v234\n\t"
      "v_cmp_u_f32_e32 vcc, v212, v212\n\t"
      "s_cbranch_vccnz LA34R_%=\n\t"
      "LA34B_%=:\n\t"
      "v_mfma_f32_16x16x32_bf16 v[216:219], v[52:55], v[188:191], v[216:219]\n\t"
      "v_add_f32_e32 v229, v229, v231\n\t"
      "v_add_f32_e32 v233, v233, v235\n\t"
      "v_mfma_f32_16x16x32_bf16 v[216:219], v[68:71], v[192:195], v[216:219]\n\t"
      "s_waitcnt lgkmcnt(0)\n\t"
      "v_add_f32_e32 v236, v236, v238\n\t"
      "v_add_f32_e32 v240, v240, v242\n\t"
      "global_load_dwordx4 v[164:167], v250, s[76:77] offset:-3072 sc1\n\t"
      "v_mfma_f32_16x16x32_bf16 v[216:219], v[84:87], v[196:199], v[216:219]\n\t"
      "v_add_f32_e32 v237, v237, v239\n\t"
      "v_add_f32_e32 v241, v241, v243\n\t"
      "v_mfma_f32_16x16x32_bf16 v[216:219], v[100:103], v[200:203], v[216:219]\n\t"
      "v_add_f32_e32 v228, v228, v232\n\t"
      "v_add_f32_e32 v236, v236, v240\n\t"
      "v_add_f32_e32 v229, v229, v233\n\t"
      "ds_write_b128 v245, v[212:215]\n\t"
      "v_mfma_f32_16x16x32_bf16 v[216:219], v[116:119], v[204:207], v[216:219]\n\t"
      "v_add_f32_e32 v237, v237, v241\n\t"
      "v_add_f32_e32 v228, v228, v248\n\t"
      "v_mfma_f32_16x16x32_bf16 v[220:223], v[8:11], v[176:179], 0\n\t"
      "v_add_f32_e32 v229, v229, v249\n\t"
      "v_add_f32_e32 v228, v228, v236\n\t"
      "v_add_f32_e32 v229, v229, v237\n\t"
      "global_load_dwordx4 v[168:171], v250, s[76:77] offset:-2048 sc1\n\t"
      "v_mfma_f32_16x16x32_bf16 v[220:223], v[24:27], v[180:183], v[220:223]\n\t"
      "v_mul_f32_e32 v230, v228, v228\n\t"
      "v_mul_f32_e32 v231, v229, v229\n\t"
      "v_mfma_f32_16x16x32_bf16 v[220:223], v[40:43], v[184:187], v[220:223]\n\t"
      "v_fmaak_f32 v230, v252, v230, 0xc0135761\n\t"
      "v_fmaak_f32 v231, v252, v231, 0xc0135761\n\t"
      "v_mul_f32_e32 v230, v228, v230\n\t"
      "v_mfma_f32_16x16x32_bf16 v[220:223], v[56:59], v[188:191], v[220:223]\n\t"
      "v_mul_f32_e32 v231, v229, v231\n\t"
      "v_exp_f32_e32 v230, v230\n\t"
      "v_mfma_f32_16x16x32_bf16 v[220:223], v[72:75], v[192:195], v[220:223]\n\t"
      "v_exp_f32_e32 v231, v231\n\t"
      "v_add_f32_e32 v230, 1.0, v230\n\t"
      "v_add_f32_e32 v231, 1.0, v231\n\t"
      "global_load_dwordx4 v[172:175], v250, s[76:77] offset:-1024 sc1\n\t"
      "v_mfma_f32_16x16x32_bf16 v[220:223], v[88:91], v[196:199], v[220:223]\n\t"
      "v_rcp_f32_e32 v230, v230\n\t"
      "v_rcp_f32_e32 v231, v231\n\t"
      "v_mfma_f32_16x16x32_bf16 v[220:223], v[104:107], v[200:203], v[220:223]\n\t"
      "v_mul_f32_e32 v228, v228, v230\n\t"
      "v_mul_f32_e32 v229, v229, v231\n\t"
      "v_cvt_pk_bf16_f32 v232, v228, v229\n\t"
      "ds_write_b128 v253, v[216:219] offset:1024\n\t"
      "v_mfma_f32_16x16x32_bf16 v[220:223], v[120:123], v[204:207], v[220:223]\n\t"
      "v_min_u32_e32 v254, -2, v232\n\t"
      "global_store_dword v247, v254, s[62:63] sc1\n\t"
      "s_add_u32 s62, s62, 57344\n\t"
      "s_addc_u32 s63, s63, 0\n\t"
      "v_mfma_f32_16x16x32_bf16 v[224:227], v[12:15], v[176:179], 0\n\t"
      "global_load_dwordx4 v[176:179], v250, s[76:77] sc1\n\t"
      "v_mfma_f32_16x16x32_bf16 v[224:227], v[28:31], v[180:183], v[224:227]\n\t"
      "v_mfma_f32_16x16x32_bf16 v[224:227], v[44:47], v[184:187], v[224:227]\n\t"
      "v_mfma_f32_16x16x32_bf16 v[224:227], v[60:63], v[188:191], v[224:227]\n\t"
      "v_mfma_f32_16x16x32_bf16 v[224:227], v[76:79], v[192:195], v[224:227]\n\t"
      "global_load_dwordx4 v[180:183], v250, s[76:77] offset:1024 sc1\n\t"
      "v_mfma_f32_16x16x32_bf16 v[224:227], v[92:95], v[196:199], v[224:227]\n\t"
      "v_mfma_f32_16x16x32_bf16 v[224:227], v[108:111], v[200:203], v[224:227]\n\t"
      "ds_write_b128 v245, v[220:223] offset:2048\n\t"
      "v_mfma_f32_16x16x32_bf16 v[224:227], v[124:127], v[204:207], v[224:227]\n\t"
      "s_waitcnt vmcnt(12)\n\t"
      "v_mfma_f32_16x16x32_bf16 v[208:211], v[0:3], v[128:131], 0\n\t"
      "global_load_dwordx4 v[184:187], v250, s[76:77] offset:2048 sc1\n\t"
      "v_mfma_f32_16x16x32_bf16 v[208:211], v[16:19], v[132:135], v[208:211]\n\t"
      "s_nop 3\n\t"
      "ds_write_b128 v253, v[224:227] offset:3072\n\t"
      "v_mfma_f32_16x16x32_bf16 v[208:211], v[32:35], v[136:139], v[208:211]\n\t"
      "v_mfma_f32_16x16x32_bf16 v[208:211], v[48:51], v[140:143], v[208:211]\n\t"
      "s_waitcnt vmcnt(8)\n\t"
      "v_mfma_f32_16x16x32_bf16 v[208:211], v[64:67], v[144:147], v[208:211]\n\t"
      "global_load_dwordx4 v[188:191], v250, s[76:77] offset:3072 sc1\n\t"
      "v_mfma_f32_16x16x32_bf16 v[208:211], v[80:83], v[148:151], v[208:211]\n\t"
      "s_waitcnt lgkmcnt(0)\n\t"
      "s_barrier\n\t"
      "ds_read_b64 v[228:229], v246\n\t"
      "ds_read_b64 v[230:231], v246 offset:4096\n\t"
      "v_mfma_f32_16x16x32_bf16 v[208:211], v[96:99], v[152:155], v[208:211]\n\t"
      "ds_read_b64 v[232:233], v246 offset:8192\n\t"
      "ds_read_b64 v[234:235], v246 offset:12288\n\t"
      "v_mfma_f32_16x16x32_bf16 v[208:211], v[112:115], v[156:159], v[208:211]\n\t"
      "ds_read_b64 v[236:237], v246 offset:16384\n\t"
      "ds_read_b64 v[238:239], v246 offset:20480\n\t"
      "v_mfma_f32_16x16x32_bf16 v[212:215], v[4:7], v[128:131], 0\n\t"
      "ds_read_b64 v[240:241], v246 offset:24576\n\t"
      "ds_read_b64 v[242:243], v246 offset:28672\n\t"
      "v_mfma_f32_16x16x32_bf16 v[212:215], v[20:23], v[132:135], v[212:215]\n\t"
      "v_mfma_f32_16x16x32_bf16 v[212:215], v[36:39], v[136:139], v[212:215]\n\t"
      "s_waitcnt lgkmcnt(4)\n\t"
      "v_add_f32_e32 v228, v228, v230\n\t"
      "v_add_f32_e32 v232, v232, v234\n\t"
      "v_cmp_u_f32_e32 vcc, v208, v208\n\t"
      "s_cbranch_vccnz LA35R_%=\n\t"
      "LA35B_%=:\n\t"
      "v_mfma_f32_16x16x32_bf16 v[212:215], v[52:55], v[140:143], v[212:215]\n\t"
      "v_add_f32_e32 v229, v229, v231\n\t"
      "v_add_f32_e32 v233, v233, v235\n\t"
      "v_mfma_f32_16x16x32_bf16 v[212:215], v[68:71], v[144:147], v[212:215]\n\t"
      "s_waitcnt lgkmcnt(0)\n\t"
      "v_add_f32_e32 v236, v236, v238\n\t"
      "v_add_f32_e32 v240, v240, v242\n\t"
      "v_mfma_f32_16x16x32_bf16 v[212:215], v[84:87], v[148:151], v[212:215]\n\t"
      "v_add_f32_e32 v237, v237, v239\n\t"
      "v_add_f32_e32 v241, v241, v243\n\t"
      "v_mfma_f32_16x16x32_bf16 v[212:215], v[100:103], v[152:155], v[212:215]\n\t"
      "v_add_f32_e32 v228, v228, v232\n\t"
      "v_add_f32_e32 v236, v236, v240\n\t"
      "v_add_f32_e32 v229, v229, v233\n\t"
      "ds_write_b128 v245, v[208:211] offset:32768\n\t"
      "v_mfma_f32_16x16x32_bf16 v[212:215], v[116:119], v[156:159], v[212:215]\n\t"
      "v_add_f32_e32 v237, v237, v241\n\t"
      "v_add_f32_e32 v228, v228, v248\n\t"
      "v_mfma_f32_16x16x32_bf16 v[216:219], v[8:11], v[128:131], 0\n\t"
      "v_add_f32_e32 v229, v229, v249\n\t"
      "v_add_f32_e32 v228, v228, v236\n\t"
      "v_add_f32_e32 v229, v229, v237\n\t"
      "v_mfma_f32_16x16x32_bf16 v[216:219], v[24:27], v[132:135], v[216:219]\n\t"
      "v_mul_f32_e32 v230, v228, v228\n\t"
      "v_mul_f32_e32 v231, v229, v229\n\t"
      "v_mfma_f32_16x16x32_bf16 v[216:219], v[40:43], v[136:139], v[216:219]\n\t"
      "v_fmaak_f32 v230, v252, v230, 0xc0135761\n\t"
      "v_fmaak_f32 v231, v252, v231, 0xc0135761\n\t"
      "v_mul_f32_e32 v230, v228, v230\n\t"
      "v_mfma_f32_16x16x32_bf16 v[216:219], v[56:59], v[140:143], v[216:219]\n\t"
      "v_mul_f32_e32 v231, v229, v231\n\t"
      "v_exp_f32_e32 v230, v230\n\t"
      "v_mfma_f32_16x16x32_bf16 v[216:219], v[72:75], v[144:147], v[216:219]\n\t"
      "v_exp_f32_e32 v231, v231\n\t"
      "v_add_f32_e32 v230, 1.0, v230\n\t"
      "v_add_f32_e32 v231, 1.0, v231\n\t"
      "v_mfma_f32_16x16x32_bf16 v[216:219], v[88:91], v[148:151], v[216:219]\n\t"
      "v_rcp_f32_e32 v230, v230\n\t"
      "v_rcp_f32_e32 v231, v231\n\t"
      "v_mfma_f32_16x16x32_bf16 v[216:219], v[104:107], v[152:155], v[216:219]\n\t"
      "v_mul_f32_e32 v228, v228, v230\n\t"
      "v_mul_f32_e32 v229, v229, v231\n\t"
      "v_cvt_pk_bf16_f32 v232, v228, v229\n\t"
      "ds_write_b128 v253, v[212:215] offset:33792\n\t"
      "v_mfma_f32_16x16x32_bf16 v[216:219], v[120:123], v[156:159], v[216:219]\n\t"
      "v_min_u32_e32 v254, -2, v232\n\t"
      "global_store_dword v247, v254, s[62:63] sc1\n\t"
      "s_add_u32 s62, s62, 57344\n\t"
      "s_addc_u32 s63, s63, 0\n\t"
      "v_mfma_f32_16x16x32_bf16 v[220:223], v[12:15], v[128:131], 0\n\t"
      "v_mfma_f32_16x16x32_bf16 v[220:223], v[28:31], v[132:135], v[220:223]\n\t"
      "v_mfma_f32_16x16x32_bf16 v[220:223], v[44:47], v[136:139], v[220:223]\n\t"
      "v_mfma_f32_16x16x32_bf16 v[220:223], v[60:63], v[140:143], v[220:223]\n\t"
      "v_mfma_f32_16x16x32_bf16 v[220:223], v[76:79], v[144:147], v[220:223]\n\t"
      "v_mfma_f32_16x16x32_bf16 v[220:223], v[92:95], v[148:151], v[220:223]\n\t"
      "v_mfma_f32_16x16x32_bf16 v[220:223], v[108:111], v[152:155], v[220:223]\n\t"
      "ds_write_b128 v245, v[216:219] offset:34816\n\t"
      "v_mfma_f32_16x16x32_bf16 v[220:223], v[124:127], v[156:159], v[220:223]\n\t"
      "s_waitcnt vmcnt(6)\n\t"
      "v_mfma_f32_16x16x32_bf16 v[224:227], v[0:3], v[160:163], 0\n\t"
      "v_mfma_f32_16x16x32_bf16 v[224:227], v[16:19], v[164:167], v[224:227]\n\t"
      "s_nop 4\n\t"
      "ds_write_b128 v253, v[220:223] offset:35840\n\t"
      "v_mfma_f32_16x16x32_bf16 v[224:227], v[32:35], v[168:171], v[224:227]\n\t"
      "v_mfma_f32_16x16x32_bf16 v[224:227], v[48:51], v[172:175], v[224:227]\n\t"
      "s_waitcnt vmcnt(1)\n\t"
      "v_mfma_f32_16x16x32_bf16 v[224:227], v[64:67], v[176:179], v[224:227]\n\t"
      "v_mfma_f32_16x16x32_bf16 v[224:227], v[80:83], v[180:183], v[224:227]\n\t"
      "s_waitcnt lgkmcnt(0)\n\t"
      "s_barrier\n\t"
      "ds_read_b64 v[228:229], v246 offset:32768\n\t"
      "ds_read_b64 v[230:231], v246 offset:36864\n\t"
      "v_mfma_f32_16x16x32_bf16 v[224:227], v[96:99], v[184:187], v[224:227]\n\t"
      "ds_read_b64 v[232:233], v246 offset:40960\n\t"
      "ds_read_b64 v[234:235], v246 offset:45056\n\t"
      "v_mfma_f32_16x16x32_bf16 v[224:227], v[112:115], v[188:191], v[224:227]\n\t"
      "ds_read_b64 v[236:237], v246 offset:49152\n\t"
      "ds_read_b64 v[238:239], v246 offset:53248\n\t"
      "v_mfma_f32_16x16x32_bf16 v[208:211], v[4:7], v[160:163], 0\n\t"
      "ds_read_b64 v[240:241], v246 offset:57344\n\t"
      "ds_read_b64 v[242:243], v246 offset:61440\n\t"
      "v_mfma_f32_16x16x32_bf16 v[208:211], v[20:23], v[164:167], v[208:211]\n\t"
      "v_mfma_f32_16x16x32_bf16 v[208:211], v[36:39], v[168:171], v[208:211]\n\t"
      "s_waitcnt lgkmcnt(4)\n\t"
      "v_add_f32_e32 v228, v228, v230\n\t"
      "v_add_f32_e32 v232, v232, v234\n\t"
      "v_cmp_u_f32_e32 vcc, v224, v224\n\t"
      "s_cbranch_vccnz LA36R_%=\n\t"
      "LA36B_%=:\n\t"
      "v_mfma_f32_16x16x32_bf16 v[208:211], v[52:55], v[172:175], v[208:211]\n\t"
      "v_add_f32_e32 v229, v229, v231\n\t"
      "v_add_f32_e32 v233, v233, v235\n\t"
      "v_mfma_f32_16x16x32_bf16 v[208:211], v[68:71], v[176:179], v[208:211]\n\t"
      "s_waitcnt lgkmcnt(0)\n\t"
      "v_add_f32_e32 v236, v236, v238\n\t"
      "v_add_f32_e32 v240, v240, v242\n\t"
      "v_mfma_f32_16x16x32_bf16 v[208:211], v[84:87], v[180:183], v[208:211]\n\t"
      "v_add_f32_e32 v237, v237, v239\n\t"
      "v_add_f32_e32 v241, v241, v243\n\t"
      "v_mfma_f32_16x16x32_bf16 v[208:211], v[100:103], v[184:187], v[208:211]\n\t"
      "v_add_f32_e32 v228, v228, v232\n\t"
      "v_add_f32_e32 v236, v236, v240\n\t"
      "v_add_f32_e32 v229, v229, v233\n\t"
      "ds_write_b128 v245, v[224:227]\n\t"
      "v_mfma_f32_16x16x32_bf16 v[208:211], v[116:119], v[188:191], v[208:211]\n\t"
      "v_add_f32_e32 v237, v237, v241\n\t"
      "v_add_f32_e32 v228, v228, v248\n\t"
      "v_mfma_f32_16x16x32_bf16 v[212:215], v[8:11], v[160:163], 0\n\t"
      "v_add_f32_e32 v229, v229, v249\n\t"
      "v_add_f32_e32 v228, v228, v236\n\t"
      "v_add_f32_e32 v229, v229, v237\n\t"
      "v_mfma_f32_16x16x32_bf16 v[212:215], v[24:27], v[164:167], v[212:215]\n\t"
      "v_mul_f32_e32 v230, v228, v228\n\t"
      "v_mul_f32_e32 v231, v229, v229\n\t"
      "v_mfma_f32_16x16x32_bf16 v[212:215], v[40:43], v[168:171], v[212:215]\n\t"
      "v_fmaak_f32 v230, v252, v230, 0xc0135761\n\t"
      "v_fmaak_f32 v231, v252, v231, 0xc0135761\n\t"
      "v_mul_f32_e32 v230, v228, v230\n\t"
      "v_mfma_f32_16x16x32_bf16 v[212:215], v[56:59], v[172:175], v[212:215]\n\t"
      "v_mul_f32_e32 v231, v229, v231\n\t"
      "v_exp_f32_e32 v230, v230\n\t"
      "v_mfma_f32_16x16x32_bf16 v[212:215], v[72:75], v[176:179], v[212:215]\n\t"
      "v_exp_f32_e32 v231, v231\n\t"
      "v_add_f32_e32 v230, 1.0, v230\n\t"
      "v_add_f32_e32 v231, 1.0, v231\n\t"
      "v_mfma_f32_16x16x32_bf16 v[212:215], v[88:91], v[180:183], v[212:215]\n\t"
      "v_rcp_f32_e32 v230, v230\n\t"
      "v_rcp_f32_e32 v231, v231\n\t"
      "v_mfma_f32_16x16x32_bf16 v[212:215], v[104:107], v[184:187], v[212:215]\n\t"
      "v_mul_f32_e32 v228, v228, v230\n\t"
      "v_mul_f32_e32 v229, v229, v231\n\t"
      "v_cvt_pk_bf16_f32 v232, v228, v229\n\t"
      "ds_write_b128 v253, v[208:211] offset:1024\n\t"
      "v_mfma_f32_16x16x32_bf16 v[212:215], v[120:123], v[188:191], v[212:215]\n\t"
      "v_min_u32_e32 v254, -2, v232\n\t"
      "global_store_dword v247, v254, s[62:63] sc1\n\t"
      "s_add_u32 s62, s62, 57344\n\t"
      "s_addc_u32 s63, s63, 0\n\t"
      "v_mfma_f32_16x16x32_bf16 v[216:219], v[12:15], v[160:163], 0\n\t"
      "v_mfma_f32_16x16x32_bf16 v[216:219], v[28:31], v[164:167], v[216:219]\n\t"
      "v_mfma_f32_16x16x32_bf16 v[216:219], v[44:47], v[168:171], v[216:219]\n\t"
      "v_mfma_f32_16x16x32_bf16 v[216:219], v[60:63], v[172:175], v[216:219]\n\t"
      "v_mfma_f32_16x16x32_bf16 v[216:219], v[76:79], v[176:179], v[216:219]\n\t"
      "v_mfma_f32_16x16x32_bf16 v[216:219], v[92:95], v[180:183], v[216:219]\n\t"
      "v_mfma_f32_16x16x32_bf16 v[216:219], v[108:111], v[184:187], v[216:219]\n\t"
      "ds_write_b128 v245, v[212:215] offset:2048\n\t"
      "v_mfma_f32_16x16x32_bf16 v[216:219], v[124:127], v[188:191], v[216:219]\n\t"
      "s_nop 7\n\t"
      "ds_write_b128 v253, v[216:219] offset:3072\n\t"
      "s_waitcnt lgkmcnt(0)\n\t"
      "s_barrier\n\t"
      "ds_read_b64 v[228:229], v246\n\t"
      "ds_read_b64 v[230:231], v246 offset:4096\n\t"
      "ds_read_b64 v[232:233], v246 offset:8192\n\t"
      "ds_read_b64 v[234:235], v246 offset:12288\n\t"
      "ds_read_b64 v[236:237], v246 offset:16384\n\t"
      "ds_read_b64 v[238:239], v246 offset:20480\n\t"
      "ds_read_b64 v[240:241], v246 offset:24576\n\t"
      "ds_read_b64 v[242:243], v246 offset:28672\n\t"
      "s_waitcnt lgkmcnt(4)\n\t"
      "v_add_f32_e32 v228, v228, v230\n\t"
      "v_add_f32_e32 v232, v232, v234\n\t"
      "v_add_f32_e32 v229, v229, v231\n\t"
      "v_add_f32_e32 v233, v233, v235\n\t"
      "s_waitcnt lgkmcnt(0)\n\t"
      "v_add_f32_e32 v236, v236, v238\n\t"
      "v_add_f32_e32 v240, v240, v242\n\t"
      "v_add_f32_e32 v237, v237, v239\n\t"
      "v_add_f32_e32 v241, v241, v243\n\t"
      "v_add_f32_e32 v228, v228, v232\n\t"
      "v_add_f32_e32 v236, v236, v240\n\t"
      "v_add_f32_e32 v229, v229, v233\n\t"
      "v_add_f32_e32 v237, v237, v241\n\t"
      "v_add_f32_e32 v228, v228, v248\n\t"
      "v_add_f32_e32 v229, v229, v249\n\t"
      "v_add_f32_e32 v228, v228, v236\n\t"
      "v_add_f32_e32 v229, v229, v237\n\t"
      "v_mul_f32_e32 v230, v228, v228\n\t"
      "v_mul_f32_e32 v231, v229, v229\n\t"
      "v_fmaak_f32 v230, v252, v230, 0xc0135761\n\t"
      "v_fmaak_f32 v231, v252, v231, 0xc0135761\n\t"
      "v_mul_f32_e32 v230, v228, v230\n\t"
      "v_mul_f32_e32 v231, v229, v231\n\t"
      "v_exp_f32_e32 v230, v230\n\t"
      "v_exp_f32_e32 v231, v231\n\t"
      "v_add_f32_e32 v230, 1.0, v230\n\t"
      "v_add_f32_e32 v231, 1.0, v231\n\t"
      "v_rcp_f32_e32 v230, v230\n\t"
      "v_rcp_f32_e32 v231, v231\n\t"
      "v_mul_f32_e32 v228, v228, v230\n\t"
      "v_mul_f32_e32 v229, v229, v231\n\t"
      "v_cvt_pk_bf16_f32 v232, v228, v229\n\t"
      "v_min_u32_e32 v254, -2, v232\n\t"
      "global_store_dword v247, v254, s[62:63] sc1\n\t"
      "s_add_u32 s62, s62, 57344\n\t"
      "s_addc_u32 s63, s63, 0\n\t"
      "s_or_b32 s34, s2, s24\n\t"
      "s_cbranch_scc1 EPB_%=\n\t"
      "v_mov_b32_e32 v228, s22\n\t"
      "v_mov_b32_e32 v229, s23\n\t"
      "v_mov_b32_e32 v230, 0\n\t"
      "s_mov_b64 exec, 1\n\t"
      "global_store_dwordx2 v230, v[228:229], s[60:61] sc1\n\t"
      "s_mov_b64 exec, -1\n\t"
      "EPB_%=:\n\t"
      "s_endpgm\n\t"
      "LPATH_%=:\n\t"
      "s_add_u32 s22, s22, 1\n\t"
      "s_addc_u32 s23, s23, 0\n\t"
      "s_add_u32 s72, s26, 1\n\t"
      "s_and_b32 s72, s72, 6\n\t"
      "s_cselect_b32 s72, 0, 2\n\t"
      "s_or_b32 s27, s27, s72\n\t"
      "s_mul_i32 s72, s81, 172032\n\t"
      "s_add_u32 s72, s72, s37\n\t"
      "s_add_u32 s40, s6, s72\n\t"
      "s_addc_u32 s41, s7, 0\n\t"
      "s_add_u32 s81, s81, 1\n\t"
      "s_cmp_eq_u32 s81, 7\n\t"
      "s_cselect_b32 s81, 0, s81\n\t"
      "s_add_u32 m0, s39, 81920\n\t"
      "s_nop 0\n\t"
      "global_load_lds_dwordx4 v148, s[40:41]\n\t"
      "global_load_lds_dwordx4 v149, s[40:41] offset:1024\n\t"
      "global_load_lds_dwordx4 v150, s[40:41] offset:2048\n\t"
      "global_load_lds_dwordx4 v151, s[40:41] offset:3072\n\t"
      "s_mul_i32 s72, s81, 172032\n\t"
      "s_add_u32 s72, s72, s37\n\t"
      "s_add_u32 s40, s6, s72\n\t"
      "s_addc_u32 s41, s7, 0\n\t"
      "s_add_u32 s81, s81, 1\n\t"
      "s_cmp_eq_u32 s81, 7\n\t"
      "s_cselect_b32 s81, 0, s81\n\t"
      "s_add_u32 m0, s39, 98304\n\t"
      "s_nop 0\n\t"
      "global_load_lds_dwordx4 v148, s[40:41]\n\t"
      "global_load_lds_dwordx4 v149, s[40:41] offset:1024\n\t"
      "s_waitcnt vmcnt(14)\n\t"
      "s_barrier\n\t"
      "v_add_u32_e32 v172, 0x10000, v224\n\t"
      "v_add_u32_e32 v173, 0x10000, v225\n\t"
      "v_add_u32_e32 v174, 0x10000, v226\n\t"
      "v_add_u32_e32 v175, 0x10000, v227\n\t"
      "s_lshl_b32 s34, s25, 8\n\t"
      "s_add_u32 s34, s34, 4096\n\t"
      "v_and_b32_e32 v136, 1, v192\n\t"
      "v_lshl_add_u32 v136, v136, 7, s34\n\t"
      "s_lshl_b32 s34, s25, 7\n\t"
      "s_add_u32 s34, s34, 16384\n\t"
      "v_mov_b32_e32 v137, s34\n\t"
      "s_lshl_b32 s34, s2, 7\n\t"
      "s_add_u32 s34, s34, 32768\n\t"
      "v_mov_b32_e32 v143, s34\n\t"
      "s_bitcmp0_b32 s27, 1\n\t"
      "s_cbranch_scc1 SK6_%=\n\t"
      "s_lshr_b32 s73, s26, 2\n\t"
      "s_lshl1_add_u32 s73, s25, s73\n\t"
      "s_lshl_b32 s72, s73, 7\n\t"
      "s_add_u32 s72, s72, 4096\n\t"
      "v_mov_b32_e32 v130, s72\n\t"
      "s_lshl2_add_u32 s75, s73, s24\n\t"
      "v_mov_b32_e32 v128, s22\n\t"
      "v_mov_b32_e32 v129, s23\n\t"
      "v_lshrrev_b32_e32 v140, 1, v244\n\t"
      "s_lshl_b32 s72, s75, 9\n\t"
      "s_add_u32 s72, s72, 12582912\n\t"
      "v_mov_b32_e32 v132, -1\n\t"
      "v_mov_b32_e32 v133, -1\n\t"
      "v_mov_b32_e32 v134, -1\n\t"
      "v_mov_b32_e32 v135, -1\n\t"
      "s_add_u32 s42, s20, s72\n\t"
      "s_addc_u32 s43, s21, 0\n\t"
      "global_store_dwordx2 v140, v[132:133], s[42:43] sc1\n\t"
      "s_lshl_b32 s72, s75, 13\n\t"
      "s_add_u32 s72, s72, 4096\n\t"
      "s_add_u32 s42, s20, s72\n\t"
      "s_addc_u32 s43, s21, 0\n\t"
      "global_store_dwordx4 v244, v[132:135], s[42:43] offset:-4096 sc1\n\t"
      "global_store_dwordx4 v244, v[132:135], s[42:43] offset:-3072 sc1\n\t"
      "global_store_dwordx4 v244, v[132:135], s[42:43] offset:-2048 sc1\n\t"
      "SK6_%=:\n\t"
      "s_waitcnt vmcnt(10)\n\t"
      "s_barrier\n\t"
      "global_load_lds_dwordx4 v150, s[40:41] offset:2048\n\t"
      "global_load_lds_dwordx4 v151, s[40:41] offset:3072\n\t"
      "s_bitcmp0_b32 s27, 1\n\t"
      "s_cbranch_scc1 SK7_%=\n\t"
      "global_store_dwordx4 v244, v[132:135], s[42:43] offset:-1024 sc1\n\t"
      "global_store_dwordx4 v244, v[132:135], s[42:43] sc1\n\t"
      "global_store_dwordx4 v244, v[132:135], s[42:43] offset:1024 sc1\n\t"
      "SK7_%=:\n\t"
      "s_waitcnt vmcnt(8)\n\t"
      "s_barrier\n\t"
      "s_mul_i32 s72, s81, 172032\n\t"
      "s_add_u32 s72, s72, s37\n\t"
      "s_add_u32 s40, s6, s72\n\t"
      "s_addc_u32 s41, s7, 0\n\t"
      "s_add_u32 s81, s81, 1\n\t"
      "s_cmp_eq_u32 s81, 7\n\t"
      "s_cselect_b32 s81, 0, s81\n\t"
      "s_add_u32 m0, s39, 114688\n\t"
      "s_nop 0\n\t"
      "global_load_lds_dwordx4 v148, s[40:41]\n\t"
      "global_load_lds_dwordx4 v149, s[40:41] offset:1024\n\t"
      "global_load_lds_dwordx4 v150, s[40:41] offset:2048\n\t"
      "global_load_lds_dwordx4 v151, s[40:41] offset:3072\n\t"
      "s_bitcmp0_b32 s27, 1\n\t"
      "s_cbranch_scc1 SK8_%=\n\t"
      "global_store_dwordx4 v244, v[132:135], s[42:43] offset:2048 sc1\n\t"
      "global_store_dwordx4 v244, v[132:135], s[42:43] offset:3072 sc1\n\t"
      "SK8_%=:\n\t"
      "s_waitcnt vmcnt(8)\n\t"
      "s_barrier\n\t"
      "s_mul_i32 s72, s81, 172032\n\t"
      "s_add_u32 s72, s72, s37\n\t"
      "s_add_u32 s40, s6, s72\n\t"
      "s_addc_u32 s41, s7, 0\n\t"
      "s_add_u32 s81, s81, 1\n\t"
      "s_cmp_eq_u32 s81, 7\n\t"
      "s_cselect_b32 s81, 0, s81\n\t"
      "s_add_u32 m0, s39, 131072\n\t"
      "s_nop 0\n\t"
      "global_load_lds_dwordx4 v148, s[40:41]\n\t"
      "global_load_lds_dwordx4 v149, s[40:41] offset:1024\n\t"
      "global_load_lds_dwordx4 v150, s[40:41] offset:2048\n\t"
      "global_load_lds_dwordx4 v151, s[40:41] offset:3072\n\t"
      "s_waitcnt vmcnt(8)\n\t"
      "s_bitcmp0_b32 s27, 1\n\t"
      "s_cbranch_scc1 SK9_%=\n\t"
      "s_waitcnt vmcnt(4)\n\t"
      "SK9_%=:\n\t"
      "s_barrier\n\t"
      "s_mul_i32 s72, s81, 172032\n\t"
      "s_add_u32 s72, s72, s37\n\t"
      "s_add_u32 s40, s6, s72\n\t"
      "s_addc_u32 s41, s7, 0\n\t"
      "s_add_u32 s81, s81, 1\n\t"
      "s_cmp_eq_u32 s81, 7\n\t"
      "s_cselect_b32 s81, 0, s81\n\t"
      "s_add_u32 m0, s39, 147456\n\t"
      "s_nop 0\n\t"
      "global_load_lds_dwordx4 v148, s[40:41]\n\t"
      "global_load_lds_dwordx4 v149, s[40:41] offset:1024\n\t"
      "global_load_lds_dwordx4 v150, s[40:41] offset:2048\n\t"
      "global_load_lds_dwordx4 v151, s[40:41] offset:3072\n\t"
      "s_bitcmp0_b32 s27, 1\n\t"
      "s_cbranch_scc1 SK10_%=\n\t"
      "s_cmp_eq_u32 s24, 0\n\t"
      "s_cselect_b64 exec, 1, 0\n\t"
      "global_store_dwordx2 v130, v[128:129], s[60:61] sc1\n\t"
      "s_mov_b64 exec, -1\n\t"
      "SK10_%=:\n\t"
      "s_waitcnt vmcnt(8)\n\t"
      "s_barrier\n\t"
      "s_mul_i32 s72, s82, 172032\n\t"
      "s_add_u32 s72, s72, s37\n\t"
      "s_add_u32 s72, s72, 1792\n\t"
      "s_add_u32 s40, s6, s72\n\t"
      "s_addc_u32 s41, s7, 0\n\t"
      "s_add_u32 s82, s82, 1\n\t"
      "s_cmp_eq_u32 s82, 7\n\t"
      "s_cselect_b32 s82, 0, s82\n\t"
      "s_add_u32 m0, s39, 65536\n\t"
      "s_nop 0\n\t"
      "global_load_lds_dwordx4 v148, s[40:41]\n\t"
      "global_load_lds_dwordx4 v152, s[40:41] offset:1024\n\t"
      "s_mul_i32 s72, s82, 172032\n\t"
      "s_add_u32 s72, s72, s37\n\t"
      "s_add_u32 s72, s72, 1792\n\t"
      "s_add_u32 s40, s6, s72\n\t"
      "s_addc_u32 s41, s7, 0\n\t"
      "s_add_u32 s82, s82, 1\n\t"
      "s_cmp_eq_u32 s82, 7\n\t"
      "s_cselect_b32 s82, 0, s82\n\t"
      "s_add_u32 m0, s39, 67584\n\t"
      "s_nop 0\n\t"
      "global_load_lds_dwordx4 v148, s[40:41]\n\t"
      "global_load_lds_dwordx4 v152, s[40:41] offset:1024\n\t"
      "s_waitcnt vmcnt(8)\n\t"
      "s_barrier\n\t"
      "s_mul_i32 s72, s82, 172032\n\t"
      "s_add_u32 s72, s72, s37\n\t"
      "s_add_u32 s72, s72, 1792\n\t"
      "s_add_u32 s40, s6, s72\n\t"
      "s_addc_u32 s41, s7, 0\n\t"
      "s_add_u32 s82, s82, 1\n\t"
      "s_cmp_eq_u32 s82, 7\n\t"
      "s_cselect_b32 s82, 0, s82\n\t"
      "s_add_u32 m0, s39, 81920\n\t"
      "s_nop 0\n\t"
      "global_load_lds_dwordx4 v148, s[40:41]\n\t"
      "global_load_lds_dwordx4 v152, s[40:41] offset:1024\n\t"
      "s_mul_i32 s72, s82, 172032\n\t"
      "s_add_u32 s72, s72, s37\n\t"
      "s_add_u32 s72, s72, 1792\n\t"
      "s_add_u32 s40, s6, s72\n\t"
      "s_addc_u32 s41, s7, 0\n\t"
      "s_add_u32 s82, s82, 1\n\t"
      "s_cmp_eq_u32 s82, 7\n\t"
      "s_cselect_b32 s82, 0, s82\n\t"
      "s_add_u32 m0, s39, 83968\n\t"
      "s_nop 0\n\t"
      "global_load_lds_dwordx4 v148, s[40:41]\n\t"
      "global_load_lds_dwordx4 v152, s[40:41] offset:1024\n\t"
      "s_waitcnt vmcnt(8)\n\t"
      "s_barrier\n\t"
      "s_cmp_lg_u32 s24, 3\n\t"
      "s_cbranch_scc1 SK11_%=\n\t"
      "global_load_dwordx2 v[138:139], v136, s[60:61] sc1\n\t"
      "SK11_%=:\n\t"
      "s_mul_i32 s72, s82, 172032\n\t"
      "s_add_u32 s72, s72, s37\n\t"
      "s_add_u32 s72, s72, 1792\n\t"
      "s_add_u32 s40, s6, s72\n\t"
      "s_addc_u32 s41, s7, 0\n\t"
      "s_add_u32 s82, s82, 1\n\t"
      "s_cmp_eq_u32 s82, 7\n\t"
      "s_cselect_b32 s82, 0, s82\n\t"
      "s_lshr_b32 s72, s39, 1\n\t"
      "s_add_u32 m0, s72, 98304\n\t"
      "s_nop 0\n\t"
      "global_load_lds_dwordx4 v148, s[40:41]\n\t"
      "global_load_lds_dwordx4 v152, s[40:41] offset:1024\n\t"
      "global_load_dword v249, v248, s[12:13]\n\t"
      "s_cmp_lt_u32 s31, 14\n\t"
      "s_cbranch_scc1 NTA_%=\n\t"
      "s_lshl2_add_u32 s72, s31, s24\n\t"
      "s_sub_u32 s72, s72, 56\n\t"
      "s_mov_b32 m0, 64512\n\t"
      "s_cmp_ge_u32 s72, 64\n\t"
      "s_cselect_b32 s73, 8, 0\n\t"
      "s_sub_u32 s73, s72, s73\n\t"
      "s_and_b32 s73, s73, 0x38\n\t"
      "s_add_u32 s72, s72, s73\n\t"
      "s_mul_i32 s72, s72, 57344\n\t"
      "s_and_b32 s73, s2, 7\n\t"
      "s_mul_i32 s73, s73, 448\n\t"
      "s_and_b32 s73, s73, 0xffffff80\n\t"
      "s_add_u32 s72, s72, s73\n\t"
      "v_lshrrev_b32_e32 v141, 2, v192\n\t"
      "v_mul_u32_u24_e32 v141, 3584, v141\n\t"
      "v_lshl_add_u32 v141, v196, 7, v141\n\t"
      "v_add_u32_e32 v141, s72, v141\n\t"
      "global_load_lds_dword v141, s[10:11]\n\t"
      "NTA_%=:\n\t"
      "s_waitcnt vmcnt(7)\n\t"
      "s_barrier\n\t"
      "s_mul_i32 s72, s24, 14336\n\t"
      "s_add_u32 s40, s56, s72\n\t"
      "s_addc_u32 s41, s57, 0\n\t"
      "s_lshr_b32 s72, s39, 1\n\t"
      "s_add_u32 m0, s72, 106496\n\t"
      "s_nop 0\n\t"
      "global_load_lds_dwordx4 v154, s[40:41]\n\t"
      "global_load_lds_dwordx4 v155, s[40:41] offset:1024\n\t"
      "s_waitcnt vmcnt(5)\n\t"
      "s_barrier\n\t"
      "s_mul_i32 s72, s24, 28672\n\t"
      "s_add_u32 s72, s72, 57344\n\t"
      "s_add_u32 s40, s56, s72\n\t"
      "s_addc_u32 s41, s57, 0\n\t"
      "s_lshr_b32 s72, s39, 0\n\t"
      "s_add_u32 m0, s72, 114688\n\t"
      "s_nop 0\n\t"
      "global_load_lds_dwordx4 v154, s[40:41]\n\t"
      "global_load_lds_dwordx4 v155, s[40:41] offset:1024\n\t"
      "global_load_lds_dwordx4 v156, s[40:41] offset:2048\n\t"
      "global_load_lds_dwordx4 v157, s[40:41] offset:3072\n\t"
      "s_waitcnt vmcnt(7)\n\t"
      "s_cmp_lg_u32 s24, 3\n\t"
      "s_cbranch_scc1 LF1B_%=\n\t"
      "v_cmp_ne_u64_e32 vcc, s[22:23], v[138:139]\n\t"
      "s_cbranch_vccnz LF1R_%=\n\t"
      "LF1B_%=:\n\t"
      "s_barrier\n\t"
      "s_mul_i32 s72, s24, 28672\n\t"
      "s_add_u32 s72, s72, 172032\n\t"
      "s_add_u32 s40, s56, s72\n\t"
      "s_addc_u32 s41, s57, 0\n\t"
      "s_lshr_b32 s72, s39, 0\n\t"
      "s_add_u32 m0, s72, 131072\n\t"
      "s_nop 0\n\t"
      "global_load_lds_dwordx4 v154, s[40:41]\n\t"
      "global_load_lds_dwordx4 v155, s[40:41] offset:1024\n\t"
      "global_load_lds_dwordx4 v156, s[40:41] offset:2048\n\t"
      "global_load_lds_dwordx4 v157, s[40:41] offset:3072\n\t"
      "s_mul_i32 s72, s24, 28672\n\t"
      "s_add_u32 s72, s72, 286720\n\t"
      "s_add_u32 s40, s56, s72\n\t"
      "s_addc_u32 s41, s57, 0\n\t"
      "s_lshr_b32 s72, s39, 0\n\t"
      "s_add_u32 m0, s72, 147456\n\t"
      "s_nop 0\n\t"
      "global_load_lds_dwordx4 v154, s[40:41]\n\t"
      "global_load_lds_dwordx4 v155, s[40:41] offset:1024\n\t"
      "global_load_lds_dwordx4 v156, s[40:41] offset:2048\n\t"
      "global_load_lds_dwordx4 v157, s[40:41] offset:3072\n\t"
      "s_barrier\n\t"
      "s_cmp_eq_u32 s26, 7\n\t"
      "s_cbranch_scc1 SPARE_%=\n\t"
      "s_bitcmp0_b32 s27, 0\n\t"
      "s_cbranch_scc1 L4_%=\n\t"
      "s_add_u32 s72, s76, 0\n\t"
      "s_add_u32 s40, s52, s72\n\t"
      "s_addc_u32 s41, s53, 0\n\t"
      "s_add_u32 m0, s39, 65536\n\t"
      "s_nop 0\n\t"
      "global_load_lds_dwordx4 v144, s[40:41]\n\t"
      "global_load_lds_dwordx4 v145, s[40:41] offset:1024\n\t"
      "global_load_lds_dwordx4 v146, s[40:41] offset:2048\n\t"
      "global_load_lds_dwordx4 v147, s[40:41] offset:3072\n\t"
      "s_add_u32 s72, s76, 229376\n\t"
      "s_add_u32 s40, s52, s72\n\t"
      "s_addc_u32 s41, s53, 0\n\t"
      "s_add_u32 m0, s39, 81920\n\t"
      "s_nop 0\n\t"
      "global_load_lds_dwordx4 v144, s[40:41]\n\t"
      "global_load_lds_dwordx4 v145, s[40:41] offset:1024\n\t"
      "global_load_lds_dwordx4 v146, s[40:41] offset:2048\n\t"
      "global_load_lds_dwordx4 v147, s[40:41] offset:3072\n\t"
      "s_waitcnt vmcnt(8)\n\t"
      "s_barrier\n\t"
      "s_add_u32 s72, s76, 3670016\n\t"
      "s_add_u32 s40, s52, s72\n\t"
      "s_addc_u32 s41, s53, 0\n\t"
      "s_add_u32 m0, s39, 16384\n\t"
      "s_nop 0\n\t"
      "global_load_lds_dwordx4 v144, s[40:41]\n\t"
      "global_load_lds_dwordx4 v145, s[40:41] offset:1024\n\t"
      "global_load_lds_dwordx4 v146, s[40:41] offset:2048\n\t"
      "global_load_lds_dwordx4 v147, s[40:41] offset:3072\n\t"
      "s_barrier\n\t"
      "s_add_u32 s72, s76, 3899392\n\t"
      "s_add_u32 s40, s52, s72\n\t"
      "s_addc_u32 s41, s53, 0\n\t"
      "s_add_u32 m0, s39, 49152\n\t"
      "s_nop 0\n\t"
      "global_load_lds_dwordx4 v144, s[40:41]\n\t"
      "global_load_lds_dwordx4 v145, s[40:41] offset:1024\n\t"
      "global_load_lds_dwordx4 v146, s[40:41] offset:2048\n\t"
      "global_load_lds_dwordx4 v147, s[40:41] offset:3072\n\t"
      "s_waitcnt vmcnt(12)\n\t"
      "ds_read_b64_tr_b16 v[0:1], v172\n\t"
      "ds_read_b64_tr_b16 v[2:3], v172 offset:512\n\t"
      "ds_read_b64_tr_b16 v[4:5], v173\n\t"
      "ds_read_b64_tr_b16 v[6:7], v173 offset:512\n\t"
      "ds_read_b64_tr_b16 v[8:9], v174\n\t"
      "ds_read_b64_tr_b16 v[10:11], v174 offset:512\n\t"
      "ds_read_b64_tr_b16 v[12:13], v175\n\t"
      "ds_read_b64_tr_b16 v[14:15], v175 offset:512\n\t"
      "s_waitcnt lgkmcnt(0)\n\t"
      "s_add_u32 s72, s77, 0\n\t"
      "s_add_u32 s40, s52, s72\n\t"
      "s_addc_u32 s41, s53, 0\n\t"
      "s_add_u32 m0, s39, 65536\n\t"
      "s_nop 0\n\t"
      "global_load_lds_dwordx4 v144, s[40:41]\n\t"
      "global_load_lds_dwordx4 v145, s[40:41] offset:1024\n\t"
      "global_load_lds_dwordx4 v146, s[40:41] offset:2048\n\t"
      "global_load_lds_dwordx4 v147, s[40:41] offset:3072\n\t"
      "s_waitcnt vmcnt(12)\n\t"
      "ds_read_b64_tr_b16 v[16:17], v172 offset:16384\n\t"
      "ds_read_b64_tr_b16 v[18:19], v172 offset:16896\n\t"
      "ds_read_b64_tr_b16 v[20:21], v173 offset:16384\n\t"
      "ds_read_b64_tr_b16 v[22:23], v173 offset:16896\n\t"
      "ds_read_b64_tr_b16 v[24:25], v174 offset:16384\n\t"
      "ds_read_b64_tr_b16 v[26:27], v174 offset:16896\n\t"
      "ds_read_b64_tr_b16 v[28:29], v175 offset:16384\n\t"
      "ds_read_b64_tr_b16 v[30:31], v175 offset:16896\n\t"
      "s_waitcnt lgkmcnt(0)\n\t"
      "s_add_u32 s72, s77, 229376\n\t"
      "s_add_u32 s40, s52, s72\n\t"
      "s_addc_u32 s41, s53, 0\n\t"
      "s_add_u32 m0, s39, 81920\n\t"
      "s_nop 0\n\t"
      "global_load_lds_dwordx4 v144, s[40:41]\n\t"
      "global_load_lds_dwordx4 v145, s[40:41] offset:1024\n\t"
      "global_load_lds_dwordx4 v146, s[40:41] offset:2048\n\t"
      "global_load_lds_dwordx4 v147, s[40:41] offset:3072\n\t"
      "s_add_u32 s72, s77, 3670016\n\t"
      "s_add_u32 s40, s52, s72\n\t"
      "s_addc_u32 s41, s53, 0\n\t"
      "s_add_u32 m0, s39, 0\n\t"
      "s_nop 0\n\t"
      "global_load_lds_dwordx4 v144, s[40:41]\n\t"
      "global_load_lds_dwordx4 v145, s[40:41] offset:1024\n\t"
      "global_load_lds_dwordx4 v146, s[40:41] offset:2048\n\t"
      "global_load_lds_dwordx4 v147, s[40:41] offset:3072\n\t"
      "s_add_u32 s72, s77, 3899392\n\t"
      "s_add_u32 s40, s52, s72\n\t"
      "s_addc_u32 s41, s53, 0\n\t"
      "s_add_u32 m0, s39, 32768\n\t"
      "s_nop 0\n\t"
      "global_load_lds_dwordx4 v144, s[40:41]\n\t"
      "global_load_lds_dwordx4 v145, s[40:41] offset:1024\n\t"
      "global_load_lds_dwordx4 v146, s[40:41] offset:2048\n\t"
      "global_load_lds_dwordx4 v147, s[40:41] offset:3072\n\t"
      "s_waitcnt vmcnt(12)\n\t"
      "ds_read_b64_tr_b16 v[32:33], v172\n\t"
      "ds_read_b64_tr_b16 v[34:35], v172 offset:512\n\t"
      "ds_read_b64_tr_b16 v[36:37], v173\n\t"
      "ds_read_b64_tr_b16 v[38:39], v173 offset:512\n\t"
      "ds_read_b64_tr_b16 v[40:41], v174\n\t"
      "ds_read_b64_tr_b16 v[42:43], v174 offset:512\n\t"
      "ds_read_b64_tr_b16 v[44:45], v175\n\t"
      "ds_read_b64_tr_b16 v[46:47], v175 offset:512\n\t"
      "s_waitcnt lgkmcnt(0)\n\t"
      "s_add_u32 s72, s78, 0\n\t"
      "s_add_u32 s40, s52, s72\n\t"
      "s_addc_u32 s41, s53, 0\n\t"
      "s_add_u32 m0, s39, 65536\n\t"
      "s_nop 0\n\t"
      "global_load_lds_dwordx4 v144, s[40:41]\n\t"
      "global_load_lds_dwordx4 v145, s[40:41] offset:1024\n\t"
      "global_load_lds_dwordx4 v146, s[40:41] offset:2048\n\t"
      "global_load_lds_dwordx4 v147, s[40:41] offset:3072\n\t"
      "s_waitcnt vmcnt(12)\n\t"
      "ds_read_b64_tr_b16 v[48:49], v172 offset:16384\n\t"
      "ds_read_b64_tr_b16 v[50:51], v172 offset:16896\n\t"
      "ds_read_b64_tr_b16 v[52:53], v173 offset:16384\n\t"
      "ds_read_b64_tr_b16 v[54:55], v173 offset:16896\n\t"
      "ds_read_b64_tr_b16 v[56:57], v174 offset:16384\n\t"
      "ds_read_b64_tr_b16 v[58:59], v174 offset:16896\n\t"
      "ds_read_b64_tr_b16 v[60:61], v175 offset:16384\n\t"
      "ds_read_b64_tr_b16 v[62:63], v175 offset:16896\n\t"
      "s_waitcnt lgkmcnt(0)\n\t"
      "s_add_u32 s72, s78, 229376\n\t"
      "s_add_u32 s40, s52, s72\n\t"
      "s_addc_u32 s41, s53, 0\n\t"
      "s_add_u32 m0, s39, 81920\n\t"
      "s_nop 0\n\t"
      "global_load_lds_dwordx4 v144, s[40:41]\n\t"
      "global_load_lds_dwordx4 v145, s[40:41] offset:1024\n\t"
      "global_load_lds_dwordx4 v146, s[40:41] offset:2048\n\t"
      "global_load_lds_dwordx4 v147, s[40:41] offset:3072\n\t"
      "s_waitcnt vmcnt(8)\n\t"
      "s_barrier\n\t"
      "s_cmp_lg_u32 s24, 3\n\t"
      "s_cbranch_scc1 SK13_%=\n\t"
      "global_load_dwordx2 v[138:139], v137, s[60:61] sc1\n\t"
      "SK13_%=:\n\t"
      "s_cmp_lg_u32 s24, 3\n\t"
      "s_cbranch_scc1 LF2B_%=\n\t"
      "s_waitcnt vmcnt(0)\n\t"
      "v_cmp_ne_u64_e32 vcc, s[22:23], v[138:139]\n\t"
      "s_cbranch_vccnz LF2R_%=\n\t"
      "LF2B_%=:\n\t"
      "s_barrier\n\t"
      "s_add_u32 s72, s78, 3670016\n\t"
      "s_add_u32 s40, s52, s72\n\t"
      "s_addc_u32 s41, s53, 0\n\t"
      "s_add_u32 m0, s39, 16384\n\t"
      "s_nop 0\n\t"
      "global_load_lds_dwordx4 v144, s[40:41]\n\t"
      "global_load_lds_dwordx4 v145, s[40:41] offset:1024\n\t"
      "global_load_lds_dwordx4 v146, s[40:41] offset:2048\n\t"
      "global_load_lds_dwordx4 v147, s[40:41] offset:3072\n\t"
      "s_add_u32 s72, s78, 3899392\n\t"
      "s_add_u32 s40, s52, s72\n\t"
      "s_addc_u32 s41, s53, 0\n\t"
      "s_add_u32 m0, s39, 49152\n\t"
      "s_nop 0\n\t"
      "global_load_lds_dwordx4 v144, s[40:41]\n\t"
      "global_load_lds_dwordx4 v145, s[40:41] offset:1024\n\t"
      "global_load_lds_dwordx4 v146, s[40:41] offset:2048\n\t"
      "global_load_lds_dwordx4 v147, s[40:41] offset:3072\n\t"
      "s_add_u32 s72, s79, 3670016\n\t"
      "s_add_u32 s40, s52, s72\n\t"
      "s_addc_u32 s41, s53, 0\n\t"
      "s_add_u32 m0, s39, 0\n\t"
      "s_nop 0\n\t"
      "global_load_lds_dwordx4 v144, s[40:41]\n\t"
      "global_load_lds_dwordx4 v145, s[40:41] offset:1024\n\t"
      "global_load_lds_dwordx4 v146, s[40:41] offset:2048\n\t"
      "global_load_lds_dwordx4 v147, s[40:41] offset:3072\n\t"
      "s_add_u32 s72, s79, 3899392\n\t"
      "s_add_u32 s40, s52, s72\n\t"
      "s_addc_u32 s41, s53, 0\n\t"
      "s_add_u32 m0, s39, 32768\n\t"
      "s_nop 0\n\t"
      "global_load_lds_dwordx4 v144, s[40:41]\n\t"
      "global_load_lds_dwordx4 v145, s[40:41] offset:1024\n\t"
      "global_load_lds_dwordx4 v146, s[40:41] offset:2048\n\t"
      "global_load_lds_dwordx4 v147, s[40:41] offset:3072\n\t"
      "s_waitcnt vmcnt(20)\n\t"
      "ds_read_b64_tr_b16 v[64:65], v172\n\t"
      "ds_read_b64_tr_b16 v[66:67], v172 offset:512\n\t"
      "ds_read_b64_tr_b16 v[68:69], v173\n\t"
      "ds_read_b64_tr_b16 v[70:71], v173 offset:512\n\t"
      "ds_read_b64_tr_b16 v[72:73], v174\n\t"
      "ds_read_b64_tr_b16 v[74:75], v174 offset:512\n\t"
      "ds_read_b64_tr_b16 v[76:77], v175\n\t"
      "ds_read_b64_tr_b16 v[78:79], v175 offset:512\n\t"
      "s_waitcnt vmcnt(16)\n\t"
      "ds_read_b64_tr_b16 v[80:81], v172 offset:16384\n\t"
      "ds_read_b64_tr_b16 v[82:83], v172 offset:16896\n\t"
      "ds_read_b64_tr_b16 v[84:85], v173 offset:16384\n\t"
      "ds_read_b64_tr_b16 v[86:87], v173 offset:16896\n\t"
      "ds_read_b64_tr_b16 v[88:89], v174 offset:16384\n\t"
      "ds_read_b64_tr_b16 v[90:91], v174 offset:16896\n\t"
      "ds_read_b64_tr_b16 v[92:93], v175 offset:16384\n\t"
      "ds_read_b64_tr_b16 v[94:95], v175 offset:16896\n\t"
      "s_cmp_lg_u32 s24, 3\n\t"
      "s_cbranch_scc1 SK14_%=\n\t"
      "global_load_dwordx2 v[138:139], v143, s[60:61] sc1\n\t"
      "SK14_%=:\n\t"
      "s_waitcnt vmcnt(0)\n\t"
      "s_cmp_lg_u32 s24, 3\n\t"
      "s_cbranch_scc1 LF3B_%=\n\t"
      "s_waitcnt vmcnt(0)\n\t"
      "v_cmp_ne_u64_e32 vcc, s[22:23], v[138:139]\n\t"
      "s_cbranch_vccnz LF3R_%=\n\t"
      "LF3B_%=:\n\t"
      "s_barrier\n\t"
      "s_waitcnt lgkmcnt(0)\n\t"
      "s_add_u32 s72, s79, 0\n\t"
      "s_add_u32 s40, s52, s72\n\t"
      "s_addc_u32 s41, s53, 0\n\t"
      "s_add_u32 m0, s39, 65536\n\t"
      "s_nop 0\n\t"
      "global_load_lds_dwordx4 v144, s[40:41]\n\t"
      "global_load_lds_dwordx4 v145, s[40:41] offset:1024\n\t"
      "global_load_lds_dwordx4 v146, s[40:41] offset:2048\n\t"
      "global_load_lds_dwordx4 v147, s[40:41] offset:3072\n\t"
      "s_add_u32 s72, s79, 229376\n\t"
      "s_add_u32 s40, s52, s72\n\t"
      "s_addc_u32 s41, s53, 0\n\t"
      "s_add_u32 m0, s39, 81920\n\t"
      "s_nop 0\n\t"
      "global_load_lds_dwordx4 v144, s[40:41]\n\t"
      "global_load_lds_dwordx4 v145, s[40:41] offset:1024\n\t"
      "global_load_lds_dwordx4 v146, s[40:41] offset:2048\n\t"
      "global_load_lds_dwordx4 v147, s[40:41] offset:3072\n\t"
      "s_mul_i32 s34, s30, 65536\n\t"
      "s_mul_i32 s35, s24, 8192\n\t"
      "s_add_u32 s34, s34, s35\n\t"
      "s_add_u32 s64, s48, s34\n\t"
      "s_addc_u32 s65, s49, 0\n\t"
      "v_add_u32_e32 v250, 4096, v244\n\t"
      "global_load_dwordx4 v[128:131], v250, s[64:65] offset:-4096 sc1\n\t"
      "global_load_dwordx4 v[132:135], v250, s[64:65] offset:-3072 sc1\n\t"
      "global_load_dwordx4 v[136:139], v250, s[64:65] offset:-2048 sc1\n\t"
      "global_load_dwordx4 v[140:143], v250, s[64:65] offset:-1024 sc1\n\t"
      "global_load_dwordx4 v[144:147], v250, s[64:65] sc1\n\t"
      "global_load_dwordx4 v[148:151], v250, s[64:65] offset:1024 sc1\n\t"
      "global_load_dwordx4 v[152:155], v250, s[64:65] offset:2048 sc1\n\t"
      "global_load_dwordx4 v[156:159], v250, s[64:65] offset:3072 sc1\n\t"
      "s_waitcnt vmcnt(12)\n\t"
      "ds_read_b64_tr_b16 v[96:97], v172\n\t"
      "ds_read_b64_tr_b16 v[98:99], v172 offset:512\n\t"
      "ds_read_b64_tr_b16 v[100:101], v173\n\t"
      "ds_read_b64_tr_b16 v[102:103], v173 offset:512\n\t"
      "ds_read_b64_tr_b16 v[104:105], v174\n\t"
      "ds_read_b64_tr_b16 v[106:107], v174 offset:512\n\t"
      "ds_read_b64_tr_b16 v[108:109], v175\n\t"
      "ds_read_b64_tr_b16 v[110:111], v175 offset:512\n\t"
      "s_waitcnt vmcnt(8)\n\t"
      "ds_read_b64_tr_b16 v[112:113], v172 offset:16384\n\t"
      "ds_read_b64_tr_b16 v[114:115], v172 offset:16896\n\t"
      "ds_read_b64_tr_b16 v[116:117], v173 offset:16384\n\t"
      "ds_read_b64_tr_b16 v[118:119], v173 offset:16896\n\t"
      "ds_read_b64_tr_b16 v[120:121], v174 offset:16384\n\t"
      "ds_read_b64_tr_b16 v[122:123], v174 offset:16896\n\t"
      "ds_read_b64_tr_b16 v[124:125], v175 offset:16384\n\t"
      "ds_read_b64_tr_b16 v[126:127], v175 offset:16896\n\t"
      "s_waitcnt lgkmcnt(0)\n\t"
      "s_branch GST_%=\n\t"
      "G4_%=:\n\t"
      "s_mul_i32 s34, s30, 57344\n\t"
      "s_mul_i32 s35, s24, 7168\n\t"
      "s_add_u32 s34, s34, s35\n\t"
      "s_add_u32 s34, s34, 1835008\n\t"
      "s_add_u32 s64, s48, s34\n\t"
      "s_addc_u32 s65, s49, 0\n\t"
      "s_and_b32 s34, s30, 3\n\t"
      "s_lshl_b32 s34, s34, 11\n\t"
      "s_add_u32 s40, s34, 0\n\t"
      "s_sub_u32 s35, s40, 7168\n\t"
      "s_min_u32 s40, s40, s35\n\t"
      "s_add_u32 s41, s34, 1024\n\t"
      "s_sub_u32 s35, s41, 7168\n\t"
      "s_min_u32 s41, s41, s35\n\t"
      "s_add_u32 s42, s34, 2048\n\t"
      "s_sub_u32 s35, s42, 7168\n\t"
      "s_min_u32 s42, s42, s35\n\t"
      "s_add_u32 s43, s34, 3072\n\t"
      "s_sub_u32 s35, s43, 7168\n\t"
      "s_min_u32 s43, s43, s35\n\t"
      "s_add_u32 s44, s34, 4096\n\t"
      "s_sub_u32 s35, s44, 7168\n\t"
      "s_min_u32 s44, s44, s35\n\t"
      "s_add_u32 s45, s34, 5120\n\t"
      "s_sub_u32 s35, s45, 7168\n\t"
      "s_min_u32 s45, s45, s35\n\t"
      "s_add_u32 s46, s34, 6144\n\t"
      "s_sub_u32 s35, s46, 7168\n\t"
      "s_min_u32 s46, s46, s35\n\t"
      "s_add_u32 s66, s64, 57344\n\t"
      "s_addc_u32 s67, s65, 0\n\t"
      "s_add_u32 s68, s66, 57344\n\t"
      "s_addc_u32 s69, s67, 0\n\t"
      "s_add_u32 s70, s68, 57344\n\t"
      "s_addc_u32 s71, s69, 0\n\t"
      "s_add_u32 s72, s70, 57344\n\t"
      "s_addc_u32 s73, s71, 0\n\t"
      "s_add_u32 s74, s72, 57344\n\t"
      "s_addc_u32 s75, s73, 0\n\t"
      "s_add_u32 s76, s74, 57344\n\t"
      "s_addc_u32 s77, s75, 0\n\t"
      "v_lshrrev_b32_e32 v240, 7, v244\n\t"
      "v_and_b32_e32 v241, 0x70, v244\n\t"
      "v_and_b32_e32 v242, 6, v240\n\t"
      "v_lshlrev_b32_e32 v242, 4, v242\n\t"
      "v_xor_b32_e32 v242, v242, v241\n\t"
      "v_lshl_or_b32 v228, v240, 12, v242\n\t"
      "s_mul_i32 s34, s24, 917504\n\t"
      "s_lshl_b32 s35, s29, 7\n\t"
      "s_add_u32 s34, s34, s35\n\t"
      "s_lshl_b32 s39, s24, 12\n\t"
      "s_add_u32 s54, s14, s34\n\t"
      "s_addc_u32 s55, s15, 0\n\t"
      "v_add_u32_e32 v229, 31744, v228\n\t"
      "v_add_u32_e32 v230, 63488, v228\n\t"
      "v_add_u32_e32 v231, 95232, v228\n\t"
      "s_lshl_b32 s37, s40, 7\n\t"
      "s_add_u32 s58, s54, s37\n\t"
      "s_addc_u32 s59, s55, 0\n\t"
      "s_add_u32 m0, s39, 0\n\t"
      "s_nop 0\n\t"
      "global_load_lds_dwordx4 v228, s[58:59]\n\t"
      "global_load_lds_dwordx4 v229, s[58:59] offset:1024\n\t"
      "global_load_lds_dwordx4 v230, s[58:59] offset:2048\n\t"
      "global_load_lds_dwordx4 v231, s[58:59] offset:3072\n\t"
      "s_lshl_b32 s37, s41, 7\n\t"
      "s_add_u32 s58, s54, s37\n\t"
      "s_addc_u32 s59, s55, 0\n\t"
      "s_add_u32 m0, s39, 32768\n\t"
      "s_nop 0\n\t"
      "global_load_lds_dwordx4 v228, s[58:59]\n\t"
      "global_load_lds_dwordx4 v229, s[58:59] offset:1024\n\t"
      "global_load_lds_dwordx4 v230, s[58:59] offset:2048\n\t"
      "global_load_lds_dwordx4 v231, s[58:59] offset:3072\n\t"
      "s_lshl_b32 s37, s42, 7\n\t"
      "s_add_u32 s58, s54, s37\n\t"
      "s_addc_u32 s59, s55, 0\n\t"
      "s_add_u32 m0, s39, 65536\n\t"
      "s_nop 0\n\t"
      "global_load_lds_dwordx4 v228, s[58:59]\n\t"
      "global_load_lds_dwordx4 v229, s[58:59] offset:1024\n\t"
      "global_load_lds_dwordx4 v230, s[58:59] offset:2048\n\t"
      "global_load_lds_dwordx4 v231, s[58:59] offset:3072\n\t"
      "s_lshl_b32 s37, s43, 7\n\t"
      "s_add_u32 s58, s54, s37\n\t"
      "s_addc_u32 s59, s55, 0\n\t"
      "s_add_u32 m0, s39, 98304\n\t"
      "s_nop 0\n\t"
      "global_load_lds_dwordx4 v228, s[58:59]\n\t"
      "global_load_lds_dwordx4 v229, s[58:59] offset:1024\n\t"
      "global_load_lds_dwordx4 v230, s[58:59] offset:2048\n\t"
      "global_load_lds_dwordx4 v231, s[58:59] offset:3072\n\t"
      "s_lshr_b32 s34, s24, 1\n\t"
      "s_lshl2_add_u32 s35, s29, s34\n\t"
      "s_and_b32 s36, s24, 1\n\t"
      "s_lshl4_add_u32 s35, s35, s36\n\t"
      "v_lshl_add_u32 v242, v240, 1, s35\n\t"
      "v_lshlrev_b32_e32 v248, 1, v242\n\t"
      "global_load_ushort v249, v248, s[16:17]\n\t"
      "s_movk_i32 s35, 896\n\t"
      "v_and_b32_e32 v247, 64, v244\n\t"
      "v_lshrrev_b32_e32 v247, 2, v247\n\t"
      "v_mad_u32_u24 v247, v242, s35, v247\n\t"
      "v_and_b32_e32 v242, 0x30, v244\n\t"
      "v_lshl_add_u32 v247, v242, 1, v247\n\t"
      "s_mov_b32 s82, 0x77777777\n\t"
      "s_mov_b32 s83, 0x77777777\n\t"
      "s_lshl_b32 s35, s30, 5\n\t"
      "s_add_u32 s62, s18, s35\n\t"
      "s_addc_u32 s63, s19, 0\n\t"
      "s_mul_i32 s35, s29, 28\n\t"
      "s_add_u32 s35, s35, s30\n\t"
      "s_lshl1_add_u32 s35, s35, s36\n\t"
      "s_lshl2_add_u32 s35, s35, s34\n\t"
      "s_lshl_b32 s35, s35, 8\n\t"
      "s_add_u32 s35, s35, 6291456\n\t"
      "s_add_u32 s50, s20, s35\n\t"
      "s_addc_u32 s51, s21, 0\n\t"
      "v_lshrrev_b32_e32 v248, 2, v244\n\t"
      "v_bfe_u32 v242, v244, 8, 1\n\t"
      "v_lshlrev_b32_e32 v242, 4, v242\n\t"
      "v_xor_b32_e32 v245, v244, v242\n\t"
      "v_bfe_u32 v242, v244, 9, 1\n\t"
      "v_lshlrev_b32_e32 v242, 7, v242\n\t"
      "v_xor_b32_e32 v245, v245, v242\n\t"
      "s_add_u32 s35, s39, 98304\n\t"
      "v_add_u32_e32 v245, s35, v245\n\t"
      "v_lshl_or_b32 v246, v240, 1, s36\n\t"
      "v_bfe_u32 v242, v244, 5, 1\n\t"
      "v_xor_b32_e32 v246, v246, v242\n\t"
      "v_bfe_u32 v242, v244, 6, 1\n\t"
      "v_lshlrev_b32_e32 v242, 3, v242\n\t"
      "v_xor_b32_e32 v246, v246, v242\n\t"
      "v_lshlrev_b32_e32 v246, 4, v246\n\t"
      "v_and_b32_e32 v242, 0x60, v244\n\t"
      "v_lshl_or_b32 v246, v242, 3, v246\n\t"
      "v_bfe_u32 v242, v244, 4, 1\n\t"
      "v_lshl_or_b32 v246, v242, 3, v246\n\t"
      "s_lshl_b32 s34, s34, 10\n\t"
      "s_add_u32 s34, s34, 98304\n\t"
      "v_add_u32_e32 v246, s34, v246\n\t"
      "v_and_b32_e32 v240, 0x3c0, v244\n\t"
      "v_lshlrev_b32_e32 v240, 1, v240\n\t"
      "v_and_b32_e32 v241, 0x180, v244\n\t"
      "v_lshrrev_b32_e32 v241, 2, v241\n\t"
      "v_and_b32_e32 v242, 0x30, v244\n\t"
      "v_lshrrev_b32_e32 v242, 1, v242\n\t"
      "v_or3_b32 v232, v240, v241, v242\n\t"
      "v_add_u32_e32 v232, s39, v232\n\t"
      "v_min_u32_e32 v252, 432, v244\n\t"
      "v_lshlrev_b32_e32 v252, 4, v252\n\t"
      "v_xor_b32_e32 v233, 32, v232\n\t"
      "v_xor_b32_e32 v234, 64, v232\n\t"
      "v_xor_b32_e32 v235, 96, v232\n\t"
      "v_xor_b32_e32 v236, 65536, v232\n\t"
      "v_xor_b32_e32 v237, 65568, v232\n\t"
      "v_xor_b32_e32 v238, 65600, v232\n\t"
      "v_xor_b32_e32 v239, 65632, v232\n\t"
      "s_waitcnt vmcnt(13)\n\t"
      "ds_read_b64_tr_b16 v[0:1], v232\n\t"
      "ds_read_b64_tr_b16 v[2:3], v232 offset:2048\n\t"
      "ds_read_b64_tr_b16 v[4:5], v233\n\t"
      "ds_read_b64_tr_b16 v[6:7], v233 offset:2048\n\t"
      "ds_read_b64_tr_b16 v[8:9], v234\n\t"
      "ds_read_b64_tr_b16 v[10:11], v234 offset:2048\n\t"
      "ds_read_b64_tr_b16 v[12:13], v235\n\t"
      "ds_read_b64_tr_b16 v[14:15], v235 offset:2048\n\t"
      "s_waitcnt lgkmcnt(0)\n\t"
      "s_lshl_b32 s37, s44, 7\n\t"
      "s_add_u32 s58, s54, s37\n\t"
      "s_addc_u32 s59, s55, 0\n\t"
      "s_add_u32 m0, s39, 0\n\t"
      "s_nop 0\n\t"
      "global_load_lds_dwordx4 v228, s[58:59]\n\t"
      "global_load_lds_dwordx4 v229, s[58:59] offset:1024\n\t"
      "global_load_lds_dwordx4 v230, s[58:59] offset:2048\n\t"
      "global_load_lds_dwordx4 v231, s[58:59] offset:3072\n\t"
      "s_waitcnt vmcnt(13)\n\t"
      "ds_read_b64_tr_b16 v[16:17], v232 offset:32768\n\t"
      "ds_read_b64_tr_b16 v[18:19], v232 offset:34816\n\t"
      "ds_read_b64_tr_b16 v[20:21], v233 offset:32768\n\t"
      "ds_read_b64_tr_b16 v[22:23], v233 offset:34816\n\t"
      "ds_read_b64_tr_b16 v[24:25], v234 offset:32768\n\t"
      "ds_read_b64_tr_b16 v[26:27], v234 offset:34816\n\t"
      "ds_read_b64_tr_b16 v[28:29], v235 offset:32768\n\t"
      "ds_read_b64_tr_b16 v[30:31], v235 offset:34816\n\t"
      "s_waitcnt lgkmcnt(0)\n\t"
      "s_lshl_b32 s37, s45, 7\n\t"
      "s_add_u32 s58, s54, s37\n\t"
      "s_addc_u32 s59, s55, 0\n\t"
      "s_add_u32 m0, s39, 32768\n\t"
      "s_nop 0\n\t"
      "global_load_lds_dwordx4 v228, s[58:59]\n\t"
      "global_load_lds_dwordx4 v229, s[58:59] offset:1024\n\t"
      "global_load_lds_dwordx4 v230, s[58:59] offset:2048\n\t"
      "global_load_lds_dwordx4 v231, s[58:59] offset:3072\n\t"
      "s_waitcnt vmcnt(13)\n\t"
      "ds_read_b64_tr_b16 v[32:33], v236\n\t"
      "ds_read_b64_tr_b16 v[34:35], v236 offset:2048\n\t"
      "ds_read_b64_tr_b16 v[36:37], v237\n\t"
      "ds_read_b64_tr_b16 v[38:39], v237 offset:2048\n\t"
      "ds_read_b64_tr_b16 v[40:41], v238\n\t"
      "ds_read_b64_tr_b16 v[42:43], v238 offset:2048\n\t"
      "ds_read_b64_tr_b16 v[44:45], v239\n\t"
      "ds_read_b64_tr_b16 v[46:47], v239 offset:2048\n\t"
      "s_waitcnt lgkmcnt(0)\n\t"
      "s_lshl_b32 s37, s46, 7\n\t"
      "s_add_u32 s58, s54, s37\n\t"
      "s_addc_u32 s59, s55, 0\n\t"
      "s_add_u32 m0, s39, 65536\n\t"
      "s_nop 0\n\t"
      "global_load_lds_dwordx4 v228, s[58:59]\n\t"
      "global_load_lds_dwordx4 v229, s[58:59] offset:1024\n\t"
      "global_load_lds_dwordx4 v230, s[58:59] offset:2048\n\t"
      "global_load_lds_dwordx4 v231, s[58:59] offset:3072\n\t"
      "s_waitcnt vmcnt(13)\n\t"
      "ds_read_b64_tr_b16 v[48:49], v236 offset:32768\n\t"
      "ds_read_b64_tr_b16 v[50:51], v236 offset:34816\n\t"
      "ds_read_b64_tr_b16 v[52:53], v237 offset:32768\n\t"
      "ds_read_b64_tr_b16 v[54:55], v237 offset:34816\n\t"
      "ds_read_b64_tr_b16 v[56:57], v238 offset:32768\n\t"
      "ds_read_b64_tr_b16 v[58:59], v238 offset:34816\n\t"
      "ds_read_b64_tr_b16 v[60:61], v239 offset:32768\n\t"
      "ds_read_b64_tr_b16 v[62:63], v239 offset:34816\n\t"
      "s_waitcnt vmcnt(8)\n\t"
      "ds_read_b64_tr_b16 v[64:65], v232\n\t"
      "ds_read_b64_tr_b16 v[66:67], v232 offset:2048\n\t"
      "ds_read_b64_tr_b16 v[68:69], v233\n\t"
      "ds_read_b64_tr_b16 v[70:71], v233 offset:2048\n\t"
      "ds_read_b64_tr_b16 v[72:73], v234\n\t"
      "ds_read_b64_tr_b16 v[74:75], v234 offset:2048\n\t"
      "ds_read_b64_tr_b16 v[76:77], v235\n\t"
      "ds_read_b64_tr_b16 v[78:79], v235 offset:2048\n\t"
      "s_waitcnt vmcnt(4)\n\t"
      "ds_read_b64_tr_b16 v[80:81], v232 offset:32768\n\t"
      "ds_read_b64_tr_b16 v[82:83], v232 offset:34816\n\t"
      "ds_read_b64_tr_b16 v[84:85], v233 offset:32768\n\t"
      "ds_read_b64_tr_b16 v[86:87], v233 offset:34816\n\t"
      "ds_read_b64_tr_b16 v[88:89], v234 offset:32768\n\t"
      "ds_read_b64_tr_b16 v[90:91], v234 offset:34816\n\t"
      "ds_read_b64_tr_b16 v[92:93], v235 offset:32768\n\t"
      "ds_read_b64_tr_b16 v[94:95], v235 offset:34816\n\t"
      "s_waitcnt vmcnt(0)\n\t"
      "ds_read_b64_tr_b16 v[96:97], v236\n\t"
      "ds_read_b64_tr_b16 v[98:99], v236 offset:2048\n\t"
      "ds_read_b64_tr_b16 v[100:101], v237\n\t"
      "ds_read_b64_tr_b16 v[102:103], v237 offset:2048\n\t"
      "ds_read_b64_tr_b16 v[104:105], v238\n\t"
      "ds_read_b64_tr_b16 v[106:107], v238 offset:2048\n\t"
      "ds_read_b64_tr_b16 v[108:109], v239\n\t"
      "ds_read_b64_tr_b16 v[110:111], v239 offset:2048\n\t"
      "v_lshlrev_b32_e32 v249, 16, v249\n\t"
      "s_waitcnt lgkmcnt(0)\n\t"
      "v_min_u32_e32 v240, 48, v244\n\t"
      "v_mul_u32_u24_e32 v240, 7, v240\n\t"
      "s_lshl_b32 s34, s29, 4\n\t"
      "v_add_u32_e32 v240, s34, v240\n\t"
      "v_subrev_u32_e32 v241, 448, v240\n\t"
      "v_min_u32_e32 v240, v240, v241\n\t"
      "v_subrev_u32_e32 v241, 448, v240\n\t"
      "v_min_u32_e32 v240, v240, v241\n\t"
      "s_and_b32 s34, s29, 7\n\t"
      "s_lshl_b32 s34, s34, 8\n\t"
      "v_lshl_add_u32 v253, v240, 7, s34\n\t"
      "s_cmp_lg_u32 s24, 3\n\t"
      "s_cbranch_scc1 SK16_%=\n\t"
      "global_load_dwordx2 v[138:139], v143, s[60:61] sc1\n\t"
      "SK16_%=:\n\t"
      "s_cmp_lg_u32 s24, 3\n\t"
      "s_cbranch_scc1 LF5B_%=\n\t"
      "s_waitcnt vmcnt(0)\n\t"
      "v_cmp_ne_u64_e32 vcc, s[22:23], v[138:139]\n\t"
      "s_cbranch_vccnz LF5R_%=\n\t"
      "LF5B_%=:\n\t"
      "s_barrier\n\t"
      "s_cmp_eq_u32 s24, 0\n\t"
      "s_cbranch_scc1 PG0R_%=\n\t"
      "PG0B_%=:\n\t"
      "s_barrier\n\t"
      "s_add_u32 s58, s64, s40\n\t"
      "s_addc_u32 s59, s65, 0\n\t"
      "global_load_dwordx4 v[128:131], v244, s[58:59] sc1\n\t"
      "s_add_u32 s58, s64, s41\n\t"
      "s_addc_u32 s59, s65, 0\n\t"
      "global_load_dwordx4 v[132:135], v244, s[58:59] sc1\n\t"
      "s_add_u32 s58, s64, s42\n\t"
      "s_addc_u32 s59, s65, 0\n\t"
      "global_load_dwordx4 v[136:139], v244, s[58:59] sc1\n\t"
      "s_add_u32 s58, s64, s43\n\t"
      "s_addc_u32 s59, s65, 0\n\t"
      "global_load_dwordx4 v[140:143], v244, s[58:59] sc1\n\t"
      "s_add_u32 s58, s64, s44\n\t"
      "s_addc_u32 s59, s65, 0\n\t"
      "global_load_dwordx4 v[144:147], v244, s[58:59] sc1\n\t"
      "s_add_u32 s58, s64, s45\n\t"
      "s_addc_u32 s59, s65, 0\n\t"
      "global_load_dwordx4 v[148:151], v244, s[58:59] sc1\n\t"
      "s_add_u32 s58, s64, s46\n\t"
      "s_addc_u32 s59, s65, 0\n\t"
      "global_load_dwordx4 v[152:155], v244, s[58:59] sc1\n\t"
      "s_cmp_eq_u32 s24, 0\n\t"
      "s_cbranch_scc1 PG1R_%=\n\t"
      "PG1B_%=:\n\t"
      "s_barrier\n\t"
      "s_waitcnt vmcnt(3)\n\t"
      "v_mfma_f32_16x16x32_bf16 v[208:211], v[128:131], v[0:3], 0\n\t"
      "s_add_u32 s58, s66, s40\n\t"
      "s_addc_u32 s59, s67, 0\n\t"
      "global_load_dwordx4 v[156:159], v244, s[58:59] sc1\n\t"
      "v_mfma_f32_16x16x32_bf16 v[208:211], v[132:135], v[16:19], v[208:211]\n\t"
      "v_mfma_f32_16x16x32_bf16 v[208:211], v[136:139], v[32:35], v[208:211]\n\t"
      "s_add_u32 s58, s66, s41\n\t"
      "s_addc_u32 s59, s67, 0\n\t"
      "global_load_dwordx4 v[160:163], v244, s[58:59] sc1\n\t"
      "v_mfma_f32_16x16x32_bf16 v[208:211], v[140:143], v[48:51], v[208:211]\n\t"
      "s_waitcnt vmcnt(2)\n\t"
      "v_mfma_f32_16x16x32_bf16 v[208:211], v[144:147], v[64:67], v[208:211]\n\t"
      "s_add_u32 s58, s66, s42\n\t"
      "s_addc_u32 s59, s67, 0\n\t"
      "global_load_dwordx4 v[164:167], v244, s[58:59] sc1\n\t"
      "v_mfma_f32_16x16x32_bf16 v[208:211], v[148:151], v[80:83], v[208:211]\n\t"
      "v_mfma_f32_16x16x32_bf16 v[208:211], v[152:155], v[96:99], v[208:211]\n\t"
      "s_add_u32 s58, s66, s43\n\t"
      "s_addc_u32 s59, s67, 0\n\t"
      "global_load_dwordx4 v[168:171], v244, s[58:59] sc1\n\t"
      "v_mfma_f32_16x16x32_bf16 v[212:215], v[128:131], v[4:7], 0\n\t"
      "v_mfma_f32_16x16x32_bf16 v[212:215], v[132:135], v[20:23], v[212:215]\n\t"
      "s_add_u32 s58, s66, s44\n\t"
      "s_addc_u32 s59, s67, 0\n\t"
      "global_load_dwordx4 v[172:175], v244, s[58:59] sc1\n\t"
      "v_mfma_f32_16x16x32_bf16 v[212:215], v[136:139], v[36:39], v[212:215]\n\t"
      "v_add_f32_e32 v251, v208, v209\n\t"
      "v_add_f32_e32 v251, v251, v210\n\t"
      "v_cmp_u_f32_e32 vcc, v251, v211\n\t"
      "s_cbranch_vccnz LA40R_%=\n\t"
      "LA40B_%=:\n\t"
      "v_mfma_f32_16x16x32_bf16 v[212:215], v[140:143], v[52:55], v[212:215]\n\t"
      "s_add_u32 s58, s66, s45\n\t"
      "s_addc_u32 s59, s67, 0\n\t"
      "global_load_dwordx4 v[176:179], v244, s[58:59] sc1\n\t"
      "v_mfma_f32_16x16x32_bf16 v[212:215], v[144:147], v[68:71], v[212:215]\n\t"
      "v_mfma_f32_16x16x32_bf16 v[212:215], v[148:151], v[84:87], v[212:215]\n\t"
      "ds_write_b128 v245, v[208:211]\n\t"
      "global_load_dword v112, v248, s[50:51] sc1\n\t"
      "s_add_u32 s50, s50, 2048\n\t"
      "s_addc_u32 s51, s51, 0\n\t"
      "s_add_u32 s58, s66, s46\n\t"
      "s_addc_u32 s59, s67, 0\n\t"
      "global_load_dwordx4 v[180:183], v244, s[58:59] sc1\n\t"
      "v_mfma_f32_16x16x32_bf16 v[212:215], v[152:155], v[100:103], v[212:215]\n\t"
      "v_mfma_f32_16x16x32_bf16 v[216:219], v[128:131], v[8:11], 0\n\t"
      "v_mfma_f32_16x16x32_bf16 v[216:219], v[132:135], v[24:27], v[216:219]\n\t"
      "v_mfma_f32_16x16x32_bf16 v[216:219], v[136:139], v[40:43], v[216:219]\n\t"
      "v_mfma_f32_16x16x32_bf16 v[216:219], v[140:143], v[56:59], v[216:219]\n\t"
      "s_add_u32 s58, s68, s40\n\t"
      "s_addc_u32 s59, s69, 0\n\t"
      "global_load_dwordx4 v[184:187], v244, s[58:59] sc1\n\t"
      "v_mfma_f32_16x16x32_bf16 v[216:219], v[144:147], v[72:75], v[216:219]\n\t"
      "v_mfma_f32_16x16x32_bf16 v[216:219], v[148:151], v[88:91], v[216:219]\n\t"
      "ds_write_b128 v245, v[212:215] offset:1024\n\t"
      "v_mfma_f32_16x16x32_bf16 v[216:219], v[152:155], v[104:107], v[216:219]\n\t"
      "v_mfma_f32_16x16x32_bf16 v[220:223], v[128:131], v[12:15], 0\n\t"
      "s_add_u32 s58, s68, s41\n\t"
      "s_addc_u32 s59, s69, 0\n\t"
      "global_load_dwordx4 v[188:191], v244, s[58:59] sc1\n\t"
      "v_mfma_f32_16x16x32_bf16 v[220:223], v[132:135], v[28:31], v[220:223]\n\t"
      "v_mfma_f32_16x16x32_bf16 v[220:223], v[136:139], v[44:47], v[220:223]\n\t"
      "v_mfma_f32_16x16x32_bf16 v[220:223], v[140:143], v[60:63], v[220:223]\n\t"
      "v_mfma_f32_16x16x32_bf16 v[220:223], v[144:147], v[76:79], v[220:223]\n\t"
      "s_add_u32 s58, s68, s42\n\t"
      "s_addc_u32 s59, s69, 0\n\t"
      "global_load_dwordx4 v[192:195], v244, s[58:59] sc1\n\t"
      "v_mfma_f32_16x16x32_bf16 v[220:223], v[148:151], v[92:95], v[220:223]\n\t"
      "ds_write_b128 v245, v[216:219] offset:2048\n\t"
      "v_mfma_f32_16x16x32_bf16 v[220:223], v[152:155], v[108:111], v[220:223]\n\t"
      "s_waitcnt vmcnt(7)\n\t"
      "v_mfma_f32_16x16x32_bf16 v[224:227], v[156:159], v[0:3], 0\n\t"
      "v_mfma_f32_16x16x32_bf16 v[224:227], v[160:163], v[16:19], v[224:227]\n\t"
      "s_add_u32 s58, s68, s43\n\t"
      "s_addc_u32 s59, s69, 0\n\t"
      "global_load_dwordx4 v[196:199], v244, s[58:59] sc1\n\t"
      "s_nop 1\n\t"
      "ds_write_b128 v245, v[220:223] offset:3072\n\t"
      "v_mfma_f32_16x16x32_bf16 v[224:227], v[164:167], v[32:35], v[224:227]\n\t"
      "v_mfma_f32_16x16x32_bf16 v[224:227], v[168:171], v[48:51], v[224:227]\n\t"
      "s_waitcnt vmcnt(4)\n\t"
      "v_mfma_f32_16x16x32_bf16 v[224:227], v[172:175], v[64:67], v[224:227]\n\t"
      "v_mfma_f32_16x16x32_bf16 v[224:227], v[176:179], v[80:83], v[224:227]\n\t"
      "s_add_u32 s58, s68, s44\n\t"
      "s_addc_u32 s59, s69, 0\n\t"
      "global_load_dwordx4 v[200:203], v244, s[58:59] sc1\n\t"
      "s_waitcnt lgkmcnt(0)\n\t"
      "s_barrier\n\t"
      "ds_read_b64 v[228:229], v246\n\t"
      "ds_read_b64 v[230:231], v246 offset:4096\n\t"
      "v_mfma_f32_16x16x32_bf16 v[224:227], v[180:183], v[96:99], v[224:227]\n\t"
      "ds_read_b64 v[232:233], v246 offset:8192\n\t"
      "ds_read_b64 v[234:235], v246 offset:12288\n\t"
      "v_mfma_f32_16x16x32_bf16 v[208:211], v[156:159], v[4:7], 0\n\t"
      "ds_read_b64 v[236:237], v246 offset:16384\n\t"
      "ds_read_b64 v[238:239], v246 offset:20480\n\t"
      "v_mfma_f32_16x16x32_bf16 v[208:211], v[160:163], v[20:23], v[208:211]\n\t"
      "ds_read_b64 v[240:241], v246 offset:24576\n\t"
      "ds_read_b64 v[242:243], v246 offset:28672\n\t"
      "v_lshlrev_b32_e32 v113, 16, v112\n\t"
      "v_mfma_f32_16x16x32_bf16 v[208:211], v[164:167], v[36:39], v[208:211]\n\t"
      "s_add_u32 s58, s68, s45\n\t"
      "s_addc_u32 s59, s69, 0\n\t"
      "global_load_dwordx4 v[204:207], v244, s[58:59] sc1\n\t"
      "v_and_b32_e32 v114, 0xffff0000, v112\n\t"
      "v_add_f32_e32 v251, v224, v225\n\t"
      "v_add_f32_e32 v251, v251, v226\n\t"
      "v_cmp_u_f32_e32 vcc, v251, v227\n\t"
      "s_cbranch_vccnz LA41R_%=\n\t"
      "LA41B_%=:\n\t"
      "v_mfma_f32_16x16x32_bf16 v[208:211], v[168:171], v[52:55], v[208:211]\n\t"
      "v_add_f32_e32 v113, v249, v113\n\t"
      "v_add_f32_e32 v114, v249, v114\n\t"
      "v_mfma_f32_16x16x32_bf16 v[208:211], v[172:175], v[68:71], v[208:211]\n\t"
      "s_waitcnt lgkmcnt(4)\n\t"
      "v_mfma_f32_16x16x32_bf16 v[208:211], v[176:179], v[84:87], v[208:211]\n\t"
      "v_add_f32_e32 v228, v228, v230\n\t"
      "ds_write_b128 v245, v[224:227] offset:32768\n\t"
      "global_load_dword v112, v248, s[50:51] sc1\n\t"
      "s_add_u32 s50, s50, 2048\n\t"
      "s_addc_u32 s51, s51, 0\n\t"
      "v_mfma_f32_16x16x32_bf16 v[208:211], v[180:183], v[100:103], v[208:211]\n\t"
      "s_add_u32 s58, s68, s46\n\t"
      "s_addc_u32 s59, s69, 0\n\t"
      "global_load_dwordx4 v[128:131], v244, s[58:59] sc1\n\t"
      "v_add_f32_e32 v232, v232, v234\n\t"
      "v_add_f32_e32 v229, v229, v231\n\t"
      "v_mfma_f32_16x16x32_bf16 v[212:215], v[156:159], v[8:11], 0\n\t"
      "v_add_f32_e32 v233, v233, v235\n\t"
      "v_mfma_f32_16x16x32_bf16 v[212:215], v[160:163], v[24:27], v[212:215]\n\t"
      "s_waitcnt lgkmcnt(1)\n\t"
      "v_add_f32_e32 v236, v236, v238\n\t"
      "v_mfma_f32_16x16x32_bf16 v[212:215], v[164:167], v[40:43], v[212:215]\n\t"
      "v_add_f32_e32 v240, v240, v242\n\t"
      "v_mfma_f32_16x16x32_bf16 v[212:215], v[168:171], v[56:59], v[212:215]\n\t"
      "v_add_f32_e32 v237, v237, v239\n\t"
      "s_add_u32 s58, s70, s40\n\t"
      "s_addc_u32 s59, s71, 0\n\t"
      "global_load_dwordx4 v[132:135], v244, s[58:59] sc1\n\t"
      "v_mfma_f32_16x16x32_bf16 v[212:215], v[172:175], v[72:75], v[212:215]\n\t"
      "v_add_f32_e32 v241, v241, v243\n\t"
      "v_add_f32_e32 v228, v228, v232\n\t"
      "v_mfma_f32_16x16x32_bf16 v[212:215], v[176:179], v[88:91], v[212:215]\n\t"
      "v_add_f32_e32 v236, v236, v240\n\t"
      "ds_write_b128 v245, v[208:211] offset:33792\n\t"
      "v_mfma_f32_16x16x32_bf16 v[212:215], v[180:183], v[104:107], v[212:215]\n\t"
      "v_add_f32_e32 v229, v229, v233\n\t"
      "v_add_f32_e32 v237, v237, v241\n\t"
      "v_mfma_f32_16x16x32_bf16 v[216:219], v[156:159], v[12:15], 0\n\t"
      "v_add_f32_e32 v228, v228, v113\n\t"
      "s_add_u32 s58, s70, s41\n\t"
      "s_addc_u32 s59, s71, 0\n\t"
      "global_load_dwordx4 v[136:139], v244, s[58:59] sc1\n\t"
      "v_mfma_f32_16x16x32_bf16 v[216:219], v[160:163], v[28:31], v[216:219]\n\t"
      "v_add_f32_e32 v229, v229, v114\n\t"
      "v_mfma_f32_16x16x32_bf16 v[216:219], v[164:167], v[44:47], v[216:219]\n\t"
      "v_add_f32_e32 v228, v228, v236\n\t"
      "v_add_f32_e32 v229, v229, v237\n\t"
      "v_mfma_f32_16x16x32_bf16 v[216:219], v[168:171], v[60:63], v[216:219]\n\t"
      "v_cvt_pk_bf16_f32 v232, v228, v229\n\t"
      "v_mfma_f32_16x16x32_bf16 v[216:219], v[172:175], v[76:79], v[216:219]\n\t"
      "s_mov_b32 vcc_lo, 0xeeeeeeee\n\t"
      "s_mov_b32 vcc_hi, 0xeeeeeeee\n\t"
      "v_cndmask_b32_dpp v116, v232, v116, vcc quad_perm:[0,0,0,0] row_mask:0xf bank_mask:0xf\n\t"
      "v_cndmask_b32_dpp v117, v232, v117, vcc quad_perm:[1,1,1,1] row_mask:0xf bank_mask:0xf\n\t"
      "v_cndmask_b32_dpp v118, v232, v118, vcc quad_perm:[2,2,2,2] row_mask:0xf bank_mask:0xf\n\t"
      "v_cndmask_b32_dpp v119, v232, v119, vcc quad_perm:[3,3,3,3] row_mask:0xf bank_mask:0xf\n\t"
      "s_add_u32 s58, s70, s42\n\t"
      "s_addc_u32 s59, s71, 0\n\t"
      "global_load_dwordx4 v[140:143], v244, s[58:59] sc1\n\t"
      "v_mfma_f32_16x16x32_bf16 v[216:219], v[176:179], v[92:95], v[216:219]\n\t"
      "ds_write_b128 v245, v[212:215] offset:34816\n\t"
      "v_mfma_f32_16x16x32_bf16 v[216:219], v[180:183], v[108:111], v[216:219]\n\t"
      "s_waitcnt vmcnt(7)\n\t"
      "v_mfma_f32_16x16x32_bf16 v[220:223], v[184:187], v[0:3], 0\n\t"
      "v_mfma_f32_16x16x32_bf16 v[220:223], v[188:191], v[16:19], v[220:223]\n\t"
      "s_add_u32 s58, s70, s43\n\t"
      "s_addc_u32 s59, s71, 0\n\t"
      "global_load_dwordx4 v[144:147], v244, s[58:59] sc1\n\t"
      "s_nop 1\n\t"
      "ds_write_b128 v245, v[216:219] offset:35840\n\t"
      "v_mfma_f32_16x16x32_bf16 v[220:223], v[192:195], v[32:35], v[220:223]\n\t"
      "v_mfma_f32_16x16x32_bf16 v[220:223], v[196:199], v[48:51], v[220:223]\n\t"
      "s_waitcnt vmcnt(4)\n\t"
      "v_mfma_f32_16x16x32_bf16 v[220:223], v[200:203], v[64:67], v[220:223]\n\t"
      "v_mfma_f32_16x16x32_bf16 v[220:223], v[204:207], v[80:83], v[220:223]\n\t"
      "s_add_u32 s58, s70, s44\n\t"
      "s_addc_u32 s59, s71, 0\n\t"
      "global_load_dwordx4 v[148:151], v244, s[58:59] sc1\n\t"
      "s_waitcnt lgkmcnt(0)\n\t"
      "s_barrier\n\t"
      "ds_read_b64 v[228:229], v246 offset:32768\n\t"
      "ds_read_b64 v[230:231], v246 offset:36864\n\t"
      "v_mfma_f32_16x16x32_bf16 v[220:223], v[128:131], v[96:99], v[220:223]\n\t"
      "ds_read_b64 v[232:233], v246 offset:40960\n\t"
      "ds_read_b64 v[234:235], v246 offset:45056\n\t"
      "v_mfma_f32_16x16x32_bf16 v[224:227], v[184:187], v[4:7], 0\n\t"
      "ds_read_b64 v[236:237], v246 offset:49152\n\t"
      "ds_read_b64 v[238:239], v246 offset:53248\n\t"
      "v_mfma_f32_16x16x32_bf16 v[224:227], v[188:191], v[20:23], v[224:227]\n\t"
      "ds_read_b64 v[240:241], v246 offset:57344\n\t"
      "ds_read_b64 v[242:243], v246 offset:61440\n\t"
      "v_lshlrev_b32_e32 v113, 16, v112\n\t"
      "v_mfma_f32_16x16x32_bf16 v[224:227], v[192:195], v[36:39], v[224:227]\n\t"
      "s_add_u32 s58, s70, s45\n\t"
      "s_addc_u32 s59, s71, 0\n\t"
      "global_load_dwordx4 v[152:155], v244, s[58:59] sc1\n\t"
      "v_and_b32_e32 v114, 0xffff0000, v112\n\t"
      "v_add_f32_e32 v251, v220, v221\n\t"
      "v_add_f32_e32 v251, v251, v222\n\t"
      "v_cmp_u_f32_e32 vcc, v251, v223\n\t"
      "s_cbranch_vccnz LA42R_%=\n\t"
      "LA42B_%=:\n\t"
      "v_mfma_f32_16x16x32_bf16 v[224:227], v[196:199], v[52:55], v[224:227]\n\t"
      "v_add_f32_e32 v113, v249, v113\n\t"
      "v_add_f32_e32 v114, v249, v114\n\t"
      "v_mfma_f32_16x16x32_bf16 v[224:227], v[200:203], v[68:71], v[224:227]\n\t"
      "s_waitcnt lgkmcnt(4)\n\t"
      "v_mfma_f32_16x16x32_bf16 v[224:227], v[204:207], v[84:87], v[224:227]\n\t"
      "v_add_f32_e32 v228, v228, v230\n\t"
      "ds_write_b128 v245, v[220:223]\n\t"
      "global_load_dword v112, v248, s[50:51] sc1\n\t"
      "s_add_u32 s50, s50, 2048\n\t"
      "s_addc_u32 s51, s51, 0\n\t"
      "v_mfma_f32_16x16x32_bf16 v[224:227], v[128:131], v[100:103], v[224:227]\n\t"
      "s_add_u32 s58, s70, s46\n\t"
      "s_addc_u32 s59, s71, 0\n\t"
      "global_load_dwordx4 v[156:159], v244, s[58:59] sc1\n\t"
      "v_add_f32_e32 v232, v232, v234\n\t"
      "v_add_f32_e32 v229, v229, v231\n\t"
      "v_mfma_f32_16x16x32_bf16 v[208:211], v[184:187], v[8:11], 0\n\t"
      "v_add_f32_e32 v233, v233, v235\n\t"
      "v_mfma_f32_16x16x32_bf16 v[208:211], v[188:191], v[24:27], v[208:211]\n\t"
      "s_waitcnt lgkmcnt(1)\n\t"
      "v_add_f32_e32 v236, v236, v238\n\t"
      "v_mfma_f32_16x16x32_bf16 v[208:211], v[192:195], v[40:43], v[208:211]\n\t"
      "v_add_f32_e32 v240, v240, v242\n\t"
      "v_mfma_f32_16x16x32_bf16 v[208:211], v[196:199], v[56:59], v[208:211]\n\t"
      "v_add_f32_e32 v237, v237, v239\n\t"
      "s_add_u32 s58, s72, s40\n\t"
      "s_addc_u32 s59, s73, 0\n\t"
      "global_load_dwordx4 v[160:163], v244, s[58:59] sc1\n\t"
      "v_mfma_f32_16x16x32_bf16 v[208:211], v[200:203], v[72:75], v[208:211]\n\t"
      "v_add_f32_e32 v241, v241, v243\n\t"
      "v_add_f32_e32 v228, v228, v232\n\t"
      "v_mfma_f32_16x16x32_bf16 v[208:211], v[204:207], v[88:91], v[208:211]\n\t"
      "v_add_f32_e32 v236, v236, v240\n\t"
      "ds_write_b128 v245, v[224:227] offset:1024\n\t"
      "v_mfma_f32_16x16x32_bf16 v[208:211], v[128:131], v[104:107], v[208:211]\n\t"
      "v_add_f32_e32 v229, v229, v233\n\t"
      "v_add_f32_e32 v237, v237, v241\n\t"
      "v_mfma_f32_16x16x32_bf16 v[212:215], v[184:187], v[12:15], 0\n\t"
      "v_add_f32_e32 v228, v228, v113\n\t"
      "s_add_u32 s58, s72, s41\n\t"
      "s_addc_u32 s59, s73, 0\n\t"
      "global_load_dwordx4 v[164:167], v244, s[58:59] sc1\n\t"
      "v_mfma_f32_16x16x32_bf16 v[212:215], v[188:191], v[28:31], v[212:215]\n\t"
      "v_add_f32_e32 v229, v229, v114\n\t"
      "v_mfma_f32_16x16x32_bf16 v[212:215], v[192:195], v[44:47], v[212:215]\n\t"
      "v_add_f32_e32 v228, v228, v236\n\t"
      "v_add_f32_e32 v229, v229, v237\n\t"
      "v_mfma_f32_16x16x32_bf16 v[212:215], v[196:199], v[60:63], v[212:215]\n\t"
      "v_cvt_pk_bf16_f32 v232, v228, v229\n\t"
      "v_mfma_f32_16x16x32_bf16 v[212:215], v[200:203], v[76:79], v[212:215]\n\t"
      "s_mov_b32 vcc_lo, 0xdddddddd\n\t"
      "s_mov_b32 vcc_hi, 0xdddddddd\n\t"
      "v_cndmask_b32_dpp v116, v232, v116, vcc quad_perm:[0,0,0,0] row_mask:0xf bank_mask:0xf\n\t"
      "v_cndmask_b32_dpp v117, v232, v117, vcc quad_perm:[1,1,1,1] row_mask:0xf bank_mask:0xf\n\t"
      "v_cndmask_b32_dpp v118, v232, v118, vcc quad_perm:[2,2,2,2] row_mask:0xf bank_mask:0xf\n\t"
      "v_cndmask_b32_dpp v119, v232, v119, vcc quad_perm:[3,3,3,3] row_mask:0xf bank_mask:0xf\n\t"
      "s_add_u32 s58, s72, s42\n\t"
      "s_addc_u32 s59, s73, 0\n\t"
      "global_load_dwordx4 v[168:171], v244, s[58:59] sc1\n\t"
      "v_mfma_f32_16x16x32_bf16 v[212:215], v[204:207], v[92:95], v[212:215]\n\t"
      "ds_write_b128 v245, v[208:211] offset:2048\n\t"
      "v_mfma_f32_16x16x32_bf16 v[212:215], v[128:131], v[108:111], v[212:215]\n\t"
      "s_waitcnt vmcnt(7)\n\t"
      "v_mfma_f32_16x16x32_bf16 v[216:219], v[132:135], v[0:3], 0\n\t"
      "v_mfma_f32_16x16x32_bf16 v[216:219], v[136:139], v[16:19], v[216:219]\n\t"
      "s_add_u32 s58, s72, s43\n\t"
      "s_addc_u32 s59, s73, 0\n\t"
      "global_load_dwordx4 v[172:175], v244, s[58:59] sc1\n\t"
      "s_nop 1\n\t"
      "ds_write_b128 v245, v[212:215] offset:3072\n\t"
      "v_mfma_f32_16x16x32_bf16 v[216:219], v[140:143], v[32:35], v[216:219]\n\t"
      "v_mfma_f32_16x16x32_bf16 v[216:219], v[144:147], v[48:51], v[216:219]\n\t"
      "s_waitcnt vmcnt(4)\n\t"
      "v_mfma_f32_16x16x32_bf16 v[216:219], v[148:151], v[64:67], v[216:219]\n\t"
      "v_mfma_f32_16x16x32_bf16 v[216:219], v[152:155], v[80:83], v[216:219]\n\t"
      "s_add_u32 s58, s72, s44\n\t"
      "s_addc_u32 s59, s73, 0\n\t"
      "global_load_dwordx4 v[176:179], v244, s[58:59] sc1\n\t"
      "s_waitcnt lgkmcnt(0)\n\t"
      "s_barrier\n\t"
      "ds_read_b64 v[228:229], v246\n\t"
      "ds_read_b64 v[230:231], v246 offset:4096\n\t"
      "v_mfma_f32_16x16x32_bf16 v[216:219], v[156:159], v[96:99], v[216:219]\n\t"
      "ds_read_b64 v[232:233], v246 offset:8192\n\t"
      "ds_read_b64 v[234:235], v246 offset:12288\n\t"
      "v_mfma_f32_16x16x32_bf16 v[220:223], v[132:135], v[4:7], 0\n\t"
      "ds_read_b64 v[236:237], v246 offset:16384\n\t"
      "ds_read_b64 v[238:239], v246 offset:20480\n\t"
      "v_mfma_f32_16x16x32_bf16 v[220:223], v[136:139], v[20:23], v[220:223]\n\t"
      "ds_read_b64 v[240:241], v246 offset:24576\n\t"
      "ds_read_b64 v[242:243], v246 offset:28672\n\t"
      "v_lshlrev_b32_e32 v113, 16, v112\n\t"
      "v_mfma_f32_16x16x32_bf16 v[220:223], v[140:143], v[36:39], v[220:223]\n\t"
      "s_add_u32 s58, s72, s45\n\t"
      "s_addc_u32 s59, s73, 0\n\t"
      "global_load_dwordx4 v[180:183], v244, s[58:59] sc1\n\t"
      "v_and_b32_e32 v114, 0xffff0000, v112\n\t"
      "v_add_f32_e32 v251, v216, v217\n\t"
      "v_add_f32_e32 v251, v251, v218\n\t"
      "v_cmp_u_f32_e32 vcc, v251, v219\n\t"
      "s_cbranch_vccnz LA43R_%=\n\t"
      "LA43B_%=:\n\t"
      "v_mfma_f32_16x16x32_bf16 v[220:223], v[144:147], v[52:55], v[220:223]\n\t"
      "v_add_f32_e32 v113, v249, v113\n\t"
      "v_add_f32_e32 v114, v249, v114\n\t"
      "v_mfma_f32_16x16x32_bf16 v[220:223], v[148:151], v[68:71], v[220:223]\n\t"
      "s_waitcnt lgkmcnt(4)\n\t"
      "v_mfma_f32_16x16x32_bf16 v[220:223], v[152:155], v[84:87], v[220:223]\n\t"
      "v_add_f32_e32 v228, v228, v230\n\t"
      "ds_write_b128 v245, v[216:219] offset:32768\n\t"
      "global_load_dword v112, v248, s[50:51] sc1\n\t"
      "s_add_u32 s50, s50, 2048\n\t"
      "s_addc_u32 s51, s51, 0\n\t"
      "v_mfma_f32_16x16x32_bf16 v[220:223], v[156:159], v[100:103], v[220:223]\n\t"
      "s_add_u32 s58, s72, s46\n\t"
      "s_addc_u32 s59, s73, 0\n\t"
      "global_load_dwordx4 v[184:187], v244, s[58:59] sc1\n\t"
      "v_add_f32_e32 v232, v232, v234\n\t"
      "v_add_f32_e32 v229, v229, v231\n\t"
      "v_mfma_f32_16x16x32_bf16 v[224:227], v[132:135], v[8:11], 0\n\t"
      "v_add_f32_e32 v233, v233, v235\n\t"
      "v_mfma_f32_16x16x32_bf16 v[224:227], v[136:139], v[24:27], v[224:227]\n\t"
      "s_waitcnt lgkmcnt(1)\n\t"
      "v_add_f32_e32 v236, v236, v238\n\t"
      "v_mfma_f32_16x16x32_bf16 v[224:227], v[140:143], v[40:43], v[224:227]\n\t"
      "v_add_f32_e32 v240, v240, v242\n\t"
      "v_mfma_f32_16x16x32_bf16 v[224:227], v[144:147], v[56:59], v[224:227]\n\t"
      "v_add_f32_e32 v237, v237, v239\n\t"
      "s_add_u32 s58, s74, s40\n\t"
      "s_addc_u32 s59, s75, 0\n\t"
      "global_load_dwordx4 v[188:191], v244, s[58:59] sc1\n\t"
      "v_mfma_f32_16x16x32_bf16 v[224:227], v[148:151], v[72:75], v[224:227]\n\t"
      "v_add_f32_e32 v241, v241, v243\n\t"
      "v_add_f32_e32 v228, v228, v232\n\t"
      "v_mfma_f32_16x16x32_bf16 v[224:227], v[152:155], v[88:91], v[224:227]\n\t"
      "v_add_f32_e32 v236, v236, v240\n\t"
      "ds_write_b128 v245, v[220:223] offset:33792\n\t"
      "v_mfma_f32_16x16x32_bf16 v[224:227], v[156:159], v[104:107], v[224:227]\n\t"
      "v_add_f32_e32 v229, v229, v233\n\t"
      "v_add_f32_e32 v237, v237, v241\n\t"
      "v_mfma_f32_16x16x32_bf16 v[208:211], v[132:135], v[12:15], 0\n\t"
      "v_add_f32_e32 v228, v228, v113\n\t"
      "s_add_u32 s58, s74, s41\n\t"
      "s_addc_u32 s59, s75, 0\n\t"
      "global_load_dwordx4 v[192:195], v244, s[58:59] sc1\n\t"
      "v_mfma_f32_16x16x32_bf16 v[208:211], v[136:139], v[28:31], v[208:211]\n\t"
      "v_add_f32_e32 v229, v229, v114\n\t"
      "v_mfma_f32_16x16x32_bf16 v[208:211], v[140:143], v[44:47], v[208:211]\n\t"
      "v_add_f32_e32 v228, v228, v236\n\t"
      "v_add_f32_e32 v229, v229, v237\n\t"
      "v_mfma_f32_16x16x32_bf16 v[208:211], v[144:147], v[60:63], v[208:211]\n\t"
      "v_cvt_pk_bf16_f32 v232, v228, v229\n\t"
      "v_mfma_f32_16x16x32_bf16 v[208:211], v[148:151], v[76:79], v[208:211]\n\t"
      "s_mov_b32 vcc_lo, 0xbbbbbbbb\n\t"
      "s_mov_b32 vcc_hi, 0xbbbbbbbb\n\t"
      "v_cndmask_b32_dpp v116, v232, v116, vcc quad_perm:[0,0,0,0] row_mask:0xf bank_mask:0xf\n\t"
      "v_cndmask_b32_dpp v117, v232, v117, vcc quad_perm:[1,1,1,1] row_mask:0xf bank_mask:0xf\n\t"
      "v_cndmask_b32_dpp v118, v232, v118, vcc quad_perm:[2,2,2,2] row_mask:0xf bank_mask:0xf\n\t"
      "v_cndmask_b32_dpp v119, v232, v119, vcc quad_perm:[3,3,3,3] row_mask:0xf bank_mask:0xf\n\t"
      "s_add_u32 s58, s74, s42\n\t"
      "s_addc_u32 s59, s75, 0\n\t"
      "global_load_dwordx4 v[196:199], v244, s[58:59] sc1\n\t"
      "v_mfma_f32_16x16x32_bf16 v[208:211], v[152:155], v[92:95], v[208:211]\n\t"
      "ds_write_b128 v245, v[224:227] offset:34816\n\t"
      "v_mfma_f32_16x16x32_bf16 v[208:211], v[156:159], v[108:111], v[208:211]\n\t"
      "s_waitcnt vmcnt(7)\n\t"
      "v_mfma_f32_16x16x32_bf16 v[212:215], v[160:163], v[0:3], 0\n\t"
      "v_mfma_f32_16x16x32_bf16 v[212:215], v[164:167], v[16:19], v[212:215]\n\t"
      "s_add_u32 s58, s74, s43\n\t"
      "s_addc_u32 s59, s75, 0\n\t"
      "global_load_dwordx4 v[200:203], v244, s[58:59] sc1\n\t"
      "s_nop 1\n\t"
      "ds_write_b128 v245, v[208:211] offset:35840\n\t"
      "v_mfma_f32_16x16x32_bf16 v[212:215], v[168:171], v[32:35], v[212:215]\n\t"
      "v_mfma_f32_16x16x32_bf16 v[212:215], v[172:175], v[48:51], v[212:215]\n\t"
      "s_waitcnt vmcnt(4)\n\t"
      "v_mfma_f32_16x16x32_bf16 v[212:215], v[176:179], v[64:67], v[212:215]\n\t"
      "v_mfma_f32_16x16x32_bf16 v[212:215], v[180:183], v[80:83], v[212:215]\n\t"
      "s_add_u32 s58, s74, s44\n\t"
      "s_addc_u32 s59, s75, 0\n\t"
      "global_load_dwordx4 v[204:207], v244, s[58:59] sc1\n\t"
      "s_waitcnt lgkmcnt(0)\n\t"
      "s_barrier\n\t"
      "ds_read_b64 v[228:229], v246 offset:32768\n\t"
      "ds_read_b64 v[230:231], v246 offset:36864\n\t"
      "v_mfma_f32_16x16x32_bf16 v[212:215], v[184:187], v[96:99], v[212:215]\n\t"
      "ds_read_b64 v[232:233], v246 offset:40960\n\t"
      "ds_read_b64 v[234:235], v246 offset:45056\n\t"
      "v_mfma_f32_16x16x32_bf16 v[216:219], v[160:163], v[4:7], 0\n\t"
      "ds_read_b64 v[236:237], v246 offset:49152\n\t"
      "ds_read_b64 v[238:239], v246 offset:53248\n\t"
      "v_mfma_f32_16x16x32_bf16 v[216:219], v[164:167], v[20:23], v[216:219]\n\t"
      "ds_read_b64 v[240:241], v246 offset:57344\n\t"
      "ds_read_b64 v[242:243], v246 offset:61440\n\t"
      "v_lshlrev_b32_e32 v113, 16, v112\n\t"
      "v_mfma_f32_16x16x32_bf16 v[216:219], v[168:171], v[36:39], v[216:219]\n\t"
      "s_add_u32 s58, s74, s45\n\t"
      "s_addc_u32 s59, s75, 0\n\t"
      "global_load_dwordx4 v[128:131], v244, s[58:59] sc1\n\t"
      "v_and_b32_e32 v114, 0xffff0000, v112\n\t"
      "v_add_f32_e32 v251, v212, v213\n\t"
      "v_add_f32_e32 v251, v251, v214\n\t"
      "v_cmp_u_f32_e32 vcc, v251, v215\n\t"
      "s_cbranch_vccnz LA44R_%=\n\t"
      "LA44B_%=:\n\t"
      "v_mfma_f32_16x16x32_bf16 v[216:219], v[172:175], v[52:55], v[216:219]\n\t"
      "v_add_f32_e32 v113, v249, v113\n\t"
      "v_add_f32_e32 v114, v249, v114\n\t"
      "v_mfma_f32_16x16x32_bf16 v[216:219], v[176:179], v[68:71], v[216:219]\n\t"
      "s_waitcnt lgkmcnt(4)\n\t"
      "v_mfma_f32_16x16x32_bf16 v[216:219], v[180:183], v[84:87], v[216:219]\n\t"
      "v_add_f32_e32 v228, v228, v230\n\t"
      "ds_write_b128 v245, v[212:215]\n\t"
      "global_load_dword v112, v248, s[50:51] sc1\n\t"
      "s_add_u32 s50, s50, 2048\n\t"
      "s_addc_u32 s51, s51, 0\n\t"
      "v_mfma_f32_16x16x32_bf16 v[216:219], v[184:187], v[100:103], v[216:219]\n\t"
      "s_add_u32 s58, s74, s46\n\t"
      "s_addc_u32 s59, s75, 0\n\t"
      "global_load_dwordx4 v[132:135], v244, s[58:59] sc1\n\t"
      "v_add_f32_e32 v232, v232, v234\n\t"
      "v_add_f32_e32 v229, v229, v231\n\t"
      "v_mfma_f32_16x16x32_bf16 v[220:223], v[160:163], v[8:11], 0\n\t"
      "v_add_f32_e32 v233, v233, v235\n\t"
      "v_mfma_f32_16x16x32_bf16 v[220:223], v[164:167], v[24:27], v[220:223]\n\t"
      "s_waitcnt lgkmcnt(1)\n\t"
      "v_add_f32_e32 v236, v236, v238\n\t"
      "v_mfma_f32_16x16x32_bf16 v[220:223], v[168:171], v[40:43], v[220:223]\n\t"
      "v_add_f32_e32 v240, v240, v242\n\t"
      "v_mfma_f32_16x16x32_bf16 v[220:223], v[172:175], v[56:59], v[220:223]\n\t"
      "v_add_f32_e32 v237, v237, v239\n\t"
      "s_add_u32 s58, s76, s40\n\t"
      "s_addc_u32 s59, s77, 0\n\t"
      "global_load_dwordx4 v[136:139], v244, s[58:59] sc1\n\t"
      "v_mfma_f32_16x16x32_bf16 v[220:223], v[176:179], v[72:75], v[220:223]\n\t"
      "v_add_f32_e32 v241, v241, v243\n\t"
      "v_add_f32_e32 v228, v228, v232\n\t"
      "v_mfma_f32_16x16x32_bf16 v[220:223], v[180:183], v[88:91], v[220:223]\n\t"
      "v_add_f32_e32 v236, v236, v240\n\t"
      "ds_write_b128 v245, v[216:219] offset:1024\n\t"
      "v_mfma_f32_16x16x32_bf16 v[220:223], v[184:187], v[104:107], v[220:223]\n\t"
      "v_add_f32_e32 v229, v229, v233\n\t"
      "v_add_f32_e32 v237, v237, v241\n\t"
      "v_mfma_f32_16x16x32_bf16 v[224:227], v[160:163], v[12:15], 0\n\t"
      "v_add_f32_e32 v228, v228, v113\n\t"
      "s_add_u32 s58, s76, s41\n\t"
      "s_addc_u32 s59, s77, 0\n\t"
      "global_load_dwordx4 v[140:143], v244, s[58:59] sc1\n\t"
      "v_mfma_f32_16x16x32_bf16 v[224:227], v[164:167], v[28:31], v[224:227]\n\t"
      "v_add_f32_e32 v229, v229, v114\n\t"
      "v_mfma_f32_16x16x32_bf16 v[224:227], v[168:171], v[44:47], v[224:227]\n\t"
      "v_add_f32_e32 v228, v228, v236\n\t"
      "v_add_f32_e32 v229, v229, v237\n\t"
      "v_mfma_f32_16x16x32_bf16 v[224:227], v[172:175], v[60:63], v[224:227]\n\t"
      "v_cvt_pk_bf16_f32 v232, v228, v229\n\t"
      "v_mfma_f32_16x16x32_bf16 v[224:227], v[176:179], v[76:79], v[224:227]\n\t"
      "s_mov_b32 vcc_lo, 0x77777777\n\t"
      "s_mov_b32 vcc_hi, 0x77777777\n\t"
      "v_cndmask_b32_dpp v116, v232, v116, vcc quad_perm:[0,0,0,0] row_mask:0xf bank_mask:0xf\n\t"
      "v_cndmask_b32_dpp v117, v232, v117, vcc quad_perm:[1,1,1,1] row_mask:0xf bank_mask:0xf\n\t"
      "v_cndmask_b32_dpp v118, v232, v118, vcc quad_perm:[2,2,2,2] row_mask:0xf bank_mask:0xf\n\t"
      "v_cndmask_b32_dpp v119, v232, v119, vcc quad_perm:[3,3,3,3] row_mask:0xf bank_mask:0xf\n\t"
      "global_store_dwordx4 v247, v[116:119], s[62:63] sc1\n\t"
      "s_add_u32 s58, s76, s42\n\t"
      "s_addc_u32 s59, s77, 0\n\t"
      "global_load_dwordx4 v[144:147], v244, s[58:59] sc1\n\t"
      "v_mfma_f32_16x16x32_bf16 v[224:227], v[180:183], v[92:95], v[224:227]\n\t"
      "ds_write_b128 v245, v[220:223] offset:2048\n\t"
      "v_mfma_f32_16x16x32_bf16 v[224:227], v[184:187], v[108:111], v[224:227]\n\t"
      "s_waitcnt vmcnt(8)\n\t"
      "v_mfma_f32_16x16x32_bf16 v[208:211], v[188:191], v[0:3], 0\n\t"
      "v_mfma_f32_16x16x32_bf16 v[208:211], v[192:195], v[16:19], v[208:211]\n\t"
      "s_add_u32 s58, s76, s43\n\t"
      "s_addc_u32 s59, s77, 0\n\t"
      "global_load_dwordx4 v[148:151], v244, s[58:59] sc1\n\t"
      "s_nop 1\n\t"
      "ds_write_b128 v245, v[224:227] offset:3072\n\t"
      "v_mfma_f32_16x16x32_bf16 v[208:211], v[196:199], v[32:35], v[208:211]\n\t"
      "v_mfma_f32_16x16x32_bf16 v[208:211], v[200:203], v[48:51], v[208:211]\n\t"
      "s_waitcnt vmcnt(5)\n\t"
      "v_mfma_f32_16x16x32_bf16 v[208:211], v[204:207], v[64:67], v[208:211]\n\t"
      "v_mfma_f32_16x16x32_bf16 v[208:211], v[128:131], v[80:83], v[208:211]\n\t"
      "s_add_u32 s58, s76, s44\n\t"
      "s_addc_u32 s59, s77, 0\n\t"
      "global_load_dwordx4 v[152:155], v244, s[58:59] sc1\n\t"
      "s_waitcnt lgkmcnt(0)\n\t"
      "s_barrier\n\t"
      "ds_read_b64 v[228:229], v246\n\t"
      "ds_read_b64 v[230:231], v246 offset:4096\n\t"
      "v_mfma_f32_16x16x32_bf16 v[208:211], v[132:135], v[96:99], v[208:211]\n\t"
      "ds_read_b64 v[232:233], v246 offset:8192\n\t"
      "ds_read_b64 v[234:235], v246 offset:12288\n\t"
      "v_mfma_f32_16x16x32_bf16 v[212:215], v[188:191], v[4:7], 0\n\t"
      "ds_read_b64 v[236:237], v246 offset:16384\n\t"
      "ds_read_b64 v[238:239], v246 offset:20480\n\t"
      "v_mfma_f32_16x16x32_bf16 v[212:215], v[192:195], v[20:23], v[212:215]\n\t"
      "ds_read_b64 v[240:241], v246 offset:24576\n\t"
      "ds_read_b64 v[242:243], v246 offset:28672\n\t"
      "v_lshlrev_b32_e32 v113, 16, v112\n\t"
      "v_mfma_f32_16x16x32_bf16 v[212:215], v[196:199], v[36:39], v[212:215]\n\t"
      "s_add_u32 s58, s76, s45\n\t"
      "s_addc_u32 s59, s77, 0\n\t"
      "global_load_dwordx4 v[156:159], v244, s[58:59] sc1\n\t"
      "v_and_b32_e32 v114, 0xffff0000, v112\n\t"
      "v_add_f32_e32 v251, v208, v209\n\t"
      "v_add_f32_e32 v251, v251, v210\n\t"
      "v_cmp_u_f32_e32 vcc, v251, v211\n\t"
      "s_cbranch_vccnz LA45R_%=\n\t"
      "LA45B_%=:\n\t"
      "v_mfma_f32_16x16x32_bf16 v[212:215], v[200:203], v[52:55], v[212:215]\n\t"
      "v_add_f32_e32 v113, v249, v113\n\t"
      "v_add_f32_e32 v114, v249, v114\n\t"
      "v_mfma_f32_16x16x32_bf16 v[212:215], v[204:207], v[68:71], v[212:215]\n\t"
      "s_waitcnt lgkmcnt(4)\n\t"
      "v_mfma_f32_16x16x32_bf16 v[212:215], v[128:131], v[84:87], v[212:215]\n\t"
      "v_add_f32_e32 v228, v228, v230\n\t"
      "ds_write_b128 v245, v[208:211] offset:32768\n\t"
      "global_load_dword v112, v248, s[50:51] sc1\n\t"
      "s_add_u32 s50, s50, 2048\n\t"
      "s_addc_u32 s51, s51, 0\n\t"
      "v_mfma_f32_16x16x32_bf16 v[212:215], v[132:135], v[100:103], v[212:215]\n\t"
      "s_add_u32 s58, s76, s46\n\t"
      "s_addc_u32 s59, s77, 0\n\t"
      "global_load_dwordx4 v[160:163], v244, s[58:59] sc1\n\t"
      "v_add_f32_e32 v232, v232, v234\n\t"
      "v_add_f32_e32 v229, v229, v231\n\t"
      "v_mfma_f32_16x16x32_bf16 v[216:219], v[188:191], v[8:11], 0\n\t"
      "v_add_f32_e32 v233, v233, v235\n\t"
      "v_mfma_f32_16x16x32_bf16 v[216:219], v[192:195], v[24:27], v[216:219]\n\t"
      "s_waitcnt lgkmcnt(1)\n\t"
      "v_add_f32_e32 v236, v236, v238\n\t"
      "v_mfma_f32_16x16x32_bf16 v[216:219], v[196:199], v[40:43], v[216:219]\n\t"
      "v_add_f32_e32 v240, v240, v242\n\t"
      "v_mfma_f32_16x16x32_bf16 v[216:219], v[200:203], v[56:59], v[216:219]\n\t"
      "v_add_f32_e32 v237, v237, v239\n\t"
      "v_mfma_f32_16x16x32_bf16 v[216:219], v[204:207], v[72:75], v[216:219]\n\t"
      "v_add_f32_e32 v241, v241, v243\n\t"
      "v_add_f32_e32 v228, v228, v232\n\t"
      "v_mfma_f32_16x16x32_bf16 v[216:219], v[128:131], v[88:91], v[216:219]\n\t"
      "v_add_f32_e32 v236, v236, v240\n\t"
      "ds_write_b128 v245, v[212:215] offset:33792\n\t"
      "v_mfma_f32_16x16x32_bf16 v[216:219], v[132:135], v[104:107], v[216:219]\n\t"
      "v_add_f32_e32 v229, v229, v233\n\t"
      "v_add_f32_e32 v237, v237, v241\n\t"
      "v_mfma_f32_16x16x32_bf16 v[220:223], v[188:191], v[12:15], 0\n\t"
      "v_add_f32_e32 v228, v228, v113\n\t"
      "v_mfma_f32_16x16x32_bf16 v[220:223], v[192:195], v[28:31], v[220:223]\n\t"
      "v_add_f32_e32 v229, v229, v114\n\t"
      "v_mfma_f32_16x16x32_bf16 v[220:223], v[196:199], v[44:47], v[220:223]\n\t"
      "v_add_f32_e32 v228, v228, v236\n\t"
      "v_add_f32_e32 v229, v229, v237\n\t"
      "v_mfma_f32_16x16x32_bf16 v[220:223], v[200:203], v[60:63], v[220:223]\n\t"
      "v_cvt_pk_bf16_f32 v232, v228, v229\n\t"
      "v_mfma_f32_16x16x32_bf16 v[220:223], v[204:207], v[76:79], v[220:223]\n\t"
      "s_mov_b32 vcc_lo, 0xeeeeeeee\n\t"
      "s_mov_b32 vcc_hi, 0xeeeeeeee\n\t"
      "v_cndmask_b32_dpp v116, v232, v116, vcc quad_perm:[0,0,0,0] row_mask:0xf bank_mask:0xf\n\t"
      "v_cndmask_b32_dpp v117, v232, v117, vcc quad_perm:[1,1,1,1] row_mask:0xf bank_mask:0xf\n\t"
      "v_cndmask_b32_dpp v118, v232, v118, vcc quad_perm:[2,2,2,2] row_mask:0xf bank_mask:0xf\n\t"
      "v_cndmask_b32_dpp v119, v232, v119, vcc quad_perm:[3,3,3,3] row_mask:0xf bank_mask:0xf\n\t"
      "v_mfma_f32_16x16x32_bf16 v[220:223], v[128:131], v[92:95], v[220:223]\n\t"
      "ds_write_b128 v245, v[216:219] offset:34816\n\t"
      "v_mfma_f32_16x16x32_bf16 v[220:223], v[132:135], v[108:111], v[220:223]\n\t"
      "s_waitcnt vmcnt(4)\n\t"
      "v_mfma_f32_16x16x32_bf16 v[224:227], v[136:139], v[0:3], 0\n\t"
      "v_mfma_f32_16x16x32_bf16 v[224:227], v[140:143], v[16:19], v[224:227]\n\t"
      "s_nop 4\n\t"
      "ds_write_b128 v245, v[220:223] offset:35840\n\t"
      "v_mfma_f32_16x16x32_bf16 v[224:227], v[144:147], v[32:35], v[224:227]\n\t"
      "v_mfma_f32_16x16x32_bf16 v[224:227], v[148:151], v[48:51], v[224:227]\n\t"
      "s_waitcnt vmcnt(0)\n\t"
      "v_mfma_f32_16x16x32_bf16 v[224:227], v[152:155], v[64:67], v[224:227]\n\t"
      "v_mfma_f32_16x16x32_bf16 v[224:227], v[156:159], v[80:83], v[224:227]\n\t"
      "s_waitcnt lgkmcnt(0)\n\t"
      "s_barrier\n\t"
      "ds_read_b64 v[228:229], v246 offset:32768\n\t"
      "ds_read_b64 v[230:231], v246 offset:36864\n\t"
      "v_mfma_f32_16x16x32_bf16 v[224:227], v[160:163], v[96:99], v[224:227]\n\t"
      "ds_read_b64 v[232:233], v246 offset:40960\n\t"
      "ds_read_b64 v[234:235], v246 offset:45056\n\t"
      "v_mfma_f32_16x16x32_bf16 v[208:211], v[136:139], v[4:7], 0\n\t"
      "ds_read_b64 v[236:237], v246 offset:49152\n\t"
      "ds_read_b64 v[238:239], v246 offset:53248\n\t"
      "v_mfma_f32_16x16x32_bf16 v[208:211], v[140:143], v[20:23], v[208:211]\n\t"
      "ds_read_b64 v[240:241], v246 offset:57344\n\t"
      "ds_read_b64 v[242:243], v246 offset:61440\n\t"
      "v_lshlrev_b32_e32 v113, 16, v112\n\t"
      "v_and_b32_e32 v114, 0xffff0000, v112\n\t"
      "v_mfma_f32_16x16x32_bf16 v[208:211], v[144:147], v[36:39], v[208:211]\n\t"
      "v_add_f32_e32 v113, v249, v113\n\t"
      "v_add_f32_e32 v114, v249, v114\n\t"
      "v_add_f32_e32 v251, v224, v225\n\t"
      "v_add_f32_e32 v251, v251, v226\n\t"
      "v_cmp_u_f32_e32 vcc, v251, v227\n\t"
      "s_cbranch_vccnz LA46R_%=\n\t"
      "LA46B_%=:\n\t"
      "v_mfma_f32_16x16x32_bf16 v[208:211], v[148:151], v[52:55], v[208:211]\n\t"
      "s_waitcnt lgkmcnt(4)\n\t"
      "v_add_f32_e32 v228, v228, v230\n\t"
      "v_add_f32_e32 v232, v232, v234\n\t"
      "v_mfma_f32_16x16x32_bf16 v[208:211], v[152:155], v[68:71], v[208:211]\n\t"
      "v_add_f32_e32 v229, v229, v231\n\t"
      "v_add_f32_e32 v233, v233, v235\n\t"
      "v_mfma_f32_16x16x32_bf16 v[208:211], v[156:159], v[84:87], v[208:211]\n\t"
      "s_waitcnt lgkmcnt(0)\n\t"
      "v_add_f32_e32 v236, v236, v238\n\t"
      "v_add_f32_e32 v240, v240, v242\n\t"
      "ds_write_b128 v245, v[224:227]\n\t"
      "global_load_dword v112, v248, s[50:51] sc1\n\t"
      "s_add_u32 s50, s50, 2048\n\t"
      "s_addc_u32 s51, s51, 0\n\t"
      "v_mfma_f32_16x16x32_bf16 v[208:211], v[160:163], v[100:103], v[208:211]\n\t"
      "v_add_f32_e32 v237, v237, v239\n\t"
      "v_add_f32_e32 v241, v241, v243\n\t"
      "v_mfma_f32_16x16x32_bf16 v[212:215], v[136:139], v[8:11], 0\n\t"
      "v_add_f32_e32 v228, v228, v232\n\t"
      "v_add_f32_e32 v236, v236, v240\n\t"
      "v_add_f32_e32 v229, v229, v233\n\t"
      "v_mfma_f32_16x16x32_bf16 v[212:215], v[140:143], v[24:27], v[212:215]\n\t"
      "v_add_f32_e32 v237, v237, v241\n\t"
      "v_add_f32_e32 v228, v228, v113\n\t"
      "v_mfma_f32_16x16x32_bf16 v[212:215], v[144:147], v[40:43], v[212:215]\n\t"
      "v_add_f32_e32 v229, v229, v114\n\t"
      "v_add_f32_e32 v228, v228, v236\n\t"
      "v_add_f32_e32 v229, v229, v237\n\t"
      "v_mfma_f32_16x16x32_bf16 v[212:215], v[148:151], v[56:59], v[212:215]\n\t"
      "v_cvt_pk_bf16_f32 v232, v228, v229\n\t"
      "s_mov_b32 vcc_lo, 0xdddddddd\n\t"
      "s_mov_b32 vcc_hi, 0xdddddddd\n\t"
      "v_cndmask_b32_dpp v116, v232, v116, vcc quad_perm:[0,0,0,0] row_mask:0xf bank_mask:0xf\n\t"
      "v_cndmask_b32_dpp v117, v232, v117, vcc quad_perm:[1,1,1,1] row_mask:0xf bank_mask:0xf\n\t"
      "v_cndmask_b32_dpp v118, v232, v118, vcc quad_perm:[2,2,2,2] row_mask:0xf bank_mask:0xf\n\t"
      "v_cndmask_b32_dpp v119, v232, v119, vcc quad_perm:[3,3,3,3] row_mask:0xf bank_mask:0xf\n\t"
      "v_mfma_f32_16x16x32_bf16 v[212:215], v[152:155], v[72:75], v[212:215]\n\t"
      "v_mfma_f32_16x16x32_bf16 v[212:215], v[156:159], v[88:91], v[212:215]\n\t"
      "ds_write_b128 v245, v[208:211] offset:1024\n\t"
      "v_mfma_f32_16x16x32_bf16 v[212:215], v[160:163], v[104:107], v[212:215]\n\t"
      "v_mfma_f32_16x16x32_bf16 v[216:219], v[136:139], v[12:15], 0\n\t"
      "v_mfma_f32_16x16x32_bf16 v[216:219], v[140:143], v[28:31], v[216:219]\n\t"
      "v_mfma_f32_16x16x32_bf16 v[216:219], v[144:147], v[44:47], v[216:219]\n\t"
      "v_mfma_f32_16x16x32_bf16 v[216:219], v[148:151], v[60:63], v[216:219]\n\t"
      "v_mfma_f32_16x16x32_bf16 v[216:219], v[152:155], v[76:79], v[216:219]\n\t"
      "v_mfma_f32_16x16x32_bf16 v[216:219], v[156:159], v[92:95], v[216:219]\n\t"
      "s_nop 1\n\t"
      "ds_write_b128 v245, v[212:215] offset:2048\n\t"
      "v_mfma_f32_16x16x32_bf16 v[216:219], v[160:163], v[108:111], v[216:219]\n\t"
      "s_nop 7\n\t"
      "ds_write_b128 v245, v[216:219] offset:3072\n\t"
      "s_waitcnt lgkmcnt(0)\n\t"
      "s_barrier\n\t"
      "ds_read_b64 v[228:229], v246\n\t"
      "ds_read_b64 v[230:231], v246 offset:4096\n\t"
      "ds_read_b64 v[232:233], v246 offset:8192\n\t"
      "ds_read_b64 v[234:235], v246 offset:12288\n\t"
      "ds_read_b64 v[236:237], v246 offset:16384\n\t"
      "ds_read_b64 v[238:239], v246 offset:20480\n\t"
      "ds_read_b64 v[240:241], v246 offset:24576\n\t"
      "ds_read_b64 v[242:243], v246 offset:28672\n\t"
      "s_waitcnt vmcnt(0)\n\t"
      "v_lshlrev_b32_e32 v113, 16, v112\n\t"
      "v_and_b32_e32 v114, 0xffff0000, v112\n\t"
      "v_add_f32_e32 v113, v249, v113\n\t"
      "v_add_f32_e32 v114, v249, v114\n\t"
      "s_waitcnt lgkmcnt(4)\n\t"
      "v_add_f32_e32 v228, v228, v230\n\t"
      "v_add_f32_e32 v232, v232, v234\n\t"
      "v_add_f32_e32 v229, v229, v231\n\t"
      "v_add_f32_e32 v233, v233, v235\n\t"
      "s_waitcnt lgkmcnt(0)\n\t"
      "v_add_f32_e32 v236, v236, v238\n\t"
      "v_add_f32_e32 v240, v240, v242\n\t"
      "v_add_f32_e32 v237, v237, v239\n\t"
      "v_add_f32_e32 v241, v241, v243\n\t"
      "v_add_f32_e32 v228, v228, v232\n\t"
      "v_add_f32_e32 v236, v236, v240\n\t"
      "v_add_f32_e32 v229, v229, v233\n\t"
      "v_add_f32_e32 v237, v237, v241\n\t"
      "v_add_f32_e32 v228, v228, v113\n\t"
      "v_add_f32_e32 v229, v229, v114\n\t"
      "v_add_f32_e32 v228, v228, v236\n\t"
      "v_add_f32_e32 v229, v229, v237\n\t"
      "v_cvt_pk_bf16_f32 v232, v228, v229\n\t"
      "s_mov_b32 vcc_lo, 0xbbbbbbbb\n\t"
      "s_mov_b32 vcc_hi, 0xbbbbbbbb\n\t"
      "v_cndmask_b32_dpp v116, v232, v116, vcc quad_perm:[0,0,0,0] row_mask:0xf bank_mask:0xf\n\t"
      "v_cndmask_b32_dpp v117, v232, v117, vcc quad_perm:[1,1,1,1] row_mask:0xf bank_mask:0xf\n\t"
      "v_cndmask_b32_dpp v118, v232, v118, vcc quad_perm:[2,2,2,2] row_mask:0xf bank_mask:0xf\n\t"
      "v_cndmask_b32_dpp v119, v232, v119, vcc quad_perm:[3,3,3,3] row_mask:0xf bank_mask:0xf\n\t"
      "s_mov_b64 exec, s[82:83]\n\t"
      "global_store_dwordx4 v247, v[116:119], s[62:63] offset:128 sc1\n\t"
      "s_mov_b64 exec, -1\n\t"
      "s_endpgm\n\t"
      "LO0R_%=:\n\t"
      "s_mov_b32 s80, 0\n\t"
      "LO0L_%=:\n\t"
      "s_add_u32 s80, s80, 1\n\t"
      "s_cmp_lt_u32 s80, 262144\n\t"
      "s_cbranch_scc0 LO0X_%=\n\t"
      "s_waitcnt vmcnt(0)\n\t"
      "s_sleep 1\n\t"
      "global_load_dwordx4 v[128:131], v244, s[42:43] offset:-4096 sc1\n\t"
      "global_load_dwordx4 v[132:135], v244, s[42:43] offset:-3072 sc1\n\t"
      "global_load_dwordx4 v[136:139], v244, s[42:43] offset:-2048 sc1\n\t"
      "global_load_dwordx4 v[140:143], v244, s[42:43] offset:-1024 sc1\n\t"
      "global_load_dwordx4 v[144:147], v244, s[42:43] sc1\n\t"
      "global_load_dwordx4 v[148:151], v244, s[42:43] offset:1024 sc1\n\t"
      "global_load_dwordx4 v[152:155], v244, s[42:43] offset:2048 sc1\n\t"
      "global_load_dwordx4 v[156:159], v244, s[42:43] offset:3072 sc1\n\t"
      "global_load_dwordx4 v[160:163], v244, s[44:45] offset:-4096 sc1\n\t"
      "global_load_dwordx4 v[164:167], v244, s[44:45] offset:-3072 sc1\n\t"
      "global_load_dwordx4 v[168:171], v244, s[44:45] offset:-2048 sc1\n\t"
      "global_load_dwordx4 v[172:175], v244, s[44:45] offset:-1024 sc1\n\t"
      "global_load_dwordx4 v[176:179], v244, s[44:45] sc1\n\t"
      "global_load_dwordx4 v[180:183], v244, s[44:45] offset:1024 sc1\n\t"
      "global_load_dwordx4 v[184:187], v244, s[44:45] offset:2048 sc1\n\t"
      "global_load_dwordx4 v[188:191], v244, s[44:45] offset:3072 sc1\n\t"
      "s_waitcnt vmcnt(0)\n\t"
      "v_max3_u32 v232, v128, v129, v130\n\t"
      "v_max3_u32 v232, v232, v131, v132\n\t"
      "v_max3_u32 v232, v232, v133, v134\n\t"
      "v_max_u32_e32 v232, v232, v135\n\t"
      "v_cmp_eq_u32_e32 vcc, -1, v232\n\t"
      "s_cbranch_vccnz LO0L_%=\n\t"
      "LO0X_%=:\n\t"
      "s_branch LO0B_%=\n\t"
      "LO2R_%=:\n\t"
      "s_mov_b32 s80, 0\n\t"
      "LO2L_%=:\n\t"
      "s_add_u32 s80, s80, 1\n\t"
      "s_cmp_lt_u32 s80, 262144\n\t"
      "s_cbranch_scc0 LO2X_%=\n\t"
      "s_waitcnt vmcnt(0)\n\t"
      "s_sleep 1\n\t"
      "global_load_dwordx4 v[136:139], v244, s[42:43] offset:-2048 sc1\n\t"
      "global_load_dwordx4 v[140:143], v244, s[42:43] offset:-1024 sc1\n\t"
      "global_load_dwordx4 v[144:147], v244, s[42:43] sc1\n\t"
      "global_load_dwordx4 v[148:151], v244, s[42:43] offset:1024 sc1\n\t"
      "global_load_dwordx4 v[152:155], v244, s[42:43] offset:2048 sc1\n\t"
      "global_load_dwordx4 v[156:159], v244, s[42:43] offset:3072 sc1\n\t"
      "global_load_dwordx4 v[160:163], v244, s[44:45] offset:-4096 sc1\n\t"
      "global_load_dwordx4 v[164:167], v244, s[44:45] offset:-3072 sc1\n\t"
      "global_load_dwordx4 v[168:171], v244, s[44:45] offset:-2048 sc1\n\t"
      "global_load_dwordx4 v[172:175], v244, s[44:45] offset:-1024 sc1\n\t"
      "global_load_dwordx4 v[176:179], v244, s[44:45] sc1\n\t"
      "global_load_dwordx4 v[180:183], v244, s[44:45] offset:1024 sc1\n\t"
      "global_load_dwordx4 v[184:187], v244, s[44:45] offset:2048 sc1\n\t"
      "global_load_dwordx4 v[188:191], v244, s[44:45] offset:3072 sc1\n\t"
      "s_waitcnt vmcnt(0)\n\t"
      "v_max3_u32 v232, v136, v137, v138\n\t"
      "v_max3_u32 v232, v232, v139, v140\n\t"
      "v_max3_u32 v232, v232, v141, v142\n\t"
      "v_max_u32_e32 v232, v232, v143\n\t"
      "v_cmp_eq_u32_e32 vcc, -1, v232\n\t"
      "s_cbranch_vccnz LO2L_%=\n\t"
      "LO2X_%=:\n\t"
      "s_branch LO2B_%=\n\t"
      "LO4R_%=:\n\t"
      "s_mov_b32 s80, 0\n\t"
      "LO4L_%=:\n\t"
      "s_add_u32 s80, s80, 1\n\t"
      "s_cmp_lt_u32 s80, 262144\n\t"
      "s_cbranch_scc0 LO4X_%=\n\t"
      "s_waitcnt vmcnt(0)\n\t"
      "s_sleep 1\n\t"
      "global_load_dwordx4 v[144:147], v244, s[42:43] sc1\n\t"
      "global_load_dwordx4 v[148:151], v244, s[42:43] offset:1024 sc1\n\t"
      "global_load_dwordx4 v[152:155], v244, s[42:43] offset:2048 sc1\n\t"
      "global_load_dwordx4 v[156:159], v244, s[42:43] offset:3072 sc1\n\t"
      "global_load_dwordx4 v[160:163], v244, s[44:45] offset:-4096 sc1\n\t"
      "global_load_dwordx4 v[164:167], v244, s[44:45] offset:-3072 sc1\n\t"
      "global_load_dwordx4 v[168:171], v244, s[44:45] offset:-2048 sc1\n\t"
      "global_load_dwordx4 v[172:175], v244, s[44:45] offset:-1024 sc1\n\t"
      "global_load_dwordx4 v[176:179], v244, s[44:45] sc1\n\t"
      "global_load_dwordx4 v[180:183], v244, s[44:45] offset:1024 sc1\n\t"
      "global_load_dwordx4 v[184:187], v244, s[44:45] offset:2048 sc1\n\t"
      "global_load_dwordx4 v[188:191], v244, s[44:45] offset:3072 sc1\n\t"
      "s_waitcnt vmcnt(0)\n\t"
      "v_max3_u32 v232, v144, v145, v146\n\t"
      "v_max3_u32 v232, v232, v147, v148\n\t"
      "v_max3_u32 v232, v232, v149, v150\n\t"
      "v_max_u32_e32 v232, v232, v151\n\t"
      "v_cmp_eq_u32_e32 vcc, -1, v232\n\t"
      "s_cbranch_vccnz LO4L_%=\n\t"
      "LO4X_%=:\n\t"
      "s_branch LO4B_%=\n\t"
      "LO6R_%=:\n\t"
      "s_mov_b32 s80, 0\n\t"
      "LO6L_%=:\n\t"
      "s_add_u32 s80, s80, 1\n\t"
      "s_cmp_lt_u32 s80, 262144\n\t"
      "s_cbranch_scc0 LO6X_%=\n\t"
      "s_waitcnt vmcnt(0)\n\t"
      "s_sleep 1\n\t"
      "global_load_dwordx4 v[152:155], v244, s[42:43] offset:2048 sc1\n\t"
      "global_load_dwordx4 v[156:159], v244, s[42:43] offset:3072 sc1\n\t"
      "global_load_dwordx4 v[160:163], v244, s[44:45] offset:-4096 sc1\n\t"
      "global_load_dwordx4 v[164:167], v244, s[44:45] offset:-3072 sc1\n\t"
      "global_load_dwordx4 v[168:171], v244, s[44:45] offset:-2048 sc1\n\t"
      "global_load_dwordx4 v[172:175], v244, s[44:45] offset:-1024 sc1\n\t"
      "global_load_dwordx4 v[176:179], v244, s[44:45] sc1\n\t"
      "global_load_dwordx4 v[180:183], v244, s[44:45] offset:1024 sc1\n\t"
      "global_load_dwordx4 v[184:187], v244, s[44:45] offset:2048 sc1\n\t"
      "global_load_dwordx4 v[188:191], v244, s[44:45] offset:3072 sc1\n\t"
      "s_waitcnt vmcnt(0)\n\t"
      "v_max3_u32 v232, v152, v153, v154\n\t"
      "v_max3_u32 v232, v232, v155, v156\n\t"
      "v_max3_u32 v232, v232, v157, v158\n\t"
      "v_max_u32_e32 v232, v232, v159\n\t"
      "v_cmp_eq_u32_e32 vcc, -1, v232\n\t"
      "s_cbranch_vccnz LO6L_%=\n\t"
      "LO6X_%=:\n\t"
      "s_branch LO6B_%=\n\t"
      "LO8R_%=:\n\t"
      "s_mov_b32 s80, 0\n\t"
      "LO8L_%=:\n\t"
      "s_add_u32 s80, s80, 1\n\t"
      "s_cmp_lt_u32 s80, 262144\n\t"
      "s_cbranch_scc0 LO8X_%=\n\t"
      "s_waitcnt vmcnt(0)\n\t"
      "s_sleep 1\n\t"
      "global_load_dwordx4 v[160:163], v244, s[44:45] offset:-4096 sc1\n\t"
      "global_load_dwordx4 v[164:167], v244, s[44:45] offset:-3072 sc1\n\t"
      "global_load_dwordx4 v[168:171], v244, s[44:45] offset:-2048 sc1\n\t"
      "global_load_dwordx4 v[172:175], v244, s[44:45] offset:-1024 sc1\n\t"
      "global_load_dwordx4 v[176:179], v244, s[44:45] sc1\n\t"
      "global_load_dwordx4 v[180:183], v244, s[44:45] offset:1024 sc1\n\t"
      "global_load_dwordx4 v[184:187], v244, s[44:45] offset:2048 sc1\n\t"
      "global_load_dwordx4 v[188:191], v244, s[44:45] offset:3072 sc1\n\t"
      "s_waitcnt vmcnt(0)\n\t"
      "v_max3_u32 v232, v160, v161, v162\n\t"
      "v_max3_u32 v232, v232, v163, v164\n\t"
      "v_max3_u32 v232, v232, v165, v166\n\t"
      "v_max_u32_e32 v232, v232, v167\n\t"
      "v_cmp_eq_u32_e32 vcc, -1, v232\n\t"
      "s_cbranch_vccnz LO8L_%=\n\t"
      "LO8X_%=:\n\t"
      "s_branch LO8B_%=\n\t"
      "LO10R_%=:\n\t"
      "s_mov_b32 s80, 0\n\t"
      "LO10L_%=:\n\t"
      "s_add_u32 s80, s80, 1\n\t"
      "s_cmp_lt_u32 s80, 262144\n\t"
      "s_cbranch_scc0 LO10X_%=\n\t"
      "s_waitcnt vmcnt(0)\n\t"
      "s_sleep 1\n\t"
      "global_load_dwordx4 v[168:171], v244, s[44:45] offset:-2048 sc1\n\t"
      "global_load_dwordx4 v[172:175], v244, s[44:45] offset:-1024 sc1\n\t"
      "global_load_dwordx4 v[176:179], v244, s[44:45] sc1\n\t"
      "global_load_dwordx4 v[180:183], v244, s[44:45] offset:1024 sc1\n\t"
      "global_load_dwordx4 v[184:187], v244, s[44:45] offset:2048 sc1\n\t"
      "global_load_dwordx4 v[188:191], v244, s[44:45] offset:3072 sc1\n\t"
      "s_waitcnt vmcnt(0)\n\t"
      "v_max3_u32 v232, v168, v169, v170\n\t"
      "v_max3_u32 v232, v232, v171, v172\n\t"
      "v_max3_u32 v232, v232, v173, v174\n\t"
      "v_max_u32_e32 v232, v232, v175\n\t"
      "v_cmp_eq_u32_e32 vcc, -1, v232\n\t"
      "s_cbranch_vccnz LO10L_%=\n\t"
      "LO10X_%=:\n\t"
      "s_branch LO10B_%=\n\t"
      "LO12R_%=:\n\t"
      "s_mov_b32 s80, 0\n\t"
      "LO12L_%=:\n\t"
      "s_add_u32 s80, s80, 1\n\t"
      "s_cmp_lt_u32 s80, 262144\n\t"
      "s_cbranch_scc0 LO12X_%=\n\t"
      "s_waitcnt vmcnt(0)\n\t"
      "s_sleep 1\n\t"
      "global_load_dwordx4 v[176:179], v244, s[44:45] sc1\n\t"
      "global_load_dwordx4 v[180:183], v244, s[44:45] offset:1024 sc1\n\t"
      "global_load_dwordx4 v[184:187], v244, s[44:45] offset:2048 sc1\n\t"
      "global_load_dwordx4 v[188:191], v244, s[44:45] offset:3072 sc1\n\t"
      "s_waitcnt vmcnt(0)\n\t"
      "v_max3_u32 v232, v176, v177, v178\n\t"
      "v_max3_u32 v232, v232, v179, v180\n\t"
      "v_max3_u32 v232, v232, v181, v182\n\t"
      "v_max_u32_e32 v232, v232, v183\n\t"
      "v_cmp_eq_u32_e32 vcc, -1, v232\n\t"
      "s_cbranch_vccnz LO12L_%=\n\t"
      "LO12X_%=:\n\t"
      "s_branch LO12B_%=\n\t"
      "LO14R_%=:\n\t"
      "s_mov_b32 s80, 0\n\t"
      "LO14L_%=:\n\t"
      "s_add_u32 s80, s80, 1\n\t"
      "s_cmp_lt_u32 s80, 262144\n\t"
      "s_cbranch_scc0 LO14X_%=\n\t"
      "s_waitcnt vmcnt(0)\n\t"
      "s_sleep 1\n\t"
      "global_load_dwordx4 v[184:187], v244, s[44:45] offset:2048 sc1\n\t"
      "global_load_dwordx4 v[188:191], v244, s[44:45] offset:3072 sc1\n\t"
      "s_waitcnt vmcnt(0)\n\t"
      "v_max3_u32 v232, v184, v185, v186\n\t"
      "v_max3_u32 v232, v232, v187, v188\n\t"
      "v_max3_u32 v232, v232, v189, v190\n\t"
      "v_max_u32_e32 v232, v232, v191\n\t"
      "v_cmp_eq_u32_e32 vcc, -1, v232\n\t"
      "s_cbranch_vccnz LO14L_%=\n\t"
      "LO14X_%=:\n\t"
      "s_branch LO14B_%=\n\t"
      "LSR_%=:\n\t"
      "s_mov_b32 s80, 0\n\t"
      "LSL_%=:\n\t"
      "s_add_u32 s80, s80, 1\n\t"
      "s_cmp_lt_u32 s80, 262144\n\t"
      "s_cbranch_scc0 LSX_%=\n\t"
      "s_sleep 1\n\t"
      "global_load_dwordx2 v[148:149], v231, s[46:47] sc1\n\t"
      "global_load_dwordx2 v[150:151], v231, s[46:47] offset:384 sc1\n\t"
      "s_waitcnt vmcnt(0)\n\t"
      "v_max3_u32 v232, v148, v149, v150\n\t"
      "v_max_u32_e32 v232, v232, v151\n\t"
      "v_cmp_eq_u32_e32 vcc, -1, v232\n\t"
      "s_cbranch_vccnz LSL_%=\n\t"
      "LSX_%=:\n\t"
      "s_branch LSB_%=\n\t"
      "LF1R_%=:\n\t"
      "s_mov_b32 s80, 0\n\t"
      "LF1L_%=:\n\t"
      "s_add_u32 s80, s80, 1\n\t"
      "s_cmp_lt_u32 s80, 262144\n\t"
      "s_cbranch_scc0 LF1X_%=\n\t"
      "s_sleep 2\n\t"
      "global_load_dwordx2 v[138:139], v136, s[60:61] sc1\n\t"
      "s_waitcnt vmcnt(0)\n\t"
      "v_cmp_ne_u64_e32 vcc, s[22:23], v[138:139]\n\t"
      "s_cbranch_vccnz LF1L_%=\n\t"
      "LF1X_%=:\n\t"
      "s_branch LF1B_%=\n\t"
      "LF4R_%=:\n\t"
      "s_mov_b32 s80, 0\n\t"
      "LF4L_%=:\n\t"
      "s_add_u32 s80, s80, 1\n\t"
      "s_cmp_lt_u32 s80, 262144\n\t"
      "s_cbranch_scc0 LF4X_%=\n\t"
      "s_sleep 2\n\t"
      "global_load_dwordx2 v[138:139], v137, s[60:61] sc1\n\t"
      "s_waitcnt vmcnt(0)\n\t"
      "v_cmp_ne_u64_e32 vcc, s[22:23], v[138:139]\n\t"
      "s_cbranch_vccnz LF4L_%=\n\t"
      "LF4X_%=:\n\t"
      "s_branch LF4B_%=\n\t"
      "L4_%=:\n\t"
      "s_waitcnt vmcnt(0)\n\t"
      "s_barrier\n\t"
      "s_barrier\n\t"
      "s_lshl2_add_u32 s72, s31, s24\n\t"
      "s_sub_u32 s72, s72, 56\n\t"
      "s_mov_b32 m0, 64512\n\t"
      "s_cmp_ge_u32 s72, 64\n\t"
      "s_cselect_b32 s73, 8, 0\n\t"
      "s_sub_u32 s73, s72, s73\n\t"
      "s_and_b32 s73, s73, 0x38\n\t"
      "s_add_u32 s72, s72, s73\n\t"
      "s_add_u32 s72, s72, 8\n\t"
      "s_mul_i32 s72, s72, 57344\n\t"
      "s_and_b32 s73, s2, 7\n\t"
      "s_mul_i32 s73, s73, 448\n\t"
      "s_and_b32 s73, s73, 0xffffff80\n\t"
      "s_add_u32 s72, s72, s73\n\t"
      "v_lshrrev_b32_e32 v141, 2, v192\n\t"
      "v_mul_u32_u24_e32 v141, 3584, v141\n\t"
      "v_lshl_add_u32 v141, v196, 7, v141\n\t"
      "v_add_u32_e32 v141, s72, v141\n\t"
      "global_load_lds_dword v141, s[10:11]\n\t"
      "s_barrier\n\t"
      "s_cmp_lg_u32 s24, 3\n\t"
      "s_cbranch_scc1 SK12_%=\n\t"
      "global_load_dwordx2 v[138:139], v137, s[60:61] sc1\n\t"
      "SK12_%=:\n\t"
      "s_cmp_lg_u32 s24, 3\n\t"
      "s_cbranch_scc1 LF4B_%=\n\t"
      "s_waitcnt vmcnt(0)\n\t"
      "v_cmp_ne_u64_e32 vcc, s[22:23], v[138:139]\n\t"
      "s_cbranch_vccnz LF4R_%=\n\t"
      "LF4B_%=:\n\t"
      "s_barrier\n\t"
      "s_waitcnt vmcnt(0)\n\t"
      "s_barrier\n\t"
      "s_branch G4_%=\n\t"
      "LF2R_%=:\n\t"
      "s_mov_b32 s80, 0\n\t"
      "LF2L_%=:\n\t"
      "s_add_u32 s80, s80, 1\n\t"
      "s_cmp_lt_u32 s80, 262144\n\t"
      "s_cbranch_scc0 LF2X_%=\n\t"
      "s_sleep 2\n\t"
      "global_load_dwordx2 v[138:139], v137, s[60:61] sc1\n\t"
      "s_waitcnt vmcnt(0)\n\t"
      "v_cmp_ne_u64_e32 vcc, s[22:23], v[138:139]\n\t"
      "s_cbranch_vccnz LF2L_%=\n\t"
      "LF2X_%=:\n\t"
      "s_branch LF2B_%=\n\t"
      "LF3R_%=:\n\t"
      "s_mov_b32 s80, 0\n\t"
      "LF3L_%=:\n\t"
      "s_add_u32 s80, s80, 1\n\t"
      "s_cmp_lt_u32 s80, 262144\n\t"
      "s_cbranch_scc0 LF3X_%=\n\t"
      "s_sleep 2\n\t"
      "global_load_dwordx2 v[138:139], v143, s[60:61] sc1\n\t"
      "s_waitcnt vmcnt(0)\n\t"
      "v_cmp_ne_u64_e32 vcc, s[22:23], v[138:139]\n\t"
      "s_cbranch_vccnz LF3L_%=\n\t"
      "LF3X_%=:\n\t"
      "s_branch LF3B_%=\n\t"
      "LF7R_%=:\n\t"
      "s_mov_b32 s80, 0\n\t"
      "LF7L_%=:\n\t"
      "s_add_u32 s80, s80, 1\n\t"
      "s_cmp_lt_u32 s80, 262144\n\t"
      "s_cbranch_scc0 LF7X_%=\n\t"
      "s_sleep 4\n\t"
      "global_load_dwordx2 v[138:139], v136, s[60:61] sc1\n\t"
      "s_waitcnt vmcnt(0)\n\t"
      "v_cmp_ne_u64_e32 vcc, s[22:23], v[138:139]\n\t"
      "s_cbranch_vccnz LF7L_%=\n\t"
      "LF7X_%=:\n\t"
      "s_branch LF7B_%=\n\t"
      "SPARE_%=:\n\t"
      "v_mov_b32_e32 v132, -1\n\t"
      "v_mov_b32_e32 v133, -1\n\t"
      "v_mov_b32_e32 v134, -1\n\t"
      "v_mov_b32_e32 v135, -1\n\t"
      "s_and_b32 s34, s25, 3\n\t"
      "s_lshl_b32 s34, s34, 15\n\t"
      "s_mul_i32 s35, s24, 917504\n\t"
      "s_add_u32 s34, s34, s35\n\t"
      "s_and_b32 s35, s2, 7\n\t"
      "s_lshl_b32 s35, s35, 9\n\t"
      "s_add_u32 s34, s34, s35\n\t"
      "v_and_b32_e32 v141, 0x1c0, v244\n\t"
      "v_lshlrev_b32_e32 v141, 6, v141\n\t"
      "v_and_b32_e32 v142, 0x30, v244\n\t"
      "v_lshl_add_u32 v141, v142, 3, v141\n\t"
      "v_add_u32_e32 v141, s34, v141\n\t"
      "s_mov_b32 m0, 64512\n\t"
      "s_lshr_b32 s34, s24, 1\n\t"
      "s_lshl_b32 s34, s34, 5\n\t"
      "s_add_u32 s34, s34, s25\n\t"
      "s_and_b32 s35, s24, 1\n\t"
      "s_lshl1_add_u32 s34, s34, s35\n\t"
      "s_lshl_b32 s34, s34, 10\n\t"
      "s_add_u32 s34, s34, 8388608\n\t"
      "s_add_u32 s40, s20, s34\n\t"
      "s_addc_u32 s41, s21, 0\n\t"
      "global_store_dwordx4 v244, v[132:135], s[40:41] sc1\n\t"
      "s_add_u32 s40, s40, 262144\n\t"
      "s_addc_u32 s41, s41, 0\n\t"
      "global_store_dwordx4 v244, v[132:135], s[40:41] sc1\n\t"
      "s_add_u32 s40, s40, 262144\n\t"
      "s_addc_u32 s41, s41, 0\n\t"
      "global_store_dwordx4 v244, v[132:135], s[40:41] sc1\n\t"
      "s_add_u32 s40, s40, 262144\n\t"
      "s_addc_u32 s41, s41, 0\n\t"
      "global_store_dwordx4 v244, v[132:135], s[40:41] sc1\n\t"
      "s_add_u32 s40, s40, 262144\n\t"
      "s_addc_u32 s41, s41, 0\n\t"
      "global_store_dwordx4 v244, v[132:135], s[40:41] sc1\n\t"
      "s_add_u32 s40, s40, 262144\n\t"
      "s_addc_u32 s41, s41, 0\n\t"
      "global_store_dwordx4 v244, v[132:135], s[40:41] sc1\n\t"
      "s_add_u32 s40, s40, 262144\n\t"
      "s_addc_u32 s41, s41, 0\n\t"
      "global_store_dwordx4 v244, v[132:135], s[40:41] sc1\n\t"
      "s_add_u32 s40, s40, 262144\n\t"
      "s_addc_u32 s41, s41, 0\n\t"
      "s_lshl_b32 s34, s25, 7\n\t"
      "s_add_u32 s34, s34, 16384\n\t"
      "v_mov_b32_e32 v131, s34\n\t"
      "s_waitcnt vmcnt(0)\n\t"
      "s_barrier\n\t"
      "s_cmp_lg_u32 s24, 0\n\t"
      "s_cbranch_scc1 SP0_%=\n\t"
      "s_mov_b64 exec, 1\n\t"
      "global_store_dwordx2 v131, v[128:129], s[60:61] sc1\n\t"
      "s_mov_b64 exec, -1\n\t"
      "SP0_%=:\n\t"
      "s_lshl3_add_u32 s34, s25, s24\n\t"
      "s_mul_i32 s34, s34, 7168\n\t"
      "s_add_u32 s34, s34, 10227712\n\t"
      "s_add_u32 s40, s20, s34\n\t"
      "s_addc_u32 s41, s21, 0\n\t"
      "global_store_dwordx4 v244, v[132:135], s[40:41] offset:-4096 sc1\n\t"
      "global_store_dwordx4 v244, v[132:135], s[40:41] offset:-3072 sc1\n\t"
      "global_store_dwordx4 v244, v[132:135], s[40:41] offset:-2048 sc1\n\t"
      "global_store_dwordx4 v244, v[132:135], s[40:41] offset:-1024 sc1\n\t"
      "global_store_dwordx4 v244, v[132:135], s[40:41] sc1\n\t"
      "global_store_dwordx4 v244, v[132:135], s[40:41] offset:1024 sc1\n\t"
      "global_store_dwordx4 v244, v[132:135], s[40:41] offset:2048 sc1\n\t"
      "s_lshl_b32 s34, s25, 7\n\t"
      "s_add_u32 s34, s34, 24576\n\t"
      "v_mov_b32_e32 v131, s34\n\t"
      "s_waitcnt vmcnt(0)\n\t"
      "s_barrier\n\t"
      "s_cmp_lg_u32 s24, 0\n\t"
      "s_cbranch_scc1 SP1_%=\n\t"
      "s_mov_b64 exec, 1\n\t"
      "global_store_dwordx2 v131, v[128:129], s[60:61] sc1\n\t"
      "s_mov_b64 exec, -1\n\t"
      "SP1_%=:\n\t"
      "s_and_b32 s34, s25, 3\n\t"
      "s_lshl3_add_u32 s34, s34, s24\n\t"
      "s_cmp_lg_u32 s34, 27\n\t"
      "s_cbranch_scc1 SP3_%=\n\t"
      "v_and_b32_e32 v136, 0x1f0, v244\n\t"
      "v_lshlrev_b32_e32 v131, 6, v136\n\t"
      "v_lshlrev_b32_e32 v136, 3, v136\n\t"
      "v_add_u32_e32 v136, 24576, v136\n\t"
      "s_and_b32 s34, s2, 7\n\t"
      "s_lshl_b32 s34, s34, 7\n\t"
      "s_add_u32 s34, s34, 32768\n\t"
      "v_add_u32_e32 v131, s34, v131\n\t"
      "s_cmp_lg_u32 s24, 3\n\t"
      "s_cbranch_scc1 SK15_%=\n\t"
      "global_load_dwordx2 v[138:139], v136, s[60:61] sc1\n\t"
      "SK15_%=:\n\t"
      "s_cmp_lg_u32 s24, 3\n\t"
      "s_cbranch_scc1 LF7B_%=\n\t"
      "s_waitcnt vmcnt(0)\n\t"
      "v_cmp_ne_u64_e32 vcc, s[22:23], v[138:139]\n\t"
      "s_cbranch_vccnz LF7R_%=\n\t"
      "LF7B_%=:\n\t"
      "global_store_dwordx2 v131, v[128:129], s[60:61] sc1\n\t"
      "SP3_%=:\n\t"
      "s_barrier\n\t"
      "s_lshr_b32 s35, s2, 6\n\t"
      "s_lshl2_add_u32 s31, s26, s35\n\t"
      "s_and_b32 s35, s2, 7\n\t"
      "s_mul_i32 s34, s35, 14\n\t"
      "s_lshl4_add_u32 s35, s35, -14\n\t"
      "s_cmp_lt_u32 s31, 14\n\t"
      "s_cselect_b32 s34, s34, s35\n\t"
      "s_cselect_b32 s36, 1, 0\n\t"
      "s_or_b32 s27, s27, s36\n\t"
      "s_add_u32 s34, s34, s31\n\t"
      "s_lshr_b32 s29, s34, 2\n\t"
      "s_and_b32 s30, s34, 3\n\t"
      "s_mul_i32 s30, s30, 7\n\t"
      "v_and_b32_e32 v199, 60, v192\n\t"
      "v_and_b32_e32 v200, 24, v192\n\t"
      "v_lshlrev_b32_e32 v202, 3, v196\n\t"
      "v_lshl_or_b32 v202, v200, 2, v202\n\t"
      "v_lshl_or_b32 v202, v195, 7, v202\n\t"
      "v_lshl_or_b32 v202, v193, 10, v202\n\t"
      "v_add_u32_e32 v224, s39, v202\n\t"
      "v_xor_b32_e32 v225, 32, v224\n\t"
      "v_xor_b32_e32 v226, 64, v224\n\t"
      "v_xor_b32_e32 v227, 96, v224\n\t"
      "s_lshl_b32 s34, s24, 12\n\t"
      "s_add_u32 s34, s34, 98304\n\t"
      "v_add_u32_e32 v245, s34, v244\n\t"
      "v_lshlrev_b32_e32 v199, 2, v199\n\t"
      "s_lshl_b32 s34, s24, 8\n\t"
      "v_lshl_add_u32 v247, v192, 2, s34\n\t"
      "v_and_b32_e32 v200, 2, v192\n\t"
      "v_mul_u32_u24_e32 v202, 576, v200\n\t"
      "v_xor_b32_e32 v199, v202, v199\n\t"
      "v_and_b32_e32 v202, 1, v192\n\t"
      "v_lshl_or_b32 v199, v202, 3, v199\n\t"
      "s_and_b32 s35, s34, 0x400\n\t"
      "s_add_u32 s34, s34, s35\n\t"
      "s_add_u32 s34, s34, 98304\n\t"
      "v_add_u32_e32 v246, s34, v199\n\t"
      "v_lshlrev_b32_e32 v199, 2, v202\n\t"
      "v_lshl_or_b32 v199, v200, 4, v199\n\t"
      "s_min_u32 s34, s29, 27\n\t"
      "s_lshl_b32 s34, s34, 7\n\t"
      "s_lshr_b32 s35, s24, 2\n\t"
      "s_lshl_b32 s35, s35, 6\n\t"
      "s_add_u32 s34, s34, s35\n\t"
      "s_lshr_b32 s35, s39, 9\n\t"
      "s_add_u32 s34, s34, s35\n\t"
      "v_add_u32_e32 v248, s34, v199\n\t"
      "s_add_u32 s48, s20, 8388608\n\t"
      "s_addc_u32 s49, s21, 0\n\t"
      "s_cmp_ge_u32 s31, 30\n\t"
      "s_cbranch_scc1 BYE_%=\n\t"
      "s_setprio 0\n\t"
      "s_waitcnt vmcnt(0)\n\t"
      "s_barrier\n\t"
      "s_branch G4_%=\n\t"
      "BYE_%=:\n\t"
      "s_endpgm\n\t"
      "LA30R_%=:\n\t"
      "s_mov_b32 s80, 0\n\t"
      "LA30L_%=:\n\t"
      "s_add_u32 s80, s80, 1\n\t"
      "s_cmp_lt_u32 s80, 262144\n\t"
      "s_cbranch_scc0 LA30X_%=\n\t"
      "s_waitcnt vmcnt(0)\n\t"
      "s_sleep 1\n\t"
      "global_load_dwordx4 v[128:131], v250, s[64:65] offset:-4096 sc1\n\t"
      "global_load_dwordx4 v[132:135], v250, s[64:65] offset:-3072 sc1\n\t"
      "global_load_dwordx4 v[136:139], v250, s[64:65] offset:-2048 sc1\n\t"
      "global_load_dwordx4 v[140:143], v250, s[64:65] offset:-1024 sc1\n\t"
      "global_load_dwordx4 v[144:147], v250, s[64:65] sc1\n\t"
      "global_load_dwordx4 v[148:151], v250, s[64:65] offset:1024 sc1\n\t"
      "global_load_dwordx4 v[152:155], v250, s[64:65] offset:2048 sc1\n\t"
      "global_load_dwordx4 v[156:159], v250, s[64:65] offset:3072 sc1\n\t"
      "s_waitcnt vmcnt(0)\n\t"
      "v_max3_u32 v251, v128, v129, v130\n\t"
      "v_max3_u32 v251, v251, v131, v132\n\t"
      "v_max3_u32 v251, v251, v133, v134\n\t"
      "v_max3_u32 v251, v251, v135, v136\n\t"
      "v_max3_u32 v251, v251, v137, v138\n\t"
      "v_max3_u32 v251, v251, v139, v140\n\t"
      "v_max3_u32 v251, v251, v141, v142\n\t"
      "v_max3_u32 v251, v251, v143, v144\n\t"
      "v_max3_u32 v251, v251, v145, v146\n\t"
      "v_max3_u32 v251, v251, v147, v148\n\t"
      "v_max3_u32 v251, v251, v149, v150\n\t"
      "v_max3_u32 v251, v251, v151, v152\n\t"
      "v_max3_u32 v251, v251, v153, v154\n\t"
      "v_max3_u32 v251, v251, v155, v156\n\t"
      "v_max3_u32 v251, v251, v157, v158\n\t"
      "v_max_u32_e32 v251, v251, v159\n\t"
      "v_cmp_eq_u32_e32 vcc, -1, v251\n\t"
      "s_cbranch_vccnz LA30L_%=\n\t"
      "LA30X_%=:\n\t"
      "s_branch LA30B_%=\n\t"
      "LA31R_%=:\n\t"
      "s_mov_b32 s80, 0\n\t"
      "LA31L_%=:\n\t"
      "s_add_u32 s80, s80, 1\n\t"
      "s_cmp_lt_u32 s80, 262144\n\t"
      "s_cbranch_scc0 LA31X_%=\n\t"
      "s_waitcnt vmcnt(0)\n\t"
      "s_sleep 1\n\t"
      "global_load_dwordx4 v[160:163], v250, s[66:67] offset:-4096 sc1\n\t"
      "global_load_dwordx4 v[164:167], v250, s[66:67] offset:-3072 sc1\n\t"
      "global_load_dwordx4 v[168:171], v250, s[66:67] offset:-2048 sc1\n\t"
      "global_load_dwordx4 v[172:175], v250, s[66:67] offset:-1024 sc1\n\t"
      "global_load_dwordx4 v[176:179], v250, s[66:67] sc1\n\t"
      "global_load_dwordx4 v[180:183], v250, s[66:67] offset:1024 sc1\n\t"
      "global_load_dwordx4 v[184:187], v250, s[66:67] offset:2048 sc1\n\t"
      "global_load_dwordx4 v[188:191], v250, s[66:67] offset:3072 sc1\n\t"
      "s_waitcnt vmcnt(0)\n\t"
      "v_max3_u32 v251, v160, v161, v162\n\t"
      "v_max3_u32 v251, v251, v163, v164\n\t"
      "v_max3_u32 v251, v251, v165, v166\n\t"
      "v_max3_u32 v251, v251, v167, v168\n\t"
      "v_max3_u32 v251, v251, v169, v170\n\t"
      "v_max3_u32 v251, v251, v171, v172\n\t"
      "v_max3_u32 v251, v251, v173, v174\n\t"
      "v_max3_u32 v251, v251, v175, v176\n\t"
      "v_max3_u32 v251, v251, v177, v178\n\t"
      "v_max3_u32 v251, v251, v179, v180\n\t"
      "v_max3_u32 v251, v251, v181, v182\n\t"
      "v_max3_u32 v251, v251, v183, v184\n\t"
      "v_max3_u32 v251, v251, v185, v186\n\t"
      "v_max3_u32 v251, v251, v187, v188\n\t"
      "v_max3_u32 v251, v251, v189, v190\n\t"
      "v_max_u32_e32 v251, v251, v191\n\t"
      "v_cmp_eq_u32_e32 vcc, -1, v251\n\t"
      "s_cbranch_vccnz LA31L_%=\n\t"
      "LA31X_%=:\n\t"
      "v_mfma_f32_16x16x32_bf16 v[224:227], v[0:3], v[160:163], 0\n\t"
      "v_mfma_f32_16x16x32_bf16 v[224:227], v[16:19], v[164:167], v[224:227]\n\t"
      "v_mfma_f32_16x16x32_bf16 v[224:227], v[32:35], v[168:171], v[224:227]\n\t"
      "v_mfma_f32_16x16x32_bf16 v[224:227], v[48:51], v[172:175], v[224:227]\n\t"
      "v_mfma_f32_16x16x32_bf16 v[224:227], v[64:67], v[176:179], v[224:227]\n\t"
      "v_mfma_f32_16x16x32_bf16 v[224:227], v[80:83], v[180:183], v[224:227]\n\t"
      "v_mfma_f32_16x16x32_bf16 v[224:227], v[96:99], v[184:187], v[224:227]\n\t"
      "v_mfma_f32_16x16x32_bf16 v[224:227], v[112:115], v[188:191], v[224:227]\n\t"
      "v_mfma_f32_16x16x32_bf16 v[208:211], v[4:7], v[160:163], 0\n\t"
      "v_mfma_f32_16x16x32_bf16 v[208:211], v[20:23], v[164:167], v[208:211]\n\t"
      "v_mfma_f32_16x16x32_bf16 v[208:211], v[36:39], v[168:171], v[208:211]\n\t"
      "s_nop 7\n\t"
      "s_branch LA31B_%=\n\t"
      "LA32R_%=:\n\t"
      "s_mov_b32 s80, 0\n\t"
      "LA32L_%=:\n\t"
      "s_add_u32 s80, s80, 1\n\t"
      "s_cmp_lt_u32 s80, 262144\n\t"
      "s_cbranch_scc0 LA32X_%=\n\t"
      "s_waitcnt vmcnt(0)\n\t"
      "s_sleep 1\n\t"
      "global_load_dwordx4 v[192:195], v250, s[68:69] offset:-4096 sc1\n\t"
      "global_load_dwordx4 v[196:199], v250, s[68:69] offset:-3072 sc1\n\t"
      "global_load_dwordx4 v[200:203], v250, s[68:69] offset:-2048 sc1\n\t"
      "global_load_dwordx4 v[204:207], v250, s[68:69] offset:-1024 sc1\n\t"
      "global_load_dwordx4 v[128:131], v250, s[68:69] sc1\n\t"
      "global_load_dwordx4 v[132:135], v250, s[68:69] offset:1024 sc1\n\t"
      "global_load_dwordx4 v[136:139], v250, s[68:69] offset:2048 sc1\n\t"
      "global_load_dwordx4 v[140:143], v250, s[68:69] offset:3072 sc1\n\t"
      "s_waitcnt vmcnt(0)\n\t"
      "v_max3_u32 v251, v192, v193, v194\n\t"
      "v_max3_u32 v251, v251, v195, v196\n\t"
      "v_max3_u32 v251, v251, v197, v198\n\t"
      "v_max3_u32 v251, v251, v199, v200\n\t"
      "v_max3_u32 v251, v251, v201, v202\n\t"
      "v_max3_u32 v251, v251, v203, v204\n\t"
      "v_max3_u32 v251, v251, v205, v206\n\t"
      "v_max3_u32 v251, v251, v207, v128\n\t"
      "v_max3_u32 v251, v251, v129, v130\n\t"
      "v_max3_u32 v251, v251, v131, v132\n\t"
      "v_max3_u32 v251, v251, v133, v134\n\t"
      "v_max3_u32 v251, v251, v135, v136\n\t"
      "v_max3_u32 v251, v251, v137, v138\n\t"
      "v_max3_u32 v251, v251, v139, v140\n\t"
      "v_max3_u32 v251, v251, v141, v142\n\t"
      "v_max_u32_e32 v251, v251, v143\n\t"
      "v_cmp_eq_u32_e32 vcc, -1, v251\n\t"
      "s_cbranch_vccnz LA32L_%=\n\t"
      "LA32X_%=:\n\t"
      "v_mfma_f32_16x16x32_bf16 v[220:223], v[0:3], v[192:195], 0\n\t"
      "v_mfma_f32_16x16x32_bf16 v[220:223], v[16:19], v[196:199], v[220:223]\n\t"
      "v_mfma_f32_16x16x32_bf16 v[220:223], v[32:35], v[200:203], v[220:223]\n\t"
      "v_mfma_f32_16x16x32_bf16 v[220:223], v[48:51], v[204:207], v[220:223]\n\t"
      "v_mfma_f32_16x16x32_bf16 v[220:223], v[64:67], v[128:131], v[220:223]\n\t"
      "v_mfma_f32_16x16x32_bf16 v[220:223], v[80:83], v[132:135], v[220:223]\n\t"
      "v_mfma_f32_16x16x32_bf16 v[220:223], v[96:99], v[136:139], v[220:223]\n\t"
      "v_mfma_f32_16x16x32_bf16 v[220:223], v[112:115], v[140:143], v[220:223]\n\t"
      "v_mfma_f32_16x16x32_bf16 v[224:227], v[4:7], v[192:195], 0\n\t"
      "v_mfma_f32_16x16x32_bf16 v[224:227], v[20:23], v[196:199], v[224:227]\n\t"
      "v_mfma_f32_16x16x32_bf16 v[224:227], v[36:39], v[200:203], v[224:227]\n\t"
      "s_nop 7\n\t"
      "s_branch LA32B_%=\n\t"
      "LA33R_%=:\n\t"
      "s_mov_b32 s80, 0\n\t"
      "LA33L_%=:\n\t"
      "s_add_u32 s80, s80, 1\n\t"
      "s_cmp_lt_u32 s80, 262144\n\t"
      "s_cbranch_scc0 LA33X_%=\n\t"
      "s_waitcnt vmcnt(0)\n\t"
      "s_sleep 1\n\t"
      "global_load_dwordx4 v[144:147], v250, s[70:71] offset:-4096 sc1\n\t"
      "global_load_dwordx4 v[148:151], v250, s[70:71] offset:-3072 sc1\n\t"
      "global_load_dwordx4 v[152:155], v250, s[70:71] offset:-2048 sc1\n\t"
      "global_load_dwordx4 v[156:159], v250, s[70:71] offset:-1024 sc1\n\t"
      "global_load_dwordx4 v[160:163], v250, s[70:71] sc1\n\t"
      "global_load_dwordx4 v[164:167], v250, s[70:71] offset:1024 sc1\n\t"
      "global_load_dwordx4 v[168:171], v250, s[70:71] offset:2048 sc1\n\t"
      "global_load_dwordx4 v[172:175], v250, s[70:71] offset:3072 sc1\n\t"
      "s_waitcnt vmcnt(0)\n\t"
      "v_max3_u32 v251, v144, v145, v146\n\t"
      "v_max3_u32 v251, v251, v147, v148\n\t"
      "v_max3_u32 v251, v251, v149, v150\n\t"
      "v_max3_u32 v251, v251, v151, v152\n\t"
      "v_max3_u32 v251, v251, v153, v154\n\t"
      "v_max3_u32 v251, v251, v155, v156\n\t"
      "v_max3_u32 v251, v251, v157, v158\n\t"
      "v_max3_u32 v251, v251, v159, v160\n\t"
      "v_max3_u32 v251, v251, v161, v162\n\t"
      "v_max3_u32 v251, v251, v163, v164\n\t"
      "v_max3_u32 v251, v251, v165, v166\n\t"
      "v_max3_u32 v251, v251, v167, v168\n\t"
      "v_max3_u32 v251, v251, v169, v170\n\t"
      "v_max3_u32 v251, v251, v171, v172\n\t"
      "v_max3_u32 v251, v251, v173, v174\n\t"
      "v_max_u32_e32 v251, v251, v175\n\t"
      "v_cmp_eq_u32_e32 vcc, -1, v251\n\t"
      "s_cbranch_vccnz LA33L_%=\n\t"
      "LA33X_%=:\n\t"
      "v_mfma_f32_16x16x32_bf16 v[216:219], v[0:3], v[144:147], 0\n\t"
      "v_mfma_f32_16x16x32_bf16 v[216:219], v[16:19], v[148:151], v[216:219]\n\t"
      "v_mfma_f32_16x16x32_bf16 v[216:219], v[32:35], v[152:155], v[216:219]\n\t"
      "v_mfma_f32_16x16x32_bf16 v[216:219], v[48:51], v[156:159], v[216:219]\n\t"
      "v_mfma_f32_16x16x32_bf16 v[216:219], v[64:67], v[160:163], v[216:219]\n\t"
      "v_mfma_f32_16x16x32_bf16 v[216:219], v[80:83], v[164:167], v[216:219]\n\t"
      "v_mfma_f32_16x16x32_bf16 v[216:219], v[96:99], v[168:171], v[216:219]\n\t"
      "v_mfma_f32_16x16x32_bf16 v[216:219], v[112:115], v[172:175], v[216:219]\n\t"
      "v_mfma_f32_16x16x32_bf16 v[220:223], v[4:7], v[144:147], 0\n\t"
      "v_mfma_f32_16x16x32_bf16 v[220:223], v[20:23], v[148:151], v[220:223]\n\t"
      "v_mfma_f32_16x16x32_bf16 v[220:223], v[36:39], v[152:155], v[220:223]\n\t"
      "s_nop 7\n\t"
      "s_branch LA33B_%=\n\t"
      "LA34R_%=:\n\t"
      "s_mov_b32 s80, 0\n\t"
      "LA34L_%=:\n\t"
      "s_add_u32 s80, s80, 1\n\t"
      "s_cmp_lt_u32 s80, 262144\n\t"
      "s_cbranch_scc0 LA34X_%=\n\t"
      "s_waitcnt vmcnt(0)\n\t"
      "s_sleep 1\n\t"
      "global_load_dwordx4 v[176:179], v250, s[72:73] offset:-4096 sc1\n\t"
      "global_load_dwordx4 v[180:183], v250, s[72:73] offset:-3072 sc1\n\t"
      "global_load_dwordx4 v[184:187], v250, s[72:73] offset:-2048 sc1\n\t"
      "global_load_dwordx4 v[188:191], v250, s[72:73] offset:-1024 sc1\n\t"
      "global_load_dwordx4 v[192:195], v250, s[72:73] sc1\n\t"
      "global_load_dwordx4 v[196:199], v250, s[72:73] offset:1024 sc1\n\t"
      "global_load_dwordx4 v[200:203], v250, s[72:73] offset:2048 sc1\n\t"
      "global_load_dwordx4 v[204:207], v250, s[72:73] offset:3072 sc1\n\t"
      "s_waitcnt vmcnt(0)\n\t"
      "v_max3_u32 v251, v176, v177, v178\n\t"
      "v_max3_u32 v251, v251, v179, v180\n\t"
      "v_max3_u32 v251, v251, v181, v182\n\t"
      "v_max3_u32 v251, v251, v183, v184\n\t"
      "v_max3_u32 v251, v251, v185, v186\n\t"
      "v_max3_u32 v251, v251, v187, v188\n\t"
      "v_max3_u32 v251, v251, v189, v190\n\t"
      "v_max3_u32 v251, v251, v191, v192\n\t"
      "v_max3_u32 v251, v251, v193, v194\n\t"
      "v_max3_u32 v251, v251, v195, v196\n\t"
      "v_max3_u32 v251, v251, v197, v198\n\t"
      "v_max3_u32 v251, v251, v199, v200\n\t"
      "v_max3_u32 v251, v251, v201, v202\n\t"
      "v_max3_u32 v251, v251, v203, v204\n\t"
      "v_max3_u32 v251, v251, v205, v206\n\t"
      "v_max_u32_e32 v251, v251, v207\n\t"
      "v_cmp_eq_u32_e32 vcc, -1, v251\n\t"
      "s_cbranch_vccnz LA34L_%=\n\t"
      "LA34X_%=:\n\t"
      "v_mfma_f32_16x16x32_bf16 v[212:215], v[0:3], v[176:179], 0\n\t"
      "v_mfma_f32_16x16x32_bf16 v[212:215], v[16:19], v[180:183], v[212:215]\n\t"
      "v_mfma_f32_16x16x32_bf16 v[212:215], v[32:35], v[184:187], v[212:215]\n\t"
      "v_mfma_f32_16x16x32_bf16 v[212:215], v[48:51], v[188:191], v[212:215]\n\t"
      "v_mfma_f32_16x16x32_bf16 v[212:215], v[64:67], v[192:195], v[212:215]\n\t"
      "v_mfma_f32_16x16x32_bf16 v[212:215], v[80:83], v[196:199], v[212:215]\n\t"
      "v_mfma_f32_16x16x32_bf16 v[212:215], v[96:99], v[200:203], v[212:215]\n\t"
      "v_mfma_f32_16x16x32_bf16 v[212:215], v[112:115], v[204:207], v[212:215]\n\t"
      "v_mfma_f32_16x16x32_bf16 v[216:219], v[4:7], v[176:179], 0\n\t"
      "v_mfma_f32_16x16x32_bf16 v[216:219], v[20:23], v[180:183], v[216:219]\n\t"
      "v_mfma_f32_16x16x32_bf16 v[216:219], v[36:39], v[184:187], v[216:219]\n\t"
      "s_nop 7\n\t"
      "s_branch LA34B_%=\n\t"
      "LA35R_%=:\n\t"
      "s_mov_b32 s80, 0\n\t"
      "LA35L_%=:\n\t"
      "s_add_u32 s80, s80, 1\n\t"
      "s_cmp_lt_u32 s80, 262144\n\t"
      "s_cbranch_scc0 LA35X_%=\n\t"
      "s_waitcnt vmcnt(0)\n\t"
      "s_sleep 1\n\t"
      "global_load_dwordx4 v[128:131], v250, s[74:75] offset:-4096 sc1\n\t"
      "global_load_dwordx4 v[132:135], v250, s[74:75] offset:-3072 sc1\n\t"
      "global_load_dwordx4 v[136:139], v250, s[74:75] offset:-2048 sc1\n\t"
      "global_load_dwordx4 v[140:143], v250, s[74:75] offset:-1024 sc1\n\t"
      "global_load_dwordx4 v[144:147], v250, s[74:75] sc1\n\t"
      "global_load_dwordx4 v[148:151], v250, s[74:75] offset:1024 sc1\n\t"
      "global_load_dwordx4 v[152:155], v250, s[74:75] offset:2048 sc1\n\t"
      "global_load_dwordx4 v[156:159], v250, s[74:75] offset:3072 sc1\n\t"
      "s_waitcnt vmcnt(0)\n\t"
      "v_max3_u32 v251, v128, v129, v130\n\t"
      "v_max3_u32 v251, v251, v131, v132\n\t"
      "v_max3_u32 v251, v251, v133, v134\n\t"
      "v_max3_u32 v251, v251, v135, v136\n\t"
      "v_max3_u32 v251, v251, v137, v138\n\t"
      "v_max3_u32 v251, v251, v139, v140\n\t"
      "v_max3_u32 v251, v251, v141, v142\n\t"
      "v_max3_u32 v251, v251, v143, v144\n\t"
      "v_max3_u32 v251, v251, v145, v146\n\t"
      "v_max3_u32 v251, v251, v147, v148\n\t"
      "v_max3_u32 v251, v251, v149, v150\n\t"
      "v_max3_u32 v251, v251, v151, v152\n\t"
      "v_max3_u32 v251, v251, v153, v154\n\t"
      "v_max3_u32 v251, v251, v155, v156\n\t"
      "v_max3_u32 v251, v251, v157, v158\n\t"
      "v_max_u32_e32 v251, v251, v159\n\t"
      "v_cmp_eq_u32_e32 vcc, -1, v251\n\t"
      "s_cbranch_vccnz LA35L_%=\n\t"
      "LA35X_%=:\n\t"
      "v_mfma_f32_16x16x32_bf16 v[208:211], v[0:3], v[128:131], 0\n\t"
      "v_mfma_f32_16x16x32_bf16 v[208:211], v[16:19], v[132:135], v[208:211]\n\t"
      "v_mfma_f32_16x16x32_bf16 v[208:211], v[32:35], v[136:139], v[208:211]\n\t"
      "v_mfma_f32_16x16x32_bf16 v[208:211], v[48:51], v[140:143], v[208:211]\n\t"
      "v_mfma_f32_16x16x32_bf16 v[208:211], v[64:67], v[144:147], v[208:211]\n\t"
      "v_mfma_f32_16x16x32_bf16 v[208:211], v[80:83], v[148:151], v[208:211]\n\t"
      "v_mfma_f32_16x16x32_bf16 v[208:211], v[96:99], v[152:155], v[208:211]\n\t"
      "v_mfma_f32_16x16x32_bf16 v[208:211], v[112:115], v[156:159], v[208:211]\n\t"
      "v_mfma_f32_16x16x32_bf16 v[212:215], v[4:7], v[128:131], 0\n\t"
      "v_mfma_f32_16x16x32_bf16 v[212:215], v[20:23], v[132:135], v[212:215]\n\t"
      "v_mfma_f32_16x16x32_bf16 v[212:215], v[36:39], v[136:139], v[212:215]\n\t"
      "s_nop 7\n\t"
      "s_branch LA35B_%=\n\t"
      "LA36R_%=:\n\t"
      "s_mov_b32 s80, 0\n\t"
      "LA36L_%=:\n\t"
      "s_add_u32 s80, s80, 1\n\t"
      "s_cmp_lt_u32 s80, 262144\n\t"
      "s_cbranch_scc0 LA36X_%=\n\t"
      "s_waitcnt vmcnt(0)\n\t"
      "s_sleep 1\n\t"
      "global_load_dwordx4 v[160:163], v250, s[76:77] offset:-4096 sc1\n\t"
      "global_load_dwordx4 v[164:167], v250, s[76:77] offset:-3072 sc1\n\t"
      "global_load_dwordx4 v[168:171], v250, s[76:77] offset:-2048 sc1\n\t"
      "global_load_dwordx4 v[172:175], v250, s[76:77] offset:-1024 sc1\n\t"
      "global_load_dwordx4 v[176:179], v250, s[76:77] sc1\n\t"
      "global_load_dwordx4 v[180:183], v250, s[76:77] offset:1024 sc1\n\t"
      "global_load_dwordx4 v[184:187], v250, s[76:77] offset:2048 sc1\n\t"
      "global_load_dwordx4 v[188:191], v250, s[76:77] offset:3072 sc1\n\t"
      "s_waitcnt vmcnt(0)\n\t"
      "v_max3_u32 v251, v160, v161, v162\n\t"
      "v_max3_u32 v251, v251, v163, v164\n\t"
      "v_max3_u32 v251, v251, v165, v166\n\t"
      "v_max3_u32 v251, v251, v167, v168\n\t"
      "v_max3_u32 v251, v251, v169, v170\n\t"
      "v_max3_u32 v251, v251, v171, v172\n\t"
      "v_max3_u32 v251, v251, v173, v174\n\t"
      "v_max3_u32 v251, v251, v175, v176\n\t"
      "v_max3_u32 v251, v251, v177, v178\n\t"
      "v_max3_u32 v251, v251, v179, v180\n\t"
      "v_max3_u32 v251, v251, v181, v182\n\t"
      "v_max3_u32 v251, v251, v183, v184\n\t"
      "v_max3_u32 v251, v251, v185, v186\n\t"
      "v_max3_u32 v251, v251, v187, v188\n\t"
      "v_max3_u32 v251, v251, v189, v190\n\t"
      "v_max_u32_e32 v251, v251, v191\n\t"
      "v_cmp_eq_u32_e32 vcc, -1, v251\n\t"
      "s_cbranch_vccnz LA36L_%=\n\t"
      "LA36X_%=:\n\t"
      "v_mfma_f32_16x16x32_bf16 v[224:227], v[0:3], v[160:163], 0\n\t"
      "v_mfma_f32_16x16x32_bf16 v[224:227], v[16:19], v[164:167], v[224:227]\n\t"
      "v_mfma_f32_16x16x32_bf16 v[224:227], v[32:35], v[168:171], v[224:227]\n\t"
      "v_mfma_f32_16x16x32_bf16 v[224:227], v[48:51], v[172:175], v[224:227]\n\t"
      "v_mfma_f32_16x16x32_bf16 v[224:227], v[64:67], v[176:179], v[224:227]\n\t"
      "v_mfma_f32_16x16x32_bf16 v[224:227], v[80:83], v[180:183], v[224:227]\n\t"
      "v_mfma_f32_16x16x32_bf16 v[224:227], v[96:99], v[184:187], v[224:227]\n\t"
      "v_mfma_f32_16x16x32_bf16 v[224:227], v[112:115], v[188:191], v[224:227]\n\t"
      "v_mfma_f32_16x16x32_bf16 v[208:211], v[4:7], v[160:163], 0\n\t"
      "v_mfma_f32_16x16x32_bf16 v[208:211], v[20:23], v[164:167], v[208:211]\n\t"
      "v_mfma_f32_16x16x32_bf16 v[208:211], v[36:39], v[168:171], v[208:211]\n\t"
      "s_nop 7\n\t"
      "s_branch LA36B_%=\n\t"
      "LF5R_%=:\n\t"
      "s_mov_b32 s80, 0\n\t"
      "LF5L_%=:\n\t"
      "s_add_u32 s80, s80, 1\n\t"
      "s_cmp_lt_u32 s80, 262144\n\t"
      "s_cbranch_scc0 LF5X_%=\n\t"
      "s_sleep 2\n\t"
      "global_load_dwordx2 v[138:139], v143, s[60:61] sc1\n\t"
      "s_waitcnt vmcnt(0)\n\t"
      "v_cmp_ne_u64_e32 vcc, s[22:23], v[138:139]\n\t"
      "s_cbranch_vccnz LF5L_%=\n\t"
      "LF5X_%=:\n\t"
      "s_branch LF5B_%=\n\t"
      "PG0R_%=:\n\t"
      "s_mov_b32 s80, 0\n\t"
      "PG0L_%=:\n\t"
      "s_add_u32 s80, s80, 1\n\t"
      "s_cmp_lt_u32 s80, 262144\n\t"
      "s_cbranch_scc0 PG0X_%=\n\t"
      "s_sleep 2\n\t"
      "global_load_dword v251, v253, s[66:67] offset:252 sc1\n\t"
      "s_waitcnt vmcnt(0)\n\t"
      "v_cmp_eq_u32_e32 vcc, -1, v251\n\t"
      "s_cbranch_vccnz PG0L_%=\n\t"
      "PG0X_%=:\n\t"
      "s_branch PG0B_%=\n\t"
      "PG1R_%=:\n\t"
      "s_mov_b32 s80, 0\n\t"
      "PG1L_%=:\n\t"
      "s_add_u32 s80, s80, 1\n\t"
      "s_cmp_lt_u32 s80, 262144\n\t"
      "s_cbranch_scc0 PG1X_%=\n\t"
      "s_sleep 2\n\t"
      "global_load_dword v251, v253, s[66:67] offset:252 sc1\n\t"
      "s_waitcnt vmcnt(0)\n\t"
      "v_cmp_eq_u32_e32 vcc, -1, v251\n\t"
      "s_cbranch_vccnz PG1L_%=\n\t"
      "PG1X_%=:\n\t"
      "s_branch PG1B_%=\n\t"
      "LA40R_%=:\n\t"
      "s_mov_b32 s80, 0\n\t"
      "LA40L_%=:\n\t"
      "s_add_u32 s80, s80, 1\n\t"
      "s_cmp_lt_u32 s80, 262144\n\t"
      "s_cbranch_scc0 LA40X_%=\n\t"
      "s_waitcnt vmcnt(0)\n\t"
      "s_mov_b32 s81, s80\n\t"
      "PQ0R_%=:\n\t"
      "s_mov_b32 s80, 0\n\t"
      "PQ0L_%=:\n\t"
      "s_add_u32 s80, s80, 1\n\t"
      "s_cmp_lt_u32 s80, 262144\n\t"
      "s_cbranch_scc0 PQ0X_%=\n\t"
      "s_sleep 1\n\t"
      "global_load_dword v251, v252, s[64:65] offset:252 sc1\n\t"
      "s_waitcnt vmcnt(0)\n\t"
      "v_cmp_eq_u32_e32 vcc, -1, v251\n\t"
      "s_cbranch_vccnz PQ0L_%=\n\t"
      "PQ0X_%=:\n\t"
      "s_mov_b32 s80, s81\n\t"
      "s_add_u32 s58, s64, s40\n\t"
      "s_addc_u32 s59, s65, 0\n\t"
      "global_load_dwordx4 v[128:131], v244, s[58:59] sc1\n\t"
      "s_add_u32 s58, s64, s41\n\t"
      "s_addc_u32 s59, s65, 0\n\t"
      "global_load_dwordx4 v[132:135], v244, s[58:59] sc1\n\t"
      "s_add_u32 s58, s64, s42\n\t"
      "s_addc_u32 s59, s65, 0\n\t"
      "global_load_dwordx4 v[136:139], v244, s[58:59] sc1\n\t"
      "s_add_u32 s58, s64, s43\n\t"
      "s_addc_u32 s59, s65, 0\n\t"
      "global_load_dwordx4 v[140:143], v244, s[58:59] sc1\n\t"
      "s_add_u32 s58, s64, s44\n\t"
      "s_addc_u32 s59, s65, 0\n\t"
      "global_load_dwordx4 v[144:147], v244, s[58:59] sc1\n\t"
      "s_add_u32 s58, s64, s45\n\t"
      "s_addc_u32 s59, s65, 0\n\t"
      "global_load_dwordx4 v[148:151], v244, s[58:59] sc1\n\t"
      "s_add_u32 s58, s64, s46\n\t"
      "s_addc_u32 s59, s65, 0\n\t"
      "global_load_dwordx4 v[152:155], v244, s[58:59] sc1\n\t"
      "s_waitcnt vmcnt(0)\n\t"
      "v_max3_u32 v251, v128, v129, v130\n\t"
      "v_max3_u32 v251, v251, v131, v132\n\t"
      "v_max3_u32 v251, v251, v133, v134\n\t"
      "v_max3_u32 v251, v251, v135, v136\n\t"
      "v_max3_u32 v251, v251, v137, v138\n\t"
      "v_max3_u32 v251, v251, v139, v140\n\t"
      "v_max3_u32 v251, v251, v141, v142\n\t"
      "v_max3_u32 v251, v251, v143, v144\n\t"
      "v_max3_u32 v251, v251, v145, v146\n\t"
      "v_max3_u32 v251, v251, v147, v148\n\t"
      "v_max3_u32 v251, v251, v149, v150\n\t"
      "v_max3_u32 v251, v251, v151, v152\n\t"
      "v_max3_u32 v251, v251, v153, v154\n\t"
      "v_max_u32_e32 v251, v251, v155\n\t"
      "v_cmp_eq_u32_e32 vcc, -1, v251\n\t"
      "s_cbranch_vccnz LA40L_%=\n\t"
      "LA40X_%=:\n\t"
      "v_mfma_f32_16x16x32_bf16 v[208:211], v[128:131], v[0:3], 0\n\t"
      "v_mfma_f32_16x16x32_bf16 v[208:211], v[132:135], v[16:19], v[208:211]\n\t"
      "v_mfma_f32_16x16x32_bf16 v[208:211], v[136:139], v[32:35], v[208:211]\n\t"
      "v_mfma_f32_16x16x32_bf16 v[208:211], v[140:143], v[48:51], v[208:211]\n\t"
      "v_mfma_f32_16x16x32_bf16 v[208:211], v[144:147], v[64:67], v[208:211]\n\t"
      "v_mfma_f32_16x16x32_bf16 v[208:211], v[148:151], v[80:83], v[208:211]\n\t"
      "v_mfma_f32_16x16x32_bf16 v[208:211], v[152:155], v[96:99], v[208:211]\n\t"
      "v_mfma_f32_16x16x32_bf16 v[212:215], v[128:131], v[4:7], 0\n\t"
      "v_mfma_f32_16x16x32_bf16 v[212:215], v[132:135], v[20:23], v[212:215]\n\t"
      "v_mfma_f32_16x16x32_bf16 v[212:215], v[136:139], v[36:39], v[212:215]\n\t"
      "s_nop 7\n\t"
      "s_branch LA40B_%=\n\t"
      "LA41R_%=:\n\t"
      "s_mov_b32 s80, 0\n\t"
      "LA41L_%=:\n\t"
      "s_add_u32 s80, s80, 1\n\t"
      "s_cmp_lt_u32 s80, 262144\n\t"
      "s_cbranch_scc0 LA41X_%=\n\t"
      "s_waitcnt vmcnt(0)\n\t"
      "s_mov_b32 s81, s80\n\t"
      "PQ1R_%=:\n\t"
      "s_mov_b32 s80, 0\n\t"
      "PQ1L_%=:\n\t"
      "s_add_u32 s80, s80, 1\n\t"
      "s_cmp_lt_u32 s80, 262144\n\t"
      "s_cbranch_scc0 PQ1X_%=\n\t"
      "s_sleep 1\n\t"
      "global_load_dword v251, v252, s[66:67] offset:252 sc1\n\t"
      "s_waitcnt vmcnt(0)\n\t"
      "v_cmp_eq_u32_e32 vcc, -1, v251\n\t"
      "s_cbranch_vccnz PQ1L_%=\n\t"
      "PQ1X_%=:\n\t"
      "s_mov_b32 s80, s81\n\t"
      "s_add_u32 s58, s66, s40\n\t"
      "s_addc_u32 s59, s67, 0\n\t"
      "global_load_dwordx4 v[156:159], v244, s[58:59] sc1\n\t"
      "s_add_u32 s58, s66, s41\n\t"
      "s_addc_u32 s59, s67, 0\n\t"
      "global_load_dwordx4 v[160:163], v244, s[58:59] sc1\n\t"
      "s_add_u32 s58, s66, s42\n\t"
      "s_addc_u32 s59, s67, 0\n\t"
      "global_load_dwordx4 v[164:167], v244, s[58:59] sc1\n\t"
      "s_add_u32 s58, s66, s43\n\t"
      "s_addc_u32 s59, s67, 0\n\t"
      "global_load_dwordx4 v[168:171], v244, s[58:59] sc1\n\t"
      "s_add_u32 s58, s66, s44\n\t"
      "s_addc_u32 s59, s67, 0\n\t"
      "global_load_dwordx4 v[172:175], v244, s[58:59] sc1\n\t"
      "s_add_u32 s58, s66, s45\n\t"
      "s_addc_u32 s59, s67, 0\n\t"
      "global_load_dwordx4 v[176:179], v244, s[58:59] sc1\n\t"
      "s_add_u32 s58, s66, s46\n\t"
      "s_addc_u32 s59, s67, 0\n\t"
      "global_load_dwordx4 v[180:183], v244, s[58:59] sc1\n\t"
      "s_waitcnt vmcnt(0)\n\t"
      "v_max3_u32 v251, v156, v157, v158\n\t"
      "v_max3_u32 v251, v251, v159, v160\n\t"
      "v_max3_u32 v251, v251, v161, v162\n\t"
      "v_max3_u32 v251, v251, v163, v164\n\t"
      "v_max3_u32 v251, v251, v165, v166\n\t"
      "v_max3_u32 v251, v251, v167, v168\n\t"
      "v_max3_u32 v251, v251, v169, v170\n\t"
      "v_max3_u32 v251, v251, v171, v172\n\t"
      "v_max3_u32 v251, v251, v173, v174\n\t"
      "v_max3_u32 v251, v251, v175, v176\n\t"
      "v_max3_u32 v251, v251, v177, v178\n\t"
      "v_max3_u32 v251, v251, v179, v180\n\t"
      "v_max3_u32 v251, v251, v181, v182\n\t"
      "v_max_u32_e32 v251, v251, v183\n\t"
      "v_cmp_eq_u32_e32 vcc, -1, v251\n\t"
      "s_cbranch_vccnz LA41L_%=\n\t"
      "LA41X_%=:\n\t"
      "v_mfma_f32_16x16x32_bf16 v[224:227], v[156:159], v[0:3], 0\n\t"
      "v_mfma_f32_16x16x32_bf16 v[224:227], v[160:163], v[16:19], v[224:227]\n\t"
      "v_mfma_f32_16x16x32_bf16 v[224:227], v[164:167], v[32:35], v[224:227]\n\t"
      "v_mfma_f32_16x16x32_bf16 v[224:227], v[168:171], v[48:51], v[224:227]\n\t"
      "v_mfma_f32_16x16x32_bf16 v[224:227], v[172:175], v[64:67], v[224:227]\n\t"
      "v_mfma_f32_16x16x32_bf16 v[224:227], v[176:179], v[80:83], v[224:227]\n\t"
      "v_mfma_f32_16x16x32_bf16 v[224:227], v[180:183], v[96:99], v[224:227]\n\t"
      "v_mfma_f32_16x16x32_bf16 v[208:211], v[156:159], v[4:7], 0\n\t"
      "v_mfma_f32_16x16x32_bf16 v[208:211], v[160:163], v[20:23], v[208:211]\n\t"
      "v_mfma_f32_16x16x32_bf16 v[208:211], v[164:167], v[36:39], v[208:211]\n\t"
      "s_nop 7\n\t"
      "s_branch LA41B_%=\n\t"
      "LA42R_%=:\n\t"
      "s_mov_b32 s80, 0\n\t"
      "LA42L_%=:\n\t"
      "s_add_u32 s80, s80, 1\n\t"
      "s_cmp_lt_u32 s80, 262144\n\t"
      "s_cbranch_scc0 LA42X_%=\n\t"
      "s_waitcnt vmcnt(0)\n\t"
      "s_mov_b32 s81, s80\n\t"
      "PQ2R_%=:\n\t"
      "s_mov_b32 s80, 0\n\t"
      "PQ2L_%=:\n\t"
      "s_add_u32 s80, s80, 1\n\t"
      "s_cmp_lt_u32 s80, 262144\n\t"
      "s_cbranch_scc0 PQ2X_%=\n\t"
      "s_sleep 1\n\t"
      "global_load_dword v251, v252, s[68:69] offset:252 sc1\n\t"
      "s_waitcnt vmcnt(0)\n\t"
      "v_cmp_eq_u32_e32 vcc, -1, v251\n\t"
      "s_cbranch_vccnz PQ2L_%=\n\t"
      "PQ2X_%=:\n\t"
      "s_mov_b32 s80, s81\n\t"
      "s_add_u32 s58, s68, s40\n\t"
      "s_addc_u32 s59, s69, 0\n\t"
      "global_load_dwordx4 v[184:187], v244, s[58:59] sc1\n\t"
      "s_add_u32 s58, s68, s41\n\t"
      "s_addc_u32 s59, s69, 0\n\t"
      "global_load_dwordx4 v[188:191], v244, s[58:59] sc1\n\t"
      "s_add_u32 s58, s68, s42\n\t"
      "s_addc_u32 s59, s69, 0\n\t"
      "global_load_dwordx4 v[192:195], v244, s[58:59] sc1\n\t"
      "s_add_u32 s58, s68, s43\n\t"
      "s_addc_u32 s59, s69, 0\n\t"
      "global_load_dwordx4 v[196:199], v244, s[58:59] sc1\n\t"
      "s_add_u32 s58, s68, s44\n\t"
      "s_addc_u32 s59, s69, 0\n\t"
      "global_load_dwordx4 v[200:203], v244, s[58:59] sc1\n\t"
      "s_add_u32 s58, s68, s45\n\t"
      "s_addc_u32 s59, s69, 0\n\t"
      "global_load_dwordx4 v[204:207], v244, s[58:59] sc1\n\t"
      "s_add_u32 s58, s68, s46\n\t"
      "s_addc_u32 s59, s69, 0\n\t"
      "global_load_dwordx4 v[128:131], v244, s[58:59] sc1\n\t"
      "s_waitcnt vmcnt(0)\n\t"
      "v_max3_u32 v251, v184, v185, v186\n\t"
      "v_max3_u32 v251, v251, v187, v188\n\t"
      "v_max3_u32 v251, v251, v189, v190\n\t"
      "v_max3_u32 v251, v251, v191, v192\n\t"
      "v_max3_u32 v251, v251, v193, v194\n\t"
      "v_max3_u32 v251, v251, v195, v196\n\t"
      "v_max3_u32 v251, v251, v197, v198\n\t"
      "v_max3_u32 v251, v251, v199, v200\n\t"
      "v_max3_u32 v251, v251, v201, v202\n\t"
      "v_max3_u32 v251, v251, v203, v204\n\t"
      "v_max3_u32 v251, v251, v205, v206\n\t"
      "v_max3_u32 v251, v251, v207, v128\n\t"
      "v_max3_u32 v251, v251, v129, v130\n\t"
      "v_max_u32_e32 v251, v251, v131\n\t"
      "v_cmp_eq_u32_e32 vcc, -1, v251\n\t"
      "s_cbranch_vccnz LA42L_%=\n\t"
      "LA42X_%=:\n\t"
      "v_mfma_f32_16x16x32_bf16 v[220:223], v[184:187], v[0:3], 0\n\t"
      "v_mfma_f32_16x16x32_bf16 v[220:223], v[188:191], v[16:19], v[220:223]\n\t"
      "v_mfma_f32_16x16x32_bf16 v[220:223], v[192:195], v[32:35], v[220:223]\n\t"
      "v_mfma_f32_16x16x32_bf16 v[220:223], v[196:199], v[48:51], v[220:223]\n\t"
      "v_mfma_f32_16x16x32_bf16 v[220:223], v[200:203], v[64:67], v[220:223]\n\t"
      "v_mfma_f32_16x16x32_bf16 v[220:223], v[204:207], v[80:83], v[220:223]\n\t"
      "v_mfma_f32_16x16x32_bf16 v[220:223], v[128:131], v[96:99], v[220:223]\n\t"
      "v_mfma_f32_16x16x32_bf16 v[224:227], v[184:187], v[4:7], 0\n\t"
      "v_mfma_f32_16x16x32_bf16 v[224:227], v[188:191], v[20:23], v[224:227]\n\t"
      "v_mfma_f32_16x16x32_bf16 v[224:227], v[192:195], v[36:39], v[224:227]\n\t"
      "s_nop 7\n\t"
      "s_branch LA42B_%=\n\t"
      "LA43R_%=:\n\t"
      "s_mov_b32 s80, 0\n\t"
      "LA43L_%=:\n\t"
      "s_add_u32 s80, s80, 1\n\t"
      "s_cmp_lt_u32 s80, 262144\n\t"
      "s_cbranch_scc0 LA43X_%=\n\t"
      "s_waitcnt vmcnt(0)\n\t"
      "s_mov_b32 s81, s80\n\t"
      "PQ3R_%=:\n\t"
      "s_mov_b32 s80, 0\n\t"
      "PQ3L_%=:\n\t"
      "s_add_u32 s80, s80, 1\n\t"
      "s_cmp_lt_u32 s80, 262144\n\t"
      "s_cbranch_scc0 PQ3X_%=\n\t"
      "s_sleep 1\n\t"
      "global_load_dword v251, v252, s[70:71] offset:252 sc1\n\t"
      "s_waitcnt vmcnt(0)\n\t"
      "v_cmp_eq_u32_e32 vcc, -1, v251\n\t"
      "s_cbranch_vccnz PQ3L_%=\n\t"
      "PQ3X_%=:\n\t"
      "s_mov_b32 s80, s81\n\t"
      "s_add_u32 s58, s70, s40\n\t"
      "s_addc_u32 s59, s71, 0\n\t"
      "global_load_dwordx4 v[132:135], v244, s[58:59] sc1\n\t"
      "s_add_u32 s58, s70, s41\n\t"
      "s_addc_u32 s59, s71, 0\n\t"
      "global_load_dwordx4 v[136:139], v244, s[58:59] sc1\n\t"
      "s_add_u32 s58, s70, s42\n\t"
      "s_addc_u32 s59, s71, 0\n\t"
      "global_load_dwordx4 v[140:143], v244, s[58:59] sc1\n\t"
      "s_add_u32 s58, s70, s43\n\t"
      "s_addc_u32 s59, s71, 0\n\t"
      "global_load_dwordx4 v[144:147], v244, s[58:59] sc1\n\t"
      "s_add_u32 s58, s70, s44\n\t"
      "s_addc_u32 s59, s71, 0\n\t"
      "global_load_dwordx4 v[148:151], v244, s[58:59] sc1\n\t"
      "s_add_u32 s58, s70, s45\n\t"
      "s_addc_u32 s59, s71, 0\n\t"
      "global_load_dwordx4 v[152:155], v244, s[58:59] sc1\n\t"
      "s_add_u32 s58, s70, s46\n\t"
      "s_addc_u32 s59, s71, 0\n\t"
      "global_load_dwordx4 v[156:159], v244, s[58:59] sc1\n\t"
      "s_waitcnt vmcnt(0)\n\t"
      "v_max3_u32 v251, v132, v133, v134\n\t"
      "v_max3_u32 v251, v251, v135, v136\n\t"
      "v_max3_u32 v251, v251, v137, v138\n\t"
      "v_max3_u32 v251, v251, v139, v140\n\t"
      "v_max3_u32 v251, v251, v141, v142\n\t"
      "v_max3_u32 v251, v251, v143, v144\n\t"
      "v_max3_u32 v251, v251, v145, v146\n\t"
      "v_max3_u32 v251, v251, v147, v148\n\t"
      "v_max3_u32 v251, v251, v149, v150\n\t"
      "v_max3_u32 v251, v251, v151, v152\n\t"
      "v_max3_u32 v251, v251, v153, v154\n\t"
      "v_max3_u32 v251, v251, v155, v156\n\t"
      "v_max3_u32 v251, v251, v157, v158\n\t"
      "v_max_u32_e32 v251, v251, v159\n\t"
      "v_cmp_eq_u32_e32 vcc, -1, v251\n\t"
      "s_cbranch_vccnz LA43L_%=\n\t"
      "LA43X_%=:\n\t"
      "v_mfma_f32_16x16x32_bf16 v[216:219], v[132:135], v[0:3], 0\n\t"
      "v_mfma_f32_16x16x32_bf16 v[216:219], v[136:139], v[16:19], v[216:219]\n\t"
      "v_mfma_f32_16x16x32_bf16 v[216:219], v[140:143], v[32:35], v[216:219]\n\t"
      "v_mfma_f32_16x16x32_bf16 v[216:219], v[144:147], v[48:51], v[216:219]\n\t"
      "v_mfma_f32_16x16x32_bf16 v[216:219], v[148:151], v[64:67], v[216:219]\n\t"
      "v_mfma_f32_16x16x32_bf16 v[216:219], v[152:155], v[80:83], v[216:219]\n\t"
      "v_mfma_f32_16x16x32_bf16 v[216:219], v[156:159], v[96:99], v[216:219]\n\t"
      "v_mfma_f32_16x16x32_bf16 v[220:223], v[132:135], v[4:7], 0\n\t"
      "v_mfma_f32_16x16x32_bf16 v[220:223], v[136:139], v[20:23], v[220:223]\n\t"
      "v_mfma_f32_16x16x32_bf16 v[220:223], v[140:143], v[36:39], v[220:223]\n\t"
      "s_nop 7\n\t"
      "s_branch LA43B_%=\n\t"
      "LA44R_%=:\n\t"
      "s_mov_b32 s80, 0\n\t"
      "LA44L_%=:\n\t"
      "s_add_u32 s80, s80, 1\n\t"
      "s_cmp_lt_u32 s80, 262144\n\t"
      "s_cbranch_scc0 LA44X_%=\n\t"
      "s_waitcnt vmcnt(0)\n\t"
      "s_mov_b32 s81, s80\n\t"
      "PQ4R_%=:\n\t"
      "s_mov_b32 s80, 0\n\t"
      "PQ4L_%=:\n\t"
      "s_add_u32 s80, s80, 1\n\t"
      "s_cmp_lt_u32 s80, 262144\n\t"
      "s_cbranch_scc0 PQ4X_%=\n\t"
      "s_sleep 1\n\t"
      "global_load_dword v251, v252, s[72:73] offset:252 sc1\n\t"
      "s_waitcnt vmcnt(0)\n\t"
      "v_cmp_eq_u32_e32 vcc, -1, v251\n\t"
      "s_cbranch_vccnz PQ4L_%=\n\t"
      "PQ4X_%=:\n\t"
      "s_mov_b32 s80, s81\n\t"
      "s_add_u32 s58, s72, s40\n\t"
      "s_addc_u32 s59, s73, 0\n\t"
      "global_load_dwordx4 v[160:163], v244, s[58:59] sc1\n\t"
      "s_add_u32 s58, s72, s41\n\t"
      "s_addc_u32 s59, s73, 0\n\t"
      "global_load_dwordx4 v[164:167], v244, s[58:59] sc1\n\t"
      "s_add_u32 s58, s72, s42\n\t"
      "s_addc_u32 s59, s73, 0\n\t"
      "global_load_dwordx4 v[168:171], v244, s[58:59] sc1\n\t"
      "s_add_u32 s58, s72, s43\n\t"
      "s_addc_u32 s59, s73, 0\n\t"
      "global_load_dwordx4 v[172:175], v244, s[58:59] sc1\n\t"
      "s_add_u32 s58, s72, s44\n\t"
      "s_addc_u32 s59, s73, 0\n\t"
      "global_load_dwordx4 v[176:179], v244, s[58:59] sc1\n\t"
      "s_add_u32 s58, s72, s45\n\t"
      "s_addc_u32 s59, s73, 0\n\t"
      "global_load_dwordx4 v[180:183], v244, s[58:59] sc1\n\t"
      "s_add_u32 s58, s72, s46\n\t"
      "s_addc_u32 s59, s73, 0\n\t"
      "global_load_dwordx4 v[184:187], v244, s[58:59] sc1\n\t"
      "s_waitcnt vmcnt(0)\n\t"
      "v_max3_u32 v251, v160, v161, v162\n\t"
      "v_max3_u32 v251, v251, v163, v164\n\t"
      "v_max3_u32 v251, v251, v165, v166\n\t"
      "v_max3_u32 v251, v251, v167, v168\n\t"
      "v_max3_u32 v251, v251, v169, v170\n\t"
      "v_max3_u32 v251, v251, v171, v172\n\t"
      "v_max3_u32 v251, v251, v173, v174\n\t"
      "v_max3_u32 v251, v251, v175, v176\n\t"
      "v_max3_u32 v251, v251, v177, v178\n\t"
      "v_max3_u32 v251, v251, v179, v180\n\t"
      "v_max3_u32 v251, v251, v181, v182\n\t"
      "v_max3_u32 v251, v251, v183, v184\n\t"
      "v_max3_u32 v251, v251, v185, v186\n\t"
      "v_max_u32_e32 v251, v251, v187\n\t"
      "v_cmp_eq_u32_e32 vcc, -1, v251\n\t"
      "s_cbranch_vccnz LA44L_%=\n\t"
      "LA44X_%=:\n\t"
      "v_mfma_f32_16x16x32_bf16 v[212:215], v[160:163], v[0:3], 0\n\t"
      "v_mfma_f32_16x16x32_bf16 v[212:215], v[164:167], v[16:19], v[212:215]\n\t"
      "v_mfma_f32_16x16x32_bf16 v[212:215], v[168:171], v[32:35], v[212:215]\n\t"
      "v_mfma_f32_16x16x32_bf16 v[212:215], v[172:175], v[48:51], v[212:215]\n\t"
      "v_mfma_f32_16x16x32_bf16 v[212:215], v[176:179], v[64:67], v[212:215]\n\t"
      "v_mfma_f32_16x16x32_bf16 v[212:215], v[180:183], v[80:83], v[212:215]\n\t"
      "v_mfma_f32_16x16x32_bf16 v[212:215], v[184:187], v[96:99], v[212:215]\n\t"
      "v_mfma_f32_16x16x32_bf16 v[216:219], v[160:163], v[4:7], 0\n\t"
      "v_mfma_f32_16x16x32_bf16 v[216:219], v[164:167], v[20:23], v[216:219]\n\t"
      "v_mfma_f32_16x16x32_bf16 v[216:219], v[168:171], v[36:39], v[216:219]\n\t"
      "s_nop 7\n\t"
      "s_branch LA44B_%=\n\t"
      "LA45R_%=:\n\t"
      "s_mov_b32 s80, 0\n\t"
      "LA45L_%=:\n\t"
      "s_add_u32 s80, s80, 1\n\t"
      "s_cmp_lt_u32 s80, 262144\n\t"
      "s_cbranch_scc0 LA45X_%=\n\t"
      "s_waitcnt vmcnt(0)\n\t"
      "s_mov_b32 s81, s80\n\t"
      "PQ5R_%=:\n\t"
      "s_mov_b32 s80, 0\n\t"
      "PQ5L_%=:\n\t"
      "s_add_u32 s80, s80, 1\n\t"
      "s_cmp_lt_u32 s80, 262144\n\t"
      "s_cbranch_scc0 PQ5X_%=\n\t"
      "s_sleep 1\n\t"
      "global_load_dword v251, v252, s[74:75] offset:252 sc1\n\t"
      "s_waitcnt vmcnt(0)\n\t"
      "v_cmp_eq_u32_e32 vcc, -1, v251\n\t"
      "s_cbranch_vccnz PQ5L_%=\n\t"
      "PQ5X_%=:\n\t"
      "s_mov_b32 s80, s81\n\t"
      "s_add_u32 s58, s74, s40\n\t"
      "s_addc_u32 s59, s75, 0\n\t"
      "global_load_dwordx4 v[188:191], v244, s[58:59] sc1\n\t"
      "s_add_u32 s58, s74, s41\n\t"
      "s_addc_u32 s59, s75, 0\n\t"
      "global_load_dwordx4 v[192:195], v244, s[58:59] sc1\n\t"
      "s_add_u32 s58, s74, s42\n\t"
      "s_addc_u32 s59, s75, 0\n\t"
      "global_load_dwordx4 v[196:199], v244, s[58:59] sc1\n\t"
      "s_add_u32 s58, s74, s43\n\t"
      "s_addc_u32 s59, s75, 0\n\t"
      "global_load_dwordx4 v[200:203], v244, s[58:59] sc1\n\t"
      "s_add_u32 s58, s74, s44\n\t"
      "s_addc_u32 s59, s75, 0\n\t"
      "global_load_dwordx4 v[204:207], v244, s[58:59] sc1\n\t"
      "s_add_u32 s58, s74, s45\n\t"
      "s_addc_u32 s59, s75, 0\n\t"
      "global_load_dwordx4 v[128:131], v244, s[58:59] sc1\n\t"
      "s_add_u32 s58, s74, s46\n\t"
      "s_addc_u32 s59, s75, 0\n\t"
      "global_load_dwordx4 v[132:135], v244, s[58:59] sc1\n\t"
      "s_waitcnt vmcnt(0)\n\t"
      "v_max3_u32 v251, v188, v189, v190\n\t"
      "v_max3_u32 v251, v251, v191, v192\n\t"
      "v_max3_u32 v251, v251, v193, v194\n\t"
      "v_max3_u32 v251, v251, v195, v196\n\t"
      "v_max3_u32 v251, v251, v197, v198\n\t"
      "v_max3_u32 v251, v251, v199, v200\n\t"
      "v_max3_u32 v251, v251, v201, v202\n\t"
      "v_max3_u32 v251, v251, v203, v204\n\t"
      "v_max3_u32 v251, v251, v205, v206\n\t"
      "v_max3_u32 v251, v251, v207, v128\n\t"
      "v_max3_u32 v251, v251, v129, v130\n\t"
      "v_max3_u32 v251, v251, v131, v132\n\t"
      "v_max3_u32 v251, v251, v133, v134\n\t"
      "v_max_u32_e32 v251, v251, v135\n\t"
      "v_cmp_eq_u32_e32 vcc, -1, v251\n\t"
      "s_cbranch_vccnz LA45L_%=\n\t"
      "LA45X_%=:\n\t"
      "v_mfma_f32_16x16x32_bf16 v[208:211], v[188:191], v[0:3], 0\n\t"
      "v_mfma_f32_16x16x32_bf16 v[208:211], v[192:195], v[16:19], v[208:211]\n\t"
      "v_mfma_f32_16x16x32_bf16 v[208:211], v[196:199], v[32:35], v[208:211]\n\t"
      "v_mfma_f32_16x16x32_bf16 v[208:211], v[200:203], v[48:51], v[208:211]\n\t"
      "v_mfma_f32_16x16x32_bf16 v[208:211], v[204:207], v[64:67], v[208:211]\n\t"
      "v_mfma_f32_16x16x32_bf16 v[208:211], v[128:131], v[80:83], v[208:211]\n\t"
      "v_mfma_f32_16x16x32_bf16 v[208:211], v[132:135], v[96:99], v[208:211]\n\t"
      "v_mfma_f32_16x16x32_bf16 v[212:215], v[188:191], v[4:7], 0\n\t"
      "v_mfma_f32_16x16x32_bf16 v[212:215], v[192:195], v[20:23], v[212:215]\n\t"
      "v_mfma_f32_16x16x32_bf16 v[212:215], v[196:199], v[36:39], v[212:215]\n\t"
      "s_nop 7\n\t"
      "s_branch LA45B_%=\n\t"
      "LA46R_%=:\n\t"
      "s_mov_b32 s80, 0\n\t"
      "LA46L_%=:\n\t"
      "s_add_u32 s80, s80, 1\n\t"
      "s_cmp_lt_u32 s80, 262144\n\t"
      "s_cbranch_scc0 LA46X_%=\n\t"
      "s_waitcnt vmcnt(0)\n\t"
      "s_mov_b32 s81, s80\n\t"
      "PQ6R_%=:\n\t"
      "s_mov_b32 s80, 0\n\t"
      "PQ6L_%=:\n\t"
      "s_add_u32 s80, s80, 1\n\t"
      "s_cmp_lt_u32 s80, 262144\n\t"
      "s_cbranch_scc0 PQ6X_%=\n\t"
      "s_sleep 1\n\t"
      "global_load_dword v251, v252, s[76:77] offset:252 sc1\n\t"
      "s_waitcnt vmcnt(0)\n\t"
      "v_cmp_eq_u32_e32 vcc, -1, v251\n\t"
      "s_cbranch_vccnz PQ6L_%=\n\t"
      "PQ6X_%=:\n\t"
      "s_mov_b32 s80, s81\n\t"
      "s_add_u32 s58, s76, s40\n\t"
      "s_addc_u32 s59, s77, 0\n\t"
      "global_load_dwordx4 v[136:139], v244, s[58:59] sc1\n\t"
      "s_add_u32 s58, s76, s41\n\t"
      "s_addc_u32 s59, s77, 0\n\t"
      "global_load_dwordx4 v[140:143], v244, s[58:59] sc1\n\t"
      "s_add_u32 s58, s76, s42\n\t"
      "s_addc_u32 s59, s77, 0\n\t"
      "global_load_dwordx4 v[144:147], v244, s[58:59] sc1\n\t"
      "s_add_u32 s58, s76, s43\n\t"
      "s_addc_u32 s59, s77, 0\n\t"
      "global_load_dwordx4 v[148:151], v244, s[58:59] sc1\n\t"
      "s_add_u32 s58, s76, s44\n\t"
      "s_addc_u32 s59, s77, 0\n\t"
      "global_load_dwordx4 v[152:155], v244, s[58:59] sc1\n\t"
      "s_add_u32 s58, s76, s45\n\t"
      "s_addc_u32 s59, s77, 0\n\t"
      "global_load_dwordx4 v[156:159], v244, s[58:59] sc1\n\t"
      "s_add_u32 s58, s76, s46\n\t"
      "s_addc_u32 s59, s77, 0\n\t"
      "global_load_dwordx4 v[160:163], v244, s[58:59] sc1\n\t"
      "s_waitcnt vmcnt(0)\n\t"
      "v_max3_u32 v251, v136, v137, v138\n\t"
      "v_max3_u32 v251, v251, v139, v140\n\t"
      "v_max3_u32 v251, v251, v141, v142\n\t"
      "v_max3_u32 v251, v251, v143, v144\n\t"
      "v_max3_u32 v251, v251, v145, v146\n\t"
      "v_max3_u32 v251, v251, v147, v148\n\t"
      "v_max3_u32 v251, v251, v149, v150\n\t"
      "v_max3_u32 v251, v251, v151, v152\n\t"
      "v_max3_u32 v251, v251, v153, v154\n\t"
      "v_max3_u32 v251, v251, v155, v156\n\t"
      "v_max3_u32 v251, v251, v157, v158\n\t"
      "v_max3_u32 v251, v251, v159, v160\n\t"
      "v_max3_u32 v251, v251, v161, v162\n\t"
      "v_max_u32_e32 v251, v251, v163\n\t"
      "v_cmp_eq_u32_e32 vcc, -1, v251\n\t"
      "s_cbranch_vccnz LA46L_%=\n\t"
      "LA46X_%=:\n\t"
      "v_mfma_f32_16x16x32_bf16 v[224:227], v[136:139], v[0:3], 0\n\t"
      "v_mfma_f32_16x16x32_bf16 v[224:227], v[140:143], v[16:19], v[224:227]\n\t"
      "v_mfma_f32_16x16x32_bf16 v[224:227], v[144:147], v[32:35], v[224:227]\n\t"
      "v_mfma_f32_16x16x32_bf16 v[224:227], v[148:151], v[48:51], v[224:227]\n\t"
      "v_mfma_f32_16x16x32_bf16 v[224:227], v[152:155], v[64:67], v[224:227]\n\t"
      "v_mfma_f32_16x16x32_bf16 v[224:227], v[156:159], v[80:83], v[224:227]\n\t"
      "v_mfma_f32_16x16x32_bf16 v[224:227], v[160:163], v[96:99], v[224:227]\n\t"
      "v_mfma_f32_16x16x32_bf16 v[208:211], v[136:139], v[4:7], 0\n\t"
      "v_mfma_f32_16x16x32_bf16 v[208:211], v[140:143], v[20:23], v[208:211]\n\t"
      "v_mfma_f32_16x16x32_bf16 v[208:211], v[144:147], v[36:39], v[208:211]\n\t"
      "s_nop 7\n\t"
      "s_branch LA46B_%=\n\t"
      : "+{s[0:1]}"(ka), "+{v0}"(tid)
      : "{s2}"(blockIdx.x)
      : "memory", "vcc", "scc", "m0",
        "v1", "v2", "v3", "v4", "v5", "v6", "v7", "v8", "v9", "v10", "v11", "v12", "v13", "v14", "v15", "v16", "v17",
        "v18", "v19", "v20", "v21", "v22", "v23", "v24", "v25", "v26", "v27", "v28", "v29", "v30", "v31", "v32", "v33",
        "v34", "v35", "v36", "v37", "v38", "v39", "v40", "v41", "v42", "v43", "v44", "v45", "v46", "v47", "v48", "v49",
        "v50", "v51", "v52", "v53", "v54", "v55", "v56", "v57", "v58", "v59", "v60", "v61", "v62", "v63", "v64", "v65",
        "v66", "v67", "v68", "v69", "v70", "v71", "v72", "v73", "v74", "v75", "v76", "v77", "v78", "v79", "v80", "v81",
        "v82", "v83", "v84", "v85", "v86", "v87", "v88", "v89", "v90", "v91", "v92", "v93", "v94", "v95", "v96", "v97",
        "v98", "v99", "v100", "v101", "v102", "v103", "v104", "v105", "v106", "v107", "v108", "v109", "v110", "v111",
        "v112", "v113", "v114", "v115", "v116", "v117", "v118", "v119", "v120", "v121", "v122", "v123", "v124", "v125",
        "v126", "v127", "v128", "v129", "v130", "v131", "v132", "v133", "v134", "v135", "v136", "v137", "v138", "v139",
        "v140", "v141", "v142", "v143", "v144", "v145", "v146", "v147", "v148", "v149", "v150", "v151", "v152", "v153",
        "v154", "v155", "v156", "v157", "v158", "v159", "v160", "v161", "v162", "v163", "v164", "v165", "v166", "v167",
        "v168", "v169", "v170", "v171", "v172", "v173", "v174", "v175", "v176", "v177", "v178", "v179", "v180", "v181",
        "v182", "v183", "v184", "v185", "v186", "v187", "v188", "v189", "v190", "v191", "v192", "v193", "v194", "v195",
        "v196", "v197", "v198", "v199", "v200", "v201", "v202", "v203", "v204", "v205", "v206", "v207", "v208", "v209",
        "v210", "v211", "v212", "v213", "v214", "v215", "v216", "v217", "v218", "v219", "v220", "v221", "v222", "v223",
        "v224", "v225", "v226", "v227", "v228", "v229", "v230", "v231", "v232", "v233", "v234", "v235", "v236", "v237",
        "v238", "v239", "v240", "v241", "v242", "v243", "v244", "v245", "v246", "v247", "v248", "v249", "v250", "v251",
        "v252", "v253", "v254", "v255", "s3", "s4", "s5", "s6", "s7", "s8", "s9", "s10", "s11", "s12", "s13", "s14",
        "s15", "s16", "s17", "s18", "s19", "s20", "s21", "s22", "s23", "s24", "s25", "s26", "s27", "s28", "s29", "s30",
        "s31", "s32", "s33", "s34", "s35", "s36", "s37", "s38", "s39", "s40", "s41", "s42", "s43", "s44", "s45", "s46",
        "s47", "s48", "s49", "s50", "s51", "s52", "s53", "s54", "s55", "s56", "s57", "s58", "s59", "s60", "s61", "s62",
        "s63", "s64", "s65", "s66", "s67", "s68", "s69", "s70", "s71", "s72", "s73", "s74", "s75", "s76", "s77", "s78",
        "s79", "s80", "s81", "s82", "s83"
  );
#endif
}

extern "C" void kernel_launch(void* const* d_in, const int* in_sizes, int n_in, void* d_out, int out_size,
                              void* d_ws, size_t ws_size, hipStream_t stream) {
  (void)hipFuncSetAttribute((const void*)fused_block, hipFuncAttributeMaxDynamicSharedMemorySize, 163840);
  fused_block<<<256, 512, 163840, stream>>>((const u16*)d_in[0], (const u16*)d_in[1], (const u16*)d_in[2],
                                            (const u16*)d_in[4], (const u16*)d_in[5], (const u16*)d_in[6],
                                            (const u16*)d_in[7], (u16*)d_out, (char*)d_ws);
}
```

```cpp
#include <hip/hip_runtime.h>
#include <stdint.h>
typedef unsigned short u16;
__global__ __launch_bounds__(512, 2) void fused_block(const u16* x, const u16* wqkv, const u16* wo, const u16* w1, const u16* b1,
                                                   const u16* w2, const u16* b2, u16* out, char* ws) {
#if defined(__HIP_DEVICE_COMPILE__)
  unsigned long long ka = (unsigned long long)__builtin_amdgcn_kernarg_segment_ptr();
  unsigned tid = threadIdx.x;
  asm volatile(
      "s_load_dwordx16 s[4:19], s[0:1], 0x0\n\t"
      "s_load_dwordx2 s[20:21], s[0:1], 0x40\n\t"
      "v_lshrrev_b32_e32 v199, 6, v0\n\t"
      "v_and_b32_e32 v192, 63, v0\n\t"
      "v_readfirstlane_b32 s24, v199\n\t"
      "s_getpc_b64 s[58:59]\n\t"
      "s_lshr_b32 s34, s2, 3\n\t"
      "v_min_u32_e32 v200, 8, v0\n\t"
      "v_lshl_add_u32 v200, v200, 5, s34\n\t"
      "s_mov_b32 m0, 64512\n\t"
      "v_min_u32_e32 v200, 267, v200\n\t"
      "v_lshlrev_b32_e32 v200, 7, v200\n\t"
      "s_cmp_lg_u32 s24, 0\n\t"
      "s_cbranch_scc1 NTC_%=\n\t"
      "global_load_lds_dword v200, s[58:59]\n\t"
      "NTC_%=:\n\t"
      "s_and_b32 s34, s2, 7\n\t"
      "s_lshr_b32 s35, s2, 6\n\t"
      "s_lshl2_add_u32 s25, s34, s35\n\t"
      "s_bfe_u32 s26, s2, 196611\n\t"
      "s_and_b32 s34, s25, 30\n\t"
      "s_lshl_b32 s34, s34, 6\n\t"
      "s_and_b32 s35, s25, 1\n\t"
      "s_add_u32 s38, s34, s35\n\t"
      "v_lshrrev_b32_e32 v197, 3, v192\n\t"
      "v_and_b32_e32 v198, 7, v192\n\t"
      "s_lshr_b32 s3, s2, 1\n\t"
      "s_and_b32 s3, s3, 0\n\t"
      "s_xor_b32 s3, s3, s24\n\t"
      "s_lshl4_add_u32 s34, s3, s38\n\t"
      "s_mul_i32 s34, s34, 896\n\t"
      "s_movk_i32 s35, 1792\n\t"
      "v_lshl_add_u32 v199, v198, 4, s34\n\t"
      "v_mad_u32_u24 v120, v197, s35, v199\n\t"
      "s_min_u32 s74, s26, 6\n\t"
      "s_lshl_b32 s34, s74, 7\n\t"
      "s_add_u32 s35, s34, 768\n\t"
      "s_sub_u32 s36, s35, 896\n\t"
      "s_min_u32 s35, s35, s36\n\t"
      "v_add_u32_e32 v126, s35, v120\n\t"
      "s_add_u32 s35, s34, 640\n\t"
      "s_sub_u32 s36, s35, 896\n\t"
      "s_min_u32 s35, s35, s36\n\t"
      "v_add_u32_e32 v125, s35, v120\n\t"
      "s_add_u32 s35, s34, 512\n\t"
      "s_sub_u32 s36, s35, 896\n\t"
      "s_min_u32 s35, s35, s36\n\t"
      "v_add_u32_e32 v124, s35, v120\n\t"
      "s_add_u32 s35, s34, 384\n\t"
      "s_sub_u32 s36, s35, 896\n\t"
      "s_min_u32 s35, s35, s36\n\t"
      "v_add_u32_e32 v123, s35, v120\n\t"
      "s_add_u32 s35, s34, 256\n\t"
      "s_sub_u32 s36, s35, 896\n\t"
      "s_min_u32 s35, s35, s36\n\t"
      "v_add_u32_e32 v122, s35, v120\n\t"
      "s_add_u32 s35, s34, 128\n\t"
      "s_sub_u32 s36, s35, 896\n\t"
      "s_min_u32 s35, s35, s36\n\t"
      "v_add_u32_e32 v121, s35, v120\n\t"
      "s_add_u32 s35, s34, 0\n\t"
      "s_sub_u32 s36, s35, 896\n\t"
      "s_min_u32 s35, s35, s36\n\t"
      "v_add_u32_e32 v120, s35, v120\n\t"
      "s_waitcnt lgkmcnt(0)\n\t"
      "global_load_dwordx4 v[0:3], v120, s[4:5]\n\t"
      "global_load_dwordx4 v[4:7], v121, s[4:5]\n\t"
      "global_load_dwordx4 v[8:11], v122, s[4:5]\n\t"
      "global_load_dwordx4 v[12:15], v123, s[4:5]\n\t"
      "global_load_dwordx4 v[16:19], v124, s[4:5]\n\t"
      "global_load_dwordx4 v[20:23], v125, s[4:5]\n\t"
      "s_cmp_lt_u32 s25, 2\n\t"
      "s_cselect_b32 s34, 0, 16\n\t"
      "s_sub_u32 s34, s38, s34\n\t"
      "s_lshl1_add_u32 s34, s24, s34\n\t"
      "s_mul_i32 s34, s34, 896\n\t"
      "s_and_b32 s33, s25, 3\n\t"
      "s_lshl_b32 s33, s33, 1\n\t"
      "s_and_b32 s35, s26, 1\n\t"
      "s_add_u32 s33, s33, s35\n\t"
      "s_cmp_eq_u32 s33, 7\n\t"
      "s_cselect_b32 s33, 0, s33\n\t"
      "v_min_u32_e32 v200, 55, v192\n\t"
      "s_lshl_b32 s35, s33, 3\n\t"
      "v_add_u32_e32 v201, s35, v200\n\t"
      "v_subrev_u32_e32 v202, 56, v201\n\t"
      "v_min_u32_e32 v201, v201, v202\n\t"
      "v_lshl_add_u32 v127, v201, 4, s34\n\t"
      "s_min_u32 s28, s26, 6\n\t"
      "s_and_b32 s39, s24, 3\n\t"
      "s_lshl_b32 s39, s39, 12\n\t"
      "s_cmp_lt_u32 s24, 4\n\t"
      "s_cselect_b32 s27, 4, 0\n\t"
      "v_lshrrev_b32_e32 v193, 4, v192\n\t"
      "v_and_b32_e32 v194, 15, v192\n\t"
      "v_bfe_u32 v195, v192, 2, 2\n\t"
      "v_and_b32_e32 v196, 3, v192\n\t"
      "v_lshlrev_b32_e32 v244, 4, v192\n\t"
      "s_lshr_b32 s34, s3, 1\n\t"
      "s_mul_i32 s34, s34, 14336\n\t"
      "s_and_b32 s35, s3, 1\n\t"
      "s_lshl_b32 s35, s35, 7\n\t"
      "s_add_u32 s34, s34, s35\n\t"
      "v_xor_b32_e32 v199, v197, v198\n\t"
      "v_lshl_add_u32 v199, v199, 4, s34\n\t"
      "v_lshl_add_u32 v199, v198, 8, v199\n\t"
      "s_sub_u32 s35, s74, s33\n\t"
      "s_cselect_b32 s36, 7, 0\n\t"
      "s_add_u32 s35, s35, s36\n\t"
      "s_lshl_b32 s35, s35, 11\n\t"
      "s_add_u32 s34, s35, 0\n\t"
      "s_sub_u32 s36, s34, 14336\n\t"
      "s_min_u32 s34, s34, s36\n\t"
      "v_add_u32_e32 v112, s34, v199\n\t"
      "s_add_u32 s34, s35, 2048\n\t"
      "s_sub_u32 s36, s34, 14336\n\t"
      "s_min_u32 s34, s34, s36\n\t"
      "v_add_u32_e32 v113, s34, v199\n\t"
      "s_add_u32 s34, s35, 4096\n\t"
      "s_sub_u32 s36, s34, 14336\n\t"
      "s_min_u32 s34, s34, s36\n\t"
      "v_add_u32_e32 v114, s34, v199\n\t"
      "s_add_u32 s34, s35, 6144\n\t"
      "s_sub_u32 s36, s34, 14336\n\t"
      "s_min_u32 s34, s34, s36\n\t"
      "v_add_u32_e32 v115, s34, v199\n\t"
      "s_add_u32 s34, s35, 8192\n\t"
      "s_sub_u32 s36, s34, 14336\n\t"
      "s_min_u32 s34, s34, s36\n\t"
      "v_add_u32_e32 v116, s34, v199\n\t"
      "s_add_u32 s34, s35, 10240\n\t"
      "s_sub_u32 s36, s34, 14336\n\t"
      "s_min_u32 s34, s34, s36\n\t"
      "v_add_u32_e32 v117, s34, v199\n\t"
      "s_add_u32 s34, s35, 12288\n\t"
      "s_sub_u32 s36, s34, 14336\n\t"
      "s_min_u32 s34, s34, s36\n\t"
      "v_add_u32_e32 v118, s34, v199\n\t"
      "v_min_u32_e32 v200, 55, v192\n\t"
      "v_lshrrev_b32_e32 v201, 2, v200\n\t"
      "v_lshlrev_b32_e32 v119, 10, v201\n\t"
      "v_cmp_lt_u32_e32 vcc, 6, v201\n\t"
      "v_mov_b32_e32 v202, 57344\n\t"
      "v_mov_b32_e32 v201, 50688\n\t"
      "v_cndmask_b32_e32 v202, v202, v201, vcc\n\t"
      "v_and_b32_e32 v201, 3, v200\n\t"
      "v_lshl_add_u32 v202, v201, 7, v202\n\t"
      "v_and_b32_e32 v200, 7, v200\n\t"
      "v_xor_b32_e32 v200, s24, v200\n\t"
      "v_lshl_add_u32 v202, v200, 4, v202\n\t"
      "v_add_u32_e32 v119, v119, v202\n\t"
      "s_bitcmp1_b32 s27, 2\n\t"
      "s_cbranch_scc1 LSET_%=\n\t"
      "v_xor_b32_e32 v199, v194, v193\n\t"
      "v_lshlrev_b32_e32 v199, 4, v199\n\t"
      "v_lshl_or_b32 v200, v193, 8, v199\n\t"
      "s_and_b32 s34, s24, 1\n\t"
      "s_mul_i32 s35, s34, 28672\n\t"
      "v_add_u32_e32 v160, s35, v200\n\t"
      "v_xor_b32_e32 v161, 64, v160\n\t"
      "v_and_b32_e32 v199, 0x70, v199\n\t"
      "v_lshl_or_b32 v199, v193, 7, v199\n\t"
      "v_add_u32_e32 v199, 57344, v199\n\t"
      "v_add_u32_e32 v200, 14336, v200\n\t"
      "s_cmp_eq_u32 s34, 1\n\t"
      "s_cselect_b64 vcc, -1, 0\n\t"
      "s_cselect_b32 s35, 0, 6656\n\t"
      "v_cndmask_b32_e32 v162, v199, v200, vcc\n\t"
      "v_xor_b32_e32 v163, 64, v162\n\t"
      "v_subrev_u32_e32 v164, s35, v162\n\t"
      "v_subrev_u32_e32 v165, s35, v163\n\t"
      "s_bfe_u32 s35, s24, 65537\n\t"
      "s_mul_i32 s35, s35, 7\n\t"
      "v_and_b32_e32 v201, 24, v192\n\t"
      "v_lshlrev_b32_e32 v201, 2, v201\n\t"
      "v_lshlrev_b32_e32 v202, 7, v195\n\t"
      "v_lshl_or_b32 v202, v193, 12, v202\n\t"
      "v_add_u32_e32 v202, 65536, v202\n\t"
      "v_add_lshl_u32 v199, v196, s35, 3\n\t"
      "v_xor_b32_e32 v199, v199, v201\n\t"
      "v_add_u32_e32 v166, v199, v202\n\t"
      "v_min_u32_e32 v199, 2, v196\n\t"
      "s_add_u32 s35, s35, 4\n\t"
      "v_add_lshl_u32 v199, v199, s35, 3\n\t"
      "v_xor_b32_e32 v199, v199, v201\n\t"
      "v_add_u32_e32 v167, v199, v202\n\t"
      "v_add_u32_e32 v168, 0x10000, v166\n\t"
      "v_add_u32_e32 v169, 0x10000, v167\n\t"
      "v_lshlrev_b32_e32 v199, 11, v193\n\t"
      "v_sub_u32_e32 v170, v166, v199\n\t"
      "v_sub_u32_e32 v171, v167, v199\n\t"
      "s_branch NORM_%=\n\t"
      "LSET_%=:\n\t"
      "s_add_u32 s60, s20, 16777216\n\t"
      "s_addc_u32 s61, s21, 0\n\t"
      "s_load_dwordx2 s[22:23], s[60:61], 0x0\n\t"
      "s_lshr_b32 s34, s39, 9\n\t"
      "v_add_u32_e32 v199, s34, v197\n\t"
      "s_and_b32 s34, s24, 1\n\t"
      "s_lshl_b32 s34, s34, 2\n\t"
      "v_and_b32_e32 v200, 2, v197\n\t"
      "v_or_b32_e32 v200, s34, v200\n\t"
      "v_xor_b32_e32 v200, v200, v198\n\t"
      "v_min_u32_e32 v200, 6, v200\n\t"
      "v_lshlrev_b32_e32 v200, 4, v200\n\t"
      "s_movk_i32 s34, 2688\n\t"
      "v_mad_u32_u24 v149, v199, s34, v200\n\t"
      "v_add_u32_e32 v148, 128, v149\n\t"
      "v_add_u32_e32 v150, 84096, v149\n\t"
      "v_add_u32_e32 v151, 83968, v149\n\t"
      "v_add_u32_e32 v152, 85120, v149\n\t"
      "s_sub_u32 s6, s6, 128\n\t"
      "s_subb_u32 s7, s7, 0\n\t"
      "s_mul_i32 s37, s26, 112\n\t"
      "s_mov_b32 s81, s33\n\t"
      "s_mov_b32 s82, s33\n\t"
      "s_lshr_b32 s35, s2, 6\n\t"
      "s_lshl2_add_u32 s31, s26, s35\n\t"
      "s_and_b32 s35, s2, 7\n\t"
      "s_mul_i32 s34, s35, 14\n\t"
      "s_lshl4_add_u32 s35, s35, -14\n\t"
      "s_cmp_lt_u32 s31, 14\n\t"
      "s_cselect_b32 s34, s34, s35\n\t"
      "s_cselect_b32 s36, 1, 0\n\t"
      "s_or_b32 s27, s27, s36\n\t"
      "s_add_u32 s34, s34, s31\n\t"
      "s_lshr_b32 s29, s34, 2\n\t"
      "s_and_b32 s30, s34, 3\n\t"
      "s_mul_i32 s30, s30, 7\n\t"
      "v_and_b32_e32 v199, 60, v192\n\t"
      "v_and_b32_e32 v200, 24, v192\n\t"
      "v_lshlrev_b32_e32 v202, 3, v196\n\t"
      "v_lshl_or_b32 v202, v200, 2, v202\n\t"
      "v_lshl_or_b32 v202, v195, 7, v202\n\t"
      "v_lshl_or_b32 v202, v193, 10, v202\n\t"
      "v_add_u32_e32 v224, s39, v202\n\t"
      "v_xor_b32_e32 v225, 32, v224\n\t"
      "v_xor_b32_e32 v226, 64, v224\n\t"
      "v_xor_b32_e32 v227, 96, v224\n\t"
      "s_lshl_b32 s34, s24, 12\n\t"
      "s_add_u32 s34, s34, 98304\n\t"
      "v_add_u32_e32 v245, s34, v244\n\t"
      "v_lshlrev_b32_e32 v199, 2, v199\n\t"
      "s_lshl_b32 s34, s24, 8\n\t"
      "v_lshl_add_u32 v247, v192, 2, s34\n\t"
      "v_and_b32_e32 v200, 2, v192\n\t"
      "v_mul_u32_u24_e32 v202, 576, v200\n\t"
      "v_xor_b32_e32 v199, v202, v199\n\t"
      "v_and_b32_e32 v202, 1, v192\n\t"
      "v_lshl_or_b32 v199, v202, 3, v199\n\t"
      "s_and_b32 s35, s34, 0x400\n\t"
      "s_add_u32 s34, s34, s35\n\t"
      "s_add_u32 s34, s34, 98304\n\t"
      "v_add_u32_e32 v246, s34, v199\n\t"
      "v_lshlrev_b32_e32 v199, 2, v202\n\t"
      "v_lshl_or_b32 v199, v200, 4, v199\n\t"
      "s_min_u32 s34, s29, 27\n\t"
      "s_lshl_b32 s34, s34, 7\n\t"
      "s_lshr_b32 s35, s24, 2\n\t"
      "s_lshl_b32 s35, s35, 6\n\t"
      "s_add_u32 s34, s34, s35\n\t"
      "s_lshr_b32 s35, s39, 9\n\t"
      "s_add_u32 s34, s34, s35\n\t"
      "v_add_u32_e32 v248, s34, v199\n\t"
      "s_add_u32 s48, s20, 8388608\n\t"
      "s_addc_u32 s49, s21, 0\n\t"
      "v_and_b32_e32 v199, 2, v197\n\t"
      "v_xor_b32_e32 v199, v199, v198\n\t"
      "v_lshlrev_b32_e32 v199, 4, v199\n\t"
      "s_movk_i32 s34, 7168\n\t"
      "v_mad_u32_u24 v144, v197, s34, v199\n\t"
      "v_xor_b32_e32 v199, 64, v144\n\t"
      "v_add_u32_e32 v145, 56320, v199\n\t"
      "v_add_u32_e32 v146, 112640, v144\n\t"
      "v_add_u32_e32 v147, 168960, v199\n\t"
      "v_and_b32_e32 v199, 6, v197\n\t"
      "v_xor_b32_e32 v199, v199, v198\n\t"
      "v_lshlrev_b32_e32 v199, 4, v199\n\t"
      "s_movk_i32 s35, 896\n\t"
      "v_mad_u32_u24 v154, v197, s35, v199\n\t"
      "v_add_u32_e32 v155, 6144, v154\n\t"
      "v_add_u32_e32 v156, 12288, v154\n\t"
      "v_add_u32_e32 v157, 18432, v154\n\t"
      "s_mul_i32 s34, s24, 917504\n\t"
      "s_lshl_b32 s35, s29, 7\n\t"
      "s_add_u32 s34, s34, s35\n\t"
      "s_add_u32 s52, s10, s34\n\t"
      "s_addc_u32 s53, s11, 0\n\t"
      "s_lshl_b32 s34, s28, 7\n\t"
      "s_add_u32 s56, s8, s34\n\t"
      "s_addc_u32 s57, s9, 0\n\t"
      "s_mov_b32 s34, 0\n\t"
      "s_and_b32 s35, s34, 1\n\t"
      "s_and_b32 s34, s34, 2\n\t"
      "s_lshl_b32 s34, s34, 6\n\t"
      "s_add_u32 s34, s34, s35\n\t"
      "s_mul_i32 s76, s34, 3584\n\t"
      "s_mov_b32 s34, 1\n\t"
      "s_and_b32 s35, s34, 1\n\t"
      "s_and_b32 s34, s34, 2\n\t"
      "s_lshl_b32 s34, s34, 6\n\t"
      "s_add_u32 s34, s34, s35\n\t"
      "s_mul_i32 s77, s34, 3584\n\t"
      "s_mov_b32 s34, 2\n\t"
      "s_and_b32 s35, s34, 1\n\t"
      "s_and_b32 s34, s34, 2\n\t"
      "s_lshl_b32 s34, s34, 6\n\t"
      "s_add_u32 s34, s34, s35\n\t"
      "s_mul_i32 s78, s34, 3584\n\t"
      "s_mov_b32 s34, 3\n\t"
      "s_and_b32 s35, s34, 1\n\t"
      "s_and_b32 s34, s34, 2\n\t"
      "s_lshl_b32 s34, s34, 6\n\t"
      "s_add_u32 s34, s34, s35\n\t"
      "s_mul_i32 s79, s34, 3584\n\t"
      "NORM_%=:\n\t"
      "global_load_dwordx4 v[24:27], v126, s[4:5]\n\t"
      "global_load_dwordx4 v[28:31], v127, s[4:5]\n\t"
      "v_mov_b32_e32 v199, 0x3f803f80\n\t"
      "s_waitcnt vmcnt(7)\n\t"
      "v_dot2_f32_bf16 v88, v0, v199, 0\n\t"
      "v_dot2_f32_bf16 v92, v0, v0, 0\n\t"
      "v_lshlrev_b32_e32 v32, 16, v0\n\t"
      "v_and_b32_e32 v33, 0xffff0000, v0\n\t"
      "v_dot2_f32_bf16 v89, v1, v199, 0\n\t"
      "v_dot2_f32_bf16 v93, v1, v1, 0\n\t"
      "v_lshlrev_b32_e32 v34, 16, v1\n\t"
      "v_and_b32_e32 v35, 0xffff0000, v1\n\t"
      "v_dot2_f32_bf16 v90, v2, v199, 0\n\t"
      "v_dot2_f32_bf16 v94, v2, v2, 0\n\t"
      "v_lshlrev_b32_e32 v36, 16, v2\n\t"
      "v_and_b32_e32 v37, 0xffff0000, v2\n\t"
      "v_dot2_f32_bf16 v91, v3, v199, 0\n\t"
      "v_dot2_f32_bf16 v95, v3, v3, 0\n\t"
      "v_lshlrev_b32_e32 v38, 16, v3\n\t"
      "v_and_b32_e32 v39, 0xffff0000, v3\n\t"
      "s_waitcnt vmcnt(6)\n\t"
      "v_dot2_f32_bf16 v88, v4, v199, v88\n\t"
      "v_dot2_f32_bf16 v92, v4, v4, v92\n\t"
      "v_lshlrev_b32_e32 v40, 16, v4\n\t"
      "v_and_b32_e32 v41, 0xffff0000, v4\n\t"
      "v_dot2_f32_bf16 v89, v5, v199, v89\n\t"
      "v_dot2_f32_bf16 v93, v5, v5, v93\n\t"
      "v_lshlrev_b32_e32 v42, 16, v5\n\t"
      "v_and_b32_e32 v43, 0xffff0000, v5\n\t"
      "v_dot2_f32_bf16 v90, v6, v199, v90\n\t"
      "v_dot2_f32_bf16 v94, v6, v6, v94\n\t"
      "v_lshlrev_b32_e32 v44, 16, v6\n\t"
      "v_and_b32_e32 v45, 0xffff0000, v6\n\t"
      "v_dot2_f32_bf16 v91, v7, v199, v91\n\t"
      "v_dot2_f32_bf16 v95, v7, v7, v95\n\t"
      "v_lshlrev_b32_e32 v46, 16, v7\n\t"
      "v_and_b32_e32 v47, 0xffff0000, v7\n\t"
      "s_waitcnt vmcnt(5)\n\t"
      "v_dot2_f32_bf16 v88, v8, v199, v88\n\t"
      "v_dot2_f32_bf16 v92, v8, v8, v92\n\t"
      "v_lshlrev_b32_e32 v48, 16, v8\n\t"
      "v_and_b32_e32 v49, 0xffff0000, v8\n\t"
      "v_dot2_f32_bf16 v89, v9, v199, v89\n\t"
      "v_dot2_f32_bf16 v93, v9, v9, v93\n\t"
      "v_lshlrev_b32_e32 v50, 16, v9\n\t"
      "v_and_b32_e32 v51, 0xffff0000, v9\n\t"
      "v_dot2_f32_bf16 v90, v10, v199, v90\n\t"
      "v_dot2_f32_bf16 v94, v10, v10, v94\n\t"
      "v_lshlrev_b32_e32 v52, 16, v10\n\t"
      "v_and_b32_e32 v53, 0xffff0000, v10\n\t"
      "v_dot2_f32_bf16 v91, v11, v199, v91\n\t"
      "v_dot2_f32_bf16 v95, v11, v11, v95\n\t"
      "v_lshlrev_b32_e32 v54, 16, v11\n\t"
      "v_and_b32_e32 v55, 0xffff0000, v11\n\t"
      "s_waitcnt vmcnt(4)\n\t"
      "v_dot2_f32_bf16 v88, v12, v199, v88\n\t"
      "v_dot2_f32_bf16 v92, v12, v12, v92\n\t"
      "v_lshlrev_b32_e32 v56, 16, v12\n\t"
      "v_and_b32_e32 v57, 0xffff0000, v12\n\t"
      "v_dot2_f32_bf16 v89, v13, v199, v89\n\t"
      "v_dot2_f32_bf16 v93, v13, v13, v93\n\t"
      "v_lshlrev_b32_e32 v58, 16, v13\n\t"
      "v_and_b32_e32 v59, 0xffff0000, v13\n\t"
      "v_dot2_f32_bf16 v90, v14, v199, v90\n\t"
      "v_dot2_f32_bf16 v94, v14, v14, v94\n\t"
      "v_lshlrev_b32_e32 v60, 16, v14\n\t"
      "v_and_b32_e32 v61, 0xffff0000, v14\n\t"
      "v_dot2_f32_bf16 v91, v15, v199, v91\n\t"
      "v_dot2_f32_bf16 v95, v15, v15, v95\n\t"
      "v_lshlrev_b32_e32 v62, 16, v15\n\t"
      "v_and_b32_e32 v63, 0xffff0000, v15\n\t"
      "s_waitcnt vmcnt(3)\n\t"
      "v_dot2_f32_bf16 v88, v16, v199, v88\n\t"
      "v_dot2_f32_bf16 v92, v16, v16, v92\n\t"
      "v_lshlrev_b32_e32 v64, 16, v16\n\t"
      "v_and_b32_e32 v65, 0xffff0000, v16\n\t"
      "v_dot2_f32_bf16 v89, v17, v199, v89\n\t"
      "v_dot2_f32_bf16 v93, v17, v17, v93\n\t"
      "v_lshlrev_b32_e32 v66, 16, v17\n\t"
      "v_and_b32_e32 v67, 0xffff0000, v17\n\t"
      "v_dot2_f32_bf16 v90, v18, v199, v90\n\t"
      "v_dot2_f32_bf16 v94, v18, v18, v94\n\t"
      "v_lshlrev_b32_e32 v68, 16, v18\n\t"
      "v_and_b32_e32 v69, 0xffff0000, v18\n\t"
      "v_dot2_f32_bf16 v91, v19, v199, v91\n\t"
      "v_dot2_f32_bf16 v95, v19, v19, v95\n\t"
      "v_lshlrev_b32_e32 v70, 16, v19\n\t"
      "v_and_b32_e32 v71, 0xffff0000, v19\n\t"
      "s_waitcnt vmcnt(2)\n\t"
      "v_dot2_f32_bf16 v88, v20, v199, v88\n\t"
      "v_dot2_f32_bf16 v92, v20, v20, v92\n\t"
      "v_lshlrev_b32_e32 v72, 16, v20\n\t"
      "v_and_b32_e32 v73, 0xffff0000, v20\n\t"
      "v_dot2_f32_bf16 v89, v21, v199, v89\n\t"
      "v_dot2_f32_bf16 v93, v21, v21, v93\n\t"
      "v_lshlrev_b32_e32 v74, 16, v21\n\t"
      "v_and_b32_e32 v75, 0xffff0000, v21\n\t"
      "v_dot2_f32_bf16 v90, v22, v199, v90\n\t"
      "v_dot2_f32_bf16 v94, v22, v22, v94\n\t"
      "v_lshlrev_b32_e32 v76, 16, v22\n\t"
      "v_and_b32_e32 v77, 0xffff0000, v22\n\t"
      "v_dot2_f32_bf16 v91, v23, v199, v91\n\t"
      "v_dot2_f32_bf16 v95, v23, v23, v95\n\t"
      "v_lshlrev_b32_e32 v78, 16, v23\n\t"
      "v_and_b32_e32 v79, 0xffff0000, v23\n\t"
      "s_waitcnt vmcnt(1)\n\t"
      "v_dot2_f32_bf16 v88, v24, v199, v88\n\t"
      "v_dot2_f32_bf16 v92, v24, v24, v92\n\t"
      "v_lshlrev_b32_e32 v80, 16, v24\n\t"
      "v_and_b32_e32 v81, 0xffff0000, v24\n\t"
      "v_dot2_f32_bf16 v89, v25, v199, v89\n\t"
      "v_dot2_f32_bf16 v93, v25, v25, v93\n\t"
      "v_lshlrev_b32_e32 v82, 16, v25\n\t"
      "v_and_b32_e32 v83, 0xffff0000, v25\n\t"
      "v_dot2_f32_bf16 v90, v26, v199, v90\n\t"
      "v_dot2_f32_bf16 v94, v26, v26, v94\n\t"
      "v_lshlrev_b32_e32 v84, 16, v26\n\t"
      "v_and_b32_e32 v85, 0xffff0000, v26\n\t"
      "v_dot2_f32_bf16 v91, v27, v199, v91\n\t"
      "v_dot2_f32_bf16 v95, v27, v27, v95\n\t"
      "v_lshlrev_b32_e32 v86, 16, v27\n\t"
      "v_and_b32_e32 v87, 0xffff0000, v27\n\t"
      "s_waitcnt vmcnt(0)\n\t"
      "s_bitcmp0_b32 s27, 2\n\t"
      "s_cbranch_scc1 SK1_%=\n\t"
      "s_mul_i32 s72, s81, 172032\n\t"
      "s_add_u32 s72, s72, s37\n\t"
      "s_add_u32 s40, s6, s72\n\t"
      "s_addc_u32 s41, s7, 0\n\t"
      "s_add_u32 s81, s81, 1\n\t"
      "s_cmp_eq_u32 s81, 7\n\t"
      "s_cselect_b32 s81, 0, s81\n\t"
      "s_add_u32 m0, s39, 131072\n\t"
      "s_nop 0\n\t"
      "global_load_lds_dwordx4 v148, s[40:41]\n\t"
      "global_load_lds_dwordx4 v149, s[40:41] offset:1024\n\t"
      "global_load_lds_dwordx4 v150, s[40:41] offset:2048\n\t"
      "global_load_lds_dwordx4 v151, s[40:41] offset:3072\n\t"
      "SK1_%=:\n\t"
      "v_dot2_f32_bf16 v96, v28, v199, 0\n\t"
      "v_dot2_f32_bf16 v97, v28, v28, 0\n\t"
      "v_dot2_f32_bf16 v96, v29, v199, v96\n\t"
      "v_dot2_f32_bf16 v97, v29, v29, v97\n\t"
      "v_dot2_f32_bf16 v96, v30, v199, v96\n\t"
      "v_dot2_f32_bf16 v97, v30, v30, v97\n\t"
      "v_dot2_f32_bf16 v96, v31, v199, v96\n\t"
      "v_dot2_f32_bf16 v97, v31, v31, v97\n\t"
      "v_cmp_gt_u32_e32 vcc, 56, v192\n\t"
      "v_pk_add_f32 v[88:89], v[88:89], v[90:91]\n\t"
      "v_pk_add_f32 v[92:93], v[92:93], v[94:95]\n\t"
      "v_cndmask_b32_e32 v96, 0, v96, vcc\n\t"
      "v_cndmask_b32_e32 v97, 0, v97, vcc\n\t"
      "v_add_f32_e32 v88, v88, v89\n\t"
      "v_add_f32_e32 v92, v92, v93\n\t"
      "v_add_f32_dpp v96, v96, v96 quad_perm:[1,0,3,2] row_mask:0xf bank_mask:0xf\n\t"
      "v_add_f32_dpp v97, v97, v97 quad_perm:[1,0,3,2] row_mask:0xf bank_mask:0xf\n\t"
      "v_add_f32_dpp v88, v88, v88 quad_perm:[1,0,3,2] row_mask:0xf bank_mask:0xf\n\t"
      "v_add_f32_dpp v92, v92, v92 quad_perm:[1,0,3,2] row_mask:0xf bank_mask:0xf\n\t"
      "v_add_f32_dpp v96, v96, v96 quad_perm:[2,3,0,1] row_mask:0xf bank_mask:0xf\n\t"
      "v_add_f32_dpp v97, v97, v97 quad_perm:[2,3,0,1] row_mask:0xf bank_mask:0xf\n\t"
      "v_add_f32_dpp v88, v88, v88 quad_perm:[2,3,0,1] row_mask:0xf bank_mask:0xf\n\t"
      "v_add_f32_dpp v92, v92, v92 quad_perm:[2,3,0,1] row_mask:0xf bank_mask:0xf\n\t"
      "v_add_f32_dpp v96, v96, v96 row_half_mirror row_mask:0xf bank_mask:0xf\n\t"
      "v_add_f32_dpp v97, v97, v97 row_half_mirror row_mask:0xf bank_mask:0xf\n\t"
      "v_add_f32_dpp v88, v88, v88 row_half_mirror row_mask:0xf bank_mask:0xf\n\t"
      "v_add_f32_dpp v92, v92, v92 row_half_mirror row_mask:0xf bank_mask:0xf\n\t"
      "v_mul_f32_e32 v88, 0xbb124925, v88\n\t"
      "v_add_f32_dpp v96, v96, v96 row_mirror row_mask:0xf bank_mask:0xf\n\t"
      "v_mul_f32_e32 v92, 0x3b124925, v92\n\t"
      "s_bitcmp0_b32 s27, 2\n\t"
      "s_cbranch_scc1 SK2_%=\n\t"
      "s_mul_i32 s72, s81, 172032\n\t"
      "s_add_u32 s72, s72, s37\n\t"
      "s_add_u32 s40, s6, s72\n\t"
      "s_addc_u32 s41, s7, 0\n\t"
      "s_add_u32 s81, s81, 1\n\t"
      "s_cmp_eq_u32 s81, 7\n\t"
      "s_cselect_b32 s81, 0, s81\n\t"
      "s_add_u32 m0, s39, 147456\n\t"
      "s_nop 0\n\t"
      "global_load_lds_dwordx4 v148, s[40:41]\n\t"
      "global_load_lds_dwordx4 v149, s[40:41] offset:1024\n\t"
      "SK2_%=:\n\t"
      "v_add_f32_dpp v97, v97, v97 row_mirror row_mask:0xf bank_mask:0xf\n\t"
      "v_fma_f32 v92, -v88, v88, v92\n\t"
      "s_nop 0\n\t"
      "v_permlane16_swap_b32_e32 v96, v97\n\t"
      "v_max_f32_e32 v92, 0, v92\n\t"
      "v_add_f32_e32 v96, v96, v97\n\t"
      "v_add_f32_e32 v92, 0x3727c5ac, v92\n\t"
      "v_mov_b32_e32 v97, v96\n\t"
      "v_rsq_f32_e32 v92, v92\n\t"
      "s_nop 0\n\t"
      "v_permlane32_swap_b32_e32 v96, v97\n\t"
      "v_add_f32_e32 v96, v96, v97\n\t"
      "v_mul_f32_e32 v88, v88, v92\n\t"
      "v_mov_b32_e32 v97, v96\n\t"
      "v_pk_fma_f32 v[32:33], v[32:33], v[92:93], v[88:89] op_sel_hi:[1,0,0]\n\t"
      "v_pk_fma_f32 v[34:35], v[34:35], v[92:93], v[88:89] op_sel_hi:[1,0,0]\n\t"
      "v_permlane16_swap_b32_e32 v96, v97\n\t"
      "v_pk_fma_f32 v[36:37], v[36:37], v[92:93], v[88:89] op_sel_hi:[1,0,0]\n\t"
      "v_pk_fma_f32 v[38:39], v[38:39], v[92:93], v[88:89] op_sel_hi:[1,0,0]\n\t"
      "v_mul_f32_e32 v96, 0xbb124925, v96\n\t"
      "v_cvt_pk_bf16_f32 v0, v32, v33\n\t"
      "v_cvt_pk_bf16_f32 v1, v34, v35\n\t"
      "v_cvt_pk_bf16_f32 v2, v36, v37\n\t"
      "v_cvt_pk_bf16_f32 v3, v38, v39\n\t"
      "ds_write_b128 v112, v[0:3]\n\t"
      "v_pk_fma_f32 v[40:41], v[40:41], v[92:93], v[88:89] op_sel_hi:[1,0,0]\n\t"
      "v_pk_fma_f32 v[42:43], v[42:43], v[92:93], v[88:89] op_sel_hi:[1,0,0]\n\t"
      "v_mul_f32_e32 v97, 0x3b124925, v97\n\t"
      "v_pk_fma_f32 v[44:45], v[44:45], v[92:93], v[88:89] op_sel_hi:[1,0,0]\n\t"
      "v_pk_fma_f32 v[46:47], v[46:47], v[92:93], v[88:89] op_sel_hi:[1,0,0]\n\t"
      "v_fma_f32 v97, -v96, v96, v97\n\t"
      "v_cvt_pk_bf16_f32 v4, v40, v41\n\t"
      "s_bitcmp0_b32 s27, 2\n\t"
      "s_cbranch_scc1 SK3_%=\n\t"
      "global_load_lds_dwordx4 v150, s[40:41] offset:2048\n\t"
      "global_load_lds_dwordx4 v151, s[40:41] offset:3072\n\t"
      "SK3_%=:\n\t"
      "v_cvt_pk_bf16_f32 v5, v42, v43\n\t"
      "v_cvt_pk_bf16_f32 v6, v44, v45\n\t"
      "v_cvt_pk_bf16_f32 v7, v46, v47\n\t"
      "ds_write_b128 v113, v[4:7]\n\t"
      "v_pk_fma_f32 v[48:49], v[48:49], v[92:93], v[88:89] op_sel_hi:[1,0,0]\n\t"
      "v_pk_fma_f32 v[50:51], v[50:51], v[92:93], v[88:89] op_sel_hi:[1,0,0]\n\t"
      "v_max_f32_e32 v97, 0, v97\n\t"
      "v_pk_fma_f32 v[52:53], v[52:53], v[92:93], v[88:89] op_sel_hi:[1,0,0]\n\t"
      "v_pk_fma_f32 v[54:55], v[54:55], v[92:93], v[88:89] op_sel_hi:[1,0,0]\n\t"
      "v_add_f32_e32 v97, 0x3727c5ac, v97\n\t"
      "v_cvt_pk_bf16_f32 v8, v48, v49\n\t"
      "v_cvt_pk_bf16_f32 v9, v50, v51\n\t"
      "v_cvt_pk_bf16_f32 v10, v52, v53\n\t"
      "v_cvt_pk_bf16_f32 v11, v54, v55\n\t"
      "ds_write_b128 v114, v[8:11]\n\t"
      "v_pk_fma_f32 v[56:57], v[56:57], v[92:93], v[88:89] op_sel_hi:[1,0,0]\n\t"
      "v_pk_fma_f32 v[58:59], v[58:59], v[92:93], v[88:89] op_sel_hi:[1,0,0]\n\t"
      "v_rsq_f32_e32 v97, v97\n\t"
      "v_pk_fma_f32 v[60:61], v[60:61], v[92:93], v[88:89] op_sel_hi:[1,0,0]\n\t"
      "v_pk_fma_f32 v[62:63], v[62:63], v[92:93], v[88:89] op_sel_hi:[1,0,0]\n\t"
      "v_cvt_pk_bf16_f32 v12, v56, v57\n\t"
      "v_cvt_pk_bf16_f32 v13, v58, v59\n\t"
      "v_cvt_pk_bf16_f32 v14, v60, v61\n\t"
      "v_cvt_pk_bf16_f32 v15, v62, v63\n\t"
      "ds_write_b128 v115, v[12:15]\n\t"
      "v_pk_fma_f32 v[64:65], v[64:65], v[92:93], v[88:89] op_sel_hi:[1,0,0]\n\t"
      "v_pk_fma_f32 v[66:67], v[66:67], v[92:93], v[88:89] op_sel_hi:[1,0,0]\n\t"
      "v_mul_f32_e32 v96, v96, v97\n\t"
      "v_pk_fma_f32 v[68:69], v[68:69], v[92:93], v[88:89] op_sel_hi:[1,0,0]\n\t"
      "v_pk_fma_f32 v[70:71], v[70:71], v[92:93], v[88:89] op_sel_hi:[1,0,0]\n\t"
      "s_bitcmp0_b32 s27, 2\n\t"
      "s_cbranch_scc1 SK4_%=\n\t"
      "s_mul_i32 s72, s82, 172032\n\t"
      "s_add_u32 s72, s72, s37\n\t"
      "s_add_u32 s72, s72, 1792\n\t"
      "s_add_u32 s40, s6, s72\n\t"
      "s_addc_u32 s41, s7, 0\n\t"
      "s_add_u32 s82, s82, 1\n\t"
      "s_cmp_eq_u32 s82, 7\n\t"
      "s_cselect_b32 s82, 0, s82\n\t"
      "s_add_u32 m0, s39, 65536\n\t"
      "s_nop 0\n\t"
      "global_load_lds_dwordx4 v148, s[40:41]\n\t"
      "global_load_lds_dwordx4 v152, s[40:41] offset:1024\n\t"
      "SK4_%=:\n\t"
      "v_cvt_pk_bf16_f32 v16, v64, v65\n\t"
      "v_cvt_pk_bf16_f32 v17, v66, v67\n\t"
      "v_cvt_pk_bf16_f32 v18, v68, v69\n\t"
      "v_cvt_pk_bf16_f32 v19, v70, v71\n\t"
      "ds_write_b128 v116, v[16:19]\n\t"
      "v_pk_fma_f32 v[72:73], v[72:73], v[92:93], v[88:89] op_sel_hi:[1,0,0]\n\t"
      "v_pk_fma_f32 v[74:75], v[74:75], v[92:93], v[88:89] op_sel_hi:[1,0,0]\n\t"
      "v_pk_fma_f32 v[76:77], v[76:77], v[92:93], v[88:89] op_sel_hi:[1,0,0]\n\t"
      "v_pk_fma_f32 v[78:79], v[78:79], v[92:93], v[88:89] op_sel_hi:[1,0,0]\n\t"
      "v_cvt_pk_bf16_f32 v20, v72, v73\n\t"
      "v_cvt_pk_bf16_f32 v21, v74, v75\n\t"
      "v_cvt_pk_bf16_f32 v22, v76, v77\n\t"
      "v_cvt_pk_bf16_f32 v23, v78, v79\n\t"
      "ds_write_b128 v117, v[20:23]\n\t"
      "v_pk_fma_f32 v[80:81], v[80:81], v[92:93], v[88:89] op_sel_hi:[1,0,0]\n\t"
      "v_pk_fma_f32 v[82:83], v[82:83], v[92:93], v[88:89] op_sel_hi:[1,0,0]\n\t"
      "v_pk_fma_f32 v[84:85], v[84:85], v[92:93], v[88:89] op_sel_hi:[1,0,0]\n\t"
      "v_pk_fma_f32 v[86:87], v[86:87], v[92:93], v[88:89] op_sel_hi:[1,0,0]\n\t"
      "v_cvt_pk_bf16_f32 v24, v80, v81\n\t"
      "v_cvt_pk_bf16_f32 v25, v82, v83\n\t"
      "v_cvt_pk_bf16_f32 v26, v84, v85\n\t"
      "v_cvt_pk_bf16_f32 v27, v86, v87\n\t"
      "ds_write_b128 v118, v[24:27]\n\t"
      "v_lshlrev_b32_e32 v32, 16, v28\n\t"
      "v_and_b32_e32 v33, 0xffff0000, v28\n\t"
      "v_lshlrev_b32_e32 v34, 16, v29\n\t"
      "v_and_b32_e32 v35, 0xffff0000, v29\n\t"
      "v_lshlrev_b32_e32 v36, 16, v30\n\t"
      "v_and_b32_e32 v37, 0xffff0000, v30\n\t"
      "v_lshlrev_b32_e32 v38, 16, v31\n\t"
      "s_bitcmp0_b32 s27, 2\n\t"
      "s_cbranch_scc1 SK5_%=\n\t"
      "s_mul_i32 s72, s82, 172032\n\t"
      "s_add_u32 s72, s72, s37\n\t"
      "s_add_u32 s72, s72, 1792\n\t"
      "s_add_u32 s40, s6, s72\n\t"
      "s_addc_u32 s41, s7, 0\n\t"
      "s_add_u32 s82, s82, 1\n\t"
      "s_cmp_eq_u32 s82, 7\n\t"
      "s_cselect_b32 s82, 0, s82\n\t"
      "s_add_u32 m0, s39, 67584\n\t"
      "s_nop 0\n\t"
      "global_load_lds_dwordx4 v148, s[40:41]\n\t"
      "global_load_lds_dwordx4 v152, s[40:41] offset:1024\n\t"
      "SK5_%=:\n\t"
      "v_and_b32_e32 v39, 0xffff0000, v31\n\t"
      "v_pk_fma_f32 v[32:33], v[32:33], v[96:97], v[96:97] op_sel:[0,1,0] op_sel_hi:[1,1,0]\n\t"
      "v_pk_fma_f32 v[34:35], v[34:35], v[96:97], v[96:97] op_sel:[0,1,0] op_sel_hi:[1,1,0]\n\t"
      "v_pk_fma_f32 v[36:37], v[36:37], v[96:97], v[96:97] op_sel:[0,1,0] op_sel_hi:[1,1,0]\n\t"
      "v_pk_fma_f32 v[38:39], v[38:39], v[96:97], v[96:97] op_sel:[0,1,0] op_sel_hi:[1,1,0]\n\t"
      "v_cvt_pk_bf16_f32 v28, v32, v33\n\t"
      "v_cvt_pk_bf16_f32 v29, v34, v35\n\t"
      "v_cvt_pk_bf16_f32 v30, v36, v37\n\t"
      "v_cvt_pk_bf16_f32 v31, v38, v39\n\t"
      "ds_write_b128 v119, v[28:31]\n\t"
      "s_waitcnt lgkmcnt(0)\n\t"
      "s_bitcmp1_b32 s27, 2\n\t"
      "s_cbranch_scc1 LPATH_%=\n\t"
      "s_setprio 2\n\t"
      "s_barrier\n\t"
      "ds_read_b64_tr_b16 v[76:77], v168\n\t"
      "ds_read_b64_tr_b16 v[78:79], v168 offset:512\n\t"
      "ds_read_b128 v[68:71], v160\n\t"
      "ds_read_b128 v[72:75], v160 offset:14336\n\t"
      "ds_read_b64_tr_b16 v[80:81], v169\n\t"
      "ds_read_b64_tr_b16 v[82:83], v169 offset:512\n\t"
      "ds_read_b64_tr_b16 v[84:85], v168 offset:1024\n\t"
      "ds_read_b64_tr_b16 v[86:87], v168 offset:1536\n\t"
      "ds_read_b128 v[64:67], v162\n\t"
      "ds_read_b64_tr_b16 v[88:89], v169 offset:1024\n\t"
      "ds_read_b64_tr_b16 v[90:91], v169 offset:1536\n\t"
      "ds_read_b64_tr_b16 v[104:105], v168 offset:2048\n\t"
      "ds_read_b64_tr_b16 v[106:107], v168 offset:2560\n\t"
      "ds_read_b128 v[96:99], v161 offset:1024\n\t"
      "ds_read_b128 v[100:103], v161 offset:15360\n\t"
      "ds_read_b64_tr_b16 v[108:109], v169 offset:2048\n\t"
      "ds_read_b64_tr_b16 v[110:111], v169 offset:2560\n\t"
      "ds_read_b64_tr_b16 v[112:113], v168 offset:3072\n\t"
      "ds_read_b64_tr_b16 v[114:115], v168 offset:3584\n\t"
      "ds_read_b128 v[92:95], v163 offset:1024\n\t"
      "ds_read_b64_tr_b16 v[116:117], v169 offset:3072\n\t"
      "ds_read_b64_tr_b16 v[118:119], v169 offset:3584\n\t"
      "s_waitcnt vmcnt(0)\n\t"
      "s_barrier\n\t"
      "s_waitcnt lgkmcnt(14)\n\t"
      "v_mfma_f32_16x16x32_bf16 v[0:3], v[76:79], v[68:71], 0\n\t"
      "ds_read_b64_tr_b16 v[132:133], v168 offset:16384\n\t"
      "ds_read_b64_tr_b16 v[134:135], v168 offset:16896\n\t"
      "v_mfma_f32_16x16x32_bf16 v[8:11], v[76:79], v[72:75], 0\n\t"
      "ds_read_b128 v[124:127], v160 offset:2048\n\t"
      "v_mfma_f32_16x16x32_bf16 v[4:7], v[80:83], v[68:71], 0\n\t"
      "ds_read_b128 v[128:131], v160 offset:16384\n\t"
      "v_mfma_f32_16x16x32_bf16 v[12:15], v[80:83], v[72:75], 0\n\t"
      "ds_read_b64_tr_b16 v[136:137], v169 offset:16384\n\t"
      "v_mfma_f32_16x16x32_bf16 v[24:27], v[84:87], v[68:71], 0\n\t"
      "ds_read_b64_tr_b16 v[138:139], v169 offset:16896\n\t"
      "v_mfma_f32_16x16x32_bf16 v[32:35], v[84:87], v[72:75], 0\n\t"
      "ds_read_b64_tr_b16 v[140:141], v168 offset:17408\n\t"
      "s_waitcnt lgkmcnt(14)\n\t"
      "v_mfma_f32_16x16x32_bf16 v[16:19], v[84:87], v[64:67], 0\n\t"
      "ds_read_b64_tr_b16 v[142:143], v168 offset:17920\n\t"
      "v_mfma_f32_16x16x32_bf16 v[28:31], v[88:91], v[68:71], 0\n\t"
      "ds_read_b128 v[120:123], v162 offset:2048\n\t"
      "v_mfma_f32_16x16x32_bf16 v[36:39], v[88:91], v[72:75], 0\n\t"
      "ds_read_b64_tr_b16 v[144:145], v169 offset:17408\n\t"
      "v_mfma_f32_16x16x32_bf16 v[20:23], v[88:91], v[64:67], 0\n\t"
      "ds_read_b64_tr_b16 v[146:147], v169 offset:17920\n\t"
      "s_waitcnt lgkmcnt(11)\n\t"
      "v_mfma_f32_16x16x32_bf16 v[0:3], v[104:107], v[96:99], v[0:3]\n\t"
      "ds_read_b64_tr_b16 v[76:77], v168 offset:18432\n\t"
      "ds_read_b64_tr_b16 v[78:79], v168 offset:18944\n\t"
      "v_mfma_f32_16x16x32_bf16 v[8:11], v[104:107], v[100:103], v[8:11]\n\t"
      "ds_read_b128 v[68:71], v161 offset:3072\n\t"
      "v_mfma_f32_16x16x32_bf16 v[4:7], v[108:111], v[96:99], v[4:7]\n\t"
      "ds_read_b128 v[72:75], v161 offset:17408\n\t"
      "v_mfma_f32_16x16x32_bf16 v[12:15], v[108:111], v[100:103], v[12:15]\n\t"
      "ds_read_b64_tr_b16 v[80:81], v169 offset:18432\n\t"
      "v_mfma_f32_16x16x32_bf16 v[24:27], v[112:115], v[96:99], v[24:27]\n\t"
      "ds_read_b64_tr_b16 v[82:83], v169 offset:18944\n\t"
      "v_mfma_f32_16x16x32_bf16 v[32:35], v[112:115], v[100:103], v[32:35]\n\t"
      "ds_read_b64_tr_b16 v[84:85], v168 offset:19456\n\t"
      "v_mfma_f32_16x16x32_bf16 v[16:19], v[112:115], v[92:95], v[16:19]\n\t"
      "ds_read_b64_tr_b16 v[86:87], v168 offset:19968\n\t"
      "v_mfma_f32_16x16x32_bf16 v[28:31], v[116:119], v[96:99], v[28:31]\n\t"
      "ds_read_b128 v[64:67], v163 offset:3072\n\t"
      "v_mfma_f32_16x16x32_bf16 v[36:39], v[116:119], v[100:103], v[36:39]\n\t"
      "ds_read_b64_tr_b16 v[88:89], v169 offset:19456\n\t"
      "v_mfma_f32_16x16x32_bf16 v[20:23], v[116:119], v[92:95], v[20:23]\n\t"
      "ds_read_b64_tr_b16 v[90:91], v169 offset:19968\n\t"
      "s_barrier\n\t"
      "s_waitcnt lgkmcnt(11)\n\t"
      "v_mfma_f32_16x16x32_bf16 v[0:3], v[132:135], v[124:127], v[0:3]\n\t"
      "ds_read_b64_tr_b16 v[104:105], v166\n\t"
      "v_lshlrev_b32_e32 v199, 2, v193\n\t"
      "v_mfma_f32_16x16x32_bf16 v[8:11], v[132:135], v[128:131], v[8:11]\n\t"
      "ds_read_b64_tr_b16 v[106:107], v166 offset:512\n\t"
      "v_sub_u32_e32 v199, v194, v199\n\t"
      "v_mfma_f32_16x16x32_bf16 v[4:7], v[136:139], v[124:127], v[4:7]\n\t"
      "ds_read_b128 v[96:99], v160\n\t"
      "v_mov_b32_e32 v200, 0xff800000\n\t"
      "v_mfma_f32_16x16x32_bf16 v[12:15], v[136:139], v[128:131], v[12:15]\n\t"
      "ds_read_b128 v[100:103], v160 offset:14336\n\t"
      "s_cmp_lt_u32 s25, 2\n\t"
      "v_mfma_f32_16x16x32_bf16 v[24:27], v[140:143], v[124:127], v[24:27]\n\t"
      "ds_read_b128 v[92:95], v162\n\t"
      "s_cselect_b32 s35, 0xff800000, 0\n\t"
      "v_mfma_f32_16x16x32_bf16 v[32:35], v[140:143], v[128:131], v[32:35]\n\t"
      "ds_read_b64_tr_b16 v[108:109], v167\n\t"
      "s_bitcmp0_b32 s24, 0\n\t"
      "v_mfma_f32_16x16x32_bf16 v[16:19], v[140:143], v[120:123], v[16:19]\n\t"
      "ds_read_b64_tr_b16 v[110:111], v167 offset:512\n\t"
      "s_cselect_b32 s35, s35, 0\n\t"
      "v_mfma_f32_16x16x32_bf16 v[28:31], v[144:147], v[124:127], v[28:31]\n\t"
      "v_cmp_ge_i32_e32 vcc, -9, v199\n\t"
      "v_mfma_f32_16x16x32_bf16 v[36:39], v[144:147], v[128:131], v[36:39]\n\t"
      "v_subrev_u32_e32 v201, 0, v199\n\t"
      "v_mfma_f32_16x16x32_bf16 v[20:23], v[144:147], v[120:123], v[20:23]\n\t"
      "v_cmp_gt_u32_e64 s[64:65], 8, v201\n\t"
      "s_waitcnt lgkmcnt(7)\n\t"
      "v_mfma_f32_16x16x32_bf16 v[0:3], v[76:79], v[68:71], v[0:3]\n\t"
      "ds_read_b64_tr_b16 v[132:133], v166 offset:1024\n\t"
      "v_cndmask_b32_e64 v172, v200, 0, vcc\n\t"
      "v_mfma_f32_16x16x32_bf16 v[8:11], v[76:79], v[72:75], v[8:11]\n\t"
      "ds_read_b64_tr_b16 v[134:135], v166 offset:1536\n\t"
      "v_cndmask_b32_e64 v176, v200, 0, s[64:65]\n\t"
      "v_mfma_f32_16x16x32_bf16 v[4:7], v[80:83], v[68:71], v[4:7]\n\t"
      "ds_read_b128 v[124:127], v161 offset:1024\n\t"
      "v_or_b32_e32 v180, s35, v172\n\t"
      "v_mfma_f32_16x16x32_bf16 v[12:15], v[80:83], v[72:75], v[12:15]\n\t"
      "ds_read_b128 v[128:131], v161 offset:15360\n\t"
      "v_cmp_ge_i32_e32 vcc, -8, v199\n\t"
      "v_mfma_f32_16x16x32_bf16 v[24:27], v[84:87], v[68:71], v[24:27]\n\t"
      "ds_read_b128 v[120:123], v163 offset:1024\n\t"
      "v_subrev_u32_e32 v201, 1, v199\n\t"
      "v_mfma_f32_16x16x32_bf16 v[32:35], v[84:87], v[72:75], v[32:35]\n\t"
      "ds_read_b64_tr_b16 v[136:137], v167 offset:1024\n\t"
      "v_cmp_gt_u32_e64 s[64:65], 8, v201\n\t"
      "v_mfma_f32_16x16x32_bf16 v[16:19], v[84:87], v[64:67], v[16:19]\n\t"
      "ds_read_b64_tr_b16 v[138:139], v167 offset:1536\n\t"
      "v_cndmask_b32_e64 v173, v200, 0, vcc\n\t"
      "v_mfma_f32_16x16x32_bf16 v[28:31], v[88:91], v[68:71], v[28:31]\n\t"
      "v_cndmask_b32_e64 v177, v200, 0, s[64:65]\n\t"
      "v_mfma_f32_16x16x32_bf16 v[36:39], v[88:91], v[72:75], v[36:39]\n\t"
      "v_or_b32_e32 v181, s35, v173\n\t"
      "v_mfma_f32_16x16x32_bf16 v[20:23], v[88:91], v[64:67], v[20:23]\n\t"
      "v_cmp_ge_i32_e32 vcc, -7, v199\n\t"
      "s_waitcnt lgkmcnt(7)\n\t"
      "v_mfma_f32_16x16x32_bf16 v[48:51], v[96:99], v[104:107], 0\n\t"
      "ds_read_b64_tr_b16 v[76:77], v166 offset:2048\n\t"
      "ds_read_b64_tr_b16 v[78:79], v166 offset:2560\n\t"
      "v_subrev_u32_e32 v201, 2, v199\n\t"
      "v_mfma_f32_16x16x32_bf16 v[56:59], v[100:103], v[104:107], 0\n\t"
      "ds_read_b128 v[68:71], v160 offset:2048\n\t"
      "v_cmp_gt_u32_e64 s[64:65], 8, v201\n\t"
      "v_mfma_f32_16x16x32_bf16 v[40:43], v[92:95], v[104:107], 0\n\t"
      "ds_read_b128 v[72:75], v160 offset:16384\n\t"
      "v_cndmask_b32_e64 v174, v200, 0, vcc\n\t"
      "v_mfma_f32_16x16x32_bf16 v[52:55], v[96:99], v[108:111], 0\n\t"
      "ds_read_b128 v[64:67], v162 offset:2048\n\t"
      "v_cndmask_b32_e64 v178, v200, 0, s[64:65]\n\t"
      "v_mfma_f32_16x16x32_bf16 v[60:63], v[100:103], v[108:111], 0\n\t"
      "ds_read_b64_tr_b16 v[80:81], v167 offset:2048\n\t"
      "v_or_b32_e32 v182, s35, v174\n\t"
      "v_mfma_f32_16x16x32_bf16 v[44:47], v[92:95], v[108:111], 0\n\t"
      "ds_read_b64_tr_b16 v[82:83], v167 offset:2560\n\t"
      "v_cmp_ge_i32_e32 vcc, -6, v199\n\t"
      "s_waitcnt lgkmcnt(7)\n\t"
      "v_mfma_f32_16x16x32_bf16 v[48:51], v[124:127], v[132:135], v[48:51]\n\t"
      "ds_read_b64_tr_b16 v[104:105], v166 offset:3072\n\t"
      "ds_read_b64_tr_b16 v[106:107], v166 offset:3584\n\t"
      "v_subrev_u32_e32 v201, 3, v199\n\t"
      "v_mfma_f32_16x16x32_bf16 v[56:59], v[128:131], v[132:135], v[56:59]\n\t"
      "ds_read_b128 v[96:99], v161 offset:3072\n\t"
      "v_cmp_gt_u32_e64 s[64:65], 8, v201\n\t"
      "v_mfma_f32_16x16x32_bf16 v[40:43], v[120:123], v[132:135], v[40:43]\n\t"
      "ds_read_b128 v[100:103], v161 offset:17408\n\t"
      "v_cndmask_b32_e64 v175, v200, 0, vcc\n\t"
      "v_mfma_f32_16x16x32_bf16 v[52:55], v[124:127], v[136:139], v[52:55]\n\t"
      "ds_read_b128 v[92:95], v163 offset:3072\n\t"
      "v_cndmask_b32_e64 v179, v200, 0, s[64:65]\n\t"
      "v_mfma_f32_16x16x32_bf16 v[60:63], v[128:131], v[136:139], v[60:63]\n\t"
      "ds_read_b64_tr_b16 v[108:109], v167 offset:3072\n\t"
      "v_or_b32_e32 v183, s35, v175\n\t"
      "v_mfma_f32_16x16x32_bf16 v[44:47], v[120:123], v[136:139], v[44:47]\n\t"
      "ds_read_b64_tr_b16 v[110:111], v167 offset:3584\n\t"
      "v_lshrrev_b32_e32 v201, 31, v172\n\t"
      "s_barrier\n\t"
      "s_waitcnt lgkmcnt(7)\n\t"
      "v_mfma_f32_16x16x32_bf16 v[48:51], v[68:71], v[76:79], v[48:51]\n\t"
      "ds_read_b64_tr_b16 v[132:133], v166 offset:16384\n\t"
      "ds_read_b64_tr_b16 v[134:135], v166 offset:16896\n\t"
      "ds_read_b128 v[124:127], v160 offset:4096\n\t"
      "ds_read_b128 v[128:131], v160 offset:18432\n\t"
      "ds_read_b64_tr_b16 v[136:137], v167 offset:16384\n\t"
      "ds_read_b64_tr_b16 v[138:139], v167 offset:16896\n\t"
      "v_lshrrev_b32_e32 v202, 31, v173\n\t"
      "v_mfma_f32_16x16x32_bf16 v[56:59], v[72:75], v[76:79], v[56:59]\n\t"
      "ds_read_b64_tr_b16 v[140:141], v166 offset:17408\n\t"
      "v_lshl_or_b32 v201, v202, 16, v201\n\t"
      "v_mfma_f32_16x16x32_bf16 v[40:43], v[64:67], v[76:79], v[40:43]\n\t"
      "ds_read_b64_tr_b16 v[142:143], v166 offset:17920\n\t"
      "v_mul_u32_u24_e32 v184, 0xffff, v201\n\t"
      "v_mfma_f32_16x16x32_bf16 v[52:55], v[68:71], v[80:83], v[52:55]\n\t"
      "ds_read_b128 v[120:123], v162 offset:4096\n\t"
      "v_lshrrev_b32_e32 v201, 31, v174\n\t"
      "v_mfma_f32_16x16x32_bf16 v[60:63], v[72:75], v[80:83], v[60:63]\n\t"
      "ds_read_b64_tr_b16 v[144:145], v167 offset:17408\n\t"
      "v_lshrrev_b32_e32 v202, 31, v175\n\t"
      "v_mfma_f32_16x16x32_bf16 v[44:47], v[64:67], v[80:83], v[44:47]\n\t"
      "ds_read_b64_tr_b16 v[146:147], v167 offset:17920\n\t"
      "v_lshl_or_b32 v201, v202, 16, v201\n\t"
      "s_waitcnt lgkmcnt(11)\n\t"
      "v_mfma_f32_16x16x32_bf16 v[48:51], v[96:99], v[104:107], v[48:51]\n\t"
      "ds_read_b64_tr_b16 v[76:77], v166 offset:18432\n\t"
      "ds_read_b64_tr_b16 v[78:79], v166 offset:18944\n\t"
      "ds_read_b128 v[68:71], v161 offset:5120\n\t"
      "ds_read_b128 v[72:75], v161 offset:19456\n\t"
      "ds_read_b64_tr_b16 v[80:81], v167 offset:18432\n\t"
      "ds_read_b64_tr_b16 v[82:83], v167 offset:18944\n\t"
      "v_mul_u32_u24_e32 v185, 0xffff, v201\n\t"
      "v_mfma_f32_16x16x32_bf16 v[56:59], v[100:103], v[104:107], v[56:59]\n\t"
      "ds_read_b64_tr_b16 v[84:85], v166 offset:19456\n\t"
      "s_mov_b32 s68, 0x0\n\t"
      "v_mfma_f32_16x16x32_bf16 v[40:43], v[92:95], v[104:107], v[40:43]\n\t"
      "ds_read_b64_tr_b16 v[86:87], v166 offset:19968\n\t"
      "s_mov_b32 s69, 0xffff0000\n\t"
      "v_mfma_f32_16x16x32_bf16 v[52:55], v[96:99], v[108:111], v[52:55]\n\t"
      "ds_read_b128 v[64:67], v163 offset:5120\n\t"
      "s_mov_b32 s70, 0xffff\n\t"
      "v_mfma_f32_16x16x32_bf16 v[60:63], v[100:103], v[108:111], v[60:63]\n\t"
      "ds_read_b64_tr_b16 v[88:89], v167 offset:19456\n\t"
      "s_mov_b32 s71, 0x0\n\t"
      "v_mfma_f32_16x16x32_bf16 v[44:47], v[92:95], v[108:111], v[44:47]\n\t"
      "ds_read_b64_tr_b16 v[90:91], v167 offset:19968\n\t"
      "v_lshlrev_b32_e32 v231, 3, v192\n\t"
      "s_barrier\n\t"
      "s_waitcnt lgkmcnt(11)\n\t"
      "v_mfma_f32_16x16x32_bf16 v[0:3], v[132:135], v[124:127], v[0:3]\n\t"
      "ds_read_b64_tr_b16 v[104:105], v166 offset:32768\n\t"
      "ds_read_b64_tr_b16 v[106:107], v166 offset:33280\n\t"
      "s_lshl_b32 s34, s25, 12\n\t"
      "v_mfma_f32_16x16x32_bf16 v[8:11], v[132:135], v[128:131], v[8:11]\n\t"
      "ds_read_b128 v[96:99], v160 offset:6144\n\t"
      "s_lshr_b32 s35, s39, 2\n\t"
      "v_mfma_f32_16x16x32_bf16 v[4:7], v[136:139], v[124:127], v[4:7]\n\t"
      "ds_read_b128 v[100:103], v160 offset:20480\n\t"
      "s_add_u32 s34, s34, s35\n\t"
      "v_mfma_f32_16x16x32_bf16 v[12:15], v[136:139], v[128:131], v[12:15]\n\t"
      "ds_read_b64_tr_b16 v[108:109], v167 offset:32768\n\t"
      "s_add_u32 s34, s34, 12582912\n\t"
      "v_mfma_f32_16x16x32_bf16 v[24:27], v[140:143], v[124:127], v[24:27]\n\t"
      "ds_read_b64_tr_b16 v[110:111], v167 offset:33280\n\t"
      "s_add_u32 s46, s20, s34\n\t"
      "v_mfma_f32_16x16x32_bf16 v[32:35], v[140:143], v[128:131], v[32:35]\n\t"
      "ds_read_b64_tr_b16 v[112:113], v166 offset:33792\n\t"
      "s_addc_u32 s47, s21, 0\n\t"
      "v_mfma_f32_16x16x32_bf16 v[16:19], v[140:143], v[120:123], v[16:19]\n\t"
      "ds_read_b64_tr_b16 v[114:115], v166 offset:34304\n\t"
      "s_lshl_b32 s34, s28, 7\n\t"
      "v_mfma_f32_16x16x32_bf16 v[28:31], v[144:147], v[124:127], v[28:31]\n\t"
      "ds_read_b128 v[92:95], v162 offset:6144\n\t"
      "s_add_u32 s58, s46, s34\n\t"
      "v_mfma_f32_16x16x32_bf16 v[36:39], v[144:147], v[128:131], v[36:39]\n\t"
      "ds_read_b64_tr_b16 v[116:117], v167 offset:33792\n\t"
      "s_addc_u32 s59, s47, 0\n\t"
      "v_mfma_f32_16x16x32_bf16 v[20:23], v[144:147], v[120:123], v[20:23]\n\t"
      "ds_read_b64_tr_b16 v[118:119], v167 offset:34304\n\t"
      "s_and_b32 s34, s24, 1\n\t"
      "s_waitcnt lgkmcnt(11)\n\t"
      "v_mfma_f32_16x16x32_bf16 v[0:3], v[76:79], v[68:71], v[0:3]\n\t"
      "ds_read_b64_tr_b16 v[132:133], v166 offset:34816\n\t"
      "ds_read_b64_tr_b16 v[134:135], v166 offset:35328\n\t"
      "s_lshl1_add_u32 s34, s25, s34\n\t"
      "v_mfma_f32_16x16x32_bf16 v[8:11], v[76:79], v[72:75], v[8:11]\n\t"
      "ds_read_b128 v[124:127], v161 offset:7168\n\t"
      "s_lshl_b32 s34, s34, 5\n\t"
      "v_mfma_f32_16x16x32_bf16 v[4:7], v[80:83], v[68:71], v[4:7]\n\t"
      "ds_read_b128 v[128:131], v161 offset:21504\n\t"
      "s_lshl1_add_u32 s34, s26, s34\n\t"
      "v_mfma_f32_16x16x32_bf16 v[12:15], v[80:83], v[72:75], v[12:15]\n\t"
      "ds_read_b64_tr_b16 v[136:137], v167 offset:34816\n\t"
      "s_bfe_u32 s35, s24, 65537\n\t"
      "v_mfma_f32_16x16x32_bf16 v[24:27], v[84:87], v[68:71], v[24:27]\n\t"
      "ds_read_b64_tr_b16 v[138:139], v167 offset:35328\n\t"
      "s_add_u32 s34, s34, s35\n\t"
      "v_mfma_f32_16x16x32_bf16 v[32:35], v[84:87], v[72:75], v[32:35]\n\t"
      "ds_read_b64_tr_b16 v[140:141], v166 offset:35840\n\t"
      "s_lshl_b32 s34, s34, 10\n\t"
      "v_mfma_f32_16x16x32_bf16 v[16:19], v[84:87], v[64:67], v[16:19]\n\t"
      "ds_read_b64_tr_b16 v[142:143], v166 offset:36352\n\t"
      "s_add_u32 s42, s20, s34\n\t"
      "v_mfma_f32_16x16x32_bf16 v[28:31], v[88:91], v[68:71], v[28:31]\n\t"
      "ds_read_b128 v[120:123], v165 offset:7168\n\t"
      "s_addc_u32 s43, s21, 0\n\t"
      "v_mfma_f32_16x16x32_bf16 v[36:39], v[88:91], v[72:75], v[36:39]\n\t"
      "ds_read_b64_tr_b16 v[144:145], v167 offset:35840\n\t"
      "s_add_u32 s44, s42, 16384\n\t"
      "v_mfma_f32_16x16x32_bf16 v[20:23], v[88:91], v[64:67], v[20:23]\n\t"
      "ds_read_b64_tr_b16 v[146:147], v167 offset:36352\n\t"
      "s_addc_u32 s45, s43, 0\n\t"
      "s_barrier\n\t"
      "s_waitcnt lgkmcnt(11)\n\t"
      "v_mfma_f32_16x16x32_bf16 v[0:3], v[104:107], v[96:99], v[0:3]\n\t"
      "ds_read_b64_tr_b16 v[76:77], v166 offset:49152\n\t"
      "ds_read_b64_tr_b16 v[78:79], v166 offset:49664\n\t"
      "v_and_b32_e32 v199, 60, v192\n\t"
      "v_mfma_f32_16x16x32_bf16 v[8:11], v[104:107], v[100:103], v[8:11]\n\t"
      "ds_read_b128 v[68:71], v160 offset:8192\n\t"
      "v_and_b32_e32 v200, 24, v192\n\t"
      "v_mfma_f32_16x16x32_bf16 v[4:7], v[108:111], v[96:99], v[4:7]\n\t"
      "ds_read_b128 v[72:75], v160 offset:22528\n\t"
      "v_lshlrev_b32_e32 v202, 3, v196\n\t"
      "v_mfma_f32_16x16x32_bf16 v[12:15], v[108:111], v[100:103], v[12:15]\n\t"
      "ds_read_b64_tr_b16 v[80:81], v167 offset:49152\n\t"
      "v_lshl_or_b32 v202, v200, 2, v202\n\t"
      "v_mfma_f32_16x16x32_bf16 v[24:27], v[112:115], v[96:99], v[24:27]\n\t"
      "ds_read_b64_tr_b16 v[82:83], v167 offset:49664\n\t"
      "v_lshl_or_b32 v201, v199, 5, v202\n\t"
      "v_mfma_f32_16x16x32_bf16 v[32:35], v[112:115], v[100:103], v[32:35]\n\t"
      "ds_read_b64_tr_b16 v[84:85], v166 offset:50176\n\t"
      "v_add_u32_e32 v216, 106496, v201\n\t"
      "v_mfma_f32_16x16x32_bf16 v[16:19], v[112:115], v[92:95], v[16:19]\n\t"
      "ds_read_b64_tr_b16 v[86:87], v166 offset:50688\n\t"
      "v_mov_b32_e32 v199, 2048\n\t"
      "v_mfma_f32_16x16x32_bf16 v[28:31], v[116:119], v[96:99], v[28:31]\n\t"
      "ds_read_b128 v[64:67], v164 offset:8192\n\t"
      "v_cndmask_b32_e64 v199, v199, 0, s[68:69]\n\t"
      "v_mfma_f32_16x16x32_bf16 v[36:39], v[116:119], v[100:103], v[36:39]\n\t"
      "ds_read_b64_tr_b16 v[88:89], v167 offset:50176\n\t"
      "v_add_u32_e32 v220, v216, v199\n\t"
      "v_mfma_f32_16x16x32_bf16 v[20:23], v[116:119], v[92:95], v[20:23]\n\t"
      "ds_read_b64_tr_b16 v[90:91], v167 offset:50688\n\t"
      "v_xor_b32_e32 v217, 32, v216\n\t"
      "s_waitcnt lgkmcnt(11)\n\t"
      "v_mfma_f32_16x16x32_bf16 v[0:3], v[132:135], v[124:127], v[0:3]\n\t"
      "ds_read_b64_tr_b16 v[104:105], v166 offset:51200\n\t"
      "ds_read_b64_tr_b16 v[106:107], v166 offset:51712\n\t"
      "v_xor_b32_e32 v218, 64, v216\n\t"
      "v_mfma_f32_16x16x32_bf16 v[8:11], v[132:135], v[128:131], v[8:11]\n\t"
      "ds_read_b128 v[96:99], v161 offset:9216\n\t"
      "v_xor_b32_e32 v219, 96, v216\n\t"
      "v_mfma_f32_16x16x32_bf16 v[4:7], v[136:139], v[124:127], v[4:7]\n\t"
      "ds_read_b128 v[100:103], v161 offset:23552\n\t"
      "v_xor_b32_e32 v221, 32, v220\n\t"
      "v_mfma_f32_16x16x32_bf16 v[12:15], v[136:139], v[128:131], v[12:15]\n\t"
      "ds_read_b64_tr_b16 v[108:109], v167 offset:51200\n\t"
      "v_xor_b32_e32 v222, 64, v220\n\t"
      "v_mfma_f32_16x16x32_bf16 v[24:27], v[140:143], v[124:127], v[24:27]\n\t"
      "ds_read_b64_tr_b16 v[110:111], v167 offset:51712\n\t"
      "v_xor_b32_e32 v223, 96, v220\n\t"
      "v_mfma_f32_16x16x32_bf16 v[32:35], v[140:143], v[128:131], v[32:35]\n\t"
      "ds_read_b64_tr_b16 v[112:113], v166 offset:52224\n\t"
      "s_lshr_b32 s34, s39, 7\n\t"
      "v_mfma_f32_16x16x32_bf16 v[16:19], v[140:143], v[120:123], v[16:19]\n\t"
      "ds_read_b64_tr_b16 v[114:115], v166 offset:52736\n\t"
      "s_add_u32 s34, s34, s38\n\t"
      "v_mfma_f32_16x16x32_bf16 v[28:31], v[144:147], v[124:127], v[28:31]\n\t"
      "ds_read_b128 v[92:95], v165 offset:9216\n\t"
      "s_mul_i32 s34, s34, 896\n\t"
      "v_mfma_f32_16x16x32_bf16 v[36:39], v[144:147], v[128:131], v[36:39]\n\t"
      "ds_read_b64_tr_b16 v[116:117], v167 offset:52224\n\t"
      "s_lshl_b32 s35, s28, 7\n\t"
      "v_mfma_f32_16x16x32_bf16 v[20:23], v[144:147], v[120:123], v[20:23]\n\t"
      "ds_read_b64_tr_b16 v[118:119], v167 offset:52736\n\t"
      "s_add_u32 s34, s34, s35\n\t"
      "s_barrier\n\t"
      "s_waitcnt lgkmcnt(11)\n\t"
      "v_mfma_f32_16x16x32_bf16 v[0:3], v[76:79], v[68:71], v[0:3]\n\t"
      "ds_read_b64_tr_b16 v[132:133], v168\n\t"
      "ds_read_b64_tr_b16 v[134:135], v168 offset:512\n\t"
      "v_and_b32_e32 v199, 16, v192\n\t"
      "v_mfma_f32_16x16x32_bf16 v[8:11], v[76:79], v[72:75], v[8:11]\n\t"
      "ds_read_b128 v[124:127], v160 offset:10240\n\t"
      "v_lshlrev_b32_e32 v199, 1, v199\n\t"
      "v_mfma_f32_16x16x32_bf16 v[4:7], v[80:83], v[68:71], v[4:7]\n\t"
      "ds_read_b128 v[128:131], v160 offset:24576\n\t"
      "v_and_b32_e32 v200, 32, v192\n\t"
      "v_mfma_f32_16x16x32_bf16 v[12:15], v[80:83], v[72:75], v[12:15]\n\t"
      "ds_read_b64_tr_b16 v[136:137], v169\n\t"
      "v_lshrrev_b32_e32 v201, 1, v200\n\t"
      "v_mfma_f32_16x16x32_bf16 v[24:27], v[84:87], v[68:71], v[24:27]\n\t"
      "ds_read_b64_tr_b16 v[138:139], v169 offset:512\n\t"
      "v_add3_u32 v228, v199, v201, s34\n\t"
      "v_mfma_f32_16x16x32_bf16 v[32:35], v[84:87], v[72:75], v[32:35]\n\t"
      "ds_read_b64_tr_b16 v[140:141], v168 offset:1024\n\t"
      "s_movk_i32 s35, 1792\n\t"
      "v_mfma_f32_16x16x32_bf16 v[16:19], v[84:87], v[64:67], v[16:19]\n\t"
      "ds_read_b64_tr_b16 v[142:143], v168 offset:1536\n\t"
      "v_mad_u32_u24 v228, v194, s35, v228\n\t"
      "v_mfma_f32_16x16x32_bf16 v[28:31], v[88:91], v[68:71], v[28:31]\n\t"
      "ds_read_b128 v[120:123], v164 offset:10240\n\t"
      "v_mfma_f32_16x16x32_bf16 v[36:39], v[88:91], v[72:75], v[36:39]\n\t"
      "ds_read_b64_tr_b16 v[144:145], v169 offset:1024\n\t"
      "v_mfma_f32_16x16x32_bf16 v[20:23], v[88:91], v[64:67], v[20:23]\n\t"
      "ds_read_b64_tr_b16 v[146:147], v169 offset:1536\n\t"
      "s_waitcnt lgkmcnt(11)\n\t"
      "v_mfma_f32_16x16x32_bf16 v[0:3], v[104:107], v[96:99], v[0:3]\n\t"
      "ds_read_b64_tr_b16 v[76:77], v168 offset:2048\n\t"
      "ds_read_b64_tr_b16 v[78:79], v168 offset:2560\n\t"
      "v_mfma_f32_16x16x32_bf16 v[8:11], v[104:107], v[100:103], v[8:11]\n\t"
      "ds_read_b128 v[68:71], v161 offset:11264\n\t"
      "v_mfma_f32_16x16x32_bf16 v[4:7], v[108:111], v[96:99], v[4:7]\n\t"
      "ds_read_b128 v[72:75], v161 offset:25600\n\t"
      "v_mfma_f32_16x16x32_bf16 v[12:15], v[108:111], v[100:103], v[12:15]\n\t"
      "ds_read_b64_tr_b16 v[80:81], v169 offset:2048\n\t"
      "v_mfma_f32_16x16x32_bf16 v[24:27], v[112:115], v[96:99], v[24:27]\n\t"
      "ds_read_b64_tr_b16 v[82:83], v169 offset:2560\n\t"
      "v_mfma_f32_16x16x32_bf16 v[32:35], v[112:115], v[100:103], v[32:35]\n\t"
      "ds_read_b64_tr_b16 v[84:85], v168 offset:3072\n\t"
      "v_mfma_f32_16x16x32_bf16 v[16:19], v[112:115], v[92:95], v[16:19]\n\t"
      "ds_read_b64_tr_b16 v[86:87], v168 offset:3584\n\t"
      "v_mfma_f32_16x16x32_bf16 v[28:31], v[116:119], v[96:99], v[28:31]\n\t"
      "ds_read_b128 v[64:67], v165 offset:11264\n\t"
      "v_mfma_f32_16x16x32_bf16 v[36:39], v[116:119], v[100:103], v[36:39]\n\t"
      "ds_read_b64_tr_b16 v[88:89], v169 offset:3072\n\t"
      "v_mfma_f32_16x16x32_bf16 v[20:23], v[116:119], v[92:95], v[20:23]\n\t"
      "ds_read_b64_tr_b16 v[90:91], v169 offset:3584\n\t"
      "s_barrier\n\t"
      "s_waitcnt lgkmcnt(11)\n\t"
      "v_mfma_f32_16x16x32_bf16 v[0:3], v[132:135], v[124:127], v[0:3]\n\t"
      "ds_read_b64_tr_b16 v[104:105], v168 offset:16384\n\t"
      "ds_read_b64_tr_b16 v[106:107], v168 offset:16896\n\t"
      "v_mfma_f32_16x16x32_bf16 v[8:11], v[132:135], v[128:131], v[8:11]\n\t"
      "ds_read_b128 v[96:99], v160 offset:12288\n\t"
      "v_mfma_f32_16x16x32_bf16 v[4:7], v[136:139], v[124:127], v[4:7]\n\t"
      "ds_read_b128 v[100:103], v160 offset:26624\n\t"
      "v_mfma_f32_16x16x32_bf16 v[12:15], v[136:139], v[128:131], v[12:15]\n\t"
      "ds_read_b64_tr_b16 v[108:109], v169 offset:16384\n\t"
      "v_mfma_f32_16x16x32_bf16 v[24:27], v[140:143], v[124:127], v[24:27]\n\t"
      "ds_read_b64_tr_b16 v[110:111], v169 offset:16896\n\t"
      "v_mfma_f32_16x16x32_bf16 v[32:35], v[140:143], v[128:131], v[32:35]\n\t"
      "ds_read_b64_tr_b16 v[112:113], v168 offset:17408\n\t"
      "v_mfma_f32_16x16x32_bf16 v[16:19], v[140:143], v[120:123], v[16:19]\n\t"
      "ds_read_b64_tr_b16 v[114:115], v168 offset:17920\n\t"
      "v_mfma_f32_16x16x32_bf16 v[28:31], v[144:147], v[124:127], v[28:31]\n\t"
      "ds_read_b128 v[92:95], v164 offset:12288\n\t"
      "v_mfma_f32_16x16x32_bf16 v[36:39], v[144:147], v[128:131], v[36:39]\n\t"
      "ds_read_b64_tr_b16 v[116:117], v169 offset:17408\n\t"
      "v_mfma_f32_16x16x32_bf16 v[20:23], v[144:147], v[120:123], v[20:23]\n\t"
      "ds_read_b64_tr_b16 v[118:119], v169 offset:17920\n\t"
      "s_waitcnt lgkmcnt(11)\n\t"
      "v_mfma_f32_16x16x32_bf16 v[0:3], v[76:79], v[68:71], v[0:3]\n\t"
      "ds_read_b64_tr_b16 v[132:133], v168 offset:18432\n\t"
      "ds_read_b64_tr_b16 v[134:135], v168 offset:18944\n\t"
      "v_mfma_f32_16x16x32_bf16 v[8:11], v[76:79], v[72:75], v[8:11]\n\t"
      "ds_read_b128 v[124:127], v161 offset:13312\n\t"
      "v_mfma_f32_16x16x32_bf16 v[4:7], v[80:83], v[68:71], v[4:7]\n\t"
      "ds_read_b128 v[128:131], v161 offset:27648\n\t"
      "v_mfma_f32_16x16x32_bf16 v[12:15], v[80:83], v[72:75], v[12:15]\n\t"
      "ds_read_b64_tr_b16 v[136:137], v169 offset:18432\n\t"
      "v_mfma_f32_16x16x32_bf16 v[24:27], v[84:87], v[68:71], v[24:27]\n\t"
      "ds_read_b64_tr_b16 v[138:139], v169 offset:18944\n\t"
      "v_mfma_f32_16x16x32_bf16 v[32:35], v[84:87], v[72:75], v[32:35]\n\t"
      "ds_read_b64_tr_b16 v[140:141], v168 offset:19456\n\t"
      "v_mfma_f32_16x16x32_bf16 v[16:19], v[84:87], v[64:67], v[16:19]\n\t"
      "ds_read_b64_tr_b16 v[142:143], v168 offset:19968\n\t"
      "v_mfma_f32_16x16x32_bf16 v[28:31], v[88:91], v[68:71], v[28:31]\n\t"
      "ds_read_b128 v[120:123], v165 offset:13312\n\t"
      "v_mfma_f32_16x16x32_bf16 v[36:39], v[88:91], v[72:75], v[36:39]\n\t"
      "ds_read_b64_tr_b16 v[144:145], v169 offset:19456\n\t"
      "v_mfma_f32_16x16x32_bf16 v[20:23], v[88:91], v[64:67], v[20:23]\n\t"
      "ds_read_b64_tr_b16 v[146:147], v169 offset:19968\n\t"
      "s_barrier\n\t"
      "s_waitcnt lgkmcnt(11)\n\t"
      "v_mfma_f32_16x16x32_bf16 v[0:3], v[104:107], v[96:99], v[0:3]\n\t"
      "ds_read_b64_tr_b16 v[76:77], v166\n\t"
      "v_mfma_f32_16x16x32_bf16 v[8:11], v[104:107], v[100:103], v[8:11]\n\t"
      "ds_read_b64_tr_b16 v[78:79], v166 offset:512\n\t"
      "v_mfma_f32_16x16x32_bf16 v[4:7], v[108:111], v[96:99], v[4:7]\n\t"
      "ds_read_b128 v[68:71], v160 offset:4096\n\t"
      "v_mfma_f32_16x16x32_bf16 v[12:15], v[108:111], v[100:103], v[12:15]\n\t"
      "ds_read_b128 v[72:75], v160 offset:18432\n\t"
      "v_mfma_f32_16x16x32_bf16 v[24:27], v[112:115], v[96:99], v[24:27]\n\t"
      "ds_read_b128 v[64:67], v162 offset:4096\n\t"
      "v_mfma_f32_16x16x32_bf16 v[32:35], v[112:115], v[100:103], v[32:35]\n\t"
      "ds_read_b64_tr_b16 v[80:81], v167\n\t"
      "v_mfma_f32_16x16x32_bf16 v[16:19], v[112:115], v[92:95], v[16:19]\n\t"
      "ds_read_b64_tr_b16 v[82:83], v167 offset:512\n\t"
      "v_mfma_f32_16x16x32_bf16 v[28:31], v[116:119], v[96:99], v[28:31]\n\t"
      "v_mfma_f32_16x16x32_bf16 v[36:39], v[116:119], v[100:103], v[36:39]\n\t"
      "v_mfma_f32_16x16x32_bf16 v[20:23], v[116:119], v[92:95], v[20:23]\n\t"
      "s_waitcnt lgkmcnt(7)\n\t"
      "v_mfma_f32_16x16x32_bf16 v[0:3], v[132:135], v[124:127], v[0:3]\n\t"
      "ds_read_b64_tr_b16 v[104:105], v166 offset:1024\n\t"
      "v_mfma_f32_16x16x32_bf16 v[8:11], v[132:135], v[128:131], v[8:11]\n\t"
      "ds_read_b64_tr_b16 v[106:107], v166 offset:1536\n\t"
      "v_mfma_f32_16x16x32_bf16 v[4:7], v[136:139], v[124:127], v[4:7]\n\t"
      "ds_read_b128 v[96:99], v161 offset:5120\n\t"
      "v_mfma_f32_16x16x32_bf16 v[12:15], v[136:139], v[128:131], v[12:15]\n\t"
      "ds_read_b128 v[100:103], v161 offset:19456\n\t"
      "v_mfma_f32_16x16x32_bf16 v[24:27], v[140:143], v[124:127], v[24:27]\n\t"
      "ds_read_b128 v[92:95], v163 offset:5120\n\t"
      "v_mfma_f32_16x16x32_bf16 v[32:35], v[140:143], v[128:131], v[32:35]\n\t"
      "ds_read_b64_tr_b16 v[108:109], v167 offset:1024\n\t"
      "v_mfma_f32_16x16x32_bf16 v[16:19], v[140:143], v[120:123], v[16:19]\n\t"
      "ds_read_b64_tr_b16 v[110:111], v167 offset:1536\n\t"
      "v_mfma_f32_16x16x32_bf16 v[28:31], v[144:147], v[124:127], v[28:31]\n\t"
      "v_mfma_f32_16x16x32_bf16 v[36:39], v[144:147], v[128:131], v[36:39]\n\t"
      "v_mfma_f32_16x16x32_bf16 v[20:23], v[144:147], v[120:123], v[20:23]\n\t"
      "s_waitcnt lgkmcnt(7)\n\t"
      "v_mfma_f32_16x16x32_bf16 v[48:51], v[68:71], v[76:79], v[48:51]\n\t"
      "ds_read_b64_tr_b16 v[132:133], v166 offset:2048\n\t"
      "ds_read_b64_tr_b16 v[134:135], v166 offset:2560\n\t"
      "v_mfma_f32_16x16x32_bf16 v[56:59], v[72:75], v[76:79], v[56:59]\n\t"
      "ds_read_b128 v[124:127], v160 offset:6144\n\t"
      "v_mfma_f32_16x16x32_bf16 v[40:43], v[64:67], v[76:79], v[40:43]\n\t"
      "ds_read_b128 v[128:131], v160 offset:20480\n\t"
      "v_cvt_pk_bf16_f32 v84, v0, v1\n\t"
      "v_cvt_pk_bf16_f32 v85, v2, v3\n\t"
      "v_cvt_pk_bf16_f32 v86, v4, v5\n\t"
      "v_mfma_f32_16x16x32_bf16 v[52:55], v[68:71], v[80:83], v[52:55]\n\t"
      "ds_read_b128 v[120:123], v162 offset:6144\n\t"
      "v_cvt_pk_bf16_f32 v87, v6, v7\n\t"
      "v_cvt_pk_bf16_f32 v88, v8, v9\n\t"
      "v_cvt_pk_bf16_f32 v89, v10, v11\n\t"
      "v_mfma_f32_16x16x32_bf16 v[60:63], v[72:75], v[80:83], v[60:63]\n\t"
      "ds_read_b64_tr_b16 v[136:137], v167 offset:2048\n\t"
      "v_cvt_pk_bf16_f32 v90, v12, v13\n\t"
      "v_cvt_pk_bf16_f32 v91, v14, v15\n\t"
      "v_cvt_pk_bf16_f32 v152, v24, v25\n\t"
      "v_mfma_f32_16x16x32_bf16 v[44:47], v[64:67], v[80:83], v[44:47]\n\t"
      "ds_read_b64_tr_b16 v[138:139], v167 offset:2560\n\t"
      "v_cvt_pk_bf16_f32 v153, v26, v27\n\t"
      "v_cvt_pk_bf16_f32 v154, v28, v29\n\t"
      "v_cvt_pk_bf16_f32 v155, v30, v31\n\t"
      "s_waitcnt lgkmcnt(7)\n\t"
      "v_mfma_f32_16x16x32_bf16 v[48:51], v[96:99], v[104:107], v[48:51]\n\t"
      "ds_read_b64_tr_b16 v[76:77], v166 offset:3072\n\t"
      "ds_read_b64_tr_b16 v[78:79], v166 offset:3584\n\t"
      "v_cndmask_b32_e64 v86, v86, 0, s[68:69]\n\t"
      "v_cndmask_b32_e64 v87, v87, 0, s[68:69]\n\t"
      "v_cvt_pk_bf16_f32 v156, v32, v33\n\t"
      "v_mfma_f32_16x16x32_bf16 v[56:59], v[100:103], v[104:107], v[56:59]\n\t"
      "ds_read_b128 v[68:71], v161 offset:7168\n\t"
      "v_cvt_pk_bf16_f32 v157, v34, v35\n\t"
      "v_cvt_pk_bf16_f32 v158, v36, v37\n\t"
      "v_cvt_pk_bf16_f32 v159, v38, v39\n\t"
      "v_mfma_f32_16x16x32_bf16 v[40:43], v[92:95], v[104:107], v[40:43]\n\t"
      "ds_read_b128 v[72:75], v161 offset:21504\n\t"
      "v_cndmask_b32_e64 v90, v90, 0, s[68:69]\n\t"
      "v_cndmask_b32_e64 v91, v91, 0, s[68:69]\n\t"
      "v_cvt_pk_bf16_f32 v148, v16, v17\n\t"
      "v_mfma_f32_16x16x32_bf16 v[52:55], v[96:99], v[108:111], v[52:55]\n\t"
      "ds_read_b128 v[64:67], v165 offset:7168\n\t"
      "v_cvt_pk_bf16_f32 v149, v18, v19\n\t"
      "v_cvt_pk_bf16_f32 v150, v20, v21\n\t"
      "v_cvt_pk_bf16_f32 v151, v22, v23\n\t"
      "v_mfma_f32_16x16x32_bf16 v[60:63], v[100:103], v[108:111], v[60:63]\n\t"
      "ds_read_b64_tr_b16 v[80:81], v167 offset:3072\n\t"
      "v_mov_b32_e32 v34, 0x3e8b97f6\n\t"
      "v_mfma_f32_16x16x32_bf16 v[28:31], v[156:159], v[88:91], v[176:179]\n\t"
      "v_mfma_f32_16x16x32_bf16 v[24:27], v[152:155], v[88:91], v[172:175]\n\t"
      "v_mfma_f32_16x16x32_bf16 v[44:47], v[92:95], v[108:111], v[44:47]\n\t"
      "ds_read_b64_tr_b16 v[82:83], v167 offset:3584\n\t"
      "v_mfma_f32_16x16x32_bf16 v[20:23], v[152:155], v[84:87], v[176:179]\n\t"
      "v_mfma_f32_16x16x32_bf16 v[16:19], v[148:151], v[84:87], v[180:183]\n\t"
      "s_barrier\n\t"
      "s_waitcnt lgkmcnt(7)\n\t"
      "v_mfma_f32_16x16x32_bf16 v[48:51], v[124:127], v[132:135], v[48:51]\n\t"
      "ds_read_b64_tr_b16 v[104:105], v166 offset:16384\n\t"
      "ds_read_b64_tr_b16 v[106:107], v166 offset:16896\n\t"
      "v_mfma_f32_16x16x32_bf16 v[56:59], v[128:131], v[132:135], v[56:59]\n\t"
      "ds_read_b128 v[96:99], v160 offset:8192\n\t"
      "v_max_f32_e32 v24, v24, v28\n\t"
      "v_mfma_f32_16x16x32_bf16 v[40:43], v[120:123], v[132:135], v[40:43]\n\t"
      "ds_read_b128 v[100:103], v160 offset:22528\n\t"
      "v_max_f32_e32 v25, v25, v29\n\t"
      "v_max_f32_e32 v26, v26, v30\n\t"
      "v_max_f32_e32 v27, v27, v31\n\t"
      "v_mfma_f32_16x16x32_bf16 v[52:55], v[124:127], v[136:139], v[52:55]\n\t"
      "ds_read_b128 v[92:95], v164 offset:8192\n\t"
      "v_max_f32_e32 v16, v16, v20\n\t"
      "v_max_f32_e32 v17, v17, v21\n\t"
      "v_max_f32_e32 v18, v18, v22\n\t"
      "v_mfma_f32_16x16x32_bf16 v[60:63], v[128:131], v[136:139], v[60:63]\n\t"
      "ds_read_b64_tr_b16 v[108:109], v167 offset:16384\n\t"
      "v_max_f32_e32 v19, v19, v23\n\t"
      "v_max3_f32 v33, v24, v25, v26\n\t"
      "v_max3_f32 v32, v16, v17, v18\n\t"
      "v_mfma_f32_16x16x32_bf16 v[44:47], v[120:123], v[136:139], v[44:47]\n\t"
      "ds_read_b64_tr_b16 v[110:111], v167 offset:16896\n\t"
      "v_max_f32_e32 v33, v33, v27\n\t"
      "v_max_f32_e32 v32, v32, v19\n\t"
      "s_waitcnt lgkmcnt(7)\n\t"
      "v_mfma_f32_16x16x32_bf16 v[48:51], v[68:71], v[76:79], v[48:51]\n\t"
      "ds_read_b64_tr_b16 v[132:133], v166 offset:17408\n\t"
      "ds_read_b64_tr_b16 v[134:135], v166 offset:17920\n\t"
      "v_permlane16_swap_b32_e32 v32, v33\n\t"
      "v_max_f32_e32 v32, v32, v33\n\t"
      "v_mov_b32_e32 v33, v32\n\t"
      "v_mfma_f32_16x16x32_bf16 v[56:59], v[72:75], v[76:79], v[56:59]\n\t"
      "ds_read_b128 v[124:127], v161 offset:9216\n\t"
      "v_permlane32_swap_b32_e32 v32, v33\n\t"
      "v_max_f32_e32 v32, v32, v33\n\t"
      "v_mfma_f32_16x16x32_bf16 v[40:43], v[64:67], v[76:79], v[40:43]\n\t"
      "ds_read_b128 v[128:131], v161 offset:23552\n\t"
      "v_mov_b32_e32 v33, v32\n\t"
      "s_nop 1\n\t"
      "v_permlane16_swap_b32_e32 v32, v33\n\t"
      "v_mfma_f32_16x16x32_bf16 v[52:55], v[68:71], v[80:83], v[52:55]\n\t"
      "ds_read_b128 v[120:123], v165 offset:9216\n\t"
      "v_mul_f32_e64 v33, -v33, v34\n\t"
      "v_mul_f32_e64 v32, -v32, v34\n\t"
      "v_fma_f32 v24, v24, v34, v33\n\t"
      "v_mfma_f32_16x16x32_bf16 v[60:63], v[72:75], v[80:83], v[60:63]\n\t"
      "ds_read_b64_tr_b16 v[136:137], v167 offset:17408\n\t"
      "v_fma_f32 v25, v25, v34, v33\n\t"
      "v_fma_f32 v26, v26, v34, v33\n\t"
      "v_fma_f32 v27, v27, v34, v33\n\t"
      "v_mfma_f32_16x16x32_bf16 v[44:47], v[64:67], v[80:83], v[44:47]\n\t"
      "ds_read_b64_tr_b16 v[138:139], v167 offset:17920\n\t"
      "v_fma_f32 v16, v16, v34, v32\n\t"
      "v_fma_f32 v17, v17, v34, v32\n\t"
      "v_fma_f32 v18, v18, v34, v32\n\t"
      "s_waitcnt lgkmcnt(7)\n\t"
      "v_mfma_f32_16x16x32_bf16 v[48:51], v[96:99], v[104:107], v[48:51]\n\t"
      "ds_read_b64_tr_b16 v[76:77], v166 offset:18432\n\t"
      "ds_read_b64_tr_b16 v[78:79], v166 offset:18944\n\t"
      "v_fma_f32 v19, v19, v34, v32\n\t"
      "v_exp_f32_e32 v24, v24\n\t"
      "v_mfma_f32_16x16x32_bf16 v[56:59], v[100:103], v[104:107], v[56:59]\n\t"
      "ds_read_b128 v[68:71], v160 offset:10240\n\t"
      "v_exp_f32_e32 v25, v25\n\t"
      "v_exp_f32_e32 v26, v26\n\t"
      "v_mfma_f32_16x16x32_bf16 v[40:43], v[92:95], v[104:107], v[40:43]\n\t"
      "ds_read_b128 v[72:75], v160 offset:24576\n\t"
      "v_exp_f32_e32 v27, v27\n\t"
      "v_mfma_f32_16x16x32_bf16 v[52:55], v[96:99], v[108:111], v[52:55]\n\t"
      "ds_read_b128 v[64:67], v164 offset:10240\n\t"
      "v_exp_f32_e32 v16, v16\n\t"
      "v_exp_f32_e32 v17, v17\n\t"
      "v_mfma_f32_16x16x32_bf16 v[60:63], v[100:103], v[108:111], v[60:63]\n\t"
      "ds_read_b64_tr_b16 v[80:81], v167 offset:18432\n\t"
      "v_exp_f32_e32 v18, v18\n\t"
      "v_mfma_f32_16x16x32_bf16 v[44:47], v[92:95], v[108:111], v[44:47]\n\t"
      "ds_read_b64_tr_b16 v[82:83], v167 offset:18944\n\t"
      "v_exp_f32_e32 v19, v19\n\t"
      "v_add_f32_e32 v33, v24, v25\n\t"
      "s_waitcnt lgkmcnt(7)\n\t"
      "v_mfma_f32_16x16x32_bf16 v[48:51], v[124:127], v[132:135], v[48:51]\n\t"
      "ds_read_b64_tr_b16 v[104:105], v166 offset:19456\n\t"
      "ds_read_b64_tr_b16 v[106:107], v166 offset:19968\n\t"
      "v_add_f32_e32 v28, v26, v27\n\t"
      "v_add_f32_e32 v32, v16, v17\n\t"
      "v_add_f32_e32 v20, v18, v19\n\t"
      "v_mfma_f32_16x16x32_bf16 v[56:59], v[128:131], v[132:135], v[56:59]\n\t"
      "ds_read_b128 v[96:99], v161 offset:11264\n\t"
      "v_add_f32_e32 v33, v33, v28\n\t"
      "v_add_f32_e32 v32, v32, v20\n\t"
      "v_mfma_f32_16x16x32_bf16 v[40:43], v[120:123], v[132:135], v[40:43]\n\t"
      "ds_read_b128 v[100:103], v161 offset:25600\n\t"
      "v_permlane16_swap_b32_e32 v32, v33\n\t"
      "v_add_f32_e32 v32, v32, v33\n\t"
      "v_mov_b32_e32 v33, v32\n\t"
      "v_mfma_f32_16x16x32_bf16 v[52:55], v[124:127], v[136:139], v[52:55]\n\t"
      "ds_read_b128 v[92:95], v165 offset:11264\n\t"
      "v_permlane32_swap_b32_e32 v32, v33\n\t"
      "v_add_f32_e32 v32, v32, v33\n\t"
      "v_mfma_f32_16x16x32_bf16 v[60:63], v[128:131], v[136:139], v[60:63]\n\t"
      "ds_read_b64_tr_b16 v[108:109], v167 offset:19456\n\t"
      "v_mov_b32_e32 v33, v32\n\t"
      "s_nop 1\n\t"
      "v_permlane16_swap_b32_e32 v32, v33\n\t"
      "v_mfma_f32_16x16x32_bf16 v[44:47], v[120:123], v[136:139], v[44:47]\n\t"
      "ds_read_b64_tr_b16 v[110:111], v167 offset:19968\n\t"
      "v_rcp_f32_e32 v33, v33\n\t"
      "v_rcp_f32_e32 v32, v32\n\t"
      "s_barrier\n\t"
      "s_waitcnt lgkmcnt(7)\n\t"
      "v_mfma_f32_16x16x32_bf16 v[48:51], v[68:71], v[76:79], v[48:51]\n\t"
      "ds_read_b64_tr_b16 v[132:133], v170 offset:32768\n\t"
      "ds_read_b64_tr_b16 v[134:135], v170 offset:33280\n\t"
      "v_mul_f32_e32 v24, v24, v33\n\t"
      "v_mul_f32_e32 v25, v25, v33\n\t"
      "v_mfma_f32_16x16x32_bf16 v[56:59], v[72:75], v[76:79], v[56:59]\n\t"
      "ds_read_b128 v[124:127], v160 offset:12288\n\t"
      "v_mul_f32_e32 v26, v26, v33\n\t"
      "v_mul_f32_e32 v27, v27, v33\n\t"
      "v_mul_f32_e32 v16, v16, v32\n\t"
      "v_mfma_f32_16x16x32_bf16 v[40:43], v[64:67], v[76:79], v[40:43]\n\t"
      "ds_read_b128 v[128:131], v160 offset:26624\n\t"
      "v_mul_f32_e32 v17, v17, v32\n\t"
      "v_mul_f32_e32 v18, v18, v32\n\t"
      "v_mul_f32_e32 v19, v19, v32\n\t"
      "v_mfma_f32_16x16x32_bf16 v[52:55], v[68:71], v[80:83], v[52:55]\n\t"
      "ds_read_b128 v[120:123], v164 offset:12288\n\t"
      "v_cvt_pk_bf16_f32 v4, v24, v25\n\t"
      "v_cvt_pk_bf16_f32 v5, v26, v27\n\t"
      "v_cvt_pk_bf16_f32 v0, v16, v17\n\t"
      "v_mfma_f32_16x16x32_bf16 v[60:63], v[72:75], v[80:83], v[60:63]\n\t"
      "ds_read_b64_tr_b16 v[136:137], v171 offset:32768\n\t"
      "v_cvt_pk_bf16_f32 v1, v18, v19\n\t"
      "v_and_b32_e32 v6, v184, v4\n\t"
      "v_and_b32_e32 v7, v185, v5\n\t"
      "v_mfma_f32_16x16x32_bf16 v[44:47], v[64:67], v[80:83], v[44:47]\n\t"
      "ds_read_b64_tr_b16 v[138:139], v171 offset:33280\n\t"
      "v_and_b32_e32 v2, v184, v0\n\t"
      "v_and_b32_e32 v3, v185, v1\n\t"
      "v_xor_b32_e32 v4, v4, v6\n\t"
      "s_waitcnt lgkmcnt(7)\n\t"
      "v_mfma_f32_16x16x32_bf16 v[48:51], v[96:99], v[104:107], v[48:51]\n\t"
      "ds_read_b64_tr_b16 v[76:77], v170 offset:33792\n\t"
      "ds_read_b64_tr_b16 v[78:79], v170 offset:34304\n\t"
      "v_xor_b32_e32 v5, v5, v7\n\t"
      "v_xor_b32_e32 v0, v0, v2\n\t"
      "v_xor_b32_e32 v1, v1, v3\n\t"
      "v_mfma_f32_16x16x32_bf16 v[56:59], v[100:103], v[104:107], v[56:59]\n\t"
      "ds_read_b128 v[68:71], v161 offset:13312\n\t"
      "v_mfma_f32_16x16x32_bf16 v[40:43], v[92:95], v[104:107], v[40:43]\n\t"
      "ds_read_b128 v[72:75], v161 offset:27648\n\t"
      "v_mfma_f32_16x16x32_bf16 v[52:55], v[96:99], v[108:111], v[52:55]\n\t"
      "ds_read_b128 v[64:67], v165 offset:13312\n\t"
      "v_mfma_f32_16x16x32_bf16 v[60:63], v[100:103], v[108:111], v[60:63]\n\t"
      "ds_read_b64_tr_b16 v[80:81], v171 offset:33792\n\t"
      "v_mfma_f32_16x16x32_bf16 v[44:47], v[92:95], v[108:111], v[44:47]\n\t"
      "ds_read_b64_tr_b16 v[82:83], v171 offset:34304\n\t"
      "s_waitcnt lgkmcnt(7)\n\t"
      "v_mfma_f32_16x16x32_bf16 v[48:51], v[124:127], v[132:135], v[48:51]\n\t"
      "v_mfma_f32_16x16x32_bf16 v[56:59], v[128:131], v[132:135], v[56:59]\n\t"
      "v_mfma_f32_16x16x32_bf16 v[40:43], v[120:123], v[132:135], v[40:43]\n\t"
      "v_mfma_f32_16x16x32_bf16 v[52:55], v[124:127], v[136:139], v[52:55]\n\t"
      "v_mfma_f32_16x16x32_bf16 v[60:63], v[128:131], v[136:139], v[60:63]\n\t"
      "v_mfma_f32_16x16x32_bf16 v[44:47], v[120:123], v[136:139], v[44:47]\n\t"
      "s_waitcnt lgkmcnt(0)\n\t"
      "v_mfma_f32_16x16x32_bf16 v[48:51], v[68:71], v[76:79], v[48:51]\n\t"
      "v_mfma_f32_16x16x32_bf16 v[56:59], v[72:75], v[76:79], v[56:59]\n\t"
      "v_mfma_f32_16x16x32_bf16 v[40:43], v[64:67], v[76:79], v[40:43]\n\t"
      "v_mfma_f32_16x16x32_bf16 v[52:55], v[68:71], v[80:83], v[52:55]\n\t"
      "v_mfma_f32_16x16x32_bf16 v[60:63], v[72:75], v[80:83], v[60:63]\n\t"
      "v_mfma_f32_16x16x32_bf16 v[44:47], v[64:67], v[80:83], v[44:47]\n\t"
      "s_nop 2\n\t"
      "v_cvt_pk_bf16_f32 v66, v48, v49\n\t"
      "v_cvt_pk_bf16_f32 v67, v50, v51\n\t"
      "v_cvt_pk_bf16_f32 v68, v56, v57\n\t"
      "v_cvt_pk_bf16_f32 v69, v58, v59\n\t"
      "v_cvt_pk_bf16_f32 v64, v40, v41\n\t"
      "v_cvt_pk_bf16_f32 v65, v42, v43\n\t"
      "v_mfma_f32_16x16x32_bf16 v[88:91], v[66:69], v[4:7], 0\n\t"
      "s_nop 0\n\t"
      "v_mfma_f32_16x16x32_bf16 v[80:83], v[64:67], v[0:3], 0\n\t"
      "v_cvt_pk_bf16_f32 v72, v52, v53\n\t"
      "v_cvt_pk_bf16_f32 v73, v54, v55\n\t"
      "v_cvt_pk_bf16_f32 v74, v60, v61\n\t"
      "v_cvt_pk_bf16_f32 v75, v62, v63\n\t"
      "v_cvt_pk_bf16_f32 v70, v44, v45\n\t"
      "v_cvt_pk_bf16_f32 v71, v46, v47\n\t"
      "v_mfma_f32_16x16x32_bf16 v[92:95], v[72:75], v[4:7], 0\n\t"
      "s_nop 0\n\t"
      "v_mfma_f32_16x16x32_bf16 v[84:87], v[70:73], v[0:3], 0\n\t"
      "v_cvt_pk_bf16_f32 v152, v88, v89\n\t"
      "v_cvt_pk_bf16_f32 v153, v90, v91\n\t"
      "v_min_u32_e32 v152, -2, v152\n\t"
      "v_min_u32_e32 v153, -2, v153\n\t"
      "v_cvt_pk_bf16_f32 v148, v80, v81\n\t"
      "v_cvt_pk_bf16_f32 v149, v82, v83\n\t"
      "v_min_u32_e32 v148, -2, v148\n\t"
      "v_min_u32_e32 v149, -2, v149\n\t"
      "v_cvt_pk_bf16_f32 v154, v92, v93\n\t"
      "v_cvt_pk_bf16_f32 v155, v94, v95\n\t"
      "v_cndmask_b32_e64 v154, v154, 0, s[68:69]\n\t"
      "v_min_u32_e32 v154, -2, v154\n\t"
      "v_cndmask_b32_e64 v155, v155, 0, s[68:69]\n\t"
      "v_min_u32_e32 v155, -2, v155\n\t"
      "global_store_dwordx4 v244, v[152:155], s[44:45] sc1\n\t"
      "v_cvt_pk_bf16_f32 v150, v84, v85\n\t"
      "v_cvt_pk_bf16_f32 v151, v86, v87\n\t"
      "v_cndmask_b32_e64 v150, v150, 0, s[68:69]\n\t"
      "v_min_u32_e32 v150, -2, v150\n\t"
      "v_cndmask_b32_e64 v151, v151, 0, s[68:69]\n\t"
      "v_min_u32_e32 v151, -2, v151\n\t"
      "global_store_dwordx4 v244, v[148:151], s[42:43] sc1\n\t"
      "s_barrier\n\t"
      "s_cmp_eq_u32 s26, 7\n\t"
      "s_cbranch_scc1 SPARE_%=\n\t"
      "s_lshl_b32 s34, s25, 16\n\t"
      "s_lshl2_add_u32 s34, s39, s34\n\t"
      "s_add_u32 s34, s34, 4096\n\t"
      "s_add_u32 s42, s20, s34\n\t"
      "s_addc_u32 s43, s21, 0\n\t"
      "s_add_u32 s44, s42, 8192\n\t"
      "s_addc_u32 s45, s43, 0\n\t"
      "global_load_dwordx4 v[208:211], v228, s[4:5]\n\t"
      "global_load_dwordx4 v[212:215], v228, s[4:5] offset:64\n\t"
      "s_lshr_b32 s35, s2, 6\n\t"
      "s_lshl2_add_u32 s31, s26, s35\n\t"
      "s_and_b32 s35, s2, 7\n\t"
      "s_mul_i32 s34, s35, 14\n\t"
      "s_lshl4_add_u32 s35, s35, -14\n\t"
      "s_cmp_lt_u32 s31, 14\n\t"
      "s_cselect_b32 s34, s34, s35\n\t"
      "s_cselect_b32 s36, 1, 0\n\t"
      "s_or_b32 s27, s27, s36\n\t"
      "s_add_u32 s34, s34, s31\n\t"
      "s_lshr_b32 s29, s34, 2\n\t"
      "s_and_b32 s30, s34, 3\n\t"
      "s_mul_i32 s30, s30, 7\n\t"
      "v_and_b32_e32 v199, 60, v192\n\t"
      "v_and_b32_e32 v200, 24, v192\n\t"
      "v_lshlrev_b32_e32 v202, 3, v196\n\t"
      "v_lshl_or_b32 v202, v200, 2, v202\n\t"
      "v_lshl_or_b32 v202, v195, 7, v202\n\t"
      "v_lshl_or_b32 v202, v193, 10, v202\n\t"
      "v_add_u32_e32 v224, s39, v202\n\t"
      "v_xor_b32_e32 v225, 32, v224\n\t"
      "v_xor_b32_e32 v226, 64, v224\n\t"
      "v_xor_b32_e32 v227, 96, v224\n\t"
      "s_lshl_b32 s34, s24, 12\n\t"
      "s_add_u32 s34, s34, 98304\n\t"
      "v_add_u32_e32 v245, s34, v244\n\t"
      "v_lshlrev_b32_e32 v199, 2, v199\n\t"
      "s_lshl_b32 s34, s24, 8\n\t"
      "v_lshl_add_u32 v247, v192, 2, s34\n\t"
      "v_and_b32_e32 v200, 2, v192\n\t"
      "v_mul_u32_u24_e32 v202, 576, v200\n\t"
      "v_xor_b32_e32 v199, v202, v199\n\t"
      "v_and_b32_e32 v202, 1, v192\n\t"
      "v_lshl_or_b32 v199, v202, 3, v199\n\t"
      "s_and_b32 s35, s34, 0x400\n\t"
      "s_add_u32 s34, s34, s35\n\t"
      "s_add_u32 s34, s34, 98304\n\t"
      "v_add_u32_e32 v246, s34, v199\n\t"
      "v_lshlrev_b32_e32 v199, 2, v202\n\t"
      "v_lshl_or_b32 v199, v200, 4, v199\n\t"
      "s_barrier\n\t"
      "ds_read_b64_tr_b16 v[0:1], v216\n\t"
      "ds_read_b64_tr_b16 v[2:3], v220\n\t"
      "ds_read_b64_tr_b16 v[4:5], v217\n\t"
      "ds_read_b64_tr_b16 v[6:7], v221\n\t"
      "ds_read_b64_tr_b16 v[8:9], v218\n\t"
      "ds_read_b64_tr_b16 v[10:11], v222\n\t"
      "ds_read_b64_tr_b16 v[12:13], v219\n\t"
      "ds_read_b64_tr_b16 v[14:15], v223\n\t"
      "ds_read_b64_tr_b16 v[16:17], v218 offset:3584\n\t"
      "ds_read_b64_tr_b16 v[18:19], v222 offset:3584\n\t"
      "ds_read_b64_tr_b16 v[20:21], v219 offset:3584\n\t"
      "ds_read_b64_tr_b16 v[22:23], v223 offset:3584\n\t"
      "ds_read_b64_tr_b16 v[24:25], v216 offset:3584\n\t"
      "ds_read_b64_tr_b16 v[26:27], v220 offset:3584\n\t"
      "ds_read_b64_tr_b16 v[28:29], v217 offset:3584\n\t"
      "ds_read_b64_tr_b16 v[30:31], v221 offset:3584\n\t"
      "ds_read_b64_tr_b16 v[32:33], v216 offset:7168\n\t"
      "ds_read_b64_tr_b16 v[34:35], v220 offset:7168\n\t"
      "ds_read_b64_tr_b16 v[36:37], v217 offset:7168\n\t"
      "ds_read_b64_tr_b16 v[38:39], v221 offset:7168\n\t"
      "ds_read_b64_tr_b16 v[40:41], v218 offset:7168\n\t"
      "ds_read_b64_tr_b16 v[42:43], v222 offset:7168\n\t"
      "ds_read_b64_tr_b16 v[44:45], v219 offset:7168\n\t"
      "ds_read_b64_tr_b16 v[46:47], v223 offset:7168\n\t"
      "ds_read_b64_tr_b16 v[48:49], v218 offset:10752\n\t"
      "ds_read_b64_tr_b16 v[50:51], v222 offset:10752\n\t"
      "ds_read_b64_tr_b16 v[52:53], v219 offset:10752\n\t"
      "ds_read_b64_tr_b16 v[54:55], v223 offset:10752\n\t"
      "ds_read_b64_tr_b16 v[56:57], v216 offset:10752\n\t"
      "ds_read_b64_tr_b16 v[58:59], v220 offset:10752\n\t"
      "ds_read_b64_tr_b16 v[60:61], v217 offset:10752\n\t"
      "ds_read_b64_tr_b16 v[62:63], v221 offset:10752\n\t"
      "ds_read_b64_tr_b16 v[64:65], v216 offset:14336\n\t"
      "ds_read_b64_tr_b16 v[66:67], v220 offset:14336\n\t"
      "ds_read_b64_tr_b16 v[68:69], v217 offset:14336\n\t"
      "ds_read_b64_tr_b16 v[70:71], v221 offset:14336\n\t"
      "ds_read_b64_tr_b16 v[72:73], v218 offset:14336\n\t"
      "ds_read_b64_tr_b16 v[74:75], v222 offset:14336\n\t"
      "ds_read_b64_tr_b16 v[76:77], v219 offset:14336\n\t"
      "ds_read_b64_tr_b16 v[78:79], v223 offset:14336\n\t"
      "ds_read_b64_tr_b16 v[80:81], v218 offset:17920\n\t"
      "ds_read_b64_tr_b16 v[82:83], v222 offset:17920\n\t"
      "ds_read_b64_tr_b16 v[84:85], v219 offset:17920\n\t"
      "ds_read_b64_tr_b16 v[86:87], v223 offset:17920\n\t"
      "ds_read_b64_tr_b16 v[88:89], v216 offset:17920\n\t"
      "ds_read_b64_tr_b16 v[90:91], v220 offset:17920\n\t"
      "ds_read_b64_tr_b16 v[92:93], v217 offset:17920\n\t"
      "ds_read_b64_tr_b16 v[94:95], v221 offset:17920\n\t"
      "ds_read_b64_tr_b16 v[96:97], v216 offset:21504\n\t"
      "ds_read_b64_tr_b16 v[98:99], v220 offset:21504\n\t"
      "ds_read_b64_tr_b16 v[100:101], v217 offset:21504\n\t"
      "ds_read_b64_tr_b16 v[102:103], v221 offset:21504\n\t"
      "ds_read_b64_tr_b16 v[104:105], v218 offset:21504\n\t"
      "ds_read_b64_tr_b16 v[106:107], v222 offset:21504\n\t"
      "ds_read_b64_tr_b16 v[108:109], v219 offset:21504\n\t"
      "ds_read_b64_tr_b16 v[110:111], v223 offset:21504\n\t"
      "ds_read_b64_tr_b16 v[112:113], v218 offset:25088\n\t"
      "ds_read_b64_tr_b16 v[114:115], v222 offset:25088\n\t"
      "ds_read_b64_tr_b16 v[116:117], v219 offset:25088\n\t"
      "ds_read_b64_tr_b16 v[118:119], v223 offset:25088\n\t"
      "ds_read_b64_tr_b16 v[120:121], v216 offset:25088\n\t"
      "ds_read_b64_tr_b16 v[122:123], v220 offset:25088\n\t"
      "ds_read_b64_tr_b16 v[124:125], v217 offset:25088\n\t"
      "ds_read_b64_tr_b16 v[126:127], v221 offset:25088\n\t"
      "global_load_dwordx4 v[128:131], v244, s[42:43] offset:-4096 sc1\n\t"
      "global_load_dwordx4 v[132:135], v244, s[42:43] offset:-3072 sc1\n\t"
      "global_load_dwordx4 v[136:139], v244, s[42:43] offset:-2048 sc1\n\t"
      "global_load_dwordx4 v[140:143], v244, s[42:43] offset:-1024 sc1\n\t"
      "global_load_dwordx4 v[144:147], v244, s[42:43] sc1\n\t"
      "global_load_dwordx4 v[148:151], v244, s[42:43] offset:1024 sc1\n\t"
      "global_load_dwordx4 v[152:155], v244, s[42:43] offset:2048 sc1\n\t"
      "global_load_dwordx4 v[156:159], v244, s[42:43] offset:3072 sc1\n\t"
      "global_load_dwordx4 v[160:163], v244, s[44:45] offset:-4096 sc1\n\t"
      "global_load_dwordx4 v[164:167], v244, s[44:45] offset:-3072 sc1\n\t"
      "s_min_u32 s34, s29, 27\n\t"
      "s_lshl_b32 s34, s34, 7\n\t"
      "s_lshr_b32 s35, s24, 2\n\t"
      "s_lshl_b32 s35, s35, 6\n\t"
      "s_add_u32 s34, s34, s35\n\t"
      "s_lshr_b32 s35, s39, 9\n\t"
      "s_add_u32 s34, s34, s35\n\t"
      "v_add_u32_e32 v248, s34, v199\n\t"
      "s_add_u32 s48, s20, 8388608\n\t"
      "s_addc_u32 s49, s21, 0\n\t"
      "v_and_b32_e32 v199, 16, v192\n\t"
      "v_lshlrev_b32_e32 v199, 1, v199\n\t"
      "v_and_b32_e32 v200, 32, v192\n\t"
      "s_lshl_b32 s34, s28, 18\n\t"
      "s_lshl_b32 s35, s25, 11\n\t"
      "s_add_u32 s34, s34, s35\n\t"
      "s_lshr_b32 s35, s39, 3\n\t"
      "s_add_u32 s34, s34, s35\n\t"
      "v_and_b32_e32 v201, 0x1f0, v244\n\t"
      "v_lshl_or_b32 v201, v200, 11, v201\n\t"
      "v_add_u32_e32 v229, s34, v201\n\t"
      "v_add_u32_e32 v230, 131072, v229\n\t"
      "s_and_b32 s34, s25, 30\n\t"
      "s_lshr_b32 s35, s39, 13\n\t"
      "s_add_u32 s34, s34, s35\n\t"
      "s_mul_i32 s34, s34, 7\n\t"
      "s_add_u32 s34, s34, s28\n\t"
      "s_lshl_b32 s34, s34, 3\n\t"
      "s_and_b32 s35, s25, 1\n\t"
      "s_add_u32 s34, s34, s35\n\t"
      "s_lshl_b32 s34, s34, 10\n\t"
      "s_and_b32 s35, s24, 1\n\t"
      "s_lshl_b32 s35, s35, 9\n\t"
      "s_add_u32 s34, s34, s35\n\t"
      "s_add_u32 s34, s34, 6293504\n\t"
      "v_lshrrev_b32_e32 v201, 1, v200\n\t"
      "v_lshl_or_b32 v201, v199, 6, v201\n\t"
      "v_lshl_or_b32 v201, v194, 5, v201\n\t"
      "v_add_u32_e32 v241, s34, v201\n\t"
      "s_lshl_b32 s34, s24, 12\n\t"
      "s_add_u32 s34, s34, 131072\n\t"
      "v_xor_b32_e32 v201, v193, v194\n\t"
      "v_lshlrev_b32_e32 v201, 3, v201\n\t"
      "v_lshl_or_b32 v201, v194, 7, v201\n\t"
      "v_add_u32_e32 v233, s34, v201\n\t"
      "v_lshrrev_b32_e32 v199, 2, v199\n\t"
      "v_or_b32_e32 v199, v199, v195\n\t"
      "v_lshrrev_b32_e32 v200, 3, v200\n\t"
      "v_or_b32_e32 v200, v196, v200\n\t"
      "v_xor_b32_e32 v200, v200, v199\n\t"
      "v_lshlrev_b32_e32 v200, 3, v200\n\t"
      "v_lshl_or_b32 v200, v199, 7, v200\n\t"
      "v_add_u32_e32 v237, s34, v200\n\t"
      "v_xor_b32_e32 v234, 32, v233\n\t"
      "v_xor_b32_e32 v238, 32, v237\n\t"
      "v_xor_b32_e32 v235, 64, v233\n\t"
      "v_xor_b32_e32 v239, 64, v237\n\t"
      "v_xor_b32_e32 v236, 96, v233\n\t"
      "v_xor_b32_e32 v240, 96, v237\n\t"
      "s_waitcnt vmcnt(10)\n\t"
      "v_permlane16_swap_b32_e32 v208, v210\n\t"
      "v_permlane16_swap_b32_e32 v209, v211\n\t"
      "v_permlane16_swap_b32_e32 v212, v214\n\t"
      "v_permlane16_swap_b32_e32 v213, v215\n\t"
      "v_lshlrev_b32_e32 v192, 16, v208\n\t"
      "v_and_b32_e32 v193, 0xffff0000, v208\n\t"
      "v_lshlrev_b32_e32 v194, 16, v209\n\t"
      "v_and_b32_e32 v195, 0xffff0000, v209\n\t"
      "v_lshlrev_b32_e32 v196, 16, v210\n\t"
      "v_and_b32_e32 v197, 0xffff0000, v210\n\t"
      "v_lshlrev_b32_e32 v198, 16, v211\n\t"
      "v_and_b32_e32 v199, 0xffff0000, v211\n\t"
      "v_lshlrev_b32_e32 v200, 16, v212\n\t"
      "v_and_b32_e32 v201, 0xffff0000, v212\n\t"
      "v_lshlrev_b32_e32 v202, 16, v213\n\t"
      "v_and_b32_e32 v203, 0xffff0000, v213\n\t"
      "v_lshlrev_b32_e32 v204, 16, v214\n\t"
      "v_and_b32_e32 v205, 0xffff0000, v214\n\t"
      "v_lshlrev_b32_e32 v206, 16, v215\n\t"
      "v_and_b32_e32 v207, 0xffff0000, v215\n\t"
      "global_load_dwordx4 v[168:171], v244, s[44:45] offset:-2048 sc1\n\t"
      "global_load_dwordx4 v[172:175], v244, s[44:45] offset:-1024 sc1\n\t"
      "global_load_dwordx4 v[176:179], v244, s[44:45] sc1\n\t"
      "global_load_dwordx4 v[180:183], v244, s[44:45] offset:1024 sc1\n\t"
      "global_load_dwordx4 v[184:187], v244, s[44:45] offset:2048 sc1\n\t"
      "global_load_dwordx4 v[188:191], v244, s[44:45] offset:3072 sc1\n\t"
      "global_load_dword v249, v248, s[12:13]\n\t"
      "s_barrier\n\t"
      "s_waitcnt lgkmcnt(0)\n\t"
      "s_waitcnt vmcnt(15)\n\t"
      "v_max3_u32 v232, v128, v129, v130\n\t"
      "v_max3_u32 v232, v232, v131, v132\n\t"
      "v_max3_u32 v232, v232, v133, v134\n\t"
      "v_max_u32_e32 v232, v232, v135\n\t"
      "v_cmp_eq_u32_e32 vcc, -1, v232\n\t"
      "s_cbranch_vccnz LO0R_%=\n\t"
      "LO0B_%=:\n\t"
      "v_mfma_f32_16x16x32_bf16 v[192:195], v[0:3], v[128:131], v[192:195]\n\t"
      "ds_read_b64_tr_b16 v[0:1], v216 offset:28672\n\t"
      "ds_read_b64_tr_b16 v[2:3], v220 offset:28672\n\t"
      "v_mfma_f32_16x16x32_bf16 v[196:199], v[4:7], v[128:131], v[196:199]\n\t"
      "ds_read_b64_tr_b16 v[4:5], v217 offset:28672\n\t"
      "v_mfma_f32_16x16x32_bf16 v[200:203], v[8:11], v[128:131], v[200:203]\n\t"
      "ds_read_b64_tr_b16 v[6:7], v221 offset:28672\n\t"
      "v_mfma_f32_16x16x32_bf16 v[204:207], v[12:15], v[128:131], v[204:207]\n\t"
      "ds_read_b64_tr_b16 v[8:9], v218 offset:28672\n\t"
      "ds_read_b64_tr_b16 v[10:11], v222 offset:28672\n\t"
      "v_mfma_f32_16x16x32_bf16 v[192:195], v[16:19], v[132:135], v[192:195]\n\t"
      "ds_read_b64_tr_b16 v[12:13], v219 offset:28672\n\t"
      "v_mfma_f32_16x16x32_bf16 v[196:199], v[20:23], v[132:135], v[196:199]\n\t"
      "ds_read_b64_tr_b16 v[14:15], v223 offset:28672\n\t"
      "v_mfma_f32_16x16x32_bf16 v[200:203], v[24:27], v[132:135], v[200:203]\n\t"
      "ds_read_b64_tr_b16 v[16:17], v218 offset:32256\n\t"
      "ds_read_b64_tr_b16 v[18:19], v222 offset:32256\n\t"
      "v_mfma_f32_16x16x32_bf16 v[204:207], v[28:31], v[132:135], v[204:207]\n\t"
      "ds_read_b64_tr_b16 v[20:21], v219 offset:32256\n\t"
      "s_waitcnt vmcnt(13)\n\t"
      "v_max3_u32 v232, v136, v137, v138\n\t"
      "v_max3_u32 v232, v232, v139, v140\n\t"
      "v_max3_u32 v232, v232, v141, v142\n\t"
      "v_max_u32_e32 v232, v232, v143\n\t"
      "v_cmp_eq_u32_e32 vcc, -1, v232\n\t"
      "s_cbranch_vccnz LO2R_%=\n\t"
      "LO2B_%=:\n\t"
      "v_mfma_f32_16x16x32_bf16 v[192:195], v[32:35], v[136:139], v[192:195]\n\t"
      "ds_read_b64_tr_b16 v[22:23], v223 offset:32256\n\t"
      "v_mfma_f32_16x16x32_bf16 v[196:199], v[36:39], v[136:139], v[196:199]\n\t"
      "ds_read_b64_tr_b16 v[24:25], v216 offset:32256\n\t"
      "ds_read_b64_tr_b16 v[26:27], v220 offset:32256\n\t"
      "v_mfma_f32_16x16x32_bf16 v[200:203], v[40:43], v[136:139], v[200:203]\n\t"
      "ds_read_b64_tr_b16 v[28:29], v217 offset:32256\n\t"
      "v_mfma_f32_16x16x32_bf16 v[204:207], v[44:47], v[136:139], v[204:207]\n\t"
      "ds_read_b64_tr_b16 v[30:31], v221 offset:32256\n\t"
      "v_mfma_f32_16x16x32_bf16 v[192:195], v[48:51], v[140:143], v[192:195]\n\t"
      "ds_read_b64_tr_b16 v[32:33], v216 offset:35840\n\t"
      "ds_read_b64_tr_b16 v[34:35], v220 offset:35840\n\t"
      "v_mfma_f32_16x16x32_bf16 v[196:199], v[52:55], v[140:143], v[196:199]\n\t"
      "ds_read_b64_tr_b16 v[36:37], v217 offset:35840\n\t"
      "v_mfma_f32_16x16x32_bf16 v[200:203], v[56:59], v[140:143], v[200:203]\n\t"
      "ds_read_b64_tr_b16 v[38:39], v221 offset:35840\n\t"
      "v_mfma_f32_16x16x32_bf16 v[204:207], v[60:63], v[140:143], v[204:207]\n\t"
      "ds_read_b64_tr_b16 v[40:41], v218 offset:35840\n\t"
      "ds_read_b64_tr_b16 v[42:43], v222 offset:35840\n\t"
      "s_waitcnt vmcnt(11)\n\t"
      "v_max3_u32 v232, v144, v145, v146\n\t"
      "v_max3_u32 v232, v232, v147, v148\n\t"
      "v_max3_u32 v232, v232, v149, v150\n\t"
      "v_max_u32_e32 v232, v232, v151\n\t"
      "v_cmp_eq_u32_e32 vcc, -1, v232\n\t"
      "s_cbranch_vccnz LO4R_%=\n\t"
      "LO4B_%=:\n\t"
      "v_mfma_f32_16x16x32_bf16 v[192:195], v[64:67], v[144:147], v[192:195]\n\t"
      "ds_read_b64_tr_b16 v[44:45], v219 offset:35840\n\t"
      "v_mfma_f32_16x16x32_bf16 v[196:199], v[68:71], v[144:147], v[196:199]\n\t"
      "ds_read_b64_tr_b16 v[46:47], v223 offset:35840\n\t"
      "v_mfma_f32_16x16x32_bf16 v[200:203], v[72:75], v[144:147], v[200:203]\n\t"
      "ds_read_b64_tr_b16 v[48:49], v218 offset:39424\n\t"
      "ds_read_b64_tr_b16 v[50:51], v222 offset:39424\n\t"
      "v_mfma_f32_16x16x32_bf16 v[204:207], v[76:79], v[144:147], v[204:207]\n\t"
      "ds_read_b64_tr_b16 v[52:53], v219 offset:39424\n\t"
      "v_mfma_f32_16x16x32_bf16 v[192:195], v[80:83], v[148:151], v[192:195]\n\t"
      "ds_read_b64_tr_b16 v[54:55], v223 offset:39424\n\t"
      "v_mfma_f32_16x16x32_bf16 v[196:199], v[84:87], v[148:151], v[196:199]\n\t"
      "ds_read_b64_tr_b16 v[56:57], v216 offset:39424\n\t"
      "ds_read_b64_tr_b16 v[58:59], v220 offset:39424\n\t"
      "v_mfma_f32_16x16x32_bf16 v[200:203], v[88:91], v[148:151], v[200:203]\n\t"
      "ds_read_b64_tr_b16 v[60:61], v217 offset:39424\n\t"
      "v_mfma_f32_16x16x32_bf16 v[204:207], v[92:95], v[148:151], v[204:207]\n\t"
      "ds_read_b64_tr_b16 v[62:63], v221 offset:39424\n\t"
      "s_waitcnt vmcnt(9)\n\t"
      "v_max3_u32 v232, v152, v153, v154\n\t"
      "v_max3_u32 v232, v232, v155, v156\n\t"
      "v_max3_u32 v232, v232, v157, v158\n\t"
      "v_max_u32_e32 v232, v232, v159\n\t"
      "v_cmp_eq_u32_e32 vcc, -1, v232\n\t"
      "s_cbranch_vccnz LO6R_%=\n\t"
      "LO6B_%=:\n\t"
      "v_mfma_f32_16x16x32_bf16 v[192:195], v[96:99], v[152:155], v[192:195]\n\t"
      "ds_read_b64_tr_b16 v[64:65], v216 offset:43008\n\t"
      "ds_read_b64_tr_b16 v[66:67], v220 offset:43008\n\t"
      "v_mfma_f32_16x16x32_bf16 v[196:199], v[100:103], v[152:155], v[196:199]\n\t"
      "ds_read_b64_tr_b16 v[68:69], v217 offset:43008\n\t"
      "v_mfma_f32_16x16x32_bf16 v[200:203], v[104:107], v[152:155], v[200:203]\n\t"
      "ds_read_b64_tr_b16 v[70:71], v221 offset:43008\n\t"
      "v_mfma_f32_16x16x32_bf16 v[204:207], v[108:111], v[152:155], v[204:207]\n\t"
      "ds_read_b64_tr_b16 v[72:73], v218 offset:43008\n\t"
      "ds_read_b64_tr_b16 v[74:75], v222 offset:43008\n\t"
      "v_mfma_f32_16x16x32_bf16 v[192:195], v[112:115], v[156:159], v[192:195]\n\t"
      "ds_read_b64_tr_b16 v[76:77], v219 offset:43008\n\t"
      "v_mfma_f32_16x16x32_bf16 v[196:199], v[116:119], v[156:159], v[196:199]\n\t"
      "ds_read_b64_tr_b16 v[78:79], v223 offset:43008\n\t"
      "v_mfma_f32_16x16x32_bf16 v[200:203], v[120:123], v[156:159], v[200:203]\n\t"
      "ds_read_b64_tr_b16 v[80:81], v218 offset:46592\n\t"
      "ds_read_b64_tr_b16 v[82:83], v222 offset:46592\n\t"
      "v_mfma_f32_16x16x32_bf16 v[204:207], v[124:127], v[156:159], v[204:207]\n\t"
      "ds_read_b64_tr_b16 v[84:85], v219 offset:46592\n\t"
      "s_waitcnt vmcnt(7)\n\t"
      "v_max3_u32 v232, v160, v161, v162\n\t"
      "v_max3_u32 v232, v232, v163, v164\n\t"
      "v_max3_u32 v232, v232, v165, v166\n\t"
      "v_max_u32_e32 v232, v232, v167\n\t"
      "v_cmp_eq_u32_e32 vcc, -1, v232\n\t"
      "s_cbranch_vccnz LO8R_%=\n\t"
      "LO8B_%=:\n\t"
      "s_waitcnt lgkmcnt(14)\n\t"
      "v_mfma_f32_16x16x32_bf16 v[192:195], v[0:3], v[160:163], v[192:195]\n\t"
      "ds_read_b64_tr_b16 v[86:87], v223 offset:46592\n\t"
      "v_mfma_f32_16x16x32_bf16 v[196:199], v[4:7], v[160:163], v[196:199]\n\t"
      "ds_read_b64_tr_b16 v[88:89], v216 offset:46592\n\t"
      "ds_read_b64_tr_b16 v[90:91], v220 offset:46592\n\t"
      "v_mfma_f32_16x16x32_bf16 v[200:203], v[8:11], v[160:163], v[200:203]\n\t"
      "ds_read_b64_tr_b16 v[92:93], v217 offset:46592\n\t"
      "v_mfma_f32_16x16x32_bf16 v[204:207], v[12:15], v[160:163], v[204:207]\n\t"
      "ds_read_b64_tr_b16 v[94:95], v221 offset:46592\n\t"
      "v_mfma_f32_16x16x32_bf16 v[192:195], v[16:19], v[164:167], v[192:195]\n\t"
      "ds_read_b64_tr_b16 v[96:97], v216 offset:50176\n\t"
      "ds_read_b64_tr_b16 v[98:99], v220 offset:50176\n\t"
      "v_mfma_f32_16x16x32_bf16 v[196:199], v[20:23], v[164:167], v[196:199]\n\t"
      "ds_read_b64_tr_b16 v[100:101], v217 offset:50176\n\t"
      "v_mfma_f32_16x16x32_bf16 v[200:203], v[24:27], v[164:167], v[200:203]\n\t"
      "ds_read_b64_tr_b16 v[102:103], v221 offset:50176\n\t"
      "v_mfma_f32_16x16x32_bf16 v[204:207], v[28:31], v[164:167], v[204:207]\n\t"
      "ds_read_b64_tr_b16 v[104:105], v218 offset:50176\n\t"
      "ds_read_b64_tr_b16 v[106:107], v222 offset:50176\n\t"
      "s_waitcnt vmcnt(5)\n\t"
      "v_max3_u32 v232, v168, v169, v170\n\t"
      "v_max3_u32 v232, v232, v171, v172\n\t"
      "v_max3_u32 v232, v232, v173, v174\n\t"
      "v_max_u32_e32 v232, v232, v175\n\t"
      "v_cmp_eq_u32_e32 vcc, -1, v232\n\t"
      "s_cbranch_vccnz LO10R_%=\n\t"
      "LO10B_%=:\n\t"
      "v_mfma_f32_16x16x32_bf16 v[192:195], v[32:35], v[168:171], v[192:195]\n\t"
      "ds_read_b64_tr_b16 v[108:109], v219 offset:50176\n\t"
      "v_mfma_f32_16x16x32_bf16 v[196:199], v[36:39], v[168:171], v[196:199]\n\t"
      "ds_read_b64_tr_b16 v[110:111], v223 offset:50176\n\t"
      "v_mfma_f32_16x16x32_bf16 v[200:203], v[40:43], v[168:171], v[200:203]\n\t"
      "ds_read_b64_tr_b16 v[112:113], v218 offset:53760\n\t"
      "ds_read_b64_tr_b16 v[114:115], v222 offset:53760\n\t"
      "v_mfma_f32_16x16x32_bf16 v[204:207], v[44:47], v[168:171], v[204:207]\n\t"
      "ds_read_b64_tr_b16 v[116:117], v219 offset:53760\n\t"
      "s_waitcnt lgkmcnt(14)\n\t"
      "v_mfma_f32_16x16x32_bf16 v[192:195], v[48:51], v[172:175], v[192:195]\n\t"
      "ds_read_b64_tr_b16 v[118:119], v223 offset:53760\n\t"
      "v_mfma_f32_16x16x32_bf16 v[196:199], v[52:55], v[172:175], v[196:199]\n\t"
      "ds_read_b64_tr_b16 v[120:121], v216 offset:53760\n\t"
      "ds_read_b64_tr_b16 v[122:123], v220 offset:53760\n\t"
      "v_mfma_f32_16x16x32_bf16 v[200:203], v[56:59], v[172:175], v[200:203]\n\t"
      "ds_read_b64_tr_b16 v[124:125], v217 offset:53760\n\t"
      "v_mfma_f32_16x16x32_bf16 v[204:207], v[60:63], v[172:175], v[204:207]\n\t"
      "ds_read_b64_tr_b16 v[126:127], v221 offset:53760\n\t"
      "s_waitcnt vmcnt(3)\n\t"
      "v_max3_u32 v232, v176, v177, v178\n\t"
      "v_max3_u32 v232, v232, v179, v180\n\t"
      "v_max3_u32 v232, v232, v181, v182\n\t"
      "v_max_u32_e32 v232, v232, v183\n\t"
      "v_cmp_eq_u32_e32 vcc, -1, v232\n\t"
      "s_cbranch_vccnz LO12R_%=\n\t"
      "LO12B_%=:\n\t"
      "v_mfma_f32_16x16x32_bf16 v[192:195], v[64:67], v[176:179], v[192:195]\n\t"
      "v_mfma_f32_16x16x32_bf16 v[196:199], v[68:71], v[176:179], v[196:199]\n\t"
      "v_mfma_f32_16x16x32_bf16 v[200:203], v[72:75], v[176:179], v[200:203]\n\t"
      "v_mfma_f32_16x16x32_bf16 v[204:207], v[76:79], v[176:179], v[204:207]\n\t"
      "s_waitcnt lgkmcnt(14)\n\t"
      "v_mfma_f32_16x16x32_bf16 v[192:195], v[80:83], v[180:183], v[192:195]\n\t"
      "v_mfma_f32_16x16x32_bf16 v[196:199], v[84:87], v[180:183], v[196:199]\n\t"
      "v_mfma_f32_16x16x32_bf16 v[200:203], v[88:91], v[180:183], v[200:203]\n\t"
      "v_mfma_f32_16x16x32_bf16 v[204:207], v[92:95], v[180:183], v[204:207]\n\t"
      "s_waitcnt vmcnt(1)\n\t"
      "v_max3_u32 v232, v184, v185, v186\n\t"
      "v_max3_u32 v232, v232, v187, v188\n\t"
      "v_max3_u32 v232, v232, v189, v190\n\t"
      "v_max_u32_e32 v232, v232, v191\n\t"
      "v_cmp_eq_u32_e32 vcc, -1, v232\n\t"
      "s_cbranch_vccnz LO14R_%=\n\t"
      "LO14B_%=:\n\t"
      "s_waitcnt lgkmcnt(8)\n\t"
      "v_mfma_f32_16x16x32_bf16 v[192:195], v[96:99], v[184:187], v[192:195]\n\t"
      "v_mfma_f32_16x16x32_bf16 v[196:199], v[100:103], v[184:187], v[196:199]\n\t"
      "v_mfma_f32_16x16x32_bf16 v[200:203], v[104:107], v[184:187], v[200:203]\n\t"
      "v_mfma_f32_16x16x32_bf16 v[204:207], v[108:111], v[184:187], v[204:207]\n\t"
      "s_waitcnt lgkmcnt(0)\n\t"
      "v_mfma_f32_16x16x32_bf16 v[192:195], v[112:115], v[188:191], v[192:195]\n\t"
      "v_mfma_f32_16x16x32_bf16 v[196:199], v[116:119], v[188:191], v[196:199]\n\t"
      "v_mfma_f32_16x16x32_bf16 v[200:203], v[120:123], v[188:191], v[200:203]\n\t"
      "v_mfma_f32_16x16x32_bf16 v[204:207], v[124:127], v[188:191], v[204:207]\n\t"
      "s_nop 4\n\t"
      "v_cvt_pk_bf16_f32 v208, v192, v193\n\t"
      "v_cvt_pk_bf16_f32 v209, v194, v195\n\t"
      "v_cvt_pk_bf16_f32 v210, v196, v197\n\t"
      "v_cvt_pk_bf16_f32 v211, v198, v199\n\t"
      "v_cvt_pk_bf16_f32 v212, v200, v201\n\t"
      "v_cvt_pk_bf16_f32 v213, v202, v203\n\t"
      "v_cvt_pk_bf16_f32 v214, v204, v205\n\t"
      "v_cvt_pk_bf16_f32 v215, v206, v207\n\t"
      "v_mov_b32_e32 v148, 0x3f803f80\n\t"
      "v_dot2_f32_bf16 v144, v208, v148, 0\n\t"
      "v_dot2_f32_bf16 v145, v208, v208, 0\n\t"
      "v_dot2_f32_bf16 v146, v209, v148, 0\n\t"
      "v_dot2_f32_bf16 v147, v209, v209, 0\n\t"
      "v_dot2_f32_bf16 v144, v210, v148, v144\n\t"
      "v_dot2_f32_bf16 v145, v210, v210, v145\n\t"
      "v_dot2_f32_bf16 v146, v211, v148, v146\n\t"
      "v_dot2_f32_bf16 v147, v211, v211, v147\n\t"
      "v_dot2_f32_bf16 v144, v212, v148, v144\n\t"
      "v_dot2_f32_bf16 v145, v212, v212, v145\n\t"
      "v_dot2_f32_bf16 v146, v213, v148, v146\n\t"
      "v_dot2_f32_bf16 v147, v213, v213, v147\n\t"
      "v_dot2_f32_bf16 v144, v214, v148, v144\n\t"
      "v_dot2_f32_bf16 v145, v214, v214, v145\n\t"
      "v_dot2_f32_bf16 v146, v215, v148, v146\n\t"
      "v_dot2_f32_bf16 v147, v215, v215, v147\n\t"
      "v_lshlrev_b32_e32 v128, 16, v208\n\t"
      "v_and_b32_e32 v129, 0xffff0000, v208\n\t"
      "v_lshlrev_b32_e32 v130, 16, v209\n\t"
      "v_pk_add_f32 v[144:145], v[144:145], v[146:147]\n\t"
      "v_and_b32_e32 v131, 0xffff0000, v209\n\t"
      "v_lshlrev_b32_e32 v132, 16, v210\n\t"
      "v_permlane16_swap_b32_e32 v144, v145\n\t"
      "v_add_f32_e32 v144, v144, v145\n\t"
      "v_mov_b32_e32 v145, v144\n\t"
      "v_and_b32_e32 v133, 0xffff0000, v210\n\t"
      "v_lshlrev_b32_e32 v134, 16, v211\n\t"
      "v_permlane32_swap_b32_e32 v144, v145\n\t"
      "v_add_f32_e32 v144, v144, v145\n\t"
      "v_mov_b32_e32 v145, v144\n\t"
      "v_and_b32_e32 v135, 0xffff0000, v211\n\t"
      "v_lshlrev_b32_e32 v136, 16, v212\n\t"
      "v_permlane16_swap_b32_e32 v144, v145\n\t"
      "s_mov_b64 exec, s[70:71]\n\t"
      "v_min_u32_e32 v144, -2, v144\n\t"
      "v_min_u32_e32 v145, -2, v145\n\t"
      "global_store_dwordx2 v231, v[144:145], s[58:59] sc1\n\t"
      "s_mov_b64 exec, -1\n\t"
      "s_barrier\n\t"
      "v_and_b32_e32 v137, 0xffff0000, v212\n\t"
      "v_lshlrev_b32_e32 v138, 16, v213\n\t"
      "v_and_b32_e32 v139, 0xffff0000, v213\n\t"
      "v_lshlrev_b32_e32 v140, 16, v214\n\t"
      "v_and_b32_e32 v141, 0xffff0000, v214\n\t"
      "v_lshlrev_b32_e32 v142, 16, v215\n\t"
      "v_and_b32_e32 v143, 0xffff0000, v215\n\t"
      "v_permlane16_swap_b32_e32 v208, v210\n\t"
      "v_permlane16_swap_b32_e32 v209, v211\n\t"
      "v_permlane16_swap_b32_e32 v212, v214\n\t"
      "v_permlane16_swap_b32_e32 v213, v215\n\t"
      "global_store_dwordx4 v241, v[208:211], s[20:21] offset:-2048 sc1\n\t"
      "global_store_dwordx4 v241, v[212:215], s[20:21] offset:2048 sc1\n\t"
      "s_bitcmp0_b32 s27, 0\n\t"
      "s_cbranch_scc1 NTK0_%=\n\t"
      "ds_read_b64_tr_b16 v[0:1], v224 offset:16384\n\t"
      "ds_read_b64_tr_b16 v[2:3], v224 offset:16896\n\t"
      "ds_read_b64_tr_b16 v[4:5], v225 offset:16384\n\t"
      "ds_read_b64_tr_b16 v[6:7], v225 offset:16896\n\t"
      "ds_read_b64_tr_b16 v[8:9], v226 offset:16384\n\t"
      "ds_read_b64_tr_b16 v[10:11], v226 offset:16896\n\t"
      "ds_read_b64_tr_b16 v[12:13], v227 offset:16384\n\t"
      "ds_read_b64_tr_b16 v[14:15], v227 offset:16896\n\t"
      "ds_read_b64_tr_b16 v[16:17], v224 offset:49152\n\t"
      "ds_read_b64_tr_b16 v[18:19], v224 offset:49664\n\t"
      "ds_read_b64_tr_b16 v[20:21], v225 offset:49152\n\t"
      "ds_read_b64_tr_b16 v[22:23], v225 offset:49664\n\t"
      "ds_read_b64_tr_b16 v[24:25], v226 offset:49152\n\t"
      "ds_read_b64_tr_b16 v[26:27], v226 offset:49664\n\t"
      "ds_read_b64_tr_b16 v[28:29], v227 offset:49152\n\t"
      "ds_read_b64_tr_b16 v[30:31], v227 offset:49664\n\t"
      "ds_read_b64_tr_b16 v[32:33], v224\n\t"
      "ds_read_b64_tr_b16 v[34:35], v224 offset:512\n\t"
      "ds_read_b64_tr_b16 v[36:37], v225\n\t"
      "ds_read_b64_tr_b16 v[38:39], v225 offset:512\n\t"
      "ds_read_b64_tr_b16 v[40:41], v226\n\t"
      "ds_read_b64_tr_b16 v[42:43], v226 offset:512\n\t"
      "ds_read_b64_tr_b16 v[44:45], v227\n\t"
      "ds_read_b64_tr_b16 v[46:47], v227 offset:512\n\t"
      "ds_read_b64_tr_b16 v[48:49], v224 offset:32768\n\t"
      "ds_read_b64_tr_b16 v[50:51], v224 offset:33280\n\t"
      "ds_read_b64_tr_b16 v[52:53], v225 offset:32768\n\t"
      "ds_read_b64_tr_b16 v[54:55], v225 offset:33280\n\t"
      "ds_read_b64_tr_b16 v[56:57], v226 offset:32768\n\t"
      "ds_read_b64_tr_b16 v[58:59], v226 offset:33280\n\t"
      "ds_read_b64_tr_b16 v[60:61], v227 offset:32768\n\t"
      "ds_read_b64_tr_b16 v[62:63], v227 offset:33280\n\t"
      "NTK0_%=:\n\t"
      "s_sleep 3\n\t"
      "global_load_dwordx2 v[148:149], v231, s[46:47] sc1\n\t"
      "global_load_dwordx2 v[150:151], v231, s[46:47] offset:384 sc1\n\t"
      "s_waitcnt lgkmcnt(0)\n\t"
      "s_barrier\n\t"
      "s_waitcnt vmcnt(0)\n\t"
      "v_max3_u32 v232, v148, v149, v150\n\t"
      "v_max_u32_e32 v232, v232, v151\n\t"
      "v_cmp_eq_u32_e32 vcc, -1, v232\n\t"
      "s_cbranch_vccnz LSR_%=\n\t"
      "LSB_%=:\n\t"
      "v_cndmask_b32_e64 v150, v150, 0, s[70:71]\n\t"
      "v_cndmask_b32_e64 v151, v151, 0, s[70:71]\n\t"
      "v_pk_add_f32 v[144:145], v[148:149], v[150:151]\n\t"
      "s_nop 1\n\t"
      "v_permlane16_swap_b32_e32 v144, v145\n\t"
      "v_add_f32_e32 v144, v144, v145\n\t"
      "v_mov_b32_e32 v145, v144\n\t"
      "s_nop 1\n\t"
      "v_permlane32_swap_b32_e32 v144, v145\n\t"
      "v_add_f32_e32 v144, v144, v145\n\t"
      "v_mov_b32_e32 v145, v144\n\t"
      "s_nop 1\n\t"
      "v_permlane16_swap_b32_e32 v144, v145\n\t"
      "v_mul_f32_e32 v144, 0xbb124925, v144\n\t"
      "v_mul_f32_e32 v145, 0x3b124925, v145\n\t"
      "v_fma_f32 v145, -v144, v144, v145\n\t"
      "v_max_f32_e32 v145, 0, v145\n\t"
      "v_add_f32_e32 v146, 0x3727c5ac, v145\n\t"
      "v_fmac_f32_e32 v145, 0x3727c5ac, v146\n\t"
      "v_rsq_f32_e32 v145, v145\n\t"
      "s_nop 0\n\t"
      "v_mul_f32_e32 v144, v144, v145\n\t"
      "v_pk_fma_f32 v[128:129], v[128:129], v[144:145], v[144:145] op_sel:[0,1,0] op_sel_hi:[1,1,0]\n\t"
      "v_pk_fma_f32 v[130:131], v[130:131], v[144:145], v[144:145] op_sel:[0,1,0] op_sel_hi:[1,1,0]\n\t"
      "v_cvt_pk_bf16_f32 v152, v128, v129\n\t"
      "v_cvt_pk_bf16_f32 v153, v130, v131\n\t"
      "ds_write_b64 v233, v[152:153]\n\t"
      "v_pk_fma_f32 v[132:133], v[132:133], v[144:145], v[144:145] op_sel:[0,1,0] op_sel_hi:[1,1,0]\n\t"
      "v_pk_fma_f32 v[134:135], v[134:135], v[144:145], v[144:145] op_sel:[0,1,0] op_sel_hi:[1,1,0]\n\t"
      "v_cvt_pk_bf16_f32 v154, v132, v133\n\t"
      "v_cvt_pk_bf16_f32 v155, v134, v135\n\t"
      "ds_write_b64 v234, v[154:155]\n\t"
      "ds_read_b64_tr_b16 v[160:161], v237\n\t"
      "ds_read_b64_tr_b16 v[162:163], v238 offset:512\n\t"
      "v_pk_fma_f32 v[136:137], v[136:137], v[144:145], v[144:145] op_sel:[0,1,0] op_sel_hi:[1,1,0]\n\t"
      "v_pk_fma_f32 v[138:139], v[138:139], v[144:145], v[144:145] op_sel:[0,1,0] op_sel_hi:[1,1,0]\n\t"
      "v_cvt_pk_bf16_f32 v156, v136, v137\n\t"
      "v_cvt_pk_bf16_f32 v157, v138, v139\n\t"
      "ds_write_b64 v235, v[156:157]\n\t"
      "v_pk_fma_f32 v[140:141], v[140:141], v[144:145], v[144:145] op_sel:[0,1,0] op_sel_hi:[1,1,0]\n\t"
      "v_pk_fma_f32 v[142:143], v[142:143], v[144:145], v[144:145] op_sel:[0,1,0] op_sel_hi:[1,1,0]\n\t"
      "v_cvt_pk_bf16_f32 v158, v140, v141\n\t"
      "v_cvt_pk_bf16_f32 v159, v142, v143\n\t"
      "ds_write_b64 v236, v[158:159]\n\t"
      "ds_read_b64_tr_b16 v[164:165], v239\n\t"
      "ds_read_b64_tr_b16 v[166:167], v240 offset:512\n\t"
      "s_waitcnt lgkmcnt(4)\n\t"
      "v_min_u32_e32 v160, -2, v160\n\t"
      "v_min_u32_e32 v161, -2, v161\n\t"
      "v_min_u32_e32 v162, -2, v162\n\t"
      "v_min_u32_e32 v163, -2, v163\n\t"
      "global_store_dwordx4 v229, v[160:163], s[48:49] sc1\n\t"
      "s_waitcnt lgkmcnt(0)\n\t"
      "v_min_u32_e32 v164, -2, v164\n\t"
      "v_min_u32_e32 v165, -2, v165\n\t"
      "v_min_u32_e32 v166, -2, v166\n\t"
      "v_min_u32_e32 v167, -2, v167\n\t"
      "global_store_dwordx4 v230, v[164:167], s[48:49] sc1\n\t"
      "s_setprio 0\n\t"
      "s_barrier\n\t"
      "s_bitcmp0_b32 s27, 0\n\t"
      "s_cbranch_scc1 G4_%=\n\t"
      "s_mul_i32 s34, s30, 65536\n\t"
      "s_mul_i32 s35, s24, 8192\n\t"
      "s_add_u32 s34, s34, s35\n\t"
      "s_add_u32 s64, s48, s34\n\t"
      "s_addc_u32 s65, s49, 0\n\t"
      "v_add_u32_e32 v250, 4096, v244\n\t"
      "global_load_dwordx4 v[128:131], v250, s[64:65] offset:-4096 sc1\n\t"
      "global_load_dwordx4 v[132:135], v250, s[64:65] offset:-3072 sc1\n\t"
      "global_load_dwordx4 v[136:139], v250, s[64:65] offset:-2048 sc1\n\t"
      "global_load_dwordx4 v[140:143], v250, s[64:65] offset:-1024 sc1\n\t"
      "global_load_dwordx4 v[144:147], v250, s[64:65] sc1\n\t"
      "global_load_dwordx4 v[148:151], v250, s[64:65] offset:1024 sc1\n\t"
      "global_load_dwordx4 v[152:155], v250, s[64:65] offset:2048 sc1\n\t"
      "global_load_dwordx4 v[156:159], v250, s[64:65] offset:3072 sc1\n\t"
      "ds_read_b64_tr_b16 v[64:65], v224 offset:16384\n\t"
      "ds_read_b64_tr_b16 v[66:67], v224 offset:16896\n\t"
      "ds_read_b64_tr_b16 v[68:69], v225 offset:16384\n\t"
      "ds_read_b64_tr_b16 v[70:71], v225 offset:16896\n\t"
      "ds_read_b64_tr_b16 v[72:73], v226 offset:16384\n\t"
      "ds_read_b64_tr_b16 v[74:75], v226 offset:16896\n\t"
      "ds_read_b64_tr_b16 v[76:77], v227 offset:16384\n\t"
      "ds_read_b64_tr_b16 v[78:79], v227 offset:16896\n\t"
      "ds_read_b64_tr_b16 v[80:81], v224 offset:49152\n\t"
      "ds_read_b64_tr_b16 v[82:83], v224 offset:49664\n\t"
      "ds_read_b64_tr_b16 v[84:85], v225 offset:49152\n\t"
      "ds_read_b64_tr_b16 v[86:87], v225 offset:49664\n\t"
      "ds_read_b64_tr_b16 v[88:89], v226 offset:49152\n\t"
      "ds_read_b64_tr_b16 v[90:91], v226 offset:49664\n\t"
      "ds_read_b64_tr_b16 v[92:93], v227 offset:49152\n\t"
      "ds_read_b64_tr_b16 v[94:95], v227 offset:49664\n\t"
      "ds_read_b64_tr_b16 v[96:97], v224\n\t"
      "ds_read_b64_tr_b16 v[98:99], v224 offset:512\n\t"
      "ds_read_b64_tr_b16 v[100:101], v225\n\t"
      "ds_read_b64_tr_b16 v[102:103], v225 offset:512\n\t"
      "ds_read_b64_tr_b16 v[104:105], v226\n\t"
      "ds_read_b64_tr_b16 v[106:107], v226 offset:512\n\t"
      "ds_read_b64_tr_b16 v[108:109], v227\n\t"
      "ds_read_b64_tr_b16 v[110:111], v227 offset:512\n\t"
      "ds_read_b64_tr_b16 v[112:113], v224 offset:32768\n\t"
      "ds_read_b64_tr_b16 v[114:115], v224 offset:33280\n\t"
      "ds_read_b64_tr_b16 v[116:117], v225 offset:32768\n\t"
      "ds_read_b64_tr_b16 v[118:119], v225 offset:33280\n\t"
      "ds_read_b64_tr_b16 v[120:121], v226 offset:32768\n\t"
      "ds_read_b64_tr_b16 v[122:123], v226 offset:33280\n\t"
      "ds_read_b64_tr_b16 v[124:125], v227 offset:32768\n\t"
      "ds_read_b64_tr_b16 v[126:127], v227 offset:33280\n\t"
      "s_waitcnt lgkmcnt(0)\n\t"
      "GST_%=:\n\t"
      "s_add_u32 s66, s64, 65536\n\t"
      "s_addc_u32 s67, s65, 0\n\t"
      "s_add_u32 s68, s66, 65536\n\t"
      "s_addc_u32 s69, s67, 0\n\t"
      "s_add_u32 s70, s68, 65536\n\t"
      "s_addc_u32 s71, s69, 0\n\t"
      "s_add_u32 s72, s70, 65536\n\t"
      "s_addc_u32 s73, s71, 0\n\t"
      "s_add_u32 s74, s72, 65536\n\t"
      "s_addc_u32 s75, s73, 0\n\t"
      "s_add_u32 s76, s74, 65536\n\t"
      "s_addc_u32 s77, s75, 0\n\t"
      "s_mul_i32 s34, s30, 28\n\t"
      "s_add_u32 s34, s34, s29\n\t"
      "s_lshl_b32 s34, s34, 11\n\t"
      "s_add_u32 s34, s34, 10223616\n\t"
      "s_add_u32 s62, s20, s34\n\t"
      "s_addc_u32 s63, s21, 0\n\t"
      "v_lshlrev_b32_e32 v248, 16, v249\n\t"
      "v_and_b32_e32 v249, 0xffff0000, v249\n\t"
      "v_mov_b32_e32 v252, 0xbdd2d3e8\n\t"
      "v_xor_b32_e32 v253, 128, v245\n\t"
      "s_waitcnt vmcnt(0)\n\t"
      "v_max3_u32 v251, v128, v129, v130\n\t"
      "v_max3_u32 v251, v251, v131, v132\n\t"
      "v_max3_u32 v251, v251, v133, v134\n\t"
      "v_max3_u32 v251, v251, v135, v136\n\t"
      "v_max3_u32 v251, v251, v137, v138\n\t"
      "v_max3_u32 v251, v251, v139, v140\n\t"
      "v_max3_u32 v251, v251, v141, v142\n\t"
      "v_max3_u32 v251, v251, v143, v144\n\t"
      "v_max3_u32 v251, v251, v145, v146\n\t"
      "v_max3_u32 v251, v251, v147, v148\n\t"
      "v_max3_u32 v251, v251, v149, v150\n\t"
      "v_max3_u32 v251, v251, v151, v152\n\t"
      "v_max3_u32 v251, v251, v153, v154\n\t"
      "v_max3_u32 v251, v251, v155, v156\n\t"
      "v_max3_u32 v251, v251, v157, v158\n\t"
      "v_max_u32_e32 v251, v251, v159\n\t"
      "v_cmp_eq_u32_e32 vcc, -1, v251\n\t"
      "s_cbranch_vccnz LA30R_%=\n\t"
      "LA30B_%=:\n\t"
      "v_mfma_f32_16x16x32_bf16 v[208:211], v[0:3], v[128:131], 0\n\t"
      "global_load_dwordx4 v[160:163], v250, s[66:67] offset:-4096 sc1\n\t"
      "v_mfma_f32_16x16x32_bf16 v[208:211], v[16:19], v[132:135], v[208:211]\n\t"
      "v_mfma_f32_16x16x32_bf16 v[208:211], v[32:35], v[136:139], v[208:211]\n\t"
      "global_load_dwordx4 v[164:167], v250, s[66:67] offset:-3072 sc1\n\t"
      "v_mfma_f32_16x16x32_bf16 v[208:211], v[48:51], v[140:143], v[208:211]\n\t"
      "v_mfma_f32_16x16x32_bf16 v[208:211], v[64:67], v[144:147], v[208:211]\n\t"
      "global_load_dwordx4 v[168:171], v250, s[66:67] offset:-2048 sc1\n\t"
      "v_mfma_f32_16x16x32_bf16 v[208:211], v[80:83], v[148:151], v[208:211]\n\t"
      "s_barrier\n\t"
      "v_mfma_f32_16x16x32_bf16 v[208:211], v[96:99], v[152:155], v[208:211]\n\t"
      "global_load_dwordx4 v[172:175], v250, s[66:67] offset:-1024 sc1\n\t"
      "v_mfma_f32_16x16x32_bf16 v[208:211], v[112:115], v[156:159], v[208:211]\n\t"
      "v_mfma_f32_16x16x32_bf16 v[212:215], v[4:7], v[128:131], 0\n\t"
      "global_load_dwordx4 v[176:179], v250, s[66:67] sc1\n\t"
      "v_mfma_f32_16x16x32_bf16 v[212:215], v[20:23], v[132:135], v[212:215]\n\t"
      "v_mfma_f32_16x16x32_bf16 v[212:215], v[36:39], v[136:139], v[212:215]\n\t"
      "global_load_dwordx4 v[180:183], v250, s[66:67] offset:1024 sc1\n\t"
      "v_mfma_f32_16x16x32_bf16 v[212:215], v[52:55], v[140:143], v[212:215]\n\t"
      "v_mfma_f32_16x16x32_bf16 v[212:215], v[68:71], v[144:147], v[212:215]\n\t"
      "global_load_dwordx4 v[184:187], v250, s[66:67] offset:2048 sc1\n\t"
      "v_mfma_f32_16x16x32_bf16 v[212:215], v[84:87], v[148:151], v[212:215]\n\t"
      "v_mfma_f32_16x16x32_bf16 v[212:215], v[100:103], v[152:155], v[212:215]\n\t"
      "ds_write_b128 v245, v[208:211]\n\t"
      "global_load_dwordx4 v[188:191], v250, s[66:67] offset:3072 sc1\n\t"
      "v_mfma_f32_16x16x32_bf16 v[212:215], v[116:119], v[156:159], v[212:215]\n\t"
      "v_mfma_f32_16x16x32_bf16 v[216:219], v[8:11], v[128:131], 0\n\t"
      "global_load_dwordx4 v[192:195], v250, s[68:69] offset:-4096 sc1\n\t"
      "v_mfma_f32_16x16x32_bf16 v[216:219], v[24:27], v[132:135], v[216:219]\n\t"
      "v_mfma_f32_16x16x32_bf16 v[216:219], v[40:43], v[136:139], v[216:219]\n\t"
      "global_load_dwordx4 v[196:199], v250, s[68:69] offset:-3072 sc1\n\t"
      "v_mfma_f32_16x16x32_bf16 v[216:219], v[56:59], v[140:143], v[216:219]\n\t"
      "v_mfma_f32_16x16x32_bf16 v[216:219], v[72:75], v[144:147], v[216:219]\n\t"
      "global_load_dwordx4 v[200:203], v250, s[68:69] offset:-2048 sc1\n\t"
      "v_mfma_f32_16x16x32_bf16 v[216:219], v[88:91], v[148:151], v[216:219]\n\t"
      "v_mfma_f32_16x16x32_bf16 v[216:219], v[104:107], v[152:155], v[216:219]\n\t"
      "ds_write_b128 v253, v[212:215] offset:1024\n\t"
      "global_load_dwordx4 v[204:207], v250, s[68:69] offset:-1024 sc1\n\t"
      "v_mfma_f32_16x16x32_bf16 v[216:219], v[120:123], v[156:159], v[216:219]\n\t"
      "v_mfma_f32_16x16x32_bf16 v[220:223], v[12:15], v[128:131], 0\n\t"
      "global_load_dwordx4 v[128:131], v250, s[68:69] sc1\n\t"
      "v_mfma_f32_16x16x32_bf16 v[220:223], v[28:31], v[132:135], v[220:223]\n\t"
      "v_mfma_f32_16x16x32_bf16 v[220:223], v[44:47], v[136:139], v[220:223]\n\t"
      "v_mfma_f32_16x16x32_bf16 v[220:223], v[60:63], v[140:143], v[220:223]\n\t"
      "v_mfma_f32_16x16x32_bf16 v[220:223], v[76:79], v[144:147], v[220:223]\n\t"
      "global_load_dwordx4 v[132:135], v250, s[68:69] offset:1024 sc1\n\t"
      "v_mfma_f32_16x16x32_bf16 v[220:223], v[92:95], v[148:151], v[220:223]\n\t"
      "v_mfma_f32_16x16x32_bf16 v[220:223], v[108:111], v[152:155], v[220:223]\n\t"
      "ds_write_b128 v245, v[216:219] offset:2048\n\t"
      "v_mfma_f32_16x16x32_bf16 v[220:223], v[124:127], v[156:159], v[220:223]\n\t"
      "s_waitcnt vmcnt(10)\n\t"
      "v_mfma_f32_16x16x32_bf16 v[224:227], v[0:3], v[160:163], 0\n\t"
      "global_load_dwordx4 v[136:139], v250, s[68:69] offset:2048 sc1\n\t"
      "v_mfma_f32_16x16x32_bf16 v[224:227], v[16:19], v[164:167], v[224:227]\n\t"
      "s_nop 3\n\t"
      "ds_write_b128 v253, v[220:223] offset:3072\n\t"
      "v_mfma_f32_16x16x32_bf16 v[224:227], v[32:35], v[168:171], v[224:227]\n\t"
      "v_mfma_f32_16x16x32_bf16 v[224:227], v[48:51], v[172:175], v[224:227]\n\t"
      "s_waitcnt vmcnt(7)\n\t"
      "v_mfma_f32_16x16x32_bf16 v[224:227], v[64:67], v[176:179], v[224:227]\n\t"
      "global_load_dwordx4 v[140:143], v250, s[68:69] offset:3072 sc1\n\t"
      "v_mfma_f32_16x16x32_bf16 v[224:227], v[80:83], v[180:183], v[224:227]\n\t"
      "s_waitcnt lgkmcnt(0)\n\t"
      "s_barrier\n\t"
      "ds_read_b64 v[228:229], v246\n\t"
      "ds_read_b64 v[230:231], v246 offset:4096\n\t"
      "v_mfma_f32_16x16x32_bf16 v[224:227], v[96:99], v[184:187], v[224:227]\n\t"
      "ds_read_b64 v[232:233], v246 offset:8192\n\t"
      "ds_read_b64 v[234:235], v246 offset:12288\n\t"
      "v_mfma_f32_16x16x32_bf16 v[224:227], v[112:115], v[188:191], v[224:227]\n\t"
      "ds_read_b64 v[236:237], v246 offset:16384\n\t"
      "ds_read_b64 v[238:239], v246 offset:20480\n\t"
      "v_mfma_f32_16x16x32_bf16 v[208:211], v[4:7], v[160:163], 0\n\t"
      "ds_read_b64 v[240:241], v246 offset:24576\n\t"
      "ds_read_b64 v[242:243], v246 offset:28672\n\t"
      "global_load_dwordx4 v[144:147], v250, s[70:71] offset:-4096 sc1\n\t"
      "v_mfma_f32_16x16x32_bf16 v[208:211], v[20:23], v[164:167], v[208:211]\n\t"
      "v_mfma_f32_16x16x32_bf16 v[208:211], v[36:39], v[168:171], v[208:211]\n\t"
      "s_waitcnt lgkmcnt(4)\n\t"
      "v_add_f32_e32 v228, v228, v230\n\t"
      "v_add_f32_e32 v232, v232, v234\n\t"
      "v_cmp_u_f32_e32 vcc, v224, v224\n\t"
      "s_cbranch_vccnz LA31R_%=\n\t"
      "LA31B_%=:\n\t"
      "v_mfma_f32_16x16x32_bf16 v[208:211], v[52:55], v[172:175], v[208:211]\n\t"
      "v_add_f32_e32 v229, v229, v231\n\t"
      "v_add_f32_e32 v233, v233, v235\n\t"
      "v_mfma_f32_16x16x32_bf16 v[208:211], v[68:71], v[176:179], v[208:211]\n\t"
      "s_waitcnt lgkmcnt(0)\n\t"
      "v_add_f32_e32 v236, v236, v238\n\t"
      "v_add_f32_e32 v240, v240, v242\n\t"
      "global_load_dwordx4 v[148:151], v250, s[70:71] offset:-3072 sc1\n\t"
      "v_mfma_f32_16x16x32_bf16 v[208:211], v[84:87], v[180:183], v[208:211]\n\t"
      "v_add_f32_e32 v237, v237, v239\n\t"
      "v_add_f32_e32 v241, v241, v243\n\t"
      "v_mfma_f32_16x16x32_bf16 v[208:211], v[100:103], v[184:187], v[208:211]\n\t"
      "v_add_f32_e32 v228, v228, v232\n\t"
      "v_add_f32_e32 v236, v236, v240\n\t"
      "v_add_f32_e32 v229, v229, v233\n\t"
      "ds_write_b128 v245, v[224:227] offset:32768\n\t"
      "v_mfma_f32_16x16x32_bf16 v[208:211], v[116:119], v[188:191], v[208:211]\n\t"
      "v_add_f32_e32 v237, v237, v241\n\t"
      "v_add_f32_e32 v228, v228, v248\n\t"
      "v_mfma_f32_16x16x32_bf16 v[212:215], v[8:11], v[160:163], 0\n\t"
      "v_add_f32_e32 v229, v229, v249\n\t"
      "v_add_f32_e32 v228, v228, v236\n\t"
      "v_add_f32_e32 v229, v229, v237\n\t"
      "global_load_dwordx4 v[152:155], v250, s[70:71] offset:-2048 sc1\n\t"
      "v_mfma_f32_16x16x32_bf16 v[212:215], v[24:27], v[164:167], v[212:215]\n\t"
      "v_mul_f32_e32 v230, v228, v228\n\t"
      "v_mul_f32_e32 v231, v229, v229\n\t"
      "v_mfma_f32_16x16x32_bf16 v[212:215], v[40:43], v[168:171], v[212:215]\n\t"
      "v_fmaak_f32 v230, v252, v230, 0xc0135761\n\t"
      "v_fmaak_f32 v231, v252, v231, 0xc0135761\n\t"
      "v_mul_f32_e32 v230, v228, v230\n\t"
      "v_mfma_f32_16x16x32_bf16 v[212:215], v[56:59], v[172:175], v[212:215]\n\t"
      "v_mul_f32_e32 v231, v229, v231\n\t"
      "v_exp_f32_e32 v230, v230\n\t"
      "v_mfma_f32_16x16x32_bf16 v[212:215], v[72:75], v[176:179], v[212:215]\n\t"
      "v_exp_f32_e32 v231, v231\n\t"
      "v_add_f32_e32 v230, 1.0, v230\n\t"
      "v_add_f32_e32 v231, 1.0, v231\n\t"
      "global_load_dwordx4 v[156:159], v250, s[70:71] offset:-1024 sc1\n\t"
      "v_mfma_f32_16x16x32_bf16 v[212:215], v[88:91], v[180:183], v[212:215]\n\t"
      "v_rcp_f32_e32 v230, v230\n\t"
      "v_rcp_f32_e32 v231, v231\n\t"
      "v_mfma_f32_16x16x32_bf16 v[212:215], v[104:107], v[184:187], v[212:215]\n\t"
      "v_mul_f32_e32 v228, v228, v230\n\t"
      "v_mul_f32_e32 v229, v229, v231\n\t"
      "v_cvt_pk_bf16_f32 v232, v228, v229\n\t"
      "ds_write_b128 v253, v[208:211] offset:33792\n\t"
      "v_mfma_f32_16x16x32_bf16 v[212:215], v[120:123], v[188:191], v[212:215]\n\t"
      "v_min_u32_e32 v254, -2, v232\n\t"
      "global_store_dword v247, v254, s[62:63] sc1\n\t"
      "s_add_u32 s62, s62, 57344\n\t"
      "s_addc_u32 s63, s63, 0\n\t"
      "v_mfma_f32_16x16x32_bf16 v[216:219], v[12:15], v[160:163], 0\n\t"
      "global_load_dwordx4 v[160:163], v250, s[70:71] sc1\n\t"
      "v_mfma_f32_16x16x32_bf16 v[216:219], v[28:31], v[164:167], v[216:219]\n\t"
      "v_mfma_f32_16x16x32_bf16 v[216:219], v[44:47], v[168:171], v[216:219]\n\t"
      "v_mfma_f32_16x16x32_bf16 v[216:219], v[60:63], v[172:175], v[216:219]\n\t"
      "v_mfma_f32_16x16x32_bf16 v[216:219], v[76:79], v[176:179], v[216:219]\n\t"
      "global_load_dwordx4 v[164:167], v250, s[70:71] offset:1024 sc1\n\t"
      "v_mfma_f32_16x16x32_bf16 v[216:219], v[92:95], v[180:183], v[216:219]\n\t"
      "v_mfma_f32_16x16x32_bf16 v[216:219], v[108:111], v[184:187], v[216:219]\n\t"
      "ds_write_b128 v245, v[212:215] offset:34816\n\t"
      "v_mfma_f32_16x16x32_bf16 v[216:219], v[124:127], v[188:191], v[216:219]\n\t"
      "s_waitcnt vmcnt(11)\n\t"
      "v_mfma_f32_16x16x32_bf16 v[220:223], v[0:3], v[192:195], 0\n\t"
      "global_load_dwordx4 v[168:171], v250, s[70:71] offset:2048 sc1\n\t"
      "v_mfma_f32_16x16x32_bf16 v[220:223], v[16:19], v[196:199], v[220:223]\n\t"
      "s_nop 3\n\t"
      "ds_write_b128 v253, v[216:219] offset:35840\n\t"
      "v_mfma_f32_16x16x32_bf16 v[220:223], v[32:35], v[200:203], v[220:223]\n\t"
      "v_mfma_f32_16x16x32_bf16 v[220:223], v[48:51], v[204:207], v[220:223]\n\t"
      "s_waitcnt vmcnt(8)\n\t"
      "v_mfma_f32_16x16x32_bf16 v[220:223], v[64:67], v[128:131], v[220:223]\n\t"
      "global_load_dwordx4 v[172:175], v250, s[70:71] offset:3072 sc1\n\t"
      "v_mfma_f32_16x16x32_bf16 v[220:223], v[80:83], v[132:135], v[220:223]\n\t"
      "s_waitcnt lgkmcnt(0)\n\t"
      "s_barrier\n\t"
      "ds_read_b64 v[228:229], v246 offset:32768\n\t"
      "ds_read_b64 v[230:231], v246 offset:36864\n\t"
      "v_mfma_f32_16x16x32_bf16 v[220:223], v[96:99], v[136:139], v[220:223]\n\t"
      "ds_read_b64 v[232:233], v246 offset:40960\n\t"
      "ds_read_b64 v[234:235], v246 offset:45056\n\t"
      "v_mfma_f32_16x16x32_bf16 v[220:223], v[112:115], v[140:143], v[220:223]\n\t"
      "ds_read_b64 v[236:237], v246 offset:49152\n\t"
      "ds_read_b64 v[238:239], v246 offset:53248\n\t"
      "v_mfma_f32_16x16x32_bf16 v[224:227], v[4:7], v[192:195], 0\n\t"
      "ds_read_b64 v[240:241], v246 offset:57344\n\t"
      "ds_read_b64 v[242:243], v246 offset:61440\n\t"
      "global_load_dwordx4 v[176:179], v250, s[72:73] offset:-4096 sc1\n\t"
      "v_mfma_f32_16x16x32_bf16 v[224:227], v[20:23], v[196:199], v[224:227]\n\t"
      "v_mfma_f32_16x16x32_bf16 v[224:227], v[36:39], v[200:203], v[224:227]\n\t"
      "s_waitcnt lgkmcnt(4)\n\t"
      "v_add_f32_e32 v228, v228, v230\n\t"
      "v_add_f32_e32 v232, v232, v234\n\t"
      "v_cmp_u_f32_e32 vcc, v220, v220\n\t"
      "s_cbranch_vccnz LA32R_%=\n\t"
      "LA32B_%=:\n\t"
      "v_mfma_f32_16x16x32_bf16 v[224:227], v[52:55], v[204:207], v[224:227]\n\t"
      "v_add_f32_e32 v229, v229, v231\n\t"
      "v_add_f32_e32 v233, v233, v235\n\t"
      "v_mfma_f32_16x16x32_bf16 v[224:227], v[68:71], v[128:131], v[224:227]\n\t"
      "s_waitcnt lgkmcnt(0)\n\t"
      "v_add_f32_e32 v236, v236, v238\n\t"
      "v_add_f32_e32 v240, v240, v242\n\t"
      "global_load_dwordx4 v[180:183], v250, s[72:73] offset:-3072 sc1\n\t"
      "v_mfma_f32_16x16x32_bf16 v[224:227], v[84:87], v[132:135], v[224:227]\n\t"
      "v_add_f32_e32 v237, v237, v239\n\t"
      "v_add_f32_e32 v241, v241, v243\n\t"
      "v_mfma_f32_16x16x32_bf16 v[224:227], v[100:103], v[136:139], v[224:227]\n\t"
      "v_add_f32_e32 v228, v228, v232\n\t"
      "v_add_f32_e32 v236, v236, v240\n\t"
      "v_add_f32_e32 v229, v229, v233\n\t"
      "ds_write_b128 v245, v[220:223]\n\t"
      "v_mfma_f32_16x16x32_bf16 v[224:227], v[116:119], v[140:143], v[224:227]\n\t"
      "v_add_f32_e32 v237, v237, v241\n\t"
      "v_add_f32_e32 v228, v228, v248\n\t"
      "v_mfma_f32_16x16x32_bf16 v[208:211], v[8:11], v[192:195], 0\n\t"
      "v_add_f32_e32 v229, v229, v249\n\t"
      "v_add_f32_e32 v228, v228, v236\n\t"
      "v_add_f32_e32 v229, v229, v237\n\t"
      "global_load_dwordx4 v[184:187], v250, s[72:73] offset:-2048 sc1\n\t"
      "v_mfma_f32_16x16x32_bf16 v[208:211], v[24:27], v[196:199], v[208:211]\n\t"
      "v_mul_f32_e32 v230, v228, v228\n\t"
      "v_mul_f32_e32 v231, v229, v229\n\t"
      "v_mfma_f32_16x16x32_bf16 v[208:211], v[40:43], v[200:203], v[208:211]\n\t"
      "v_fmaak_f32 v230, v252, v230, 0xc0135761\n\t"
      "v_fmaak_f32 v231, v252, v231, 0xc0135761\n\t"
      "v_mul_f32_e32 v230, v228, v230\n\t"
      "v_mfma_f32_16x16x32_bf16 v[208:211], v[56:59], v[204:207], v[208:211]\n\t"
      "v_mul_f32_e32 v231, v229, v231\n\t"
      "v_exp_f32_e32 v230, v230\n\t"
      "v_mfma_f32_16x16x32_bf16 v[208:211], v[72:75], v[128:131], v[208:211]\n\t"
      "v_exp_f32_e32 v231, v231\n\t"
      "v_add_f32_e32 v230, 1.0, v230\n\t"
      "v_add_f32_e32 v231, 1.0, v231\n\t"
      "global_load_dwordx4 v[188:191], v250, s[72:73] offset:-1024 sc1\n\t"
      "v_mfma_f32_16x16x32_bf16 v[208:211], v[88:91], v[132:135], v[208:211]\n\t"
      "v_rcp_f32_e32 v230, v230\n\t"
      "v_rcp_f32_e32 v231, v231\n\t"
      "v_mfma_f32_16x16x32_bf16 v[208:211], v[104:107], v[136:139], v[208:211]\n\t"
      "v_mul_f32_e32 v228, v228, v230\n\t"
      "v_mul_f32_e32 v229, v229, v231\n\t"
      "v_cvt_pk_bf16_f32 v232, v228, v229\n\t"
      "ds_write_b128 v253, v[224:227] offset:1024\n\t"
      "v_mfma_f32_16x16x32_bf16 v[208:211], v[120:123], v[140:143], v[208:211]\n\t"
      "v_min_u32_e32 v254, -2, v232\n\t"
      "global_store_dword v247, v254, s[62:63] sc1\n\t"
      "s_add_u32 s62, s62, 57344\n\t"
      "s_addc_u32 s63, s63, 0\n\t"
      "v_mfma_f32_16x16x32_bf16 v[212:215], v[12:15], v[192:195], 0\n\t"
      "global_load_dwordx4 v[192:195], v250, s[72:73] sc1\n\t"
      "v_mfma_f32_16x16x32_bf16 v[212:215], v[28:31], v[196:199], v[212:215]\n\t"
      "v_mfma_f32_16x16x32_bf16 v[212:215], v[44:47], v[200:203], v[212:215]\n\t"
      "v_mfma_f32_16x16x32_bf16 v[212:215], v[60:63], v[204:207], v[212:215]\n\t"
      "v_mfma_f32_16x16x32_bf16 v[212:215], v[76:79], v[128:131], v[212:215]\n\t"
      "global_load_dwordx4 v[196:199], v250, s[72:73] offset:1024 sc1\n\t"
      "v_mfma_f32_16x16x32_bf16 v[212:215], v[92:95], v[132:135], v[212:215]\n\t"
      "v_mfma_f32_16x16x32_bf16 v[212:215], v[108:111], v[136:139], v[212:215]\n\t"
      "ds_write_b128 v245, v[208:211] offset:2048\n\t"
      "v_mfma_f32_16x16x32_bf16 v[212:215], v[124:127], v[140:143], v[212:215]\n\t"
      "s_waitcnt vmcnt(12)\n\t"
      "v_mfma_f32_16x16x32_bf16 v[216:219], v[0:3], v[144:147], 0\n\t"
      "global_load_dwordx4 v[200:203], v250, s[72:73] offset:2048 sc1\n\t"
      "v_mfma_f32_16x16x32_bf16 v[216:219], v[16:19], v[148:151], v[216:219]\n\t"
      "s_nop 3\n\t"
      "ds_write_b128 v253, v[212:215] offset:3072\n\t"
      "v_mfma_f32_16x16x32_bf16 v[216:219], v[32:35], v[152:155], v[216:219]\n\t"
      "v_mfma_f32_16x16x32_bf16 v[216:219], v[48:51], v[156:159], v[216:219]\n\t"
      "s_waitcnt vmcnt(8)\n\t"
      "v_mfma_f32_16x16x32_bf16 v[216:219], v[64:67], v[160:163], v[216:219]\n\t"
      "global_load_dwordx4 v[204:207], v250, s[72:73] offset:3072 sc1\n\t"
      "v_mfma_f32_16x16x32_bf16 v[216:219], v[80:83], v[164:167], v[216:219]\n\t"
      "s_waitcnt lgkmcnt(0)\n\t"
      "s_barrier\n\t"
      "ds_read_b64 v[228:229], v246\n\t"
      "ds_read_b64 v[230:231], v246 offset:4096\n\t"
      "v_mfma_f32_16x16x32_bf16 v[216:219], v[96:99], v[168:171], v[216:219]\n\t"
      "ds_read_b64 v[232:233], v246 offset:8192\n\t"
      "ds_read_b64 v[234:235], v246 offset:12288\n\t"
      "v_mfma_f32_16x16x32_bf16 v[216:219], v[112:115], v[172:175], v[216:219]\n\t"
      "ds_read_b64 v[236:237], v246 offset:16384\n\t"
      "ds_read_b64 v[238:239], v246 offset:20480\n\t"
      "v_mfma_f32_16x16x32_bf16 v[220:223], v[4:7], v[144:147], 0\n\t"
      "ds_read_b64 v[240:241], v246 offset:24576\n\t"
      "ds_read_b64 v[242:243], v246 offset:28672\n\t"
      "global_load_dwordx4 v[128:131], v250, s[74:75] offset:-4096 sc1\n\t"
      "v_mfma_f32_16x16x32_bf16 v[220:223], v[20:23], v[148:151], v[220:223]\n\t"
      "v_mfma_f32_16x16x32_bf16 v[220:223], v[36:39], v[152:155], v[220:223]\n\t"
      "s_waitcnt lgkmcnt(4)\n\t"
      "v_add_f32_e32 v228, v228, v230\n\t"
      "v_add_f32_e32 v232, v232, v234\n\t"
      "v_cmp_u_f32_e32 vcc, v216, v216\n\t"
      "s_cbranch_vccnz LA33R_%=\n\t"
      "LA33B_%=:\n\t"
      "v_mfma_f32_16x16x32_bf16 v[220:223], v[52:55], v[156:159], v[220:223]\n\t"
      "v_add_f32_e32 v229, v229, v231\n\t"
      "v_add_f32_e32 v233, v233, v235\n\t"
      "v_mfma_f32_16x16x32_bf16 v[220:223], v[68:71], v[160:163], v[220:223]\n\t"
      "s_waitcnt lgkmcnt(0)\n\t"
      "v_add_f32_e32 v236, v236, v238\n\t"
      "v_add_f32_e32 v240, v240, v242\n\t"
      "global_load_dwordx4 v[132:135], v250, s[74:75] offset:-3072 sc1\n\t"
      "v_mfma_f32_16x16x32_bf16 v[220:223], v[84:87], v[164:167], v[220:223]\n\t"
      "v_add_f32_e32 v237, v237, v239\n\t"
      "v_add_f32_e32 v241, v241, v243\n\t"
      "v_mfma_f32_16x16x32_bf16 v[220:223], v[100:103], v[168:171], v[220:223]\n\t"
      "v_add_f32_e32 v228, v228, v232\n\t"
      "v_add_f32_e32 v236, v236, v240\n\t"
      "v_add_f32_e32 v229, v229, v233\n\t"
      "ds_write_b128 v245, v[216:219] offset:32768\n\t"
      "v_mfma_f32_16x16x32_bf16 v[220:223], v[116:119], v[172:175], v[220:223]\n\t"
      "v_add_f32_e32 v237, v237, v241\n\t"
      "v_add_f32_e32 v228, v228, v248\n\t"
      "v_mfma_f32_16x16x32_bf16 v[224:227], v[8:11], v[144:147], 0\n\t"
      "v_add_f32_e32 v229, v229, v249\n\t"
      "v_add_f32_e32 v228, v228, v236\n\t"
      "v_add_f32_e32 v229, v229, v237\n\t"
      "global_load_dwordx4 v[136:139], v250, s[74:75] offset:-2048 sc1\n\t"
      "v_mfma_f32_16x16x32_bf16 v[224:227], v[24:27], v[148:151], v[224:227]\n\t"
      "v_mul_f32_e32 v230, v228, v228\n\t"
      "v_mul_f32_e32 v231, v229, v229\n\t"
      "v_mfma_f32_16x16x32_bf16 v[224:227], v[40:43], v[152:155], v[224:227]\n\t"
      "v_fmaak_f32 v230, v252, v230, 0xc0135761\n\t"
      "v_fmaak_f32 v231, v252, v231, 0xc0135761\n\t"
      "v_mul_f32_e32 v230, v228, v230\n\t"
      "v_mfma_f32_16x16x32_bf16 v[224:227], v[56:59], v[156:159], v[224:227]\n\t"
      "v_mul_f32_e32 v231, v229, v231\n\t"
      "v_exp_f32_e32 v230, v230\n\t"
      "v_mfma_f32_16x16x32_bf16 v[224:227], v[72:75], v[160:163], v[224:227]\n\t"
      "v_exp_f32_e32 v231, v231\n\t"
      "v_add_f32_e32 v230, 1.0, v230\n\t"
      "v_add_f32_e32 v231, 1.0, v231\n\t"
      "global_load_dwordx4 v[140:143], v250, s[74:75] offset:-1024 sc1\n\t"
      "v_mfma_f32_16x16x32_bf16 v[224:227], v[88:91], v[164:167], v[224:227]\n\t"
      "v_rcp_f32_e32 v230, v230\n\t"
      "v_rcp_f32_e32 v231, v231\n\t"
      "v_mfma_f32_16x16x32_bf16 v[224:227], v[104:107], v[168:171], v[224:227]\n\t"
      "v_mul_f32_e32 v228, v228, v230\n\t"
      "v_mul_f32_e32 v229, v229, v231\n\t"
      "v_cvt_pk_bf16_f32 v232, v228, v229\n\t"
      "ds_write_b128 v253, v[220:223] offset:33792\n\t"
      "v_mfma_f32_16x16x32_bf16 v[224:227], v[120:123], v[172:175], v[224:227]\n\t"
      "v_min_u32_e32 v254, -2, v232\n\t"
      "global_store_dword v247, v254, s[62:63] sc1\n\t"
      "s_add_u32 s62, s62, 57344\n\t"
      "s_addc_u32 s63, s63, 0\n\t"
      "v_mfma_f32_16x16x32_bf16 v[208:211], v[12:15], v[144:147], 0\n\t"
      "global_load_dwordx4 v[144:147], v250, s[74:75] sc1\n\t"
      "v_mfma_f32_16x16x32_bf16 v[208:211], v[28:31], v[148:151], v[208:211]\n\t"
      "v_mfma_f32_16x16x32_bf16 v[208:211], v[44:47], v[152:155], v[208:211]\n\t"
      "v_mfma_f32_16x16x32_bf16 v[208:211], v[60:63], v[156:159], v[208:211]\n\t"
      "v_mfma_f32_16x16x32_bf16 v[208:211], v[76:79], v[160:163], v[208:211]\n\t"
      "global_load_dwordx4 v[148:151], v250, s[74:75] offset:1024 sc1\n\t"
      "v_mfma_f32_16x16x32_bf16 v[208:211], v[92:95], v[164:167], v[208:211]\n\t"
      "v_mfma_f32_16x16x32_bf16 v[208:211], v[108:111], v[168:171], v[208:211]\n\t"
      "ds_write_b128 v245, v[224:227] offset:34816\n\t"
      "v_mfma_f32_16x16x32_bf16 v[208:211], v[124:127], v[172:175], v[208:211]\n\t"
      "s_waitcnt vmcnt(12)\n\t"
      "v_mfma_f32_16x16x32_bf16 v[212:215], v[0:3], v[176:179], 0\n\t"
      "global_load_dwordx4 v[152:155], v250, s[74:75] offset:2048 sc1\n\t"
      "v_mfma_f32_16x16x32_bf16 v[212:215], v[16:19], v[180:183], v[212:215]\n\t"
      "s_nop 3\n\t"
      "ds_write_b128 v253, v[208:211] offset:35840\n\t"
      "v_mfma_f32_16x16x32_bf16 v[212:215], v[32:35], v[184:187], v[212:215]\n\t"
      "v_mfma_f32_16x16x32_bf16 v[212:215], v[48:51], v[188:191], v[212:215]\n\t"
      "s_waitcnt vmcnt(8)\n\t"
      "v_mfma_f32_16x16x32_bf16 v[212:215], v[64:67], v[192:195], v[212:215]\n\t"
      "global_load_dwordx4 v[156:159], v250, s[74:75] offset:3072 sc1\n\t"
      "v_mfma_f32_16x16x32_bf16 v[212:215], v[80:83], v[196:199], v[212:215]\n\t"
      "s_waitcnt lgkmcnt(0)\n\t"
      "s_barrier\n\t"
      "ds_read_b64 v[228:229], v246 offset:32768\n\t"
      "ds_read_b64 v[230:231], v246 offset:36864\n\t"
      "v_mfma_f32_16x16x32_bf16 v[212:215], v[96:99], v[200:203], v[212:215]\n\t"
      "ds_read_b64 v[232:233], v246 offset:40960\n\t"
      "ds_read_b64 v[234:235], v246 offset:45056\n\t"
      "v_mfma_f32_16x16x32_bf16 v[212:215], v[112:115], v[204:207], v[212:215]\n\t"
      "ds_read_b64 v[236:237], v246 offset:49152\n\t"
      "ds_read_b64 v[238:239], v246 offset:53248\n\t"
      "v_mfma_f32_16x16x32_bf16 v[216:219], v[4:7], v[176:179], 0\n\t"
      "ds_read_b64 v[240:241], v246 offset:57344\n\t"
      "ds_read_b64 v[242:243], v246 offset:61440\n\t"
      "global_load_dwordx4 v[160:163], v250, s[76:77] offset:-4096 sc1\n\t"
      "v_mfma_f32_16x16x32_bf16 v[216:219], v[20:23], v[180:183], v[216:219]\n\t"
      "v_mfma_f32_16x16x32_bf16 v[216:219], v[36:39], v[184:187], v[216:219]\n\t"
      "s_waitcnt lgkmcnt(4)\n\t"
      "v_add_f32_e32 v228, v228, v230\n\t"
      "v_add_f32_e32 v232, v232, v234\n\t"
      "v_cmp_u_f32_e32 vcc, v212, v212\n\t"
      "s_cbranch_vccnz LA34R_%=\n\t"
      "LA34B_%=:\n\t"
      "v_mfma_f32_16x16x32_bf16 v[216:219], v[52:55], v[188:191], v[216:219]\n\t"
      "v_add_f32_e32 v229, v229, v231\n\t"
      "v_add_f32_e32 v233, v233, v235\n\t"
      "v_mfma_f32_16x16x32_bf16 v[216:219], v[68:71], v[192:195], v[216:219]\n\t"
      "s_waitcnt lgkmcnt(0)\n\t"
      "v_add_f32_e32 v236, v236, v238\n\t"
      "v_add_f32_e32 v240, v240, v242\n\t"
      "global_load_dwordx4 v[164:167], v250, s[76:77] offset:-3072 sc1\n\t"
      "v_mfma_f32_16x16x32_bf16 v[216:219], v[84:87], v[196:199], v[216:219]\n\t"
      "v_add_f32_e32 v237, v237, v239\n\t"
      "v_add_f32_e32 v241, v241, v243\n\t"
      "v_mfma_f32_16x16x32_bf16 v[216:219], v[100:103], v[200:203], v[216:219]\n\t"
      "v_add_f32_e32 v228, v228, v232\n\t"
      "v_add_f32_e32 v236, v236, v240\n\t"
      "v_add_f32_e32 v229, v229, v233\n\t"
      "ds_write_b128 v245, v[212:215]\n\t"
      "v_mfma_f32_16x16x32_bf16 v[216:219], v[116:119], v[204:207], v[216:219]\n\t"
      "v_add_f32_e32 v237, v237, v241\n\t"
      "v_add_f32_e32 v228, v228, v248\n\t"
      "v_mfma_f32_16x16x32_bf16 v[220:223], v[8:11], v[176:179], 0\n\t"
      "v_add_f32_e32 v229, v229, v249\n\t"
      "v_add_f32_e32 v228, v228, v236\n\t"
      "v_add_f32_e32 v229, v229, v237\n\t"
      "global_load_dwordx4 v[168:171], v250, s[76:77] offset:-2048 sc1\n\t"
      "v_mfma_f32_16x16x32_bf16 v[220:223], v[24:27], v[180:183], v[220:223]\n\t"
      "v_mul_f32_e32 v230, v228, v228\n\t"
      "v_mul_f32_e32 v231, v229, v229\n\t"
      "v_mfma_f32_16x16x32_bf16 v[220:223], v[40:43], v[184:187], v[220:223]\n\t"
      "v_fmaak_f32 v230, v252, v230, 0xc0135761\n\t"
      "v_fmaak_f32 v231, v252, v231, 0xc0135761\n\t"
      "v_mul_f32_e32 v230, v228, v230\n\t"
      "v_mfma_f32_16x16x32_bf16 v[220:223], v[56:59], v[188:191], v[220:223]\n\t"
      "v_mul_f32_e32 v231, v229, v231\n\t"
      "v_exp_f32_e32 v230, v230\n\t"
      "v_mfma_f32_16x16x32_bf16 v[220:223], v[72:75], v[192:195], v[220:223]\n\t"
      "v_exp_f32_e32 v231, v231\n\t"
      "v_add_f32_e32 v230, 1.0, v230\n\t"
      "v_add_f32_e32 v231, 1.0, v231\n\t"
      "global_load_dwordx4 v[172:175], v250, s[76:77] offset:-1024 sc1\n\t"
      "v_mfma_f32_16x16x32_bf16 v[220:223], v[88:91], v[196:199], v[220:223]\n\t"
      "v_rcp_f32_e32 v230, v230\n\t"
      "v_rcp_f32_e32 v231, v231\n\t"
      "v_mfma_f32_16x16x32_bf16 v[220:223], v[104:107], v[200:203], v[220:223]\n\t"
      "v_mul_f32_e32 v228, v228, v230\n\t"
      "v_mul_f32_e32 v229, v229, v231\n\t"
      "v_cvt_pk_bf16_f32 v232, v228, v229\n\t"
      "ds_write_b128 v253, v[216:219] offset:1024\n\t"
      "v_mfma_f32_16x16x32_bf16 v[220:223], v[120:123], v[204:207], v[220:223]\n\t"
      "v_min_u32_e32 v254, -2, v232\n\t"
      "global_store_dword v247, v254, s[62:63] sc1\n\t"
      "s_add_u32 s62, s62, 57344\n\t"
      "s_addc_u32 s63, s63, 0\n\t"
      "v_mfma_f32_16x16x32_bf16 v[224:227], v[12:15], v[176:179], 0\n\t"
      "global_load_dwordx4 v[176:179], v250, s[76:77] sc1\n\t"
      "v_mfma_f32_16x16x32_bf16 v[224:227], v[28:31], v[180:183], v[224:227]\n\t"
      "v_mfma_f32_16x16x32_bf16 v[224:227], v[44:47], v[184:187], v[224:227]\n\t"
      "v_mfma_f32_16x16x32_bf16 v[224:227], v[60:63], v[188:191], v[224:227]\n\t"
      "v_mfma_f32_16x16x32_bf16 v[224:227], v[76:79], v[192:195], v[224:227]\n\t"
      "global_load_dwordx4 v[180:183], v250, s[76:77] offset:1024 sc1\n\t"
      "v_mfma_f32_16x16x32_bf16 v[224:227], v[92:95], v[196:199], v[224:227]\n\t"
      "v_mfma_f32_16x16x32_bf16 v[224:227], v[108:111], v[200:203], v[224:227]\n\t"
      "ds_write_b128 v245, v[220:223] offset:2048\n\t"
      "v_mfma_f32_16x16x32_bf16 v[224:227], v[124:127], v[204:207], v[224:227]\n\t"
      "s_waitcnt vmcnt(12)\n\t"
      "v_mfma_f32_16x16x32_bf16 v[208:211], v[0:3], v[128:131], 0\n\t"
      "global_load_dwordx4 v[184:187], v250, s[76:77] offset:2048 sc1\n\t"
      "v_mfma_f32_16x16x32_bf16 v[208:211], v[16:19], v[132:135], v[208:211]\n\t"
      "s_nop 3\n\t"
      "ds_write_b128 v253, v[224:227] offset:3072\n\t"
      "v_mfma_f32_16x16x32_bf16 v[208:211], v[32:35], v[136:139], v[208:211]\n\t"
      "v_mfma_f32_16x16x32_bf16 v[208:211], v[48:51], v[140:143], v[208:211]\n\t"
      "s_waitcnt vmcnt(8)\n\t"
      "v_mfma_f32_16x16x32_bf16 v[208:211], v[64:67], v[144:147], v[208:211]\n\t"
      "global_load_dwordx4 v[188:191], v250, s[76:77] offset:3072 sc1\n\t"
      "v_mfma_f32_16x16x32_bf16 v[208:211], v[80:83], v[148:151], v[208:211]\n\t"
      "s_waitcnt lgkmcnt(0)\n\t"
      "s_barrier\n\t"
      "ds_read_b64 v[228:229], v246\n\t"
      "ds_read_b64 v[230:231], v246 offset:4096\n\t"
      "v_mfma_f32_16x16x32_bf16 v[208:211], v[96:99], v[152:155], v[208:211]\n\t"
      "ds_read_b64 v[232:233], v246 offset:8192\n\t"
      "ds_read_b64 v[234:235], v246 offset:12288\n\t"
      "v_mfma_f32_16x16x32_bf16 v[208:211], v[112:115], v[156:159], v[208:211]\n\t"
      "ds_read_b64 v[236:237], v246 offset:16384\n\t"
      "ds_read_b64 v[238:239], v246 offset:20480\n\t"
      "v_mfma_f32_16x16x32_bf16 v[212:215], v[4:7], v[128:131], 0\n\t"
      "ds_read_b64 v[240:241], v246 offset:24576\n\t"
      "ds_read_b64 v[242:243], v246 offset:28672\n\t"
      "v_mfma_f32_16x16x32_bf16 v[212:215], v[20:23], v[132:135], v[212:215]\n\t"
      "v_mfma_f32_16x16x32_bf16 v[212:215], v[36:39], v[136:139], v[212:215]\n\t"
      "s_waitcnt lgkmcnt(4)\n\t"
      "v_add_f32_e32 v228, v228, v230\n\t"
      "v_add_f32_e32 v232, v232, v234\n\t"
      "v_cmp_u_f32_e32 vcc, v208, v208\n\t"
      "s_cbranch_vccnz LA35R_%=\n\t"
      "LA35B_%=:\n\t"
      "v_mfma_f32_16x16x32_bf16 v[212:215], v[52:55], v[140:143], v[212:215]\n\t"
      "v_add_f32_e32 v229, v229, v231\n\t"
      "v_add_f32_e32 v233, v233, v235\n\t"
      "v_mfma_f32_16x16x32_bf16 v[212:215], v[68:71], v[144:147], v[212:215]\n\t"
      "s_waitcnt lgkmcnt(0)\n\t"
      "v_add_f32_e32 v236, v236, v238\n\t"
      "v_add_f32_e32 v240, v240, v242\n\t"
      "v_mfma_f32_16x16x32_bf16 v[212:215], v[84:87], v[148:151], v[212:215]\n\t"
      "v_add_f32_e32 v237, v237, v239\n\t"
      "v_add_f32_e32 v241, v241, v243\n\t"
      "v_mfma_f32_16x16x32_bf16 v[212:215], v[100:103], v[152:155], v[212:215]\n\t"
      "v_add_f32_e32 v228, v228, v232\n\t"
      "v_add_f32_e32 v236, v236, v240\n\t"
      "v_add_f32_e32 v229, v229, v233\n\t"
      "ds_write_b128 v245, v[208:211] offset:32768\n\t"
      "v_mfma_f32_16x16x32_bf16 v[212:215], v[116:119], v[156:159], v[212:215]\n\t"
      "v_add_f32_e32 v237, v237, v241\n\t"
      "v_add_f32_e32 v228, v228, v248\n\t"
      "v_mfma_f32_16x16x32_bf16 v[216:219], v[8:11], v[128:131], 0\n\t"
      "v_add_f32_e32 v229, v229, v249\n\t"
      "v_add_f32_e32 v228, v228, v236\n\t"
      "v_add_f32_e32 v229, v229, v237\n\t"
      "v_mfma_f32_16x16x32_bf16 v[216:219], v[24:27], v[132:135], v[216:219]\n\t"
      "v_mul_f32_e32 v230, v228, v228\n\t"
      "v_mul_f32_e32 v231, v229, v229\n\t"
      "v_mfma_f32_16x16x32_bf16 v[216:219], v[40:43], v[136:139], v[216:219]\n\t"
      "v_fmaak_f32 v230, v252, v230, 0xc0135761\n\t"
      "v_fmaak_f32 v231, v252, v231, 0xc0135761\n\t"
      "v_mul_f32_e32 v230, v228, v230\n\t"
      "v_mfma_f32_16x16x32_bf16 v[216:219], v[56:59], v[140:143], v[216:219]\n\t"
      "v_mul_f32_e32 v231, v229, v231\n\t"
      "v_exp_f32_e32 v230, v230\n\t"
      "v_mfma_f32_16x16x32_bf16 v[216:219], v[72:75], v[144:147], v[216:219]\n\t"
      "v_exp_f32_e32 v231, v231\n\t"
      "v_add_f32_e32 v230, 1.0, v230\n\t"
      "v_add_f32_e32 v231, 1.0, v231\n\t"
      "v_mfma_f32_16x16x32_bf16 v[216:219], v[88:91], v[148:151], v[216:219]\n\t"
      "v_rcp_f32_e32 v230, v230\n\t"
      "v_rcp_f32_e32 v231, v231\n\t"
      "v_mfma_f32_16x16x32_bf16 v[216:219], v[104:107], v[152:155], v[216:219]\n\t"
      "v_mul_f32_e32 v228, v228, v230\n\t"
      "v_mul_f32_e32 v229, v229, v231\n\t"
      "v_cvt_pk_bf16_f32 v232, v228, v229\n\t"
      "ds_write_b128 v253, v[212:215] offset:33792\n\t"
      "v_mfma_f32_16x16x32_bf16 v[216:219], v[120:123], v[156:159], v[216:219]\n\t"
      "v_min_u32_e32 v254, -2, v232\n\t"
      "global_store_dword v247, v254, s[62:63] sc1\n\t"
      "s_add_u32 s62, s62, 57344\n\t"
      "s_addc_u32 s63, s63, 0\n\t"
      "v_mfma_f32_16x16x32_bf16 v[220:223], v[12:15], v[128:131], 0\n\t"
      "v_mfma_f32_16x16x32_bf16 v[220:223], v[28:31], v[132:135], v[220:223]\n\t"
      "v_mfma_f32_16x16x32_bf16 v[220:223], v[44:47], v[136:139], v[220:223]\n\t"
      "v_mfma_f32_16x16x32_bf16 v[220:223], v[60:63], v[140:143], v[220:223]\n\t"
      "v_mfma_f32_16x16x32_bf16 v[220:223], v[76:79], v[144:147], v[220:223]\n\t"
      "v_mfma_f32_16x16x32_bf16 v[220:223], v[92:95], v[148:151], v[220:223]\n\t"
      "v_mfma_f32_16x16x32_bf16 v[220:223], v[108:111], v[152:155], v[220:223]\n\t"
      "ds_write_b128 v245, v[216:219] offset:34816\n\t"
      "v_mfma_f32_16x16x32_bf16 v[220:223], v[124:127], v[156:159], v[220:223]\n\t"
      "s_waitcnt vmcnt(6)\n\t"
      "v_mfma_f32_16x16x32_bf16 v[224:227], v[0:3], v[160:163], 0\n\t"
      "v_mfma_f32_16x16x32_bf16 v[224:227], v[16:19], v[164:167], v[224:227]\n\t"
      "s_nop 4\n\t"
      "ds_write_b128 v253, v[220:223] offset:35840\n\t"
      "v_mfma_f32_16x16x32_bf16 v[224:227], v[32:35], v[168:171], v[224:227]\n\t"
      "v_mfma_f32_16x16x32_bf16 v[224:227], v[48:51], v[172:175], v[224:227]\n\t"
      "s_waitcnt vmcnt(1)\n\t"
      "v_mfma_f32_16x16x32_bf16 v[224:227], v[64:67], v[176:179], v[224:227]\n\t"
      "v_mfma_f32_16x16x32_bf16 v[224:227], v[80:83], v[180:183], v[224:227]\n\t"
      "s_waitcnt lgkmcnt(0)\n\t"
      "s_barrier\n\t"
      "ds_read_b64 v[228:229], v246 offset:32768\n\t"
      "ds_read_b64 v[230:231], v246 offset:36864\n\t"
      "v_mfma_f32_16x16x32_bf16 v[224:227], v[96:99], v[184:187], v[224:227]\n\t"
      "ds_read_b64 v[232:233], v246 offset:40960\n\t"
      "ds_read_b64 v[234:235], v246 offset:45056\n\t"
      "v_mfma_f32_16x16x32_bf16 v[224:227], v[112:115], v[188:191], v[224:227]\n\t"
      "ds_read_b64 v[236:237], v246 offset:49152\n\t"
      "ds_read_b64 v[238:239], v246 offset:53248\n\t"
      "v_mfma_f32_16x16x32_bf16 v[208:211], v[4:7], v[160:163], 0\n\t"
      "ds_read_b64 v[240:241], v246 offset:57344\n\t"
      "ds_read_b64 v[242:243], v246 offset:61440\n\t"
      "v_mfma_f32_16x16x32_bf16 v[208:211], v[20:23], v[164:167], v[208:211]\n\t"
      "v_mfma_f32_16x16x32_bf16 v[208:211], v[36:39], v[168:171], v[208:211]\n\t"
      "s_waitcnt lgkmcnt(4)\n\t"
      "v_add_f32_e32 v228, v228, v230\n\t"
      "v_add_f32_e32 v232, v232, v234\n\t"
      "v_cmp_u_f32_e32 vcc, v224, v224\n\t"
      "s_cbranch_vccnz LA36R_%=\n\t"
      "LA36B_%=:\n\t"
      "v_mfma_f32_16x16x32_bf16 v[208:211], v[52:55], v[172:175], v[208:211]\n\t"
      "v_add_f32_e32 v229, v229, v231\n\t"
      "v_add_f32_e32 v233, v233, v235\n\t"
      "v_mfma_f32_16x16x32_bf16 v[208:211], v[68:71], v[176:179], v[208:211]\n\t"
      "s_waitcnt lgkmcnt(0)\n\t"
      "v_add_f32_e32 v236, v236, v238\n\t"
      "v_add_f32_e32 v240, v240, v242\n\t"
      "v_mfma_f32_16x16x32_bf16 v[208:211], v[84:87], v[180:183], v[208:211]\n\t"
      "v_add_f32_e32 v237, v237, v239\n\t"
      "v_add_f32_e32 v241, v241, v243\n\t"
      "v_mfma_f32_16x16x32_bf16 v[208:211], v[100:103], v[184:187], v[208:211]\n\t"
      "v_add_f32_e32 v228, v228, v232\n\t"
      "v_add_f32_e32 v236, v236, v240\n\t"
      "v_add_f32_e32 v229, v229, v233\n\t"
      "ds_write_b128 v245, v[224:227]\n\t"
      "v_mfma_f32_16x16x32_bf16 v[208:211], v[116:119], v[188:191], v[208:211]\n\t"
      "v_add_f32_e32 v237, v237, v241\n\t"
      "v_add_f32_e32 v228, v228, v248\n\t"
      "v_mfma_f32_16x16x32_bf16 v[212:215], v[8:11], v[160:163], 0\n\t"
      "v_add_f32_e32 v229, v229, v249\n\t"
      "v_add_f32_e32 v228, v228, v236\n\t"
      "v_add_f32_e32 v229, v229, v237\n\t"
      "v_mfma_f32_16x16x32_bf16 v[212:215], v[24:27], v[164:167], v[212:215]\n\t"
      "v_mul_f32_e32 v230, v228, v228\n\t"
      "v_mul_f32_e32 v231, v229, v229\n\t"
      "v_mfma_f32_16x16x32_bf16 v[212:215], v[40:43], v[168:171], v[212:215]\n\t"
      "v_fmaak_f32 v230, v252, v230, 0xc0135761\n\t"
      "v_fmaak_f32 v231, v252, v231, 0xc0135761\n\t"
      "v_mul_f32_e32 v230, v228, v230\n\t"
      "v_mfma_f32_16x16x32_bf16 v[212:215], v[56:59], v[172:175], v[212:215]\n\t"
      "v_mul_f32_e32 v231, v229, v231\n\t"
      "v_exp_f32_e32 v230, v230\n\t"
      "v_mfma_f32_16x16x32_bf16 v[212:215], v[72:75], v[176:179], v[212:215]\n\t"
      "v_exp_f32_e32 v231, v231\n\t"
      "v_add_f32_e32 v230, 1.0, v230\n\t"
      "v_add_f32_e32 v231, 1.0, v231\n\t"
      "v_mfma_f32_16x16x32_bf16 v[212:215], v[88:91], v[180:183], v[212:215]\n\t"
      "v_rcp_f32_e32 v230, v230\n\t"
      "v_rcp_f32_e32 v231, v231\n\t"
      "v_mfma_f32_16x16x32_bf16 v[212:215], v[104:107], v[184:187], v[212:215]\n\t"
      "v_mul_f32_e32 v228, v228, v230\n\t"
      "v_mul_f32_e32 v229, v229, v231\n\t"
      "v_cvt_pk_bf16_f32 v232, v228, v229\n\t"
      "ds_write_b128 v253, v[208:211] offset:1024\n\t"
      "v_mfma_f32_16x16x32_bf16 v[212:215], v[120:123], v[188:191], v[212:215]\n\t"
      "v_min_u32_e32 v254, -2, v232\n\t"
      "global_store_dword v247, v254, s[62:63] sc1\n\t"
      "s_add_u32 s62, s62, 57344\n\t"
      "s_addc_u32 s63, s63, 0\n\t"
      "v_mfma_f32_16x16x32_bf16 v[216:219], v[12:15], v[160:163], 0\n\t"
      "v_mfma_f32_16x16x32_bf16 v[216:219], v[28:31], v[164:167], v[216:219]\n\t"
      "v_mfma_f32_16x16x32_bf16 v[216:219], v[44:47], v[168:171], v[216:219]\n\t"
      "v_mfma_f32_16x16x32_bf16 v[216:219], v[60:63], v[172:175], v[216:219]\n\t"
      "v_mfma_f32_16x16x32_bf16 v[216:219], v[76:79], v[176:179], v[216:219]\n\t"
      "v_mfma_f32_16x16x32_bf16 v[216:219], v[92:95], v[180:183], v[216:219]\n\t"
      "v_mfma_f32_16x16x32_bf16 v[216:219], v[108:111], v[184:187], v[216:219]\n\t"
      "ds_write_b128 v245, v[212:215] offset:2048\n\t"
      "v_mfma_f32_16x16x32_bf16 v[216:219], v[124:127], v[188:191], v[216:219]\n\t"
      "s_nop 7\n\t"
      "ds_write_b128 v253, v[216:219] offset:3072\n\t"
      "s_waitcnt lgkmcnt(0)\n\t"
      "s_barrier\n\t"
      "ds_read_b64 v[228:229], v246\n\t"
      "ds_read_b64 v[230:231], v246 offset:4096\n\t"
      "ds_read_b64 v[232:233], v246 offset:8192\n\t"
      "ds_read_b64 v[234:235], v246 offset:12288\n\t"
      "ds_read_b64 v[236:237], v246 offset:16384\n\t"
      "ds_read_b64 v[238:239], v246 offset:20480\n\t"
      "ds_read_b64 v[240:241], v246 offset:24576\n\t"
      "ds_read_b64 v[242:243], v246 offset:28672\n\t"
      "s_waitcnt lgkmcnt(4)\n\t"
      "v_add_f32_e32 v228, v228, v230\n\t"
      "v_add_f32_e32 v232, v232, v234\n\t"
      "v_add_f32_e32 v229, v229, v231\n\t"
      "v_add_f32_e32 v233, v233, v235\n\t"
      "s_waitcnt lgkmcnt(0)\n\t"
      "v_add_f32_e32 v236, v236, v238\n\t"
      "v_add_f32_e32 v240, v240, v242\n\t"
      "v_add_f32_e32 v237, v237, v239\n\t"
      "v_add_f32_e32 v241, v241, v243\n\t"
      "v_add_f32_e32 v228, v228, v232\n\t"
      "v_add_f32_e32 v236, v236, v240\n\t"
      "v_add_f32_e32 v229, v229, v233\n\t"
      "v_add_f32_e32 v237, v237, v241\n\t"
      "v_add_f32_e32 v228, v228, v248\n\t"
      "v_add_f32_e32 v229, v229, v249\n\t"
      "v_add_f32_e32 v228, v228, v236\n\t"
      "v_add_f32_e32 v229, v229, v237\n\t"
      "v_mul_f32_e32 v230, v228, v228\n\t"
      "v_mul_f32_e32 v231, v229, v229\n\t"
      "v_fmaak_f32 v230, v252, v230, 0xc0135761\n\t"
      "v_fmaak_f32 v231, v252, v231, 0xc0135761\n\t"
      "v_mul_f32_e32 v230, v228, v230\n\t"
      "v_mul_f32_e32 v231, v229, v231\n\t"
      "v_exp_f32_e32 v230, v230\n\t"
      "v_exp_f32_e32 v231, v231\n\t"
      "v_add_f32_e32 v230, 1.0, v230\n\t"
      "v_add_f32_e32 v231, 1.0, v231\n\t"
      "v_rcp_f32_e32 v230, v230\n\t"
      "v_rcp_f32_e32 v231, v231\n\t"
      "v_mul_f32_e32 v228, v228, v230\n\t"
      "v_mul_f32_e32 v229, v229, v231\n\t"
      "v_cvt_pk_bf16_f32 v232, v228, v229\n\t"
      "v_min_u32_e32 v254, -2, v232\n\t"
      "global_store_dword v247, v254, s[62:63] sc1\n\t"
      "s_add_u32 s62, s62, 57344\n\t"
      "s_addc_u32 s63, s63, 0\n\t"
      "s_or_b32 s34, s2, s24\n\t"
      "s_cbranch_scc1 EPB_%=\n\t"
      "v_mov_b32_e32 v228, s22\n\t"
      "v_mov_b32_e32 v229, s23\n\t"
      "v_mov_b32_e32 v230, 0\n\t"
      "s_mov_b64 exec, 1\n\t"
      "global_store_dwordx2 v230, v[228:229], s[60:61] sc1\n\t"
      "s_mov_b64 exec, -1\n\t"
      "EPB_%=:\n\t"
      "s_endpgm\n\t"
      "LPATH_%=:\n\t"
      "s_add_u32 s22, s22, 1\n\t"
      "s_addc_u32 s23, s23, 0\n\t"
      "s_add_u32 s72, s26, 1\n\t"
      "s_and_b32 s72, s72, 6\n\t"
      "s_cselect_b32 s72, 0, 2\n\t"
      "s_or_b32 s27, s27, s72\n\t"
      "s_mul_i32 s72, s81, 172032\n\t"
      "s_add_u32 s72, s72, s37\n\t"
      "s_add_u32 s40, s6, s72\n\t"
      "s_addc_u32 s41, s7, 0\n\t"
      "s_add_u32 s81, s81, 1\n\t"
      "s_cmp_eq_u32 s81, 7\n\t"
      "s_cselect_b32 s81, 0, s81\n\t"
      "s_add_u32 m0, s39, 81920\n\t"
      "s_nop 0\n\t"
      "global_load_lds_dwordx4 v148, s[40:41]\n\t"
      "global_load_lds_dwordx4 v149, s[40:41] offset:1024\n\t"
      "global_load_lds_dwordx4 v150, s[40:41] offset:2048\n\t"
      "global_load_lds_dwordx4 v151, s[40:41] offset:3072\n\t"
      "s_mul_i32 s72, s81, 172032\n\t"
      "s_add_u32 s72, s72, s37\n\t"
      "s_add_u32 s40, s6, s72\n\t"
      "s_addc_u32 s41, s7, 0\n\t"
      "s_add_u32 s81, s81, 1\n\t"
      "s_cmp_eq_u32 s81, 7\n\t"
      "s_cselect_b32 s81, 0, s81\n\t"
      "s_add_u32 m0, s39, 98304\n\t"
      "s_nop 0\n\t"
      "global_load_lds_dwordx4 v148, s[40:41]\n\t"
      "global_load_lds_dwordx4 v149, s[40:41] offset:1024\n\t"
      "s_waitcnt vmcnt(14)\n\t"
      "s_barrier\n\t"
      "v_add_u32_e32 v172, 0x10000, v224\n\t"
      "v_add_u32_e32 v173, 0x10000, v225\n\t"
      "v_add_u32_e32 v174, 0x10000, v226\n\t"
      "v_add_u32_e32 v175, 0x10000, v227\n\t"
      "s_lshl_b32 s34, s25, 8\n\t"
      "s_add_u32 s34, s34, 4096\n\t"
      "v_and_b32_e32 v136, 1, v192\n\t"
      "v_lshl_add_u32 v136, v136, 7, s34\n\t"
      "s_lshl_b32 s34, s25, 7\n\t"
      "s_add_u32 s34, s34, 16384\n\t"
      "v_mov_b32_e32 v137, s34\n\t"
      "s_lshl_b32 s34, s2, 7\n\t"
      "s_add_u32 s34, s34, 32768\n\t"
      "v_mov_b32_e32 v143, s34\n\t"
      "s_bitcmp0_b32 s27, 1\n\t"
      "s_cbranch_scc1 SK6_%=\n\t"
      "s_lshr_b32 s73, s26, 2\n\t"
      "s_lshl1_add_u32 s73, s25, s73\n\t"
      "s_lshl_b32 s72, s73, 7\n\t"
      "s_add_u32 s72, s72, 4096\n\t"
      "v_mov_b32_e32 v130, s72\n\t"
      "s_lshl2_add_u32 s75, s73, s24\n\t"
      "v_mov_b32_e32 v128, s22\n\t"
      "v_mov_b32_e32 v129, s23\n\t"
      "v_lshrrev_b32_e32 v140, 1, v244\n\t"
      "s_lshl_b32 s72, s75, 9\n\t"
      "s_add_u32 s72, s72, 12582912\n\t"
      "v_mov_b32_e32 v132, -1\n\t"
      "v_mov_b32_e32 v133, -1\n\t"
      "v_mov_b32_e32 v134, -1\n\t"
      "v_mov_b32_e32 v135, -1\n\t"
      "s_add_u32 s42, s20, s72\n\t"
      "s_addc_u32 s43, s21, 0\n\t"
      "global_store_dwordx2 v140, v[132:133], s[42:43] sc1\n\t"
      "s_lshl_b32 s72, s75, 13\n\t"
      "s_add_u32 s72, s72, 4096\n\t"
      "s_add_u32 s42, s20, s72\n\t"
      "s_addc_u32 s43, s21, 0\n\t"
      "global_store_dwordx4 v244, v[132:135], s[42:43] offset:-4096 sc1\n\t"
      "global_store_dwordx4 v244, v[132:135], s[42:43] offset:-3072 sc1\n\t"
      "global_store_dwordx4 v244, v[132:135], s[42:43] offset:-2048 sc1\n\t"
      "SK6_%=:\n\t"
      "s_waitcnt vmcnt(10)\n\t"
      "s_barrier\n\t"
      "global_load_lds_dwordx4 v150, s[40:41] offset:2048\n\t"
      "global_load_lds_dwordx4 v151, s[40:41] offset:3072\n\t"
      "s_bitcmp0_b32 s27, 1\n\t"
      "s_cbranch_scc1 SK7_%=\n\t"
      "global_store_dwordx4 v244, v[132:135], s[42:43] offset:-1024 sc1\n\t"
      "global_store_dwordx4 v244, v[132:135], s[42:43] sc1\n\t"
      "global_store_dwordx4 v244, v[132:135], s[42:43] offset:1024 sc1\n\t"
      "SK7_%=:\n\t"
      "s_waitcnt vmcnt(8)\n\t"
      "s_barrier\n\t"
      "s_mul_i32 s72, s81, 172032\n\t"
      "s_add_u32 s72, s72, s37\n\t"
      "s_add_u32 s40, s6, s72\n\t"
      "s_addc_u32 s41, s7, 0\n\t"
      "s_add_u32 s81, s81, 1\n\t"
      "s_cmp_eq_u32 s81, 7\n\t"
      "s_cselect_b32 s81, 0, s81\n\t"
      "s_add_u32 m0, s39, 114688\n\t"
      "s_nop 0\n\t"
      "global_load_lds_dwordx4 v148, s[40:41]\n\t"
      "global_load_lds_dwordx4 v149, s[40:41] offset:1024\n\t"
      "global_load_lds_dwordx4 v150, s[40:41] offset:2048\n\t"
      "global_load_lds_dwordx4 v151, s[40:41] offset:3072\n\t"
      "s_bitcmp0_b32 s27, 1\n\t"
      "s_cbranch_scc1 SK8_%=\n\t"
      "global_store_dwordx4 v244, v[132:135], s[42:43] offset:2048 sc1\n\t"
      "global_store_dwordx4 v244, v[132:135], s[42:43] offset:3072 sc1\n\t"
      "SK8_%=:\n\t"
      "s_waitcnt vmcnt(8)\n\t"
      "s_barrier\n\t"
      "s_mul_i32 s72, s81, 172032\n\t"
      "s_add_u32 s72, s72, s37\n\t"
      "s_add_u32 s40, s6, s72\n\t"
      "s_addc_u32 s41, s7, 0\n\t"
      "s_add_u32 s81, s81, 1\n\t"
      "s_cmp_eq_u32 s81, 7\n\t"
      "s_cselect_b32 s81, 0, s81\n\t"
      "s_add_u32 m0, s39, 131072\n\t"
      "s_nop 0\n\t"
      "global_load_lds_dwordx4 v148, s[40:41]\n\t"
      "global_load_lds_dwordx4 v149, s[40:41] offset:1024\n\t"
      "global_load_lds_dwordx4 v150, s[40:41] offset:2048\n\t"
      "global_load_lds_dwordx4 v151, s[40:41] offset:3072\n\t"
      "s_waitcnt vmcnt(8)\n\t"
      "s_bitcmp0_b32 s27, 1\n\t"
      "s_cbranch_scc1 SK9_%=\n\t"
      "s_waitcnt vmcnt(4)\n\t"
      "SK9_%=:\n\t"
      "s_barrier\n\t"
      "s_mul_i32 s72, s81, 172032\n\t"
      "s_add_u32 s72, s72, s37\n\t"
      "s_add_u32 s40, s6, s72\n\t"
      "s_addc_u32 s41, s7, 0\n\t"
      "s_add_u32 s81, s81, 1\n\t"
      "s_cmp_eq_u32 s81, 7\n\t"
      "s_cselect_b32 s81, 0, s81\n\t"
      "s_add_u32 m0, s39, 147456\n\t"
      "s_nop 0\n\t"
      "global_load_lds_dwordx4 v148, s[40:41]\n\t"
      "global_load_lds_dwordx4 v149, s[40:41] offset:1024\n\t"
      "global_load_lds_dwordx4 v150, s[40:41] offset:2048\n\t"
      "global_load_lds_dwordx4 v151, s[40:41] offset:3072\n\t"
      "s_bitcmp0_b32 s27, 1\n\t"
      "s_cbranch_scc1 SK10_%=\n\t"
      "s_cmp_eq_u32 s24, 0\n\t"
      "s_cselect_b64 exec, 1, 0\n\t"
      "global_store_dwordx2 v130, v[128:129], s[60:61] sc1\n\t"
      "s_mov_b64 exec, -1\n\t"
      "SK10_%=:\n\t"
      "s_waitcnt vmcnt(8)\n\t"
      "s_barrier\n\t"
      "s_mul_i32 s72, s82, 172032\n\t"
      "s_add_u32 s72, s72, s37\n\t"
      "s_add_u32 s72, s72, 1792\n\t"
      "s_add_u32 s40, s6, s72\n\t"
      "s_addc_u32 s41, s7, 0\n\t"
      "s_add_u32 s82, s82, 1\n\t"
      "s_cmp_eq_u32 s82, 7\n\t"
      "s_cselect_b32 s82, 0, s82\n\t"
      "s_add_u32 m0, s39, 65536\n\t"
      "s_nop 0\n\t"
      "global_load_lds_dwordx4 v148, s[40:41]\n\t"
      "global_load_lds_dwordx4 v152, s[40:41] offset:1024\n\t"
      "s_mul_i32 s72, s82, 172032\n\t"
      "s_add_u32 s72, s72, s37\n\t"
      "s_add_u32 s72, s72, 1792\n\t"
      "s_add_u32 s40, s6, s72\n\t"
      "s_addc_u32 s41, s7, 0\n\t"
      "s_add_u32 s82, s82, 1\n\t"
      "s_cmp_eq_u32 s82, 7\n\t"
      "s_cselect_b32 s82, 0, s82\n\t"
      "s_add_u32 m0, s39, 67584\n\t"
      "s_nop 0\n\t"
      "global_load_lds_dwordx4 v148, s[40:41]\n\t"
      "global_load_lds_dwordx4 v152, s[40:41] offset:1024\n\t"
      "s_waitcnt vmcnt(8)\n\t"
      "s_barrier\n\t"
      "s_mul_i32 s72, s82, 172032\n\t"
      "s_add_u32 s72, s72, s37\n\t"
      "s_add_u32 s72, s72, 1792\n\t"
      "s_add_u32 s40, s6, s72\n\t"
      "s_addc_u32 s41, s7, 0\n\t"
      "s_add_u32 s82, s82, 1\n\t"
      "s_cmp_eq_u32 s82, 7\n\t"
      "s_cselect_b32 s82, 0, s82\n\t"
      "s_add_u32 m0, s39, 81920\n\t"
      "s_nop 0\n\t"
      "global_load_lds_dwordx4 v148, s[40:41]\n\t"
      "global_load_lds_dwordx4 v152, s[40:41] offset:1024\n\t"
      "s_mul_i32 s72, s82, 172032\n\t"
      "s_add_u32 s72, s72, s37\n\t"
      "s_add_u32 s72, s72, 1792\n\t"
      "s_add_u32 s40, s6, s72\n\t"
      "s_addc_u32 s41, s7, 0\n\t"
      "s_add_u32 s82, s82, 1\n\t"
      "s_cmp_eq_u32 s82, 7\n\t"
      "s_cselect_b32 s82, 0, s82\n\t"
      "s_add_u32 m0, s39, 83968\n\t"
      "s_nop 0\n\t"
      "global_load_lds_dwordx4 v148, s[40:41]\n\t"
      "global_load_lds_dwordx4 v152, s[40:41] offset:1024\n\t"
      "s_waitcnt vmcnt(8)\n\t"
      "s_barrier\n\t"
      "s_cmp_lg_u32 s24, 3\n\t"
      "s_cbranch_scc1 SK11_%=\n\t"
      "global_load_dwordx2 v[138:139], v136, s[60:61] sc1\n\t"
      "SK11_%=:\n\t"
      "s_mul_i32 s72, s82, 172032\n\t"
      "s_add_u32 s72, s72, s37\n\t"
      "s_add_u32 s72, s72, 1792\n\t"
      "s_add_u32 s40, s6, s72\n\t"
      "s_addc_u32 s41, s7, 0\n\t"
      "s_add_u32 s82, s82, 1\n\t"
      "s_cmp_eq_u32 s82, 7\n\t"
      "s_cselect_b32 s82, 0, s82\n\t"
      "s_lshr_b32 s72, s39, 1\n\t"
      "s_add_u32 m0, s72, 98304\n\t"
      "s_nop 0\n\t"
      "global_load_lds_dwordx4 v148, s[40:41]\n\t"
      "global_load_lds_dwordx4 v152, s[40:41] offset:1024\n\t"
      "global_load_dword v249, v248, s[12:13]\n\t"
      "s_lshl2_add_u32 s72, s31, s24\n\t"
      "s_and_b32 s73, s72, 0x70\n\t"
      "s_add_u32 s72, s72, s73\n\t"
      "s_mov_b32 m0, 64512\n\t"
      "s_mul_i32 s72, s72, 28672\n\t"
      "s_and_b32 s73, s2, 7\n\t"
      "s_mul_i32 s73, s73, 448\n\t"
      "s_and_b32 s73, s73, 0xffffff80\n\t"
      "s_add_u32 s72, s72, s73\n\t"
      "v_bfe_u32 v141, v192, 2, 3\n\t"
      "s_cmp_ge_u32 s31, 28\n\t"
      "s_cselect_b32 s73, 32, 0\n\t"
      "v_and_b32_e32 v142, s73, v192\n\t"
      "v_lshl_or_b32 v141, v142, 2, v141\n\t"
      "v_mul_u32_u24_e32 v141, 3584, v141\n\t"
      "v_lshl_add_u32 v141, v196, 7, v141\n\t"
      "v_add_u32_e32 v141, s72, v141\n\t"
      "global_load_lds_dword v141, s[10:11]\n\t"
      "s_waitcnt vmcnt(8)\n\t"
      "s_barrier\n\t"
      "s_mul_i32 s72, s24, 14336\n\t"
      "s_add_u32 s40, s56, s72\n\t"
      "s_addc_u32 s41, s57, 0\n\t"
      "s_lshr_b32 s72, s39, 1\n\t"
      "s_add_u32 m0, s72, 106496\n\t"
      "s_nop 0\n\t"
      "global_load_lds_dwordx4 v154, s[40:41]\n\t"
      "global_load_lds_dwordx4 v155, s[40:41] offset:1024\n\t"
      "s_waitcnt vmcnt(6)\n\t"
      "s_barrier\n\t"
      "s_mul_i32 s72, s24, 28672\n\t"
      "s_add_u32 s72, s72, 57344\n\t"
      "s_add_u32 s40, s56, s72\n\t"
      "s_addc_u32 s41, s57, 0\n\t"
      "s_lshr_b32 s72, s39, 0\n\t"
      "s_add_u32 m0, s72, 114688\n\t"
      "s_nop 0\n\t"
      "global_load_lds_dwordx4 v154, s[40:41]\n\t"
      "global_load_lds_dwordx4 v155, s[40:41] offset:1024\n\t"
      "global_load_lds_dwordx4 v156, s[40:41] offset:2048\n\t"
      "global_load_lds_dwordx4 v157, s[40:41] offset:3072\n\t"
      "s_waitcnt vmcnt(8)\n\t"
      "s_cmp_lg_u32 s24, 3\n\t"
      "s_cbranch_scc1 LF1B_%=\n\t"
      "v_cmp_ne_u64_e32 vcc, s[22:23], v[138:139]\n\t"
      "s_cbranch_vccnz LF1R_%=\n\t"
      "LF1B_%=:\n\t"
      "s_barrier\n\t"
      "s_mul_i32 s72, s24, 28672\n\t"
      "s_add_u32 s72, s72, 172032\n\t"
      "s_add_u32 s40, s56, s72\n\t"
      "s_addc_u32 s41, s57, 0\n\t"
      "s_lshr_b32 s72, s39, 0\n\t"
      "s_add_u32 m0, s72, 131072\n\t"
      "s_nop 0\n\t"
      "global_load_lds_dwordx4 v154, s[40:41]\n\t"
      "global_load_lds_dwordx4 v155, s[40:41] offset:1024\n\t"
      "global_load_lds_dwordx4 v156, s[40:41] offset:2048\n\t"
      "global_load_lds_dwordx4 v157, s[40:41] offset:3072\n\t"
      "s_mul_i32 s72, s24, 28672\n\t"
      "s_add_u32 s72, s72, 286720\n\t"
      "s_add_u32 s40, s56, s72\n\t"
      "s_addc_u32 s41, s57, 0\n\t"
      "s_lshr_b32 s72, s39, 0\n\t"
      "s_add_u32 m0, s72, 147456\n\t"
      "s_nop 0\n\t"
      "global_load_lds_dwordx4 v154, s[40:41]\n\t"
      "global_load_lds_dwordx4 v155, s[40:41] offset:1024\n\t"
      "global_load_lds_dwordx4 v156, s[40:41] offset:2048\n\t"
      "global_load_lds_dwordx4 v157, s[40:41] offset:3072\n\t"
      "s_barrier\n\t"
      "s_cmp_eq_u32 s26, 7\n\t"
      "s_cbranch_scc1 SPARE_%=\n\t"
      "s_bitcmp0_b32 s27, 0\n\t"
      "s_cbranch_scc1 L4_%=\n\t"
      "s_add_u32 s72, s76, 0\n\t"
      "s_add_u32 s40, s52, s72\n\t"
      "s_addc_u32 s41, s53, 0\n\t"
      "s_add_u32 m0, s39, 65536\n\t"
      "s_nop 0\n\t"
      "global_load_lds_dwordx4 v144, s[40:41]\n\t"
      "global_load_lds_dwordx4 v145, s[40:41] offset:1024\n\t"
      "global_load_lds_dwordx4 v146, s[40:41] offset:2048\n\t"
      "global_load_lds_dwordx4 v147, s[40:41] offset:3072\n\t"
      "s_add_u32 s72, s76, 229376\n\t"
      "s_add_u32 s40, s52, s72\n\t"
      "s_addc_u32 s41, s53, 0\n\t"
      "s_add_u32 m0, s39, 81920\n\t"
      "s_nop 0\n\t"
      "global_load_lds_dwordx4 v144, s[40:41]\n\t"
      "global_load_lds_dwordx4 v145, s[40:41] offset:1024\n\t"
      "global_load_lds_dwordx4 v146, s[40:41] offset:2048\n\t"
      "global_load_lds_dwordx4 v147, s[40:41] offset:3072\n\t"
      "s_waitcnt vmcnt(8)\n\t"
      "s_barrier\n\t"
      "s_add_u32 s72, s76, 3670016\n\t"
      "s_add_u32 s40, s52, s72\n\t"
      "s_addc_u32 s41, s53, 0\n\t"
      "s_add_u32 m0, s39, 16384\n\t"
      "s_nop 0\n\t"
      "global_load_lds_dwordx4 v144, s[40:41]\n\t"
      "global_load_lds_dwordx4 v145, s[40:41] offset:1024\n\t"
      "global_load_lds_dwordx4 v146, s[40:41] offset:2048\n\t"
      "global_load_lds_dwordx4 v147, s[40:41] offset:3072\n\t"
      "s_barrier\n\t"
      "s_add_u32 s72, s76, 3899392\n\t"
      "s_add_u32 s40, s52, s72\n\t"
      "s_addc_u32 s41, s53, 0\n\t"
      "s_add_u32 m0, s39, 49152\n\t"
      "s_nop 0\n\t"
      "global_load_lds_dwordx4 v144, s[40:41]\n\t"
      "global_load_lds_dwordx4 v145, s[40:41] offset:1024\n\t"
      "global_load_lds_dwordx4 v146, s[40:41] offset:2048\n\t"
      "global_load_lds_dwordx4 v147, s[40:41] offset:3072\n\t"
      "s_waitcnt vmcnt(12)\n\t"
      "ds_read_b64_tr_b16 v[0:1], v172\n\t"
      "ds_read_b64_tr_b16 v[2:3], v172 offset:512\n\t"
      "ds_read_b64_tr_b16 v[4:5], v173\n\t"
      "ds_read_b64_tr_b16 v[6:7], v173 offset:512\n\t"
      "ds_read_b64_tr_b16 v[8:9], v174\n\t"
      "ds_read_b64_tr_b16 v[10:11], v174 offset:512\n\t"
      "ds_read_b64_tr_b16 v[12:13], v175\n\t"
      "ds_read_b64_tr_b16 v[14:15], v175 offset:512\n\t"
      "s_waitcnt lgkmcnt(0)\n\t"
      "s_add_u32 s72, s77, 0\n\t"
      "s_add_u32 s40, s52, s72\n\t"
      "s_addc_u32 s41, s53, 0\n\t"
      "s_add_u32 m0, s39, 65536\n\t"
      "s_nop 0\n\t"
      "global_load_lds_dwordx4 v144, s[40:41]\n\t"
      "global_load_lds_dwordx4 v145, s[40:41] offset:1024\n\t"
      "global_load_lds_dwordx4 v146, s[40:41] offset:2048\n\t"
      "global_load_lds_dwordx4 v147, s[40:41] offset:3072\n\t"
      "s_waitcnt vmcnt(12)\n\t"
      "ds_read_b64_tr_b16 v[16:17], v172 offset:16384\n\t"
      "ds_read_b64_tr_b16 v[18:19], v172 offset:16896\n\t"
      "ds_read_b64_tr_b16 v[20:21], v173 offset:16384\n\t"
      "ds_read_b64_tr_b16 v[22:23], v173 offset:16896\n\t"
      "ds_read_b64_tr_b16 v[24:25], v174 offset:16384\n\t"
      "ds_read_b64_tr_b16 v[26:27], v174 offset:16896\n\t"
      "ds_read_b64_tr_b16 v[28:29], v175 offset:16384\n\t"
      "ds_read_b64_tr_b16 v[30:31], v175 offset:16896\n\t"
      "s_waitcnt lgkmcnt(0)\n\t"
      "s_add_u32 s72, s77, 229376\n\t"
      "s_add_u32 s40, s52, s72\n\t"
      "s_addc_u32 s41, s53, 0\n\t"
      "s_add_u32 m0, s39, 81920\n\t"
      "s_nop 0\n\t"
      "global_load_lds_dwordx4 v144, s[40:41]\n\t"
      "global_load_lds_dwordx4 v145, s[40:41] offset:1024\n\t"
      "global_load_lds_dwordx4 v146, s[40:41] offset:2048\n\t"
      "global_load_lds_dwordx4 v147, s[40:41] offset:3072\n\t"
      "s_add_u32 s72, s77, 3670016\n\t"
      "s_add_u32 s40, s52, s72\n\t"
      "s_addc_u32 s41, s53, 0\n\t"
      "s_add_u32 m0, s39, 0\n\t"
      "s_nop 0\n\t"
      "global_load_lds_dwordx4 v144, s[40:41]\n\t"
      "global_load_lds_dwordx4 v145, s[40:41] offset:1024\n\t"
      "global_load_lds_dwordx4 v146, s[40:41] offset:2048\n\t"
      "global_load_lds_dwordx4 v147, s[40:41] offset:3072\n\t"
      "s_add_u32 s72, s77, 3899392\n\t"
      "s_add_u32 s40, s52, s72\n\t"
      "s_addc_u32 s41, s53, 0\n\t"
      "s_add_u32 m0, s39, 32768\n\t"
      "s_nop 0\n\t"
      "global_load_lds_dwordx4 v144, s[40:41]\n\t"
      "global_load_lds_dwordx4 v145, s[40:41] offset:1024\n\t"
      "global_load_lds_dwordx4 v146, s[40:41] offset:2048\n\t"
      "global_load_lds_dwordx4 v147, s[40:41] offset:3072\n\t"
      "s_waitcnt vmcnt(12)\n\t"
      "ds_read_b64_tr_b16 v[32:33], v172\n\t"
      "ds_read_b64_tr_b16 v[34:35], v172 offset:512\n\t"
      "ds_read_b64_tr_b16 v[36:37], v173\n\t"
      "ds_read_b64_tr_b16 v[38:39], v173 offset:512\n\t"
      "ds_read_b64_tr_b16 v[40:41], v174\n\t"
      "ds_read_b64_tr_b16 v[42:43], v174 offset:512\n\t"
      "ds_read_b64_tr_b16 v[44:45], v175\n\t"
      "ds_read_b64_tr_b16 v[46:47], v175 offset:512\n\t"
      "s_waitcnt lgkmcnt(0)\n\t"
      "s_add_u32 s72, s78, 0\n\t"
      "s_add_u32 s40, s52, s72\n\t"
      "s_addc_u32 s41, s53, 0\n\t"
      "s_add_u32 m0, s39, 65536\n\t"
      "s_nop 0\n\t"
      "global_load_lds_dwordx4 v144, s[40:41]\n\t"
      "global_load_lds_dwordx4 v145, s[40:41] offset:1024\n\t"
      "global_load_lds_dwordx4 v146, s[40:41] offset:2048\n\t"
      "global_load_lds_dwordx4 v147, s[40:41] offset:3072\n\t"
      "s_waitcnt vmcnt(12)\n\t"
      "ds_read_b64_tr_b16 v[48:49], v172 offset:16384\n\t"
      "ds_read_b64_tr_b16 v[50:51], v172 offset:16896\n\t"
      "ds_read_b64_tr_b16 v[52:53], v173 offset:16384\n\t"
      "ds_read_b64_tr_b16 v[54:55], v173 offset:16896\n\t"
      "ds_read_b64_tr_b16 v[56:57], v174 offset:16384\n\t"
      "ds_read_b64_tr_b16 v[58:59], v174 offset:16896\n\t"
      "ds_read_b64_tr_b16 v[60:61], v175 offset:16384\n\t"
      "ds_read_b64_tr_b16 v[62:63], v175 offset:16896\n\t"
      "s_waitcnt lgkmcnt(0)\n\t"
      "s_add_u32 s72, s78, 229376\n\t"
      "s_add_u32 s40, s52, s72\n\t"
      "s_addc_u32 s41, s53, 0\n\t"
      "s_add_u32 m0, s39, 81920\n\t"
      "s_nop 0\n\t"
      "global_load_lds_dwordx4 v144, s[40:41]\n\t"
      "global_load_lds_dwordx4 v145, s[40:41] offset:1024\n\t"
      "global_load_lds_dwordx4 v146, s[40:41] offset:2048\n\t"
      "global_load_lds_dwordx4 v147, s[40:41] offset:3072\n\t"
      "s_waitcnt vmcnt(8)\n\t"
      "s_barrier\n\t"
      "s_cmp_lg_u32 s24, 3\n\t"
      "s_cbranch_scc1 SK13_%=\n\t"
      "global_load_dwordx2 v[138:139], v137, s[60:61] sc1\n\t"
      "SK13_%=:\n\t"
      "s_cmp_lg_u32 s24, 3\n\t"
      "s_cbranch_scc1 LF2B_%=\n\t"
      "s_waitcnt vmcnt(0)\n\t"
      "v_cmp_ne_u64_e32 vcc, s[22:23], v[138:139]\n\t"
      "s_cbranch_vccnz LF2R_%=\n\t"
      "LF2B_%=:\n\t"
      "s_barrier\n\t"
      "s_add_u32 s72, s78, 3670016\n\t"
      "s_add_u32 s40, s52, s72\n\t"
      "s_addc_u32 s41, s53, 0\n\t"
      "s_add_u32 m0, s39, 16384\n\t"
      "s_nop 0\n\t"
      "global_load_lds_dwordx4 v144, s[40:41]\n\t"
      "global_load_lds_dwordx4 v145, s[40:41] offset:1024\n\t"
      "global_load_lds_dwordx4 v146, s[40:41] offset:2048\n\t"
      "global_load_lds_dwordx4 v147, s[40:41] offset:3072\n\t"
      "s_add_u32 s72, s78, 3899392\n\t"
      "s_add_u32 s40, s52, s72\n\t"
      "s_addc_u32 s41, s53, 0\n\t"
      "s_add_u32 m0, s39, 49152\n\t"
      "s_nop 0\n\t"
      "global_load_lds_dwordx4 v144, s[40:41]\n\t"
      "global_load_lds_dwordx4 v145, s[40:41] offset:1024\n\t"
      "global_load_lds_dwordx4 v146, s[40:41] offset:2048\n\t"
      "global_load_lds_dwordx4 v147, s[40:41] offset:3072\n\t"
      "s_add_u32 s72, s79, 3670016\n\t"
      "s_add_u32 s40, s52, s72\n\t"
      "s_addc_u32 s41, s53, 0\n\t"
      "s_add_u32 m0, s39, 0\n\t"
      "s_nop 0\n\t"
      "global_load_lds_dwordx4 v144, s[40:41]\n\t"
      "global_load_lds_dwordx4 v145, s[40:41] offset:1024\n\t"
      "global_load_lds_dwordx4 v146, s[40:41] offset:2048\n\t"
      "global_load_lds_dwordx4 v147, s[40:41] offset:3072\n\t"
      "s_add_u32 s72, s79, 3899392\n\t"
      "s_add_u32 s40, s52, s72\n\t"
      "s_addc_u32 s41, s53, 0\n\t"
      "s_add_u32 m0, s39, 32768\n\t"
      "s_nop 0\n\t"
      "global_load_lds_dwordx4 v144, s[40:41]\n\t"
      "global_load_lds_dwordx4 v145, s[40:41] offset:1024\n\t"
      "global_load_lds_dwordx4 v146, s[40:41] offset:2048\n\t"
      "global_load_lds_dwordx4 v147, s[40:41] offset:3072\n\t"
      "s_waitcnt vmcnt(20)\n\t"
      "ds_read_b64_tr_b16 v[64:65], v172\n\t"
      "ds_read_b64_tr_b16 v[66:67], v172 offset:512\n\t"
      "ds_read_b64_tr_b16 v[68:69], v173\n\t"
      "ds_read_b64_tr_b16 v[70:71], v173 offset:512\n\t"
      "ds_read_b64_tr_b16 v[72:73], v174\n\t"
      "ds_read_b64_tr_b16 v[74:75], v174 offset:512\n\t"
      "ds_read_b64_tr_b16 v[76:77], v175\n\t"
      "ds_read_b64_tr_b16 v[78:79], v175 offset:512\n\t"
      "s_waitcnt vmcnt(16)\n\t"
      "ds_read_b64_tr_b16 v[80:81], v172 offset:16384\n\t"
      "ds_read_b64_tr_b16 v[82:83], v172 offset:16896\n\t"
      "ds_read_b64_tr_b16 v[84:85], v173 offset:16384\n\t"
      "ds_read_b64_tr_b16 v[86:87], v173 offset:16896\n\t"
      "ds_read_b64_tr_b16 v[88:89], v174 offset:16384\n\t"
      "ds_read_b64_tr_b16 v[90:91], v174 offset:16896\n\t"
      "ds_read_b64_tr_b16 v[92:93], v175 offset:16384\n\t"
      "ds_read_b64_tr_b16 v[94:95], v175 offset:16896\n\t"
      "s_cmp_lg_u32 s24, 3\n\t"
      "s_cbranch_scc1 SK14_%=\n\t"
      "global_load_dwordx2 v[138:139], v143, s[60:61] sc1\n\t"
      "SK14_%=:\n\t"
      "s_waitcnt vmcnt(0)\n\t"
      "s_cmp_lg_u32 s24, 3\n\t"
      "s_cbranch_scc1 LF3B_%=\n\t"
      "s_waitcnt vmcnt(0)\n\t"
      "v_cmp_ne_u64_e32 vcc, s[22:23], v[138:139]\n\t"
      "s_cbranch_vccnz LF3R_%=\n\t"
      "LF3B_%=:\n\t"
      "s_barrier\n\t"
      "s_waitcnt lgkmcnt(0)\n\t"
      "s_add_u32 s72, s79, 0\n\t"
      "s_add_u32 s40, s52, s72\n\t"
      "s_addc_u32 s41, s53, 0\n\t"
      "s_add_u32 m0, s39, 65536\n\t"
      "s_nop 0\n\t"
      "global_load_lds_dwordx4 v144, s[40:41]\n\t"
      "global_load_lds_dwordx4 v145, s[40:41] offset:1024\n\t"
      "global_load_lds_dwordx4 v146, s[40:41] offset:2048\n\t"
      "global_load_lds_dwordx4 v147, s[40:41] offset:3072\n\t"
      "s_add_u32 s72, s79, 229376\n\t"
      "s_add_u32 s40, s52, s72\n\t"
      "s_addc_u32 s41, s53, 0\n\t"
      "s_add_u32 m0, s39, 81920\n\t"
      "s_nop 0\n\t"
      "global_load_lds_dwordx4 v144, s[40:41]\n\t"
      "global_load_lds_dwordx4 v145, s[40:41] offset:1024\n\t"
      "global_load_lds_dwordx4 v146, s[40:41] offset:2048\n\t"
      "global_load_lds_dwordx4 v147, s[40:41] offset:3072\n\t"
      "s_mul_i32 s34, s30, 65536\n\t"
      "s_mul_i32 s35, s24, 8192\n\t"
      "s_add_u32 s34, s34, s35\n\t"
      "s_add_u32 s64, s48, s34\n\t"
      "s_addc_u32 s65, s49, 0\n\t"
      "v_add_u32_e32 v250, 4096, v244\n\t"
      "global_load_dwordx4 v[128:131], v250, s[64:65] offset:-4096 sc1\n\t"
      "global_load_dwordx4 v[132:135], v250, s[64:65] offset:-3072 sc1\n\t"
      "global_load_dwordx4 v[136:139], v250, s[64:65] offset:-2048 sc1\n\t"
      "global_load_dwordx4 v[140:143], v250, s[64:65] offset:-1024 sc1\n\t"
      "global_load_dwordx4 v[144:147], v250, s[64:65] sc1\n\t"
      "global_load_dwordx4 v[148:151], v250, s[64:65] offset:1024 sc1\n\t"
      "global_load_dwordx4 v[152:155], v250, s[64:65] offset:2048 sc1\n\t"
      "global_load_dwordx4 v[156:159], v250, s[64:65] offset:3072 sc1\n\t"
      "s_waitcnt vmcnt(12)\n\t"
      "ds_read_b64_tr_b16 v[96:97], v172\n\t"
      "ds_read_b64_tr_b16 v[98:99], v172 offset:512\n\t"
      "ds_read_b64_tr_b16 v[100:101], v173\n\t"
      "ds_read_b64_tr_b16 v[102:103], v173 offset:512\n\t"
      "ds_read_b64_tr_b16 v[104:105], v174\n\t"
      "ds_read_b64_tr_b16 v[106:107], v174 offset:512\n\t"
      "ds_read_b64_tr_b16 v[108:109], v175\n\t"
      "ds_read_b64_tr_b16 v[110:111], v175 offset:512\n\t"
      "s_waitcnt vmcnt(8)\n\t"
      "ds_read_b64_tr_b16 v[112:113], v172 offset:16384\n\t"
      "ds_read_b64_tr_b16 v[114:115], v172 offset:16896\n\t"
      "ds_read_b64_tr_b16 v[116:117], v173 offset:16384\n\t"
      "ds_read_b64_tr_b16 v[118:119], v173 offset:16896\n\t"
      "ds_read_b64_tr_b16 v[120:121], v174 offset:16384\n\t"
      "ds_read_b64_tr_b16 v[122:123], v174 offset:16896\n\t"
      "ds_read_b64_tr_b16 v[124:125], v175 offset:16384\n\t"
      "ds_read_b64_tr_b16 v[126:127], v175 offset:16896\n\t"
      "s_waitcnt lgkmcnt(0)\n\t"
      "s_branch GST_%=\n\t"
      "G4_%=:\n\t"
      "s_mul_i32 s34, s30, 57344\n\t"
      "s_mul_i32 s35, s24, 7168\n\t"
      "s_add_u32 s34, s34, s35\n\t"
      "s_add_u32 s34, s34, 1835008\n\t"
      "s_add_u32 s64, s48, s34\n\t"
      "s_addc_u32 s65, s49, 0\n\t"
      "s_and_b32 s34, s30, 3\n\t"
      "s_lshl_b32 s34, s34, 11\n\t"
      "s_add_u32 s40, s34, 0\n\t"
      "s_sub_u32 s35, s40, 7168\n\t"
      "s_min_u32 s40, s40, s35\n\t"
      "s_add_u32 s41, s34, 1024\n\t"
      "s_sub_u32 s35, s41, 7168\n\t"
      "s_min_u32 s41, s41, s35\n\t"
      "s_add_u32 s42, s34, 2048\n\t"
      "s_sub_u32 s35, s42, 7168\n\t"
      "s_min_u32 s42, s42, s35\n\t"
      "s_add_u32 s43, s34, 3072\n\t"
      "s_sub_u32 s35, s43, 7168\n\t"
      "s_min_u32 s43, s43, s35\n\t"
      "s_add_u32 s44, s34, 4096\n\t"
      "s_sub_u32 s35, s44, 7168\n\t"
      "s_min_u32 s44, s44, s35\n\t"
      "s_add_u32 s45, s34, 5120\n\t"
      "s_sub_u32 s35, s45, 7168\n\t"
      "s_min_u32 s45, s45, s35\n\t"
      "s_add_u32 s46, s34, 6144\n\t"
      "s_sub_u32 s35, s46, 7168\n\t"
      "s_min_u32 s46, s46, s35\n\t"
      "s_add_u32 s66, s64, 57344\n\t"
      "s_addc_u32 s67, s65, 0\n\t"
      "s_add_u32 s68, s66, 57344\n\t"
      "s_addc_u32 s69, s67, 0\n\t"
      "s_add_u32 s70, s68, 57344\n\t"
      "s_addc_u32 s71, s69, 0\n\t"
      "s_add_u32 s72, s70, 57344\n\t"
      "s_addc_u32 s73, s71, 0\n\t"
      "s_add_u32 s74, s72, 57344\n\t"
      "s_addc_u32 s75, s73, 0\n\t"
      "s_add_u32 s76, s74, 57344\n\t"
      "s_addc_u32 s77, s75, 0\n\t"
      "v_lshrrev_b32_e32 v240, 7, v244\n\t"
      "v_and_b32_e32 v241, 0x70, v244\n\t"
      "v_and_b32_e32 v242, 6, v240\n\t"
      "v_lshlrev_b32_e32 v242, 4, v242\n\t"
      "v_xor_b32_e32 v242, v242, v241\n\t"
      "v_lshl_or_b32 v228, v240, 12, v242\n\t"
      "s_mul_i32 s34, s24, 917504\n\t"
      "s_lshl_b32 s35, s29, 7\n\t"
      "s_add_u32 s34, s34, s35\n\t"
      "s_lshl_b32 s39, s24, 12\n\t"
      "s_add_u32 s54, s14, s34\n\t"
      "s_addc_u32 s55, s15, 0\n\t"
      "v_add_u32_e32 v229, 31744, v228\n\t"
      "v_add_u32_e32 v230, 63488, v228\n\t"
      "v_add_u32_e32 v231, 95232, v228\n\t"
      "s_lshl_b32 s37, s40, 7\n\t"
      "s_add_u32 s58, s54, s37\n\t"
      "s_addc_u32 s59, s55, 0\n\t"
      "s_add_u32 m0, s39, 0\n\t"
      "s_nop 0\n\t"
      "global_load_lds_dwordx4 v228, s[58:59]\n\t"
      "global_load_lds_dwordx4 v229, s[58:59] offset:1024\n\t"
      "global_load_lds_dwordx4 v230, s[58:59] offset:2048\n\t"
      "global_load_lds_dwordx4 v231, s[58:59] offset:3072\n\t"
      "s_lshl_b32 s37, s41, 7\n\t"
      "s_add_u32 s58, s54, s37\n\t"
      "s_addc_u32 s59, s55, 0\n\t"
      "s_add_u32 m0, s39, 32768\n\t"
      "s_nop 0\n\t"
      "global_load_lds_dwordx4 v228, s[58:59]\n\t"
      "global_load_lds_dwordx4 v229, s[58:59] offset:1024\n\t"
      "global_load_lds_dwordx4 v230, s[58:59] offset:2048\n\t"
      "global_load_lds_dwordx4 v231, s[58:59] offset:3072\n\t"
      "s_lshl_b32 s37, s42, 7\n\t"
      "s_add_u32 s58, s54, s37\n\t"
      "s_addc_u32 s59, s55, 0\n\t"
      "s_add_u32 m0, s39, 65536\n\t"
      "s_nop 0\n\t"
      "global_load_lds_dwordx4 v228, s[58:59]\n\t"
      "global_load_lds_dwordx4 v229, s[58:59] offset:1024\n\t"
      "global_load_lds_dwordx4 v230, s[58:59] offset:2048\n\t"
      "global_load_lds_dwordx4 v231, s[58:59] offset:3072\n\t"
      "s_lshl_b32 s37, s43, 7\n\t"
      "s_add_u32 s58, s54, s37\n\t"
      "s_addc_u32 s59, s55, 0\n\t"
      "s_add_u32 m0, s39, 98304\n\t"
      "s_nop 0\n\t"
      "global_load_lds_dwordx4 v228, s[58:59]\n\t"
      "global_load_lds_dwordx4 v229, s[58:59] offset:1024\n\t"
      "global_load_lds_dwordx4 v230, s[58:59] offset:2048\n\t"
      "global_load_lds_dwordx4 v231, s[58:59] offset:3072\n\t"
      "s_lshr_b32 s34, s24, 1\n\t"
      "s_lshl2_add_u32 s35, s29, s34\n\t"
      "s_and_b32 s36, s24, 1\n\t"
      "s_lshl4_add_u32 s35, s35, s36\n\t"
      "v_lshl_add_u32 v242, v240, 1, s35\n\t"
      "v_lshlrev_b32_e32 v248, 1, v242\n\t"
      "global_load_ushort v249, v248, s[16:17]\n\t"
      "s_movk_i32 s35, 896\n\t"
      "v_and_b32_e32 v247, 64, v244\n\t"
      "v_lshrrev_b32_e32 v247, 2, v247\n\t"
      "v_mad_u32_u24 v247, v242, s35, v247\n\t"
      "v_and_b32_e32 v242, 0x30, v244\n\t"
      "v_lshl_add_u32 v247, v242, 1, v247\n\t"
      "s_mov_b32 s82, 0x77777777\n\t"
      "s_mov_b32 s83, 0x77777777\n\t"
      "s_lshl_b32 s35, s30, 5\n\t"
      "s_add_u32 s62, s18, s35\n\t"
      "s_addc_u32 s63, s19, 0\n\t"
      "s_mul_i32 s35, s29, 28\n\t"
      "s_add_u32 s35, s35, s30\n\t"
      "s_lshl1_add_u32 s35, s35, s36\n\t"
      "s_lshl2_add_u32 s35, s35, s34\n\t"
      "s_lshl_b32 s35, s35, 8\n\t"
      "s_add_u32 s35, s35, 6291456\n\t"
      "s_add_u32 s50, s20, s35\n\t"
      "s_addc_u32 s51, s21, 0\n\t"
      "v_lshrrev_b32_e32 v248, 2, v244\n\t"
      "v_bfe_u32 v242, v244, 8, 1\n\t"
      "v_lshlrev_b32_e32 v242, 4, v242\n\t"
      "v_xor_b32_e32 v245, v244, v242\n\t"
      "v_bfe_u32 v242, v244, 9, 1\n\t"
      "v_lshlrev_b32_e32 v242, 7, v242\n\t"
      "v_xor_b32_e32 v245, v245, v242\n\t"
      "s_add_u32 s35, s39, 98304\n\t"
      "v_add_u32_e32 v245, s35, v245\n\t"
      "v_lshl_or_b32 v246, v240, 1, s36\n\t"
      "v_bfe_u32 v242, v244, 5, 1\n\t"
      "v_xor_b32_e32 v246, v246, v242\n\t"
      "v_bfe_u32 v242, v244, 6, 1\n\t"
      "v_lshlrev_b32_e32 v242, 3, v242\n\t"
      "v_xor_b32_e32 v246, v246, v242\n\t"
      "v_lshlrev_b32_e32 v246, 4, v246\n\t"
      "v_and_b32_e32 v242, 0x60, v244\n\t"
      "v_lshl_or_b32 v246, v242, 3, v246\n\t"
      "v_bfe_u32 v242, v244, 4, 1\n\t"
      "v_lshl_or_b32 v246, v242, 3, v246\n\t"
      "s_lshl_b32 s34, s34, 10\n\t"
      "s_add_u32 s34, s34, 98304\n\t"
      "v_add_u32_e32 v246, s34, v246\n\t"
      "v_and_b32_e32 v240, 0x3c0, v244\n\t"
      "v_lshlrev_b32_e32 v240, 1, v240\n\t"
      "v_and_b32_e32 v241, 0x180, v244\n\t"
      "v_lshrrev_b32_e32 v241, 2, v241\n\t"
      "v_and_b32_e32 v242, 0x30, v244\n\t"
      "v_lshrrev_b32_e32 v242, 1, v242\n\t"
      "v_or3_b32 v232, v240, v241, v242\n\t"
      "v_add_u32_e32 v232, s39, v232\n\t"
      "v_min_u32_e32 v252, 432, v244\n\t"
      "v_lshlrev_b32_e32 v252, 4, v252\n\t"
      "v_xor_b32_e32 v233, 32, v232\n\t"
      "v_xor_b32_e32 v234, 64, v232\n\t"
      "v_xor_b32_e32 v235, 96, v232\n\t"
      "v_xor_b32_e32 v236, 65536, v232\n\t"
      "v_xor_b32_e32 v237, 65568, v232\n\t"
      "v_xor_b32_e32 v238, 65600, v232\n\t"
      "v_xor_b32_e32 v239, 65632, v232\n\t"
      "s_waitcnt vmcnt(13)\n\t"
      "ds_read_b64_tr_b16 v[0:1], v232\n\t"
      "ds_read_b64_tr_b16 v[2:3], v232 offset:2048\n\t"
      "ds_read_b64_tr_b16 v[4:5], v233\n\t"
      "ds_read_b64_tr_b16 v[6:7], v233 offset:2048\n\t"
      "ds_read_b64_tr_b16 v[8:9], v234\n\t"
      "ds_read_b64_tr_b16 v[10:11], v234 offset:2048\n\t"
      "ds_read_b64_tr_b16 v[12:13], v235\n\t"
      "ds_read_b64_tr_b16 v[14:15], v235 offset:2048\n\t"
      "s_waitcnt lgkmcnt(0)\n\t"
      "s_lshl_b32 s37, s44, 7\n\t"
      "s_add_u32 s58, s54, s37\n\t"
      "s_addc_u32 s59, s55, 0\n\t"
      "s_add_u32 m0, s39, 0\n\t"
      "s_nop 0\n\t"
      "global_load_lds_dwordx4 v228, s[58:59]\n\t"
      "global_load_lds_dwordx4 v229, s[58:59] offset:1024\n\t"
      "global_load_lds_dwordx4 v230, s[58:59] offset:2048\n\t"
      "global_load_lds_dwordx4 v231, s[58:59] offset:3072\n\t"
      "s_waitcnt vmcnt(13)\n\t"
      "ds_read_b64_tr_b16 v[16:17], v232 offset:32768\n\t"
      "ds_read_b64_tr_b16 v[18:19], v232 offset:34816\n\t"
      "ds_read_b64_tr_b16 v[20:21], v233 offset:32768\n\t"
      "ds_read_b64_tr_b16 v[22:23], v233 offset:34816\n\t"
      "ds_read_b64_tr_b16 v[24:25], v234 offset:32768\n\t"
      "ds_read_b64_tr_b16 v[26:27], v234 offset:34816\n\t"
      "ds_read_b64_tr_b16 v[28:29], v235 offset:32768\n\t"
      "ds_read_b64_tr_b16 v[30:31], v235 offset:34816\n\t"
      "s_waitcnt lgkmcnt(0)\n\t"
      "s_lshl_b32 s37, s45, 7\n\t"
      "s_add_u32 s58, s54, s37\n\t"
      "s_addc_u32 s59, s55, 0\n\t"
      "s_add_u32 m0, s39, 32768\n\t"
      "s_nop 0\n\t"
      "global_load_lds_dwordx4 v228, s[58:59]\n\t"
      "global_load_lds_dwordx4 v229, s[58:59] offset:1024\n\t"
      "global_load_lds_dwordx4 v230, s[58:59] offset:2048\n\t"
      "global_load_lds_dwordx4 v231, s[58:59] offset:3072\n\t"
      "s_waitcnt vmcnt(13)\n\t"
      "ds_read_b64_tr_b16 v[32:33], v236\n\t"
      "ds_read_b64_tr_b16 v[34:35], v236 offset:2048\n\t"
      "ds_read_b64_tr_b16 v[36:37], v237\n\t"
      "ds_read_b64_tr_b16 v[38:39], v237 offset:2048\n\t"
      "ds_read_b64_tr_b16 v[40:41], v238\n\t"
      "ds_read_b64_tr_b16 v[42:43], v238 offset:2048\n\t"
      "ds_read_b64_tr_b16 v[44:45], v239\n\t"
      "ds_read_b64_tr_b16 v[46:47], v239 offset:2048\n\t"
      "s_waitcnt lgkmcnt(0)\n\t"
      "s_lshl_b32 s37, s46, 7\n\t"
      "s_add_u32 s58, s54, s37\n\t"
      "s_addc_u32 s59, s55, 0\n\t"
      "s_add_u32 m0, s39, 65536\n\t"
      "s_nop 0\n\t"
      "global_load_lds_dwordx4 v228, s[58:59]\n\t"
      "global_load_lds_dwordx4 v229, s[58:59] offset:1024\n\t"
      "global_load_lds_dwordx4 v230, s[58:59] offset:2048\n\t"
      "global_load_lds_dwordx4 v231, s[58:59] offset:3072\n\t"
      "s_waitcnt vmcnt(13)\n\t"
      "ds_read_b64_tr_b16 v[48:49], v236 offset:32768\n\t"
      "ds_read_b64_tr_b16 v[50:51], v236 offset:34816\n\t"
      "ds_read_b64_tr_b16 v[52:53], v237 offset:32768\n\t"
      "ds_read_b64_tr_b16 v[54:55], v237 offset:34816\n\t"
      "ds_read_b64_tr_b16 v[56:57], v238 offset:32768\n\t"
      "ds_read_b64_tr_b16 v[58:59], v238 offset:34816\n\t"
      "ds_read_b64_tr_b16 v[60:61], v239 offset:32768\n\t"
      "ds_read_b64_tr_b16 v[62:63], v239 offset:34816\n\t"
      "s_waitcnt vmcnt(8)\n\t"
      "ds_read_b64_tr_b16 v[64:65], v232\n\t"
      "ds_read_b64_tr_b16 v[66:67], v232 offset:2048\n\t"
      "ds_read_b64_tr_b16 v[68:69], v233\n\t"
      "ds_read_b64_tr_b16 v[70:71], v233 offset:2048\n\t"
      "ds_read_b64_tr_b16 v[72:73], v234\n\t"
      "ds_read_b64_tr_b16 v[74:75], v234 offset:2048\n\t"
      "ds_read_b64_tr_b16 v[76:77], v235\n\t"
      "ds_read_b64_tr_b16 v[78:79], v235 offset:2048\n\t"
      "s_waitcnt vmcnt(4)\n\t"
      "ds_read_b64_tr_b16 v[80:81], v232 offset:32768\n\t"
      "ds_read_b64_tr_b16 v[82:83], v232 offset:34816\n\t"
      "ds_read_b64_tr_b16 v[84:85], v233 offset:32768\n\t"
      "ds_read_b64_tr_b16 v[86:87], v233 offset:34816\n\t"
      "ds_read_b64_tr_b16 v[88:89], v234 offset:32768\n\t"
      "ds_read_b64_tr_b16 v[90:91], v234 offset:34816\n\t"
      "ds_read_b64_tr_b16 v[92:93], v235 offset:32768\n\t"
      "ds_read_b64_tr_b16 v[94:95], v235 offset:34816\n\t"
      "s_waitcnt vmcnt(0)\n\t"
      "ds_read_b64_tr_b16 v[96:97], v236\n\t"
      "ds_read_b64_tr_b16 v[98:99], v236 offset:2048\n\t"
      "ds_read_b64_tr_b16 v[100:101], v237\n\t"
      "ds_read_b64_tr_b16 v[102:103], v237 offset:2048\n\t"
      "ds_read_b64_tr_b16 v[104:105], v238\n\t"
      "ds_read_b64_tr_b16 v[106:107], v238 offset:2048\n\t"
      "ds_read_b64_tr_b16 v[108:109], v239\n\t"
      "ds_read_b64_tr_b16 v[110:111], v239 offset:2048\n\t"
      "v_lshlrev_b32_e32 v249, 16, v249\n\t"
      "s_waitcnt lgkmcnt(0)\n\t"
      "v_min_u32_e32 v240, 48, v244\n\t"
      "v_mul_u32_u24_e32 v240, 7, v240\n\t"
      "s_lshl_b32 s34, s29, 4\n\t"
      "v_add_u32_e32 v240, s34, v240\n\t"
      "v_subrev_u32_e32 v241, 448, v240\n\t"
      "v_min_u32_e32 v240, v240, v241\n\t"
      "v_subrev_u32_e32 v241, 448, v240\n\t"
      "v_min_u32_e32 v240, v240, v241\n\t"
      "s_and_b32 s34, s29, 7\n\t"
      "s_lshl_b32 s34, s34, 8\n\t"
      "v_lshl_add_u32 v253, v240, 7, s34\n\t"
      "s_cmp_lg_u32 s24, 3\n\t"
      "s_cbranch_scc1 SK16_%=\n\t"
      "global_load_dwordx2 v[138:139], v143, s[60:61] sc1\n\t"
      "SK16_%=:\n\t"
      "s_cmp_lg_u32 s24, 3\n\t"
      "s_cbranch_scc1 LF5B_%=\n\t"
      "s_waitcnt vmcnt(0)\n\t"
      "v_cmp_ne_u64_e32 vcc, s[22:23], v[138:139]\n\t"
      "s_cbranch_vccnz LF5R_%=\n\t"
      "LF5B_%=:\n\t"
      "s_barrier\n\t"
      "s_cmp_eq_u32 s24, 0\n\t"
      "s_cbranch_scc1 PG0R_%=\n\t"
      "PG0B_%=:\n\t"
      "s_barrier\n\t"
      "s_add_u32 s58, s64, s40\n\t"
      "s_addc_u32 s59, s65, 0\n\t"
      "global_load_dwordx4 v[128:131], v244, s[58:59] sc1\n\t"
      "s_add_u32 s58, s64, s41\n\t"
      "s_addc_u32 s59, s65, 0\n\t"
      "global_load_dwordx4 v[132:135], v244, s[58:59] sc1\n\t"
      "s_add_u32 s58, s64, s42\n\t"
      "s_addc_u32 s59, s65, 0\n\t"
      "global_load_dwordx4 v[136:139], v244, s[58:59] sc1\n\t"
      "s_add_u32 s58, s64, s43\n\t"
      "s_addc_u32 s59, s65, 0\n\t"
      "global_load_dwordx4 v[140:143], v244, s[58:59] sc1\n\t"
      "s_add_u32 s58, s64, s44\n\t"
      "s_addc_u32 s59, s65, 0\n\t"
      "global_load_dwordx4 v[144:147], v244, s[58:59] sc1\n\t"
      "s_add_u32 s58, s64, s45\n\t"
      "s_addc_u32 s59, s65, 0\n\t"
      "global_load_dwordx4 v[148:151], v244, s[58:59] sc1\n\t"
      "s_add_u32 s58, s64, s46\n\t"
      "s_addc_u32 s59, s65, 0\n\t"
      "global_load_dwordx4 v[152:155], v244, s[58:59] sc1\n\t"
      "s_cmp_eq_u32 s24, 0\n\t"
      "s_cbranch_scc1 PG1R_%=\n\t"
      "PG1B_%=:\n\t"
      "s_barrier\n\t"
      "s_waitcnt vmcnt(6)\n\t"
      "v_mfma_f32_16x16x32_bf16 v[208:211], v[128:131], v[0:3], 0\n\t"
      "s_add_u32 s58, s66, s40\n\t"
      "s_addc_u32 s59, s67, 0\n\t"
      "global_load_dwordx4 v[156:159], v244, s[58:59] sc1\n\t"
      "s_waitcnt vmcnt(6)\n\t"
      "v_mfma_f32_16x16x32_bf16 v[208:211], v[132:135], v[16:19], v[208:211]\n\t"
      "s_waitcnt vmcnt(5)\n\t"
      "v_mfma_f32_16x16x32_bf16 v[208:211], v[136:139], v[32:35], v[208:211]\n\t"
      "s_add_u32 s58, s66, s41\n\t"
      "s_addc_u32 s59, s67, 0\n\t"
      "global_load_dwordx4 v[160:163], v244, s[58:59] sc1\n\t"
      "s_waitcnt vmcnt(5)\n\t"
      "v_mfma_f32_16x16x32_bf16 v[208:211], v[140:143], v[48:51], v[208:211]\n\t"
      "s_waitcnt vmcnt(4)\n\t"
      "v_mfma_f32_16x16x32_bf16 v[208:211], v[144:147], v[64:67], v[208:211]\n\t"
      "s_add_u32 s58, s66, s42\n\t"
      "s_addc_u32 s59, s67, 0\n\t"
      "global_load_dwordx4 v[164:167], v244, s[58:59] sc1\n\t"
      "s_waitcnt vmcnt(4)\n\t"
      "v_mfma_f32_16x16x32_bf16 v[208:211], v[148:151], v[80:83], v[208:211]\n\t"
      "s_waitcnt vmcnt(3)\n\t"
      "v_mfma_f32_16x16x32_bf16 v[208:211], v[152:155], v[96:99], v[208:211]\n\t"
      "s_add_u32 s58, s66, s43\n\t"
      "s_addc_u32 s59, s67, 0\n\t"
      "global_load_dwordx4 v[168:171], v244, s[58:59] sc1\n\t"
      "v_mfma_f32_16x16x32_bf16 v[212:215], v[128:131], v[4:7], 0\n\t"
      "v_mfma_f32_16x16x32_bf16 v[212:215], v[132:135], v[20:23], v[212:215]\n\t"
      "s_add_u32 s58, s66, s44\n\t"
      "s_addc_u32 s59, s67, 0\n\t"
      "global_load_dwordx4 v[172:175], v244, s[58:59] sc1\n\t"
      "v_mfma_f32_16x16x32_bf16 v[212:215], v[136:139], v[36:39], v[212:215]\n\t"
      "v_add_f32_e32 v251, v208, v209\n\t"
      "v_add_f32_e32 v251, v251, v210\n\t"
      "v_cmp_u_f32_e32 vcc, v251, v211\n\t"
      "s_cbranch_vccnz LA40R_%=\n\t"
      "LA40B_%=:\n\t"
      "v_mfma_f32_16x16x32_bf16 v[212:215], v[140:143], v[52:55], v[212:215]\n\t"
      "s_add_u32 s58, s66, s45\n\t"
      "s_addc_u32 s59, s67, 0\n\t"
      "global_load_dwordx4 v[176:179], v244, s[58:59] sc1\n\t"
      "v_mfma_f32_16x16x32_bf16 v[212:215], v[144:147], v[68:71], v[212:215]\n\t"
      "v_mfma_f32_16x16x32_bf16 v[212:215], v[148:151], v[84:87], v[212:215]\n\t"
      "ds_write_b128 v245, v[208:211]\n\t"
      "global_load_dword v112, v248, s[50:51] sc1\n\t"
      "s_add_u32 s50, s50, 2048\n\t"
      "s_addc_u32 s51, s51, 0\n\t"
      "s_add_u32 s58, s66, s46\n\t"
      "s_addc_u32 s59, s67, 0\n\t"
      "global_load_dwordx4 v[180:183], v244, s[58:59] sc1\n\t"
      "v_mfma_f32_16x16x32_bf16 v[212:215], v[152:155], v[100:103], v[212:215]\n\t"
      "v_mfma_f32_16x16x32_bf16 v[216:219], v[128:131], v[8:11], 0\n\t"
      "v_mfma_f32_16x16x32_bf16 v[216:219], v[132:135], v[24:27], v[216:219]\n\t"
      "v_mfma_f32_16x16x32_bf16 v[216:219], v[136:139], v[40:43], v[216:219]\n\t"
      "v_mfma_f32_16x16x32_bf16 v[216:219], v[140:143], v[56:59], v[216:219]\n\t"
      "s_add_u32 s58, s68, s40\n\t"
      "s_addc_u32 s59, s69, 0\n\t"
      "global_load_dwordx4 v[184:187], v244, s[58:59] sc1\n\t"
      "v_mfma_f32_16x16x32_bf16 v[216:219], v[144:147], v[72:75], v[216:219]\n\t"
      "v_mfma_f32_16x16x32_bf16 v[216:219], v[148:151], v[88:91], v[216:219]\n\t"
      "ds_write_b128 v245, v[212:215] offset:1024\n\t"
      "v_mfma_f32_16x16x32_bf16 v[216:219], v[152:155], v[104:107], v[216:219]\n\t"
      "v_mfma_f32_16x16x32_bf16 v[220:223], v[128:131], v[12:15], 0\n\t"
      "s_add_u32 s58, s68, s41\n\t"
      "s_addc_u32 s59, s69, 0\n\t"
      "global_load_dwordx4 v[188:191], v244, s[58:59] sc1\n\t"
      "v_mfma_f32_16x16x32_bf16 v[220:223], v[132:135], v[28:31], v[220:223]\n\t"
      "v_mfma_f32_16x16x32_bf16 v[220:223], v[136:139], v[44:47], v[220:223]\n\t"
      "v_mfma_f32_16x16x32_bf16 v[220:223], v[140:143], v[60:63], v[220:223]\n\t"
      "v_mfma_f32_16x16x32_bf16 v[220:223], v[144:147], v[76:79], v[220:223]\n\t"
      "s_add_u32 s58, s68, s42\n\t"
      "s_addc_u32 s59, s69, 0\n\t"
      "global_load_dwordx4 v[192:195], v244, s[58:59] sc1\n\t"
      "v_mfma_f32_16x16x32_bf16 v[220:223], v[148:151], v[92:95], v[220:223]\n\t"
      "ds_write_b128 v245, v[216:219] offset:2048\n\t"
      "v_mfma_f32_16x16x32_bf16 v[220:223], v[152:155], v[108:111], v[220:223]\n\t"
      "s_waitcnt vmcnt(10)\n\t"
      "v_mfma_f32_16x16x32_bf16 v[224:227], v[156:159], v[0:3], 0\n\t"
      "s_waitcnt vmcnt(9)\n\t"
      "v_mfma_f32_16x16x32_bf16 v[224:227], v[160:163], v[16:19], v[224:227]\n\t"
      "s_add_u32 s58, s68, s43\n\t"
      "s_addc_u32 s59, s69, 0\n\t"
      "global_load_dwordx4 v[196:199], v244, s[58:59] sc1\n\t"
      "s_nop 0\n\t"
      "ds_write_b128 v245, v[220:223] offset:3072\n\t"
      "s_waitcnt vmcnt(9)\n\t"
      "v_mfma_f32_16x16x32_bf16 v[224:227], v[164:167], v[32:35], v[224:227]\n\t"
      "s_waitcnt vmcnt(8)\n\t"
      "v_mfma_f32_16x16x32_bf16 v[224:227], v[168:171], v[48:51], v[224:227]\n\t"
      "s_waitcnt vmcnt(7)\n\t"
      "v_mfma_f32_16x16x32_bf16 v[224:227], v[172:175], v[64:67], v[224:227]\n\t"
      "s_waitcnt lgkmcnt(0)\n\t"
      "s_barrier\n\t"
      "ds_read_b64 v[228:229], v246\n\t"
      "ds_read_b64 v[230:231], v246 offset:4096\n\t"
      "s_waitcnt vmcnt(6)\n\t"
      "v_mfma_f32_16x16x32_bf16 v[224:227], v[176:179], v[80:83], v[224:227]\n\t"
      "s_add_u32 s58, s68, s44\n\t"
      "s_addc_u32 s59, s69, 0\n\t"
      "global_load_dwordx4 v[200:203], v244, s[58:59] sc1\n\t"
      "ds_read_b64 v[232:233], v246 offset:8192\n\t"
      "ds_read_b64 v[234:235], v246 offset:12288\n\t"
      "s_waitcnt vmcnt(5)\n\t"
      "v_mfma_f32_16x16x32_bf16 v[224:227], v[180:183], v[96:99], v[224:227]\n\t"
      "ds_read_b64 v[236:237], v246 offset:16384\n\t"
      "ds_read_b64 v[238:239], v246 offset:20480\n\t"
      "v_mfma_f32_16x16x32_bf16 v[208:211], v[156:159], v[4:7], 0\n\t"
      "ds_read_b64 v[240:241], v246 offset:24576\n\t"
      "ds_read_b64 v[242:243], v246 offset:28672\n\t"
      "v_lshlrev_b32_e32 v113, 16, v112\n\t"
      "v_mfma_f32_16x16x32_bf16 v[208:211], v[160:163], v[20:23], v[208:211]\n\t"
      "v_and_b32_e32 v114, 0xffff0000, v112\n\t"
      "v_mfma_f32_16x16x32_bf16 v[208:211], v[164:167], v[36:39], v[208:211]\n\t"
      "s_add_u32 s58, s68, s45\n\t"
      "s_addc_u32 s59, s69, 0\n\t"
      "global_load_dwordx4 v[204:207], v244, s[58:59] sc1\n\t"
      "v_add_f32_e32 v113, v249, v113\n\t"
      "v_add_f32_e32 v251, v224, v225\n\t"
      "v_add_f32_e32 v251, v251, v226\n\t"
      "v_cmp_u_f32_e32 vcc, v251, v227\n\t"
      "s_cbranch_vccnz LA41R_%=\n\t"
      "LA41B_%=:\n\t"
      "v_mfma_f32_16x16x32_bf16 v[208:211], v[168:171], v[52:55], v[208:211]\n\t"
      "v_add_f32_e32 v114, v249, v114\n\t"
      "s_waitcnt lgkmcnt(4)\n\t"
      "v_mfma_f32_16x16x32_bf16 v[208:211], v[172:175], v[68:71], v[208:211]\n\t"
      "v_add_f32_e32 v228, v228, v230\n\t"
      "v_mfma_f32_16x16x32_bf16 v[208:211], v[176:179], v[84:87], v[208:211]\n\t"
      "v_add_f32_e32 v232, v232, v234\n\t"
      "ds_write_b128 v245, v[224:227] offset:32768\n\t"
      "global_load_dword v112, v248, s[50:51] sc1\n\t"
      "s_add_u32 s50, s50, 2048\n\t"
      "s_addc_u32 s51, s51, 0\n\t"
      "v_mfma_f32_16x16x32_bf16 v[208:211], v[180:183], v[100:103], v[208:211]\n\t"
      "s_add_u32 s58, s68, s46\n\t"
      "s_addc_u32 s59, s69, 0\n\t"
      "global_load_dwordx4 v[128:131], v244, s[58:59] sc1\n\t"
      "v_add_f32_e32 v229, v229, v231\n\t"
      "v_add_f32_e32 v233, v233, v235\n\t"
      "v_mfma_f32_16x16x32_bf16 v[212:215], v[156:159], v[8:11], 0\n\t"
      "s_waitcnt lgkmcnt(1)\n\t"
      "v_mfma_f32_16x16x32_bf16 v[212:215], v[160:163], v[24:27], v[212:215]\n\t"
      "v_add_f32_e32 v236, v236, v238\n\t"
      "v_mfma_f32_16x16x32_bf16 v[212:215], v[164:167], v[40:43], v[212:215]\n\t"
      "v_add_f32_e32 v240, v240, v242\n\t"
      "v_mfma_f32_16x16x32_bf16 v[212:215], v[168:171], v[56:59], v[212:215]\n\t"
      "v_add_f32_e32 v237, v237, v239\n\t"
      "v_add_f32_e32 v241, v241, v243\n\t"
      "s_add_u32 s58, s70, s40\n\t"
      "s_addc_u32 s59, s71, 0\n\t"
      "global_load_dwordx4 v[132:135], v244, s[58:59] sc1\n\t"
      "v_mfma_f32_16x16x32_bf16 v[212:215], v[172:175], v[72:75], v[212:215]\n\t"
      "v_add_f32_e32 v228, v228, v232\n\t"
      "v_mfma_f32_16x16x32_bf16 v[212:215], v[176:179], v[88:91], v[212:215]\n\t"
      "v_add_f32_e32 v236, v236, v240\n\t"
      "ds_write_b128 v245, v[208:211] offset:33792\n\t"
      "v_mfma_f32_16x16x32_bf16 v[212:215], v[180:183], v[104:107], v[212:215]\n\t"
      "v_add_f32_e32 v229, v229, v233\n\t"
      "v_add_f32_e32 v237, v237, v241\n\t"
      "v_mfma_f32_16x16x32_bf16 v[216:219], v[156:159], v[12:15], 0\n\t"
      "v_add_f32_e32 v228, v228, v113\n\t"
      "s_add_u32 s58, s70, s41\n\t"
      "s_addc_u32 s59, s71, 0\n\t"
      "global_load_dwordx4 v[136:139], v244, s[58:59] sc1\n\t"
      "v_mfma_f32_16x16x32_bf16 v[216:219], v[160:163], v[28:31], v[216:219]\n\t"
      "v_add_f32_e32 v229, v229, v114\n\t"
      "v_mfma_f32_16x16x32_bf16 v[216:219], v[164:167], v[44:47], v[216:219]\n\t"
      "v_add_f32_e32 v228, v228, v236\n\t"
      "v_add_f32_e32 v229, v229, v237\n\t"
      "v_mfma_f32_16x16x32_bf16 v[216:219], v[168:171], v[60:63], v[216:219]\n\t"
      "v_cvt_pk_bf16_f32 v232, v228, v229\n\t"
      "v_mfma_f32_16x16x32_bf16 v[216:219], v[172:175], v[76:79], v[216:219]\n\t"
      "s_mov_b32 vcc_lo, 0xeeeeeeee\n\t"
      "s_mov_b32 vcc_hi, 0xeeeeeeee\n\t"
      "v_cndmask_b32_dpp v116, v232, v116, vcc quad_perm:[0,0,0,0] row_mask:0xf bank_mask:0xf\n\t"
      "v_cndmask_b32_dpp v117, v232, v117, vcc quad_perm:[1,1,1,1] row_mask:0xf bank_mask:0xf\n\t"
      "v_cndmask_b32_dpp v118, v232, v118, vcc quad_perm:[2,2,2,2] row_mask:0xf bank_mask:0xf\n\t"
      "v_cndmask_b32_dpp v119, v232, v119, vcc quad_perm:[3,3,3,3] row_mask:0xf bank_mask:0xf\n\t"
      "s_add_u32 s58, s70, s42\n\t"
      "s_addc_u32 s59, s71, 0\n\t"
      "global_load_dwordx4 v[140:143], v244, s[58:59] sc1\n\t"
      "v_mfma_f32_16x16x32_bf16 v[216:219], v[176:179], v[92:95], v[216:219]\n\t"
      "ds_write_b128 v245, v[212:215] offset:34816\n\t"
      "v_mfma_f32_16x16x32_bf16 v[216:219], v[180:183], v[108:111], v[216:219]\n\t"
      "s_waitcnt vmcnt(10)\n\t"
      "v_mfma_f32_16x16x32_bf16 v[220:223], v[184:187], v[0:3], 0\n\t"
      "s_waitcnt vmcnt(9)\n\t"
      "v_mfma_f32_16x16x32_bf16 v[220:223], v[188:191], v[16:19], v[220:223]\n\t"
      "s_add_u32 s58, s70, s43\n\t"
      "s_addc_u32 s59, s71, 0\n\t"
      "global_load_dwordx4 v[144:147], v244, s[58:59] sc1\n\t"
      "s_nop 0\n\t"
      "ds_write_b128 v245, v[216:219] offset:35840\n\t"
      "s_waitcnt vmcnt(9)\n\t"
      "v_mfma_f32_16x16x32_bf16 v[220:223], v[192:195], v[32:35], v[220:223]\n\t"
      "s_waitcnt vmcnt(8)\n\t"
      "v_mfma_f32_16x16x32_bf16 v[220:223], v[196:199], v[48:51], v[220:223]\n\t"
      "s_waitcnt vmcnt(7)\n\t"
      "v_mfma_f32_16x16x32_bf16 v[220:223], v[200:203], v[64:67], v[220:223]\n\t"
      "s_waitcnt lgkmcnt(0)\n\t"
      "s_barrier\n\t"
      "ds_read_b64 v[228:229], v246 offset:32768\n\t"
      "ds_read_b64 v[230:231], v246 offset:36864\n\t"
      "s_waitcnt vmcnt(6)\n\t"
      "v_mfma_f32_16x16x32_bf16 v[220:223], v[204:207], v[80:83], v[220:223]\n\t"
      "s_add_u32 s58, s70, s44\n\t"
      "s_addc_u32 s59, s71, 0\n\t"
      "global_load_dwordx4 v[148:151], v244, s[58:59] sc1\n\t"
      "ds_read_b64 v[232:233], v246 offset:40960\n\t"
      "ds_read_b64 v[234:235], v246 offset:45056\n\t"
      "s_waitcnt vmcnt(5)\n\t"
      "v_mfma_f32_16x16x32_bf16 v[220:223], v[128:131], v[96:99], v[220:223]\n\t"
      "ds_read_b64 v[236:237], v246 offset:49152\n\t"
      "ds_read_b64 v[238:239], v246 offset:53248\n\t"
      "v_mfma_f32_16x16x32_bf16 v[224:227], v[184:187], v[4:7], 0\n\t"
      "ds_read_b64 v[240:241], v246 offset:57344\n\t"
      "ds_read_b64 v[242:243], v246 offset:61440\n\t"
      "v_lshlrev_b32_e32 v113, 16, v112\n\t"
      "v_mfma_f32_16x16x32_bf16 v[224:227], v[188:191], v[20:23], v[224:227]\n\t"
      "v_and_b32_e32 v114, 0xffff0000, v112\n\t"
      "v_mfma_f32_16x16x32_bf16 v[224:227], v[192:195], v[36:39], v[224:227]\n\t"
      "s_add_u32 s58, s70, s45\n\t"
      "s_addc_u32 s59, s71, 0\n\t"
      "global_load_dwordx4 v[152:155], v244, s[58:59] sc1\n\t"
      "v_add_f32_e32 v113, v249, v113\n\t"
      "v_add_f32_e32 v251, v220, v221\n\t"
      "v_add_f32_e32 v251, v251, v222\n\t"
      "v_cmp_u_f32_e32 vcc, v251, v223\n\t"
      "s_cbranch_vccnz LA42R_%=\n\t"
      "LA42B_%=:\n\t"
      "v_mfma_f32_16x16x32_bf16 v[224:227], v[196:199], v[52:55], v[224:227]\n\t"
      "v_add_f32_e32 v114, v249, v114\n\t"
      "s_waitcnt lgkmcnt(4)\n\t"
      "v_mfma_f32_16x16x32_bf16 v[224:227], v[200:203], v[68:71], v[224:227]\n\t"
      "v_add_f32_e32 v228, v228, v230\n\t"
      "v_mfma_f32_16x16x32_bf16 v[224:227], v[204:207], v[84:87], v[224:227]\n\t"
      "v_add_f32_e32 v232, v232, v234\n\t"
      "ds_write_b128 v245, v[220:223]\n\t"
      "global_load_dword v112, v248, s[50:51] sc1\n\t"
      "s_add_u32 s50, s50, 2048\n\t"
      "s_addc_u32 s51, s51, 0\n\t"
      "v_mfma_f32_16x16x32_bf16 v[224:227], v[128:131], v[100:103], v[224:227]\n\t"
      "s_add_u32 s58, s70, s46\n\t"
      "s_addc_u32 s59, s71, 0\n\t"
      "global_load_dwordx4 v[156:159], v244, s[58:59] sc1\n\t"
      "v_add_f32_e32 v229, v229, v231\n\t"
      "v_add_f32_e32 v233, v233, v235\n\t"
      "v_mfma_f32_16x16x32_bf16 v[208:211], v[184:187], v[8:11], 0\n\t"
      "s_waitcnt lgkmcnt(1)\n\t"
      "v_mfma_f32_16x16x32_bf16 v[208:211], v[188:191], v[24:27], v[208:211]\n\t"
      "v_add_f32_e32 v236, v236, v238\n\t"
      "v_mfma_f32_16x16x32_bf16 v[208:211], v[192:195], v[40:43], v[208:211]\n\t"
      "v_add_f32_e32 v240, v240, v242\n\t"
      "v_mfma_f32_16x16x32_bf16 v[208:211], v[196:199], v[56:59], v[208:211]\n\t"
      "v_add_f32_e32 v237, v237, v239\n\t"
      "v_add_f32_e32 v241, v241, v243\n\t"
      "s_add_u32 s58, s72, s40\n\t"
      "s_addc_u32 s59, s73, 0\n\t"
      "global_load_dwordx4 v[160:163], v244, s[58:59] sc1\n\t"
      "v_mfma_f32_16x16x32_bf16 v[208:211], v[200:203], v[72:75], v[208:211]\n\t"
      "v_add_f32_e32 v228, v228, v232\n\t"
      "v_mfma_f32_16x16x32_bf16 v[208:211], v[204:207], v[88:91], v[208:211]\n\t"
      "v_add_f32_e32 v236, v236, v240\n\t"
      "ds_write_b128 v245, v[224:227] offset:1024\n\t"
      "v_mfma_f32_16x16x32_bf16 v[208:211], v[128:131], v[104:107], v[208:211]\n\t"
      "v_add_f32_e32 v229, v229, v233\n\t"
      "v_add_f32_e32 v237, v237, v241\n\t"
      "v_mfma_f32_16x16x32_bf16 v[212:215], v[184:187], v[12:15], 0\n\t"
      "v_add_f32_e32 v228, v228, v113\n\t"
      "s_add_u32 s58, s72, s41\n\t"
      "s_addc_u32 s59, s73, 0\n\t"
      "global_load_dwordx4 v[164:167], v244, s[58:59] sc1\n\t"
      "v_mfma_f32_16x16x32_bf16 v[212:215], v[188:191], v[28:31], v[212:215]\n\t"
      "v_add_f32_e32 v229, v229, v114\n\t"
      "v_mfma_f32_16x16x32_bf16 v[212:215], v[192:195], v[44:47], v[212:215]\n\t"
      "v_add_f32_e32 v228, v228, v236\n\t"
      "v_add_f32_e32 v229, v229, v237\n\t"
      "v_mfma_f32_16x16x32_bf16 v[212:215], v[196:199], v[60:63], v[212:215]\n\t"
      "v_cvt_pk_bf16_f32 v232, v228, v229\n\t"
      "v_mfma_f32_16x16x32_bf16 v[212:215], v[200:203], v[76:79], v[212:215]\n\t"
      "s_mov_b32 vcc_lo, 0xdddddddd\n\t"
      "s_mov_b32 vcc_hi, 0xdddddddd\n\t"
      "v_cndmask_b32_dpp v116, v232, v116, vcc quad_perm:[0,0,0,0] row_mask:0xf bank_mask:0xf\n\t"
      "v_cndmask_b32_dpp v117, v232, v117, vcc quad_perm:[1,1,1,1] row_mask:0xf bank_mask:0xf\n\t"
      "v_cndmask_b32_dpp v118, v232, v118, vcc quad_perm:[2,2,2,2] row_mask:0xf bank_mask:0xf\n\t"
      "v_cndmask_b32_dpp v119, v232, v119, vcc quad_perm:[3,3,3,3] row_mask:0xf bank_mask:0xf\n\t"
      "s_add_u32 s58, s72, s42\n\t"
      "s_addc_u32 s59, s73, 0\n\t"
      "global_load_dwordx4 v[168:171], v244, s[58:59] sc1\n\t"
      "v_mfma_f32_16x16x32_bf16 v[212:215], v[204:207], v[92:95], v[212:215]\n\t"
      "ds_write_b128 v245, v[208:211] offset:2048\n\t"
      "v_mfma_f32_16x16x32_bf16 v[212:215], v[128:131], v[108:111], v[212:215]\n\t"
      "s_waitcnt vmcnt(10)\n\t"
      "v_mfma_f32_16x16x32_bf16 v[216:219], v[132:135], v[0:3], 0\n\t"
      "s_waitcnt vmcnt(9)\n\t"
      "v_mfma_f32_16x16x32_bf16 v[216:219], v[136:139], v[16:19], v[216:219]\n\t"
      "s_add_u32 s58, s72, s43\n\t"
      "s_addc_u32 s59, s73, 0\n\t"
      "global_load_dwordx4 v[172:175], v244, s[58:59] sc1\n\t"
      "s_nop 0\n\t"
      "ds_write_b128 v245, v[212:215] offset:3072\n\t"
      "s_waitcnt vmcnt(9)\n\t"
      "v_mfma_f32_16x16x32_bf16 v[216:219], v[140:143], v[32:35], v[216:219]\n\t"
      "s_waitcnt vmcnt(8)\n\t"
      "v_mfma_f32_16x16x32_bf16 v[216:219], v[144:147], v[48:51], v[216:219]\n\t"
      "s_waitcnt vmcnt(7)\n\t"
      "v_mfma_f32_16x16x32_bf16 v[216:219], v[148:151], v[64:67], v[216:219]\n\t"
      "s_waitcnt lgkmcnt(0)\n\t"
      "s_barrier\n\t"
      "ds_read_b64 v[228:229], v246\n\t"
      "ds_read_b64 v[230:231], v246 offset:4096\n\t"
      "s_waitcnt vmcnt(6)\n\t"
      "v_mfma_f32_16x16x32_bf16 v[216:219], v[152:155], v[80:83], v[216:219]\n\t"
      "s_add_u32 s58, s72, s44\n\t"
      "s_addc_u32 s59, s73, 0\n\t"
      "global_load_dwordx4 v[176:179], v244, s[58:59] sc1\n\t"
      "ds_read_b64 v[232:233], v246 offset:8192\n\t"
      "ds_read_b64 v[234:235], v246 offset:12288\n\t"
      "s_waitcnt vmcnt(5)\n\t"
      "v_mfma_f32_16x16x32_bf16 v[216:219], v[156:159], v[96:99], v[216:219]\n\t"
      "ds_read_b64 v[236:237], v246 offset:16384\n\t"
      "ds_read_b64 v[238:239], v246 offset:20480\n\t"
      "v_mfma_f32_16x16x32_bf16 v[220:223], v[132:135], v[4:7], 0\n\t"
      "ds_read_b64 v[240:241], v246 offset:24576\n\t"
      "ds_read_b64 v[242:243], v246 offset:28672\n\t"
      "v_lshlrev_b32_e32 v113, 16, v112\n\t"
      "v_mfma_f32_16x16x32_bf16 v[220:223], v[136:139], v[20:23], v[220:223]\n\t"
      "v_and_b32_e32 v114, 0xffff0000, v112\n\t"
      "v_mfma_f32_16x16x32_bf16 v[220:223], v[140:143], v[36:39], v[220:223]\n\t"
      "s_add_u32 s58, s72, s45\n\t"
      "s_addc_u32 s59, s73, 0\n\t"
      "global_load_dwordx4 v[180:183], v244, s[58:59] sc1\n\t"
      "v_add_f32_e32 v113, v249, v113\n\t"
      "v_add_f32_e32 v251, v216, v217\n\t"
      "v_add_f32_e32 v251, v251, v218\n\t"
      "v_cmp_u_f32_e32 vcc, v251, v219\n\t"
      "s_cbranch_vccnz LA43R_%=\n\t"
      "LA43B_%=:\n\t"
      "v_mfma_f32_16x16x32_bf16 v[220:223], v[144:147], v[52:55], v[220:223]\n\t"
      "v_add_f32_e32 v114, v249, v114\n\t"
      "s_waitcnt lgkmcnt(4)\n\t"
      "v_mfma_f32_16x16x32_bf16 v[220:223], v[148:151], v[68:71], v[220:223]\n\t"
      "v_add_f32_e32 v228, v228, v230\n\t"
      "v_mfma_f32_16x16x32_bf16 v[220:223], v[152:155], v[84:87], v[220:223]\n\t"
      "v_add_f32_e32 v232, v232, v234\n\t"
      "ds_write_b128 v245, v[216:219] offset:32768\n\t"
      "global_load_dword v112, v248, s[50:51] sc1\n\t"
      "s_add_u32 s50, s50, 2048\n\t"
      "s_addc_u32 s51, s51, 0\n\t"
      "v_mfma_f32_16x16x32_bf16 v[220:223], v[156:159], v[100:103], v[220:223]\n\t"
      "s_add_u32 s58, s72, s46\n\t"
      "s_addc_u32 s59, s73, 0\n\t"
      "global_load_dwordx4 v[184:187], v244, s[58:59] sc1\n\t"
      "v_add_f32_e32 v229, v229, v231\n\t"
      "v_add_f32_e32 v233, v233, v235\n\t"
      "v_mfma_f32_16x16x32_bf16 v[224:227], v[132:135], v[8:11], 0\n\t"
      "s_waitcnt lgkmcnt(1)\n\t"
      "v_mfma_f32_16x16x32_bf16 v[224:227], v[136:139], v[24:27], v[224:227]\n\t"
      "v_add_f32_e32 v236, v236, v238\n\t"
      "v_mfma_f32_16x16x32_bf16 v[224:227], v[140:143], v[40:43], v[224:227]\n\t"
      "v_add_f32_e32 v240, v240, v242\n\t"
      "v_mfma_f32_16x16x32_bf16 v[224:227], v[144:147], v[56:59], v[224:227]\n\t"
      "v_add_f32_e32 v237, v237, v239\n\t"
      "v_add_f32_e32 v241, v241, v243\n\t"
      "s_add_u32 s58, s74, s40\n\t"
      "s_addc_u32 s59, s75, 0\n\t"
      "global_load_dwordx4 v[188:191], v244, s[58:59] sc1\n\t"
      "v_mfma_f32_16x16x32_bf16 v[224:227], v[148:151], v[72:75], v[224:227]\n\t"
      "v_add_f32_e32 v228, v228, v232\n\t"
      "v_mfma_f32_16x16x32_bf16 v[224:227], v[152:155], v[88:91], v[224:227]\n\t"
      "v_add_f32_e32 v236, v236, v240\n\t"
      "ds_write_b128 v245, v[220:223] offset:33792\n\t"
      "v_mfma_f32_16x16x32_bf16 v[224:227], v[156:159], v[104:107], v[224:227]\n\t"
      "v_add_f32_e32 v229, v229, v233\n\t"
      "v_add_f32_e32 v237, v237, v241\n\t"
      "v_mfma_f32_16x16x32_bf16 v[208:211], v[132:135], v[12:15], 0\n\t"
      "v_add_f32_e32 v228, v228, v113\n\t"
      "s_add_u32 s58, s74, s41\n\t"
      "s_addc_u32 s59, s75, 0\n\t"
      "global_load_dwordx4 v[192:195], v244, s[58:59] sc1\n\t"
      "v_mfma_f32_16x16x32_bf16 v[208:211], v[136:139], v[28:31], v[208:211]\n\t"
      "v_add_f32_e32 v229, v229, v114\n\t"
      "v_mfma_f32_16x16x32_bf16 v[208:211], v[140:143], v[44:47], v[208:211]\n\t"
      "v_add_f32_e32 v228, v228, v236\n\t"
      "v_add_f32_e32 v229, v229, v237\n\t"
      "v_mfma_f32_16x16x32_bf16 v[208:211], v[144:147], v[60:63], v[208:211]\n\t"
      "v_cvt_pk_bf16_f32 v232, v228, v229\n\t"
      "v_mfma_f32_16x16x32_bf16 v[208:211], v[148:151], v[76:79], v[208:211]\n\t"
      "s_mov_b32 vcc_lo, 0xbbbbbbbb\n\t"
      "s_mov_b32 vcc_hi, 0xbbbbbbbb\n\t"
      "v_cndmask_b32_dpp v116, v232, v116, vcc quad_perm:[0,0,0,0] row_mask:0xf bank_mask:0xf\n\t"
      "v_cndmask_b32_dpp v117, v232, v117, vcc quad_perm:[1,1,1,1] row_mask:0xf bank_mask:0xf\n\t"
      "v_cndmask_b32_dpp v118, v232, v118, vcc quad_perm:[2,2,2,2] row_mask:0xf bank_mask:0xf\n\t"
      "v_cndmask_b32_dpp v119, v232, v119, vcc quad_perm:[3,3,3,3] row_mask:0xf bank_mask:0xf\n\t"
      "s_add_u32 s58, s74, s42\n\t"
      "s_addc_u32 s59, s75, 0\n\t"
      "global_load_dwordx4 v[196:199], v244, s[58:59] sc1\n\t"
      "v_mfma_f32_16x16x32_bf16 v[208:211], v[152:155], v[92:95], v[208:211]\n\t"
      "ds_write_b128 v245, v[224:227] offset:34816\n\t"
      "v_mfma_f32_16x16x32_bf16 v[208:211], v[156:159], v[108:111], v[208:211]\n\t"
      "s_waitcnt vmcnt(10)\n\t"
      "v_mfma_f32_16x16x32_bf16 v[212:215], v[160:163], v[0:3], 0\n\t"
      "s_waitcnt vmcnt(9)\n\t"
      "v_mfma_f32_16x16x32_bf16 v[212:215], v[164:167], v[16:19], v[212:215]\n\t"
      "s_add_u32 s58, s74, s43\n\t"
      "s_addc_u32 s59, s75, 0\n\t"
      "global_load_dwordx4 v[200:203], v244, s[58:59] sc1\n\t"
      "s_nop 0\n\t"
      "ds_write_b128 v245, v[208:211] offset:35840\n\t"
      "s_waitcnt vmcnt(9)\n\t"
      "v_mfma_f32_16x16x32_bf16 v[212:215], v[168:171], v[32:35], v[212:215]\n\t"
      "s_waitcnt vmcnt(8)\n\t"
      "v_mfma_f32_16x16x32_bf16 v[212:215], v[172:175], v[48:51], v[212:215]\n\t"
      "s_waitcnt vmcnt(7)\n\t"
      "v_mfma_f32_16x16x32_bf16 v[212:215], v[176:179], v[64:67], v[212:215]\n\t"
      "s_waitcnt lgkmcnt(0)\n\t"
      "s_barrier\n\t"
      "ds_read_b64 v[228:229], v246 offset:32768\n\t"
      "ds_read_b64 v[230:231], v246 offset:36864\n\t"
      "s_waitcnt vmcnt(6)\n\t"
      "v_mfma_f32_16x16x32_bf16 v[212:215], v[180:183], v[80:83], v[212:215]\n\t"
      "s_add_u32 s58, s74, s44\n\t"
      "s_addc_u32 s59, s75, 0\n\t"
      "global_load_dwordx4 v[204:207], v244, s[58:59] sc1\n\t"
      "ds_read_b64 v[232:233], v246 offset:40960\n\t"
      "ds_read_b64 v[234:235], v246 offset:45056\n\t"
      "s_waitcnt vmcnt(5)\n\t"
      "v_mfma_f32_16x16x32_bf16 v[212:215], v[184:187], v[96:99], v[212:215]\n\t"
      "ds_read_b64 v[236:237], v246 offset:49152\n\t"
      "ds_read_b64 v[238:239], v246 offset:53248\n\t"
      "v_mfma_f32_16x16x32_bf16 v[216:219], v[160:163], v[4:7], 0\n\t"
      "ds_read_b64 v[240:241], v246 offset:57344\n\t"
      "ds_read_b64 v[242:243], v246 offset:61440\n\t"
      "v_lshlrev_b32_e32 v113, 16, v112\n\t"
      "v_mfma_f32_16x16x32_bf16 v[216:219], v[164:167], v[20:23], v[216:219]\n\t"
      "v_and_b32_e32 v114, 0xffff0000, v112\n\t"
      "v_mfma_f32_16x16x32_bf16 v[216:219], v[168:171], v[36:39], v[216:219]\n\t"
      "s_add_u32 s58, s74, s45\n\t"
      "s_addc_u32 s59, s75, 0\n\t"
      "global_load_dwordx4 v[128:131], v244, s[58:59] sc1\n\t"
      "v_add_f32_e32 v113, v249, v113\n\t"
      "v_add_f32_e32 v251, v212, v213\n\t"
      "v_add_f32_e32 v251, v251, v214\n\t"
      "v_cmp_u_f32_e32 vcc, v251, v215\n\t"
      "s_cbranch_vccnz LA44R_%=\n\t"
      "LA44B_%=:\n\t"
      "v_mfma_f32_16x16x32_bf16 v[216:219], v[172:175], v[52:55], v[216:219]\n\t"
      "v_add_f32_e32 v114, v249, v114\n\t"
      "s_waitcnt lgkmcnt(4)\n\t"
      "v_mfma_f32_16x16x32_bf16 v[216:219], v[176:179], v[68:71], v[216:219]\n\t"
      "v_add_f32_e32 v228, v228, v230\n\t"
      "v_mfma_f32_16x16x32_bf16 v[216:219], v[180:183], v[84:87], v[216:219]\n\t"
      "v_add_f32_e32 v232, v232, v234\n\t"
      "ds_write_b128 v245, v[212:215]\n\t"
      "global_load_dword v112, v248, s[50:51] sc1\n\t"
      "s_add_u32 s50, s50, 2048\n\t"
      "s_addc_u32 s51, s51, 0\n\t"
      "v_mfma_f32_16x16x32_bf16 v[216:219], v[184:187], v[100:103], v[216:219]\n\t"
      "s_add_u32 s58, s74, s46\n\t"
      "s_addc_u32 s59, s75, 0\n\t"
      "global_load_dwordx4 v[132:135], v244, s[58:59] sc1\n\t"
      "v_add_f32_e32 v229, v229, v231\n\t"
      "v_add_f32_e32 v233, v233, v235\n\t"
      "v_mfma_f32_16x16x32_bf16 v[220:223], v[160:163], v[8:11], 0\n\t"
      "s_waitcnt lgkmcnt(1)\n\t"
      "v_mfma_f32_16x16x32_bf16 v[220:223], v[164:167], v[24:27], v[220:223]\n\t"
      "v_add_f32_e32 v236, v236, v238\n\t"
      "v_mfma_f32_16x16x32_bf16 v[220:223], v[168:171], v[40:43], v[220:223]\n\t"
      "v_add_f32_e32 v240, v240, v242\n\t"
      "v_mfma_f32_16x16x32_bf16 v[220:223], v[172:175], v[56:59], v[220:223]\n\t"
      "v_add_f32_e32 v237, v237, v239\n\t"
      "v_add_f32_e32 v241, v241, v243\n\t"
      "s_add_u32 s58, s76, s40\n\t"
      "s_addc_u32 s59, s77, 0\n\t"
      "global_load_dwordx4 v[136:139], v244, s[58:59] sc1\n\t"
      "v_mfma_f32_16x16x32_bf16 v[220:223], v[176:179], v[72:75], v[220:223]\n\t"
      "v_add_f32_e32 v228, v228, v232\n\t"
      "v_mfma_f32_16x16x32_bf16 v[220:223], v[180:183], v[88:91], v[220:223]\n\t"
      "v_add_f32_e32 v236, v236, v240\n\t"
      "ds_write_b128 v245, v[216:219] offset:1024\n\t"
      "v_mfma_f32_16x16x32_bf16 v[220:223], v[184:187], v[104:107], v[220:223]\n\t"
      "v_add_f32_e32 v229, v229, v233\n\t"
      "v_add_f32_e32 v237, v237, v241\n\t"
      "v_mfma_f32_16x16x32_bf16 v[224:227], v[160:163], v[12:15], 0\n\t"
      "v_add_f32_e32 v228, v228, v113\n\t"
      "s_add_u32 s58, s76, s41\n\t"
      "s_addc_u32 s59, s77, 0\n\t"
      "global_load_dwordx4 v[140:143], v244, s[58:59] sc1\n\t"
      "v_mfma_f32_16x16x32_bf16 v[224:227], v[164:167], v[28:31], v[224:227]\n\t"
      "v_add_f32_e32 v229, v229, v114\n\t"
      "v_mfma_f32_16x16x32_bf16 v[224:227], v[168:171], v[44:47], v[224:227]\n\t"
      "v_add_f32_e32 v228, v228, v236\n\t"
      "v_add_f32_e32 v229, v229, v237\n\t"
      "v_mfma_f32_16x16x32_bf16 v[224:227], v[172:175], v[60:63], v[224:227]\n\t"
      "v_cvt_pk_bf16_f32 v232, v228, v229\n\t"
      "v_mfma_f32_16x16x32_bf16 v[224:227], v[176:179], v[76:79], v[224:227]\n\t"
      "s_mov_b32 vcc_lo, 0x77777777\n\t"
      "s_mov_b32 vcc_hi, 0x77777777\n\t"
      "v_cndmask_b32_dpp v116, v232, v116, vcc quad_perm:[0,0,0,0] row_mask:0xf bank_mask:0xf\n\t"
      "v_cndmask_b32_dpp v117, v232, v117, vcc quad_perm:[1,1,1,1] row_mask:0xf bank_mask:0xf\n\t"
      "v_cndmask_b32_dpp v118, v232, v118, vcc quad_perm:[2,2,2,2] row_mask:0xf bank_mask:0xf\n\t"
      "v_cndmask_b32_dpp v119, v232, v119, vcc quad_perm:[3,3,3,3] row_mask:0xf bank_mask:0xf\n\t"
      "s_add_u32 s58, s76, s42\n\t"
      "s_addc_u32 s59, s77, 0\n\t"
      "global_load_dwordx4 v[144:147], v244, s[58:59] sc1\n\t"
      "v_mfma_f32_16x16x32_bf16 v[224:227], v[180:183], v[92:95], v[224:227]\n\t"
      "global_store_dwordx4 v247, v[116:119], s[62:63] sc1\n\t"
      "ds_write_b128 v245, v[220:223] offset:2048\n\t"
      "v_mfma_f32_16x16x32_bf16 v[224:227], v[184:187], v[108:111], v[224:227]\n\t"
      "s_waitcnt vmcnt(11)\n\t"
      "v_mfma_f32_16x16x32_bf16 v[208:211], v[188:191], v[0:3], 0\n\t"
      "s_waitcnt vmcnt(10)\n\t"
      "v_mfma_f32_16x16x32_bf16 v[208:211], v[192:195], v[16:19], v[208:211]\n\t"
      "s_add_u32 s58, s76, s43\n\t"
      "s_addc_u32 s59, s77, 0\n\t"
      "global_load_dwordx4 v[148:151], v244, s[58:59] sc1\n\t"
      "s_nop 0\n\t"
      "ds_write_b128 v245, v[224:227] offset:3072\n\t"
      "s_waitcnt vmcnt(10)\n\t"
      "v_mfma_f32_16x16x32_bf16 v[208:211], v[196:199], v[32:35], v[208:211]\n\t"
      "s_waitcnt vmcnt(9)\n\t"
      "v_mfma_f32_16x16x32_bf16 v[208:211], v[200:203], v[48:51], v[208:211]\n\t"
      "s_waitcnt vmcnt(8)\n\t"
      "v_mfma_f32_16x16x32_bf16 v[208:211], v[204:207], v[64:67], v[208:211]\n\t"
      "s_waitcnt lgkmcnt(0)\n\t"
      "s_barrier\n\t"
      "ds_read_b64 v[228:229], v246\n\t"
      "ds_read_b64 v[230:231], v246 offset:4096\n\t"
      "s_waitcnt vmcnt(7)\n\t"
      "v_mfma_f32_16x16x32_bf16 v[208:211], v[128:131], v[80:83], v[208:211]\n\t"
      "s_add_u32 s58, s76, s44\n\t"
      "s_addc_u32 s59, s77, 0\n\t"
      "global_load_dwordx4 v[152:155], v244, s[58:59] sc1\n\t"
      "ds_read_b64 v[232:233], v246 offset:8192\n\t"
      "ds_read_b64 v[234:235], v246 offset:12288\n\t"
      "s_waitcnt vmcnt(6)\n\t"
      "v_mfma_f32_16x16x32_bf16 v[208:211], v[132:135], v[96:99], v[208:211]\n\t"
      "ds_read_b64 v[236:237], v246 offset:16384\n\t"
      "ds_read_b64 v[238:239], v246 offset:20480\n\t"
      "v_mfma_f32_16x16x32_bf16 v[212:215], v[188:191], v[4:7], 0\n\t"
      "ds_read_b64 v[240:241], v246 offset:24576\n\t"
      "ds_read_b64 v[242:243], v246 offset:28672\n\t"
      "v_lshlrev_b32_e32 v113, 16, v112\n\t"
      "v_mfma_f32_16x16x32_bf16 v[212:215], v[192:195], v[20:23], v[212:215]\n\t"
      "v_and_b32_e32 v114, 0xffff0000, v112\n\t"
      "v_mfma_f32_16x16x32_bf16 v[212:215], v[196:199], v[36:39], v[212:215]\n\t"
      "s_add_u32 s58, s76, s45\n\t"
      "s_addc_u32 s59, s77, 0\n\t"
      "global_load_dwordx4 v[156:159], v244, s[58:59] sc1\n\t"
      "v_add_f32_e32 v113, v249, v113\n\t"
      "v_add_f32_e32 v251, v208, v209\n\t"
      "v_add_f32_e32 v251, v251, v210\n\t"
      "v_cmp_u_f32_e32 vcc, v251, v211\n\t"
      "s_cbranch_vccnz LA45R_%=\n\t"
      "LA45B_%=:\n\t"
      "v_mfma_f32_16x16x32_bf16 v[212:215], v[200:203], v[52:55], v[212:215]\n\t"
      "v_add_f32_e32 v114, v249, v114\n\t"
      "s_waitcnt lgkmcnt(4)\n\t"
      "v_mfma_f32_16x16x32_bf16 v[212:215], v[204:207], v[68:71], v[212:215]\n\t"
      "v_add_f32_e32 v228, v228, v230\n\t"
      "v_mfma_f32_16x16x32_bf16 v[212:215], v[128:131], v[84:87], v[212:215]\n\t"
      "v_add_f32_e32 v232, v232, v234\n\t"
      "ds_write_b128 v245, v[208:211] offset:32768\n\t"
      "global_load_dword v112, v248, s[50:51] sc1\n\t"
      "s_add_u32 s50, s50, 2048\n\t"
      "s_addc_u32 s51, s51, 0\n\t"
      "v_mfma_f32_16x16x32_bf16 v[212:215], v[132:135], v[100:103], v[212:215]\n\t"
      "s_add_u32 s58, s76, s46\n\t"
      "s_addc_u32 s59, s77, 0\n\t"
      "global_load_dwordx4 v[160:163], v244, s[58:59] sc1\n\t"
      "v_add_f32_e32 v229, v229, v231\n\t"
      "v_add_f32_e32 v233, v233, v235\n\t"
      "v_mfma_f32_16x16x32_bf16 v[216:219], v[188:191], v[8:11], 0\n\t"
      "s_waitcnt lgkmcnt(1)\n\t"
      "v_mfma_f32_16x16x32_bf16 v[216:219], v[192:195], v[24:27], v[216:219]\n\t"
      "v_add_f32_e32 v236, v236, v238\n\t"
      "v_mfma_f32_16x16x32_bf16 v[216:219], v[196:199], v[40:43], v[216:219]\n\t"
      "v_add_f32_e32 v240, v240, v242\n\t"
      "v_mfma_f32_16x16x32_bf16 v[216:219], v[200:203], v[56:59], v[216:219]\n\t"
      "v_add_f32_e32 v237, v237, v239\n\t"
      "v_add_f32_e32 v241, v241, v243\n\t"
      "v_mfma_f32_16x16x32_bf16 v[216:219], v[204:207], v[72:75], v[216:219]\n\t"
      "v_add_f32_e32 v228, v228, v232\n\t"
      "v_mfma_f32_16x16x32_bf16 v[216:219], v[128:131], v[88:91], v[216:219]\n\t"
      "v_add_f32_e32 v236, v236, v240\n\t"
      "ds_write_b128 v245, v[212:215] offset:33792\n\t"
      "v_mfma_f32_16x16x32_bf16 v[216:219], v[132:135], v[104:107], v[216:219]\n\t"
      "v_add_f32_e32 v229, v229, v233\n\t"
      "v_add_f32_e32 v237, v237, v241\n\t"
      "v_mfma_f32_16x16x32_bf16 v[220:223], v[188:191], v[12:15], 0\n\t"
      "v_add_f32_e32 v228, v228, v113\n\t"
      "v_mfma_f32_16x16x32_bf16 v[220:223], v[192:195], v[28:31], v[220:223]\n\t"
      "v_add_f32_e32 v229, v229, v114\n\t"
      "v_mfma_f32_16x16x32_bf16 v[220:223], v[196:199], v[44:47], v[220:223]\n\t"
      "v_add_f32_e32 v228, v228, v236\n\t"
      "v_add_f32_e32 v229, v229, v237\n\t"
      "v_mfma_f32_16x16x32_bf16 v[220:223], v[200:203], v[60:63], v[220:223]\n\t"
      "v_cvt_pk_bf16_f32 v232, v228, v229\n\t"
      "v_mfma_f32_16x16x32_bf16 v[220:223], v[204:207], v[76:79], v[220:223]\n\t"
      "s_mov_b32 vcc_lo, 0xeeeeeeee\n\t"
      "s_mov_b32 vcc_hi, 0xeeeeeeee\n\t"
      "v_cndmask_b32_dpp v116, v232, v116, vcc quad_perm:[0,0,0,0] row_mask:0xf bank_mask:0xf\n\t"
      "v_cndmask_b32_dpp v117, v232, v117, vcc quad_perm:[1,1,1,1] row_mask:0xf bank_mask:0xf\n\t"
      "v_cndmask_b32_dpp v118, v232, v118, vcc quad_perm:[2,2,2,2] row_mask:0xf bank_mask:0xf\n\t"
      "v_cndmask_b32_dpp v119, v232, v119, vcc quad_perm:[3,3,3,3] row_mask:0xf bank_mask:0xf\n\t"
      "v_mfma_f32_16x16x32_bf16 v[220:223], v[128:131], v[92:95], v[220:223]\n\t"
      "ds_write_b128 v245, v[216:219] offset:34816\n\t"
      "v_mfma_f32_16x16x32_bf16 v[220:223], v[132:135], v[108:111], v[220:223]\n\t"
      "s_waitcnt vmcnt(8)\n\t"
      "v_mfma_f32_16x16x32_bf16 v[224:227], v[136:139], v[0:3], 0\n\t"
      "s_waitcnt vmcnt(7)\n\t"
      "v_mfma_f32_16x16x32_bf16 v[224:227], v[140:143], v[16:19], v[224:227]\n\t"
      "s_nop 3\n\t"
      "ds_write_b128 v245, v[220:223] offset:35840\n\t"
      "s_waitcnt vmcnt(6)\n\t"
      "v_mfma_f32_16x16x32_bf16 v[224:227], v[144:147], v[32:35], v[224:227]\n\t"
      "s_waitcnt vmcnt(4)\n\t"
      "v_mfma_f32_16x16x32_bf16 v[224:227], v[148:151], v[48:51], v[224:227]\n\t"
      "s_waitcnt vmcnt(3)\n\t"
      "v_mfma_f32_16x16x32_bf16 v[224:227], v[152:155], v[64:67], v[224:227]\n\t"
      "s_waitcnt lgkmcnt(0)\n\t"
      "s_barrier\n\t"
      "ds_read_b64 v[228:229], v246 offset:32768\n\t"
      "ds_read_b64 v[230:231], v246 offset:36864\n\t"
      "s_waitcnt vmcnt(2)\n\t"
      "v_mfma_f32_16x16x32_bf16 v[224:227], v[156:159], v[80:83], v[224:227]\n\t"
      "ds_read_b64 v[232:233], v246 offset:40960\n\t"
      "ds_read_b64 v[234:235], v246 offset:45056\n\t"
      "s_waitcnt vmcnt(0)\n\t"
      "v_mfma_f32_16x16x32_bf16 v[224:227], v[160:163], v[96:99], v[224:227]\n\t"
      "ds_read_b64 v[236:237], v246 offset:49152\n\t"
      "ds_read_b64 v[238:239], v246 offset:53248\n\t"
      "v_mfma_f32_16x16x32_bf16 v[208:211], v[136:139], v[4:7], 0\n\t"
      "ds_read_b64 v[240:241], v246 offset:57344\n\t"
      "ds_read_b64 v[242:243], v246 offset:61440\n\t"
      "v_lshlrev_b32_e32 v113, 16, v112\n\t"
      "v_and_b32_e32 v114, 0xffff0000, v112\n\t"
      "v_mfma_f32_16x16x32_bf16 v[208:211], v[140:143], v[20:23], v[208:211]\n\t"
      "v_add_f32_e32 v113, v249, v113\n\t"
      "v_add_f32_e32 v114, v249, v114\n\t"
      "v_mfma_f32_16x16x32_bf16 v[208:211], v[144:147], v[36:39], v[208:211]\n\t"
      "s_waitcnt lgkmcnt(4)\n\t"
      "v_add_f32_e32 v228, v228, v230\n\t"
      "v_add_f32_e32 v232, v232, v234\n\t"
      "v_add_f32_e32 v251, v224, v225\n\t"
      "v_add_f32_e32 v251, v251, v226\n\t"
      "v_cmp_u_f32_e32 vcc, v251, v227\n\t"
      "s_cbranch_vccnz LA46R_%=\n\t"
      "LA46B_%=:\n\t"
      "v_mfma_f32_16x16x32_bf16 v[208:211], v[148:151], v[52:55], v[208:211]\n\t"
      "v_add_f32_e32 v229, v229, v231\n\t"
      "v_add_f32_e32 v233, v233, v235\n\t"
      "v_mfma_f32_16x16x32_bf16 v[208:211], v[152:155], v[68:71], v[208:211]\n\t"
      "s_waitcnt lgkmcnt(0)\n\t"
      "v_add_f32_e32 v236, v236, v238\n\t"
      "v_add_f32_e32 v240, v240, v242\n\t"
      "v_mfma_f32_16x16x32_bf16 v[208:211], v[156:159], v[84:87], v[208:211]\n\t"
      "v_add_f32_e32 v237, v237, v239\n\t"
      "v_add_f32_e32 v241, v241, v243\n\t"
      "ds_write_b128 v245, v[224:227]\n\t"
      "global_load_dword v112, v248, s[50:51] sc1\n\t"
      "s_add_u32 s50, s50, 2048\n\t"
      "s_addc_u32 s51, s51, 0\n\t"
      "v_mfma_f32_16x16x32_bf16 v[208:211], v[160:163], v[100:103], v[208:211]\n\t"
      "v_add_f32_e32 v228, v228, v232\n\t"
      "v_add_f32_e32 v236, v236, v240\n\t"
      "v_add_f32_e32 v229, v229, v233\n\t"
      "v_mfma_f32_16x16x32_bf16 v[212:215], v[136:139], v[8:11], 0\n\t"
      "v_add_f32_e32 v237, v237, v241\n\t"
      "v_add_f32_e32 v228, v228, v113\n\t"
      "v_mfma_f32_16x16x32_bf16 v[212:215], v[140:143], v[24:27], v[212:215]\n\t"
      "v_add_f32_e32 v229, v229, v114\n\t"
      "v_add_f32_e32 v228, v228, v236\n\t"
      "v_add_f32_e32 v229, v229, v237\n\t"
      "v_mfma_f32_16x16x32_bf16 v[212:215], v[144:147], v[40:43], v[212:215]\n\t"
      "v_cvt_pk_bf16_f32 v232, v228, v229\n\t"
      "s_mov_b32 vcc_lo, 0xdddddddd\n\t"
      "s_mov_b32 vcc_hi, 0xdddddddd\n\t"
      "v_cndmask_b32_dpp v116, v232, v116, vcc quad_perm:[0,0,0,0] row_mask:0xf bank_mask:0xf\n\t"
      "v_cndmask_b32_dpp v117, v232, v117, vcc quad_perm:[1,1,1,1] row_mask:0xf bank_mask:0xf\n\t"
      "v_cndmask_b32_dpp v118, v232, v118, vcc quad_perm:[2,2,2,2] row_mask:0xf bank_mask:0xf\n\t"
      "v_cndmask_b32_dpp v119, v232, v119, vcc quad_perm:[3,3,3,3] row_mask:0xf bank_mask:0xf\n\t"
      "v_mfma_f32_16x16x32_bf16 v[212:215], v[148:151], v[56:59], v[212:215]\n\t"
      "v_mfma_f32_16x16x32_bf16 v[212:215], v[152:155], v[72:75], v[212:215]\n\t"
      "v_mfma_f32_16x16x32_bf16 v[212:215], v[156:159], v[88:91], v[212:215]\n\t"
      "ds_write_b128 v245, v[208:211] offset:1024\n\t"
      "v_mfma_f32_16x16x32_bf16 v[212:215], v[160:163], v[104:107], v[212:215]\n\t"
      "v_mfma_f32_16x16x32_bf16 v[216:219], v[136:139], v[12:15], 0\n\t"
      "v_mfma_f32_16x16x32_bf16 v[216:219], v[140:143], v[28:31], v[216:219]\n\t"
      "v_mfma_f32_16x16x32_bf16 v[216:219], v[144:147], v[44:47], v[216:219]\n\t"
      "v_mfma_f32_16x16x32_bf16 v[216:219], v[148:151], v[60:63], v[216:219]\n\t"
      "v_mfma_f32_16x16x32_bf16 v[216:219], v[152:155], v[76:79], v[216:219]\n\t"
      "v_mfma_f32_16x16x32_bf16 v[216:219], v[156:159], v[92:95], v[216:219]\n\t"
      "s_nop 1\n\t"
      "ds_write_b128 v245, v[212:215] offset:2048\n\t"
      "v_mfma_f32_16x16x32_bf16 v[216:219], v[160:163], v[108:111], v[216:219]\n\t"
      "s_nop 7\n\t"
      "ds_write_b128 v245, v[216:219] offset:3072\n\t"
      "s_waitcnt lgkmcnt(0)\n\t"
      "s_barrier\n\t"
      "ds_read_b64 v[228:229], v246\n\t"
      "ds_read_b64 v[230:231], v246 offset:4096\n\t"
      "ds_read_b64 v[232:233], v246 offset:8192\n\t"
      "ds_read_b64 v[234:235], v246 offset:12288\n\t"
      "ds_read_b64 v[236:237], v246 offset:16384\n\t"
      "ds_read_b64 v[238:239], v246 offset:20480\n\t"
      "ds_read_b64 v[240:241], v246 offset:24576\n\t"
      "ds_read_b64 v[242:243], v246 offset:28672\n\t"
      "s_waitcnt vmcnt(0)\n\t"
      "v_lshlrev_b32_e32 v113, 16, v112\n\t"
      "v_and_b32_e32 v114, 0xffff0000, v112\n\t"
      "v_add_f32_e32 v113, v249, v113\n\t"
      "v_add_f32_e32 v114, v249, v114\n\t"
      "s_waitcnt lgkmcnt(4)\n\t"
      "v_add_f32_e32 v228, v228, v230\n\t"
      "v_add_f32_e32 v232, v232, v234\n\t"
      "v_add_f32_e32 v229, v229, v231\n\t"
      "v_add_f32_e32 v233, v233, v235\n\t"
      "s_waitcnt lgkmcnt(0)\n\t"
      "v_add_f32_e32 v236, v236, v238\n\t"
      "v_add_f32_e32 v240, v240, v242\n\t"
      "v_add_f32_e32 v237, v237, v239\n\t"
      "v_add_f32_e32 v241, v241, v243\n\t"
      "v_add_f32_e32 v228, v228, v232\n\t"
      "v_add_f32_e32 v236, v236, v240\n\t"
      "v_add_f32_e32 v229, v229, v233\n\t"
      "v_add_f32_e32 v237, v237, v241\n\t"
      "v_add_f32_e32 v228, v228, v113\n\t"
      "v_add_f32_e32 v229, v229, v114\n\t"
      "v_add_f32_e32 v228, v228, v236\n\t"
      "v_add_f32_e32 v229, v229, v237\n\t"
      "v_cvt_pk_bf16_f32 v232, v228, v229\n\t"
      "s_mov_b32 vcc_lo, 0xbbbbbbbb\n\t"
      "s_mov_b32 vcc_hi, 0xbbbbbbbb\n\t"
      "v_cndmask_b32_dpp v116, v232, v116, vcc quad_perm:[0,0,0,0] row_mask:0xf bank_mask:0xf\n\t"
      "v_cndmask_b32_dpp v117, v232, v117, vcc quad_perm:[1,1,1,1] row_mask:0xf bank_mask:0xf\n\t"
      "v_cndmask_b32_dpp v118, v232, v118, vcc quad_perm:[2,2,2,2] row_mask:0xf bank_mask:0xf\n\t"
      "v_cndmask_b32_dpp v119, v232, v119, vcc quad_perm:[3,3,3,3] row_mask:0xf bank_mask:0xf\n\t"
      "s_mov_b64 exec, s[82:83]\n\t"
      "global_store_dwordx4 v247, v[116:119], s[62:63] offset:128 sc1\n\t"
      "s_mov_b64 exec, -1\n\t"
      "s_endpgm\n\t"
      "LO0R_%=:\n\t"
      "s_mov_b32 s80, 0\n\t"
      "LO0L_%=:\n\t"
      "s_add_u32 s80, s80, 1\n\t"
      "s_cmp_lt_u32 s80, 262144\n\t"
      "s_cbranch_scc0 LO0X_%=\n\t"
      "s_waitcnt vmcnt(0)\n\t"
      "s_sleep 1\n\t"
      "global_load_dwordx4 v[128:131], v244, s[42:43] offset:-4096 sc1\n\t"
      "global_load_dwordx4 v[132:135], v244, s[42:43] offset:-3072 sc1\n\t"
      "global_load_dwordx4 v[136:139], v244, s[42:43] offset:-2048 sc1\n\t"
      "global_load_dwordx4 v[140:143], v244, s[42:43] offset:-1024 sc1\n\t"
      "global_load_dwordx4 v[144:147], v244, s[42:43] sc1\n\t"
      "global_load_dwordx4 v[148:151], v244, s[42:43] offset:1024 sc1\n\t"
      "global_load_dwordx4 v[152:155], v244, s[42:43] offset:2048 sc1\n\t"
      "global_load_dwordx4 v[156:159], v244, s[42:43] offset:3072 sc1\n\t"
      "global_load_dwordx4 v[160:163], v244, s[44:45] offset:-4096 sc1\n\t"
      "global_load_dwordx4 v[164:167], v244, s[44:45] offset:-3072 sc1\n\t"
      "global_load_dwordx4 v[168:171], v244, s[44:45] offset:-2048 sc1\n\t"
      "global_load_dwordx4 v[172:175], v244, s[44:45] offset:-1024 sc1\n\t"
      "global_load_dwordx4 v[176:179], v244, s[44:45] sc1\n\t"
      "global_load_dwordx4 v[180:183], v244, s[44:45] offset:1024 sc1\n\t"
      "global_load_dwordx4 v[184:187], v244, s[44:45] offset:2048 sc1\n\t"
      "global_load_dwordx4 v[188:191], v244, s[44:45] offset:3072 sc1\n\t"
      "s_waitcnt vmcnt(0)\n\t"
      "v_max3_u32 v232, v128, v129, v130\n\t"
      "v_max3_u32 v232, v232, v131, v132\n\t"
      "v_max3_u32 v232, v232, v133, v134\n\t"
      "v_max_u32_e32 v232, v232, v135\n\t"
      "v_cmp_eq_u32_e32 vcc, -1, v232\n\t"
      "s_cbranch_vccnz LO0L_%=\n\t"
      "LO0X_%=:\n\t"
      "s_branch LO0B_%=\n\t"
      "LO2R_%=:\n\t"
      "s_mov_b32 s80, 0\n\t"
      "LO2L_%=:\n\t"
      "s_add_u32 s80, s80, 1\n\t"
      "s_cmp_lt_u32 s80, 262144\n\t"
      "s_cbranch_scc0 LO2X_%=\n\t"
      "s_waitcnt vmcnt(0)\n\t"
      "s_sleep 1\n\t"
      "global_load_dwordx4 v[136:139], v244, s[42:43] offset:-2048 sc1\n\t"
      "global_load_dwordx4 v[140:143], v244, s[42:43] offset:-1024 sc1\n\t"
      "global_load_dwordx4 v[144:147], v244, s[42:43] sc1\n\t"
      "global_load_dwordx4 v[148:151], v244, s[42:43] offset:1024 sc1\n\t"
      "global_load_dwordx4 v[152:155], v244, s[42:43] offset:2048 sc1\n\t"
      "global_load_dwordx4 v[156:159], v244, s[42:43] offset:3072 sc1\n\t"
      "global_load_dwordx4 v[160:163], v244, s[44:45] offset:-4096 sc1\n\t"
      "global_load_dwordx4 v[164:167], v244, s[44:45] offset:-3072 sc1\n\t"
      "global_load_dwordx4 v[168:171], v244, s[44:45] offset:-2048 sc1\n\t"
      "global_load_dwordx4 v[172:175], v244, s[44:45] offset:-1024 sc1\n\t"
      "global_load_dwordx4 v[176:179], v244, s[44:45] sc1\n\t"
      "global_load_dwordx4 v[180:183], v244, s[44:45] offset:1024 sc1\n\t"
      "global_load_dwordx4 v[184:187], v244, s[44:45] offset:2048 sc1\n\t"
      "global_load_dwordx4 v[188:191], v244, s[44:45] offset:3072 sc1\n\t"
      "s_waitcnt vmcnt(0)\n\t"
      "v_max3_u32 v232, v136, v137, v138\n\t"
      "v_max3_u32 v232, v232, v139, v140\n\t"
      "v_max3_u32 v232, v232, v141, v142\n\t"
      "v_max_u32_e32 v232, v232, v143\n\t"
      "v_cmp_eq_u32_e32 vcc, -1, v232\n\t"
      "s_cbranch_vccnz LO2L_%=\n\t"
      "LO2X_%=:\n\t"
      "s_branch LO2B_%=\n\t"
      "LO4R_%=:\n\t"
      "s_mov_b32 s80, 0\n\t"
      "LO4L_%=:\n\t"
      "s_add_u32 s80, s80, 1\n\t"
      "s_cmp_lt_u32 s80, 262144\n\t"
      "s_cbranch_scc0 LO4X_%=\n\t"
      "s_waitcnt vmcnt(0)\n\t"
      "s_sleep 1\n\t"
      "global_load_dwordx4 v[144:147], v244, s[42:43] sc1\n\t"
      "global_load_dwordx4 v[148:151], v244, s[42:43] offset:1024 sc1\n\t"
      "global_load_dwordx4 v[152:155], v244, s[42:43] offset:2048 sc1\n\t"
      "global_load_dwordx4 v[156:159], v244, s[42:43] offset:3072 sc1\n\t"
      "global_load_dwordx4 v[160:163], v244, s[44:45] offset:-4096 sc1\n\t"
      "global_load_dwordx4 v[164:167], v244, s[44:45] offset:-3072 sc1\n\t"
      "global_load_dwordx4 v[168:171], v244, s[44:45] offset:-2048 sc1\n\t"
      "global_load_dwordx4 v[172:175], v244, s[44:45] offset:-1024 sc1\n\t"
      "global_load_dwordx4 v[176:179], v244, s[44:45] sc1\n\t"
      "global_load_dwordx4 v[180:183], v244, s[44:45] offset:1024 sc1\n\t"
      "global_load_dwordx4 v[184:187], v244, s[44:45] offset:2048 sc1\n\t"
      "global_load_dwordx4 v[188:191], v244, s[44:45] offset:3072 sc1\n\t"
      "s_waitcnt vmcnt(0)\n\t"
      "v_max3_u32 v232, v144, v145, v146\n\t"
      "v_max3_u32 v232, v232, v147, v148\n\t"
      "v_max3_u32 v232, v232, v149, v150\n\t"
      "v_max_u32_e32 v232, v232, v151\n\t"
      "v_cmp_eq_u32_e32 vcc, -1, v232\n\t"
      "s_cbranch_vccnz LO4L_%=\n\t"
      "LO4X_%=:\n\t"
      "s_branch LO4B_%=\n\t"
      "LO6R_%=:\n\t"
      "s_mov_b32 s80, 0\n\t"
      "LO6L_%=:\n\t"
      "s_add_u32 s80, s80, 1\n\t"
      "s_cmp_lt_u32 s80, 262144\n\t"
      "s_cbranch_scc0 LO6X_%=\n\t"
      "s_waitcnt vmcnt(0)\n\t"
      "s_sleep 1\n\t"
      "global_load_dwordx4 v[152:155], v244, s[42:43] offset:2048 sc1\n\t"
      "global_load_dwordx4 v[156:159], v244, s[42:43] offset:3072 sc1\n\t"
      "global_load_dwordx4 v[160:163], v244, s[44:45] offset:-4096 sc1\n\t"
      "global_load_dwordx4 v[164:167], v244, s[44:45] offset:-3072 sc1\n\t"
      "global_load_dwordx4 v[168:171], v244, s[44:45] offset:-2048 sc1\n\t"
      "global_load_dwordx4 v[172:175], v244, s[44:45] offset:-1024 sc1\n\t"
      "global_load_dwordx4 v[176:179], v244, s[44:45] sc1\n\t"
      "global_load_dwordx4 v[180:183], v244, s[44:45] offset:1024 sc1\n\t"
      "global_load_dwordx4 v[184:187], v244, s[44:45] offset:2048 sc1\n\t"
      "global_load_dwordx4 v[188:191], v244, s[44:45] offset:3072 sc1\n\t"
      "s_waitcnt vmcnt(0)\n\t"
      "v_max3_u32 v232, v152, v153, v154\n\t"
      "v_max3_u32 v232, v232, v155, v156\n\t"
      "v_max3_u32 v232, v232, v157, v158\n\t"
      "v_max_u32_e32 v232, v232, v159\n\t"
      "v_cmp_eq_u32_e32 vcc, -1, v232\n\t"
      "s_cbranch_vccnz LO6L_%=\n\t"
      "LO6X_%=:\n\t"
      "s_branch LO6B_%=\n\t"
      "LO8R_%=:\n\t"
      "s_mov_b32 s80, 0\n\t"
      "LO8L_%=:\n\t"
      "s_add_u32 s80, s80, 1\n\t"
      "s_cmp_lt_u32 s80, 262144\n\t"
      "s_cbranch_scc0 LO8X_%=\n\t"
      "s_waitcnt vmcnt(0)\n\t"
      "s_sleep 1\n\t"
      "global_load_dwordx4 v[160:163], v244, s[44:45] offset:-4096 sc1\n\t"
      "global_load_dwordx4 v[164:167], v244, s[44:45] offset:-3072 sc1\n\t"
      "global_load_dwordx4 v[168:171], v244, s[44:45] offset:-2048 sc1\n\t"
      "global_load_dwordx4 v[172:175], v244, s[44:45] offset:-1024 sc1\n\t"
      "global_load_dwordx4 v[176:179], v244, s[44:45] sc1\n\t"
      "global_load_dwordx4 v[180:183], v244, s[44:45] offset:1024 sc1\n\t"
      "global_load_dwordx4 v[184:187], v244, s[44:45] offset:2048 sc1\n\t"
      "global_load_dwordx4 v[188:191], v244, s[44:45] offset:3072 sc1\n\t"
      "s_waitcnt vmcnt(0)\n\t"
      "v_max3_u32 v232, v160, v161, v162\n\t"
      "v_max3_u32 v232, v232, v163, v164\n\t"
      "v_max3_u32 v232, v232, v165, v166\n\t"
      "v_max_u32_e32 v232, v232, v167\n\t"
      "v_cmp_eq_u32_e32 vcc, -1, v232\n\t"
      "s_cbranch_vccnz LO8L_%=\n\t"
      "LO8X_%=:\n\t"
      "s_branch LO8B_%=\n\t"
      "LO10R_%=:\n\t"
      "s_mov_b32 s80, 0\n\t"
      "LO10L_%=:\n\t"
      "s_add_u32 s80, s80, 1\n\t"
      "s_cmp_lt_u32 s80, 262144\n\t"
      "s_cbranch_scc0 LO10X_%=\n\t"
      "s_waitcnt vmcnt(0)\n\t"
      "s_sleep 1\n\t"
      "global_load_dwordx4 v[168:171], v244, s[44:45] offset:-2048 sc1\n\t"
      "global_load_dwordx4 v[172:175], v244, s[44:45] offset:-1024 sc1\n\t"
      "global_load_dwordx4 v[176:179], v244, s[44:45] sc1\n\t"
      "global_load_dwordx4 v[180:183], v244, s[44:45] offset:1024 sc1\n\t"
      "global_load_dwordx4 v[184:187], v244, s[44:45] offset:2048 sc1\n\t"
      "global_load_dwordx4 v[188:191], v244, s[44:45] offset:3072 sc1\n\t"
      "s_waitcnt vmcnt(0)\n\t"
      "v_max3_u32 v232, v168, v169, v170\n\t"
      "v_max3_u32 v232, v232, v171, v172\n\t"
      "v_max3_u32 v232, v232, v173, v174\n\t"
      "v_max_u32_e32 v232, v232, v175\n\t"
      "v_cmp_eq_u32_e32 vcc, -1, v232\n\t"
      "s_cbranch_vccnz LO10L_%=\n\t"
      "LO10X_%=:\n\t"
      "s_branch LO10B_%=\n\t"
      "LO12R_%=:\n\t"
      "s_mov_b32 s80, 0\n\t"
      "LO12L_%=:\n\t"
      "s_add_u32 s80, s80, 1\n\t"
      "s_cmp_lt_u32 s80, 262144\n\t"
      "s_cbranch_scc0 LO12X_%=\n\t"
      "s_waitcnt vmcnt(0)\n\t"
      "s_sleep 1\n\t"
      "global_load_dwordx4 v[176:179], v244, s[44:45] sc1\n\t"
      "global_load_dwordx4 v[180:183], v244, s[44:45] offset:1024 sc1\n\t"
      "global_load_dwordx4 v[184:187], v244, s[44:45] offset:2048 sc1\n\t"
      "global_load_dwordx4 v[188:191], v244, s[44:45] offset:3072 sc1\n\t"
      "s_waitcnt vmcnt(0)\n\t"
      "v_max3_u32 v232, v176, v177, v178\n\t"
      "v_max3_u32 v232, v232, v179, v180\n\t"
      "v_max3_u32 v232, v232, v181, v182\n\t"
      "v_max_u32_e32 v232, v232, v183\n\t"
      "v_cmp_eq_u32_e32 vcc, -1, v232\n\t"
      "s_cbranch_vccnz LO12L_%=\n\t"
      "LO12X_%=:\n\t"
      "s_branch LO12B_%=\n\t"
      "LO14R_%=:\n\t"
      "s_mov_b32 s80, 0\n\t"
      "LO14L_%=:\n\t"
      "s_add_u32 s80, s80, 1\n\t"
      "s_cmp_lt_u32 s80, 262144\n\t"
      "s_cbranch_scc0 LO14X_%=\n\t"
      "s_waitcnt vmcnt(0)\n\t"
      "s_sleep 1\n\t"
      "global_load_dwordx4 v[184:187], v244, s[44:45] offset:2048 sc1\n\t"
      "global_load_dwordx4 v[188:191], v244, s[44:45] offset:3072 sc1\n\t"
      "s_waitcnt vmcnt(0)\n\t"
      "v_max3_u32 v232, v184, v185, v186\n\t"
      "v_max3_u32 v232, v232, v187, v188\n\t"
      "v_max3_u32 v232, v232, v189, v190\n\t"
      "v_max_u32_e32 v232, v232, v191\n\t"
      "v_cmp_eq_u32_e32 vcc, -1, v232\n\t"
      "s_cbranch_vccnz LO14L_%=\n\t"
      "LO14X_%=:\n\t"
      "s_branch LO14B_%=\n\t"
      "LSR_%=:\n\t"
      "s_mov_b32 s80, 0\n\t"
      "LSL_%=:\n\t"
      "s_add_u32 s80, s80, 1\n\t"
      "s_cmp_lt_u32 s80, 262144\n\t"
      "s_cbranch_scc0 LSX_%=\n\t"
      "s_sleep 1\n\t"
      "global_load_dwordx2 v[148:149], v231, s[46:47] sc1\n\t"
      "global_load_dwordx2 v[150:151], v231, s[46:47] offset:384 sc1\n\t"
      "s_waitcnt vmcnt(0)\n\t"
      "v_max3_u32 v232, v148, v149, v150\n\t"
      "v_max_u32_e32 v232, v232, v151\n\t"
      "v_cmp_eq_u32_e32 vcc, -1, v232\n\t"
      "s_cbranch_vccnz LSL_%=\n\t"
      "LSX_%=:\n\t"
      "s_branch LSB_%=\n\t"
      "LF1R_%=:\n\t"
      "s_mov_b32 s80, 0\n\t"
      "LF1L_%=:\n\t"
      "s_add_u32 s80, s80, 1\n\t"
      "s_cmp_lt_u32 s80, 262144\n\t"
      "s_cbranch_scc0 LF1X_%=\n\t"
      "s_sleep 2\n\t"
      "global_load_dwordx2 v[138:139], v136, s[60:61] sc1\n\t"
      "s_waitcnt vmcnt(0)\n\t"
      "v_cmp_ne_u64_e32 vcc, s[22:23], v[138:139]\n\t"
      "s_cbranch_vccnz LF1L_%=\n\t"
      "LF1X_%=:\n\t"
      "s_branch LF1B_%=\n\t"
      "LF4R_%=:\n\t"
      "s_mov_b32 s80, 0\n\t"
      "LF4L_%=:\n\t"
      "s_add_u32 s80, s80, 1\n\t"
      "s_cmp_lt_u32 s80, 262144\n\t"
      "s_cbranch_scc0 LF4X_%=\n\t"
      "s_sleep 2\n\t"
      "global_load_dwordx2 v[138:139], v137, s[60:61] sc1\n\t"
      "s_waitcnt vmcnt(0)\n\t"
      "v_cmp_ne_u64_e32 vcc, s[22:23], v[138:139]\n\t"
      "s_cbranch_vccnz LF4L_%=\n\t"
      "LF4X_%=:\n\t"
      "s_branch LF4B_%=\n\t"
      "L4_%=:\n\t"
      "s_waitcnt vmcnt(0)\n\t"
      "s_barrier\n\t"
      "s_barrier\n\t"
      "s_lshl2_add_u32 s72, s31, s24\n\t"
      "s_sub_u32 s72, s72, 56\n\t"
      "s_mov_b32 m0, 64512\n\t"
      "s_cmp_ge_u32 s72, 64\n\t"
      "s_cselect_b32 s73, 8, 0\n\t"
      "s_sub_u32 s73, s72, s73\n\t"
      "s_and_b32 s73, s73, 0x38\n\t"
      "s_add_u32 s72, s72, s73\n\t"
      "s_add_u32 s72, s72, 8\n\t"
      "s_mul_i32 s72, s72, 57344\n\t"
      "s_and_b32 s73, s2, 7\n\t"
      "s_mul_i32 s73, s73, 448\n\t"
      "s_and_b32 s73, s73, 0xffffff80\n\t"
      "s_add_u32 s72, s72, s73\n\t"
      "v_lshrrev_b32_e32 v141, 2, v192\n\t"
      "v_mul_u32_u24_e32 v141, 3584, v141\n\t"
      "v_lshl_add_u32 v141, v196, 7, v141\n\t"
      "v_add_u32_e32 v141, s72, v141\n\t"
      "global_load_lds_dword v141, s[10:11]\n\t"
      "s_barrier\n\t"
      "s_cmp_lg_u32 s24, 3\n\t"
      "s_cbranch_scc1 SK12_%=\n\t"
      "global_load_dwordx2 v[138:139], v137, s[60:61] sc1\n\t"
      "SK12_%=:\n\t"
      "s_cmp_lg_u32 s24, 3\n\t"
      "s_cbranch_scc1 LF4B_%=\n\t"
      "s_waitcnt vmcnt(0)\n\t"
      "v_cmp_ne_u64_e32 vcc, s[22:23], v[138:139]\n\t"
      "s_cbranch_vccnz LF4R_%=\n\t"
      "LF4B_%=:\n\t"
      "s_barrier\n\t"
      "s_waitcnt vmcnt(0)\n\t"
      "s_barrier\n\t"
      "s_branch G4_%=\n\t"
      "LF2R_%=:\n\t"
      "s_mov_b32 s80, 0\n\t"
      "LF2L_%=:\n\t"
      "s_add_u32 s80, s80, 1\n\t"
      "s_cmp_lt_u32 s80, 262144\n\t"
      "s_cbranch_scc0 LF2X_%=\n\t"
      "s_sleep 2\n\t"
      "global_load_dwordx2 v[138:139], v137, s[60:61] sc1\n\t"
      "s_waitcnt vmcnt(0)\n\t"
      "v_cmp_ne_u64_e32 vcc, s[22:23], v[138:139]\n\t"
      "s_cbranch_vccnz LF2L_%=\n\t"
      "LF2X_%=:\n\t"
      "s_branch LF2B_%=\n\t"
      "LF3R_%=:\n\t"
      "s_mov_b32 s80, 0\n\t"
      "LF3L_%=:\n\t"
      "s_add_u32 s80, s80, 1\n\t"
      "s_cmp_lt_u32 s80, 262144\n\t"
      "s_cbranch_scc0 LF3X_%=\n\t"
      "s_sleep 2\n\t"
      "global_load_dwordx2 v[138:139], v143, s[60:61] sc1\n\t"
      "s_waitcnt vmcnt(0)\n\t"
      "v_cmp_ne_u64_e32 vcc, s[22:23], v[138:139]\n\t"
      "s_cbranch_vccnz LF3L_%=\n\t"
      "LF3X_%=:\n\t"
      "s_branch LF3B_%=\n\t"
      "LF7R_%=:\n\t"
      "s_mov_b32 s80, 0\n\t"
      "LF7L_%=:\n\t"
      "s_add_u32 s80, s80, 1\n\t"
      "s_cmp_lt_u32 s80, 262144\n\t"
      "s_cbranch_scc0 LF7X_%=\n\t"
      "s_sleep 4\n\t"
      "global_load_dwordx2 v[138:139], v136, s[60:61] sc1\n\t"
      "s_waitcnt vmcnt(0)\n\t"
      "v_cmp_ne_u64_e32 vcc, s[22:23], v[138:139]\n\t"
      "s_cbranch_vccnz LF7L_%=\n\t"
      "LF7X_%=:\n\t"
      "s_branch LF7B_%=\n\t"
      "SPARE_%=:\n\t"
      "v_mov_b32_e32 v132, -1\n\t"
      "v_mov_b32_e32 v133, -1\n\t"
      "v_mov_b32_e32 v134, -1\n\t"
      "v_mov_b32_e32 v135, -1\n\t"
      "s_and_b32 s34, s25, 3\n\t"
      "s_lshl_b32 s34, s34, 15\n\t"
      "s_mul_i32 s35, s24, 917504\n\t"
      "s_add_u32 s34, s34, s35\n\t"
      "s_and_b32 s35, s2, 7\n\t"
      "s_lshl_b32 s35, s35, 9\n\t"
      "s_add_u32 s34, s34, s35\n\t"
      "v_and_b32_e32 v141, 0x1c0, v244\n\t"
      "v_lshlrev_b32_e32 v141, 6, v141\n\t"
      "v_and_b32_e32 v142, 0x30, v244\n\t"
      "v_lshl_add_u32 v141, v142, 3, v141\n\t"
      "v_add_u32_e32 v141, s34, v141\n\t"
      "s_mov_b32 m0, 64512\n\t"
      "s_lshr_b32 s34, s24, 1\n\t"
      "s_lshl_b32 s34, s34, 5\n\t"
      "s_add_u32 s34, s34, s25\n\t"
      "s_and_b32 s35, s24, 1\n\t"
      "s_lshl1_add_u32 s34, s34, s35\n\t"
      "s_lshl_b32 s34, s34, 10\n\t"
      "s_add_u32 s34, s34, 8388608\n\t"
      "s_add_u32 s40, s20, s34\n\t"
      "s_addc_u32 s41, s21, 0\n\t"
      "global_store_dwordx4 v244, v[132:135], s[40:41] sc1\n\t"
      "s_add_u32 s40, s40, 262144\n\t"
      "s_addc_u32 s41, s41, 0\n\t"
      "global_store_dwordx4 v244, v[132:135], s[40:41] sc1\n\t"
      "s_add_u32 s40, s40, 262144\n\t"
      "s_addc_u32 s41, s41, 0\n\t"
      "global_store_dwordx4 v244, v[132:135], s[40:41] sc1\n\t"
      "s_add_u32 s40, s40, 262144\n\t"
      "s_addc_u32 s41, s41, 0\n\t"
      "global_store_dwordx4 v244, v[132:135], s[40:41] sc1\n\t"
      "s_add_u32 s40, s40, 262144\n\t"
      "s_addc_u32 s41, s41, 0\n\t"
      "global_store_dwordx4 v244, v[132:135], s[40:41] sc1\n\t"
      "s_add_u32 s40, s40, 262144\n\t"
      "s_addc_u32 s41, s41, 0\n\t"
      "global_store_dwordx4 v244, v[132:135], s[40:41] sc1\n\t"
      "s_add_u32 s40, s40, 262144\n\t"
      "s_addc_u32 s41, s41, 0\n\t"
      "global_store_dwordx4 v244, v[132:135], s[40:41] sc1\n\t"
      "s_add_u32 s40, s40, 262144\n\t"
      "s_addc_u32 s41, s41, 0\n\t"
      "s_lshl_b32 s34, s25, 7\n\t"
      "s_add_u32 s34, s34, 16384\n\t"
      "v_mov_b32_e32 v131, s34\n\t"
      "s_waitcnt vmcnt(0)\n\t"
      "s_barrier\n\t"
      "s_cmp_lg_u32 s24, 0\n\t"
      "s_cbranch_scc1 SP0_%=\n\t"
      "s_mov_b64 exec, 1\n\t"
      "global_store_dwordx2 v131, v[128:129], s[60:61] sc1\n\t"
      "s_mov_b64 exec, -1\n\t"
      "SP0_%=:\n\t"
      "s_lshl3_add_u32 s34, s25, s24\n\t"
      "s_mul_i32 s34, s34, 7168\n\t"
      "s_add_u32 s34, s34, 10227712\n\t"
      "s_add_u32 s40, s20, s34\n\t"
      "s_addc_u32 s41, s21, 0\n\t"
      "global_store_dwordx4 v244, v[132:135], s[40:41] offset:-4096 sc1\n\t"
      "global_store_dwordx4 v244, v[132:135], s[40:41] offset:-3072 sc1\n\t"
      "global_store_dwordx4 v244, v[132:135], s[40:41] offset:-2048 sc1\n\t"
      "global_store_dwordx4 v244, v[132:135], s[40:41] offset:-1024 sc1\n\t"
      "global_store_dwordx4 v244, v[132:135], s[40:41] sc1\n\t"
      "global_store_dwordx4 v244, v[132:135], s[40:41] offset:1024 sc1\n\t"
      "global_store_dwordx4 v244, v[132:135], s[40:41] offset:2048 sc1\n\t"
      "s_lshl_b32 s34, s25, 7\n\t"
      "s_add_u32 s34, s34, 24576\n\t"
      "v_mov_b32_e32 v131, s34\n\t"
      "s_waitcnt vmcnt(0)\n\t"
      "s_barrier\n\t"
      "s_cmp_lg_u32 s24, 0\n\t"
      "s_cbranch_scc1 SP1_%=\n\t"
      "s_mov_b64 exec, 1\n\t"
      "global_store_dwordx2 v131, v[128:129], s[60:61] sc1\n\t"
      "s_mov_b64 exec, -1\n\t"
      "SP1_%=:\n\t"
      "s_and_b32 s34, s25, 3\n\t"
      "s_lshl3_add_u32 s34, s34, s24\n\t"
      "s_cmp_lg_u32 s34, 27\n\t"
      "s_cbranch_scc1 SP3_%=\n\t"
      "v_and_b32_e32 v136, 0x1f0, v244\n\t"
      "v_lshlrev_b32_e32 v131, 6, v136\n\t"
      "v_lshlrev_b32_e32 v136, 3, v136\n\t"
      "v_add_u32_e32 v136, 24576, v136\n\t"
      "s_and_b32 s34, s2, 7\n\t"
      "s_lshl_b32 s34, s34, 7\n\t"
      "s_add_u32 s34, s34, 32768\n\t"
      "v_add_u32_e32 v131, s34, v131\n\t"
      "s_cmp_lg_u32 s24, 3\n\t"
      "s_cbranch_scc1 SK15_%=\n\t"
      "global_load_dwordx2 v[138:139], v136, s[60:61] sc1\n\t"
      "SK15_%=:\n\t"
      "s_cmp_lg_u32 s24, 3\n\t"
      "s_cbranch_scc1 LF7B_%=\n\t"
      "s_waitcnt vmcnt(0)\n\t"
      "v_cmp_ne_u64_e32 vcc, s[22:23], v[138:139]\n\t"
      "s_cbranch_vccnz LF7R_%=\n\t"
      "LF7B_%=:\n\t"
      "global_store_dwordx2 v131, v[128:129], s[60:61] sc1\n\t"
      "SP3_%=:\n\t"
      "s_barrier\n\t"
      "s_lshr_b32 s35, s2, 6\n\t"
      "s_lshl2_add_u32 s31, s26, s35\n\t"
      "s_and_b32 s35, s2, 7\n\t"
      "s_mul_i32 s34, s35, 14\n\t"
      "s_lshl4_add_u32 s35, s35, -14\n\t"
      "s_cmp_lt_u32 s31, 14\n\t"
      "s_cselect_b32 s34, s34, s35\n\t"
      "s_cselect_b32 s36, 1, 0\n\t"
      "s_or_b32 s27, s27, s36\n\t"
      "s_add_u32 s34, s34, s31\n\t"
      "s_lshr_b32 s29, s34, 2\n\t"
      "s_and_b32 s30, s34, 3\n\t"
      "s_mul_i32 s30, s30, 7\n\t"
      "v_and_b32_e32 v199, 60, v192\n\t"
      "v_and_b32_e32 v200, 24, v192\n\t"
      "v_lshlrev_b32_e32 v202, 3, v196\n\t"
      "v_lshl_or_b32 v202, v200, 2, v202\n\t"
      "v_lshl_or_b32 v202, v195, 7, v202\n\t"
      "v_lshl_or_b32 v202, v193, 10, v202\n\t"
      "v_add_u32_e32 v224, s39, v202\n\t"
      "v_xor_b32_e32 v225, 32, v224\n\t"
      "v_xor_b32_e32 v226, 64, v224\n\t"
      "v_xor_b32_e32 v227, 96, v224\n\t"
      "s_lshl_b32 s34, s24, 12\n\t"
      "s_add_u32 s34, s34, 98304\n\t"
      "v_add_u32_e32 v245, s34, v244\n\t"
      "v_lshlrev_b32_e32 v199, 2, v199\n\t"
      "s_lshl_b32 s34, s24, 8\n\t"
      "v_lshl_add_u32 v247, v192, 2, s34\n\t"
      "v_and_b32_e32 v200, 2, v192\n\t"
      "v_mul_u32_u24_e32 v202, 576, v200\n\t"
      "v_xor_b32_e32 v199, v202, v199\n\t"
      "v_and_b32_e32 v202, 1, v192\n\t"
      "v_lshl_or_b32 v199, v202, 3, v199\n\t"
      "s_and_b32 s35, s34, 0x400\n\t"
      "s_add_u32 s34, s34, s35\n\t"
      "s_add_u32 s34, s34, 98304\n\t"
      "v_add_u32_e32 v246, s34, v199\n\t"
      "v_lshlrev_b32_e32 v199, 2, v202\n\t"
      "v_lshl_or_b32 v199, v200, 4, v199\n\t"
      "s_min_u32 s34, s29, 27\n\t"
      "s_lshl_b32 s34, s34, 7\n\t"
      "s_lshr_b32 s35, s24, 2\n\t"
      "s_lshl_b32 s35, s35, 6\n\t"
      "s_add_u32 s34, s34, s35\n\t"
      "s_lshr_b32 s35, s39, 9\n\t"
      "s_add_u32 s34, s34, s35\n\t"
      "v_add_u32_e32 v248, s34, v199\n\t"
      "s_add_u32 s48, s20, 8388608\n\t"
      "s_addc_u32 s49, s21, 0\n\t"
      "s_cmp_ge_u32 s31, 30\n\t"
      "s_cbranch_scc1 BYE_%=\n\t"
      "s_setprio 0\n\t"
      "s_waitcnt vmcnt(0)\n\t"
      "s_barrier\n\t"
      "s_branch G4_%=\n\t"
      "BYE_%=:\n\t"
      "s_endpgm\n\t"
      "LA30R_%=:\n\t"
      "s_mov_b32 s80, 0\n\t"
      "LA30L_%=:\n\t"
      "s_add_u32 s80, s80, 1\n\t"
      "s_cmp_lt_u32 s80, 262144\n\t"
      "s_cbranch_scc0 LA30X_%=\n\t"
      "s_waitcnt vmcnt(0)\n\t"
      "s_sleep 1\n\t"
      "global_load_dwordx4 v[128:131], v250, s[64:65] offset:-4096 sc1\n\t"
      "global_load_dwordx4 v[132:135], v250, s[64:65] offset:-3072 sc1\n\t"
      "global_load_dwordx4 v[136:139], v250, s[64:65] offset:-2048 sc1\n\t"
      "global_load_dwordx4 v[140:143], v250, s[64:65] offset:-1024 sc1\n\t"
      "global_load_dwordx4 v[144:147], v250, s[64:65] sc1\n\t"
      "global_load_dwordx4 v[148:151], v250, s[64:65] offset:1024 sc1\n\t"
      "global_load_dwordx4 v[152:155], v250, s[64:65] offset:2048 sc1\n\t"
      "global_load_dwordx4 v[156:159], v250, s[64:65] offset:3072 sc1\n\t"
      "s_waitcnt vmcnt(0)\n\t"
      "v_max3_u32 v251, v128, v129, v130\n\t"
      "v_max3_u32 v251, v251, v131, v132\n\t"
      "v_max3_u32 v251, v251, v133, v134\n\t"
      "v_max3_u32 v251, v251, v135, v136\n\t"
      "v_max3_u32 v251, v251, v137, v138\n\t"
      "v_max3_u32 v251, v251, v139, v140\n\t"
      "v_max3_u32 v251, v251, v141, v142\n\t"
      "v_max3_u32 v251, v251, v143, v144\n\t"
      "v_max3_u32 v251, v251, v145, v146\n\t"
      "v_max3_u32 v251, v251, v147, v148\n\t"
      "v_max3_u32 v251, v251, v149, v150\n\t"
      "v_max3_u32 v251, v251, v151, v152\n\t"
      "v_max3_u32 v251, v251, v153, v154\n\t"
      "v_max3_u32 v251, v251, v155, v156\n\t"
      "v_max3_u32 v251, v251, v157, v158\n\t"
      "v_max_u32_e32 v251, v251, v159\n\t"
      "v_cmp_eq_u32_e32 vcc, -1, v251\n\t"
      "s_cbranch_vccnz LA30L_%=\n\t"
      "LA30X_%=:\n\t"
      "s_branch LA30B_%=\n\t"
      "LA31R_%=:\n\t"
      "s_mov_b32 s80, 0\n\t"
      "LA31L_%=:\n\t"
      "s_add_u32 s80, s80, 1\n\t"
      "s_cmp_lt_u32 s80, 262144\n\t"
      "s_cbranch_scc0 LA31X_%=\n\t"
      "s_waitcnt vmcnt(0)\n\t"
      "s_sleep 1\n\t"
      "global_load_dwordx4 v[160:163], v250, s[66:67] offset:-4096 sc1\n\t"
      "global_load_dwordx4 v[164:167], v250, s[66:67] offset:-3072 sc1\n\t"
      "global_load_dwordx4 v[168:171], v250, s[66:67] offset:-2048 sc1\n\t"
      "global_load_dwordx4 v[172:175], v250, s[66:67] offset:-1024 sc1\n\t"
      "global_load_dwordx4 v[176:179], v250, s[66:67] sc1\n\t"
      "global_load_dwordx4 v[180:183], v250, s[66:67] offset:1024 sc1\n\t"
      "global_load_dwordx4 v[184:187], v250, s[66:67] offset:2048 sc1\n\t"
      "global_load_dwordx4 v[188:191], v250, s[66:67] offset:3072 sc1\n\t"
      "s_waitcnt vmcnt(0)\n\t"
      "v_max3_u32 v251, v160, v161, v162\n\t"
      "v_max3_u32 v251, v251, v163, v164\n\t"
      "v_max3_u32 v251, v251, v165, v166\n\t"
      "v_max3_u32 v251, v251, v167, v168\n\t"
      "v_max3_u32 v251, v251, v169, v170\n\t"
      "v_max3_u32 v251, v251, v171, v172\n\t"
      "v_max3_u32 v251, v251, v173, v174\n\t"
      "v_max3_u32 v251, v251, v175, v176\n\t"
      "v_max3_u32 v251, v251, v177, v178\n\t"
      "v_max3_u32 v251, v251, v179, v180\n\t"
      "v_max3_u32 v251, v251, v181, v182\n\t"
      "v_max3_u32 v251, v251, v183, v184\n\t"
      "v_max3_u32 v251, v251, v185, v186\n\t"
      "v_max3_u32 v251, v251, v187, v188\n\t"
      "v_max3_u32 v251, v251, v189, v190\n\t"
      "v_max_u32_e32 v251, v251, v191\n\t"
      "v_cmp_eq_u32_e32 vcc, -1, v251\n\t"
      "s_cbranch_vccnz LA31L_%=\n\t"
      "LA31X_%=:\n\t"
      "v_mfma_f32_16x16x32_bf16 v[224:227], v[0:3], v[160:163], 0\n\t"
      "v_mfma_f32_16x16x32_bf16 v[224:227], v[16:19], v[164:167], v[224:227]\n\t"
      "v_mfma_f32_16x16x32_bf16 v[224:227], v[32:35], v[168:171], v[224:227]\n\t"
      "v_mfma_f32_16x16x32_bf16 v[224:227], v[48:51], v[172:175], v[224:227]\n\t"
      "v_mfma_f32_16x16x32_bf16 v[224:227], v[64:67], v[176:179], v[224:227]\n\t"
      "v_mfma_f32_16x16x32_bf16 v[224:227], v[80:83], v[180:183], v[224:227]\n\t"
      "v_mfma_f32_16x16x32_bf16 v[224:227], v[96:99], v[184:187], v[224:227]\n\t"
      "v_mfma_f32_16x16x32_bf16 v[224:227], v[112:115], v[188:191], v[224:227]\n\t"
      "v_mfma_f32_16x16x32_bf16 v[208:211], v[4:7], v[160:163], 0\n\t"
      "v_mfma_f32_16x16x32_bf16 v[208:211], v[20:23], v[164:167], v[208:211]\n\t"
      "v_mfma_f32_16x16x32_bf16 v[208:211], v[36:39], v[168:171], v[208:211]\n\t"
      "s_nop 7\n\t"
      "s_branch LA31B_%=\n\t"
      "LA32R_%=:\n\t"
      "s_mov_b32 s80, 0\n\t"
      "LA32L_%=:\n\t"
      "s_add_u32 s80, s80, 1\n\t"
      "s_cmp_lt_u32 s80, 262144\n\t"
      "s_cbranch_scc0 LA32X_%=\n\t"
      "s_waitcnt vmcnt(0)\n\t"
      "s_sleep 1\n\t"
      "global_load_dwordx4 v[192:195], v250, s[68:69] offset:-4096 sc1\n\t"
      "global_load_dwordx4 v[196:199], v250, s[68:69] offset:-3072 sc1\n\t"
      "global_load_dwordx4 v[200:203], v250, s[68:69] offset:-2048 sc1\n\t"
      "global_load_dwordx4 v[204:207], v250, s[68:69] offset:-1024 sc1\n\t"
      "global_load_dwordx4 v[128:131], v250, s[68:69] sc1\n\t"
      "global_load_dwordx4 v[132:135], v250, s[68:69] offset:1024 sc1\n\t"
      "global_load_dwordx4 v[136:139], v250, s[68:69] offset:2048 sc1\n\t"
      "global_load_dwordx4 v[140:143], v250, s[68:69] offset:3072 sc1\n\t"
      "s_waitcnt vmcnt(0)\n\t"
      "v_max3_u32 v251, v192, v193, v194\n\t"
      "v_max3_u32 v251, v251, v195, v196\n\t"
      "v_max3_u32 v251, v251, v197, v198\n\t"
      "v_max3_u32 v251, v251, v199, v200\n\t"
      "v_max3_u32 v251, v251, v201, v202\n\t"
      "v_max3_u32 v251, v251, v203, v204\n\t"
      "v_max3_u32 v251, v251, v205, v206\n\t"
      "v_max3_u32 v251, v251, v207, v128\n\t"
      "v_max3_u32 v251, v251, v129, v130\n\t"
      "v_max3_u32 v251, v251, v131, v132\n\t"
      "v_max3_u32 v251, v251, v133, v134\n\t"
      "v_max3_u32 v251, v251, v135, v136\n\t"
      "v_max3_u32 v251, v251, v137, v138\n\t"
      "v_max3_u32 v251, v251, v139, v140\n\t"
      "v_max3_u32 v251, v251, v141, v142\n\t"
      "v_max_u32_e32 v251, v251, v143\n\t"
      "v_cmp_eq_u32_e32 vcc, -1, v251\n\t"
      "s_cbranch_vccnz LA32L_%=\n\t"
      "LA32X_%=:\n\t"
      "v_mfma_f32_16x16x32_bf16 v[220:223], v[0:3], v[192:195], 0\n\t"
      "v_mfma_f32_16x16x32_bf16 v[220:223], v[16:19], v[196:199], v[220:223]\n\t"
      "v_mfma_f32_16x16x32_bf16 v[220:223], v[32:35], v[200:203], v[220:223]\n\t"
      "v_mfma_f32_16x16x32_bf16 v[220:223], v[48:51], v[204:207], v[220:223]\n\t"
      "v_mfma_f32_16x16x32_bf16 v[220:223], v[64:67], v[128:131], v[220:223]\n\t"
      "v_mfma_f32_16x16x32_bf16 v[220:223], v[80:83], v[132:135], v[220:223]\n\t"
      "v_mfma_f32_16x16x32_bf16 v[220:223], v[96:99], v[136:139], v[220:223]\n\t"
      "v_mfma_f32_16x16x32_bf16 v[220:223], v[112:115], v[140:143], v[220:223]\n\t"
      "v_mfma_f32_16x16x32_bf16 v[224:227], v[4:7], v[192:195], 0\n\t"
      "v_mfma_f32_16x16x32_bf16 v[224:227], v[20:23], v[196:199], v[224:227]\n\t"
      "v_mfma_f32_16x16x32_bf16 v[224:227], v[36:39], v[200:203], v[224:227]\n\t"
      "s_nop 7\n\t"
      "s_branch LA32B_%=\n\t"
      "LA33R_%=:\n\t"
      "s_mov_b32 s80, 0\n\t"
      "LA33L_%=:\n\t"
      "s_add_u32 s80, s80, 1\n\t"
      "s_cmp_lt_u32 s80, 262144\n\t"
      "s_cbranch_scc0 LA33X_%=\n\t"
      "s_waitcnt vmcnt(0)\n\t"
      "s_sleep 1\n\t"
      "global_load_dwordx4 v[144:147], v250, s[70:71] offset:-4096 sc1\n\t"
      "global_load_dwordx4 v[148:151], v250, s[70:71] offset:-3072 sc1\n\t"
      "global_load_dwordx4 v[152:155], v250, s[70:71] offset:-2048 sc1\n\t"
      "global_load_dwordx4 v[156:159], v250, s[70:71] offset:-1024 sc1\n\t"
      "global_load_dwordx4 v[160:163], v250, s[70:71] sc1\n\t"
      "global_load_dwordx4 v[164:167], v250, s[70:71] offset:1024 sc1\n\t"
      "global_load_dwordx4 v[168:171], v250, s[70:71] offset:2048 sc1\n\t"
      "global_load_dwordx4 v[172:175], v250, s[70:71] offset:3072 sc1\n\t"
      "s_waitcnt vmcnt(0)\n\t"
      "v_max3_u32 v251, v144, v145, v146\n\t"
      "v_max3_u32 v251, v251, v147, v148\n\t"
      "v_max3_u32 v251, v251, v149, v150\n\t"
      "v_max3_u32 v251, v251, v151, v152\n\t"
      "v_max3_u32 v251, v251, v153, v154\n\t"
      "v_max3_u32 v251, v251, v155, v156\n\t"
      "v_max3_u32 v251, v251, v157, v158\n\t"
      "v_max3_u32 v251, v251, v159, v160\n\t"
      "v_max3_u32 v251, v251, v161, v162\n\t"
      "v_max3_u32 v251, v251, v163, v164\n\t"
      "v_max3_u32 v251, v251, v165, v166\n\t"
      "v_max3_u32 v251, v251, v167, v168\n\t"
      "v_max3_u32 v251, v251, v169, v170\n\t"
      "v_max3_u32 v251, v251, v171, v172\n\t"
      "v_max3_u32 v251, v251, v173, v174\n\t"
      "v_max_u32_e32 v251, v251, v175\n\t"
      "v_cmp_eq_u32_e32 vcc, -1, v251\n\t"
      "s_cbranch_vccnz LA33L_%=\n\t"
      "LA33X_%=:\n\t"
      "v_mfma_f32_16x16x32_bf16 v[216:219], v[0:3], v[144:147], 0\n\t"
      "v_mfma_f32_16x16x32_bf16 v[216:219], v[16:19], v[148:151], v[216:219]\n\t"
      "v_mfma_f32_16x16x32_bf16 v[216:219], v[32:35], v[152:155], v[216:219]\n\t"
      "v_mfma_f32_16x16x32_bf16 v[216:219], v[48:51], v[156:159], v[216:219]\n\t"
      "v_mfma_f32_16x16x32_bf16 v[216:219], v[64:67], v[160:163], v[216:219]\n\t"
      "v_mfma_f32_16x16x32_bf16 v[216:219], v[80:83], v[164:167], v[216:219]\n\t"
      "v_mfma_f32_16x16x32_bf16 v[216:219], v[96:99], v[168:171], v[216:219]\n\t"
      "v_mfma_f32_16x16x32_bf16 v[216:219], v[112:115], v[172:175], v[216:219]\n\t"
      "v_mfma_f32_16x16x32_bf16 v[220:223], v[4:7], v[144:147], 0\n\t"
      "v_mfma_f32_16x16x32_bf16 v[220:223], v[20:23], v[148:151], v[220:223]\n\t"
      "v_mfma_f32_16x16x32_bf16 v[220:223], v[36:39], v[152:155], v[220:223]\n\t"
      "s_nop 7\n\t"
      "s_branch LA33B_%=\n\t"
      "LA34R_%=:\n\t"
      "s_mov_b32 s80, 0\n\t"
      "LA34L_%=:\n\t"
      "s_add_u32 s80, s80, 1\n\t"
      "s_cmp_lt_u32 s80, 262144\n\t"
      "s_cbranch_scc0 LA34X_%=\n\t"
      "s_waitcnt vmcnt(0)\n\t"
      "s_sleep 1\n\t"
      "global_load_dwordx4 v[176:179], v250, s[72:73] offset:-4096 sc1\n\t"
      "global_load_dwordx4 v[180:183], v250, s[72:73] offset:-3072 sc1\n\t"
      "global_load_dwordx4 v[184:187], v250, s[72:73] offset:-2048 sc1\n\t"
      "global_load_dwordx4 v[188:191], v250, s[72:73] offset:-1024 sc1\n\t"
      "global_load_dwordx4 v[192:195], v250, s[72:73] sc1\n\t"
      "global_load_dwordx4 v[196:199], v250, s[72:73] offset:1024 sc1\n\t"
      "global_load_dwordx4 v[200:203], v250, s[72:73] offset:2048 sc1\n\t"
      "global_load_dwordx4 v[204:207], v250, s[72:73] offset:3072 sc1\n\t"
      "s_waitcnt vmcnt(0)\n\t"
      "v_max3_u32 v251, v176, v177, v178\n\t"
      "v_max3_u32 v251, v251, v179, v180\n\t"
      "v_max3_u32 v251, v251, v181, v182\n\t"
      "v_max3_u32 v251, v251, v183, v184\n\t"
      "v_max3_u32 v251, v251, v185, v186\n\t"
      "v_max3_u32 v251, v251, v187, v188\n\t"
      "v_max3_u32 v251, v251, v189, v190\n\t"
      "v_max3_u32 v251, v251, v191, v192\n\t"
      "v_max3_u32 v251, v251, v193, v194\n\t"
      "v_max3_u32 v251, v251, v195, v196\n\t"
      "v_max3_u32 v251, v251, v197, v198\n\t"
      "v_max3_u32 v251, v251, v199, v200\n\t"
      "v_max3_u32 v251, v251, v201, v202\n\t"
      "v_max3_u32 v251, v251, v203, v204\n\t"
      "v_max3_u32 v251, v251, v205, v206\n\t"
      "v_max_u32_e32 v251, v251, v207\n\t"
      "v_cmp_eq_u32_e32 vcc, -1, v251\n\t"
      "s_cbranch_vccnz LA34L_%=\n\t"
      "LA34X_%=:\n\t"
      "v_mfma_f32_16x16x32_bf16 v[212:215], v[0:3], v[176:179], 0\n\t"
      "v_mfma_f32_16x16x32_bf16 v[212:215], v[16:19], v[180:183], v[212:215]\n\t"
      "v_mfma_f32_16x16x32_bf16 v[212:215], v[32:35], v[184:187], v[212:215]\n\t"
      "v_mfma_f32_16x16x32_bf16 v[212:215], v[48:51], v[188:191], v[212:215]\n\t"
      "v_mfma_f32_16x16x32_bf16 v[212:215], v[64:67], v[192:195], v[212:215]\n\t"
      "v_mfma_f32_16x16x32_bf16 v[212:215], v[80:83], v[196:199], v[212:215]\n\t"
      "v_mfma_f32_16x16x32_bf16 v[212:215], v[96:99], v[200:203], v[212:215]\n\t"
      "v_mfma_f32_16x16x32_bf16 v[212:215], v[112:115], v[204:207], v[212:215]\n\t"
      "v_mfma_f32_16x16x32_bf16 v[216:219], v[4:7], v[176:179], 0\n\t"
      "v_mfma_f32_16x16x32_bf16 v[216:219], v[20:23], v[180:183], v[216:219]\n\t"
      "v_mfma_f32_16x16x32_bf16 v[216:219], v[36:39], v[184:187], v[216:219]\n\t"
      "s_nop 7\n\t"
      "s_branch LA34B_%=\n\t"
      "LA35R_%=:\n\t"
      "s_mov_b32 s80, 0\n\t"
      "LA35L_%=:\n\t"
      "s_add_u32 s80, s80, 1\n\t"
      "s_cmp_lt_u32 s80, 262144\n\t"
      "s_cbranch_scc0 LA35X_%=\n\t"
      "s_waitcnt vmcnt(0)\n\t"
      "s_sleep 1\n\t"
      "global_load_dwordx4 v[128:131], v250, s[74:75] offset:-4096 sc1\n\t"
      "global_load_dwordx4 v[132:135], v250, s[74:75] offset:-3072 sc1\n\t"
      "global_load_dwordx4 v[136:139], v250, s[74:75] offset:-2048 sc1\n\t"
      "global_load_dwordx4 v[140:143], v250, s[74:75] offset:-1024 sc1\n\t"
      "global_load_dwordx4 v[144:147], v250, s[74:75] sc1\n\t"
      "global_load_dwordx4 v[148:151], v250, s[74:75] offset:1024 sc1\n\t"
      "global_load_dwordx4 v[152:155], v250, s[74:75] offset:2048 sc1\n\t"
      "global_load_dwordx4 v[156:159], v250, s[74:75] offset:3072 sc1\n\t"
      "s_waitcnt vmcnt(0)\n\t"
      "v_max3_u32 v251, v128, v129, v130\n\t"
      "v_max3_u32 v251, v251, v131, v132\n\t"
      "v_max3_u32 v251, v251, v133, v134\n\t"
      "v_max3_u32 v251, v251, v135, v136\n\t"
      "v_max3_u32 v251, v251, v137, v138\n\t"
      "v_max3_u32 v251, v251, v139, v140\n\t"
      "v_max3_u32 v251, v251, v141, v142\n\t"
      "v_max3_u32 v251, v251, v143, v144\n\t"
      "v_max3_u32 v251, v251, v145, v146\n\t"
      "v_max3_u32 v251, v251, v147, v148\n\t"
      "v_max3_u32 v251, v251, v149, v150\n\t"
      "v_max3_u32 v251, v251, v151, v152\n\t"
      "v_max3_u32 v251, v251, v153, v154\n\t"
      "v_max3_u32 v251, v251, v155, v156\n\t"
      "v_max3_u32 v251, v251, v157, v158\n\t"
      "v_max_u32_e32 v251, v251, v159\n\t"
      "v_cmp_eq_u32_e32 vcc, -1, v251\n\t"
      "s_cbranch_vccnz LA35L_%=\n\t"
      "LA35X_%=:\n\t"
      "v_mfma_f32_16x16x32_bf16 v[208:211], v[0:3], v[128:131], 0\n\t"
      "v_mfma_f32_16x16x32_bf16 v[208:211], v[16:19], v[132:135], v[208:211]\n\t"
      "v_mfma_f32_16x16x32_bf16 v[208:211], v[32:35], v[136:139], v[208:211]\n\t"
      "v_mfma_f32_16x16x32_bf16 v[208:211], v[48:51], v[140:143], v[208:211]\n\t"
      "v_mfma_f32_16x16x32_bf16 v[208:211], v[64:67], v[144:147], v[208:211]\n\t"
      "v_mfma_f32_16x16x32_bf16 v[208:211], v[80:83], v[148:151], v[208:211]\n\t"
      "v_mfma_f32_16x16x32_bf16 v[208:211], v[96:99], v[152:155], v[208:211]\n\t"
      "v_mfma_f32_16x16x32_bf16 v[208:211], v[112:115], v[156:159], v[208:211]\n\t"
      "v_mfma_f32_16x16x32_bf16 v[212:215], v[4:7], v[128:131], 0\n\t"
      "v_mfma_f32_16x16x32_bf16 v[212:215], v[20:23], v[132:135], v[212:215]\n\t"
      "v_mfma_f32_16x16x32_bf16 v[212:215], v[36:39], v[136:139], v[212:215]\n\t"
      "s_nop 7\n\t"
      "s_branch LA35B_%=\n\t"
      "LA36R_%=:\n\t"
      "s_mov_b32 s80, 0\n\t"
      "LA36L_%=:\n\t"
      "s_add_u32 s80, s80, 1\n\t"
      "s_cmp_lt_u32 s80, 262144\n\t"
      "s_cbranch_scc0 LA36X_%=\n\t"
      "s_waitcnt vmcnt(0)\n\t"
      "s_sleep 1\n\t"
      "global_load_dwordx4 v[160:163], v250, s[76:77] offset:-4096 sc1\n\t"
      "global_load_dwordx4 v[164:167], v250, s[76:77] offset:-3072 sc1\n\t"
      "global_load_dwordx4 v[168:171], v250, s[76:77] offset:-2048 sc1\n\t"
      "global_load_dwordx4 v[172:175], v250, s[76:77] offset:-1024 sc1\n\t"
      "global_load_dwordx4 v[176:179], v250, s[76:77] sc1\n\t"
      "global_load_dwordx4 v[180:183], v250, s[76:77] offset:1024 sc1\n\t"
      "global_load_dwordx4 v[184:187], v250, s[76:77] offset:2048 sc1\n\t"
      "global_load_dwordx4 v[188:191], v250, s[76:77] offset:3072 sc1\n\t"
      "s_waitcnt vmcnt(0)\n\t"
      "v_max3_u32 v251, v160, v161, v162\n\t"
      "v_max3_u32 v251, v251, v163, v164\n\t"
      "v_max3_u32 v251, v251, v165, v166\n\t"
      "v_max3_u32 v251, v251, v167, v168\n\t"
      "v_max3_u32 v251, v251, v169, v170\n\t"
      "v_max3_u32 v251, v251, v171, v172\n\t"
      "v_max3_u32 v251, v251, v173, v174\n\t"
      "v_max3_u32 v251, v251, v175, v176\n\t"
      "v_max3_u32 v251, v251, v177, v178\n\t"
      "v_max3_u32 v251, v251, v179, v180\n\t"
      "v_max3_u32 v251, v251, v181, v182\n\t"
      "v_max3_u32 v251, v251, v183, v184\n\t"
      "v_max3_u32 v251, v251, v185, v186\n\t"
      "v_max3_u32 v251, v251, v187, v188\n\t"
      "v_max3_u32 v251, v251, v189, v190\n\t"
      "v_max_u32_e32 v251, v251, v191\n\t"
      "v_cmp_eq_u32_e32 vcc, -1, v251\n\t"
      "s_cbranch_vccnz LA36L_%=\n\t"
      "LA36X_%=:\n\t"
      "v_mfma_f32_16x16x32_bf16 v[224:227], v[0:3], v[160:163], 0\n\t"
      "v_mfma_f32_16x16x32_bf16 v[224:227], v[16:19], v[164:167], v[224:227]\n\t"
      "v_mfma_f32_16x16x32_bf16 v[224:227], v[32:35], v[168:171], v[224:227]\n\t"
      "v_mfma_f32_16x16x32_bf16 v[224:227], v[48:51], v[172:175], v[224:227]\n\t"
      "v_mfma_f32_16x16x32_bf16 v[224:227], v[64:67], v[176:179], v[224:227]\n\t"
      "v_mfma_f32_16x16x32_bf16 v[224:227], v[80:83], v[180:183], v[224:227]\n\t"
      "v_mfma_f32_16x16x32_bf16 v[224:227], v[96:99], v[184:187], v[224:227]\n\t"
      "v_mfma_f32_16x16x32_bf16 v[224:227], v[112:115], v[188:191], v[224:227]\n\t"
      "v_mfma_f32_16x16x32_bf16 v[208:211], v[4:7], v[160:163], 0\n\t"
      "v_mfma_f32_16x16x32_bf16 v[208:211], v[20:23], v[164:167], v[208:211]\n\t"
      "v_mfma_f32_16x16x32_bf16 v[208:211], v[36:39], v[168:171], v[208:211]\n\t"
      "s_nop 7\n\t"
      "s_branch LA36B_%=\n\t"
      "LF5R_%=:\n\t"
      "s_mov_b32 s80, 0\n\t"
      "LF5L_%=:\n\t"
      "s_add_u32 s80, s80, 1\n\t"
      "s_cmp_lt_u32 s80, 262144\n\t"
      "s_cbranch_scc0 LF5X_%=\n\t"
      "s_sleep 2\n\t"
      "global_load_dwordx2 v[138:139], v143, s[60:61] sc1\n\t"
      "s_waitcnt vmcnt(0)\n\t"
      "v_cmp_ne_u64_e32 vcc, s[22:23], v[138:139]\n\t"
      "s_cbranch_vccnz LF5L_%=\n\t"
      "LF5X_%=:\n\t"
      "s_branch LF5B_%=\n\t"
      "PG0R_%=:\n\t"
      "s_mov_b32 s80, 0\n\t"
      "PG0L_%=:\n\t"
      "s_add_u32 s80, s80, 1\n\t"
      "s_cmp_lt_u32 s80, 262144\n\t"
      "s_cbranch_scc0 PG0X_%=\n\t"
      "s_sleep 2\n\t"
      "global_load_dword v251, v253, s[66:67] offset:252 sc1\n\t"
      "s_waitcnt vmcnt(0)\n\t"
      "v_cmp_eq_u32_e32 vcc, -1, v251\n\t"
      "s_cbranch_vccnz PG0L_%=\n\t"
      "PG0X_%=:\n\t"
      "s_branch PG0B_%=\n\t"
      "PG1R_%=:\n\t"
      "s_mov_b32 s80, 0\n\t"
      "PG1L_%=:\n\t"
      "s_add_u32 s80, s80, 1\n\t"
      "s_cmp_lt_u32 s80, 262144\n\t"
      "s_cbranch_scc0 PG1X_%=\n\t"
      "s_sleep 2\n\t"
      "global_load_dword v251, v253, s[66:67] offset:252 sc1\n\t"
      "s_waitcnt vmcnt(0)\n\t"
      "v_cmp_eq_u32_e32 vcc, -1, v251\n\t"
      "s_cbranch_vccnz PG1L_%=\n\t"
      "PG1X_%=:\n\t"
      "s_branch PG1B_%=\n\t"
      "LA40R_%=:\n\t"
      "s_mov_b32 s80, 0\n\t"
      "LA40L_%=:\n\t"
      "s_add_u32 s80, s80, 1\n\t"
      "s_cmp_lt_u32 s80, 262144\n\t"
      "s_cbranch_scc0 LA40X_%=\n\t"
      "s_waitcnt vmcnt(0)\n\t"
      "PQ0R_%=:\n\t"
      "PQ0L_%=:\n\t"
      "s_add_u32 s80, s80, 1\n\t"
      "s_cmp_lt_u32 s80, 262144\n\t"
      "s_cbranch_scc0 PQ0X_%=\n\t"
      "s_sleep 1\n\t"
      "global_load_dword v251, v252, s[64:65] offset:252 sc1\n\t"
      "s_waitcnt vmcnt(0)\n\t"
      "v_cmp_eq_u32_e32 vcc, -1, v251\n\t"
      "s_cbranch_vccnz PQ0L_%=\n\t"
      "PQ0X_%=:\n\t"
      "s_add_u32 s58, s64, s40\n\t"
      "s_addc_u32 s59, s65, 0\n\t"
      "global_load_dwordx4 v[128:131], v244, s[58:59] sc1\n\t"
      "s_add_u32 s58, s64, s41\n\t"
      "s_addc_u32 s59, s65, 0\n\t"
      "global_load_dwordx4 v[132:135], v244, s[58:59] sc1\n\t"
      "s_add_u32 s58, s64, s42\n\t"
      "s_addc_u32 s59, s65, 0\n\t"
      "global_load_dwordx4 v[136:139], v244, s[58:59] sc1\n\t"
      "s_add_u32 s58, s64, s43\n\t"
      "s_addc_u32 s59, s65, 0\n\t"
      "global_load_dwordx4 v[140:143], v244, s[58:59] sc1\n\t"
      "s_add_u32 s58, s64, s44\n\t"
      "s_addc_u32 s59, s65, 0\n\t"
      "global_load_dwordx4 v[144:147], v244, s[58:59] sc1\n\t"
      "s_add_u32 s58, s64, s45\n\t"
      "s_addc_u32 s59, s65, 0\n\t"
      "global_load_dwordx4 v[148:151], v244, s[58:59] sc1\n\t"
      "s_add_u32 s58, s64, s46\n\t"
      "s_addc_u32 s59, s65, 0\n\t"
      "global_load_dwordx4 v[152:155], v244, s[58:59] sc1\n\t"
      "s_waitcnt vmcnt(0)\n\t"
      "v_max3_u32 v251, v128, v129, v130\n\t"
      "v_max3_u32 v251, v251, v131, v132\n\t"
      "v_max3_u32 v251, v251, v133, v134\n\t"
      "v_max3_u32 v251, v251, v135, v136\n\t"
      "v_max3_u32 v251, v251, v137, v138\n\t"
      "v_max3_u32 v251, v251, v139, v140\n\t"
      "v_max3_u32 v251, v251, v141, v142\n\t"
      "v_max3_u32 v251, v251, v143, v144\n\t"
      "v_max3_u32 v251, v251, v145, v146\n\t"
      "v_max3_u32 v251, v251, v147, v148\n\t"
      "v_max3_u32 v251, v251, v149, v150\n\t"
      "v_max3_u32 v251, v251, v151, v152\n\t"
      "v_max3_u32 v251, v251, v153, v154\n\t"
      "v_max_u32_e32 v251, v251, v155\n\t"
      "v_cmp_eq_u32_e32 vcc, -1, v251\n\t"
      "s_cbranch_vccnz LA40L_%=\n\t"
      "LA40X_%=:\n\t"
      "v_mfma_f32_16x16x32_bf16 v[208:211], v[128:131], v[0:3], 0\n\t"
      "v_mfma_f32_16x16x32_bf16 v[208:211], v[132:135], v[16:19], v[208:211]\n\t"
      "v_mfma_f32_16x16x32_bf16 v[208:211], v[136:139], v[32:35], v[208:211]\n\t"
      "v_mfma_f32_16x16x32_bf16 v[208:211], v[140:143], v[48:51], v[208:211]\n\t"
      "v_mfma_f32_16x16x32_bf16 v[208:211], v[144:147], v[64:67], v[208:211]\n\t"
      "v_mfma_f32_16x16x32_bf16 v[208:211], v[148:151], v[80:83], v[208:211]\n\t"
      "v_mfma_f32_16x16x32_bf16 v[208:211], v[152:155], v[96:99], v[208:211]\n\t"
      "v_mfma_f32_16x16x32_bf16 v[212:215], v[128:131], v[4:7], 0\n\t"
      "v_mfma_f32_16x16x32_bf16 v[212:215], v[132:135], v[20:23], v[212:215]\n\t"
      "v_mfma_f32_16x16x32_bf16 v[212:215], v[136:139], v[36:39], v[212:215]\n\t"
      "s_nop 7\n\t"
      "s_branch LA40B_%=\n\t"
      "LA41R_%=:\n\t"
      "s_mov_b32 s80, 0\n\t"
      "LA41L_%=:\n\t"
      "s_add_u32 s80, s80, 1\n\t"
      "s_cmp_lt_u32 s80, 262144\n\t"
      "s_cbranch_scc0 LA41X_%=\n\t"
      "s_waitcnt vmcnt(0)\n\t"
      "PQ1R_%=:\n\t"
      "PQ1L_%=:\n\t"
      "s_add_u32 s80, s80, 1\n\t"
      "s_cmp_lt_u32 s80, 262144\n\t"
      "s_cbranch_scc0 PQ1X_%=\n\t"
      "s_sleep 1\n\t"
      "global_load_dword v251, v252, s[66:67] offset:252 sc1\n\t"
      "s_waitcnt vmcnt(0)\n\t"
      "v_cmp_eq_u32_e32 vcc, -1, v251\n\t"
      "s_cbranch_vccnz PQ1L_%=\n\t"
      "PQ1X_%=:\n\t"
      "s_add_u32 s58, s66, s40\n\t"
      "s_addc_u32 s59, s67, 0\n\t"
      "global_load_dwordx4 v[156:159], v244, s[58:59] sc1\n\t"
      "s_add_u32 s58, s66, s41\n\t"
      "s_addc_u32 s59, s67, 0\n\t"
      "global_load_dwordx4 v[160:163], v244, s[58:59] sc1\n\t"
      "s_add_u32 s58, s66, s42\n\t"
      "s_addc_u32 s59, s67, 0\n\t"
      "global_load_dwordx4 v[164:167], v244, s[58:59] sc1\n\t"
      "s_add_u32 s58, s66, s43\n\t"
      "s_addc_u32 s59, s67, 0\n\t"
      "global_load_dwordx4 v[168:171], v244, s[58:59] sc1\n\t"
      "s_add_u32 s58, s66, s44\n\t"
      "s_addc_u32 s59, s67, 0\n\t"
      "global_load_dwordx4 v[172:175], v244, s[58:59] sc1\n\t"
      "s_add_u32 s58, s66, s45\n\t"
      "s_addc_u32 s59, s67, 0\n\t"
      "global_load_dwordx4 v[176:179], v244, s[58:59] sc1\n\t"
      "s_add_u32 s58, s66, s46\n\t"
      "s_addc_u32 s59, s67, 0\n\t"
      "global_load_dwordx4 v[180:183], v244, s[58:59] sc1\n\t"
      "s_waitcnt vmcnt(0)\n\t"
      "v_max3_u32 v251, v156, v157, v158\n\t"
      "v_max3_u32 v251, v251, v159, v160\n\t"
      "v_max3_u32 v251, v251, v161, v162\n\t"
      "v_max3_u32 v251, v251, v163, v164\n\t"
      "v_max3_u32 v251, v251, v165, v166\n\t"
      "v_max3_u32 v251, v251, v167, v168\n\t"
      "v_max3_u32 v251, v251, v169, v170\n\t"
      "v_max3_u32 v251, v251, v171, v172\n\t"
      "v_max3_u32 v251, v251, v173, v174\n\t"
      "v_max3_u32 v251, v251, v175, v176\n\t"
      "v_max3_u32 v251, v251, v177, v178\n\t"
      "v_max3_u32 v251, v251, v179, v180\n\t"
      "v_max3_u32 v251, v251, v181, v182\n\t"
      "v_max_u32_e32 v251, v251, v183\n\t"
      "v_cmp_eq_u32_e32 vcc, -1, v251\n\t"
      "s_cbranch_vccnz LA41L_%=\n\t"
      "LA41X_%=:\n\t"
      "v_mfma_f32_16x16x32_bf16 v[224:227], v[156:159], v[0:3], 0\n\t"
      "v_mfma_f32_16x16x32_bf16 v[224:227], v[160:163], v[16:19], v[224:227]\n\t"
      "v_mfma_f32_16x16x32_bf16 v[224:227], v[164:167], v[32:35], v[224:227]\n\t"
      "v_mfma_f32_16x16x32_bf16 v[224:227], v[168:171], v[48:51], v[224:227]\n\t"
      "v_mfma_f32_16x16x32_bf16 v[224:227], v[172:175], v[64:67], v[224:227]\n\t"
      "v_mfma_f32_16x16x32_bf16 v[224:227], v[176:179], v[80:83], v[224:227]\n\t"
      "v_mfma_f32_16x16x32_bf16 v[224:227], v[180:183], v[96:99], v[224:227]\n\t"
      "v_mfma_f32_16x16x32_bf16 v[208:211], v[156:159], v[4:7], 0\n\t"
      "v_mfma_f32_16x16x32_bf16 v[208:211], v[160:163], v[20:23], v[208:211]\n\t"
      "v_mfma_f32_16x16x32_bf16 v[208:211], v[164:167], v[36:39], v[208:211]\n\t"
      "s_nop 7\n\t"
      "s_branch LA41B_%=\n\t"
      "LA42R_%=:\n\t"
      "s_mov_b32 s80, 0\n\t"
      "LA42L_%=:\n\t"
      "s_add_u32 s80, s80, 1\n\t"
      "s_cmp_lt_u32 s80, 262144\n\t"
      "s_cbranch_scc0 LA42X_%=\n\t"
      "s_waitcnt vmcnt(0)\n\t"
      "PQ2R_%=:\n\t"
      "PQ2L_%=:\n\t"
      "s_add_u32 s80, s80, 1\n\t"
      "s_cmp_lt_u32 s80, 262144\n\t"
      "s_cbranch_scc0 PQ2X_%=\n\t"
      "s_sleep 1\n\t"
      "global_load_dword v251, v252, s[68:69] offset:252 sc1\n\t"
      "s_waitcnt vmcnt(0)\n\t"
      "v_cmp_eq_u32_e32 vcc, -1, v251\n\t"
      "s_cbranch_vccnz PQ2L_%=\n\t"
      "PQ2X_%=:\n\t"
      "s_add_u32 s58, s68, s40\n\t"
      "s_addc_u32 s59, s69, 0\n\t"
      "global_load_dwordx4 v[184:187], v244, s[58:59] sc1\n\t"
      "s_add_u32 s58, s68, s41\n\t"
      "s_addc_u32 s59, s69, 0\n\t"
      "global_load_dwordx4 v[188:191], v244, s[58:59] sc1\n\t"
      "s_add_u32 s58, s68, s42\n\t"
      "s_addc_u32 s59, s69, 0\n\t"
      "global_load_dwordx4 v[192:195], v244, s[58:59] sc1\n\t"
      "s_add_u32 s58, s68, s43\n\t"
      "s_addc_u32 s59, s69, 0\n\t"
      "global_load_dwordx4 v[196:199], v244, s[58:59] sc1\n\t"
      "s_add_u32 s58, s68, s44\n\t"
      "s_addc_u32 s59, s69, 0\n\t"
      "global_load_dwordx4 v[200:203], v244, s[58:59] sc1\n\t"
      "s_add_u32 s58, s68, s45\n\t"
      "s_addc_u32 s59, s69, 0\n\t"
      "global_load_dwordx4 v[204:207], v244, s[58:59] sc1\n\t"
      "s_add_u32 s58, s68, s46\n\t"
      "s_addc_u32 s59, s69, 0\n\t"
      "global_load_dwordx4 v[128:131], v244, s[58:59] sc1\n\t"
      "s_waitcnt vmcnt(0)\n\t"
      "v_max3_u32 v251, v184, v185, v186\n\t"
      "v_max3_u32 v251, v251, v187, v188\n\t"
      "v_max3_u32 v251, v251, v189, v190\n\t"
      "v_max3_u32 v251, v251, v191, v192\n\t"
      "v_max3_u32 v251, v251, v193, v194\n\t"
      "v_max3_u32 v251, v251, v195, v196\n\t"
      "v_max3_u32 v251, v251, v197, v198\n\t"
      "v_max3_u32 v251, v251, v199, v200\n\t"
      "v_max3_u32 v251, v251, v201, v202\n\t"
      "v_max3_u32 v251, v251, v203, v204\n\t"
      "v_max3_u32 v251, v251, v205, v206\n\t"
      "v_max3_u32 v251, v251, v207, v128\n\t"
      "v_max3_u32 v251, v251, v129, v130\n\t"
      "v_max_u32_e32 v251, v251, v131\n\t"
      "v_cmp_eq_u32_e32 vcc, -1, v251\n\t"
      "s_cbranch_vccnz LA42L_%=\n\t"
      "LA42X_%=:\n\t"
      "v_mfma_f32_16x16x32_bf16 v[220:223], v[184:187], v[0:3], 0\n\t"
      "v_mfma_f32_16x16x32_bf16 v[220:223], v[188:191], v[16:19], v[220:223]\n\t"
      "v_mfma_f32_16x16x32_bf16 v[220:223], v[192:195], v[32:35], v[220:223]\n\t"
      "v_mfma_f32_16x16x32_bf16 v[220:223], v[196:199], v[48:51], v[220:223]\n\t"
      "v_mfma_f32_16x16x32_bf16 v[220:223], v[200:203], v[64:67], v[220:223]\n\t"
      "v_mfma_f32_16x16x32_bf16 v[220:223], v[204:207], v[80:83], v[220:223]\n\t"
      "v_mfma_f32_16x16x32_bf16 v[220:223], v[128:131], v[96:99], v[220:223]\n\t"
      "v_mfma_f32_16x16x32_bf16 v[224:227], v[184:187], v[4:7], 0\n\t"
      "v_mfma_f32_16x16x32_bf16 v[224:227], v[188:191], v[20:23], v[224:227]\n\t"
      "v_mfma_f32_16x16x32_bf16 v[224:227], v[192:195], v[36:39], v[224:227]\n\t"
      "s_nop 7\n\t"
      "s_branch LA42B_%=\n\t"
      "LA43R_%=:\n\t"
      "s_mov_b32 s80, 0\n\t"
      "LA43L_%=:\n\t"
      "s_add_u32 s80, s80, 1\n\t"
      "s_cmp_lt_u32 s80, 262144\n\t"
      "s_cbranch_scc0 LA43X_%=\n\t"
      "s_waitcnt vmcnt(0)\n\t"
      "PQ3R_%=:\n\t"
      "PQ3L_%=:\n\t"
      "s_add_u32 s80, s80, 1\n\t"
      "s_cmp_lt_u32 s80, 262144\n\t"
      "s_cbranch_scc0 PQ3X_%=\n\t"
      "s_sleep 1\n\t"
      "global_load_dword v251, v252, s[70:71] offset:252 sc1\n\t"
      "s_waitcnt vmcnt(0)\n\t"
      "v_cmp_eq_u32_e32 vcc, -1, v251\n\t"
      "s_cbranch_vccnz PQ3L_%=\n\t"
      "PQ3X_%=:\n\t"
      "s_add_u32 s58, s70, s40\n\t"
      "s_addc_u32 s59, s71, 0\n\t"
      "global_load_dwordx4 v[132:135], v244, s[58:59] sc1\n\t"
      "s_add_u32 s58, s70, s41\n\t"
      "s_addc_u32 s59, s71, 0\n\t"
      "global_load_dwordx4 v[136:139], v244, s[58:59] sc1\n\t"
      "s_add_u32 s58, s70, s42\n\t"
      "s_addc_u32 s59, s71, 0\n\t"
      "global_load_dwordx4 v[140:143], v244, s[58:59] sc1\n\t"
      "s_add_u32 s58, s70, s43\n\t"
      "s_addc_u32 s59, s71, 0\n\t"
      "global_load_dwordx4 v[144:147], v244, s[58:59] sc1\n\t"
      "s_add_u32 s58, s70, s44\n\t"
      "s_addc_u32 s59, s71, 0\n\t"
      "global_load_dwordx4 v[148:151], v244, s[58:59] sc1\n\t"
      "s_add_u32 s58, s70, s45\n\t"
      "s_addc_u32 s59, s71, 0\n\t"
      "global_load_dwordx4 v[152:155], v244, s[58:59] sc1\n\t"
      "s_add_u32 s58, s70, s46\n\t"
      "s_addc_u32 s59, s71, 0\n\t"
      "global_load_dwordx4 v[156:159], v244, s[58:59] sc1\n\t"
      "s_waitcnt vmcnt(0)\n\t"
      "v_max3_u32 v251, v132, v133, v134\n\t"
      "v_max3_u32 v251, v251, v135, v136\n\t"
      "v_max3_u32 v251, v251, v137, v138\n\t"
      "v_max3_u32 v251, v251, v139, v140\n\t"
      "v_max3_u32 v251, v251, v141, v142\n\t"
      "v_max3_u32 v251, v251, v143, v144\n\t"
      "v_max3_u32 v251, v251, v145, v146\n\t"
      "v_max3_u32 v251, v251, v147, v148\n\t"
      "v_max3_u32 v251, v251, v149, v150\n\t"
      "v_max3_u32 v251, v251, v151, v152\n\t"
      "v_max3_u32 v251, v251, v153, v154\n\t"
      "v_max3_u32 v251, v251, v155, v156\n\t"
      "v_max3_u32 v251, v251, v157, v158\n\t"
      "v_max_u32_e32 v251, v251, v159\n\t"
      "v_cmp_eq_u32_e32 vcc, -1, v251\n\t"
      "s_cbranch_vccnz LA43L_%=\n\t"
      "LA43X_%=:\n\t"
      "v_mfma_f32_16x16x32_bf16 v[216:219], v[132:135], v[0:3], 0\n\t"
      "v_mfma_f32_16x16x32_bf16 v[216:219], v[136:139], v[16:19], v[216:219]\n\t"
      "v_mfma_f32_16x16x32_bf16 v[216:219], v[140:143], v[32:35], v[216:219]\n\t"
      "v_mfma_f32_16x16x32_bf16 v[216:219], v[144:147], v[48:51], v[216:219]\n\t"
      "v_mfma_f32_16x16x32_bf16 v[216:219], v[148:151], v[64:67], v[216:219]\n\t"
      "v_mfma_f32_16x16x32_bf16 v[216:219], v[152:155], v[80:83], v[216:219]\n\t"
      "v_mfma_f32_16x16x32_bf16 v[216:219], v[156:159], v[96:99], v[216:219]\n\t"
      "v_mfma_f32_16x16x32_bf16 v[220:223], v[132:135], v[4:7], 0\n\t"
      "v_mfma_f32_16x16x32_bf16 v[220:223], v[136:139], v[20:23], v[220:223]\n\t"
      "v_mfma_f32_16x16x32_bf16 v[220:223], v[140:143], v[36:39], v[220:223]\n\t"
      "s_nop 7\n\t"
      "s_branch LA43B_%=\n\t"
      "LA44R_%=:\n\t"
      "s_mov_b32 s80, 0\n\t"
      "LA44L_%=:\n\t"
      "s_add_u32 s80, s80, 1\n\t"
      "s_cmp_lt_u32 s80, 262144\n\t"
      "s_cbranch_scc0 LA44X_%=\n\t"
      "s_waitcnt vmcnt(0)\n\t"
      "PQ4R_%=:\n\t"
      "PQ4L_%=:\n\t"
      "s_add_u32 s80, s80, 1\n\t"
      "s_cmp_lt_u32 s80, 262144\n\t"
      "s_cbranch_scc0 PQ4X_%=\n\t"
      "s_sleep 1\n\t"
      "global_load_dword v251, v252, s[72:73] offset:252 sc1\n\t"
      "s_waitcnt vmcnt(0)\n\t"
      "v_cmp_eq_u32_e32 vcc, -1, v251\n\t"
      "s_cbranch_vccnz PQ4L_%=\n\t"
      "PQ4X_%=:\n\t"
      "s_add_u32 s58, s72, s40\n\t"
      "s_addc_u32 s59, s73, 0\n\t"
      "global_load_dwordx4 v[160:163], v244, s[58:59] sc1\n\t"
      "s_add_u32 s58, s72, s41\n\t"
      "s_addc_u32 s59, s73, 0\n\t"
      "global_load_dwordx4 v[164:167], v244, s[58:59] sc1\n\t"
      "s_add_u32 s58, s72, s42\n\t"
      "s_addc_u32 s59, s73, 0\n\t"
      "global_load_dwordx4 v[168:171], v244, s[58:59] sc1\n\t"
      "s_add_u32 s58, s72, s43\n\t"
      "s_addc_u32 s59, s73, 0\n\t"
      "global_load_dwordx4 v[172:175], v244, s[58:59] sc1\n\t"
      "s_add_u32 s58, s72, s44\n\t"
      "s_addc_u32 s59, s73, 0\n\t"
      "global_load_dwordx4 v[176:179], v244, s[58:59] sc1\n\t"
      "s_add_u32 s58, s72, s45\n\t"
      "s_addc_u32 s59, s73, 0\n\t"
      "global_load_dwordx4 v[180:183], v244, s[58:59] sc1\n\t"
      "s_add_u32 s58, s72, s46\n\t"
      "s_addc_u32 s59, s73, 0\n\t"
      "global_load_dwordx4 v[184:187], v244, s[58:59] sc1\n\t"
      "s_waitcnt vmcnt(0)\n\t"
      "v_max3_u32 v251, v160, v161, v162\n\t"
      "v_max3_u32 v251, v251, v163, v164\n\t"
      "v_max3_u32 v251, v251, v165, v166\n\t"
      "v_max3_u32 v251, v251, v167, v168\n\t"
      "v_max3_u32 v251, v251, v169, v170\n\t"
      "v_max3_u32 v251, v251, v171, v172\n\t"
      "v_max3_u32 v251, v251, v173, v174\n\t"
      "v_max3_u32 v251, v251, v175, v176\n\t"
      "v_max3_u32 v251, v251, v177, v178\n\t"
      "v_max3_u32 v251, v251, v179, v180\n\t"
      "v_max3_u32 v251, v251, v181, v182\n\t"
      "v_max3_u32 v251, v251, v183, v184\n\t"
      "v_max3_u32 v251, v251, v185, v186\n\t"
      "v_max_u32_e32 v251, v251, v187\n\t"
      "v_cmp_eq_u32_e32 vcc, -1, v251\n\t"
      "s_cbranch_vccnz LA44L_%=\n\t"
      "LA44X_%=:\n\t"
      "v_mfma_f32_16x16x32_bf16 v[212:215], v[160:163], v[0:3], 0\n\t"
      "v_mfma_f32_16x16x32_bf16 v[212:215], v[164:167], v[16:19], v[212:215]\n\t"
      "v_mfma_f32_16x16x32_bf16 v[212:215], v[168:171], v[32:35], v[212:215]\n\t"
      "v_mfma_f32_16x16x32_bf16 v[212:215], v[172:175], v[48:51], v[212:215]\n\t"
      "v_mfma_f32_16x16x32_bf16 v[212:215], v[176:179], v[64:67], v[212:215]\n\t"
      "v_mfma_f32_16x16x32_bf16 v[212:215], v[180:183], v[80:83], v[212:215]\n\t"
      "v_mfma_f32_16x16x32_bf16 v[212:215], v[184:187], v[96:99], v[212:215]\n\t"
      "v_mfma_f32_16x16x32_bf16 v[216:219], v[160:163], v[4:7], 0\n\t"
      "v_mfma_f32_16x16x32_bf16 v[216:219], v[164:167], v[20:23], v[216:219]\n\t"
      "v_mfma_f32_16x16x32_bf16 v[216:219], v[168:171], v[36:39], v[216:219]\n\t"
      "s_nop 7\n\t"
      "s_branch LA44B_%=\n\t"
      "LA45R_%=:\n\t"
      "s_mov_b32 s80, 0\n\t"
      "LA45L_%=:\n\t"
      "s_add_u32 s80, s80, 1\n\t"
      "s_cmp_lt_u32 s80, 262144\n\t"
      "s_cbranch_scc0 LA45X_%=\n\t"
      "s_waitcnt vmcnt(0)\n\t"
      "PQ5R_%=:\n\t"
      "PQ5L_%=:\n\t"
      "s_add_u32 s80, s80, 1\n\t"
      "s_cmp_lt_u32 s80, 262144\n\t"
      "s_cbranch_scc0 PQ5X_%=\n\t"
      "s_sleep 1\n\t"
      "global_load_dword v251, v252, s[74:75] offset:252 sc1\n\t"
      "s_waitcnt vmcnt(0)\n\t"
      "v_cmp_eq_u32_e32 vcc, -1, v251\n\t"
      "s_cbranch_vccnz PQ5L_%=\n\t"
      "PQ5X_%=:\n\t"
      "s_add_u32 s58, s74, s40\n\t"
      "s_addc_u32 s59, s75, 0\n\t"
      "global_load_dwordx4 v[188:191], v244, s[58:59] sc1\n\t"
      "s_add_u32 s58, s74, s41\n\t"
      "s_addc_u32 s59, s75, 0\n\t"
      "global_load_dwordx4 v[192:195], v244, s[58:59] sc1\n\t"
      "s_add_u32 s58, s74, s42\n\t"
      "s_addc_u32 s59, s75, 0\n\t"
      "global_load_dwordx4 v[196:199], v244, s[58:59] sc1\n\t"
      "s_add_u32 s58, s74, s43\n\t"
      "s_addc_u32 s59, s75, 0\n\t"
      "global_load_dwordx4 v[200:203], v244, s[58:59] sc1\n\t"
      "s_add_u32 s58, s74, s44\n\t"
      "s_addc_u32 s59, s75, 0\n\t"
      "global_load_dwordx4 v[204:207], v244, s[58:59] sc1\n\t"
      "s_add_u32 s58, s74, s45\n\t"
      "s_addc_u32 s59, s75, 0\n\t"
      "global_load_dwordx4 v[128:131], v244, s[58:59] sc1\n\t"
      "s_add_u32 s58, s74, s46\n\t"
      "s_addc_u32 s59, s75, 0\n\t"
      "global_load_dwordx4 v[132:135], v244, s[58:59] sc1\n\t"
      "s_waitcnt vmcnt(0)\n\t"
      "v_max3_u32 v251, v188, v189, v190\n\t"
      "v_max3_u32 v251, v251, v191, v192\n\t"
      "v_max3_u32 v251, v251, v193, v194\n\t"
      "v_max3_u32 v251, v251, v195, v196\n\t"
      "v_max3_u32 v251, v251, v197, v198\n\t"
      "v_max3_u32 v251, v251, v199, v200\n\t"
      "v_max3_u32 v251, v251, v201, v202\n\t"
      "v_max3_u32 v251, v251, v203, v204\n\t"
      "v_max3_u32 v251, v251, v205, v206\n\t"
      "v_max3_u32 v251, v251, v207, v128\n\t"
      "v_max3_u32 v251, v251, v129, v130\n\t"
      "v_max3_u32 v251, v251, v131, v132\n\t"
      "v_max3_u32 v251, v251, v133, v134\n\t"
      "v_max_u32_e32 v251, v251, v135\n\t"
      "v_cmp_eq_u32_e32 vcc, -1, v251\n\t"
      "s_cbranch_vccnz LA45L_%=\n\t"
      "LA45X_%=:\n\t"
      "v_mfma_f32_16x16x32_bf16 v[208:211], v[188:191], v[0:3], 0\n\t"
      "v_mfma_f32_16x16x32_bf16 v[208:211], v[192:195], v[16:19], v[208:211]\n\t"
      "v_mfma_f32_16x16x32_bf16 v[208:211], v[196:199], v[32:35], v[208:211]\n\t"
      "v_mfma_f32_16x16x32_bf16 v[208:211], v[200:203], v[48:51], v[208:211]\n\t"
      "v_mfma_f32_16x16x32_bf16 v[208:211], v[204:207], v[64:67], v[208:211]\n\t"
      "v_mfma_f32_16x16x32_bf16 v[208:211], v[128:131], v[80:83], v[208:211]\n\t"
      "v_mfma_f32_16x16x32_bf16 v[208:211], v[132:135], v[96:99], v[208:211]\n\t"
      "v_mfma_f32_16x16x32_bf16 v[212:215], v[188:191], v[4:7], 0\n\t"
      "v_mfma_f32_16x16x32_bf16 v[212:215], v[192:195], v[20:23], v[212:215]\n\t"
      "v_mfma_f32_16x16x32_bf16 v[212:215], v[196:199], v[36:39], v[212:215]\n\t"
      "s_nop 7\n\t"
      "s_branch LA45B_%=\n\t"
      "LA46R_%=:\n\t"
      "s_mov_b32 s80, 0\n\t"
      "LA46L_%=:\n\t"
      "s_add_u32 s80, s80, 1\n\t"
      "s_cmp_lt_u32 s80, 262144\n\t"
      "s_cbranch_scc0 LA46X_%=\n\t"
      "s_waitcnt vmcnt(0)\n\t"
      "PQ6R_%=:\n\t"
      "PQ6L_%=:\n\t"
      "s_add_u32 s80, s80, 1\n\t"
      "s_cmp_lt_u32 s80, 262144\n\t"
      "s_cbranch_scc0 PQ6X_%=\n\t"
      "s_sleep 1\n\t"
      "global_load_dword v251, v252, s[76:77] offset:252 sc1\n\t"
      "s_waitcnt vmcnt(0)\n\t"
      "v_cmp_eq_u32_e32 vcc, -1, v251\n\t"
      "s_cbranch_vccnz PQ6L_%=\n\t"
      "PQ6X_%=:\n\t"
      "s_add_u32 s58, s76, s40\n\t"
      "s_addc_u32 s59, s77, 0\n\t"
      "global_load_dwordx4 v[136:139], v244, s[58:59] sc1\n\t"
      "s_add_u32 s58, s76, s41\n\t"
      "s_addc_u32 s59, s77, 0\n\t"
      "global_load_dwordx4 v[140:143], v244, s[58:59] sc1\n\t"
      "s_add_u32 s58, s76, s42\n\t"
      "s_addc_u32 s59, s77, 0\n\t"
      "global_load_dwordx4 v[144:147], v244, s[58:59] sc1\n\t"
      "s_add_u32 s58, s76, s43\n\t"
      "s_addc_u32 s59, s77, 0\n\t"
      "global_load_dwordx4 v[148:151], v244, s[58:59] sc1\n\t"
      "s_add_u32 s58, s76, s44\n\t"
      "s_addc_u32 s59, s77, 0\n\t"
      "global_load_dwordx4 v[152:155], v244, s[58:59] sc1\n\t"
      "s_add_u32 s58, s76, s45\n\t"
      "s_addc_u32 s59, s77, 0\n\t"
      "global_load_dwordx4 v[156:159], v244, s[58:59] sc1\n\t"
      "s_add_u32 s58, s76, s46\n\t"
      "s_addc_u32 s59, s77, 0\n\t"
      "global_load_dwordx4 v[160:163], v244, s[58:59] sc1\n\t"
      "s_waitcnt vmcnt(0)\n\t"
      "v_max3_u32 v251, v136, v137, v138\n\t"
      "v_max3_u32 v251, v251, v139, v140\n\t"
      "v_max3_u32 v251, v251, v141, v142\n\t"
      "v_max3_u32 v251, v251, v143, v144\n\t"
      "v_max3_u32 v251, v251, v145, v146\n\t"
      "v_max3_u32 v251, v251, v147, v148\n\t"
      "v_max3_u32 v251, v251, v149, v150\n\t"
      "v_max3_u32 v251, v251, v151, v152\n\t"
      "v_max3_u32 v251, v251, v153, v154\n\t"
      "v_max3_u32 v251, v251, v155, v156\n\t"
      "v_max3_u32 v251, v251, v157, v158\n\t"
      "v_max3_u32 v251, v251, v159, v160\n\t"
      "v_max3_u32 v251, v251, v161, v162\n\t"
      "v_max_u32_e32 v251, v251, v163\n\t"
      "v_cmp_eq_u32_e32 vcc, -1, v251\n\t"
      "s_cbranch_vccnz LA46L_%=\n\t"
      "LA46X_%=:\n\t"
      "v_mfma_f32_16x16x32_bf16 v[224:227], v[136:139], v[0:3], 0\n\t"
      "v_mfma_f32_16x16x32_bf16 v[224:227], v[140:143], v[16:19], v[224:227]\n\t"
      "v_mfma_f32_16x16x32_bf16 v[224:227], v[144:147], v[32:35], v[224:227]\n\t"
      "v_mfma_f32_16x16x32_bf16 v[224:227], v[148:151], v[48:51], v[224:227]\n\t"
      "v_mfma_f32_16x16x32_bf16 v[224:227], v[152:155], v[64:67], v[224:227]\n\t"
      "v_mfma_f32_16x16x32_bf16 v[224:227], v[156:159], v[80:83], v[224:227]\n\t"
      "v_mfma_f32_16x16x32_bf16 v[224:227], v[160:163], v[96:99], v[224:227]\n\t"
      "v_mfma_f32_16x16x32_bf16 v[208:211], v[136:139], v[4:7], 0\n\t"
      "v_mfma_f32_16x16x32_bf16 v[208:211], v[140:143], v[20:23], v[208:211]\n\t"
      "v_mfma_f32_16x16x32_bf16 v[208:211], v[144:147], v[36:39], v[208:211]\n\t"
      "s_nop 7\n\t"
      "s_branch LA46B_%=\n\t"
      : "+{s[0:1]}"(ka), "+{v0}"(tid)
      : "{s2}"(blockIdx.x)
      : "memory", "vcc", "scc", "m0",
        "v1", "v2", "v3", "v4", "v5", "v6", "v7", "v8", "v9", "v10", "v11", "v12", "v13", "v14", "v15", "v16", "v17",
        "v18", "v19", "v20", "v21", "v22", "v23", "v24", "v25", "v26", "v27", "v28", "v29", "v30", "v31", "v32", "v33",
        "v34", "v35", "v36", "v37", "v38", "v39", "v40", "v41", "v42", "v43", "v44", "v45", "v46", "v47", "v48", "v49",
        "v50", "v51", "v52", "v53", "v54", "v55", "v56", "v57", "v58", "v59", "v60", "v61", "v62", "v63", "v64", "v65",
        "v66", "v67", "v68", "v69", "v70", "v71", "v72", "v73", "v74", "v75", "v76", "v77", "v78", "v79", "v80", "v81",
        "v82", "v83", "v84", "v85", "v86", "v87", "v88", "v89", "v90", "v91", "v92", "v93", "v94", "v95", "v96", "v97",
        "v98", "v99", "v100", "v101", "v102", "v103", "v104", "v105", "v106", "v107", "v108", "v109", "v110", "v111",
        "v112", "v113", "v114", "v115", "v116", "v117", "v118", "v119", "v120", "v121", "v122", "v123", "v124", "v125",
        "v126", "v127", "v128", "v129", "v130", "v131", "v132", "v133", "v134", "v135", "v136", "v137", "v138", "v139",
        "v140", "v141", "v142", "v143", "v144", "v145", "v146", "v147", "v148", "v149", "v150", "v151", "v152", "v153",
        "v154", "v155", "v156", "v157", "v158", "v159", "v160", "v161", "v162", "v163", "v164", "v165", "v166", "v167",
        "v168", "v169", "v170", "v171", "v172", "v173", "v174", "v175", "v176", "v177", "v178", "v179", "v180", "v181",
        "v182", "v183", "v184", "v185", "v186", "v187", "v188", "v189", "v190", "v191", "v192", "v193", "v194", "v195",
        "v196", "v197", "v198", "v199", "v200", "v201", "v202", "v203", "v204", "v205", "v206", "v207", "v208", "v209",
        "v210", "v211", "v212", "v213", "v214", "v215", "v216", "v217", "v218", "v219", "v220", "v221", "v222", "v223",
        "v224", "v225", "v226", "v227", "v228", "v229", "v230", "v231", "v232", "v233", "v234", "v235", "v236", "v237",
        "v238", "v239", "v240", "v241", "v242", "v243", "v244", "v245", "v246", "v247", "v248", "v249", "v250", "v251",
        "v252", "v253", "v254", "v255", "s3", "s4", "s5", "s6", "s7", "s8", "s9", "s10", "s11", "s12", "s13", "s14",
        "s15", "s16", "s17", "s18", "s19", "s20", "s21", "s22", "s23", "s24", "s25", "s26", "s27", "s28", "s29", "s30",
        "s31", "s32", "s33", "s34", "s35", "s36", "s37", "s38", "s39", "s40", "s41", "s42", "s43", "s44", "s45", "s46",
        "s47", "s48", "s49", "s50", "s51", "s52", "s53", "s54", "s55", "s56", "s57", "s58", "s59", "s60", "s61", "s62",
        "s63", "s64", "s65", "s66", "s67", "s68", "s69", "s70", "s71", "s72", "s73", "s74", "s75", "s76", "s77", "s78",
        "s79", "s80", "s81", "s82", "s83"
  );
#endif
}

extern "C" void kernel_launch(void* const* d_in, const int* in_sizes, int n_in, void* d_out, int out_size,
                              void* d_ws, size_t ws_size, hipStream_t stream) {
  (void)hipFuncSetAttribute((const void*)fused_block, hipFuncAttributeMaxDynamicSharedMemorySize, 163840);
  fused_block<<<256, 512, 163840, stream>>>((const u16*)d_in[0], (const u16*)d_in[1], (const u16*)d_in[2],
                                            (const u16*)d_in[4], (const u16*)d_in[5], (const u16*)d_in[6],
                                            (const u16*)d_in[7], (u16*)d_out, (char*)d_ws);
}
```

```cpp
#include <hip/hip_runtime.h>
#include <stdint.h>
typedef unsigned short u16;
__global__ __launch_bounds__(512, 2) void fused_block(const u16* x, const u16* wqkv, const u16* wo, const u16* w1, const u16* b1,
                                                   const u16* w2, const u16* b2, u16* out, char* ws) {
#if defined(__HIP_DEVICE_COMPILE__)
  unsigned long long ka = (unsigned long long)__builtin_amdgcn_kernarg_segment_ptr();
  unsigned tid = threadIdx.x;
  asm volatile(
      "s_load_dwordx16 s[4:19], s[0:1], 0x0\n\t"
      "s_load_dwordx2 s[20:21], s[0:1], 0x40\n\t"
      "v_lshrrev_b32_e32 v199, 6, v0\n\t"
      "v_and_b32_e32 v192, 63, v0\n\t"
      "v_readfirstlane_b32 s24, v199\n\t"
      "s_getpc_b64 s[58:59]\n\t"
      "s_lshr_b32 s34, s2, 3\n\t"
      "v_min_u32_e32 v200, 8, v0\n\t"
      "v_lshl_add_u32 v200, v200, 5, s34\n\t"
      "s_mov_b32 m0, 64512\n\t"
      "v_min_u32_e32 v200, 281, v200\n\t"
      "v_lshlrev_b32_e32 v200, 7, v200\n\t"
      "s_cmp_lg_u32 s24, 0\n\t"
      "s_cbranch_scc1 NTC_%=\n\t"
      "global_load_lds_dword v200, s[58:59]\n\t"
      "NTC_%=:\n\t"
      "s_and_b32 s34, s2, 7\n\t"
      "s_lshr_b32 s35, s2, 6\n\t"
      "s_lshl2_add_u32 s25, s34, s35\n\t"
      "s_bfe_u32 s26, s2, 196611\n\t"
      "s_and_b32 s34, s25, 30\n\t"
      "s_lshl_b32 s34, s34, 6\n\t"
      "s_and_b32 s35, s25, 1\n\t"
      "s_add_u32 s38, s34, s35\n\t"
      "v_lshrrev_b32_e32 v197, 3, v192\n\t"
      "v_and_b32_e32 v198, 7, v192\n\t"
      "s_lshr_b32 s3, s2, 1\n\t"
      "s_and_b32 s3, s3, 0\n\t"
      "s_xor_b32 s3, s3, s24\n\t"
      "s_lshl4_add_u32 s34, s3, s38\n\t"
      "s_mul_i32 s34, s34, 896\n\t"
      "s_movk_i32 s35, 1792\n\t"
      "v_lshl_add_u32 v199, v198, 4, s34\n\t"
      "v_mad_u32_u24 v120, v197, s35, v199\n\t"
      "s_min_u32 s74, s26, 6\n\t"
      "s_lshl_b32 s34, s74, 7\n\t"
      "s_add_u32 s35, s34, 768\n\t"
      "s_sub_u32 s36, s35, 896\n\t"
      "s_min_u32 s35, s35, s36\n\t"
      "v_add_u32_e32 v126, s35, v120\n\t"
      "s_add_u32 s35, s34, 640\n\t"
      "s_sub_u32 s36, s35, 896\n\t"
      "s_min_u32 s35, s35, s36\n\t"
      "v_add_u32_e32 v125, s35, v120\n\t"
      "s_add_u32 s35, s34, 512\n\t"
      "s_sub_u32 s36, s35, 896\n\t"
      "s_min_u32 s35, s35, s36\n\t"
      "v_add_u32_e32 v124, s35, v120\n\t"
      "s_add_u32 s35, s34, 384\n\t"
      "s_sub_u32 s36, s35, 896\n\t"
      "s_min_u32 s35, s35, s36\n\t"
      "v_add_u32_e32 v123, s35, v120\n\t"
      "s_add_u32 s35, s34, 256\n\t"
      "s_sub_u32 s36, s35, 896\n\t"
      "s_min_u32 s35, s35, s36\n\t"
      "v_add_u32_e32 v122, s35, v120\n\t"
      "s_add_u32 s35, s34, 128\n\t"
      "s_sub_u32 s36, s35, 896\n\t"
      "s_min_u32 s35, s35, s36\n\t"
      "v_add_u32_e32 v121, s35, v120\n\t"
      "s_add_u32 s35, s34, 0\n\t"
      "s_sub_u32 s36, s35, 896\n\t"
      "s_min_u32 s35, s35, s36\n\t"
      "v_add_u32_e32 v120, s35, v120\n\t"
      "s_waitcnt lgkmcnt(0)\n\t"
      "global_load_dwordx4 v[204:207], v120, s[4:5]\n\t"
      "global_load_dwordx4 v[4:7], v121, s[4:5]\n\t"
      "global_load_dwordx4 v[8:11], v122, s[4:5]\n\t"
      "global_load_dwordx4 v[12:15], v123, s[4:5]\n\t"
      "global_load_dwordx4 v[16:19], v124, s[4:5]\n\t"
      "global_load_dwordx4 v[20:23], v125, s[4:5]\n\t"
      "s_cmp_lt_u32 s25, 2\n\t"
      "s_cselect_b32 s34, 0, 16\n\t"
      "s_sub_u32 s34, s38, s34\n\t"
      "s_lshl1_add_u32 s34, s24, s34\n\t"
      "s_mul_i32 s34, s34, 896\n\t"
      "s_and_b32 s33, s25, 3\n\t"
      "s_lshl_b32 s33, s33, 1\n\t"
      "s_and_b32 s35, s26, 1\n\t"
      "s_add_u32 s33, s33, s35\n\t"
      "s_cmp_eq_u32 s33, 7\n\t"
      "s_cselect_b32 s33, 0, s33\n\t"
      "v_min_u32_e32 v200, 55, v192\n\t"
      "s_lshl_b32 s35, s33, 3\n\t"
      "v_add_u32_e32 v201, s35, v200\n\t"
      "v_subrev_u32_e32 v202, 56, v201\n\t"
      "v_min_u32_e32 v201, v201, v202\n\t"
      "v_lshl_add_u32 v127, v201, 4, s34\n\t"
      "s_min_u32 s28, s26, 6\n\t"
      "s_and_b32 s39, s24, 3\n\t"
      "s_lshl_b32 s39, s39, 12\n\t"
      "s_cmp_lt_u32 s24, 4\n\t"
      "s_cselect_b32 s27, 4, 0\n\t"
      "v_lshrrev_b32_e32 v193, 4, v192\n\t"
      "v_and_b32_e32 v194, 15, v192\n\t"
      "v_bfe_u32 v195, v192, 2, 2\n\t"
      "v_and_b32_e32 v196, 3, v192\n\t"
      "v_lshlrev_b32_e32 v244, 4, v192\n\t"
      "s_lshr_b32 s34, s3, 1\n\t"
      "s_mul_i32 s34, s34, 14336\n\t"
      "s_and_b32 s35, s3, 1\n\t"
      "s_lshl_b32 s35, s35, 7\n\t"
      "s_add_u32 s34, s34, s35\n\t"
      "v_xor_b32_e32 v199, v197, v198\n\t"
      "v_lshl_add_u32 v199, v199, 4, s34\n\t"
      "v_lshl_add_u32 v199, v198, 8, v199\n\t"
      "s_sub_u32 s35, s74, s33\n\t"
      "s_cselect_b32 s36, 7, 0\n\t"
      "s_add_u32 s35, s35, s36\n\t"
      "s_lshl_b32 s35, s35, 11\n\t"
      "s_add_u32 s34, s35, 0\n\t"
      "s_sub_u32 s36, s34, 14336\n\t"
      "s_min_u32 s34, s34, s36\n\t"
      "v_add_u32_e32 v112, s34, v199\n\t"
      "s_add_u32 s34, s35, 2048\n\t"
      "s_sub_u32 s36, s34, 14336\n\t"
      "s_min_u32 s34, s34, s36\n\t"
      "v_add_u32_e32 v113, s34, v199\n\t"
      "s_add_u32 s34, s35, 4096\n\t"
      "s_sub_u32 s36, s34, 14336\n\t"
      "s_min_u32 s34, s34, s36\n\t"
      "v_add_u32_e32 v114, s34, v199\n\t"
      "s_add_u32 s34, s35, 6144\n\t"
      "s_sub_u32 s36, s34, 14336\n\t"
      "s_min_u32 s34, s34, s36\n\t"
      "v_add_u32_e32 v115, s34, v199\n\t"
      "s_add_u32 s34, s35, 8192\n\t"
      "s_sub_u32 s36, s34, 14336\n\t"
      "s_min_u32 s34, s34, s36\n\t"
      "v_add_u32_e32 v116, s34, v199\n\t"
      "s_add_u32 s34, s35, 10240\n\t"
      "s_sub_u32 s36, s34, 14336\n\t"
      "s_min_u32 s34, s34, s36\n\t"
      "v_add_u32_e32 v117, s34, v199\n\t"
      "s_add_u32 s34, s35, 12288\n\t"
      "s_sub_u32 s36, s34, 14336\n\t"
      "s_min_u32 s34, s34, s36\n\t"
      "v_add_u32_e32 v118, s34, v199\n\t"
      "v_min_u32_e32 v200, 55, v192\n\t"
      "v_lshrrev_b32_e32 v201, 2, v200\n\t"
      "v_lshlrev_b32_e32 v119, 10, v201\n\t"
      "v_cmp_lt_u32_e32 vcc, 6, v201\n\t"
      "v_mov_b32_e32 v202, 57344\n\t"
      "v_mov_b32_e32 v201, 50688\n\t"
      "v_cndmask_b32_e32 v202, v202, v201, vcc\n\t"
      "v_and_b32_e32 v201, 3, v200\n\t"
      "v_lshl_add_u32 v202, v201, 7, v202\n\t"
      "v_and_b32_e32 v200, 7, v200\n\t"
      "v_xor_b32_e32 v200, s24, v200\n\t"
      "v_lshl_add_u32 v202, v200, 4, v202\n\t"
      "v_add_u32_e32 v119, v119, v202\n\t"
      "s_bitcmp1_b32 s27, 2\n\t"
      "s_cbranch_scc1 LSET_%=\n\t"
      "v_xor_b32_e32 v199, v194, v193\n\t"
      "v_lshlrev_b32_e32 v199, 4, v199\n\t"
      "v_lshl_or_b32 v200, v193, 8, v199\n\t"
      "s_and_b32 s34, s24, 1\n\t"
      "s_mul_i32 s35, s34, 28672\n\t"
      "v_add_u32_e32 v160, s35, v200\n\t"
      "v_xor_b32_e32 v161, 64, v160\n\t"
      "v_and_b32_e32 v199, 0x70, v199\n\t"
      "v_lshl_or_b32 v199, v193, 7, v199\n\t"
      "v_add_u32_e32 v199, 57344, v199\n\t"
      "v_add_u32_e32 v200, 14336, v200\n\t"
      "s_cmp_eq_u32 s34, 1\n\t"
      "s_cselect_b64 vcc, -1, 0\n\t"
      "s_cselect_b32 s35, 0, 6656\n\t"
      "v_cndmask_b32_e32 v162, v199, v200, vcc\n\t"
      "v_xor_b32_e32 v163, 64, v162\n\t"
      "v_subrev_u32_e32 v164, s35, v162\n\t"
      "v_subrev_u32_e32 v165, s35, v163\n\t"
      "s_bfe_u32 s35, s24, 65537\n\t"
      "s_mul_i32 s35, s35, 7\n\t"
      "v_and_b32_e32 v201, 24, v192\n\t"
      "v_lshlrev_b32_e32 v201, 2, v201\n\t"
      "v_lshlrev_b32_e32 v202, 7, v195\n\t"
      "v_lshl_or_b32 v202, v193, 12, v202\n\t"
      "v_add_u32_e32 v202, 65536, v202\n\t"
      "v_add_lshl_u32 v199, v196, s35, 3\n\t"
      "v_xor_b32_e32 v199, v199, v201\n\t"
      "v_add_u32_e32 v166, v199, v202\n\t"
      "v_min_u32_e32 v199, 2, v196\n\t"
      "s_add_u32 s35, s35, 4\n\t"
      "v_add_lshl_u32 v199, v199, s35, 3\n\t"
      "v_xor_b32_e32 v199, v199, v201\n\t"
      "v_add_u32_e32 v167, v199, v202\n\t"
      "v_add_u32_e32 v168, 0x10000, v166\n\t"
      "v_add_u32_e32 v169, 0x10000, v167\n\t"
      "v_lshlrev_b32_e32 v199, 11, v193\n\t"
      "v_sub_u32_e32 v170, v166, v199\n\t"
      "v_sub_u32_e32 v171, v167, v199\n\t"
      "s_branch NORM_%=\n\t"
      "LSET_%=:\n\t"
      "s_add_u32 s60, s20, 16777216\n\t"
      "s_addc_u32 s61, s21, 0\n\t"
      "s_load_dwordx2 s[22:23], s[60:61], 0x0\n\t"
      "s_lshr_b32 s34, s39, 9\n\t"
      "v_add_u32_e32 v199, s34, v197\n\t"
      "s_and_b32 s34, s24, 1\n\t"
      "s_lshl_b32 s34, s34, 2\n\t"
      "v_and_b32_e32 v200, 2, v197\n\t"
      "v_or_b32_e32 v200, s34, v200\n\t"
      "v_xor_b32_e32 v200, v200, v198\n\t"
      "v_min_u32_e32 v200, 6, v200\n\t"
      "v_lshlrev_b32_e32 v200, 4, v200\n\t"
      "s_movk_i32 s34, 2688\n\t"
      "v_mad_u32_u24 v149, v199, s34, v200\n\t"
      "v_add_u32_e32 v148, 128, v149\n\t"
      "v_add_u32_e32 v150, 84096, v149\n\t"
      "v_add_u32_e32 v151, 83968, v149\n\t"
      "v_add_u32_e32 v152, 85120, v149\n\t"
      "s_sub_u32 s6, s6, 128\n\t"
      "s_subb_u32 s7, s7, 0\n\t"
      "s_mul_i32 s37, s26, 112\n\t"
      "s_mov_b32 s81, s33\n\t"
      "s_mov_b32 s82, s33\n\t"
      "s_lshr_b32 s35, s2, 6\n\t"
      "s_lshl2_add_u32 s31, s26, s35\n\t"
      "s_and_b32 s35, s2, 7\n\t"
      "s_mul_i32 s34, s35, 14\n\t"
      "s_lshl4_add_u32 s35, s35, -14\n\t"
      "s_cmp_lt_u32 s31, 14\n\t"
      "s_cselect_b32 s34, s34, s35\n\t"
      "s_cselect_b32 s36, 1, 0\n\t"
      "s_or_b32 s27, s27, s36\n\t"
      "s_add_u32 s34, s34, s31\n\t"
      "s_lshr_b32 s29, s34, 2\n\t"
      "s_and_b32 s30, s34, 3\n\t"
      "s_mul_i32 s30, s30, 7\n\t"
      "v_and_b32_e32 v199, 60, v192\n\t"
      "v_and_b32_e32 v200, 24, v192\n\t"
      "v_lshlrev_b32_e32 v202, 3, v196\n\t"
      "v_lshl_or_b32 v202, v200, 2, v202\n\t"
      "v_lshl_or_b32 v202, v195, 7, v202\n\t"
      "v_lshl_or_b32 v202, v193, 10, v202\n\t"
      "v_add_u32_e32 v224, s39, v202\n\t"
      "v_xor_b32_e32 v225, 32, v224\n\t"
      "v_xor_b32_e32 v226, 64, v224\n\t"
      "v_xor_b32_e32 v227, 96, v224\n\t"
      "s_lshl_b32 s34, s24, 12\n\t"
      "s_add_u32 s34, s34, 98304\n\t"
      "v_add_u32_e32 v245, s34, v244\n\t"
      "v_lshlrev_b32_e32 v199, 2, v199\n\t"
      "s_lshl_b32 s34, s24, 8\n\t"
      "v_lshl_add_u32 v247, v192, 2, s34\n\t"
      "v_and_b32_e32 v200, 2, v192\n\t"
      "v_mul_u32_u24_e32 v202, 576, v200\n\t"
      "v_xor_b32_e32 v199, v202, v199\n\t"
      "v_and_b32_e32 v202, 1, v192\n\t"
      "v_lshl_or_b32 v199, v202, 3, v199\n\t"
      "s_and_b32 s35, s34, 0x400\n\t"
      "s_add_u32 s34, s34, s35\n\t"
      "s_add_u32 s34, s34, 98304\n\t"
      "v_add_u32_e32 v246, s34, v199\n\t"
      "v_lshlrev_b32_e32 v199, 2, v202\n\t"
      "v_lshl_or_b32 v199, v200, 4, v199\n\t"
      "s_min_u32 s34, s29, 27\n\t"
      "s_lshl_b32 s34, s34, 7\n\t"
      "s_lshr_b32 s35, s24, 2\n\t"
      "s_lshl_b32 s35, s35, 6\n\t"
      "s_add_u32 s34, s34, s35\n\t"
      "s_lshr_b32 s35, s39, 9\n\t"
      "s_add_u32 s34, s34, s35\n\t"
      "v_add_u32_e32 v248, s34, v199\n\t"
      "s_add_u32 s48, s20, 8388608\n\t"
      "s_addc_u32 s49, s21, 0\n\t"
      "v_and_b32_e32 v199, 2, v197\n\t"
      "v_xor_b32_e32 v199, v199, v198\n\t"
      "v_lshlrev_b32_e32 v199, 4, v199\n\t"
      "s_movk_i32 s34, 7168\n\t"
      "v_mad_u32_u24 v144, v197, s34, v199\n\t"
      "v_xor_b32_e32 v199, 64, v144\n\t"
      "v_add_u32_e32 v145, 56320, v199\n\t"
      "v_add_u32_e32 v146, 112640, v144\n\t"
      "v_add_u32_e32 v147, 168960, v199\n\t"
      "v_and_b32_e32 v199, 6, v197\n\t"
      "v_xor_b32_e32 v199, v199, v198\n\t"
      "v_lshlrev_b32_e32 v199, 4, v199\n\t"
      "s_movk_i32 s35, 896\n\t"
      "v_mad_u32_u24 v154, v197, s35, v199\n\t"
      "v_add_u32_e32 v155, 6144, v154\n\t"
      "v_add_u32_e32 v156, 12288, v154\n\t"
      "v_add_u32_e32 v157, 18432, v154\n\t"
      "s_mul_i32 s34, s24, 917504\n\t"
      "s_lshl_b32 s35, s29, 7\n\t"
      "s_add_u32 s34, s34, s35\n\t"
      "s_add_u32 s52, s10, s34\n\t"
      "s_addc_u32 s53, s11, 0\n\t"
      "s_lshl_b32 s34, s28, 7\n\t"
      "s_add_u32 s56, s8, s34\n\t"
      "s_addc_u32 s57, s9, 0\n\t"
      "s_mov_b32 s34, 0\n\t"
      "s_and_b32 s35, s34, 1\n\t"
      "s_and_b32 s34, s34, 2\n\t"
      "s_lshl_b32 s34, s34, 6\n\t"
      "s_add_u32 s34, s34, s35\n\t"
      "s_mul_i32 s76, s34, 3584\n\t"
      "s_mov_b32 s34, 1\n\t"
      "s_and_b32 s35, s34, 1\n\t"
      "s_and_b32 s34, s34, 2\n\t"
      "s_lshl_b32 s34, s34, 6\n\t"
      "s_add_u32 s34, s34, s35\n\t"
      "s_mul_i32 s77, s34, 3584\n\t"
      "s_mov_b32 s34, 2\n\t"
      "s_and_b32 s35, s34, 1\n\t"
      "s_and_b32 s34, s34, 2\n\t"
      "s_lshl_b32 s34, s34, 6\n\t"
      "s_add_u32 s34, s34, s35\n\t"
      "s_mul_i32 s78, s34, 3584\n\t"
      "s_mov_b32 s34, 3\n\t"
      "s_and_b32 s35, s34, 1\n\t"
      "s_and_b32 s34, s34, 2\n\t"
      "s_lshl_b32 s34, s34, 6\n\t"
      "s_add_u32 s34, s34, s35\n\t"
      "s_mul_i32 s79, s34, 3584\n\t"
      "NORM_%=:\n\t"
      "global_load_dwordx4 v[24:27], v126, s[4:5]\n\t"
      "global_load_dwordx4 v[28:31], v127, s[4:5]\n\t"
      "v_mov_b32_e32 v199, 0x3f803f80\n\t"
      "s_waitcnt vmcnt(7)\n\t"
      "v_dot2_f32_bf16 v88, v204, v199, 0\n\t"
      "v_dot2_f32_bf16 v92, v204, v204, 0\n\t"
      "v_lshlrev_b32_e32 v32, 16, v204\n\t"
      "v_and_b32_e32 v33, 0xffff0000, v204\n\t"
      "v_dot2_f32_bf16 v89, v205, v199, 0\n\t"
      "v_dot2_f32_bf16 v93, v205, v205, 0\n\t"
      "v_lshlrev_b32_e32 v34, 16, v205\n\t"
      "v_and_b32_e32 v35, 0xffff0000, v205\n\t"
      "v_dot2_f32_bf16 v90, v206, v199, 0\n\t"
      "v_dot2_f32_bf16 v94, v206, v206, 0\n\t"
      "v_lshlrev_b32_e32 v36, 16, v206\n\t"
      "v_and_b32_e32 v37, 0xffff0000, v206\n\t"
      "v_dot2_f32_bf16 v91, v207, v199, 0\n\t"
      "v_dot2_f32_bf16 v95, v207, v207, 0\n\t"
      "v_lshlrev_b32_e32 v38, 16, v207\n\t"
      "v_and_b32_e32 v39, 0xffff0000, v207\n\t"
      "s_waitcnt vmcnt(6)\n\t"
      "v_dot2_f32_bf16 v88, v4, v199, v88\n\t"
      "v_dot2_f32_bf16 v92, v4, v4, v92\n\t"
      "v_lshlrev_b32_e32 v40, 16, v4\n\t"
      "v_and_b32_e32 v41, 0xffff0000, v4\n\t"
      "v_dot2_f32_bf16 v89, v5, v199, v89\n\t"
      "v_dot2_f32_bf16 v93, v5, v5, v93\n\t"
      "v_lshlrev_b32_e32 v42, 16, v5\n\t"
      "v_and_b32_e32 v43, 0xffff0000, v5\n\t"
      "v_dot2_f32_bf16 v90, v6, v199, v90\n\t"
      "v_dot2_f32_bf16 v94, v6, v6, v94\n\t"
      "v_lshlrev_b32_e32 v44, 16, v6\n\t"
      "v_and_b32_e32 v45, 0xffff0000, v6\n\t"
      "v_dot2_f32_bf16 v91, v7, v199, v91\n\t"
      "v_dot2_f32_bf16 v95, v7, v7, v95\n\t"
      "v_lshlrev_b32_e32 v46, 16, v7\n\t"
      "v_and_b32_e32 v47, 0xffff0000, v7\n\t"
      "s_waitcnt vmcnt(5)\n\t"
      "v_dot2_f32_bf16 v88, v8, v199, v88\n\t"
      "v_dot2_f32_bf16 v92, v8, v8, v92\n\t"
      "v_lshlrev_b32_e32 v48, 16, v8\n\t"
      "v_and_b32_e32 v49, 0xffff0000, v8\n\t"
      "v_dot2_f32_bf16 v89, v9, v199, v89\n\t"
      "v_dot2_f32_bf16 v93, v9, v9, v93\n\t"
      "v_lshlrev_b32_e32 v50, 16, v9\n\t"
      "v_and_b32_e32 v51, 0xffff0000, v9\n\t"
      "v_dot2_f32_bf16 v90, v10, v199, v90\n\t"
      "v_dot2_f32_bf16 v94, v10, v10, v94\n\t"
      "v_lshlrev_b32_e32 v52, 16, v10\n\t"
      "v_and_b32_e32 v53, 0xffff0000, v10\n\t"
      "v_dot2_f32_bf16 v91, v11, v199, v91\n\t"
      "v_dot2_f32_bf16 v95, v11, v11, v95\n\t"
      "v_lshlrev_b32_e32 v54, 16, v11\n\t"
      "v_and_b32_e32 v55, 0xffff0000, v11\n\t"
      "s_waitcnt vmcnt(4)\n\t"
      "v_dot2_f32_bf16 v88, v12, v199, v88\n\t"
      "v_dot2_f32_bf16 v92, v12, v12, v92\n\t"
      "v_lshlrev_b32_e32 v56, 16, v12\n\t"
      "v_and_b32_e32 v57, 0xffff0000, v12\n\t"
      "v_dot2_f32_bf16 v89, v13, v199, v89\n\t"
      "v_dot2_f32_bf16 v93, v13, v13, v93\n\t"
      "v_lshlrev_b32_e32 v58, 16, v13\n\t"
      "v_and_b32_e32 v59, 0xffff0000, v13\n\t"
      "v_dot2_f32_bf16 v90, v14, v199, v90\n\t"
      "v_dot2_f32_bf16 v94, v14, v14, v94\n\t"
      "v_lshlrev_b32_e32 v60, 16, v14\n\t"
      "v_and_b32_e32 v61, 0xffff0000, v14\n\t"
      "v_dot2_f32_bf16 v91, v15, v199, v91\n\t"
      "v_dot2_f32_bf16 v95, v15, v15, v95\n\t"
      "v_lshlrev_b32_e32 v62, 16, v15\n\t"
      "v_and_b32_e32 v63, 0xffff0000, v15\n\t"
      "s_waitcnt vmcnt(3)\n\t"
      "v_dot2_f32_bf16 v88, v16, v199, v88\n\t"
      "v_dot2_f32_bf16 v92, v16, v16, v92\n\t"
      "v_lshlrev_b32_e32 v64, 16, v16\n\t"
      "v_and_b32_e32 v65, 0xffff0000, v16\n\t"
      "v_dot2_f32_bf16 v89, v17, v199, v89\n\t"
      "v_dot2_f32_bf16 v93, v17, v17, v93\n\t"
      "v_lshlrev_b32_e32 v66, 16, v17\n\t"
      "v_and_b32_e32 v67, 0xffff0000, v17\n\t"
      "v_dot2_f32_bf16 v90, v18, v199, v90\n\t"
      "v_dot2_f32_bf16 v94, v18, v18, v94\n\t"
      "v_lshlrev_b32_e32 v68, 16, v18\n\t"
      "v_and_b32_e32 v69, 0xffff0000, v18\n\t"
      "v_dot2_f32_bf16 v91, v19, v199, v91\n\t"
      "v_dot2_f32_bf16 v95, v19, v19, v95\n\t"
      "v_lshlrev_b32_e32 v70, 16, v19\n\t"
      "v_and_b32_e32 v71, 0xffff0000, v19\n\t"
      "s_waitcnt vmcnt(2)\n\t"
      "v_dot2_f32_bf16 v88, v20, v199, v88\n\t"
      "v_dot2_f32_bf16 v92, v20, v20, v92\n\t"
      "v_lshlrev_b32_e32 v72, 16, v20\n\t"
      "v_and_b32_e32 v73, 0xffff0000, v20\n\t"
      "v_dot2_f32_bf16 v89, v21, v199, v89\n\t"
      "v_dot2_f32_bf16 v93, v21, v21, v93\n\t"
      "v_lshlrev_b32_e32 v74, 16, v21\n\t"
      "v_and_b32_e32 v75, 0xffff0000, v21\n\t"
      "v_dot2_f32_bf16 v90, v22, v199, v90\n\t"
      "v_dot2_f32_bf16 v94, v22, v22, v94\n\t"
      "v_lshlrev_b32_e32 v76, 16, v22\n\t"
      "v_and_b32_e32 v77, 0xffff0000, v22\n\t"
      "v_dot2_f32_bf16 v91, v23, v199, v91\n\t"
      "v_dot2_f32_bf16 v95, v23, v23, v95\n\t"
      "v_lshlrev_b32_e32 v78, 16, v23\n\t"
      "v_and_b32_e32 v79, 0xffff0000, v23\n\t"
      "s_waitcnt vmcnt(1)\n\t"
      "v_dot2_f32_bf16 v88, v24, v199, v88\n\t"
      "v_dot2_f32_bf16 v92, v24, v24, v92\n\t"
      "v_lshlrev_b32_e32 v80, 16, v24\n\t"
      "v_and_b32_e32 v81, 0xffff0000, v24\n\t"
      "v_dot2_f32_bf16 v89, v25, v199, v89\n\t"
      "v_dot2_f32_bf16 v93, v25, v25, v93\n\t"
      "v_lshlrev_b32_e32 v82, 16, v25\n\t"
      "v_and_b32_e32 v83, 0xffff0000, v25\n\t"
      "v_dot2_f32_bf16 v90, v26, v199, v90\n\t"
      "v_dot2_f32_bf16 v94, v26, v26, v94\n\t"
      "v_lshlrev_b32_e32 v84, 16, v26\n\t"
      "v_and_b32_e32 v85, 0xffff0000, v26\n\t"
      "v_dot2_f32_bf16 v91, v27, v199, v91\n\t"
      "v_dot2_f32_bf16 v95, v27, v27, v95\n\t"
      "v_lshlrev_b32_e32 v86, 16, v27\n\t"
      "v_and_b32_e32 v87, 0xffff0000, v27\n\t"
      "s_waitcnt vmcnt(0)\n\t"
      "s_bitcmp0_b32 s27, 2\n\t"
      "s_cbranch_scc1 SK1_%=\n\t"
      "s_mul_i32 s72, s81, 172032\n\t"
      "s_add_u32 s72, s72, s37\n\t"
      "s_add_u32 s40, s6, s72\n\t"
      "s_addc_u32 s41, s7, 0\n\t"
      "s_add_u32 s81, s81, 1\n\t"
      "s_cmp_eq_u32 s81, 7\n\t"
      "s_cselect_b32 s81, 0, s81\n\t"
      "s_add_u32 m0, s39, 131072\n\t"
      "s_nop 0\n\t"
      "global_load_lds_dwordx4 v148, s[40:41]\n\t"
      "global_load_lds_dwordx4 v149, s[40:41] offset:1024\n\t"
      "global_load_lds_dwordx4 v150, s[40:41] offset:2048\n\t"
      "global_load_lds_dwordx4 v151, s[40:41] offset:3072\n\t"
      "SK1_%=:\n\t"
      "v_dot2_f32_bf16 v96, v28, v199, 0\n\t"
      "v_dot2_f32_bf16 v97, v28, v28, 0\n\t"
      "v_dot2_f32_bf16 v96, v29, v199, v96\n\t"
      "v_dot2_f32_bf16 v97, v29, v29, v97\n\t"
      "v_dot2_f32_bf16 v96, v30, v199, v96\n\t"
      "v_dot2_f32_bf16 v97, v30, v30, v97\n\t"
      "v_dot2_f32_bf16 v96, v31, v199, v96\n\t"
      "v_dot2_f32_bf16 v97, v31, v31, v97\n\t"
      "v_cmp_gt_u32_e32 vcc, 56, v192\n\t"
      "v_pk_add_f32 v[88:89], v[88:89], v[90:91]\n\t"
      "v_pk_add_f32 v[92:93], v[92:93], v[94:95]\n\t"
      "v_cndmask_b32_e32 v96, 0, v96, vcc\n\t"
      "v_cndmask_b32_e32 v97, 0, v97, vcc\n\t"
      "v_add_f32_e32 v88, v88, v89\n\t"
      "v_add_f32_e32 v92, v92, v93\n\t"
      "v_add_f32_dpp v96, v96, v96 quad_perm:[1,0,3,2] row_mask:0xf bank_mask:0xf\n\t"
      "v_add_f32_dpp v97, v97, v97 quad_perm:[1,0,3,2] row_mask:0xf bank_mask:0xf\n\t"
      "v_add_f32_dpp v88, v88, v88 quad_perm:[1,0,3,2] row_mask:0xf bank_mask:0xf\n\t"
      "v_add_f32_dpp v92, v92, v92 quad_perm:[1,0,3,2] row_mask:0xf bank_mask:0xf\n\t"
      "v_add_f32_dpp v96, v96, v96 quad_perm:[2,3,0,1] row_mask:0xf bank_mask:0xf\n\t"
      "v_add_f32_dpp v97, v97, v97 quad_perm:[2,3,0,1] row_mask:0xf bank_mask:0xf\n\t"
      "v_add_f32_dpp v88, v88, v88 quad_perm:[2,3,0,1] row_mask:0xf bank_mask:0xf\n\t"
      "v_add_f32_dpp v92, v92, v92 quad_perm:[2,3,0,1] row_mask:0xf bank_mask:0xf\n\t"
      "v_add_f32_dpp v96, v96, v96 row_half_mirror row_mask:0xf bank_mask:0xf\n\t"
      "v_add_f32_dpp v97, v97, v97 row_half_mirror row_mask:0xf bank_mask:0xf\n\t"
      "v_add_f32_dpp v88, v88, v88 row_half_mirror row_mask:0xf bank_mask:0xf\n\t"
      "v_add_f32_dpp v92, v92, v92 row_half_mirror row_mask:0xf bank_mask:0xf\n\t"
      "v_mul_f32_e32 v88, 0xbb124925, v88\n\t"
      "v_add_f32_dpp v96, v96, v96 row_mirror row_mask:0xf bank_mask:0xf\n\t"
      "v_mul_f32_e32 v92, 0x3b124925, v92\n\t"
      "s_bitcmp0_b32 s27, 2\n\t"
      "s_cbranch_scc1 SK2_%=\n\t"
      "s_mul_i32 s72, s81, 172032\n\t"
      "s_add_u32 s72, s72, s37\n\t"
      "s_add_u32 s40, s6, s72\n\t"
      "s_addc_u32 s41, s7, 0\n\t"
      "s_add_u32 s81, s81, 1\n\t"
      "s_cmp_eq_u32 s81, 7\n\t"
      "s_cselect_b32 s81, 0, s81\n\t"
      "s_add_u32 m0, s39, 147456\n\t"
      "s_nop 0\n\t"
      "global_load_lds_dwordx4 v148, s[40:41]\n\t"
      "global_load_lds_dwordx4 v149, s[40:41] offset:1024\n\t"
      "SK2_%=:\n\t"
      "v_add_f32_dpp v97, v97, v97 row_mirror row_mask:0xf bank_mask:0xf\n\t"
      "v_fma_f32 v92, -v88, v88, v92\n\t"
      "s_nop 0\n\t"
      "v_permlane16_swap_b32_e32 v96, v97\n\t"
      "v_max_f32_e32 v92, 0, v92\n\t"
      "v_add_f32_e32 v96, v96, v97\n\t"
      "v_add_f32_e32 v92, 0x3727c5ac, v92\n\t"
      "v_mov_b32_e32 v97, v96\n\t"
      "v_rsq_f32_e32 v92, v92\n\t"
      "s_nop 0\n\t"
      "v_permlane32_swap_b32_e32 v96, v97\n\t"
      "v_add_f32_e32 v96, v96, v97\n\t"
      "v_mul_f32_e32 v88, v88, v92\n\t"
      "v_mov_b32_e32 v97, v96\n\t"
      "v_pk_fma_f32 v[32:33], v[32:33], v[92:93], v[88:89] op_sel_hi:[1,0,0]\n\t"
      "v_pk_fma_f32 v[34:35], v[34:35], v[92:93], v[88:89] op_sel_hi:[1,0,0]\n\t"
      "v_permlane16_swap_b32_e32 v96, v97\n\t"
      "v_pk_fma_f32 v[36:37], v[36:37], v[92:93], v[88:89] op_sel_hi:[1,0,0]\n\t"
      "v_pk_fma_f32 v[38:39], v[38:39], v[92:93], v[88:89] op_sel_hi:[1,0,0]\n\t"
      "v_mul_f32_e32 v96, 0xbb124925, v96\n\t"
      "v_cvt_pk_bf16_f32 v0, v32, v33\n\t"
      "v_cvt_pk_bf16_f32 v1, v34, v35\n\t"
      "v_cvt_pk_bf16_f32 v2, v36, v37\n\t"
      "v_cvt_pk_bf16_f32 v3, v38, v39\n\t"
      "ds_write_b128 v112, v[0:3]\n\t"
      "v_pk_fma_f32 v[40:41], v[40:41], v[92:93], v[88:89] op_sel_hi:[1,0,0]\n\t"
      "v_pk_fma_f32 v[42:43], v[42:43], v[92:93], v[88:89] op_sel_hi:[1,0,0]\n\t"
      "v_mul_f32_e32 v97, 0x3b124925, v97\n\t"
      "v_pk_fma_f32 v[44:45], v[44:45], v[92:93], v[88:89] op_sel_hi:[1,0,0]\n\t"
      "v_pk_fma_f32 v[46:47], v[46:47], v[92:93], v[88:89] op_sel_hi:[1,0,0]\n\t"
      "v_fma_f32 v97, -v96, v96, v97\n\t"
      "v_cvt_pk_bf16_f32 v4, v40, v41\n\t"
      "s_bitcmp0_b32 s27, 2\n\t"
      "s_cbranch_scc1 SK3_%=\n\t"
      "global_load_lds_dwordx4 v150, s[40:41] offset:2048\n\t"
      "global_load_lds_dwordx4 v151, s[40:41] offset:3072\n\t"
      "SK3_%=:\n\t"
      "v_cvt_pk_bf16_f32 v5, v42, v43\n\t"
      "v_cvt_pk_bf16_f32 v6, v44, v45\n\t"
      "v_cvt_pk_bf16_f32 v7, v46, v47\n\t"
      "ds_write_b128 v113, v[4:7]\n\t"
      "v_pk_fma_f32 v[48:49], v[48:49], v[92:93], v[88:89] op_sel_hi:[1,0,0]\n\t"
      "v_pk_fma_f32 v[50:51], v[50:51], v[92:93], v[88:89] op_sel_hi:[1,0,0]\n\t"
      "v_max_f32_e32 v97, 0, v97\n\t"
      "v_pk_fma_f32 v[52:53], v[52:53], v[92:93], v[88:89] op_sel_hi:[1,0,0]\n\t"
      "v_pk_fma_f32 v[54:55], v[54:55], v[92:93], v[88:89] op_sel_hi:[1,0,0]\n\t"
      "v_add_f32_e32 v97, 0x3727c5ac, v97\n\t"
      "v_cvt_pk_bf16_f32 v8, v48, v49\n\t"
      "v_cvt_pk_bf16_f32 v9, v50, v51\n\t"
      "v_cvt_pk_bf16_f32 v10, v52, v53\n\t"
      "v_cvt_pk_bf16_f32 v11, v54, v55\n\t"
      "ds_write_b128 v114, v[8:11]\n\t"
      "v_pk_fma_f32 v[56:57], v[56:57], v[92:93], v[88:89] op_sel_hi:[1,0,0]\n\t"
      "v_pk_fma_f32 v[58:59], v[58:59], v[92:93], v[88:89] op_sel_hi:[1,0,0]\n\t"
      "v_rsq_f32_e32 v97, v97\n\t"
      "v_pk_fma_f32 v[60:61], v[60:61], v[92:93], v[88:89] op_sel_hi:[1,0,0]\n\t"
      "v_pk_fma_f32 v[62:63], v[62:63], v[92:93], v[88:89] op_sel_hi:[1,0,0]\n\t"
      "v_cvt_pk_bf16_f32 v12, v56, v57\n\t"
      "v_cvt_pk_bf16_f32 v13, v58, v59\n\t"
      "v_cvt_pk_bf16_f32 v14, v60, v61\n\t"
      "v_cvt_pk_bf16_f32 v15, v62, v63\n\t"
      "ds_write_b128 v115, v[12:15]\n\t"
      "v_pk_fma_f32 v[64:65], v[64:65], v[92:93], v[88:89] op_sel_hi:[1,0,0]\n\t"
      "v_pk_fma_f32 v[66:67], v[66:67], v[92:93], v[88:89] op_sel_hi:[1,0,0]\n\t"
      "v_mul_f32_e32 v96, v96, v97\n\t"
      "v_pk_fma_f32 v[68:69], v[68:69], v[92:93], v[88:89] op_sel_hi:[1,0,0]\n\t"
      "v_pk_fma_f32 v[70:71], v[70:71], v[92:93], v[88:89] op_sel_hi:[1,0,0]\n\t"
      "s_bitcmp0_b32 s27, 2\n\t"
      "s_cbranch_scc1 SK4_%=\n\t"
      "s_mul_i32 s72, s82, 172032\n\t"
      "s_add_u32 s72, s72, s37\n\t"
      "s_add_u32 s72, s72, 1792\n\t"
      "s_add_u32 s40, s6, s72\n\t"
      "s_addc_u32 s41, s7, 0\n\t"
      "s_add_u32 s82, s82, 1\n\t"
      "s_cmp_eq_u32 s82, 7\n\t"
      "s_cselect_b32 s82, 0, s82\n\t"
      "s_add_u32 m0, s39, 65536\n\t"
      "s_nop 0\n\t"
      "global_load_lds_dwordx4 v148, s[40:41]\n\t"
      "global_load_lds_dwordx4 v152, s[40:41] offset:1024\n\t"
      "SK4_%=:\n\t"
      "v_cvt_pk_bf16_f32 v16, v64, v65\n\t"
      "v_cvt_pk_bf16_f32 v17, v66, v67\n\t"
      "v_cvt_pk_bf16_f32 v18, v68, v69\n\t"
      "v_cvt_pk_bf16_f32 v19, v70, v71\n\t"
      "ds_write_b128 v116, v[16:19]\n\t"
      "v_pk_fma_f32 v[72:73], v[72:73], v[92:93], v[88:89] op_sel_hi:[1,0,0]\n\t"
      "v_pk_fma_f32 v[74:75], v[74:75], v[92:93], v[88:89] op_sel_hi:[1,0,0]\n\t"
      "v_pk_fma_f32 v[76:77], v[76:77], v[92:93], v[88:89] op_sel_hi:[1,0,0]\n\t"
      "v_pk_fma_f32 v[78:79], v[78:79], v[92:93], v[88:89] op_sel_hi:[1,0,0]\n\t"
      "v_cvt_pk_bf16_f32 v20, v72, v73\n\t"
      "v_cvt_pk_bf16_f32 v21, v74, v75\n\t"
      "v_cvt_pk_bf16_f32 v22, v76, v77\n\t"
      "v_cvt_pk_bf16_f32 v23, v78, v79\n\t"
      "ds_write_b128 v117, v[20:23]\n\t"
      "v_pk_fma_f32 v[80:81], v[80:81], v[92:93], v[88:89] op_sel_hi:[1,0,0]\n\t"
      "v_pk_fma_f32 v[82:83], v[82:83], v[92:93], v[88:89] op_sel_hi:[1,0,0]\n\t"
      "v_pk_fma_f32 v[84:85], v[84:85], v[92:93], v[88:89] op_sel_hi:[1,0,0]\n\t"
      "v_pk_fma_f32 v[86:87], v[86:87], v[92:93], v[88:89] op_sel_hi:[1,0,0]\n\t"
      "v_cvt_pk_bf16_f32 v24, v80, v81\n\t"
      "v_cvt_pk_bf16_f32 v25, v82, v83\n\t"
      "v_cvt_pk_bf16_f32 v26, v84, v85\n\t"
      "v_cvt_pk_bf16_f32 v27, v86, v87\n\t"
      "ds_write_b128 v118, v[24:27]\n\t"
      "v_lshlrev_b32_e32 v32, 16, v28\n\t"
      "v_and_b32_e32 v33, 0xffff0000, v28\n\t"
      "v_lshlrev_b32_e32 v34, 16, v29\n\t"
      "v_and_b32_e32 v35, 0xffff0000, v29\n\t"
      "v_lshlrev_b32_e32 v36, 16, v30\n\t"
      "v_and_b32_e32 v37, 0xffff0000, v30\n\t"
      "v_lshlrev_b32_e32 v38, 16, v31\n\t"
      "s_bitcmp0_b32 s27, 2\n\t"
      "s_cbranch_scc1 SK5_%=\n\t"
      "s_mul_i32 s72, s82, 172032\n\t"
      "s_add_u32 s72, s72, s37\n\t"
      "s_add_u32 s72, s72, 1792\n\t"
      "s_add_u32 s40, s6, s72\n\t"
      "s_addc_u32 s41, s7, 0\n\t"
      "s_add_u32 s82, s82, 1\n\t"
      "s_cmp_eq_u32 s82, 7\n\t"
      "s_cselect_b32 s82, 0, s82\n\t"
      "s_add_u32 m0, s39, 67584\n\t"
      "s_nop 0\n\t"
      "global_load_lds_dwordx4 v148, s[40:41]\n\t"
      "global_load_lds_dwordx4 v152, s[40:41] offset:1024\n\t"
      "SK5_%=:\n\t"
      "v_and_b32_e32 v39, 0xffff0000, v31\n\t"
      "v_pk_fma_f32 v[32:33], v[32:33], v[96:97], v[96:97] op_sel:[0,1,0] op_sel_hi:[1,1,0]\n\t"
      "v_pk_fma_f32 v[34:35], v[34:35], v[96:97], v[96:97] op_sel:[0,1,0] op_sel_hi:[1,1,0]\n\t"
      "v_pk_fma_f32 v[36:37], v[36:37], v[96:97], v[96:97] op_sel:[0,1,0] op_sel_hi:[1,1,0]\n\t"
      "v_pk_fma_f32 v[38:39], v[38:39], v[96:97], v[96:97] op_sel:[0,1,0] op_sel_hi:[1,1,0]\n\t"
      "v_cvt_pk_bf16_f32 v28, v32, v33\n\t"
      "v_cvt_pk_bf16_f32 v29, v34, v35\n\t"
      "v_cvt_pk_bf16_f32 v30, v36, v37\n\t"
      "v_cvt_pk_bf16_f32 v31, v38, v39\n\t"
      "ds_write_b128 v119, v[28:31]\n\t"
      "s_waitcnt lgkmcnt(0)\n\t"
      "s_bitcmp1_b32 s27, 2\n\t"
      "s_cbranch_scc1 LPATH_%=\n\t"
      "s_setprio 2\n\t"
      "s_barrier\n\t"
      "ds_read_b64_tr_b16 v[76:77], v168\n\t"
      "ds_read_b64_tr_b16 v[78:79], v168 offset:512\n\t"
      "ds_read_b128 v[68:71], v160\n\t"
      "ds_read_b128 v[72:75], v160 offset:14336\n\t"
      "ds_read_b64_tr_b16 v[80:81], v169\n\t"
      "ds_read_b64_tr_b16 v[82:83], v169 offset:512\n\t"
      "ds_read_b64_tr_b16 v[84:85], v168 offset:1024\n\t"
      "ds_read_b64_tr_b16 v[86:87], v168 offset:1536\n\t"
      "ds_read_b128 v[64:67], v162\n\t"
      "ds_read_b64_tr_b16 v[88:89], v169 offset:1024\n\t"
      "ds_read_b64_tr_b16 v[90:91], v169 offset:1536\n\t"
      "ds_read_b64_tr_b16 v[104:105], v168 offset:2048\n\t"
      "ds_read_b64_tr_b16 v[106:107], v168 offset:2560\n\t"
      "ds_read_b128 v[96:99], v161 offset:1024\n\t"
      "ds_read_b128 v[100:103], v161 offset:15360\n\t"
      "ds_read_b64_tr_b16 v[108:109], v169 offset:2048\n\t"
      "ds_read_b64_tr_b16 v[110:111], v169 offset:2560\n\t"
      "ds_read_b64_tr_b16 v[112:113], v168 offset:3072\n\t"
      "ds_read_b64_tr_b16 v[114:115], v168 offset:3584\n\t"
      "ds_read_b128 v[92:95], v163 offset:1024\n\t"
      "ds_read_b64_tr_b16 v[116:117], v169 offset:3072\n\t"
      "ds_read_b64_tr_b16 v[118:119], v169 offset:3584\n\t"
      "s_waitcnt vmcnt(0)\n\t"
      "s_barrier\n\t"
      "s_waitcnt lgkmcnt(14)\n\t"
      "v_mfma_f32_16x16x32_bf16 v[0:3], v[76:79], v[68:71], 0\n\t"
      "ds_read_b64_tr_b16 v[132:133], v168 offset:16384\n\t"
      "ds_read_b64_tr_b16 v[134:135], v168 offset:16896\n\t"
      "v_mfma_f32_16x16x32_bf16 v[8:11], v[76:79], v[72:75], 0\n\t"
      "ds_read_b128 v[124:127], v160 offset:2048\n\t"
      "v_mfma_f32_16x16x32_bf16 v[4:7], v[80:83], v[68:71], 0\n\t"
      "ds_read_b128 v[128:131], v160 offset:16384\n\t"
      "v_mfma_f32_16x16x32_bf16 v[12:15], v[80:83], v[72:75], 0\n\t"
      "ds_read_b64_tr_b16 v[136:137], v169 offset:16384\n\t"
      "v_mfma_f32_16x16x32_bf16 v[24:27], v[84:87], v[68:71], 0\n\t"
      "ds_read_b64_tr_b16 v[138:139], v169 offset:16896\n\t"
      "v_mfma_f32_16x16x32_bf16 v[32:35], v[84:87], v[72:75], 0\n\t"
      "ds_read_b64_tr_b16 v[140:141], v168 offset:17408\n\t"
      "s_waitcnt lgkmcnt(14)\n\t"
      "v_mfma_f32_16x16x32_bf16 v[16:19], v[84:87], v[64:67], 0\n\t"
      "ds_read_b64_tr_b16 v[142:143], v168 offset:17920\n\t"
      "v_mfma_f32_16x16x32_bf16 v[28:31], v[88:91], v[68:71], 0\n\t"
      "ds_read_b128 v[120:123], v162 offset:2048\n\t"
      "v_mfma_f32_16x16x32_bf16 v[36:39], v[88:91], v[72:75], 0\n\t"
      "ds_read_b64_tr_b16 v[144:145], v169 offset:17408\n\t"
      "v_mfma_f32_16x16x32_bf16 v[20:23], v[88:91], v[64:67], 0\n\t"
      "ds_read_b64_tr_b16 v[146:147], v169 offset:17920\n\t"
      "s_waitcnt lgkmcnt(11)\n\t"
      "v_mfma_f32_16x16x32_bf16 v[0:3], v[104:107], v[96:99], v[0:3]\n\t"
      "ds_read_b64_tr_b16 v[76:77], v168 offset:18432\n\t"
      "ds_read_b64_tr_b16 v[78:79], v168 offset:18944\n\t"
      "v_mfma_f32_16x16x32_bf16 v[8:11], v[104:107], v[100:103], v[8:11]\n\t"
      "ds_read_b128 v[68:71], v161 offset:3072\n\t"
      "v_mfma_f32_16x16x32_bf16 v[4:7], v[108:111], v[96:99], v[4:7]\n\t"
      "ds_read_b128 v[72:75], v161 offset:17408\n\t"
      "v_mfma_f32_16x16x32_bf16 v[12:15], v[108:111], v[100:103], v[12:15]\n\t"
      "ds_read_b64_tr_b16 v[80:81], v169 offset:18432\n\t"
      "v_mfma_f32_16x16x32_bf16 v[24:27], v[112:115], v[96:99], v[24:27]\n\t"
      "ds_read_b64_tr_b16 v[82:83], v169 offset:18944\n\t"
      "v_mfma_f32_16x16x32_bf16 v[32:35], v[112:115], v[100:103], v[32:35]\n\t"
      "ds_read_b64_tr_b16 v[84:85], v168 offset:19456\n\t"
      "v_mfma_f32_16x16x32_bf16 v[16:19], v[112:115], v[92:95], v[16:19]\n\t"
      "ds_read_b64_tr_b16 v[86:87], v168 offset:19968\n\t"
      "v_mfma_f32_16x16x32_bf16 v[28:31], v[116:119], v[96:99], v[28:31]\n\t"
      "ds_read_b128 v[64:67], v163 offset:3072\n\t"
      "v_mfma_f32_16x16x32_bf16 v[36:39], v[116:119], v[100:103], v[36:39]\n\t"
      "ds_read_b64_tr_b16 v[88:89], v169 offset:19456\n\t"
      "v_mfma_f32_16x16x32_bf16 v[20:23], v[116:119], v[92:95], v[20:23]\n\t"
      "ds_read_b64_tr_b16 v[90:91], v169 offset:19968\n\t"
      "s_barrier\n\t"
      "s_waitcnt lgkmcnt(11)\n\t"
      "v_mfma_f32_16x16x32_bf16 v[0:3], v[132:135], v[124:127], v[0:3]\n\t"
      "ds_read_b64_tr_b16 v[104:105], v166\n\t"
      "v_lshlrev_b32_e32 v199, 2, v193\n\t"
      "v_mfma_f32_16x16x32_bf16 v[8:11], v[132:135], v[128:131], v[8:11]\n\t"
      "ds_read_b64_tr_b16 v[106:107], v166 offset:512\n\t"
      "v_sub_u32_e32 v199, v194, v199\n\t"
      "v_mfma_f32_16x16x32_bf16 v[4:7], v[136:139], v[124:127], v[4:7]\n\t"
      "ds_read_b128 v[96:99], v160\n\t"
      "v_mov_b32_e32 v200, 0xff800000\n\t"
      "v_mfma_f32_16x16x32_bf16 v[12:15], v[136:139], v[128:131], v[12:15]\n\t"
      "ds_read_b128 v[100:103], v160 offset:14336\n\t"
      "s_cmp_lt_u32 s25, 2\n\t"
      "v_mfma_f32_16x16x32_bf16 v[24:27], v[140:143], v[124:127], v[24:27]\n\t"
      "ds_read_b128 v[92:95], v162\n\t"
      "s_cselect_b32 s35, 0xff800000, 0\n\t"
      "v_mfma_f32_16x16x32_bf16 v[32:35], v[140:143], v[128:131], v[32:35]\n\t"
      "ds_read_b64_tr_b16 v[108:109], v167\n\t"
      "s_bitcmp0_b32 s24, 0\n\t"
      "v_mfma_f32_16x16x32_bf16 v[16:19], v[140:143], v[120:123], v[16:19]\n\t"
      "ds_read_b64_tr_b16 v[110:111], v167 offset:512\n\t"
      "s_cselect_b32 s35, s35, 0\n\t"
      "v_mfma_f32_16x16x32_bf16 v[28:31], v[144:147], v[124:127], v[28:31]\n\t"
      "v_cmp_ge_i32_e32 vcc, -9, v199\n\t"
      "v_mfma_f32_16x16x32_bf16 v[36:39], v[144:147], v[128:131], v[36:39]\n\t"
      "v_subrev_u32_e32 v201, 0, v199\n\t"
      "v_mfma_f32_16x16x32_bf16 v[20:23], v[144:147], v[120:123], v[20:23]\n\t"
      "v_cmp_gt_u32_e64 s[64:65], 8, v201\n\t"
      "s_waitcnt lgkmcnt(7)\n\t"
      "v_mfma_f32_16x16x32_bf16 v[0:3], v[76:79], v[68:71], v[0:3]\n\t"
      "ds_read_b64_tr_b16 v[132:133], v166 offset:1024\n\t"
      "v_cndmask_b32_e64 v172, v200, 0, vcc\n\t"
      "v_mfma_f32_16x16x32_bf16 v[8:11], v[76:79], v[72:75], v[8:11]\n\t"
      "ds_read_b64_tr_b16 v[134:135], v166 offset:1536\n\t"
      "v_cndmask_b32_e64 v176, v200, 0, s[64:65]\n\t"
      "v_mfma_f32_16x16x32_bf16 v[4:7], v[80:83], v[68:71], v[4:7]\n\t"
      "ds_read_b128 v[124:127], v161 offset:1024\n\t"
      "v_or_b32_e32 v180, s35, v172\n\t"
      "v_mfma_f32_16x16x32_bf16 v[12:15], v[80:83], v[72:75], v[12:15]\n\t"
      "ds_read_b128 v[128:131], v161 offset:15360\n\t"
      "v_cmp_ge_i32_e32 vcc, -8, v199\n\t"
      "v_mfma_f32_16x16x32_bf16 v[24:27], v[84:87], v[68:71], v[24:27]\n\t"
      "ds_read_b128 v[120:123], v163 offset:1024\n\t"
      "v_subrev_u32_e32 v201, 1, v199\n\t"
      "v_mfma_f32_16x16x32_bf16 v[32:35], v[84:87], v[72:75], v[32:35]\n\t"
      "ds_read_b64_tr_b16 v[136:137], v167 offset:1024\n\t"
      "v_cmp_gt_u32_e64 s[64:65], 8, v201\n\t"
      "v_mfma_f32_16x16x32_bf16 v[16:19], v[84:87], v[64:67], v[16:19]\n\t"
      "ds_read_b64_tr_b16 v[138:139], v167 offset:1536\n\t"
      "v_cndmask_b32_e64 v173, v200, 0, vcc\n\t"
      "v_mfma_f32_16x16x32_bf16 v[28:31], v[88:91], v[68:71], v[28:31]\n\t"
      "v_cndmask_b32_e64 v177, v200, 0, s[64:65]\n\t"
      "v_mfma_f32_16x16x32_bf16 v[36:39], v[88:91], v[72:75], v[36:39]\n\t"
      "v_or_b32_e32 v181, s35, v173\n\t"
      "v_mfma_f32_16x16x32_bf16 v[20:23], v[88:91], v[64:67], v[20:23]\n\t"
      "v_cmp_ge_i32_e32 vcc, -7, v199\n\t"
      "s_waitcnt lgkmcnt(7)\n\t"
      "v_mfma_f32_16x16x32_bf16 v[48:51], v[96:99], v[104:107], 0\n\t"
      "ds_read_b64_tr_b16 v[76:77], v166 offset:2048\n\t"
      "ds_read_b64_tr_b16 v[78:79], v166 offset:2560\n\t"
      "v_subrev_u32_e32 v201, 2, v199\n\t"
      "v_mfma_f32_16x16x32_bf16 v[56:59], v[100:103], v[104:107], 0\n\t"
      "ds_read_b128 v[68:71], v160 offset:2048\n\t"
      "v_cmp_gt_u32_e64 s[64:65], 8, v201\n\t"
      "v_mfma_f32_16x16x32_bf16 v[40:43], v[92:95], v[104:107], 0\n\t"
      "ds_read_b128 v[72:75], v160 offset:16384\n\t"
      "v_cndmask_b32_e64 v174, v200, 0, vcc\n\t"
      "v_mfma_f32_16x16x32_bf16 v[52:55], v[96:99], v[108:111], 0\n\t"
      "ds_read_b128 v[64:67], v162 offset:2048\n\t"
      "v_cndmask_b32_e64 v178, v200, 0, s[64:65]\n\t"
      "v_mfma_f32_16x16x32_bf16 v[60:63], v[100:103], v[108:111], 0\n\t"
      "ds_read_b64_tr_b16 v[80:81], v167 offset:2048\n\t"
      "v_or_b32_e32 v182, s35, v174\n\t"
      "v_mfma_f32_16x16x32_bf16 v[44:47], v[92:95], v[108:111], 0\n\t"
      "ds_read_b64_tr_b16 v[82:83], v167 offset:2560\n\t"
      "v_cmp_ge_i32_e32 vcc, -6, v199\n\t"
      "s_waitcnt lgkmcnt(7)\n\t"
      "v_mfma_f32_16x16x32_bf16 v[48:51], v[124:127], v[132:135], v[48:51]\n\t"
      "ds_read_b64_tr_b16 v[104:105], v166 offset:3072\n\t"
      "ds_read_b64_tr_b16 v[106:107], v166 offset:3584\n\t"
      "v_subrev_u32_e32 v201, 3, v199\n\t"
      "v_mfma_f32_16x16x32_bf16 v[56:59], v[128:131], v[132:135], v[56:59]\n\t"
      "ds_read_b128 v[96:99], v161 offset:3072\n\t"
      "v_cmp_gt_u32_e64 s[64:65], 8, v201\n\t"
      "v_mfma_f32_16x16x32_bf16 v[40:43], v[120:123], v[132:135], v[40:43]\n\t"
      "ds_read_b128 v[100:103], v161 offset:17408\n\t"
      "v_cndmask_b32_e64 v175, v200, 0, vcc\n\t"
      "v_mfma_f32_16x16x32_bf16 v[52:55], v[124:127], v[136:139], v[52:55]\n\t"
      "ds_read_b128 v[92:95], v163 offset:3072\n\t"
      "v_cndmask_b32_e64 v179, v200, 0, s[64:65]\n\t"
      "v_mfma_f32_16x16x32_bf16 v[60:63], v[128:131], v[136:139], v[60:63]\n\t"
      "ds_read_b64_tr_b16 v[108:109], v167 offset:3072\n\t"
      "v_or_b32_e32 v183, s35, v175\n\t"
      "v_mfma_f32_16x16x32_bf16 v[44:47], v[120:123], v[136:139], v[44:47]\n\t"
      "ds_read_b64_tr_b16 v[110:111], v167 offset:3584\n\t"
      "v_lshrrev_b32_e32 v201, 31, v172\n\t"
      "s_barrier\n\t"
      "s_waitcnt lgkmcnt(7)\n\t"
      "v_mfma_f32_16x16x32_bf16 v[48:51], v[68:71], v[76:79], v[48:51]\n\t"
      "ds_read_b64_tr_b16 v[132:133], v166 offset:16384\n\t"
      "ds_read_b64_tr_b16 v[134:135], v166 offset:16896\n\t"
      "ds_read_b128 v[124:127], v160 offset:4096\n\t"
      "ds_read_b128 v[128:131], v160 offset:18432\n\t"
      "ds_read_b64_tr_b16 v[136:137], v167 offset:16384\n\t"
      "ds_read_b64_tr_b16 v[138:139], v167 offset:16896\n\t"
      "v_lshrrev_b32_e32 v202, 31, v173\n\t"
      "v_mfma_f32_16x16x32_bf16 v[56:59], v[72:75], v[76:79], v[56:59]\n\t"
      "ds_read_b64_tr_b16 v[140:141], v166 offset:17408\n\t"
      "v_lshl_or_b32 v201, v202, 16, v201\n\t"
      "v_mfma_f32_16x16x32_bf16 v[40:43], v[64:67], v[76:79], v[40:43]\n\t"
      "ds_read_b64_tr_b16 v[142:143], v166 offset:17920\n\t"
      "v_mul_u32_u24_e32 v184, 0xffff, v201\n\t"
      "v_mfma_f32_16x16x32_bf16 v[52:55], v[68:71], v[80:83], v[52:55]\n\t"
      "ds_read_b128 v[120:123], v162 offset:4096\n\t"
      "v_lshrrev_b32_e32 v201, 31, v174\n\t"
      "v_mfma_f32_16x16x32_bf16 v[60:63], v[72:75], v[80:83], v[60:63]\n\t"
      "ds_read_b64_tr_b16 v[144:145], v167 offset:17408\n\t"
      "v_lshrrev_b32_e32 v202, 31, v175\n\t"
      "v_mfma_f32_16x16x32_bf16 v[44:47], v[64:67], v[80:83], v[44:47]\n\t"
      "ds_read_b64_tr_b16 v[146:147], v167 offset:17920\n\t"
      "v_lshl_or_b32 v201, v202, 16, v201\n\t"
      "s_waitcnt lgkmcnt(11)\n\t"
      "v_mfma_f32_16x16x32_bf16 v[48:51], v[96:99], v[104:107], v[48:51]\n\t"
      "ds_read_b64_tr_b16 v[76:77], v166 offset:18432\n\t"
      "ds_read_b64_tr_b16 v[78:79], v166 offset:18944\n\t"
      "ds_read_b128 v[68:71], v161 offset:5120\n\t"
      "ds_read_b128 v[72:75], v161 offset:19456\n\t"
      "ds_read_b64_tr_b16 v[80:81], v167 offset:18432\n\t"
      "ds_read_b64_tr_b16 v[82:83], v167 offset:18944\n\t"
      "v_mul_u32_u24_e32 v185, 0xffff, v201\n\t"
      "v_mfma_f32_16x16x32_bf16 v[56:59], v[100:103], v[104:107], v[56:59]\n\t"
      "ds_read_b64_tr_b16 v[84:85], v166 offset:19456\n\t"
      "s_mov_b32 s68, 0x0\n\t"
      "v_mfma_f32_16x16x32_bf16 v[40:43], v[92:95], v[104:107], v[40:43]\n\t"
      "ds_read_b64_tr_b16 v[86:87], v166 offset:19968\n\t"
      "s_mov_b32 s69, 0xffff0000\n\t"
      "v_mfma_f32_16x16x32_bf16 v[52:55], v[96:99], v[108:111], v[52:55]\n\t"
      "ds_read_b128 v[64:67], v163 offset:5120\n\t"
      "s_mov_b32 s70, 0xffff\n\t"
      "v_mfma_f32_16x16x32_bf16 v[60:63], v[100:103], v[108:111], v[60:63]\n\t"
      "ds_read_b64_tr_b16 v[88:89], v167 offset:19456\n\t"
      "s_mov_b32 s71, 0x0\n\t"
      "v_mfma_f32_16x16x32_bf16 v[44:47], v[92:95], v[108:111], v[44:47]\n\t"
      "ds_read_b64_tr_b16 v[90:91], v167 offset:19968\n\t"
      "v_lshlrev_b32_e32 v231, 3, v192\n\t"
      "s_barrier\n\t"
      "s_waitcnt lgkmcnt(11)\n\t"
      "v_mfma_f32_16x16x32_bf16 v[0:3], v[132:135], v[124:127], v[0:3]\n\t"
      "ds_read_b64_tr_b16 v[104:105], v166 offset:32768\n\t"
      "ds_read_b64_tr_b16 v[106:107], v166 offset:33280\n\t"
      "s_lshl_b32 s34, s25, 12\n\t"
      "v_mfma_f32_16x16x32_bf16 v[8:11], v[132:135], v[128:131], v[8:11]\n\t"
      "ds_read_b128 v[96:99], v160 offset:6144\n\t"
      "s_lshr_b32 s35, s39, 2\n\t"
      "v_mfma_f32_16x16x32_bf16 v[4:7], v[136:139], v[124:127], v[4:7]\n\t"
      "ds_read_b128 v[100:103], v160 offset:20480\n\t"
      "s_add_u32 s34, s34, s35\n\t"
      "v_mfma_f32_16x16x32_bf16 v[12:15], v[136:139], v[128:131], v[12:15]\n\t"
      "ds_read_b64_tr_b16 v[108:109], v167 offset:32768\n\t"
      "s_add_u32 s34, s34, 12582912\n\t"
      "v_mfma_f32_16x16x32_bf16 v[24:27], v[140:143], v[124:127], v[24:27]\n\t"
      "ds_read_b64_tr_b16 v[110:111], v167 offset:33280\n\t"
      "s_add_u32 s46, s20, s34\n\t"
      "v_mfma_f32_16x16x32_bf16 v[32:35], v[140:143], v[128:131], v[32:35]\n\t"
      "ds_read_b64_tr_b16 v[112:113], v166 offset:33792\n\t"
      "s_addc_u32 s47, s21, 0\n\t"
      "v_mfma_f32_16x16x32_bf16 v[16:19], v[140:143], v[120:123], v[16:19]\n\t"
      "ds_read_b64_tr_b16 v[114:115], v166 offset:34304\n\t"
      "s_lshl_b32 s34, s28, 7\n\t"
      "v_mfma_f32_16x16x32_bf16 v[28:31], v[144:147], v[124:127], v[28:31]\n\t"
      "ds_read_b128 v[92:95], v162 offset:6144\n\t"
      "s_add_u32 s58, s46, s34\n\t"
      "v_mfma_f32_16x16x32_bf16 v[36:39], v[144:147], v[128:131], v[36:39]\n\t"
      "ds_read_b64_tr_b16 v[116:117], v167 offset:33792\n\t"
      "s_addc_u32 s59, s47, 0\n\t"
      "v_mfma_f32_16x16x32_bf16 v[20:23], v[144:147], v[120:123], v[20:23]\n\t"
      "ds_read_b64_tr_b16 v[118:119], v167 offset:34304\n\t"
      "s_and_b32 s34, s24, 1\n\t"
      "s_waitcnt lgkmcnt(11)\n\t"
      "v_mfma_f32_16x16x32_bf16 v[0:3], v[76:79], v[68:71], v[0:3]\n\t"
      "ds_read_b64_tr_b16 v[132:133], v166 offset:34816\n\t"
      "ds_read_b64_tr_b16 v[134:135], v166 offset:35328\n\t"
      "s_lshl1_add_u32 s34, s25, s34\n\t"
      "v_mfma_f32_16x16x32_bf16 v[8:11], v[76:79], v[72:75], v[8:11]\n\t"
      "ds_read_b128 v[124:127], v161 offset:7168\n\t"
      "s_lshl_b32 s34, s34, 5\n\t"
      "v_mfma_f32_16x16x32_bf16 v[4:7], v[80:83], v[68:71], v[4:7]\n\t"
      "ds_read_b128 v[128:131], v161 offset:21504\n\t"
      "s_lshl1_add_u32 s34, s26, s34\n\t"
      "v_mfma_f32_16x16x32_bf16 v[12:15], v[80:83], v[72:75], v[12:15]\n\t"
      "ds_read_b64_tr_b16 v[136:137], v167 offset:34816\n\t"
      "s_bfe_u32 s35, s24, 65537\n\t"
      "v_mfma_f32_16x16x32_bf16 v[24:27], v[84:87], v[68:71], v[24:27]\n\t"
      "ds_read_b64_tr_b16 v[138:139], v167 offset:35328\n\t"
      "s_add_u32 s34, s34, s35\n\t"
      "v_mfma_f32_16x16x32_bf16 v[32:35], v[84:87], v[72:75], v[32:35]\n\t"
      "ds_read_b64_tr_b16 v[140:141], v166 offset:35840\n\t"
      "s_lshl_b32 s34, s34, 10\n\t"
      "v_mfma_f32_16x16x32_bf16 v[16:19], v[84:87], v[64:67], v[16:19]\n\t"
      "ds_read_b64_tr_b16 v[142:143], v166 offset:36352\n\t"
      "s_add_u32 s42, s20, s34\n\t"
      "v_mfma_f32_16x16x32_bf16 v[28:31], v[88:91], v[68:71], v[28:31]\n\t"
      "ds_read_b128 v[120:123], v165 offset:7168\n\t"
      "s_addc_u32 s43, s21, 0\n\t"
      "v_mfma_f32_16x16x32_bf16 v[36:39], v[88:91], v[72:75], v[36:39]\n\t"
      "ds_read_b64_tr_b16 v[144:145], v167 offset:35840\n\t"
      "s_add_u32 s44, s42, 16384\n\t"
      "v_mfma_f32_16x16x32_bf16 v[20:23], v[88:91], v[64:67], v[20:23]\n\t"
      "ds_read_b64_tr_b16 v[146:147], v167 offset:36352\n\t"
      "s_addc_u32 s45, s43, 0\n\t"
      "s_barrier\n\t"
      "s_waitcnt lgkmcnt(11)\n\t"
      "v_mfma_f32_16x16x32_bf16 v[0:3], v[104:107], v[96:99], v[0:3]\n\t"
      "ds_read_b64_tr_b16 v[76:77], v166 offset:49152\n\t"
      "ds_read_b64_tr_b16 v[78:79], v166 offset:49664\n\t"
      "v_and_b32_e32 v199, 60, v192\n\t"
      "v_mfma_f32_16x16x32_bf16 v[8:11], v[104:107], v[100:103], v[8:11]\n\t"
      "ds_read_b128 v[68:71], v160 offset:8192\n\t"
      "v_and_b32_e32 v200, 24, v192\n\t"
      "v_mfma_f32_16x16x32_bf16 v[4:7], v[108:111], v[96:99], v[4:7]\n\t"
      "ds_read_b128 v[72:75], v160 offset:22528\n\t"
      "v_lshlrev_b32_e32 v202, 3, v196\n\t"
      "v_mfma_f32_16x16x32_bf16 v[12:15], v[108:111], v[100:103], v[12:15]\n\t"
      "ds_read_b64_tr_b16 v[80:81], v167 offset:49152\n\t"
      "v_lshl_or_b32 v202, v200, 2, v202\n\t"
      "v_mfma_f32_16x16x32_bf16 v[24:27], v[112:115], v[96:99], v[24:27]\n\t"
      "ds_read_b64_tr_b16 v[82:83], v167 offset:49664\n\t"
      "v_lshl_or_b32 v201, v199, 5, v202\n\t"
      "v_mfma_f32_16x16x32_bf16 v[32:35], v[112:115], v[100:103], v[32:35]\n\t"
      "ds_read_b64_tr_b16 v[84:85], v166 offset:50176\n\t"
      "v_add_u32_e32 v216, 106496, v201\n\t"
      "v_mfma_f32_16x16x32_bf16 v[16:19], v[112:115], v[92:95], v[16:19]\n\t"
      "ds_read_b64_tr_b16 v[86:87], v166 offset:50688\n\t"
      "v_mov_b32_e32 v199, 2048\n\t"
      "v_mfma_f32_16x16x32_bf16 v[28:31], v[116:119], v[96:99], v[28:31]\n\t"
      "ds_read_b128 v[64:67], v164 offset:8192\n\t"
      "v_cndmask_b32_e64 v199, v199, 0, s[68:69]\n\t"
      "v_mfma_f32_16x16x32_bf16 v[36:39], v[116:119], v[100:103], v[36:39]\n\t"
      "ds_read_b64_tr_b16 v[88:89], v167 offset:50176\n\t"
      "v_add_u32_e32 v220, v216, v199\n\t"
      "v_mfma_f32_16x16x32_bf16 v[20:23], v[116:119], v[92:95], v[20:23]\n\t"
      "ds_read_b64_tr_b16 v[90:91], v167 offset:50688\n\t"
      "v_xor_b32_e32 v217, 32, v216\n\t"
      "s_waitcnt lgkmcnt(11)\n\t"
      "v_mfma_f32_16x16x32_bf16 v[0:3], v[132:135], v[124:127], v[0:3]\n\t"
      "ds_read_b64_tr_b16 v[104:105], v166 offset:51200\n\t"
      "ds_read_b64_tr_b16 v[106:107], v166 offset:51712\n\t"
      "v_xor_b32_e32 v218, 64, v216\n\t"
      "v_mfma_f32_16x16x32_bf16 v[8:11], v[132:135], v[128:131], v[8:11]\n\t"
      "ds_read_b128 v[96:99], v161 offset:9216\n\t"
      "v_xor_b32_e32 v219, 96, v216\n\t"
      "v_mfma_f32_16x16x32_bf16 v[4:7], v[136:139], v[124:127], v[4:7]\n\t"
      "ds_read_b128 v[100:103], v161 offset:23552\n\t"
      "v_xor_b32_e32 v221, 32, v220\n\t"
      "v_mfma_f32_16x16x32_bf16 v[12:15], v[136:139], v[128:131], v[12:15]\n\t"
      "ds_read_b64_tr_b16 v[108:109], v167 offset:51200\n\t"
      "v_xor_b32_e32 v222, 64, v220\n\t"
      "v_mfma_f32_16x16x32_bf16 v[24:27], v[140:143], v[124:127], v[24:27]\n\t"
      "ds_read_b64_tr_b16 v[110:111], v167 offset:51712\n\t"
      "v_xor_b32_e32 v223, 96, v220\n\t"
      "v_mfma_f32_16x16x32_bf16 v[32:35], v[140:143], v[128:131], v[32:35]\n\t"
      "ds_read_b64_tr_b16 v[112:113], v166 offset:52224\n\t"
      "s_lshr_b32 s34, s39, 1\n\t"
      "v_mfma_f32_16x16x32_bf16 v[16:19], v[140:143], v[120:123], v[16:19]\n\t"
      "ds_read_b64_tr_b16 v[114:115], v166 offset:52736\n\t"
      "s_add_u32 s34, s34, 98304\n\t"
      "v_mfma_f32_16x16x32_bf16 v[28:31], v[144:147], v[124:127], v[28:31]\n\t"
      "ds_read_b128 v[92:95], v165 offset:9216\n\t"
      "v_lshrrev_b32_e32 v199, 1, v193\n\t"
      "v_mfma_f32_16x16x32_bf16 v[36:39], v[144:147], v[128:131], v[36:39]\n\t"
      "ds_read_b64_tr_b16 v[116:117], v167 offset:52224\n\t"
      "v_xor_b32_e32 v199, v199, v198\n\t"
      "v_mfma_f32_16x16x32_bf16 v[20:23], v[144:147], v[120:123], v[20:23]\n\t"
      "ds_read_b64_tr_b16 v[118:119], v167 offset:52736\n\t"
      "v_lshlrev_b32_e32 v199, 4, v199\n\t"
      "s_barrier\n\t"
      "s_waitcnt lgkmcnt(11)\n\t"
      "v_mfma_f32_16x16x32_bf16 v[0:3], v[76:79], v[68:71], v[0:3]\n\t"
      "ds_read_b64_tr_b16 v[132:133], v168\n\t"
      "ds_read_b64_tr_b16 v[134:135], v168 offset:512\n\t"
      "v_lshl_or_b32 v199, v194, 7, v199\n\t"
      "v_mfma_f32_16x16x32_bf16 v[8:11], v[76:79], v[72:75], v[8:11]\n\t"
      "ds_read_b128 v[124:127], v160 offset:10240\n\t"
      "v_and_b32_e32 v200, 1, v193\n\t"
      "v_mfma_f32_16x16x32_bf16 v[4:7], v[80:83], v[68:71], v[4:7]\n\t"
      "ds_read_b128 v[128:131], v160 offset:24576\n\t"
      "v_lshl_or_b32 v199, v200, 3, v199\n\t"
      "v_mfma_f32_16x16x32_bf16 v[12:15], v[80:83], v[72:75], v[12:15]\n\t"
      "ds_read_b64_tr_b16 v[136:137], v169\n\t"
      "v_add_u32_e32 v228, s34, v199\n\t"
      "v_mfma_f32_16x16x32_bf16 v[24:27], v[84:87], v[68:71], v[24:27]\n\t"
      "ds_read_b64_tr_b16 v[138:139], v169 offset:512\n\t"
      "v_mfma_f32_16x16x32_bf16 v[32:35], v[84:87], v[72:75], v[32:35]\n\t"
      "ds_read_b64_tr_b16 v[140:141], v168 offset:1024\n\t"
      "v_mfma_f32_16x16x32_bf16 v[16:19], v[84:87], v[64:67], v[16:19]\n\t"
      "ds_read_b64_tr_b16 v[142:143], v168 offset:1536\n\t"
      "v_mfma_f32_16x16x32_bf16 v[28:31], v[88:91], v[68:71], v[28:31]\n\t"
      "ds_read_b128 v[120:123], v164 offset:10240\n\t"
      "v_mfma_f32_16x16x32_bf16 v[36:39], v[88:91], v[72:75], v[36:39]\n\t"
      "ds_read_b64_tr_b16 v[144:145], v169 offset:1024\n\t"
      "v_mfma_f32_16x16x32_bf16 v[20:23], v[88:91], v[64:67], v[20:23]\n\t"
      "ds_read_b64_tr_b16 v[146:147], v169 offset:1536\n\t"
      "s_waitcnt lgkmcnt(11)\n\t"
      "v_mfma_f32_16x16x32_bf16 v[0:3], v[104:107], v[96:99], v[0:3]\n\t"
      "ds_read_b64_tr_b16 v[76:77], v168 offset:2048\n\t"
      "ds_read_b64_tr_b16 v[78:79], v168 offset:2560\n\t"
      "v_mfma_f32_16x16x32_bf16 v[8:11], v[104:107], v[100:103], v[8:11]\n\t"
      "ds_read_b128 v[68:71], v161 offset:11264\n\t"
      "v_mfma_f32_16x16x32_bf16 v[4:7], v[108:111], v[96:99], v[4:7]\n\t"
      "ds_read_b128 v[72:75], v161 offset:25600\n\t"
      "v_mfma_f32_16x16x32_bf16 v[12:15], v[108:111], v[100:103], v[12:15]\n\t"
      "ds_read_b64_tr_b16 v[80:81], v169 offset:2048\n\t"
      "v_mfma_f32_16x16x32_bf16 v[24:27], v[112:115], v[96:99], v[24:27]\n\t"
      "ds_read_b64_tr_b16 v[82:83], v169 offset:2560\n\t"
      "v_mfma_f32_16x16x32_bf16 v[32:35], v[112:115], v[100:103], v[32:35]\n\t"
      "ds_read_b64_tr_b16 v[84:85], v168 offset:3072\n\t"
      "v_mfma_f32_16x16x32_bf16 v[16:19], v[112:115], v[92:95], v[16:19]\n\t"
      "ds_read_b64_tr_b16 v[86:87], v168 offset:3584\n\t"
      "v_mfma_f32_16x16x32_bf16 v[28:31], v[116:119], v[96:99], v[28:31]\n\t"
      "ds_read_b128 v[64:67], v165 offset:11264\n\t"
      "v_mfma_f32_16x16x32_bf16 v[36:39], v[116:119], v[100:103], v[36:39]\n\t"
      "ds_read_b64_tr_b16 v[88:89], v169 offset:3072\n\t"
      "v_mfma_f32_16x16x32_bf16 v[20:23], v[116:119], v[92:95], v[20:23]\n\t"
      "ds_read_b64_tr_b16 v[90:91], v169 offset:3584\n\t"
      "s_barrier\n\t"
      "s_waitcnt lgkmcnt(11)\n\t"
      "v_mfma_f32_16x16x32_bf16 v[0:3], v[132:135], v[124:127], v[0:3]\n\t"
      "ds_read_b64_tr_b16 v[104:105], v168 offset:16384\n\t"
      "ds_read_b64_tr_b16 v[106:107], v168 offset:16896\n\t"
      "v_mfma_f32_16x16x32_bf16 v[8:11], v[132:135], v[128:131], v[8:11]\n\t"
      "ds_read_b128 v[96:99], v160 offset:12288\n\t"
      "v_mfma_f32_16x16x32_bf16 v[4:7], v[136:139], v[124:127], v[4:7]\n\t"
      "ds_read_b128 v[100:103], v160 offset:26624\n\t"
      "v_mfma_f32_16x16x32_bf16 v[12:15], v[136:139], v[128:131], v[12:15]\n\t"
      "ds_read_b64_tr_b16 v[108:109], v169 offset:16384\n\t"
      "v_mfma_f32_16x16x32_bf16 v[24:27], v[140:143], v[124:127], v[24:27]\n\t"
      "ds_read_b64_tr_b16 v[110:111], v169 offset:16896\n\t"
      "v_mfma_f32_16x16x32_bf16 v[32:35], v[140:143], v[128:131], v[32:35]\n\t"
      "ds_read_b64_tr_b16 v[112:113], v168 offset:17408\n\t"
      "v_mfma_f32_16x16x32_bf16 v[16:19], v[140:143], v[120:123], v[16:19]\n\t"
      "ds_read_b64_tr_b16 v[114:115], v168 offset:17920\n\t"
      "v_mfma_f32_16x16x32_bf16 v[28:31], v[144:147], v[124:127], v[28:31]\n\t"
      "ds_read_b128 v[92:95], v164 offset:12288\n\t"
      "v_mfma_f32_16x16x32_bf16 v[36:39], v[144:147], v[128:131], v[36:39]\n\t"
      "ds_read_b64_tr_b16 v[116:117], v169 offset:17408\n\t"
      "v_mfma_f32_16x16x32_bf16 v[20:23], v[144:147], v[120:123], v[20:23]\n\t"
      "ds_read_b64_tr_b16 v[118:119], v169 offset:17920\n\t"
      "s_waitcnt lgkmcnt(11)\n\t"
      "v_mfma_f32_16x16x32_bf16 v[0:3], v[76:79], v[68:71], v[0:3]\n\t"
      "ds_read_b64_tr_b16 v[132:133], v168 offset:18432\n\t"
      "ds_read_b64_tr_b16 v[134:135], v168 offset:18944\n\t"
      "v_mfma_f32_16x16x32_bf16 v[8:11], v[76:79], v[72:75], v[8:11]\n\t"
      "ds_read_b128 v[124:127], v161 offset:13312\n\t"
      "v_mfma_f32_16x16x32_bf16 v[4:7], v[80:83], v[68:71], v[4:7]\n\t"
      "ds_read_b128 v[128:131], v161 offset:27648\n\t"
      "v_mfma_f32_16x16x32_bf16 v[12:15], v[80:83], v[72:75], v[12:15]\n\t"
      "ds_read_b64_tr_b16 v[136:137], v169 offset:18432\n\t"
      "v_mfma_f32_16x16x32_bf16 v[24:27], v[84:87], v[68:71], v[24:27]\n\t"
      "ds_read_b64_tr_b16 v[138:139], v169 offset:18944\n\t"
      "v_mfma_f32_16x16x32_bf16 v[32:35], v[84:87], v[72:75], v[32:35]\n\t"
      "ds_read_b64_tr_b16 v[140:141], v168 offset:19456\n\t"
      "v_mfma_f32_16x16x32_bf16 v[16:19], v[84:87], v[64:67], v[16:19]\n\t"
      "ds_read_b64_tr_b16 v[142:143], v168 offset:19968\n\t"
      "v_mfma_f32_16x16x32_bf16 v[28:31], v[88:91], v[68:71], v[28:31]\n\t"
      "ds_read_b128 v[120:123], v165 offset:13312\n\t"
      "v_mfma_f32_16x16x32_bf16 v[36:39], v[88:91], v[72:75], v[36:39]\n\t"
      "ds_read_b64_tr_b16 v[144:145], v169 offset:19456\n\t"
      "v_mfma_f32_16x16x32_bf16 v[20:23], v[88:91], v[64:67], v[20:23]\n\t"
      "ds_read_b64_tr_b16 v[146:147], v169 offset:19968\n\t"
      "s_barrier\n\t"
      "s_waitcnt lgkmcnt(11)\n\t"
      "v_mfma_f32_16x16x32_bf16 v[0:3], v[104:107], v[96:99], v[0:3]\n\t"
      "ds_read_b64_tr_b16 v[76:77], v166\n\t"
      "v_mfma_f32_16x16x32_bf16 v[8:11], v[104:107], v[100:103], v[8:11]\n\t"
      "ds_read_b64_tr_b16 v[78:79], v166 offset:512\n\t"
      "v_mfma_f32_16x16x32_bf16 v[4:7], v[108:111], v[96:99], v[4:7]\n\t"
      "ds_read_b128 v[68:71], v160 offset:4096\n\t"
      "v_mfma_f32_16x16x32_bf16 v[12:15], v[108:111], v[100:103], v[12:15]\n\t"
      "ds_read_b128 v[72:75], v160 offset:18432\n\t"
      "v_mfma_f32_16x16x32_bf16 v[24:27], v[112:115], v[96:99], v[24:27]\n\t"
      "ds_read_b128 v[64:67], v162 offset:4096\n\t"
      "v_mfma_f32_16x16x32_bf16 v[32:35], v[112:115], v[100:103], v[32:35]\n\t"
      "ds_read_b64_tr_b16 v[80:81], v167\n\t"
      "v_mfma_f32_16x16x32_bf16 v[16:19], v[112:115], v[92:95], v[16:19]\n\t"
      "ds_read_b64_tr_b16 v[82:83], v167 offset:512\n\t"
      "v_mfma_f32_16x16x32_bf16 v[28:31], v[116:119], v[96:99], v[28:31]\n\t"
      "v_mfma_f32_16x16x32_bf16 v[36:39], v[116:119], v[100:103], v[36:39]\n\t"
      "v_mfma_f32_16x16x32_bf16 v[20:23], v[116:119], v[92:95], v[20:23]\n\t"
      "s_waitcnt lgkmcnt(7)\n\t"
      "v_mfma_f32_16x16x32_bf16 v[0:3], v[132:135], v[124:127], v[0:3]\n\t"
      "ds_read_b64_tr_b16 v[104:105], v166 offset:1024\n\t"
      "v_mfma_f32_16x16x32_bf16 v[8:11], v[132:135], v[128:131], v[8:11]\n\t"
      "ds_read_b64_tr_b16 v[106:107], v166 offset:1536\n\t"
      "v_mfma_f32_16x16x32_bf16 v[4:7], v[136:139], v[124:127], v[4:7]\n\t"
      "ds_read_b128 v[96:99], v161 offset:5120\n\t"
      "v_mfma_f32_16x16x32_bf16 v[12:15], v[136:139], v[128:131], v[12:15]\n\t"
      "ds_read_b128 v[100:103], v161 offset:19456\n\t"
      "v_mfma_f32_16x16x32_bf16 v[24:27], v[140:143], v[124:127], v[24:27]\n\t"
      "ds_read_b128 v[92:95], v163 offset:5120\n\t"
      "v_mfma_f32_16x16x32_bf16 v[32:35], v[140:143], v[128:131], v[32:35]\n\t"
      "ds_read_b64_tr_b16 v[108:109], v167 offset:1024\n\t"
      "v_mfma_f32_16x16x32_bf16 v[16:19], v[140:143], v[120:123], v[16:19]\n\t"
      "ds_read_b64_tr_b16 v[110:111], v167 offset:1536\n\t"
      "v_mfma_f32_16x16x32_bf16 v[28:31], v[144:147], v[124:127], v[28:31]\n\t"
      "v_mfma_f32_16x16x32_bf16 v[36:39], v[144:147], v[128:131], v[36:39]\n\t"
      "v_mfma_f32_16x16x32_bf16 v[20:23], v[144:147], v[120:123], v[20:23]\n\t"
      "s_waitcnt lgkmcnt(7)\n\t"
      "v_mfma_f32_16x16x32_bf16 v[48:51], v[68:71], v[76:79], v[48:51]\n\t"
      "ds_read_b64_tr_b16 v[132:133], v166 offset:2048\n\t"
      "ds_read_b64_tr_b16 v[134:135], v166 offset:2560\n\t"
      "v_mfma_f32_16x16x32_bf16 v[56:59], v[72:75], v[76:79], v[56:59]\n\t"
      "ds_read_b128 v[124:127], v160 offset:6144\n\t"
      "v_mfma_f32_16x16x32_bf16 v[40:43], v[64:67], v[76:79], v[40:43]\n\t"
      "ds_read_b128 v[128:131], v160 offset:20480\n\t"
      "v_cvt_pk_bf16_f32 v84, v0, v1\n\t"
      "v_cvt_pk_bf16_f32 v85, v2, v3\n\t"
      "v_cvt_pk_bf16_f32 v86, v4, v5\n\t"
      "v_mfma_f32_16x16x32_bf16 v[52:55], v[68:71], v[80:83], v[52:55]\n\t"
      "ds_read_b128 v[120:123], v162 offset:6144\n\t"
      "v_cvt_pk_bf16_f32 v87, v6, v7\n\t"
      "v_cvt_pk_bf16_f32 v88, v8, v9\n\t"
      "v_cvt_pk_bf16_f32 v89, v10, v11\n\t"
      "v_mfma_f32_16x16x32_bf16 v[60:63], v[72:75], v[80:83], v[60:63]\n\t"
      "ds_read_b64_tr_b16 v[136:137], v167 offset:2048\n\t"
      "v_cvt_pk_bf16_f32 v90, v12, v13\n\t"
      "v_cvt_pk_bf16_f32 v91, v14, v15\n\t"
      "v_cvt_pk_bf16_f32 v152, v24, v25\n\t"
      "v_mfma_f32_16x16x32_bf16 v[44:47], v[64:67], v[80:83], v[44:47]\n\t"
      "ds_read_b64_tr_b16 v[138:139], v167 offset:2560\n\t"
      "v_cvt_pk_bf16_f32 v153, v26, v27\n\t"
      "v_cvt_pk_bf16_f32 v154, v28, v29\n\t"
      "v_cvt_pk_bf16_f32 v155, v30, v31\n\t"
      "s_waitcnt lgkmcnt(7)\n\t"
      "v_mfma_f32_16x16x32_bf16 v[48:51], v[96:99], v[104:107], v[48:51]\n\t"
      "ds_read_b64_tr_b16 v[76:77], v166 offset:3072\n\t"
      "ds_read_b64_tr_b16 v[78:79], v166 offset:3584\n\t"
      "v_cndmask_b32_e64 v86, v86, 0, s[68:69]\n\t"
      "v_cndmask_b32_e64 v87, v87, 0, s[68:69]\n\t"
      "v_cvt_pk_bf16_f32 v156, v32, v33\n\t"
      "v_mfma_f32_16x16x32_bf16 v[56:59], v[100:103], v[104:107], v[56:59]\n\t"
      "ds_read_b128 v[68:71], v161 offset:7168\n\t"
      "v_cvt_pk_bf16_f32 v157, v34, v35\n\t"
      "v_cvt_pk_bf16_f32 v158, v36, v37\n\t"
      "v_cvt_pk_bf16_f32 v159, v38, v39\n\t"
      "v_mfma_f32_16x16x32_bf16 v[40:43], v[92:95], v[104:107], v[40:43]\n\t"
      "ds_read_b128 v[72:75], v161 offset:21504\n\t"
      "v_cndmask_b32_e64 v90, v90, 0, s[68:69]\n\t"
      "v_cndmask_b32_e64 v91, v91, 0, s[68:69]\n\t"
      "v_cvt_pk_bf16_f32 v148, v16, v17\n\t"
      "v_mfma_f32_16x16x32_bf16 v[52:55], v[96:99], v[108:111], v[52:55]\n\t"
      "ds_read_b128 v[64:67], v165 offset:7168\n\t"
      "v_cvt_pk_bf16_f32 v149, v18, v19\n\t"
      "v_cvt_pk_bf16_f32 v150, v20, v21\n\t"
      "v_cvt_pk_bf16_f32 v151, v22, v23\n\t"
      "v_mfma_f32_16x16x32_bf16 v[60:63], v[100:103], v[108:111], v[60:63]\n\t"
      "ds_read_b64_tr_b16 v[80:81], v167 offset:3072\n\t"
      "v_mov_b32_e32 v34, 0x3e8b97f6\n\t"
      "v_mfma_f32_16x16x32_bf16 v[28:31], v[156:159], v[88:91], v[176:179]\n\t"
      "v_mfma_f32_16x16x32_bf16 v[24:27], v[152:155], v[88:91], v[172:175]\n\t"
      "v_mfma_f32_16x16x32_bf16 v[44:47], v[92:95], v[108:111], v[44:47]\n\t"
      "ds_read_b64_tr_b16 v[82:83], v167 offset:3584\n\t"
      "v_mfma_f32_16x16x32_bf16 v[20:23], v[152:155], v[84:87], v[176:179]\n\t"
      "v_mfma_f32_16x16x32_bf16 v[16:19], v[148:151], v[84:87], v[180:183]\n\t"
      "s_barrier\n\t"
      "s_waitcnt lgkmcnt(7)\n\t"
      "v_mfma_f32_16x16x32_bf16 v[48:51], v[124:127], v[132:135], v[48:51]\n\t"
      "ds_read_b64_tr_b16 v[104:105], v166 offset:16384\n\t"
      "ds_read_b64_tr_b16 v[106:107], v166 offset:16896\n\t"
      "v_mfma_f32_16x16x32_bf16 v[56:59], v[128:131], v[132:135], v[56:59]\n\t"
      "ds_read_b128 v[96:99], v160 offset:8192\n\t"
      "v_max_f32_e32 v24, v24, v28\n\t"
      "v_mfma_f32_16x16x32_bf16 v[40:43], v[120:123], v[132:135], v[40:43]\n\t"
      "ds_read_b128 v[100:103], v160 offset:22528\n\t"
      "v_max_f32_e32 v25, v25, v29\n\t"
      "v_max_f32_e32 v26, v26, v30\n\t"
      "v_max_f32_e32 v27, v27, v31\n\t"
      "v_mfma_f32_16x16x32_bf16 v[52:55], v[124:127], v[136:139], v[52:55]\n\t"
      "ds_read_b128 v[92:95], v164 offset:8192\n\t"
      "v_max_f32_e32 v16, v16, v20\n\t"
      "v_max_f32_e32 v17, v17, v21\n\t"
      "v_max_f32_e32 v18, v18, v22\n\t"
      "v_mfma_f32_16x16x32_bf16 v[60:63], v[128:131], v[136:139], v[60:63]\n\t"
      "ds_read_b64_tr_b16 v[108:109], v167 offset:16384\n\t"
      "v_max_f32_e32 v19, v19, v23\n\t"
      "v_max3_f32 v33, v24, v25, v26\n\t"
      "v_max3_f32 v32, v16, v17, v18\n\t"
      "v_mfma_f32_16x16x32_bf16 v[44:47], v[120:123], v[136:139], v[44:47]\n\t"
      "ds_read_b64_tr_b16 v[110:111], v167 offset:16896\n\t"
      "v_max_f32_e32 v33, v33, v27\n\t"
      "v_max_f32_e32 v32, v32, v19\n\t"
      "s_waitcnt lgkmcnt(7)\n\t"
      "v_mfma_f32_16x16x32_bf16 v[48:51], v[68:71], v[76:79], v[48:51]\n\t"
      "ds_read_b64_tr_b16 v[132:133], v166 offset:17408\n\t"
      "ds_read_b64_tr_b16 v[134:135], v166 offset:17920\n\t"
      "v_permlane16_swap_b32_e32 v32, v33\n\t"
      "v_max_f32_e32 v32, v32, v33\n\t"
      "v_mov_b32_e32 v33, v32\n\t"
      "v_mfma_f32_16x16x32_bf16 v[56:59], v[72:75], v[76:79], v[56:59]\n\t"
      "ds_read_b128 v[124:127], v161 offset:9216\n\t"
      "v_permlane32_swap_b32_e32 v32, v33\n\t"
      "v_max_f32_e32 v32, v32, v33\n\t"
      "v_mfma_f32_16x16x32_bf16 v[40:43], v[64:67], v[76:79], v[40:43]\n\t"
      "ds_read_b128 v[128:131], v161 offset:23552\n\t"
      "v_mov_b32_e32 v33, v32\n\t"
      "s_nop 1\n\t"
      "v_permlane16_swap_b32_e32 v32, v33\n\t"
      "v_mfma_f32_16x16x32_bf16 v[52:55], v[68:71], v[80:83], v[52:55]\n\t"
      "ds_read_b128 v[120:123], v165 offset:9216\n\t"
      "v_mul_f32_e64 v33, -v33, v34\n\t"
      "v_mul_f32_e64 v32, -v32, v34\n\t"
      "v_fma_f32 v24, v24, v34, v33\n\t"
      "v_mfma_f32_16x16x32_bf16 v[60:63], v[72:75], v[80:83], v[60:63]\n\t"
      "ds_read_b64_tr_b16 v[136:137], v167 offset:17408\n\t"
      "v_fma_f32 v25, v25, v34, v33\n\t"
      "v_fma_f32 v26, v26, v34, v33\n\t"
      "v_fma_f32 v27, v27, v34, v33\n\t"
      "v_mfma_f32_16x16x32_bf16 v[44:47], v[64:67], v[80:83], v[44:47]\n\t"
      "ds_read_b64_tr_b16 v[138:139], v167 offset:17920\n\t"
      "v_fma_f32 v16, v16, v34, v32\n\t"
      "v_fma_f32 v17, v17, v34, v32\n\t"
      "v_fma_f32 v18, v18, v34, v32\n\t"
      "s_waitcnt lgkmcnt(7)\n\t"
      "v_mfma_f32_16x16x32_bf16 v[48:51], v[96:99], v[104:107], v[48:51]\n\t"
      "ds_read_b64_tr_b16 v[76:77], v166 offset:18432\n\t"
      "ds_read_b64_tr_b16 v[78:79], v166 offset:18944\n\t"
      "v_fma_f32 v19, v19, v34, v32\n\t"
      "v_exp_f32_e32 v24, v24\n\t"
      "v_mfma_f32_16x16x32_bf16 v[56:59], v[100:103], v[104:107], v[56:59]\n\t"
      "ds_read_b128 v[68:71], v160 offset:10240\n\t"
      "v_exp_f32_e32 v25, v25\n\t"
      "v_exp_f32_e32 v26, v26\n\t"
      "v_mfma_f32_16x16x32_bf16 v[40:43], v[92:95], v[104:107], v[40:43]\n\t"
      "ds_read_b128 v[72:75], v160 offset:24576\n\t"
      "v_exp_f32_e32 v27, v27\n\t"
      "v_mfma_f32_16x16x32_bf16 v[52:55], v[96:99], v[108:111], v[52:55]\n\t"
      "ds_read_b128 v[64:67], v164 offset:10240\n\t"
      "v_exp_f32_e32 v16, v16\n\t"
      "v_exp_f32_e32 v17, v17\n\t"
      "v_mfma_f32_16x16x32_bf16 v[60:63], v[100:103], v[108:111], v[60:63]\n\t"
      "ds_read_b64_tr_b16 v[80:81], v167 offset:18432\n\t"
      "v_exp_f32_e32 v18, v18\n\t"
      "v_mfma_f32_16x16x32_bf16 v[44:47], v[92:95], v[108:111], v[44:47]\n\t"
      "ds_read_b64_tr_b16 v[82:83], v167 offset:18944\n\t"
      "v_exp_f32_e32 v19, v19\n\t"
      "v_add_f32_e32 v33, v24, v25\n\t"
      "s_waitcnt lgkmcnt(7)\n\t"
      "v_mfma_f32_16x16x32_bf16 v[48:51], v[124:127], v[132:135], v[48:51]\n\t"
      "ds_read_b64_tr_b16 v[104:105], v166 offset:19456\n\t"
      "ds_read_b64_tr_b16 v[106:107], v166 offset:19968\n\t"
      "v_add_f32_e32 v28, v26, v27\n\t"
      "v_add_f32_e32 v32, v16, v17\n\t"
      "v_add_f32_e32 v20, v18, v19\n\t"
      "v_mfma_f32_16x16x32_bf16 v[56:59], v[128:131], v[132:135], v[56:59]\n\t"
      "ds_read_b128 v[96:99], v161 offset:11264\n\t"
      "v_add_f32_e32 v33, v33, v28\n\t"
      "v_add_f32_e32 v32, v32, v20\n\t"
      "v_mfma_f32_16x16x32_bf16 v[40:43], v[120:123], v[132:135], v[40:43]\n\t"
      "ds_read_b128 v[100:103], v161 offset:25600\n\t"
      "v_permlane16_swap_b32_e32 v32, v33\n\t"
      "v_add_f32_e32 v32, v32, v33\n\t"
      "v_mov_b32_e32 v33, v32\n\t"
      "v_mfma_f32_16x16x32_bf16 v[52:55], v[124:127], v[136:139], v[52:55]\n\t"
      "ds_read_b128 v[92:95], v165 offset:11264\n\t"
      "v_permlane32_swap_b32_e32 v32, v33\n\t"
      "v_add_f32_e32 v32, v32, v33\n\t"
      "v_mfma_f32_16x16x32_bf16 v[60:63], v[128:131], v[136:139], v[60:63]\n\t"
      "ds_read_b64_tr_b16 v[108:109], v167 offset:19456\n\t"
      "v_mov_b32_e32 v33, v32\n\t"
      "s_nop 1\n\t"
      "v_permlane16_swap_b32_e32 v32, v33\n\t"
      "v_mfma_f32_16x16x32_bf16 v[44:47], v[120:123], v[136:139], v[44:47]\n\t"
      "ds_read_b64_tr_b16 v[110:111], v167 offset:19968\n\t"
      "v_rcp_f32_e32 v33, v33\n\t"
      "v_rcp_f32_e32 v32, v32\n\t"
      "s_barrier\n\t"
      "s_waitcnt lgkmcnt(7)\n\t"
      "v_mfma_f32_16x16x32_bf16 v[48:51], v[68:71], v[76:79], v[48:51]\n\t"
      "ds_read_b64_tr_b16 v[132:133], v170 offset:32768\n\t"
      "ds_read_b64_tr_b16 v[134:135], v170 offset:33280\n\t"
      "v_mul_f32_e32 v24, v24, v33\n\t"
      "v_mul_f32_e32 v25, v25, v33\n\t"
      "v_mfma_f32_16x16x32_bf16 v[56:59], v[72:75], v[76:79], v[56:59]\n\t"
      "ds_read_b128 v[124:127], v160 offset:12288\n\t"
      "v_mul_f32_e32 v26, v26, v33\n\t"
      "v_mul_f32_e32 v27, v27, v33\n\t"
      "v_mul_f32_e32 v16, v16, v32\n\t"
      "v_mfma_f32_16x16x32_bf16 v[40:43], v[64:67], v[76:79], v[40:43]\n\t"
      "ds_read_b128 v[128:131], v160 offset:26624\n\t"
      "v_mul_f32_e32 v17, v17, v32\n\t"
      "v_mul_f32_e32 v18, v18, v32\n\t"
      "v_mul_f32_e32 v19, v19, v32\n\t"
      "v_mfma_f32_16x16x32_bf16 v[52:55], v[68:71], v[80:83], v[52:55]\n\t"
      "ds_read_b128 v[120:123], v164 offset:12288\n\t"
      "v_cvt_pk_bf16_f32 v4, v24, v25\n\t"
      "v_cvt_pk_bf16_f32 v5, v26, v27\n\t"
      "v_cvt_pk_bf16_f32 v0, v16, v17\n\t"
      "v_mfma_f32_16x16x32_bf16 v[60:63], v[72:75], v[80:83], v[60:63]\n\t"
      "ds_read_b64_tr_b16 v[136:137], v171 offset:32768\n\t"
      "v_cvt_pk_bf16_f32 v1, v18, v19\n\t"
      "v_and_b32_e32 v6, v184, v4\n\t"
      "v_and_b32_e32 v7, v185, v5\n\t"
      "v_mfma_f32_16x16x32_bf16 v[44:47], v[64:67], v[80:83], v[44:47]\n\t"
      "ds_read_b64_tr_b16 v[138:139], v171 offset:33280\n\t"
      "v_and_b32_e32 v2, v184, v0\n\t"
      "v_and_b32_e32 v3, v185, v1\n\t"
      "v_xor_b32_e32 v4, v4, v6\n\t"
      "s_waitcnt lgkmcnt(7)\n\t"
      "v_mfma_f32_16x16x32_bf16 v[48:51], v[96:99], v[104:107], v[48:51]\n\t"
      "ds_read_b64_tr_b16 v[76:77], v170 offset:33792\n\t"
      "ds_read_b64_tr_b16 v[78:79], v170 offset:34304\n\t"
      "v_xor_b32_e32 v5, v5, v7\n\t"
      "v_xor_b32_e32 v0, v0, v2\n\t"
      "v_xor_b32_e32 v1, v1, v3\n\t"
      "v_mfma_f32_16x16x32_bf16 v[56:59], v[100:103], v[104:107], v[56:59]\n\t"
      "ds_read_b128 v[68:71], v161 offset:13312\n\t"
      "v_mfma_f32_16x16x32_bf16 v[40:43], v[92:95], v[104:107], v[40:43]\n\t"
      "ds_read_b128 v[72:75], v161 offset:27648\n\t"
      "v_mfma_f32_16x16x32_bf16 v[52:55], v[96:99], v[108:111], v[52:55]\n\t"
      "ds_read_b128 v[64:67], v165 offset:13312\n\t"
      "v_mfma_f32_16x16x32_bf16 v[60:63], v[100:103], v[108:111], v[60:63]\n\t"
      "ds_read_b64_tr_b16 v[80:81], v171 offset:33792\n\t"
      "v_mfma_f32_16x16x32_bf16 v[44:47], v[92:95], v[108:111], v[44:47]\n\t"
      "ds_read_b64_tr_b16 v[82:83], v171 offset:34304\n\t"
      "s_waitcnt lgkmcnt(7)\n\t"
      "v_mfma_f32_16x16x32_bf16 v[48:51], v[124:127], v[132:135], v[48:51]\n\t"
      "v_mfma_f32_16x16x32_bf16 v[56:59], v[128:131], v[132:135], v[56:59]\n\t"
      "v_mfma_f32_16x16x32_bf16 v[40:43], v[120:123], v[132:135], v[40:43]\n\t"
      "v_mfma_f32_16x16x32_bf16 v[52:55], v[124:127], v[136:139], v[52:55]\n\t"
      "v_mfma_f32_16x16x32_bf16 v[60:63], v[128:131], v[136:139], v[60:63]\n\t"
      "v_mfma_f32_16x16x32_bf16 v[44:47], v[120:123], v[136:139], v[44:47]\n\t"
      "s_waitcnt lgkmcnt(0)\n\t"
      "v_mfma_f32_16x16x32_bf16 v[48:51], v[68:71], v[76:79], v[48:51]\n\t"
      "v_mfma_f32_16x16x32_bf16 v[56:59], v[72:75], v[76:79], v[56:59]\n\t"
      "v_mfma_f32_16x16x32_bf16 v[40:43], v[64:67], v[76:79], v[40:43]\n\t"
      "v_mfma_f32_16x16x32_bf16 v[52:55], v[68:71], v[80:83], v[52:55]\n\t"
      "v_mfma_f32_16x16x32_bf16 v[60:63], v[72:75], v[80:83], v[60:63]\n\t"
      "v_mfma_f32_16x16x32_bf16 v[44:47], v[64:67], v[80:83], v[44:47]\n\t"
      "s_nop 2\n\t"
      "v_cvt_pk_bf16_f32 v66, v48, v49\n\t"
      "v_cvt_pk_bf16_f32 v67, v50, v51\n\t"
      "v_cvt_pk_bf16_f32 v68, v56, v57\n\t"
      "v_cvt_pk_bf16_f32 v69, v58, v59\n\t"
      "v_cvt_pk_bf16_f32 v64, v40, v41\n\t"
      "v_cvt_pk_bf16_f32 v65, v42, v43\n\t"
      "v_mfma_f32_16x16x32_bf16 v[88:91], v[66:69], v[4:7], 0\n\t"
      "s_nop 0\n\t"
      "v_mfma_f32_16x16x32_bf16 v[80:83], v[64:67], v[0:3], 0\n\t"
      "v_cvt_pk_bf16_f32 v72, v52, v53\n\t"
      "v_cvt_pk_bf16_f32 v73, v54, v55\n\t"
      "v_cvt_pk_bf16_f32 v74, v60, v61\n\t"
      "v_cvt_pk_bf16_f32 v75, v62, v63\n\t"
      "v_cvt_pk_bf16_f32 v70, v44, v45\n\t"
      "v_cvt_pk_bf16_f32 v71, v46, v47\n\t"
      "v_mfma_f32_16x16x32_bf16 v[92:95], v[72:75], v[4:7], 0\n\t"
      "s_nop 0\n\t"
      "v_mfma_f32_16x16x32_bf16 v[84:87], v[70:73], v[0:3], 0\n\t"
      "v_cvt_pk_bf16_f32 v152, v88, v89\n\t"
      "v_cvt_pk_bf16_f32 v153, v90, v91\n\t"
      "v_min_u32_e32 v152, -2, v152\n\t"
      "v_min_u32_e32 v153, -2, v153\n\t"
      "v_cvt_pk_bf16_f32 v148, v80, v81\n\t"
      "v_cvt_pk_bf16_f32 v149, v82, v83\n\t"
      "v_min_u32_e32 v148, -2, v148\n\t"
      "v_min_u32_e32 v149, -2, v149\n\t"
      "v_cvt_pk_bf16_f32 v154, v92, v93\n\t"
      "v_cvt_pk_bf16_f32 v155, v94, v95\n\t"
      "v_cndmask_b32_e64 v154, v154, 0, s[68:69]\n\t"
      "v_min_u32_e32 v154, -2, v154\n\t"
      "v_cndmask_b32_e64 v155, v155, 0, s[68:69]\n\t"
      "v_min_u32_e32 v155, -2, v155\n\t"
      "global_store_dwordx4 v244, v[152:155], s[44:45] sc1\n\t"
      "v_cvt_pk_bf16_f32 v150, v84, v85\n\t"
      "v_cvt_pk_bf16_f32 v151, v86, v87\n\t"
      "v_cndmask_b32_e64 v150, v150, 0, s[68:69]\n\t"
      "v_min_u32_e32 v150, -2, v150\n\t"
      "v_cndmask_b32_e64 v151, v151, 0, s[68:69]\n\t"
      "v_min_u32_e32 v151, -2, v151\n\t"
      "global_store_dwordx4 v244, v[148:151], s[42:43] sc1\n\t"
      "s_barrier\n\t"
      "s_cmp_eq_u32 s26, 7\n\t"
      "s_cbranch_scc1 SPARE_%=\n\t"
      "s_lshl_b32 s34, s25, 16\n\t"
      "s_lshl2_add_u32 s34, s39, s34\n\t"
      "s_add_u32 s34, s34, 4096\n\t"
      "s_add_u32 s42, s20, s34\n\t"
      "s_addc_u32 s43, s21, 0\n\t"
      "s_add_u32 s44, s42, 8192\n\t"
      "s_addc_u32 s45, s43, 0\n\t"
      "s_lshl_b32 s72, s3, 10\n\t"
      "s_add_u32 s72, s72, 98304\n\t"
      "v_xor_b32_e32 v203, v197, v198\n\t"
      "v_lshl_add_u32 v203, v203, 4, s72\n\t"
      "v_lshl_add_u32 v203, v197, 7, v203\n\t"
      "ds_write_b128 v203, v[204:207]\n\t"
      "s_lshr_b32 s35, s2, 6\n\t"
      "s_lshl2_add_u32 s31, s26, s35\n\t"
      "s_and_b32 s35, s2, 7\n\t"
      "s_mul_i32 s34, s35, 14\n\t"
      "s_lshl4_add_u32 s35, s35, -14\n\t"
      "s_cmp_lt_u32 s31, 14\n\t"
      "s_cselect_b32 s34, s34, s35\n\t"
      "s_cselect_b32 s36, 1, 0\n\t"
      "s_or_b32 s27, s27, s36\n\t"
      "s_add_u32 s34, s34, s31\n\t"
      "s_lshr_b32 s29, s34, 2\n\t"
      "s_and_b32 s30, s34, 3\n\t"
      "s_mul_i32 s30, s30, 7\n\t"
      "v_and_b32_e32 v199, 60, v192\n\t"
      "v_and_b32_e32 v200, 24, v192\n\t"
      "v_lshlrev_b32_e32 v202, 3, v196\n\t"
      "v_lshl_or_b32 v202, v200, 2, v202\n\t"
      "v_lshl_or_b32 v202, v195, 7, v202\n\t"
      "v_lshl_or_b32 v202, v193, 10, v202\n\t"
      "v_add_u32_e32 v224, s39, v202\n\t"
      "v_xor_b32_e32 v225, 32, v224\n\t"
      "v_xor_b32_e32 v226, 64, v224\n\t"
      "v_xor_b32_e32 v227, 96, v224\n\t"
      "s_lshl_b32 s34, s24, 12\n\t"
      "s_add_u32 s34, s34, 98304\n\t"
      "v_add_u32_e32 v245, s34, v244\n\t"
      "v_lshlrev_b32_e32 v199, 2, v199\n\t"
      "s_lshl_b32 s34, s24, 8\n\t"
      "v_lshl_add_u32 v247, v192, 2, s34\n\t"
      "v_and_b32_e32 v200, 2, v192\n\t"
      "v_mul_u32_u24_e32 v202, 576, v200\n\t"
      "v_xor_b32_e32 v199, v202, v199\n\t"
      "v_and_b32_e32 v202, 1, v192\n\t"
      "v_lshl_or_b32 v199, v202, 3, v199\n\t"
      "s_and_b32 s35, s34, 0x400\n\t"
      "s_add_u32 s34, s34, s35\n\t"
      "s_add_u32 s34, s34, 98304\n\t"
      "v_add_u32_e32 v246, s34, v199\n\t"
      "v_lshlrev_b32_e32 v199, 2, v202\n\t"
      "v_lshl_or_b32 v199, v200, 4, v199\n\t"
      "s_waitcnt lgkmcnt(0)\n\t"
      "s_barrier\n\t"
      "v_xor_b32_e32 v210, 32, v228\n\t"
      "v_xor_b32_e32 v212, 64, v228\n\t"
      "v_xor_b32_e32 v214, 96, v228\n\t"
      "ds_read_b64 v[208:209], v228\n\t"
      "ds_read_b64 v[210:211], v210\n\t"
      "ds_read_b64 v[212:213], v212\n\t"
      "ds_read_b64 v[214:215], v214\n\t"
      "ds_read_b64_tr_b16 v[0:1], v216\n\t"
      "ds_read_b64_tr_b16 v[2:3], v220\n\t"
      "ds_read_b64_tr_b16 v[4:5], v217\n\t"
      "ds_read_b64_tr_b16 v[6:7], v221\n\t"
      "ds_read_b64_tr_b16 v[8:9], v218\n\t"
      "ds_read_b64_tr_b16 v[10:11], v222\n\t"
      "ds_read_b64_tr_b16 v[12:13], v219\n\t"
      "ds_read_b64_tr_b16 v[14:15], v223\n\t"
      "ds_read_b64_tr_b16 v[16:17], v218 offset:3584\n\t"
      "ds_read_b64_tr_b16 v[18:19], v222 offset:3584\n\t"
      "ds_read_b64_tr_b16 v[20:21], v219 offset:3584\n\t"
      "ds_read_b64_tr_b16 v[22:23], v223 offset:3584\n\t"
      "ds_read_b64_tr_b16 v[24:25], v216 offset:3584\n\t"
      "ds_read_b64_tr_b16 v[26:27], v220 offset:3584\n\t"
      "ds_read_b64_tr_b16 v[28:29], v217 offset:3584\n\t"
      "ds_read_b64_tr_b16 v[30:31], v221 offset:3584\n\t"
      "ds_read_b64_tr_b16 v[32:33], v216 offset:7168\n\t"
      "ds_read_b64_tr_b16 v[34:35], v220 offset:7168\n\t"
      "ds_read_b64_tr_b16 v[36:37], v217 offset:7168\n\t"
      "ds_read_b64_tr_b16 v[38:39], v221 offset:7168\n\t"
      "ds_read_b64_tr_b16 v[40:41], v218 offset:7168\n\t"
      "ds_read_b64_tr_b16 v[42:43], v222 offset:7168\n\t"
      "ds_read_b64_tr_b16 v[44:45], v219 offset:7168\n\t"
      "ds_read_b64_tr_b16 v[46:47], v223 offset:7168\n\t"
      "ds_read_b64_tr_b16 v[48:49], v218 offset:10752\n\t"
      "ds_read_b64_tr_b16 v[50:51], v222 offset:10752\n\t"
      "ds_read_b64_tr_b16 v[52:53], v219 offset:10752\n\t"
      "ds_read_b64_tr_b16 v[54:55], v223 offset:10752\n\t"
      "ds_read_b64_tr_b16 v[56:57], v216 offset:10752\n\t"
      "ds_read_b64_tr_b16 v[58:59], v220 offset:10752\n\t"
      "ds_read_b64_tr_b16 v[60:61], v217 offset:10752\n\t"
      "ds_read_b64_tr_b16 v[62:63], v221 offset:10752\n\t"
      "ds_read_b64_tr_b16 v[64:65], v216 offset:14336\n\t"
      "ds_read_b64_tr_b16 v[66:67], v220 offset:14336\n\t"
      "ds_read_b64_tr_b16 v[68:69], v217 offset:14336\n\t"
      "ds_read_b64_tr_b16 v[70:71], v221 offset:14336\n\t"
      "ds_read_b64_tr_b16 v[72:73], v218 offset:14336\n\t"
      "ds_read_b64_tr_b16 v[74:75], v222 offset:14336\n\t"
      "ds_read_b64_tr_b16 v[76:77], v219 offset:14336\n\t"
      "ds_read_b64_tr_b16 v[78:79], v223 offset:14336\n\t"
      "ds_read_b64_tr_b16 v[80:81], v218 offset:17920\n\t"
      "ds_read_b64_tr_b16 v[82:83], v222 offset:17920\n\t"
      "ds_read_b64_tr_b16 v[84:85], v219 offset:17920\n\t"
      "ds_read_b64_tr_b16 v[86:87], v223 offset:17920\n\t"
      "ds_read_b64_tr_b16 v[88:89], v216 offset:17920\n\t"
      "ds_read_b64_tr_b16 v[90:91], v220 offset:17920\n\t"
      "ds_read_b64_tr_b16 v[92:93], v217 offset:17920\n\t"
      "ds_read_b64_tr_b16 v[94:95], v221 offset:17920\n\t"
      "ds_read_b64_tr_b16 v[96:97], v216 offset:21504\n\t"
      "ds_read_b64_tr_b16 v[98:99], v220 offset:21504\n\t"
      "ds_read_b64_tr_b16 v[100:101], v217 offset:21504\n\t"
      "ds_read_b64_tr_b16 v[102:103], v221 offset:21504\n\t"
      "ds_read_b64_tr_b16 v[104:105], v218 offset:21504\n\t"
      "ds_read_b64_tr_b16 v[106:107], v222 offset:21504\n\t"
      "ds_read_b64_tr_b16 v[108:109], v219 offset:21504\n\t"
      "ds_read_b64_tr_b16 v[110:111], v223 offset:21504\n\t"
      "ds_read_b64_tr_b16 v[112:113], v218 offset:25088\n\t"
      "ds_read_b64_tr_b16 v[114:115], v222 offset:25088\n\t"
      "ds_read_b64_tr_b16 v[116:117], v219 offset:25088\n\t"
      "ds_read_b64_tr_b16 v[118:119], v223 offset:25088\n\t"
      "ds_read_b64_tr_b16 v[120:121], v216 offset:25088\n\t"
      "ds_read_b64_tr_b16 v[122:123], v220 offset:25088\n\t"
      "ds_read_b64_tr_b16 v[124:125], v217 offset:25088\n\t"
      "ds_read_b64_tr_b16 v[126:127], v221 offset:25088\n\t"
      "global_load_dwordx4 v[128:131], v244, s[42:43] offset:-4096 sc1\n\t"
      "global_load_dwordx4 v[132:135], v244, s[42:43] offset:-3072 sc1\n\t"
      "global_load_dwordx4 v[136:139], v244, s[42:43] offset:-2048 sc1\n\t"
      "global_load_dwordx4 v[140:143], v244, s[42:43] offset:-1024 sc1\n\t"
      "global_load_dwordx4 v[144:147], v244, s[42:43] sc1\n\t"
      "global_load_dwordx4 v[148:151], v244, s[42:43] offset:1024 sc1\n\t"
      "global_load_dwordx4 v[152:155], v244, s[42:43] offset:2048 sc1\n\t"
      "global_load_dwordx4 v[156:159], v244, s[42:43] offset:3072 sc1\n\t"
      "global_load_dwordx4 v[160:163], v244, s[44:45] offset:-4096 sc1\n\t"
      "global_load_dwordx4 v[164:167], v244, s[44:45] offset:-3072 sc1\n\t"
      "s_min_u32 s34, s29, 27\n\t"
      "s_lshl_b32 s34, s34, 7\n\t"
      "s_lshr_b32 s35, s24, 2\n\t"
      "s_lshl_b32 s35, s35, 6\n\t"
      "s_add_u32 s34, s34, s35\n\t"
      "s_lshr_b32 s35, s39, 9\n\t"
      "s_add_u32 s34, s34, s35\n\t"
      "v_add_u32_e32 v248, s34, v199\n\t"
      "s_add_u32 s48, s20, 8388608\n\t"
      "s_addc_u32 s49, s21, 0\n\t"
      "v_and_b32_e32 v199, 16, v192\n\t"
      "v_lshlrev_b32_e32 v199, 1, v199\n\t"
      "v_and_b32_e32 v200, 32, v192\n\t"
      "s_lshl_b32 s34, s28, 18\n\t"
      "s_lshl_b32 s35, s25, 11\n\t"
      "s_add_u32 s34, s34, s35\n\t"
      "s_lshr_b32 s35, s39, 3\n\t"
      "s_add_u32 s34, s34, s35\n\t"
      "v_and_b32_e32 v201, 0x1f0, v244\n\t"
      "v_lshl_or_b32 v201, v200, 11, v201\n\t"
      "v_add_u32_e32 v229, s34, v201\n\t"
      "v_add_u32_e32 v230, 131072, v229\n\t"
      "s_and_b32 s34, s25, 30\n\t"
      "s_lshr_b32 s35, s39, 13\n\t"
      "s_add_u32 s34, s34, s35\n\t"
      "s_mul_i32 s34, s34, 7\n\t"
      "s_add_u32 s34, s34, s28\n\t"
      "s_lshl_b32 s34, s34, 3\n\t"
      "s_and_b32 s35, s25, 1\n\t"
      "s_add_u32 s34, s34, s35\n\t"
      "s_lshl_b32 s34, s34, 10\n\t"
      "s_and_b32 s35, s24, 1\n\t"
      "s_lshl_b32 s35, s35, 9\n\t"
      "s_add_u32 s34, s34, s35\n\t"
      "s_add_u32 s34, s34, 6293504\n\t"
      "v_lshrrev_b32_e32 v201, 1, v200\n\t"
      "v_lshl_or_b32 v201, v199, 6, v201\n\t"
      "v_lshl_or_b32 v201, v194, 5, v201\n\t"
      "v_add_u32_e32 v241, s34, v201\n\t"
      "s_lshl_b32 s34, s24, 12\n\t"
      "s_add_u32 s34, s34, 131072\n\t"
      "v_xor_b32_e32 v201, v193, v194\n\t"
      "v_lshlrev_b32_e32 v201, 3, v201\n\t"
      "v_lshl_or_b32 v201, v194, 7, v201\n\t"
      "v_add_u32_e32 v233, s34, v201\n\t"
      "v_lshrrev_b32_e32 v199, 2, v199\n\t"
      "v_or_b32_e32 v199, v199, v195\n\t"
      "v_lshrrev_b32_e32 v200, 3, v200\n\t"
      "v_or_b32_e32 v200, v196, v200\n\t"
      "v_xor_b32_e32 v200, v200, v199\n\t"
      "v_lshlrev_b32_e32 v200, 3, v200\n\t"
      "v_lshl_or_b32 v200, v199, 7, v200\n\t"
      "v_add_u32_e32 v237, s34, v200\n\t"
      "v_xor_b32_e32 v234, 32, v233\n\t"
      "v_xor_b32_e32 v238, 32, v237\n\t"
      "v_xor_b32_e32 v235, 64, v233\n\t"
      "v_xor_b32_e32 v239, 64, v237\n\t"
      "v_xor_b32_e32 v236, 96, v233\n\t"
      "v_xor_b32_e32 v240, 96, v237\n\t"
      "s_waitcnt lgkmcnt(14)\n\t"
      "v_lshlrev_b32_e32 v192, 16, v208\n\t"
      "v_and_b32_e32 v193, 0xffff0000, v208\n\t"
      "v_lshlrev_b32_e32 v194, 16, v209\n\t"
      "v_and_b32_e32 v195, 0xffff0000, v209\n\t"
      "v_lshlrev_b32_e32 v196, 16, v210\n\t"
      "v_and_b32_e32 v197, 0xffff0000, v210\n\t"
      "v_lshlrev_b32_e32 v198, 16, v211\n\t"
      "v_and_b32_e32 v199, 0xffff0000, v211\n\t"
      "v_lshlrev_b32_e32 v200, 16, v212\n\t"
      "v_and_b32_e32 v201, 0xffff0000, v212\n\t"
      "v_lshlrev_b32_e32 v202, 16, v213\n\t"
      "v_and_b32_e32 v203, 0xffff0000, v213\n\t"
      "v_lshlrev_b32_e32 v204, 16, v214\n\t"
      "v_and_b32_e32 v205, 0xffff0000, v214\n\t"
      "v_lshlrev_b32_e32 v206, 16, v215\n\t"
      "v_and_b32_e32 v207, 0xffff0000, v215\n\t"
      "global_load_dwordx4 v[168:171], v244, s[44:45] offset:-2048 sc1\n\t"
      "global_load_dwordx4 v[172:175], v244, s[44:45] offset:-1024 sc1\n\t"
      "global_load_dwordx4 v[176:179], v244, s[44:45] sc1\n\t"
      "global_load_dwordx4 v[180:183], v244, s[44:45] offset:1024 sc1\n\t"
      "global_load_dwordx4 v[184:187], v244, s[44:45] offset:2048 sc1\n\t"
      "global_load_dwordx4 v[188:191], v244, s[44:45] offset:3072 sc1\n\t"
      "global_load_dword v249, v248, s[12:13]\n\t"
      "s_barrier\n\t"
      "s_waitcnt lgkmcnt(0)\n\t"
      "LGM_%=:\n\t"
      "s_waitcnt vmcnt(15)\n\t"
      "v_max_u32_e32 v232, v128, v132\n\t"
      "v_cmp_eq_u32_e32 vcc, -1, v232\n\t"
      "s_cbranch_vccnz LO0R_%=\n\t"
      "LO0B_%=:\n\t"
      "v_mfma_f32_16x16x32_bf16 v[192:195], v[0:3], v[128:131], v[192:195]\n\t"
      "ds_read_b64_tr_b16 v[0:1], v216 offset:28672\n\t"
      "ds_read_b64_tr_b16 v[2:3], v220 offset:28672\n\t"
      "v_mfma_f32_16x16x32_bf16 v[196:199], v[4:7], v[128:131], v[196:199]\n\t"
      "ds_read_b64_tr_b16 v[4:5], v217 offset:28672\n\t"
      "v_mfma_f32_16x16x32_bf16 v[200:203], v[8:11], v[128:131], v[200:203]\n\t"
      "ds_read_b64_tr_b16 v[6:7], v221 offset:28672\n\t"
      "v_mfma_f32_16x16x32_bf16 v[204:207], v[12:15], v[128:131], v[204:207]\n\t"
      "ds_read_b64_tr_b16 v[8:9], v218 offset:28672\n\t"
      "ds_read_b64_tr_b16 v[10:11], v222 offset:28672\n\t"
      "v_mfma_f32_16x16x32_bf16 v[192:195], v[16:19], v[132:135], v[192:195]\n\t"
      "ds_read_b64_tr_b16 v[12:13], v219 offset:28672\n\t"
      "v_mfma_f32_16x16x32_bf16 v[196:199], v[20:23], v[132:135], v[196:199]\n\t"
      "ds_read_b64_tr_b16 v[14:15], v223 offset:28672\n\t"
      "v_mfma_f32_16x16x32_bf16 v[200:203], v[24:27], v[132:135], v[200:203]\n\t"
      "ds_read_b64_tr_b16 v[16:17], v218 offset:32256\n\t"
      "ds_read_b64_tr_b16 v[18:19], v222 offset:32256\n\t"
      "v_mfma_f32_16x16x32_bf16 v[204:207], v[28:31], v[132:135], v[204:207]\n\t"
      "ds_read_b64_tr_b16 v[20:21], v219 offset:32256\n\t"
      "s_waitcnt vmcnt(13)\n\t"
      "v_max_u32_e32 v232, v136, v140\n\t"
      "v_cmp_eq_u32_e32 vcc, -1, v232\n\t"
      "s_cbranch_vccnz LO2R_%=\n\t"
      "LO2B_%=:\n\t"
      "v_mfma_f32_16x16x32_bf16 v[192:195], v[32:35], v[136:139], v[192:195]\n\t"
      "ds_read_b64_tr_b16 v[22:23], v223 offset:32256\n\t"
      "v_mfma_f32_16x16x32_bf16 v[196:199], v[36:39], v[136:139], v[196:199]\n\t"
      "ds_read_b64_tr_b16 v[24:25], v216 offset:32256\n\t"
      "ds_read_b64_tr_b16 v[26:27], v220 offset:32256\n\t"
      "v_mfma_f32_16x16x32_bf16 v[200:203], v[40:43], v[136:139], v[200:203]\n\t"
      "ds_read_b64_tr_b16 v[28:29], v217 offset:32256\n\t"
      "v_mfma_f32_16x16x32_bf16 v[204:207], v[44:47], v[136:139], v[204:207]\n\t"
      "ds_read_b64_tr_b16 v[30:31], v221 offset:32256\n\t"
      "v_mfma_f32_16x16x32_bf16 v[192:195], v[48:51], v[140:143], v[192:195]\n\t"
      "ds_read_b64_tr_b16 v[32:33], v216 offset:35840\n\t"
      "ds_read_b64_tr_b16 v[34:35], v220 offset:35840\n\t"
      "v_mfma_f32_16x16x32_bf16 v[196:199], v[52:55], v[140:143], v[196:199]\n\t"
      "ds_read_b64_tr_b16 v[36:37], v217 offset:35840\n\t"
      "v_mfma_f32_16x16x32_bf16 v[200:203], v[56:59], v[140:143], v[200:203]\n\t"
      "ds_read_b64_tr_b16 v[38:39], v221 offset:35840\n\t"
      "v_mfma_f32_16x16x32_bf16 v[204:207], v[60:63], v[140:143], v[204:207]\n\t"
      "ds_read_b64_tr_b16 v[40:41], v218 offset:35840\n\t"
      "ds_read_b64_tr_b16 v[42:43], v222 offset:35840\n\t"
      "s_waitcnt vmcnt(11)\n\t"
      "v_max_u32_e32 v232, v144, v148\n\t"
      "v_cmp_eq_u32_e32 vcc, -1, v232\n\t"
      "s_cbranch_vccnz LO4R_%=\n\t"
      "LO4B_%=:\n\t"
      "v_mfma_f32_16x16x32_bf16 v[192:195], v[64:67], v[144:147], v[192:195]\n\t"
      "ds_read_b64_tr_b16 v[44:45], v219 offset:35840\n\t"
      "v_mfma_f32_16x16x32_bf16 v[196:199], v[68:71], v[144:147], v[196:199]\n\t"
      "ds_read_b64_tr_b16 v[46:47], v223 offset:35840\n\t"
      "v_mfma_f32_16x16x32_bf16 v[200:203], v[72:75], v[144:147], v[200:203]\n\t"
      "ds_read_b64_tr_b16 v[48:49], v218 offset:39424\n\t"
      "ds_read_b64_tr_b16 v[50:51], v222 offset:39424\n\t"
      "v_mfma_f32_16x16x32_bf16 v[204:207], v[76:79], v[144:147], v[204:207]\n\t"
      "ds_read_b64_tr_b16 v[52:53], v219 offset:39424\n\t"
      "v_mfma_f32_16x16x32_bf16 v[192:195], v[80:83], v[148:151], v[192:195]\n\t"
      "ds_read_b64_tr_b16 v[54:55], v223 offset:39424\n\t"
      "v_mfma_f32_16x16x32_bf16 v[196:199], v[84:87], v[148:151], v[196:199]\n\t"
      "ds_read_b64_tr_b16 v[56:57], v216 offset:39424\n\t"
      "ds_read_b64_tr_b16 v[58:59], v220 offset:39424\n\t"
      "v_mfma_f32_16x16x32_bf16 v[200:203], v[88:91], v[148:151], v[200:203]\n\t"
      "ds_read_b64_tr_b16 v[60:61], v217 offset:39424\n\t"
      "v_mfma_f32_16x16x32_bf16 v[204:207], v[92:95], v[148:151], v[204:207]\n\t"
      "ds_read_b64_tr_b16 v[62:63], v221 offset:39424\n\t"
      "s_waitcnt vmcnt(9)\n\t"
      "v_max_u32_e32 v232, v152, v156\n\t"
      "v_cmp_eq_u32_e32 vcc, -1, v232\n\t"
      "s_cbranch_vccnz LO6R_%=\n\t"
      "LO6B_%=:\n\t"
      "v_mfma_f32_16x16x32_bf16 v[192:195], v[96:99], v[152:155], v[192:195]\n\t"
      "ds_read_b64_tr_b16 v[64:65], v216 offset:43008\n\t"
      "ds_read_b64_tr_b16 v[66:67], v220 offset:43008\n\t"
      "v_mfma_f32_16x16x32_bf16 v[196:199], v[100:103], v[152:155], v[196:199]\n\t"
      "ds_read_b64_tr_b16 v[68:69], v217 offset:43008\n\t"
      "v_mfma_f32_16x16x32_bf16 v[200:203], v[104:107], v[152:155], v[200:203]\n\t"
      "ds_read_b64_tr_b16 v[70:71], v221 offset:43008\n\t"
      "v_mfma_f32_16x16x32_bf16 v[204:207], v[108:111], v[152:155], v[204:207]\n\t"
      "ds_read_b64_tr_b16 v[72:73], v218 offset:43008\n\t"
      "ds_read_b64_tr_b16 v[74:75], v222 offset:43008\n\t"
      "v_mfma_f32_16x16x32_bf16 v[192:195], v[112:115], v[156:159], v[192:195]\n\t"
      "ds_read_b64_tr_b16 v[76:77], v219 offset:43008\n\t"
      "v_mfma_f32_16x16x32_bf16 v[196:199], v[116:119], v[156:159], v[196:199]\n\t"
      "ds_read_b64_tr_b16 v[78:79], v223 offset:43008\n\t"
      "v_mfma_f32_16x16x32_bf16 v[200:203], v[120:123], v[156:159], v[200:203]\n\t"
      "ds_read_b64_tr_b16 v[80:81], v218 offset:46592\n\t"
      "ds_read_b64_tr_b16 v[82:83], v222 offset:46592\n\t"
      "v_mfma_f32_16x16x32_bf16 v[204:207], v[124:127], v[156:159], v[204:207]\n\t"
      "ds_read_b64_tr_b16 v[84:85], v219 offset:46592\n\t"
      "s_waitcnt vmcnt(7)\n\t"
      "v_max_u32_e32 v232, v160, v164\n\t"
      "v_cmp_eq_u32_e32 vcc, -1, v232\n\t"
      "s_cbranch_vccnz LO8R_%=\n\t"
      "LO8B_%=:\n\t"
      "s_waitcnt lgkmcnt(14)\n\t"
      "v_mfma_f32_16x16x32_bf16 v[192:195], v[0:3], v[160:163], v[192:195]\n\t"
      "ds_read_b64_tr_b16 v[86:87], v223 offset:46592\n\t"
      "v_mfma_f32_16x16x32_bf16 v[196:199], v[4:7], v[160:163], v[196:199]\n\t"
      "ds_read_b64_tr_b16 v[88:89], v216 offset:46592\n\t"
      "ds_read_b64_tr_b16 v[90:91], v220 offset:46592\n\t"
      "v_mfma_f32_16x16x32_bf16 v[200:203], v[8:11], v[160:163], v[200:203]\n\t"
      "ds_read_b64_tr_b16 v[92:93], v217 offset:46592\n\t"
      "v_mfma_f32_16x16x32_bf16 v[204:207], v[12:15], v[160:163], v[204:207]\n\t"
      "ds_read_b64_tr_b16 v[94:95], v221 offset:46592\n\t"
      "v_mfma_f32_16x16x32_bf16 v[192:195], v[16:19], v[164:167], v[192:195]\n\t"
      "ds_read_b64_tr_b16 v[96:97], v216 offset:50176\n\t"
      "ds_read_b64_tr_b16 v[98:99], v220 offset:50176\n\t"
      "v_mfma_f32_16x16x32_bf16 v[196:199], v[20:23], v[164:167], v[196:199]\n\t"
      "ds_read_b64_tr_b16 v[100:101], v217 offset:50176\n\t"
      "v_mfma_f32_16x16x32_bf16 v[200:203], v[24:27], v[164:167], v[200:203]\n\t"
      "ds_read_b64_tr_b16 v[102:103], v221 offset:50176\n\t"
      "v_mfma_f32_16x16x32_bf16 v[204:207], v[28:31], v[164:167], v[204:207]\n\t"
      "ds_read_b64_tr_b16 v[104:105], v218 offset:50176\n\t"
      "ds_read_b64_tr_b16 v[106:107], v222 offset:50176\n\t"
      "s_waitcnt vmcnt(5)\n\t"
      "v_max_u32_e32 v232, v168, v172\n\t"
      "v_cmp_eq_u32_e32 vcc, -1, v232\n\t"
      "s_cbranch_vccnz LO10R_%=\n\t"
      "LO10B_%=:\n\t"
      "v_mfma_f32_16x16x32_bf16 v[192:195], v[32:35], v[168:171], v[192:195]\n\t"
      "ds_read_b64_tr_b16 v[108:109], v219 offset:50176\n\t"
      "v_mfma_f32_16x16x32_bf16 v[196:199], v[36:39], v[168:171], v[196:199]\n\t"
      "ds_read_b64_tr_b16 v[110:111], v223 offset:50176\n\t"
      "v_mfma_f32_16x16x32_bf16 v[200:203], v[40:43], v[168:171], v[200:203]\n\t"
      "ds_read_b64_tr_b16 v[112:113], v218 offset:53760\n\t"
      "ds_read_b64_tr_b16 v[114:115], v222 offset:53760\n\t"
      "v_mfma_f32_16x16x32_bf16 v[204:207], v[44:47], v[168:171], v[204:207]\n\t"
      "ds_read_b64_tr_b16 v[116:117], v219 offset:53760\n\t"
      "s_waitcnt lgkmcnt(14)\n\t"
      "v_mfma_f32_16x16x32_bf16 v[192:195], v[48:51], v[172:175], v[192:195]\n\t"
      "ds_read_b64_tr_b16 v[118:119], v223 offset:53760\n\t"
      "v_mfma_f32_16x16x32_bf16 v[196:199], v[52:55], v[172:175], v[196:199]\n\t"
      "ds_read_b64_tr_b16 v[120:121], v216 offset:53760\n\t"
      "ds_read_b64_tr_b16 v[122:123], v220 offset:53760\n\t"
      "v_mfma_f32_16x16x32_bf16 v[200:203], v[56:59], v[172:175], v[200:203]\n\t"
      "ds_read_b64_tr_b16 v[124:125], v217 offset:53760\n\t"
      "v_mfma_f32_16x16x32_bf16 v[204:207], v[60:63], v[172:175], v[204:207]\n\t"
      "ds_read_b64_tr_b16 v[126:127], v221 offset:53760\n\t"
      "s_waitcnt vmcnt(3)\n\t"
      "v_max_u32_e32 v232, v176, v180\n\t"
      "v_cmp_eq_u32_e32 vcc, -1, v232\n\t"
      "s_cbranch_vccnz LO12R_%=\n\t"
      "LO12B_%=:\n\t"
      "v_mfma_f32_16x16x32_bf16 v[192:195], v[64:67], v[176:179], v[192:195]\n\t"
      "v_mfma_f32_16x16x32_bf16 v[196:199], v[68:71], v[176:179], v[196:199]\n\t"
      "v_mfma_f32_16x16x32_bf16 v[200:203], v[72:75], v[176:179], v[200:203]\n\t"
      "v_mfma_f32_16x16x32_bf16 v[204:207], v[76:79], v[176:179], v[204:207]\n\t"
      "s_waitcnt lgkmcnt(14)\n\t"
      "v_mfma_f32_16x16x32_bf16 v[192:195], v[80:83], v[180:183], v[192:195]\n\t"
      "v_mfma_f32_16x16x32_bf16 v[196:199], v[84:87], v[180:183], v[196:199]\n\t"
      "v_mfma_f32_16x16x32_bf16 v[200:203], v[88:91], v[180:183], v[200:203]\n\t"
      "v_mfma_f32_16x16x32_bf16 v[204:207], v[92:95], v[180:183], v[204:207]\n\t"
      "s_waitcnt vmcnt(1)\n\t"
      "v_max_u32_e32 v232, v184, v188\n\t"
      "v_cmp_eq_u32_e32 vcc, -1, v232\n\t"
      "s_cbranch_vccnz LO14R_%=\n\t"
      "LO14B_%=:\n\t"
      "s_waitcnt lgkmcnt(8)\n\t"
      "v_mfma_f32_16x16x32_bf16 v[192:195], v[96:99], v[184:187], v[192:195]\n\t"
      "v_mfma_f32_16x16x32_bf16 v[196:199], v[100:103], v[184:187], v[196:199]\n\t"
      "v_mfma_f32_16x16x32_bf16 v[200:203], v[104:107], v[184:187], v[200:203]\n\t"
      "v_mfma_f32_16x16x32_bf16 v[204:207], v[108:111], v[184:187], v[204:207]\n\t"
      "s_waitcnt lgkmcnt(0)\n\t"
      "v_mfma_f32_16x16x32_bf16 v[192:195], v[112:115], v[188:191], v[192:195]\n\t"
      "v_mfma_f32_16x16x32_bf16 v[196:199], v[116:119], v[188:191], v[196:199]\n\t"
      "v_mfma_f32_16x16x32_bf16 v[200:203], v[120:123], v[188:191], v[200:203]\n\t"
      "v_mfma_f32_16x16x32_bf16 v[204:207], v[124:127], v[188:191], v[204:207]\n\t"
      "s_nop 4\n\t"
      "v_cmp_u_f32_e32 vcc, v192, v192\n\t"
      "s_cbranch_vccnz LGX_%=\n\t"
      "LGB_%=:\n\t"
      "v_cvt_pk_bf16_f32 v208, v192, v193\n\t"
      "v_cvt_pk_bf16_f32 v209, v194, v195\n\t"
      "v_cvt_pk_bf16_f32 v210, v196, v197\n\t"
      "v_cvt_pk_bf16_f32 v211, v198, v199\n\t"
      "v_cvt_pk_bf16_f32 v212, v200, v201\n\t"
      "v_cvt_pk_bf16_f32 v213, v202, v203\n\t"
      "v_cvt_pk_bf16_f32 v214, v204, v205\n\t"
      "v_cvt_pk_bf16_f32 v215, v206, v207\n\t"
      "v_mov_b32_e32 v148, 0x3f803f80\n\t"
      "v_dot2_f32_bf16 v144, v208, v148, 0\n\t"
      "v_dot2_f32_bf16 v145, v208, v208, 0\n\t"
      "v_dot2_f32_bf16 v146, v209, v148, 0\n\t"
      "v_dot2_f32_bf16 v147, v209, v209, 0\n\t"
      "v_dot2_f32_bf16 v144, v210, v148, v144\n\t"
      "v_dot2_f32_bf16 v145, v210, v210, v145\n\t"
      "v_dot2_f32_bf16 v146, v211, v148, v146\n\t"
      "v_dot2_f32_bf16 v147, v211, v211, v147\n\t"
      "v_dot2_f32_bf16 v144, v212, v148, v144\n\t"
      "v_dot2_f32_bf16 v145, v212, v212, v145\n\t"
      "v_dot2_f32_bf16 v146, v213, v148, v146\n\t"
      "v_dot2_f32_bf16 v147, v213, v213, v147\n\t"
      "v_dot2_f32_bf16 v144, v214, v148, v144\n\t"
      "v_dot2_f32_bf16 v145, v214, v214, v145\n\t"
      "v_dot2_f32_bf16 v146, v215, v148, v146\n\t"
      "v_dot2_f32_bf16 v147, v215, v215, v147\n\t"
      "v_lshlrev_b32_e32 v128, 16, v208\n\t"
      "v_and_b32_e32 v129, 0xffff0000, v208\n\t"
      "v_lshlrev_b32_e32 v130, 16, v209\n\t"
      "v_pk_add_f32 v[144:145], v[144:145], v[146:147]\n\t"
      "v_and_b32_e32 v131, 0xffff0000, v209\n\t"
      "v_lshlrev_b32_e32 v132, 16, v210\n\t"
      "v_permlane16_swap_b32_e32 v144, v145\n\t"
      "v_add_f32_e32 v144, v144, v145\n\t"
      "v_mov_b32_e32 v145, v144\n\t"
      "v_and_b32_e32 v133, 0xffff0000, v210\n\t"
      "v_lshlrev_b32_e32 v134, 16, v211\n\t"
      "v_permlane32_swap_b32_e32 v144, v145\n\t"
      "v_add_f32_e32 v144, v144, v145\n\t"
      "v_mov_b32_e32 v145, v144\n\t"
      "v_and_b32_e32 v135, 0xffff0000, v211\n\t"
      "v_lshlrev_b32_e32 v136, 16, v212\n\t"
      "v_permlane16_swap_b32_e32 v144, v145\n\t"
      "s_mov_b64 exec, s[70:71]\n\t"
      "v_min_u32_e32 v144, -2, v144\n\t"
      "v_min_u32_e32 v145, -2, v145\n\t"
      "global_store_dwordx2 v231, v[144:145], s[58:59] sc1\n\t"
      "s_mov_b64 exec, -1\n\t"
      "s_barrier\n\t"
      "v_and_b32_e32 v137, 0xffff0000, v212\n\t"
      "v_lshlrev_b32_e32 v138, 16, v213\n\t"
      "v_and_b32_e32 v139, 0xffff0000, v213\n\t"
      "v_lshlrev_b32_e32 v140, 16, v214\n\t"
      "v_and_b32_e32 v141, 0xffff0000, v214\n\t"
      "v_lshlrev_b32_e32 v142, 16, v215\n\t"
      "v_and_b32_e32 v143, 0xffff0000, v215\n\t"
      "v_permlane16_swap_b32_e32 v208, v210\n\t"
      "v_permlane16_swap_b32_e32 v209, v211\n\t"
      "v_permlane16_swap_b32_e32 v212, v214\n\t"
      "v_permlane16_swap_b32_e32 v213, v215\n\t"
      "s_bitcmp0_b32 s27, 0\n\t"
      "s_cbranch_scc1 NTK0_%=\n\t"
      "ds_read_b64_tr_b16 v[0:1], v224 offset:16384\n\t"
      "ds_read_b64_tr_b16 v[2:3], v224 offset:16896\n\t"
      "ds_read_b64_tr_b16 v[4:5], v225 offset:16384\n\t"
      "ds_read_b64_tr_b16 v[6:7], v225 offset:16896\n\t"
      "ds_read_b64_tr_b16 v[8:9], v226 offset:16384\n\t"
      "ds_read_b64_tr_b16 v[10:11], v226 offset:16896\n\t"
      "ds_read_b64_tr_b16 v[12:13], v227 offset:16384\n\t"
      "ds_read_b64_tr_b16 v[14:15], v227 offset:16896\n\t"
      "ds_read_b64_tr_b16 v[16:17], v224 offset:49152\n\t"
      "ds_read_b64_tr_b16 v[18:19], v224 offset:49664\n\t"
      "ds_read_b64_tr_b16 v[20:21], v225 offset:49152\n\t"
      "ds_read_b64_tr_b16 v[22:23], v225 offset:49664\n\t"
      "ds_read_b64_tr_b16 v[24:25], v226 offset:49152\n\t"
      "ds_read_b64_tr_b16 v[26:27], v226 offset:49664\n\t"
      "ds_read_b64_tr_b16 v[28:29], v227 offset:49152\n\t"
      "ds_read_b64_tr_b16 v[30:31], v227 offset:49664\n\t"
      "ds_read_b64_tr_b16 v[32:33], v224\n\t"
      "ds_read_b64_tr_b16 v[34:35], v224 offset:512\n\t"
      "ds_read_b64_tr_b16 v[36:37], v225\n\t"
      "ds_read_b64_tr_b16 v[38:39], v225 offset:512\n\t"
      "ds_read_b64_tr_b16 v[40:41], v226\n\t"
      "ds_read_b64_tr_b16 v[42:43], v226 offset:512\n\t"
      "ds_read_b64_tr_b16 v[44:45], v227\n\t"
      "ds_read_b64_tr_b16 v[46:47], v227 offset:512\n\t"
      "ds_read_b64_tr_b16 v[48:49], v224 offset:32768\n\t"
      "ds_read_b64_tr_b16 v[50:51], v224 offset:33280\n\t"
      "ds_read_b64_tr_b16 v[52:53], v225 offset:32768\n\t"
      "ds_read_b64_tr_b16 v[54:55], v225 offset:33280\n\t"
      "ds_read_b64_tr_b16 v[56:57], v226 offset:32768\n\t"
      "ds_read_b64_tr_b16 v[58:59], v226 offset:33280\n\t"
      "ds_read_b64_tr_b16 v[60:61], v227 offset:32768\n\t"
      "ds_read_b64_tr_b16 v[62:63], v227 offset:33280\n\t"
      "NTK0_%=:\n\t"
      "s_sleep 6\n\t"
      "global_load_dwordx2 v[148:149], v231, s[46:47] sc1\n\t"
      "global_load_dwordx2 v[150:151], v231, s[46:47] offset:384 sc1\n\t"
      "s_waitcnt lgkmcnt(0)\n\t"
      "s_barrier\n\t"
      "s_waitcnt vmcnt(0)\n\t"
      "v_max3_u32 v232, v148, v149, v150\n\t"
      "v_max_u32_e32 v232, v232, v151\n\t"
      "v_cmp_eq_u32_e32 vcc, -1, v232\n\t"
      "s_cbranch_vccnz LSR_%=\n\t"
      "LSB_%=:\n\t"
      "v_cndmask_b32_e64 v150, v150, 0, s[70:71]\n\t"
      "v_cndmask_b32_e64 v151, v151, 0, s[70:71]\n\t"
      "v_pk_add_f32 v[144:145], v[148:149], v[150:151]\n\t"
      "s_nop 1\n\t"
      "v_permlane16_swap_b32_e32 v144, v145\n\t"
      "v_add_f32_e32 v144, v144, v145\n\t"
      "v_mov_b32_e32 v145, v144\n\t"
      "s_nop 1\n\t"
      "v_permlane32_swap_b32_e32 v144, v145\n\t"
      "v_add_f32_e32 v144, v144, v145\n\t"
      "v_mov_b32_e32 v145, v144\n\t"
      "s_nop 1\n\t"
      "v_permlane16_swap_b32_e32 v144, v145\n\t"
      "v_mul_f32_e32 v144, 0xbb124925, v144\n\t"
      "v_mul_f32_e32 v145, 0x3b124925, v145\n\t"
      "v_fma_f32 v145, -v144, v144, v145\n\t"
      "v_max_f32_e32 v145, 0, v145\n\t"
      "v_add_f32_e32 v146, 0x3727c5ac, v145\n\t"
      "v_fmac_f32_e32 v145, 0x3727c5ac, v146\n\t"
      "v_rsq_f32_e32 v145, v145\n\t"
      "s_nop 0\n\t"
      "v_mul_f32_e32 v144, v144, v145\n\t"
      "v_pk_fma_f32 v[128:129], v[128:129], v[144:145], v[144:145] op_sel:[0,1,0] op_sel_hi:[1,1,0]\n\t"
      "v_pk_fma_f32 v[130:131], v[130:131], v[144:145], v[144:145] op_sel:[0,1,0] op_sel_hi:[1,1,0]\n\t"
      "v_cvt_pk_bf16_f32 v152, v128, v129\n\t"
      "v_cvt_pk_bf16_f32 v153, v130, v131\n\t"
      "ds_write_b64 v233, v[152:153]\n\t"
      "v_pk_fma_f32 v[132:133], v[132:133], v[144:145], v[144:145] op_sel:[0,1,0] op_sel_hi:[1,1,0]\n\t"
      "v_pk_fma_f32 v[134:135], v[134:135], v[144:145], v[144:145] op_sel:[0,1,0] op_sel_hi:[1,1,0]\n\t"
      "v_cvt_pk_bf16_f32 v154, v132, v133\n\t"
      "v_cvt_pk_bf16_f32 v155, v134, v135\n\t"
      "ds_write_b64 v234, v[154:155]\n\t"
      "ds_read_b64_tr_b16 v[160:161], v237\n\t"
      "ds_read_b64_tr_b16 v[162:163], v238 offset:512\n\t"
      "v_pk_fma_f32 v[136:137], v[136:137], v[144:145], v[144:145] op_sel:[0,1,0] op_sel_hi:[1,1,0]\n\t"
      "v_pk_fma_f32 v[138:139], v[138:139], v[144:145], v[144:145] op_sel:[0,1,0] op_sel_hi:[1,1,0]\n\t"
      "v_cvt_pk_bf16_f32 v156, v136, v137\n\t"
      "v_cvt_pk_bf16_f32 v157, v138, v139\n\t"
      "ds_write_b64 v235, v[156:157]\n\t"
      "v_pk_fma_f32 v[140:141], v[140:141], v[144:145], v[144:145] op_sel:[0,1,0] op_sel_hi:[1,1,0]\n\t"
      "v_pk_fma_f32 v[142:143], v[142:143], v[144:145], v[144:145] op_sel:[0,1,0] op_sel_hi:[1,1,0]\n\t"
      "v_cvt_pk_bf16_f32 v158, v140, v141\n\t"
      "v_cvt_pk_bf16_f32 v159, v142, v143\n\t"
      "ds_write_b64 v236, v[158:159]\n\t"
      "ds_read_b64_tr_b16 v[164:165], v239\n\t"
      "ds_read_b64_tr_b16 v[166:167], v240 offset:512\n\t"
      "s_waitcnt lgkmcnt(4)\n\t"
      "v_min_u32_e32 v160, -2, v160\n\t"
      "v_min_u32_e32 v161, -2, v161\n\t"
      "v_min_u32_e32 v162, -2, v162\n\t"
      "v_min_u32_e32 v163, -2, v163\n\t"
      "global_store_dwordx4 v229, v[160:163], s[48:49] sc1\n\t"
      "s_waitcnt lgkmcnt(0)\n\t"
      "v_min_u32_e32 v164, -2, v164\n\t"
      "v_min_u32_e32 v165, -2, v165\n\t"
      "v_min_u32_e32 v166, -2, v166\n\t"
      "v_min_u32_e32 v167, -2, v167\n\t"
      "global_store_dwordx4 v230, v[164:167], s[48:49] sc1\n\t"
      "s_setprio 0\n\t"
      "s_barrier\n\t"
      "s_bitcmp0_b32 s27, 0\n\t"
      "s_cbranch_scc1 G4C_%=\n\t"
      "s_mul_i32 s34, s30, 65536\n\t"
      "s_mul_i32 s35, s24, 8192\n\t"
      "s_add_u32 s34, s34, s35\n\t"
      "s_add_u32 s64, s48, s34\n\t"
      "s_addc_u32 s65, s49, 0\n\t"
      "v_add_u32_e32 v250, 4096, v244\n\t"
      "global_load_dwordx4 v[128:131], v250, s[64:65] offset:-4096 sc1\n\t"
      "global_load_dwordx4 v[132:135], v250, s[64:65] offset:-3072 sc1\n\t"
      "global_load_dwordx4 v[136:139], v250, s[64:65] offset:-2048 sc1\n\t"
      "global_load_dwordx4 v[140:143], v250, s[64:65] offset:-1024 sc1\n\t"
      "global_load_dwordx4 v[144:147], v250, s[64:65] sc1\n\t"
      "global_load_dwordx4 v[148:151], v250, s[64:65] offset:1024 sc1\n\t"
      "global_load_dwordx4 v[152:155], v250, s[64:65] offset:2048 sc1\n\t"
      "global_load_dwordx4 v[156:159], v250, s[64:65] offset:3072 sc1\n\t"
      "global_store_dwordx4 v241, v[208:211], s[20:21] offset:-2048 sc1\n\t"
      "global_store_dwordx4 v241, v[212:215], s[20:21] offset:2048 sc1\n\t"
      "ds_read_b64_tr_b16 v[64:65], v224 offset:16384\n\t"
      "ds_read_b64_tr_b16 v[66:67], v224 offset:16896\n\t"
      "ds_read_b64_tr_b16 v[68:69], v225 offset:16384\n\t"
      "ds_read_b64_tr_b16 v[70:71], v225 offset:16896\n\t"
      "ds_read_b64_tr_b16 v[72:73], v226 offset:16384\n\t"
      "ds_read_b64_tr_b16 v[74:75], v226 offset:16896\n\t"
      "ds_read_b64_tr_b16 v[76:77], v227 offset:16384\n\t"
      "ds_read_b64_tr_b16 v[78:79], v227 offset:16896\n\t"
      "ds_read_b64_tr_b16 v[80:81], v224 offset:49152\n\t"
      "ds_read_b64_tr_b16 v[82:83], v224 offset:49664\n\t"
      "ds_read_b64_tr_b16 v[84:85], v225 offset:49152\n\t"
      "ds_read_b64_tr_b16 v[86:87], v225 offset:49664\n\t"
      "ds_read_b64_tr_b16 v[88:89], v226 offset:49152\n\t"
      "ds_read_b64_tr_b16 v[90:91], v226 offset:49664\n\t"
      "ds_read_b64_tr_b16 v[92:93], v227 offset:49152\n\t"
      "ds_read_b64_tr_b16 v[94:95], v227 offset:49664\n\t"
      "ds_read_b64_tr_b16 v[96:97], v224\n\t"
      "ds_read_b64_tr_b16 v[98:99], v224 offset:512\n\t"
      "ds_read_b64_tr_b16 v[100:101], v225\n\t"
      "ds_read_b64_tr_b16 v[102:103], v225 offset:512\n\t"
      "ds_read_b64_tr_b16 v[104:105], v226\n\t"
      "ds_read_b64_tr_b16 v[106:107], v226 offset:512\n\t"
      "ds_read_b64_tr_b16 v[108:109], v227\n\t"
      "ds_read_b64_tr_b16 v[110:111], v227 offset:512\n\t"
      "ds_read_b64_tr_b16 v[112:113], v224 offset:32768\n\t"
      "ds_read_b64_tr_b16 v[114:115], v224 offset:33280\n\t"
      "ds_read_b64_tr_b16 v[116:117], v225 offset:32768\n\t"
      "ds_read_b64_tr_b16 v[118:119], v225 offset:33280\n\t"
      "ds_read_b64_tr_b16 v[120:121], v226 offset:32768\n\t"
      "ds_read_b64_tr_b16 v[122:123], v226 offset:33280\n\t"
      "ds_read_b64_tr_b16 v[124:125], v227 offset:32768\n\t"
      "ds_read_b64_tr_b16 v[126:127], v227 offset:33280\n\t"
      "s_waitcnt vmcnt(2) lgkmcnt(0)\n\t"
      "GST_%=:\n\t"
      "s_add_u32 s66, s64, 65536\n\t"
      "s_addc_u32 s67, s65, 0\n\t"
      "s_add_u32 s68, s66, 65536\n\t"
      "s_addc_u32 s69, s67, 0\n\t"
      "s_add_u32 s70, s68, 65536\n\t"
      "s_addc_u32 s71, s69, 0\n\t"
      "s_add_u32 s72, s70, 65536\n\t"
      "s_addc_u32 s73, s71, 0\n\t"
      "s_add_u32 s74, s72, 65536\n\t"
      "s_addc_u32 s75, s73, 0\n\t"
      "s_add_u32 s76, s74, 65536\n\t"
      "s_addc_u32 s77, s75, 0\n\t"
      "s_mul_i32 s34, s30, 28\n\t"
      "s_add_u32 s34, s34, s29\n\t"
      "s_lshl_b32 s34, s34, 11\n\t"
      "s_add_u32 s34, s34, 10223616\n\t"
      "s_add_u32 s62, s20, s34\n\t"
      "s_addc_u32 s63, s21, 0\n\t"
      "v_lshlrev_b32_e32 v248, 16, v249\n\t"
      "v_and_b32_e32 v249, 0xffff0000, v249\n\t"
      "v_mov_b32_e32 v252, 0xbdd2d3e8\n\t"
      "v_xor_b32_e32 v253, 128, v245\n\t"
      "v_max3_u32 v251, v128, v129, v130\n\t"
      "v_max3_u32 v251, v251, v131, v132\n\t"
      "v_max3_u32 v251, v251, v133, v134\n\t"
      "v_max3_u32 v251, v251, v135, v136\n\t"
      "v_max3_u32 v251, v251, v137, v138\n\t"
      "v_max3_u32 v251, v251, v139, v140\n\t"
      "v_max3_u32 v251, v251, v141, v142\n\t"
      "v_max3_u32 v251, v251, v143, v144\n\t"
      "v_max3_u32 v251, v251, v145, v146\n\t"
      "v_max3_u32 v251, v251, v147, v148\n\t"
      "v_max3_u32 v251, v251, v149, v150\n\t"
      "v_max3_u32 v251, v251, v151, v152\n\t"
      "v_max3_u32 v251, v251, v153, v154\n\t"
      "v_max3_u32 v251, v251, v155, v156\n\t"
      "v_max3_u32 v251, v251, v157, v158\n\t"
      "v_max_u32_e32 v251, v251, v159\n\t"
      "v_cmp_eq_u32_e32 vcc, -1, v251\n\t"
      "s_cbranch_vccnz LA30R_%=\n\t"
      "LA30B_%=:\n\t"
      "v_mfma_f32_16x16x32_bf16 v[208:211], v[0:3], v[128:131], 0\n\t"
      "global_load_dwordx4 v[160:163], v250, s[66:67] offset:-4096 sc1\n\t"
      "v_mfma_f32_16x16x32_bf16 v[208:211], v[16:19], v[132:135], v[208:211]\n\t"
      "v_mfma_f32_16x16x32_bf16 v[208:211], v[32:35], v[136:139], v[208:211]\n\t"
      "global_load_dwordx4 v[164:167], v250, s[66:67] offset:-3072 sc1\n\t"
      "v_mfma_f32_16x16x32_bf16 v[208:211], v[48:51], v[140:143], v[208:211]\n\t"
      "v_mfma_f32_16x16x32_bf16 v[208:211], v[64:67], v[144:147], v[208:211]\n\t"
      "global_load_dwordx4 v[168:171], v250, s[66:67] offset:-2048 sc1\n\t"
      "v_mfma_f32_16x16x32_bf16 v[208:211], v[80:83], v[148:151], v[208:211]\n\t"
      "s_barrier\n\t"
      "v_mfma_f32_16x16x32_bf16 v[208:211], v[96:99], v[152:155], v[208:211]\n\t"
      "s_lshl_b32 s34, s2, 7\n\t"
      "s_add_u32 s34, s34, 65536\n\t"
      "v_mov_b32_e32 v230, s34\n\t"
      "s_cmp_lg_u32 s24, 3\n\t"
      "s_cbranch_scc1 SK17_%=\n\t"
      "global_load_dwordx2 v[228:229], v230, s[60:61] sc1\n\t"
      "SK17_%=:\n\t"
      "global_load_dwordx4 v[172:175], v250, s[66:67] offset:-1024 sc1\n\t"
      "v_mfma_f32_16x16x32_bf16 v[208:211], v[112:115], v[156:159], v[208:211]\n\t"
      "v_mfma_f32_16x16x32_bf16 v[212:215], v[4:7], v[128:131], 0\n\t"
      "global_load_dwordx4 v[176:179], v250, s[66:67] sc1\n\t"
      "v_mfma_f32_16x16x32_bf16 v[212:215], v[20:23], v[132:135], v[212:215]\n\t"
      "v_mfma_f32_16x16x32_bf16 v[212:215], v[36:39], v[136:139], v[212:215]\n\t"
      "global_load_dwordx4 v[180:183], v250, s[66:67] offset:1024 sc1\n\t"
      "v_mfma_f32_16x16x32_bf16 v[212:215], v[52:55], v[140:143], v[212:215]\n\t"
      "v_mfma_f32_16x16x32_bf16 v[212:215], v[68:71], v[144:147], v[212:215]\n\t"
      "global_load_dwordx4 v[184:187], v250, s[66:67] offset:2048 sc1\n\t"
      "v_mfma_f32_16x16x32_bf16 v[212:215], v[84:87], v[148:151], v[212:215]\n\t"
      "v_mfma_f32_16x16x32_bf16 v[212:215], v[100:103], v[152:155], v[212:215]\n\t"
      "ds_write_b128 v245, v[208:211]\n\t"
      "global_load_dwordx4 v[188:191], v250, s[66:67] offset:3072 sc1\n\t"
      "v_mfma_f32_16x16x32_bf16 v[212:215], v[116:119], v[156:159], v[212:215]\n\t"
      "v_mfma_f32_16x16x32_bf16 v[216:219], v[8:11], v[128:131], 0\n\t"
      "global_load_dwordx4 v[192:195], v250, s[68:69] offset:-4096 sc1\n\t"
      "v_mfma_f32_16x16x32_bf16 v[216:219], v[24:27], v[132:135], v[216:219]\n\t"
      "v_mfma_f32_16x16x32_bf16 v[216:219], v[40:43], v[136:139], v[216:219]\n\t"
      "global_load_dwordx4 v[196:199], v250, s[68:69] offset:-3072 sc1\n\t"
      "v_mfma_f32_16x16x32_bf16 v[216:219], v[56:59], v[140:143], v[216:219]\n\t"
      "v_mfma_f32_16x16x32_bf16 v[216:219], v[72:75], v[144:147], v[216:219]\n\t"
      "global_load_dwordx4 v[200:203], v250, s[68:69] offset:-2048 sc1\n\t"
      "v_mfma_f32_16x16x32_bf16 v[216:219], v[88:91], v[148:151], v[216:219]\n\t"
      "v_mfma_f32_16x16x32_bf16 v[216:219], v[104:107], v[152:155], v[216:219]\n\t"
      "ds_write_b128 v253, v[212:215] offset:1024\n\t"
      "global_load_dwordx4 v[204:207], v250, s[68:69] offset:-1024 sc1\n\t"
      "v_mfma_f32_16x16x32_bf16 v[216:219], v[120:123], v[156:159], v[216:219]\n\t"
      "v_mfma_f32_16x16x32_bf16 v[220:223], v[12:15], v[128:131], 0\n\t"
      "global_load_dwordx4 v[128:131], v250, s[68:69] sc1\n\t"
      "v_mfma_f32_16x16x32_bf16 v[220:223], v[28:31], v[132:135], v[220:223]\n\t"
      "v_mfma_f32_16x16x32_bf16 v[220:223], v[44:47], v[136:139], v[220:223]\n\t"
      "v_mfma_f32_16x16x32_bf16 v[220:223], v[60:63], v[140:143], v[220:223]\n\t"
      "v_mfma_f32_16x16x32_bf16 v[220:223], v[76:79], v[144:147], v[220:223]\n\t"
      "global_load_dwordx4 v[132:135], v250, s[68:69] offset:1024 sc1\n\t"
      "v_mfma_f32_16x16x32_bf16 v[220:223], v[92:95], v[148:151], v[220:223]\n\t"
      "v_mfma_f32_16x16x32_bf16 v[220:223], v[108:111], v[152:155], v[220:223]\n\t"
      "ds_write_b128 v245, v[216:219] offset:2048\n\t"
      "v_mfma_f32_16x16x32_bf16 v[220:223], v[124:127], v[156:159], v[220:223]\n\t"
      "s_waitcnt vmcnt(10)\n\t"
      "v_mfma_f32_16x16x32_bf16 v[224:227], v[0:3], v[160:163], 0\n\t"
      "global_load_dwordx4 v[136:139], v250, s[68:69] offset:2048 sc1\n\t"
      "v_mfma_f32_16x16x32_bf16 v[224:227], v[16:19], v[164:167], v[224:227]\n\t"
      "s_nop 3\n\t"
      "ds_write_b128 v253, v[220:223] offset:3072\n\t"
      "v_mfma_f32_16x16x32_bf16 v[224:227], v[32:35], v[168:171], v[224:227]\n\t"
      "v_mfma_f32_16x16x32_bf16 v[224:227], v[48:51], v[172:175], v[224:227]\n\t"
      "s_waitcnt vmcnt(7)\n\t"
      "v_mfma_f32_16x16x32_bf16 v[224:227], v[64:67], v[176:179], v[224:227]\n\t"
      "global_load_dwordx4 v[140:143], v250, s[68:69] offset:3072 sc1\n\t"
      "v_mfma_f32_16x16x32_bf16 v[224:227], v[80:83], v[180:183], v[224:227]\n\t"
      "s_cmp_lg_u32 s24, 3\n\t"
      "s_cbranch_scc1 LF6B_%=\n\t"
      "v_cmp_ne_u64_e32 vcc, s[22:23], v[228:229]\n\t"
      "s_cbranch_vccnz LF6R_%=\n\t"
      "LF6B_%=:\n\t"
      "s_waitcnt lgkmcnt(0)\n\t"
      "s_barrier\n\t"
      "s_cmp_lg_u32 s24, 3\n\t"
      "s_cbranch_scc1 HF3_%=\n\t"
      "s_lshl3_add_u32 s34, s25, s26\n\t"
      "s_lshl_b32 s34, s34, 7\n\t"
      "s_add_u32 s34, s34, 98304\n\t"
      "v_mov_b32_e32 v238, s34\n\t"
      "v_mov_b32_e32 v236, s22\n\t"
      "v_mov_b32_e32 v237, s23\n\t"
      "s_mov_b64 exec, 1\n\t"
      "global_store_dwordx2 v238, v[236:237], s[60:61] sc1\n\t"
      "s_mov_b64 exec, -1\n\t"
      "HF3_%=:\n\t"
      "ds_read_b64 v[228:229], v246\n\t"
      "ds_read_b64 v[230:231], v246 offset:4096\n\t"
      "v_mfma_f32_16x16x32_bf16 v[224:227], v[96:99], v[184:187], v[224:227]\n\t"
      "ds_read_b64 v[232:233], v246 offset:8192\n\t"
      "ds_read_b64 v[234:235], v246 offset:12288\n\t"
      "v_mfma_f32_16x16x32_bf16 v[224:227], v[112:115], v[188:191], v[224:227]\n\t"
      "ds_read_b64 v[236:237], v246 offset:16384\n\t"
      "ds_read_b64 v[238:239], v246 offset:20480\n\t"
      "v_mfma_f32_16x16x32_bf16 v[208:211], v[4:7], v[160:163], 0\n\t"
      "ds_read_b64 v[240:241], v246 offset:24576\n\t"
      "ds_read_b64 v[242:243], v246 offset:28672\n\t"
      "global_load_dwordx4 v[144:147], v250, s[70:71] offset:-4096 sc1\n\t"
      "v_mfma_f32_16x16x32_bf16 v[208:211], v[20:23], v[164:167], v[208:211]\n\t"
      "v_mfma_f32_16x16x32_bf16 v[208:211], v[36:39], v[168:171], v[208:211]\n\t"
      "s_waitcnt lgkmcnt(4)\n\t"
      "v_add_f32_e32 v228, v228, v230\n\t"
      "v_add_f32_e32 v232, v232, v234\n\t"
      "v_cmp_u_f32_e32 vcc, v224, v224\n\t"
      "s_cbranch_vccnz LA31R_%=\n\t"
      "LA31B_%=:\n\t"
      "v_mfma_f32_16x16x32_bf16 v[208:211], v[52:55], v[172:175], v[208:211]\n\t"
      "v_add_f32_e32 v229, v229, v231\n\t"
      "v_add_f32_e32 v233, v233, v235\n\t"
      "v_mfma_f32_16x16x32_bf16 v[208:211], v[68:71], v[176:179], v[208:211]\n\t"
      "s_waitcnt lgkmcnt(0)\n\t"
      "v_add_f32_e32 v236, v236, v238\n\t"
      "v_add_f32_e32 v240, v240, v242\n\t"
      "global_load_dwordx4 v[148:151], v250, s[70:71] offset:-3072 sc1\n\t"
      "v_mfma_f32_16x16x32_bf16 v[208:211], v[84:87], v[180:183], v[208:211]\n\t"
      "v_add_f32_e32 v237, v237, v239\n\t"
      "v_add_f32_e32 v241, v241, v243\n\t"
      "v_mfma_f32_16x16x32_bf16 v[208:211], v[100:103], v[184:187], v[208:211]\n\t"
      "v_add_f32_e32 v228, v228, v232\n\t"
      "v_add_f32_e32 v236, v236, v240\n\t"
      "v_add_f32_e32 v229, v229, v233\n\t"
      "ds_write_b128 v245, v[224:227] offset:32768\n\t"
      "v_mfma_f32_16x16x32_bf16 v[208:211], v[116:119], v[188:191], v[208:211]\n\t"
      "v_add_f32_e32 v237, v237, v241\n\t"
      "v_add_f32_e32 v228, v228, v248\n\t"
      "v_mfma_f32_16x16x32_bf16 v[212:215], v[8:11], v[160:163], 0\n\t"
      "v_add_f32_e32 v229, v229, v249\n\t"
      "v_add_f32_e32 v228, v228, v236\n\t"
      "v_add_f32_e32 v229, v229, v237\n\t"
      "global_load_dwordx4 v[152:155], v250, s[70:71] offset:-2048 sc1\n\t"
      "v_mfma_f32_16x16x32_bf16 v[212:215], v[24:27], v[164:167], v[212:215]\n\t"
      "v_mul_f32_e32 v230, v228, v228\n\t"
      "v_mul_f32_e32 v231, v229, v229\n\t"
      "v_mfma_f32_16x16x32_bf16 v[212:215], v[40:43], v[168:171], v[212:215]\n\t"
      "v_fmaak_f32 v230, v252, v230, 0xc0135761\n\t"
      "v_fmaak_f32 v231, v252, v231, 0xc0135761\n\t"
      "v_mul_f32_e32 v230, v228, v230\n\t"
      "v_mfma_f32_16x16x32_bf16 v[212:215], v[56:59], v[172:175], v[212:215]\n\t"
      "v_mul_f32_e32 v231, v229, v231\n\t"
      "v_exp_f32_e32 v230, v230\n\t"
      "v_mfma_f32_16x16x32_bf16 v[212:215], v[72:75], v[176:179], v[212:215]\n\t"
      "v_exp_f32_e32 v231, v231\n\t"
      "v_add_f32_e32 v230, 1.0, v230\n\t"
      "v_add_f32_e32 v231, 1.0, v231\n\t"
      "global_load_dwordx4 v[156:159], v250, s[70:71] offset:-1024 sc1\n\t"
      "v_mfma_f32_16x16x32_bf16 v[212:215], v[88:91], v[180:183], v[212:215]\n\t"
      "v_rcp_f32_e32 v230, v230\n\t"
      "v_rcp_f32_e32 v231, v231\n\t"
      "v_mfma_f32_16x16x32_bf16 v[212:215], v[104:107], v[184:187], v[212:215]\n\t"
      "v_mul_f32_e32 v228, v228, v230\n\t"
      "v_mul_f32_e32 v229, v229, v231\n\t"
      "v_cvt_pk_bf16_f32 v232, v228, v229\n\t"
      "ds_write_b128 v253, v[208:211] offset:33792\n\t"
      "v_mfma_f32_16x16x32_bf16 v[212:215], v[120:123], v[188:191], v[212:215]\n\t"
      "v_min_u32_e32 v254, -2, v232\n\t"
      "global_store_dword v247, v254, s[62:63] sc1\n\t"
      "s_add_u32 s62, s62, 57344\n\t"
      "s_addc_u32 s63, s63, 0\n\t"
      "v_mfma_f32_16x16x32_bf16 v[216:219], v[12:15], v[160:163], 0\n\t"
      "global_load_dwordx4 v[160:163], v250, s[70:71] sc1\n\t"
      "v_mfma_f32_16x16x32_bf16 v[216:219], v[28:31], v[164:167], v[216:219]\n\t"
      "v_mfma_f32_16x16x32_bf16 v[216:219], v[44:47], v[168:171], v[216:219]\n\t"
      "v_mfma_f32_16x16x32_bf16 v[216:219], v[60:63], v[172:175], v[216:219]\n\t"
      "v_mfma_f32_16x16x32_bf16 v[216:219], v[76:79], v[176:179], v[216:219]\n\t"
      "global_load_dwordx4 v[164:167], v250, s[70:71] offset:1024 sc1\n\t"
      "v_mfma_f32_16x16x32_bf16 v[216:219], v[92:95], v[180:183], v[216:219]\n\t"
      "v_mfma_f32_16x16x32_bf16 v[216:219], v[108:111], v[184:187], v[216:219]\n\t"
      "ds_write_b128 v245, v[212:215] offset:34816\n\t"
      "v_mfma_f32_16x16x32_bf16 v[216:219], v[124:127], v[188:191], v[216:219]\n\t"
      "s_waitcnt vmcnt(11)\n\t"
      "v_mfma_f32_16x16x32_bf16 v[220:223], v[0:3], v[192:195], 0\n\t"
      "global_load_dwordx4 v[168:171], v250, s[70:71] offset:2048 sc1\n\t"
      "v_mfma_f32_16x16x32_bf16 v[220:223], v[16:19], v[196:199], v[220:223]\n\t"
      "s_nop 3\n\t"
      "ds_write_b128 v253, v[216:219] offset:35840\n\t"
      "v_mfma_f32_16x16x32_bf16 v[220:223], v[32:35], v[200:203], v[220:223]\n\t"
      "v_mfma_f32_16x16x32_bf16 v[220:223], v[48:51], v[204:207], v[220:223]\n\t"
      "s_waitcnt vmcnt(8)\n\t"
      "v_mfma_f32_16x16x32_bf16 v[220:223], v[64:67], v[128:131], v[220:223]\n\t"
      "global_load_dwordx4 v[172:175], v250, s[70:71] offset:3072 sc1\n\t"
      "v_mfma_f32_16x16x32_bf16 v[220:223], v[80:83], v[132:135], v[220:223]\n\t"
      "s_waitcnt lgkmcnt(0)\n\t"
      "s_barrier\n\t"
      "ds_read_b64 v[228:229], v246 offset:32768\n\t"
      "ds_read_b64 v[230:231], v246 offset:36864\n\t"
      "v_mfma_f32_16x16x32_bf16 v[220:223], v[96:99], v[136:139], v[220:223]\n\t"
      "ds_read_b64 v[232:233], v246 offset:40960\n\t"
      "ds_read_b64 v[234:235], v246 offset:45056\n\t"
      "v_mfma_f32_16x16x32_bf16 v[220:223], v[112:115], v[140:143], v[220:223]\n\t"
      "ds_read_b64 v[236:237], v246 offset:49152\n\t"
      "ds_read_b64 v[238:239], v246 offset:53248\n\t"
      "v_mfma_f32_16x16x32_bf16 v[224:227], v[4:7], v[192:195], 0\n\t"
      "ds_read_b64 v[240:241], v246 offset:57344\n\t"
      "ds_read_b64 v[242:243], v246 offset:61440\n\t"
      "global_load_dwordx4 v[176:179], v250, s[72:73] offset:-4096 sc1\n\t"
      "v_mfma_f32_16x16x32_bf16 v[224:227], v[20:23], v[196:199], v[224:227]\n\t"
      "v_mfma_f32_16x16x32_bf16 v[224:227], v[36:39], v[200:203], v[224:227]\n\t"
      "s_waitcnt lgkmcnt(4)\n\t"
      "v_add_f32_e32 v228, v228, v230\n\t"
      "v_add_f32_e32 v232, v232, v234\n\t"
      "v_cmp_u_f32_e32 vcc, v220, v220\n\t"
      "s_cbranch_vccnz LA32R_%=\n\t"
      "LA32B_%=:\n\t"
      "v_mfma_f32_16x16x32_bf16 v[224:227], v[52:55], v[204:207], v[224:227]\n\t"
      "v_add_f32_e32 v229, v229, v231\n\t"
      "v_add_f32_e32 v233, v233, v235\n\t"
      "v_mfma_f32_16x16x32_bf16 v[224:227], v[68:71], v[128:131], v[224:227]\n\t"
      "s_waitcnt lgkmcnt(0)\n\t"
      "v_add_f32_e32 v236, v236, v238\n\t"
      "v_add_f32_e32 v240, v240, v242\n\t"
      "global_load_dwordx4 v[180:183], v250, s[72:73] offset:-3072 sc1\n\t"
      "v_mfma_f32_16x16x32_bf16 v[224:227], v[84:87], v[132:135], v[224:227]\n\t"
      "v_add_f32_e32 v237, v237, v239\n\t"
      "v_add_f32_e32 v241, v241, v243\n\t"
      "v_mfma_f32_16x16x32_bf16 v[224:227], v[100:103], v[136:139], v[224:227]\n\t"
      "v_add_f32_e32 v228, v228, v232\n\t"
      "v_add_f32_e32 v236, v236, v240\n\t"
      "v_add_f32_e32 v229, v229, v233\n\t"
      "ds_write_b128 v245, v[220:223]\n\t"
      "v_mfma_f32_16x16x32_bf16 v[224:227], v[116:119], v[140:143], v[224:227]\n\t"
      "v_add_f32_e32 v237, v237, v241\n\t"
      "v_add_f32_e32 v228, v228, v248\n\t"
      "v_mfma_f32_16x16x32_bf16 v[208:211], v[8:11], v[192:195], 0\n\t"
      "v_add_f32_e32 v229, v229, v249\n\t"
      "v_add_f32_e32 v228, v228, v236\n\t"
      "v_add_f32_e32 v229, v229, v237\n\t"
      "global_load_dwordx4 v[184:187], v250, s[72:73] offset:-2048 sc1\n\t"
      "v_mfma_f32_16x16x32_bf16 v[208:211], v[24:27], v[196:199], v[208:211]\n\t"
      "v_mul_f32_e32 v230, v228, v228\n\t"
      "v_mul_f32_e32 v231, v229, v229\n\t"
      "v_mfma_f32_16x16x32_bf16 v[208:211], v[40:43], v[200:203], v[208:211]\n\t"
      "v_fmaak_f32 v230, v252, v230, 0xc0135761\n\t"
      "v_fmaak_f32 v231, v252, v231, 0xc0135761\n\t"
      "v_mul_f32_e32 v230, v228, v230\n\t"
      "v_mfma_f32_16x16x32_bf16 v[208:211], v[56:59], v[204:207], v[208:211]\n\t"
      "v_mul_f32_e32 v231, v229, v231\n\t"
      "v_exp_f32_e32 v230, v230\n\t"
      "v_mfma_f32_16x16x32_bf16 v[208:211], v[72:75], v[128:131], v[208:211]\n\t"
      "v_exp_f32_e32 v231, v231\n\t"
      "v_add_f32_e32 v230, 1.0, v230\n\t"
      "v_add_f32_e32 v231, 1.0, v231\n\t"
      "global_load_dwordx4 v[188:191], v250, s[72:73] offset:-1024 sc1\n\t"
      "v_mfma_f32_16x16x32_bf16 v[208:211], v[88:91], v[132:135], v[208:211]\n\t"
      "v_rcp_f32_e32 v230, v230\n\t"
      "v_rcp_f32_e32 v231, v231\n\t"
      "v_mfma_f32_16x16x32_bf16 v[208:211], v[104:107], v[136:139], v[208:211]\n\t"
      "v_mul_f32_e32 v228, v228, v230\n\t"
      "v_mul_f32_e32 v229, v229, v231\n\t"
      "v_cvt_pk_bf16_f32 v232, v228, v229\n\t"
      "ds_write_b128 v253, v[224:227] offset:1024\n\t"
      "v_mfma_f32_16x16x32_bf16 v[208:211], v[120:123], v[140:143], v[208:211]\n\t"
      "v_min_u32_e32 v254, -2, v232\n\t"
      "global_store_dword v247, v254, s[62:63] sc1\n\t"
      "s_add_u32 s62, s62, 57344\n\t"
      "s_addc_u32 s63, s63, 0\n\t"
      "v_mfma_f32_16x16x32_bf16 v[212:215], v[12:15], v[192:195], 0\n\t"
      "global_load_dwordx4 v[192:195], v250, s[72:73] sc1\n\t"
      "v_mfma_f32_16x16x32_bf16 v[212:215], v[28:31], v[196:199], v[212:215]\n\t"
      "v_mfma_f32_16x16x32_bf16 v[212:215], v[44:47], v[200:203], v[212:215]\n\t"
      "v_mfma_f32_16x16x32_bf16 v[212:215], v[60:63], v[204:207], v[212:215]\n\t"
      "v_mfma_f32_16x16x32_bf16 v[212:215], v[76:79], v[128:131], v[212:215]\n\t"
      "global_load_dwordx4 v[196:199], v250, s[72:73] offset:1024 sc1\n\t"
      "v_mfma_f32_16x16x32_bf16 v[212:215], v[92:95], v[132:135], v[212:215]\n\t"
      "v_mfma_f32_16x16x32_bf16 v[212:215], v[108:111], v[136:139], v[212:215]\n\t"
      "ds_write_b128 v245, v[208:211] offset:2048\n\t"
      "v_mfma_f32_16x16x32_bf16 v[212:215], v[124:127], v[140:143], v[212:215]\n\t"
      "s_waitcnt vmcnt(12)\n\t"
      "v_mfma_f32_16x16x32_bf16 v[216:219], v[0:3], v[144:147], 0\n\t"
      "global_load_dwordx4 v[200:203], v250, s[72:73] offset:2048 sc1\n\t"
      "v_mfma_f32_16x16x32_bf16 v[216:219], v[16:19], v[148:151], v[216:219]\n\t"
      "s_nop 3\n\t"
      "ds_write_b128 v253, v[212:215] offset:3072\n\t"
      "v_mfma_f32_16x16x32_bf16 v[216:219], v[32:35], v[152:155], v[216:219]\n\t"
      "v_mfma_f32_16x16x32_bf16 v[216:219], v[48:51], v[156:159], v[216:219]\n\t"
      "s_waitcnt vmcnt(8)\n\t"
      "v_mfma_f32_16x16x32_bf16 v[216:219], v[64:67], v[160:163], v[216:219]\n\t"
      "global_load_dwordx4 v[204:207], v250, s[72:73] offset:3072 sc1\n\t"
      "v_mfma_f32_16x16x32_bf16 v[216:219], v[80:83], v[164:167], v[216:219]\n\t"
      "s_waitcnt lgkmcnt(0)\n\t"
      "s_barrier\n\t"
      "ds_read_b64 v[228:229], v246\n\t"
      "ds_read_b64 v[230:231], v246 offset:4096\n\t"
      "v_mfma_f32_16x16x32_bf16 v[216:219], v[96:99], v[168:171], v[216:219]\n\t"
      "ds_read_b64 v[232:233], v246 offset:8192\n\t"
      "ds_read_b64 v[234:235], v246 offset:12288\n\t"
      "v_mfma_f32_16x16x32_bf16 v[216:219], v[112:115], v[172:175], v[216:219]\n\t"
      "ds_read_b64 v[236:237], v246 offset:16384\n\t"
      "ds_read_b64 v[238:239], v246 offset:20480\n\t"
      "v_mfma_f32_16x16x32_bf16 v[220:223], v[4:7], v[144:147], 0\n\t"
      "ds_read_b64 v[240:241], v246 offset:24576\n\t"
      "ds_read_b64 v[242:243], v246 offset:28672\n\t"
      "global_load_dwordx4 v[128:131], v250, s[74:75] offset:-4096 sc1\n\t"
      "v_mfma_f32_16x16x32_bf16 v[220:223], v[20:23], v[148:151], v[220:223]\n\t"
      "v_mfma_f32_16x16x32_bf16 v[220:223], v[36:39], v[152:155], v[220:223]\n\t"
      "s_waitcnt lgkmcnt(4)\n\t"
      "v_add_f32_e32 v228, v228, v230\n\t"
      "v_add_f32_e32 v232, v232, v234\n\t"
      "v_cmp_u_f32_e32 vcc, v216, v216\n\t"
      "s_cbranch_vccnz LA33R_%=\n\t"
      "LA33B_%=:\n\t"
      "v_mfma_f32_16x16x32_bf16 v[220:223], v[52:55], v[156:159], v[220:223]\n\t"
      "v_add_f32_e32 v229, v229, v231\n\t"
      "v_add_f32_e32 v233, v233, v235\n\t"
      "v_mfma_f32_16x16x32_bf16 v[220:223], v[68:71], v[160:163], v[220:223]\n\t"
      "s_waitcnt lgkmcnt(0)\n\t"
      "v_add_f32_e32 v236, v236, v238\n\t"
      "v_add_f32_e32 v240, v240, v242\n\t"
      "global_load_dwordx4 v[132:135], v250, s[74:75] offset:-3072 sc1\n\t"
      "v_mfma_f32_16x16x32_bf16 v[220:223], v[84:87], v[164:167], v[220:223]\n\t"
      "v_add_f32_e32 v237, v237, v239\n\t"
      "v_add_f32_e32 v241, v241, v243\n\t"
      "v_mfma_f32_16x16x32_bf16 v[220:223], v[100:103], v[168:171], v[220:223]\n\t"
      "v_add_f32_e32 v228, v228, v232\n\t"
      "v_add_f32_e32 v236, v236, v240\n\t"
      "v_add_f32_e32 v229, v229, v233\n\t"
      "ds_write_b128 v245, v[216:219] offset:32768\n\t"
      "v_mfma_f32_16x16x32_bf16 v[220:223], v[116:119], v[172:175], v[220:223]\n\t"
      "v_add_f32_e32 v237, v237, v241\n\t"
      "v_add_f32_e32 v228, v228, v248\n\t"
      "v_mfma_f32_16x16x32_bf16 v[224:227], v[8:11], v[144:147], 0\n\t"
      "v_add_f32_e32 v229, v229, v249\n\t"
      "v_add_f32_e32 v228, v228, v236\n\t"
      "v_add_f32_e32 v229, v229, v237\n\t"
      "global_load_dwordx4 v[136:139], v250, s[74:75] offset:-2048 sc1\n\t"
      "v_mfma_f32_16x16x32_bf16 v[224:227], v[24:27], v[148:151], v[224:227]\n\t"
      "v_mul_f32_e32 v230, v228, v228\n\t"
      "v_mul_f32_e32 v231, v229, v229\n\t"
      "v_mfma_f32_16x16x32_bf16 v[224:227], v[40:43], v[152:155], v[224:227]\n\t"
      "v_fmaak_f32 v230, v252, v230, 0xc0135761\n\t"
      "v_fmaak_f32 v231, v252, v231, 0xc0135761\n\t"
      "v_mul_f32_e32 v230, v228, v230\n\t"
      "v_mfma_f32_16x16x32_bf16 v[224:227], v[56:59], v[156:159], v[224:227]\n\t"
      "v_mul_f32_e32 v231, v229, v231\n\t"
      "v_exp_f32_e32 v230, v230\n\t"
      "v_mfma_f32_16x16x32_bf16 v[224:227], v[72:75], v[160:163], v[224:227]\n\t"
      "v_exp_f32_e32 v231, v231\n\t"
      "v_add_f32_e32 v230, 1.0, v230\n\t"
      "v_add_f32_e32 v231, 1.0, v231\n\t"
      "global_load_dwordx4 v[140:143], v250, s[74:75] offset:-1024 sc1\n\t"
      "v_mfma_f32_16x16x32_bf16 v[224:227], v[88:91], v[164:167], v[224:227]\n\t"
      "v_rcp_f32_e32 v230, v230\n\t"
      "v_rcp_f32_e32 v231, v231\n\t"
      "v_mfma_f32_16x16x32_bf16 v[224:227], v[104:107], v[168:171], v[224:227]\n\t"
      "v_mul_f32_e32 v228, v228, v230\n\t"
      "v_mul_f32_e32 v229, v229, v231\n\t"
      "v_cvt_pk_bf16_f32 v232, v228, v229\n\t"
      "ds_write_b128 v253, v[220:223] offset:33792\n\t"
      "v_mfma_f32_16x16x32_bf16 v[224:227], v[120:123], v[172:175], v[224:227]\n\t"
      "v_min_u32_e32 v254, -2, v232\n\t"
      "global_store_dword v247, v254, s[62:63] sc1\n\t"
      "s_add_u32 s62, s62, 57344\n\t"
      "s_addc_u32 s63, s63, 0\n\t"
      "v_mfma_f32_16x16x32_bf16 v[208:211], v[12:15], v[144:147], 0\n\t"
      "global_load_dwordx4 v[144:147], v250, s[74:75] sc1\n\t"
      "v_mfma_f32_16x16x32_bf16 v[208:211], v[28:31], v[148:151], v[208:211]\n\t"
      "v_mfma_f32_16x16x32_bf16 v[208:211], v[44:47], v[152:155], v[208:211]\n\t"
      "v_mfma_f32_16x16x32_bf16 v[208:211], v[60:63], v[156:159], v[208:211]\n\t"
      "v_mfma_f32_16x16x32_bf16 v[208:211], v[76:79], v[160:163], v[208:211]\n\t"
      "global_load_dwordx4 v[148:151], v250, s[74:75] offset:1024 sc1\n\t"
      "v_mfma_f32_16x16x32_bf16 v[208:211], v[92:95], v[164:167], v[208:211]\n\t"
      "v_mfma_f32_16x16x32_bf16 v[208:211], v[108:111], v[168:171], v[208:211]\n\t"
      "ds_write_b128 v245, v[224:227] offset:34816\n\t"
      "v_mfma_f32_16x16x32_bf16 v[208:211], v[124:127], v[172:175], v[208:211]\n\t"
      "s_waitcnt vmcnt(12)\n\t"
      "v_mfma_f32_16x16x32_bf16 v[212:215], v[0:3], v[176:179], 0\n\t"
      "global_load_dwordx4 v[152:155], v250, s[74:75] offset:2048 sc1\n\t"
      "v_mfma_f32_16x16x32_bf16 v[212:215], v[16:19], v[180:183], v[212:215]\n\t"
      "s_nop 3\n\t"
      "ds_write_b128 v253, v[208:211] offset:35840\n\t"
      "v_mfma_f32_16x16x32_bf16 v[212:215], v[32:35], v[184:187], v[212:215]\n\t"
      "v_mfma_f32_16x16x32_bf16 v[212:215], v[48:51], v[188:191], v[212:215]\n\t"
      "s_waitcnt vmcnt(8)\n\t"
      "v_mfma_f32_16x16x32_bf16 v[212:215], v[64:67], v[192:195], v[212:215]\n\t"
      "global_load_dwordx4 v[156:159], v250, s[74:75] offset:3072 sc1\n\t"
      "v_mfma_f32_16x16x32_bf16 v[212:215], v[80:83], v[196:199], v[212:215]\n\t"
      "s_waitcnt lgkmcnt(0)\n\t"
      "s_barrier\n\t"
      "ds_read_b64 v[228:229], v246 offset:32768\n\t"
      "ds_read_b64 v[230:231], v246 offset:36864\n\t"
      "v_mfma_f32_16x16x32_bf16 v[212:215], v[96:99], v[200:203], v[212:215]\n\t"
      "ds_read_b64 v[232:233], v246 offset:40960\n\t"
      "ds_read_b64 v[234:235], v246 offset:45056\n\t"
      "v_mfma_f32_16x16x32_bf16 v[212:215], v[112:115], v[204:207], v[212:215]\n\t"
      "ds_read_b64 v[236:237], v246 offset:49152\n\t"
      "ds_read_b64 v[238:239], v246 offset:53248\n\t"
      "v_mfma_f32_16x16x32_bf16 v[216:219], v[4:7], v[176:179], 0\n\t"
      "ds_read_b64 v[240:241], v246 offset:57344\n\t"
      "ds_read_b64 v[242:243], v246 offset:61440\n\t"
      "global_load_dwordx4 v[160:163], v250, s[76:77] offset:-4096 sc1\n\t"
      "v_mfma_f32_16x16x32_bf16 v[216:219], v[20:23], v[180:183], v[216:219]\n\t"
      "v_mfma_f32_16x16x32_bf16 v[216:219], v[36:39], v[184:187], v[216:219]\n\t"
      "s_waitcnt lgkmcnt(4)\n\t"
      "v_add_f32_e32 v228, v228, v230\n\t"
      "v_add_f32_e32 v232, v232, v234\n\t"
      "v_cmp_u_f32_e32 vcc, v212, v212\n\t"
      "s_cbranch_vccnz LA34R_%=\n\t"
      "LA34B_%=:\n\t"
      "v_mfma_f32_16x16x32_bf16 v[216:219], v[52:55], v[188:191], v[216:219]\n\t"
      "v_add_f32_e32 v229, v229, v231\n\t"
      "v_add_f32_e32 v233, v233, v235\n\t"
      "v_mfma_f32_16x16x32_bf16 v[216:219], v[68:71], v[192:195], v[216:219]\n\t"
      "s_waitcnt lgkmcnt(0)\n\t"
      "v_add_f32_e32 v236, v236, v238\n\t"
      "v_add_f32_e32 v240, v240, v242\n\t"
      "global_load_dwordx4 v[164:167], v250, s[76:77] offset:-3072 sc1\n\t"
      "v_mfma_f32_16x16x32_bf16 v[216:219], v[84:87], v[196:199], v[216:219]\n\t"
      "v_add_f32_e32 v237, v237, v239\n\t"
      "v_add_f32_e32 v241, v241, v243\n\t"
      "v_mfma_f32_16x16x32_bf16 v[216:219], v[100:103], v[200:203], v[216:219]\n\t"
      "v_add_f32_e32 v228, v228, v232\n\t"
      "v_add_f32_e32 v236, v236, v240\n\t"
      "v_add_f32_e32 v229, v229, v233\n\t"
      "ds_write_b128 v245, v[212:215]\n\t"
      "v_mfma_f32_16x16x32_bf16 v[216:219], v[116:119], v[204:207], v[216:219]\n\t"
      "v_add_f32_e32 v237, v237, v241\n\t"
      "v_add_f32_e32 v228, v228, v248\n\t"
      "v_mfma_f32_16x16x32_bf16 v[220:223], v[8:11], v[176:179], 0\n\t"
      "v_add_f32_e32 v229, v229, v249\n\t"
      "v_add_f32_e32 v228, v228, v236\n\t"
      "v_add_f32_e32 v229, v229, v237\n\t"
      "global_load_dwordx4 v[168:171], v250, s[76:77] offset:-2048 sc1\n\t"
      "v_mfma_f32_16x16x32_bf16 v[220:223], v[24:27], v[180:183], v[220:223]\n\t"
      "v_mul_f32_e32 v230, v228, v228\n\t"
      "v_mul_f32_e32 v231, v229, v229\n\t"
      "v_mfma_f32_16x16x32_bf16 v[220:223], v[40:43], v[184:187], v[220:223]\n\t"
      "v_fmaak_f32 v230, v252, v230, 0xc0135761\n\t"
      "v_fmaak_f32 v231, v252, v231, 0xc0135761\n\t"
      "v_mul_f32_e32 v230, v228, v230\n\t"
      "v_mfma_f32_16x16x32_bf16 v[220:223], v[56:59], v[188:191], v[220:223]\n\t"
      "v_mul_f32_e32 v231, v229, v231\n\t"
      "v_exp_f32_e32 v230, v230\n\t"
      "v_mfma_f32_16x16x32_bf16 v[220:223], v[72:75], v[192:195], v[220:223]\n\t"
      "v_exp_f32_e32 v231, v231\n\t"
      "v_add_f32_e32 v230, 1.0, v230\n\t"
      "v_add_f32_e32 v231, 1.0, v231\n\t"
      "global_load_dwordx4 v[172:175], v250, s[76:77] offset:-1024 sc1\n\t"
      "v_mfma_f32_16x16x32_bf16 v[220:223], v[88:91], v[196:199], v[220:223]\n\t"
      "v_rcp_f32_e32 v230, v230\n\t"
      "v_rcp_f32_e32 v231, v231\n\t"
      "v_mfma_f32_16x16x32_bf16 v[220:223], v[104:107], v[200:203], v[220:223]\n\t"
      "v_mul_f32_e32 v228, v228, v230\n\t"
      "v_mul_f32_e32 v229, v229, v231\n\t"
      "v_cvt_pk_bf16_f32 v232, v228, v229\n\t"
      "ds_write_b128 v253, v[216:219] offset:1024\n\t"
      "v_mfma_f32_16x16x32_bf16 v[220:223], v[120:123], v[204:207], v[220:223]\n\t"
      "v_min_u32_e32 v254, -2, v232\n\t"
      "global_store_dword v247, v254, s[62:63] sc1\n\t"
      "s_add_u32 s62, s62, 57344\n\t"
      "s_addc_u32 s63, s63, 0\n\t"
      "v_mfma_f32_16x16x32_bf16 v[224:227], v[12:15], v[176:179], 0\n\t"
      "global_load_dwordx4 v[176:179], v250, s[76:77] sc1\n\t"
      "v_mfma_f32_16x16x32_bf16 v[224:227], v[28:31], v[180:183], v[224:227]\n\t"
      "v_mfma_f32_16x16x32_bf16 v[224:227], v[44:47], v[184:187], v[224:227]\n\t"
      "v_mfma_f32_16x16x32_bf16 v[224:227], v[60:63], v[188:191], v[224:227]\n\t"
      "v_mfma_f32_16x16x32_bf16 v[224:227], v[76:79], v[192:195], v[224:227]\n\t"
      "global_load_dwordx4 v[180:183], v250, s[76:77] offset:1024 sc1\n\t"
      "v_mfma_f32_16x16x32_bf16 v[224:227], v[92:95], v[196:199], v[224:227]\n\t"
      "v_mfma_f32_16x16x32_bf16 v[224:227], v[108:111], v[200:203], v[224:227]\n\t"
      "ds_write_b128 v245, v[220:223] offset:2048\n\t"
      "v_mfma_f32_16x16x32_bf16 v[224:227], v[124:127], v[204:207], v[224:227]\n\t"
      "s_waitcnt vmcnt(12)\n\t"
      "v_mfma_f32_16x16x32_bf16 v[208:211], v[0:3], v[128:131], 0\n\t"
      "global_load_dwordx4 v[184:187], v250, s[76:77] offset:2048 sc1\n\t"
      "v_mfma_f32_16x16x32_bf16 v[208:211], v[16:19], v[132:135], v[208:211]\n\t"
      "s_nop 3\n\t"
      "ds_write_b128 v253, v[224:227] offset:3072\n\t"
      "v_mfma_f32_16x16x32_bf16 v[208:211], v[32:35], v[136:139], v[208:211]\n\t"
      "v_mfma_f32_16x16x32_bf16 v[208:211], v[48:51], v[140:143], v[208:211]\n\t"
      "s_waitcnt vmcnt(8)\n\t"
      "v_mfma_f32_16x16x32_bf16 v[208:211], v[64:67], v[144:147], v[208:211]\n\t"
      "global_load_dwordx4 v[188:191], v250, s[76:77] offset:3072 sc1\n\t"
      "v_mfma_f32_16x16x32_bf16 v[208:211], v[80:83], v[148:151], v[208:211]\n\t"
      "s_waitcnt lgkmcnt(0)\n\t"
      "s_barrier\n\t"
      "ds_read_b64 v[228:229], v246\n\t"
      "ds_read_b64 v[230:231], v246 offset:4096\n\t"
      "v_mfma_f32_16x16x32_bf16 v[208:211], v[96:99], v[152:155], v[208:211]\n\t"
      "ds_read_b64 v[232:233], v246 offset:8192\n\t"
      "ds_read_b64 v[234:235], v246 offset:12288\n\t"
      "v_mfma_f32_16x16x32_bf16 v[208:211], v[112:115], v[156:159], v[208:211]\n\t"
      "ds_read_b64 v[236:237], v246 offset:16384\n\t"
      "ds_read_b64 v[238:239], v246 offset:20480\n\t"
      "v_mfma_f32_16x16x32_bf16 v[212:215], v[4:7], v[128:131], 0\n\t"
      "ds_read_b64 v[240:241], v246 offset:24576\n\t"
      "ds_read_b64 v[242:243], v246 offset:28672\n\t"
      "v_mfma_f32_16x16x32_bf16 v[212:215], v[20:23], v[132:135], v[212:215]\n\t"
      "v_mfma_f32_16x16x32_bf16 v[212:215], v[36:39], v[136:139], v[212:215]\n\t"
      "s_waitcnt lgkmcnt(4)\n\t"
      "v_add_f32_e32 v228, v228, v230\n\t"
      "v_add_f32_e32 v232, v232, v234\n\t"
      "v_cmp_u_f32_e32 vcc, v208, v208\n\t"
      "s_cbranch_vccnz LA35R_%=\n\t"
      "LA35B_%=:\n\t"
      "v_mfma_f32_16x16x32_bf16 v[212:215], v[52:55], v[140:143], v[212:215]\n\t"
      "v_add_f32_e32 v229, v229, v231\n\t"
      "v_add_f32_e32 v233, v233, v235\n\t"
      "v_mfma_f32_16x16x32_bf16 v[212:215], v[68:71], v[144:147], v[212:215]\n\t"
      "s_waitcnt lgkmcnt(0)\n\t"
      "v_add_f32_e32 v236, v236, v238\n\t"
      "v_add_f32_e32 v240, v240, v242\n\t"
      "v_mfma_f32_16x16x32_bf16 v[212:215], v[84:87], v[148:151], v[212:215]\n\t"
      "v_add_f32_e32 v237, v237, v239\n\t"
      "v_add_f32_e32 v241, v241, v243\n\t"
      "v_mfma_f32_16x16x32_bf16 v[212:215], v[100:103], v[152:155], v[212:215]\n\t"
      "v_add_f32_e32 v228, v228, v232\n\t"
      "v_add_f32_e32 v236, v236, v240\n\t"
      "v_add_f32_e32 v229, v229, v233\n\t"
      "ds_write_b128 v245, v[208:211] offset:32768\n\t"
      "v_mfma_f32_16x16x32_bf16 v[212:215], v[116:119], v[156:159], v[212:215]\n\t"
      "v_add_f32_e32 v237, v237, v241\n\t"
      "v_add_f32_e32 v228, v228, v248\n\t"
      "v_mfma_f32_16x16x32_bf16 v[216:219], v[8:11], v[128:131], 0\n\t"
      "v_add_f32_e32 v229, v229, v249\n\t"
      "v_add_f32_e32 v228, v228, v236\n\t"
      "v_add_f32_e32 v229, v229, v237\n\t"
      "v_mfma_f32_16x16x32_bf16 v[216:219], v[24:27], v[132:135], v[216:219]\n\t"
      "v_mul_f32_e32 v230, v228, v228\n\t"
      "v_mul_f32_e32 v231, v229, v229\n\t"
      "v_mfma_f32_16x16x32_bf16 v[216:219], v[40:43], v[136:139], v[216:219]\n\t"
      "v_fmaak_f32 v230, v252, v230, 0xc0135761\n\t"
      "v_fmaak_f32 v231, v252, v231, 0xc0135761\n\t"
      "v_mul_f32_e32 v230, v228, v230\n\t"
      "v_mfma_f32_16x16x32_bf16 v[216:219], v[56:59], v[140:143], v[216:219]\n\t"
      "v_mul_f32_e32 v231, v229, v231\n\t"
      "v_exp_f32_e32 v230, v230\n\t"
      "v_mfma_f32_16x16x32_bf16 v[216:219], v[72:75], v[144:147], v[216:219]\n\t"
      "v_exp_f32_e32 v231, v231\n\t"
      "v_add_f32_e32 v230, 1.0, v230\n\t"
      "v_add_f32_e32 v231, 1.0, v231\n\t"
      "v_mfma_f32_16x16x32_bf16 v[216:219], v[88:91], v[148:151], v[216:219]\n\t"
      "v_rcp_f32_e32 v230, v230\n\t"
      "v_rcp_f32_e32 v231, v231\n\t"
      "v_mfma_f32_16x16x32_bf16 v[216:219], v[104:107], v[152:155], v[216:219]\n\t"
      "v_mul_f32_e32 v228, v228, v230\n\t"
      "v_mul_f32_e32 v229, v229, v231\n\t"
      "v_cvt_pk_bf16_f32 v232, v228, v229\n\t"
      "ds_write_b128 v253, v[212:215] offset:33792\n\t"
      "v_mfma_f32_16x16x32_bf16 v[216:219], v[120:123], v[156:159], v[216:219]\n\t"
      "v_min_u32_e32 v254, -2, v232\n\t"
      "global_store_dword v247, v254, s[62:63] sc1\n\t"
      "s_add_u32 s62, s62, 57344\n\t"
      "s_addc_u32 s63, s63, 0\n\t"
      "v_mfma_f32_16x16x32_bf16 v[220:223], v[12:15], v[128:131], 0\n\t"
      "v_mfma_f32_16x16x32_bf16 v[220:223], v[28:31], v[132:135], v[220:223]\n\t"
      "v_mfma_f32_16x16x32_bf16 v[220:223], v[44:47], v[136:139], v[220:223]\n\t"
      "v_mfma_f32_16x16x32_bf16 v[220:223], v[60:63], v[140:143], v[220:223]\n\t"
      "v_mfma_f32_16x16x32_bf16 v[220:223], v[76:79], v[144:147], v[220:223]\n\t"
      "v_mfma_f32_16x16x32_bf16 v[220:223], v[92:95], v[148:151], v[220:223]\n\t"
      "v_mfma_f32_16x16x32_bf16 v[220:223], v[108:111], v[152:155], v[220:223]\n\t"
      "ds_write_b128 v245, v[216:219] offset:34816\n\t"
      "v_mfma_f32_16x16x32_bf16 v[220:223], v[124:127], v[156:159], v[220:223]\n\t"
      "s_waitcnt vmcnt(6)\n\t"
      "v_mfma_f32_16x16x32_bf16 v[224:227], v[0:3], v[160:163], 0\n\t"
      "v_mfma_f32_16x16x32_bf16 v[224:227], v[16:19], v[164:167], v[224:227]\n\t"
      "s_nop 4\n\t"
      "ds_write_b128 v253, v[220:223] offset:35840\n\t"
      "v_mfma_f32_16x16x32_bf16 v[224:227], v[32:35], v[168:171], v[224:227]\n\t"
      "v_mfma_f32_16x16x32_bf16 v[224:227], v[48:51], v[172:175], v[224:227]\n\t"
      "s_waitcnt vmcnt(1)\n\t"
      "v_mfma_f32_16x16x32_bf16 v[224:227], v[64:67], v[176:179], v[224:227]\n\t"
      "v_mfma_f32_16x16x32_bf16 v[224:227], v[80:83], v[180:183], v[224:227]\n\t"
      "s_waitcnt lgkmcnt(0)\n\t"
      "s_barrier\n\t"
      "ds_read_b64 v[228:229], v246 offset:32768\n\t"
      "ds_read_b64 v[230:231], v246 offset:36864\n\t"
      "v_mfma_f32_16x16x32_bf16 v[224:227], v[96:99], v[184:187], v[224:227]\n\t"
      "ds_read_b64 v[232:233], v246 offset:40960\n\t"
      "ds_read_b64 v[234:235], v246 offset:45056\n\t"
      "v_mfma_f32_16x16x32_bf16 v[224:227], v[112:115], v[188:191], v[224:227]\n\t"
      "ds_read_b64 v[236:237], v246 offset:49152\n\t"
      "ds_read_b64 v[238:239], v246 offset:53248\n\t"
      "v_mfma_f32_16x16x32_bf16 v[208:211], v[4:7], v[160:163], 0\n\t"
      "ds_read_b64 v[240:241], v246 offset:57344\n\t"
      "ds_read_b64 v[242:243], v246 offset:61440\n\t"
      "v_mfma_f32_16x16x32_bf16 v[208:211], v[20:23], v[164:167], v[208:211]\n\t"
      "v_mfma_f32_16x16x32_bf16 v[208:211], v[36:39], v[168:171], v[208:211]\n\t"
      "s_waitcnt lgkmcnt(4)\n\t"
      "v_add_f32_e32 v228, v228, v230\n\t"
      "v_add_f32_e32 v232, v232, v234\n\t"
      "v_cmp_u_f32_e32 vcc, v224, v224\n\t"
      "s_cbranch_vccnz LA36R_%=\n\t"
      "LA36B_%=:\n\t"
      "v_mfma_f32_16x16x32_bf16 v[208:211], v[52:55], v[172:175], v[208:211]\n\t"
      "v_add_f32_e32 v229, v229, v231\n\t"
      "v_add_f32_e32 v233, v233, v235\n\t"
      "v_mfma_f32_16x16x32_bf16 v[208:211], v[68:71], v[176:179], v[208:211]\n\t"
      "s_waitcnt lgkmcnt(0)\n\t"
      "v_add_f32_e32 v236, v236, v238\n\t"
      "v_add_f32_e32 v240, v240, v242\n\t"
      "v_mfma_f32_16x16x32_bf16 v[208:211], v[84:87], v[180:183], v[208:211]\n\t"
      "v_add_f32_e32 v237, v237, v239\n\t"
      "v_add_f32_e32 v241, v241, v243\n\t"
      "v_mfma_f32_16x16x32_bf16 v[208:211], v[100:103], v[184:187], v[208:211]\n\t"
      "v_add_f32_e32 v228, v228, v232\n\t"
      "v_add_f32_e32 v236, v236, v240\n\t"
      "v_add_f32_e32 v229, v229, v233\n\t"
      "ds_write_b128 v245, v[224:227]\n\t"
      "v_mfma_f32_16x16x32_bf16 v[208:211], v[116:119], v[188:191], v[208:211]\n\t"
      "v_add_f32_e32 v237, v237, v241\n\t"
      "v_add_f32_e32 v228, v228, v248\n\t"
      "v_mfma_f32_16x16x32_bf16 v[212:215], v[8:11], v[160:163], 0\n\t"
      "v_add_f32_e32 v229, v229, v249\n\t"
      "v_add_f32_e32 v228, v228, v236\n\t"
      "v_add_f32_e32 v229, v229, v237\n\t"
      "v_mfma_f32_16x16x32_bf16 v[212:215], v[24:27], v[164:167], v[212:215]\n\t"
      "v_mul_f32_e32 v230, v228, v228\n\t"
      "v_mul_f32_e32 v231, v229, v229\n\t"
      "v_mfma_f32_16x16x32_bf16 v[212:215], v[40:43], v[168:171], v[212:215]\n\t"
      "v_fmaak_f32 v230, v252, v230, 0xc0135761\n\t"
      "v_fmaak_f32 v231, v252, v231, 0xc0135761\n\t"
      "v_mul_f32_e32 v230, v228, v230\n\t"
      "v_mfma_f32_16x16x32_bf16 v[212:215], v[56:59], v[172:175], v[212:215]\n\t"
      "v_mul_f32_e32 v231, v229, v231\n\t"
      "v_exp_f32_e32 v230, v230\n\t"
      "v_mfma_f32_16x16x32_bf16 v[212:215], v[72:75], v[176:179], v[212:215]\n\t"
      "v_exp_f32_e32 v231, v231\n\t"
      "v_add_f32_e32 v230, 1.0, v230\n\t"
      "v_add_f32_e32 v231, 1.0, v231\n\t"
      "v_mfma_f32_16x16x32_bf16 v[212:215], v[88:91], v[180:183], v[212:215]\n\t"
      "v_rcp_f32_e32 v230, v230\n\t"
      "v_rcp_f32_e32 v231, v231\n\t"
      "v_mfma_f32_16x16x32_bf16 v[212:215], v[104:107], v[184:187], v[212:215]\n\t"
      "v_mul_f32_e32 v228, v228, v230\n\t"
      "v_mul_f32_e32 v229, v229, v231\n\t"
      "v_cvt_pk_bf16_f32 v232, v228, v229\n\t"
      "ds_write_b128 v253, v[208:211] offset:1024\n\t"
      "v_mfma_f32_16x16x32_bf16 v[212:215], v[120:123], v[188:191], v[212:215]\n\t"
      "v_min_u32_e32 v254, -2, v232\n\t"
      "global_store_dword v247, v254, s[62:63] sc1\n\t"
      "s_add_u32 s62, s62, 57344\n\t"
      "s_addc_u32 s63, s63, 0\n\t"
      "v_mfma_f32_16x16x32_bf16 v[216:219], v[12:15], v[160:163], 0\n\t"
      "v_mfma_f32_16x16x32_bf16 v[216:219], v[28:31], v[164:167], v[216:219]\n\t"
      "v_mfma_f32_16x16x32_bf16 v[216:219], v[44:47], v[168:171], v[216:219]\n\t"
      "v_mfma_f32_16x16x32_bf16 v[216:219], v[60:63], v[172:175], v[216:219]\n\t"
      "v_mfma_f32_16x16x32_bf16 v[216:219], v[76:79], v[176:179], v[216:219]\n\t"
      "v_mfma_f32_16x16x32_bf16 v[216:219], v[92:95], v[180:183], v[216:219]\n\t"
      "v_mfma_f32_16x16x32_bf16 v[216:219], v[108:111], v[184:187], v[216:219]\n\t"
      "ds_write_b128 v245, v[212:215] offset:2048\n\t"
      "v_mfma_f32_16x16x32_bf16 v[216:219], v[124:127], v[188:191], v[216:219]\n\t"
      "s_nop 7\n\t"
      "ds_write_b128 v253, v[216:219] offset:3072\n\t"
      "s_waitcnt lgkmcnt(0)\n\t"
      "s_barrier\n\t"
      "ds_read_b64 v[228:229], v246\n\t"
      "ds_read_b64 v[230:231], v246 offset:4096\n\t"
      "ds_read_b64 v[232:233], v246 offset:8192\n\t"
      "ds_read_b64 v[234:235], v246 offset:12288\n\t"
      "ds_read_b64 v[236:237], v246 offset:16384\n\t"
      "ds_read_b64 v[238:239], v246 offset:20480\n\t"
      "ds_read_b64 v[240:241], v246 offset:24576\n\t"
      "ds_read_b64 v[242:243], v246 offset:28672\n\t"
      "s_waitcnt lgkmcnt(4)\n\t"
      "v_add_f32_e32 v228, v228, v230\n\t"
      "v_add_f32_e32 v232, v232, v234\n\t"
      "v_add_f32_e32 v229, v229, v231\n\t"
      "v_add_f32_e32 v233, v233, v235\n\t"
      "s_waitcnt lgkmcnt(0)\n\t"
      "v_add_f32_e32 v236, v236, v238\n\t"
      "v_add_f32_e32 v240, v240, v242\n\t"
      "v_add_f32_e32 v237, v237, v239\n\t"
      "v_add_f32_e32 v241, v241, v243\n\t"
      "v_add_f32_e32 v228, v228, v232\n\t"
      "v_add_f32_e32 v236, v236, v240\n\t"
      "v_add_f32_e32 v229, v229, v233\n\t"
      "v_add_f32_e32 v237, v237, v241\n\t"
      "v_add_f32_e32 v228, v228, v248\n\t"
      "v_add_f32_e32 v229, v229, v249\n\t"
      "v_add_f32_e32 v228, v228, v236\n\t"
      "v_add_f32_e32 v229, v229, v237\n\t"
      "v_mul_f32_e32 v230, v228, v228\n\t"
      "v_mul_f32_e32 v231, v229, v229\n\t"
      "v_fmaak_f32 v230, v252, v230, 0xc0135761\n\t"
      "v_fmaak_f32 v231, v252, v231, 0xc0135761\n\t"
      "v_mul_f32_e32 v230, v228, v230\n\t"
      "v_mul_f32_e32 v231, v229, v231\n\t"
      "v_exp_f32_e32 v230, v230\n\t"
      "v_exp_f32_e32 v231, v231\n\t"
      "v_add_f32_e32 v230, 1.0, v230\n\t"
      "v_add_f32_e32 v231, 1.0, v231\n\t"
      "v_rcp_f32_e32 v230, v230\n\t"
      "v_rcp_f32_e32 v231, v231\n\t"
      "v_mul_f32_e32 v228, v228, v230\n\t"
      "v_mul_f32_e32 v229, v229, v231\n\t"
      "v_cvt_pk_bf16_f32 v232, v228, v229\n\t"
      "v_min_u32_e32 v254, -2, v232\n\t"
      "global_store_dword v247, v254, s[62:63] sc1\n\t"
      "s_add_u32 s62, s62, 57344\n\t"
      "s_addc_u32 s63, s63, 0\n\t"
      "s_or_b32 s34, s2, s24\n\t"
      "s_cbranch_scc1 EPB_%=\n\t"
      "v_mov_b32_e32 v228, s22\n\t"
      "v_mov_b32_e32 v229, s23\n\t"
      "v_mov_b32_e32 v230, 0\n\t"
      "s_mov_b64 exec, 1\n\t"
      "global_store_dwordx2 v230, v[228:229], s[60:61] sc1\n\t"
      "s_mov_b64 exec, -1\n\t"
      "EPB_%=:\n\t"
      "s_endpgm\n\t"
      "LPATH_%=:\n\t"
      "s_add_u32 s22, s22, 1\n\t"
      "s_addc_u32 s23, s23, 0\n\t"
      "s_add_u32 s72, s26, 1\n\t"
      "s_and_b32 s72, s72, 6\n\t"
      "s_cselect_b32 s72, 0, 2\n\t"
      "s_or_b32 s27, s27, s72\n\t"
      "s_mul_i32 s72, s81, 172032\n\t"
      "s_add_u32 s72, s72, s37\n\t"
      "s_add_u32 s40, s6, s72\n\t"
      "s_addc_u32 s41, s7, 0\n\t"
      "s_add_u32 s81, s81, 1\n\t"
      "s_cmp_eq_u32 s81, 7\n\t"
      "s_cselect_b32 s81, 0, s81\n\t"
      "s_add_u32 m0, s39, 81920\n\t"
      "s_nop 0\n\t"
      "global_load_lds_dwordx4 v148, s[40:41]\n\t"
      "global_load_lds_dwordx4 v149, s[40:41] offset:1024\n\t"
      "global_load_lds_dwordx4 v150, s[40:41] offset:2048\n\t"
      "global_load_lds_dwordx4 v151, s[40:41] offset:3072\n\t"
      "s_mul_i32 s72, s81, 172032\n\t"
      "s_add_u32 s72, s72, s37\n\t"
      "s_add_u32 s40, s6, s72\n\t"
      "s_addc_u32 s41, s7, 0\n\t"
      "s_add_u32 s81, s81, 1\n\t"
      "s_cmp_eq_u32 s81, 7\n\t"
      "s_cselect_b32 s81, 0, s81\n\t"
      "s_add_u32 m0, s39, 98304\n\t"
      "s_nop 0\n\t"
      "global_load_lds_dwordx4 v148, s[40:41]\n\t"
      "global_load_lds_dwordx4 v149, s[40:41] offset:1024\n\t"
      "s_waitcnt vmcnt(14)\n\t"
      "s_barrier\n\t"
      "v_add_u32_e32 v172, 0x10000, v224\n\t"
      "v_add_u32_e32 v173, 0x10000, v225\n\t"
      "v_add_u32_e32 v174, 0x10000, v226\n\t"
      "v_add_u32_e32 v175, 0x10000, v227\n\t"
      "s_lshl_b32 s34, s25, 8\n\t"
      "s_add_u32 s34, s34, 4096\n\t"
      "v_and_b32_e32 v136, 1, v192\n\t"
      "v_lshl_add_u32 v136, v136, 7, s34\n\t"
      "s_lshl_b32 s34, s25, 7\n\t"
      "s_add_u32 s34, s34, 16384\n\t"
      "v_mov_b32_e32 v137, s34\n\t"
      "s_lshl_b32 s34, s2, 7\n\t"
      "s_add_u32 s34, s34, 32768\n\t"
      "v_mov_b32_e32 v143, s34\n\t"
      "s_bitcmp0_b32 s27, 1\n\t"
      "s_cbranch_scc1 SK6_%=\n\t"
      "s_lshr_b32 s73, s26, 2\n\t"
      "s_lshl1_add_u32 s73, s25, s73\n\t"
      "s_lshl_b32 s72, s73, 7\n\t"
      "s_add_u32 s72, s72, 4096\n\t"
      "v_mov_b32_e32 v130, s72\n\t"
      "s_lshl2_add_u32 s75, s73, s24\n\t"
      "v_mov_b32_e32 v128, s22\n\t"
      "v_mov_b32_e32 v129, s23\n\t"
      "v_lshrrev_b32_e32 v140, 1, v244\n\t"
      "s_lshl_b32 s72, s75, 9\n\t"
      "s_add_u32 s72, s72, 12582912\n\t"
      "v_mov_b32_e32 v132, -1\n\t"
      "v_mov_b32_e32 v133, -1\n\t"
      "v_mov_b32_e32 v134, -1\n\t"
      "v_mov_b32_e32 v135, -1\n\t"
      "s_add_u32 s42, s20, s72\n\t"
      "s_addc_u32 s43, s21, 0\n\t"
      "global_store_dwordx2 v140, v[132:133], s[42:43] sc1\n\t"
      "s_lshl_b32 s72, s75, 13\n\t"
      "s_add_u32 s72, s72, 4096\n\t"
      "s_add_u32 s42, s20, s72\n\t"
      "s_addc_u32 s43, s21, 0\n\t"
      "global_store_dwordx4 v244, v[132:135], s[42:43] offset:-4096 sc1\n\t"
      "global_store_dwordx4 v244, v[132:135], s[42:43] offset:-3072 sc1\n\t"
      "global_store_dwordx4 v244, v[132:135], s[42:43] offset:-2048 sc1\n\t"
      "SK6_%=:\n\t"
      "s_waitcnt vmcnt(10)\n\t"
      "s_barrier\n\t"
      "global_load_lds_dwordx4 v150, s[40:41] offset:2048\n\t"
      "global_load_lds_dwordx4 v151, s[40:41] offset:3072\n\t"
      "s_bitcmp0_b32 s27, 1\n\t"
      "s_cbranch_scc1 SK7_%=\n\t"
      "global_store_dwordx4 v244, v[132:135], s[42:43] offset:-1024 sc1\n\t"
      "global_store_dwordx4 v244, v[132:135], s[42:43] sc1\n\t"
      "global_store_dwordx4 v244, v[132:135], s[42:43] offset:1024 sc1\n\t"
      "SK7_%=:\n\t"
      "s_waitcnt vmcnt(8)\n\t"
      "s_barrier\n\t"
      "s_mul_i32 s72, s81, 172032\n\t"
      "s_add_u32 s72, s72, s37\n\t"
      "s_add_u32 s40, s6, s72\n\t"
      "s_addc_u32 s41, s7, 0\n\t"
      "s_add_u32 s81, s81, 1\n\t"
      "s_cmp_eq_u32 s81, 7\n\t"
      "s_cselect_b32 s81, 0, s81\n\t"
      "s_add_u32 m0, s39, 114688\n\t"
      "s_nop 0\n\t"
      "global_load_lds_dwordx4 v148, s[40:41]\n\t"
      "global_load_lds_dwordx4 v149, s[40:41] offset:1024\n\t"
      "global_load_lds_dwordx4 v150, s[40:41] offset:2048\n\t"
      "global_load_lds_dwordx4 v151, s[40:41] offset:3072\n\t"
      "s_bitcmp0_b32 s27, 1\n\t"
      "s_cbranch_scc1 SK8_%=\n\t"
      "global_store_dwordx4 v244, v[132:135], s[42:43] offset:2048 sc1\n\t"
      "global_store_dwordx4 v244, v[132:135], s[42:43] offset:3072 sc1\n\t"
      "SK8_%=:\n\t"
      "s_waitcnt vmcnt(8)\n\t"
      "s_barrier\n\t"
      "s_mul_i32 s72, s81, 172032\n\t"
      "s_add_u32 s72, s72, s37\n\t"
      "s_add_u32 s40, s6, s72\n\t"
      "s_addc_u32 s41, s7, 0\n\t"
      "s_add_u32 s81, s81, 1\n\t"
      "s_cmp_eq_u32 s81, 7\n\t"
      "s_cselect_b32 s81, 0, s81\n\t"
      "s_add_u32 m0, s39, 131072\n\t"
      "s_nop 0\n\t"
      "global_load_lds_dwordx4 v148, s[40:41]\n\t"
      "global_load_lds_dwordx4 v149, s[40:41] offset:1024\n\t"
      "global_load_lds_dwordx4 v150, s[40:41] offset:2048\n\t"
      "global_load_lds_dwordx4 v151, s[40:41] offset:3072\n\t"
      "s_waitcnt vmcnt(8)\n\t"
      "s_bitcmp0_b32 s27, 1\n\t"
      "s_cbranch_scc1 SK9_%=\n\t"
      "s_waitcnt vmcnt(4)\n\t"
      "SK9_%=:\n\t"
      "s_barrier\n\t"
      "s_mul_i32 s72, s81, 172032\n\t"
      "s_add_u32 s72, s72, s37\n\t"
      "s_add_u32 s40, s6, s72\n\t"
      "s_addc_u32 s41, s7, 0\n\t"
      "s_add_u32 s81, s81, 1\n\t"
      "s_cmp_eq_u32 s81, 7\n\t"
      "s_cselect_b32 s81, 0, s81\n\t"
      "s_add_u32 m0, s39, 147456\n\t"
      "s_nop 0\n\t"
      "global_load_lds_dwordx4 v148, s[40:41]\n\t"
      "global_load_lds_dwordx4 v149, s[40:41] offset:1024\n\t"
      "global_load_lds_dwordx4 v150, s[40:41] offset:2048\n\t"
      "global_load_lds_dwordx4 v151, s[40:41] offset:3072\n\t"
      "s_bitcmp0_b32 s27, 1\n\t"
      "s_cbranch_scc1 SK10_%=\n\t"
      "s_cmp_eq_u32 s24, 0\n\t"
      "s_cselect_b64 exec, 1, 0\n\t"
      "global_store_dwordx2 v130, v[128:129], s[60:61] sc1\n\t"
      "s_mov_b64 exec, -1\n\t"
      "s_mov_b32 m0, 64512\n\t"
      "s_mul_i32 s72, s75, 13\n\t"
      "v_min_u32_e32 v141, 12, v192\n\t"
      "v_add_u32_e32 v141, s72, v141\n\t"
      "v_min_u32_e32 v141, 3135, v141\n\t"
      "v_lshlrev_b32_e32 v141, 7, v141\n\t"
      "global_load_lds_dword v141, s[8:9]\n\t"
      "SK10_%=:\n\t"
      "s_waitcnt vmcnt(8)\n\t"
      "s_barrier\n\t"
      "s_mul_i32 s72, s82, 172032\n\t"
      "s_add_u32 s72, s72, s37\n\t"
      "s_add_u32 s72, s72, 1792\n\t"
      "s_add_u32 s40, s6, s72\n\t"
      "s_addc_u32 s41, s7, 0\n\t"
      "s_add_u32 s82, s82, 1\n\t"
      "s_cmp_eq_u32 s82, 7\n\t"
      "s_cselect_b32 s82, 0, s82\n\t"
      "s_add_u32 m0, s39, 65536\n\t"
      "s_nop 0\n\t"
      "global_load_lds_dwordx4 v148, s[40:41]\n\t"
      "global_load_lds_dwordx4 v152, s[40:41] offset:1024\n\t"
      "s_mul_i32 s72, s82, 172032\n\t"
      "s_add_u32 s72, s72, s37\n\t"
      "s_add_u32 s72, s72, 1792\n\t"
      "s_add_u32 s40, s6, s72\n\t"
      "s_addc_u32 s41, s7, 0\n\t"
      "s_add_u32 s82, s82, 1\n\t"
      "s_cmp_eq_u32 s82, 7\n\t"
      "s_cselect_b32 s82, 0, s82\n\t"
      "s_add_u32 m0, s39, 67584\n\t"
      "s_nop 0\n\t"
      "global_load_lds_dwordx4 v148, s[40:41]\n\t"
      "global_load_lds_dwordx4 v152, s[40:41] offset:1024\n\t"
      "s_waitcnt vmcnt(8)\n\t"
      "s_barrier\n\t"
      "s_mul_i32 s72, s82, 172032\n\t"
      "s_add_u32 s72, s72, s37\n\t"
      "s_add_u32 s72, s72, 1792\n\t"
      "s_add_u32 s40, s6, s72\n\t"
      "s_addc_u32 s41, s7, 0\n\t"
      "s_add_u32 s82, s82, 1\n\t"
      "s_cmp_eq_u32 s82, 7\n\t"
      "s_cselect_b32 s82, 0, s82\n\t"
      "s_add_u32 m0, s39, 81920\n\t"
      "s_nop 0\n\t"
      "global_load_lds_dwordx4 v148, s[40:41]\n\t"
      "global_load_lds_dwordx4 v152, s[40:41] offset:1024\n\t"
      "s_mul_i32 s72, s82, 172032\n\t"
      "s_add_u32 s72, s72, s37\n\t"
      "s_add_u32 s72, s72, 1792\n\t"
      "s_add_u32 s40, s6, s72\n\t"
      "s_addc_u32 s41, s7, 0\n\t"
      "s_add_u32 s82, s82, 1\n\t"
      "s_cmp_eq_u32 s82, 7\n\t"
      "s_cselect_b32 s82, 0, s82\n\t"
      "s_add_u32 m0, s39, 83968\n\t"
      "s_nop 0\n\t"
      "global_load_lds_dwordx4 v148, s[40:41]\n\t"
      "global_load_lds_dwordx4 v152, s[40:41] offset:1024\n\t"
      "s_waitcnt vmcnt(8)\n\t"
      "s_barrier\n\t"
      "s_cmp_lg_u32 s24, 3\n\t"
      "s_cbranch_scc1 SK11_%=\n\t"
      "global_load_dwordx2 v[138:139], v136, s[60:61] sc1\n\t"
      "SK11_%=:\n\t"
      "s_mul_i32 s72, s82, 172032\n\t"
      "s_add_u32 s72, s72, s37\n\t"
      "s_add_u32 s72, s72, 1792\n\t"
      "s_add_u32 s40, s6, s72\n\t"
      "s_addc_u32 s41, s7, 0\n\t"
      "s_add_u32 s82, s82, 1\n\t"
      "s_cmp_eq_u32 s82, 7\n\t"
      "s_cselect_b32 s82, 0, s82\n\t"
      "s_lshr_b32 s72, s39, 1\n\t"
      "s_add_u32 m0, s72, 98304\n\t"
      "s_nop 0\n\t"
      "global_load_lds_dwordx4 v148, s[40:41]\n\t"
      "global_load_lds_dwordx4 v152, s[40:41] offset:1024\n\t"
      "global_load_dword v249, v248, s[12:13]\n\t"
      "s_lshl2_add_u32 s72, s31, s24\n\t"
      "s_and_b32 s73, s72, 0x70\n\t"
      "s_add_u32 s72, s72, s73\n\t"
      "s_mov_b32 m0, 64512\n\t"
      "s_mul_i32 s72, s72, 28672\n\t"
      "s_and_b32 s73, s2, 7\n\t"
      "s_mul_i32 s73, s73, 448\n\t"
      "s_and_b32 s73, s73, 0xffffff80\n\t"
      "s_add_u32 s72, s72, s73\n\t"
      "v_bfe_u32 v141, v192, 2, 3\n\t"
      "s_cmp_ge_u32 s31, 28\n\t"
      "s_cselect_b32 s73, 32, 0\n\t"
      "v_and_b32_e32 v142, s73, v192\n\t"
      "v_lshl_or_b32 v141, v142, 2, v141\n\t"
      "v_mul_u32_u24_e32 v141, 3584, v141\n\t"
      "v_lshl_add_u32 v141, v196, 7, v141\n\t"
      "v_add_u32_e32 v141, s72, v141\n\t"
      "global_load_lds_dword v141, s[10:11]\n\t"
      "s_waitcnt vmcnt(8)\n\t"
      "s_barrier\n\t"
      "s_mul_i32 s72, s24, 14336\n\t"
      "s_add_u32 s40, s56, s72\n\t"
      "s_addc_u32 s41, s57, 0\n\t"
      "s_lshr_b32 s72, s39, 1\n\t"
      "s_add_u32 m0, s72, 106496\n\t"
      "s_nop 0\n\t"
      "global_load_lds_dwordx4 v154, s[40:41]\n\t"
      "global_load_lds_dwordx4 v155, s[40:41] offset:1024\n\t"
      "s_waitcnt vmcnt(6)\n\t"
      "s_barrier\n\t"
      "s_mul_i32 s72, s24, 28672\n\t"
      "s_add_u32 s72, s72, 57344\n\t"
      "s_add_u32 s40, s56, s72\n\t"
      "s_addc_u32 s41, s57, 0\n\t"
      "s_lshr_b32 s72, s39, 0\n\t"
      "s_add_u32 m0, s72, 114688\n\t"
      "s_nop 0\n\t"
      "global_load_lds_dwordx4 v154, s[40:41]\n\t"
      "global_load_lds_dwordx4 v155, s[40:41] offset:1024\n\t"
      "global_load_lds_dwordx4 v156, s[40:41] offset:2048\n\t"
      "global_load_lds_dwordx4 v157, s[40:41] offset:3072\n\t"
      "s_waitcnt vmcnt(8)\n\t"
      "s_cmp_lg_u32 s24, 3\n\t"
      "s_cbranch_scc1 LF1B_%=\n\t"
      "v_cmp_ne_u64_e32 vcc, s[22:23], v[138:139]\n\t"
      "s_cbranch_vccnz LF1R_%=\n\t"
      "LF1B_%=:\n\t"
      "s_barrier\n\t"
      "s_mul_i32 s72, s24, 28672\n\t"
      "s_add_u32 s72, s72, 172032\n\t"
      "s_add_u32 s40, s56, s72\n\t"
      "s_addc_u32 s41, s57, 0\n\t"
      "s_lshr_b32 s72, s39, 0\n\t"
      "s_add_u32 m0, s72, 131072\n\t"
      "s_nop 0\n\t"
      "global_load_lds_dwordx4 v154, s[40:41]\n\t"
      "global_load_lds_dwordx4 v155, s[40:41] offset:1024\n\t"
      "global_load_lds_dwordx4 v156, s[40:41] offset:2048\n\t"
      "global_load_lds_dwordx4 v157, s[40:41] offset:3072\n\t"
      "s_mul_i32 s72, s24, 28672\n\t"
      "s_add_u32 s72, s72, 286720\n\t"
      "s_add_u32 s40, s56, s72\n\t"
      "s_addc_u32 s41, s57, 0\n\t"
      "s_lshr_b32 s72, s39, 0\n\t"
      "s_add_u32 m0, s72, 147456\n\t"
      "s_nop 0\n\t"
      "global_load_lds_dwordx4 v154, s[40:41]\n\t"
      "global_load_lds_dwordx4 v155, s[40:41] offset:1024\n\t"
      "global_load_lds_dwordx4 v156, s[40:41] offset:2048\n\t"
      "global_load_lds_dwordx4 v157, s[40:41] offset:3072\n\t"
      "s_barrier\n\t"
      "s_cmp_eq_u32 s26, 7\n\t"
      "s_cbranch_scc1 SPARE_%=\n\t"
      "s_lshl_b32 s72, s3, 10\n\t"
      "s_add_u32 s72, s72, 98304\n\t"
      "v_xor_b32_e32 v203, v197, v198\n\t"
      "v_lshl_add_u32 v203, v203, 4, s72\n\t"
      "v_lshl_add_u32 v203, v197, 7, v203\n\t"
      "ds_write_b128 v203, v[204:207]\n\t"
      "s_waitcnt lgkmcnt(0)\n\t"
      "s_bitcmp0_b32 s27, 0\n\t"
      "s_cbranch_scc1 L4_%=\n\t"
      "s_add_u32 s72, s76, 0\n\t"
      "s_add_u32 s40, s52, s72\n\t"
      "s_addc_u32 s41, s53, 0\n\t"
      "s_add_u32 m0, s39, 65536\n\t"
      "s_nop 0\n\t"
      "global_load_lds_dwordx4 v144, s[40:41]\n\t"
      "global_load_lds_dwordx4 v145, s[40:41] offset:1024\n\t"
      "global_load_lds_dwordx4 v146, s[40:41] offset:2048\n\t"
      "global_load_lds_dwordx4 v147, s[40:41] offset:3072\n\t"
      "s_add_u32 s72, s76, 229376\n\t"
      "s_add_u32 s40, s52, s72\n\t"
      "s_addc_u32 s41, s53, 0\n\t"
      "s_add_u32 m0, s39, 81920\n\t"
      "s_nop 0\n\t"
      "global_load_lds_dwordx4 v144, s[40:41]\n\t"
      "global_load_lds_dwordx4 v145, s[40:41] offset:1024\n\t"
      "global_load_lds_dwordx4 v146, s[40:41] offset:2048\n\t"
      "global_load_lds_dwordx4 v147, s[40:41] offset:3072\n\t"
      "s_waitcnt vmcnt(8)\n\t"
      "s_barrier\n\t"
      "s_add_u32 s72, s76, 3670016\n\t"
      "s_add_u32 s40, s52, s72\n\t"
      "s_addc_u32 s41, s53, 0\n\t"
      "s_add_u32 m0, s39, 16384\n\t"
      "s_nop 0\n\t"
      "global_load_lds_dwordx4 v144, s[40:41]\n\t"
      "global_load_lds_dwordx4 v145, s[40:41] offset:1024\n\t"
      "global_load_lds_dwordx4 v146, s[40:41] offset:2048\n\t"
      "global_load_lds_dwordx4 v147, s[40:41] offset:3072\n\t"
      "s_barrier\n\t"
      "s_add_u32 s72, s76, 3899392\n\t"
      "s_add_u32 s40, s52, s72\n\t"
      "s_addc_u32 s41, s53, 0\n\t"
      "s_add_u32 m0, s39, 49152\n\t"
      "s_nop 0\n\t"
      "global_load_lds_dwordx4 v144, s[40:41]\n\t"
      "global_load_lds_dwordx4 v145, s[40:41] offset:1024\n\t"
      "global_load_lds_dwordx4 v146, s[40:41] offset:2048\n\t"
      "global_load_lds_dwordx4 v147, s[40:41] offset:3072\n\t"
      "s_waitcnt vmcnt(12)\n\t"
      "ds_read_b64_tr_b16 v[0:1], v172\n\t"
      "ds_read_b64_tr_b16 v[2:3], v172 offset:512\n\t"
      "ds_read_b64_tr_b16 v[4:5], v173\n\t"
      "ds_read_b64_tr_b16 v[6:7], v173 offset:512\n\t"
      "ds_read_b64_tr_b16 v[8:9], v174\n\t"
      "ds_read_b64_tr_b16 v[10:11], v174 offset:512\n\t"
      "ds_read_b64_tr_b16 v[12:13], v175\n\t"
      "ds_read_b64_tr_b16 v[14:15], v175 offset:512\n\t"
      "s_waitcnt lgkmcnt(0)\n\t"
      "s_add_u32 s72, s77, 0\n\t"
      "s_add_u32 s40, s52, s72\n\t"
      "s_addc_u32 s41, s53, 0\n\t"
      "s_add_u32 m0, s39, 65536\n\t"
      "s_nop 0\n\t"
      "global_load_lds_dwordx4 v144, s[40:41]\n\t"
      "global_load_lds_dwordx4 v145, s[40:41] offset:1024\n\t"
      "global_load_lds_dwordx4 v146, s[40:41] offset:2048\n\t"
      "global_load_lds_dwordx4 v147, s[40:41] offset:3072\n\t"
      "s_waitcnt vmcnt(12)\n\t"
      "ds_read_b64_tr_b16 v[16:17], v172 offset:16384\n\t"
      "ds_read_b64_tr_b16 v[18:19], v172 offset:16896\n\t"
      "ds_read_b64_tr_b16 v[20:21], v173 offset:16384\n\t"
      "ds_read_b64_tr_b16 v[22:23], v173 offset:16896\n\t"
      "ds_read_b64_tr_b16 v[24:25], v174 offset:16384\n\t"
      "ds_read_b64_tr_b16 v[26:27], v174 offset:16896\n\t"
      "ds_read_b64_tr_b16 v[28:29], v175 offset:16384\n\t"
      "ds_read_b64_tr_b16 v[30:31], v175 offset:16896\n\t"
      "s_waitcnt lgkmcnt(0)\n\t"
      "s_add_u32 s72, s77, 229376\n\t"
      "s_add_u32 s40, s52, s72\n\t"
      "s_addc_u32 s41, s53, 0\n\t"
      "s_add_u32 m0, s39, 81920\n\t"
      "s_nop 0\n\t"
      "global_load_lds_dwordx4 v144, s[40:41]\n\t"
      "global_load_lds_dwordx4 v145, s[40:41] offset:1024\n\t"
      "global_load_lds_dwordx4 v146, s[40:41] offset:2048\n\t"
      "global_load_lds_dwordx4 v147, s[40:41] offset:3072\n\t"
      "s_add_u32 s72, s77, 3670016\n\t"
      "s_add_u32 s40, s52, s72\n\t"
      "s_addc_u32 s41, s53, 0\n\t"
      "s_add_u32 m0, s39, 0\n\t"
      "s_nop 0\n\t"
      "global_load_lds_dwordx4 v144, s[40:41]\n\t"
      "global_load_lds_dwordx4 v145, s[40:41] offset:1024\n\t"
      "global_load_lds_dwordx4 v146, s[40:41] offset:2048\n\t"
      "global_load_lds_dwordx4 v147, s[40:41] offset:3072\n\t"
      "s_add_u32 s72, s77, 3899392\n\t"
      "s_add_u32 s40, s52, s72\n\t"
      "s_addc_u32 s41, s53, 0\n\t"
      "s_add_u32 m0, s39, 32768\n\t"
      "s_nop 0\n\t"
      "global_load_lds_dwordx4 v144, s[40:41]\n\t"
      "global_load_lds_dwordx4 v145, s[40:41] offset:1024\n\t"
      "global_load_lds_dwordx4 v146, s[40:41] offset:2048\n\t"
      "global_load_lds_dwordx4 v147, s[40:41] offset:3072\n\t"
      "s_waitcnt vmcnt(12)\n\t"
      "ds_read_b64_tr_b16 v[32:33], v172\n\t"
      "ds_read_b64_tr_b16 v[34:35], v172 offset:512\n\t"
      "ds_read_b64_tr_b16 v[36:37], v173\n\t"
      "ds_read_b64_tr_b16 v[38:39], v173 offset:512\n\t"
      "ds_read_b64_tr_b16 v[40:41], v174\n\t"
      "ds_read_b64_tr_b16 v[42:43], v174 offset:512\n\t"
      "ds_read_b64_tr_b16 v[44:45], v175\n\t"
      "ds_read_b64_tr_b16 v[46:47], v175 offset:512\n\t"
      "s_waitcnt lgkmcnt(0)\n\t"
      "s_add_u32 s72, s78, 0\n\t"
      "s_add_u32 s40, s52, s72\n\t"
      "s_addc_u32 s41, s53, 0\n\t"
      "s_add_u32 m0, s39, 65536\n\t"
      "s_nop 0\n\t"
      "global_load_lds_dwordx4 v144, s[40:41]\n\t"
      "global_load_lds_dwordx4 v145, s[40:41] offset:1024\n\t"
      "global_load_lds_dwordx4 v146, s[40:41] offset:2048\n\t"
      "global_load_lds_dwordx4 v147, s[40:41] offset:3072\n\t"
      "s_waitcnt vmcnt(12)\n\t"
      "ds_read_b64_tr_b16 v[48:49], v172 offset:16384\n\t"
      "ds_read_b64_tr_b16 v[50:51], v172 offset:16896\n\t"
      "ds_read_b64_tr_b16 v[52:53], v173 offset:16384\n\t"
      "ds_read_b64_tr_b16 v[54:55], v173 offset:16896\n\t"
      "ds_read_b64_tr_b16 v[56:57], v174 offset:16384\n\t"
      "ds_read_b64_tr_b16 v[58:59], v174 offset:16896\n\t"
      "ds_read_b64_tr_b16 v[60:61], v175 offset:16384\n\t"
      "ds_read_b64_tr_b16 v[62:63], v175 offset:16896\n\t"
      "s_waitcnt lgkmcnt(0)\n\t"
      "s_add_u32 s72, s78, 229376\n\t"
      "s_add_u32 s40, s52, s72\n\t"
      "s_addc_u32 s41, s53, 0\n\t"
      "s_add_u32 m0, s39, 81920\n\t"
      "s_nop 0\n\t"
      "global_load_lds_dwordx4 v144, s[40:41]\n\t"
      "global_load_lds_dwordx4 v145, s[40:41] offset:1024\n\t"
      "global_load_lds_dwordx4 v146, s[40:41] offset:2048\n\t"
      "global_load_lds_dwordx4 v147, s[40:41] offset:3072\n\t"
      "s_waitcnt vmcnt(8)\n\t"
      "s_barrier\n\t"
      "s_cmp_lg_u32 s24, 3\n\t"
      "s_cbranch_scc1 SK13_%=\n\t"
      "global_load_dwordx2 v[138:139], v137, s[60:61] sc1\n\t"
      "SK13_%=:\n\t"
      "s_cmp_lg_u32 s24, 3\n\t"
      "s_cbranch_scc1 LF2B_%=\n\t"
      "s_waitcnt vmcnt(0)\n\t"
      "v_cmp_ne_u64_e32 vcc, s[22:23], v[138:139]\n\t"
      "s_cbranch_vccnz LF2R_%=\n\t"
      "LF2B_%=:\n\t"
      "s_barrier\n\t"
      "s_add_u32 s72, s78, 3670016\n\t"
      "s_add_u32 s40, s52, s72\n\t"
      "s_addc_u32 s41, s53, 0\n\t"
      "s_add_u32 m0, s39, 16384\n\t"
      "s_nop 0\n\t"
      "global_load_lds_dwordx4 v144, s[40:41]\n\t"
      "global_load_lds_dwordx4 v145, s[40:41] offset:1024\n\t"
      "global_load_lds_dwordx4 v146, s[40:41] offset:2048\n\t"
      "global_load_lds_dwordx4 v147, s[40:41] offset:3072\n\t"
      "s_add_u32 s72, s78, 3899392\n\t"
      "s_add_u32 s40, s52, s72\n\t"
      "s_addc_u32 s41, s53, 0\n\t"
      "s_add_u32 m0, s39, 49152\n\t"
      "s_nop 0\n\t"
      "global_load_lds_dwordx4 v144, s[40:41]\n\t"
      "global_load_lds_dwordx4 v145, s[40:41] offset:1024\n\t"
      "global_load_lds_dwordx4 v146, s[40:41] offset:2048\n\t"
      "global_load_lds_dwordx4 v147, s[40:41] offset:3072\n\t"
      "s_add_u32 s72, s79, 3670016\n\t"
      "s_add_u32 s40, s52, s72\n\t"
      "s_addc_u32 s41, s53, 0\n\t"
      "s_add_u32 m0, s39, 0\n\t"
      "s_nop 0\n\t"
      "global_load_lds_dwordx4 v144, s[40:41]\n\t"
      "global_load_lds_dwordx4 v145, s[40:41] offset:1024\n\t"
      "global_load_lds_dwordx4 v146, s[40:41] offset:2048\n\t"
      "global_load_lds_dwordx4 v147, s[40:41] offset:3072\n\t"
      "s_add_u32 s72, s79, 3899392\n\t"
      "s_add_u32 s40, s52, s72\n\t"
      "s_addc_u32 s41, s53, 0\n\t"
      "s_add_u32 m0, s39, 32768\n\t"
      "s_nop 0\n\t"
      "global_load_lds_dwordx4 v144, s[40:41]\n\t"
      "global_load_lds_dwordx4 v145, s[40:41] offset:1024\n\t"
      "global_load_lds_dwordx4 v146, s[40:41] offset:2048\n\t"
      "global_load_lds_dwordx4 v147, s[40:41] offset:3072\n\t"
      "s_waitcnt vmcnt(20)\n\t"
      "ds_read_b64_tr_b16 v[64:65], v172\n\t"
      "ds_read_b64_tr_b16 v[66:67], v172 offset:512\n\t"
      "ds_read_b64_tr_b16 v[68:69], v173\n\t"
      "ds_read_b64_tr_b16 v[70:71], v173 offset:512\n\t"
      "ds_read_b64_tr_b16 v[72:73], v174\n\t"
      "ds_read_b64_tr_b16 v[74:75], v174 offset:512\n\t"
      "ds_read_b64_tr_b16 v[76:77], v175\n\t"
      "ds_read_b64_tr_b16 v[78:79], v175 offset:512\n\t"
      "s_waitcnt vmcnt(16)\n\t"
      "ds_read_b64_tr_b16 v[80:81], v172 offset:16384\n\t"
      "ds_read_b64_tr_b16 v[82:83], v172 offset:16896\n\t"
      "ds_read_b64_tr_b16 v[84:85], v173 offset:16384\n\t"
      "ds_read_b64_tr_b16 v[86:87], v173 offset:16896\n\t"
      "ds_read_b64_tr_b16 v[88:89], v174 offset:16384\n\t"
      "ds_read_b64_tr_b16 v[90:91], v174 offset:16896\n\t"
      "ds_read_b64_tr_b16 v[92:93], v175 offset:16384\n\t"
      "ds_read_b64_tr_b16 v[94:95], v175 offset:16896\n\t"
      "s_cmp_lg_u32 s24, 3\n\t"
      "s_cbranch_scc1 SK14_%=\n\t"
      "global_load_dwordx2 v[138:139], v143, s[60:61] sc1\n\t"
      "SK14_%=:\n\t"
      "s_waitcnt vmcnt(0)\n\t"
      "s_cmp_lg_u32 s24, 3\n\t"
      "s_cbranch_scc1 LF3B_%=\n\t"
      "s_waitcnt vmcnt(0)\n\t"
      "v_cmp_ne_u64_e32 vcc, s[22:23], v[138:139]\n\t"
      "s_cbranch_vccnz LF3R_%=\n\t"
      "LF3B_%=:\n\t"
      "s_barrier\n\t"
      "s_waitcnt lgkmcnt(0)\n\t"
      "s_add_u32 s72, s79, 0\n\t"
      "s_add_u32 s40, s52, s72\n\t"
      "s_addc_u32 s41, s53, 0\n\t"
      "s_add_u32 m0, s39, 65536\n\t"
      "s_nop 0\n\t"
      "global_load_lds_dwordx4 v144, s[40:41]\n\t"
      "global_load_lds_dwordx4 v145, s[40:41] offset:1024\n\t"
      "global_load_lds_dwordx4 v146, s[40:41] offset:2048\n\t"
      "global_load_lds_dwordx4 v147, s[40:41] offset:3072\n\t"
      "s_add_u32 s72, s79, 229376\n\t"
      "s_add_u32 s40, s52, s72\n\t"
      "s_addc_u32 s41, s53, 0\n\t"
      "s_add_u32 m0, s39, 81920\n\t"
      "s_nop 0\n\t"
      "global_load_lds_dwordx4 v144, s[40:41]\n\t"
      "global_load_lds_dwordx4 v145, s[40:41] offset:1024\n\t"
      "global_load_lds_dwordx4 v146, s[40:41] offset:2048\n\t"
      "global_load_lds_dwordx4 v147, s[40:41] offset:3072\n\t"
      "s_mul_i32 s34, s30, 65536\n\t"
      "s_mul_i32 s35, s24, 8192\n\t"
      "s_add_u32 s34, s34, s35\n\t"
      "s_add_u32 s64, s48, s34\n\t"
      "s_addc_u32 s65, s49, 0\n\t"
      "v_add_u32_e32 v250, 4096, v244\n\t"
      "global_load_dwordx4 v[128:131], v250, s[64:65] offset:-4096 sc1\n\t"
      "global_load_dwordx4 v[132:135], v250, s[64:65] offset:-3072 sc1\n\t"
      "global_load_dwordx4 v[136:139], v250, s[64:65] offset:-2048 sc1\n\t"
      "global_load_dwordx4 v[140:143], v250, s[64:65] offset:-1024 sc1\n\t"
      "global_load_dwordx4 v[144:147], v250, s[64:65] sc1\n\t"
      "global_load_dwordx4 v[148:151], v250, s[64:65] offset:1024 sc1\n\t"
      "global_load_dwordx4 v[152:155], v250, s[64:65] offset:2048 sc1\n\t"
      "global_load_dwordx4 v[156:159], v250, s[64:65] offset:3072 sc1\n\t"
      "s_waitcnt vmcnt(12)\n\t"
      "ds_read_b64_tr_b16 v[96:97], v172\n\t"
      "ds_read_b64_tr_b16 v[98:99], v172 offset:512\n\t"
      "ds_read_b64_tr_b16 v[100:101], v173\n\t"
      "ds_read_b64_tr_b16 v[102:103], v173 offset:512\n\t"
      "ds_read_b64_tr_b16 v[104:105], v174\n\t"
      "ds_read_b64_tr_b16 v[106:107], v174 offset:512\n\t"
      "ds_read_b64_tr_b16 v[108:109], v175\n\t"
      "ds_read_b64_tr_b16 v[110:111], v175 offset:512\n\t"
      "s_waitcnt vmcnt(8)\n\t"
      "ds_read_b64_tr_b16 v[112:113], v172 offset:16384\n\t"
      "ds_read_b64_tr_b16 v[114:115], v172 offset:16896\n\t"
      "ds_read_b64_tr_b16 v[116:117], v173 offset:16384\n\t"
      "ds_read_b64_tr_b16 v[118:119], v173 offset:16896\n\t"
      "ds_read_b64_tr_b16 v[120:121], v174 offset:16384\n\t"
      "ds_read_b64_tr_b16 v[122:123], v174 offset:16896\n\t"
      "ds_read_b64_tr_b16 v[124:125], v175 offset:16384\n\t"
      "ds_read_b64_tr_b16 v[126:127], v175 offset:16896\n\t"
      "s_waitcnt vmcnt(0) lgkmcnt(0)\n\t"
      "s_branch GST_%=\n\t"
      "G4C_%=:\n\t"
      "global_store_dwordx4 v241, v[208:211], s[20:21] offset:-2048 sc1\n\t"
      "global_store_dwordx4 v241, v[212:215], s[20:21] offset:2048 sc1\n\t"
      "G4_%=:\n\t"
      "s_mul_i32 s34, s30, 57344\n\t"
      "s_mul_i32 s35, s24, 7168\n\t"
      "s_add_u32 s34, s34, s35\n\t"
      "s_add_u32 s34, s34, 1835008\n\t"
      "s_add_u32 s64, s48, s34\n\t"
      "s_addc_u32 s65, s49, 0\n\t"
      "s_and_b32 s34, s30, 3\n\t"
      "s_lshl_b32 s34, s34, 11\n\t"
      "s_add_u32 s40, s34, 0\n\t"
      "s_sub_u32 s35, s40, 7168\n\t"
      "s_min_u32 s40, s40, s35\n\t"
      "s_add_u32 s41, s34, 1024\n\t"
      "s_sub_u32 s35, s41, 7168\n\t"
      "s_min_u32 s41, s41, s35\n\t"
      "s_add_u32 s42, s34, 2048\n\t"
      "s_sub_u32 s35, s42, 7168\n\t"
      "s_min_u32 s42, s42, s35\n\t"
      "s_add_u32 s43, s34, 3072\n\t"
      "s_sub_u32 s35, s43, 7168\n\t"
      "s_min_u32 s43, s43, s35\n\t"
      "s_add_u32 s44, s34, 4096\n\t"
      "s_sub_u32 s35, s44, 7168\n\t"
      "s_min_u32 s44, s44, s35\n\t"
      "s_add_u32 s45, s34, 5120\n\t"
      "s_sub_u32 s35, s45, 7168\n\t"
      "s_min_u32 s45, s45, s35\n\t"
      "s_add_u32 s46, s34, 6144\n\t"
      "s_sub_u32 s35, s46, 7168\n\t"
      "s_min_u32 s46, s46, s35\n\t"
      "s_add_u32 s66, s64, 57344\n\t"
      "s_addc_u32 s67, s65, 0\n\t"
      "s_add_u32 s68, s66, 57344\n\t"
      "s_addc_u32 s69, s67, 0\n\t"
      "s_add_u32 s70, s68, 57344\n\t"
      "s_addc_u32 s71, s69, 0\n\t"
      "s_add_u32 s72, s70, 57344\n\t"
      "s_addc_u32 s73, s71, 0\n\t"
      "s_add_u32 s74, s72, 57344\n\t"
      "s_addc_u32 s75, s73, 0\n\t"
      "s_add_u32 s76, s74, 57344\n\t"
      "s_addc_u32 s77, s75, 0\n\t"
      "v_lshrrev_b32_e32 v240, 7, v244\n\t"
      "v_and_b32_e32 v241, 0x70, v244\n\t"
      "v_and_b32_e32 v242, 6, v240\n\t"
      "v_lshlrev_b32_e32 v242, 4, v242\n\t"
      "v_xor_b32_e32 v242, v242, v241\n\t"
      "v_lshl_or_b32 v228, v240, 12, v242\n\t"
      "s_mul_i32 s34, s24, 917504\n\t"
      "s_lshl_b32 s35, s29, 7\n\t"
      "s_add_u32 s34, s34, s35\n\t"
      "s_lshl_b32 s39, s24, 12\n\t"
      "s_add_u32 s54, s14, s34\n\t"
      "s_addc_u32 s55, s15, 0\n\t"
      "v_add_u32_e32 v229, 31744, v228\n\t"
      "v_add_u32_e32 v230, 63488, v228\n\t"
      "v_add_u32_e32 v231, 95232, v228\n\t"
      "s_lshl_b32 s37, s40, 7\n\t"
      "s_add_u32 s58, s54, s37\n\t"
      "s_addc_u32 s59, s55, 0\n\t"
      "s_add_u32 m0, s39, 0\n\t"
      "s_nop 0\n\t"
      "global_load_lds_dwordx4 v228, s[58:59]\n\t"
      "global_load_lds_dwordx4 v229, s[58:59] offset:1024\n\t"
      "global_load_lds_dwordx4 v230, s[58:59] offset:2048\n\t"
      "global_load_lds_dwordx4 v231, s[58:59] offset:3072\n\t"
      "s_lshl_b32 s37, s41, 7\n\t"
      "s_add_u32 s58, s54, s37\n\t"
      "s_addc_u32 s59, s55, 0\n\t"
      "s_add_u32 m0, s39, 32768\n\t"
      "s_nop 0\n\t"
      "global_load_lds_dwordx4 v228, s[58:59]\n\t"
      "global_load_lds_dwordx4 v229, s[58:59] offset:1024\n\t"
      "global_load_lds_dwordx4 v230, s[58:59] offset:2048\n\t"
      "global_load_lds_dwordx4 v231, s[58:59] offset:3072\n\t"
      "s_lshl_b32 s37, s42, 7\n\t"
      "s_add_u32 s58, s54, s37\n\t"
      "s_addc_u32 s59, s55, 0\n\t"
      "s_add_u32 m0, s39, 65536\n\t"
      "s_nop 0\n\t"
      "global_load_lds_dwordx4 v228, s[58:59]\n\t"
      "global_load_lds_dwordx4 v229, s[58:59] offset:1024\n\t"
      "global_load_lds_dwordx4 v230, s[58:59] offset:2048\n\t"
      "global_load_lds_dwordx4 v231, s[58:59] offset:3072\n\t"
      "s_lshl_b32 s37, s43, 7\n\t"
      "s_add_u32 s58, s54, s37\n\t"
      "s_addc_u32 s59, s55, 0\n\t"
      "s_add_u32 m0, s39, 98304\n\t"
      "s_nop 0\n\t"
      "global_load_lds_dwordx4 v228, s[58:59]\n\t"
      "global_load_lds_dwordx4 v229, s[58:59] offset:1024\n\t"
      "global_load_lds_dwordx4 v230, s[58:59] offset:2048\n\t"
      "global_load_lds_dwordx4 v231, s[58:59] offset:3072\n\t"
      "s_lshr_b32 s34, s24, 1\n\t"
      "s_lshl2_add_u32 s35, s29, s34\n\t"
      "s_and_b32 s36, s24, 1\n\t"
      "s_lshl4_add_u32 s35, s35, s36\n\t"
      "v_lshl_add_u32 v242, v240, 1, s35\n\t"
      "v_lshlrev_b32_e32 v248, 1, v242\n\t"
      "global_load_ushort v249, v248, s[16:17]\n\t"
      "s_movk_i32 s35, 896\n\t"
      "v_and_b32_e32 v247, 64, v244\n\t"
      "v_lshrrev_b32_e32 v247, 2, v247\n\t"
      "v_mad_u32_u24 v247, v242, s35, v247\n\t"
      "v_and_b32_e32 v242, 0x30, v244\n\t"
      "v_lshl_add_u32 v247, v242, 1, v247\n\t"
      "s_mov_b32 s82, 0x77777777\n\t"
      "s_mov_b32 s83, 0x77777777\n\t"
      "s_lshl_b32 s35, s30, 5\n\t"
      "s_add_u32 s62, s18, s35\n\t"
      "s_addc_u32 s63, s19, 0\n\t"
      "s_mul_i32 s35, s29, 28\n\t"
      "s_add_u32 s35, s35, s30\n\t"
      "s_lshl1_add_u32 s35, s35, s36\n\t"
      "s_lshl2_add_u32 s35, s35, s34\n\t"
      "s_lshl_b32 s35, s35, 8\n\t"
      "s_add_u32 s35, s35, 6295552\n\t"
      "s_add_u32 s50, s20, s35\n\t"
      "s_addc_u32 s51, s21, 0\n\t"
      "v_lshrrev_b32_e32 v248, 2, v244\n\t"
      "v_bfe_u32 v242, v244, 8, 1\n\t"
      "v_lshlrev_b32_e32 v242, 4, v242\n\t"
      "v_xor_b32_e32 v245, v244, v242\n\t"
      "v_bfe_u32 v242, v244, 9, 1\n\t"
      "v_lshlrev_b32_e32 v242, 7, v242\n\t"
      "v_xor_b32_e32 v245, v245, v242\n\t"
      "s_add_u32 s35, s39, 98304\n\t"
      "v_add_u32_e32 v245, s35, v245\n\t"
      "v_lshl_or_b32 v246, v240, 1, s36\n\t"
      "v_bfe_u32 v242, v244, 5, 1\n\t"
      "v_xor_b32_e32 v246, v246, v242\n\t"
      "v_bfe_u32 v242, v244, 6, 1\n\t"
      "v_lshlrev_b32_e32 v242, 3, v242\n\t"
      "v_xor_b32_e32 v246, v246, v242\n\t"
      "v_lshlrev_b32_e32 v246, 4, v246\n\t"
      "v_and_b32_e32 v242, 0x60, v244\n\t"
      "v_lshl_or_b32 v246, v242, 3, v246\n\t"
      "v_bfe_u32 v242, v244, 4, 1\n\t"
      "v_lshl_or_b32 v246, v242, 3, v246\n\t"
      "s_lshl_b32 s34, s34, 10\n\t"
      "s_add_u32 s34, s34, 98304\n\t"
      "v_add_u32_e32 v246, s34, v246\n\t"
      "v_and_b32_e32 v240, 0x3c0, v244\n\t"
      "v_lshlrev_b32_e32 v240, 1, v240\n\t"
      "v_and_b32_e32 v241, 0x180, v244\n\t"
      "v_lshrrev_b32_e32 v241, 2, v241\n\t"
      "v_and_b32_e32 v242, 0x30, v244\n\t"
      "v_lshrrev_b32_e32 v242, 1, v242\n\t"
      "v_or3_b32 v232, v240, v241, v242\n\t"
      "v_add_u32_e32 v232, s39, v232\n\t"
      "v_min_u32_e32 v252, 432, v244\n\t"
      "v_lshlrev_b32_e32 v252, 4, v252\n\t"
      "v_xor_b32_e32 v233, 32, v232\n\t"
      "v_xor_b32_e32 v234, 64, v232\n\t"
      "v_xor_b32_e32 v235, 96, v232\n\t"
      "v_xor_b32_e32 v236, 65536, v232\n\t"
      "v_xor_b32_e32 v237, 65568, v232\n\t"
      "v_xor_b32_e32 v238, 65600, v232\n\t"
      "v_xor_b32_e32 v239, 65632, v232\n\t"
      "s_waitcnt vmcnt(13)\n\t"
      "ds_read_b64_tr_b16 v[0:1], v232\n\t"
      "ds_read_b64_tr_b16 v[2:3], v232 offset:2048\n\t"
      "ds_read_b64_tr_b16 v[4:5], v233\n\t"
      "ds_read_b64_tr_b16 v[6:7], v233 offset:2048\n\t"
      "ds_read_b64_tr_b16 v[8:9], v234\n\t"
      "ds_read_b64_tr_b16 v[10:11], v234 offset:2048\n\t"
      "ds_read_b64_tr_b16 v[12:13], v235\n\t"
      "ds_read_b64_tr_b16 v[14:15], v235 offset:2048\n\t"
      "s_waitcnt lgkmcnt(0)\n\t"
      "s_lshl_b32 s37, s44, 7\n\t"
      "s_add_u32 s58, s54, s37\n\t"
      "s_addc_u32 s59, s55, 0\n\t"
      "s_add_u32 m0, s39, 0\n\t"
      "s_nop 0\n\t"
      "global_load_lds_dwordx4 v228, s[58:59]\n\t"
      "global_load_lds_dwordx4 v229, s[58:59] offset:1024\n\t"
      "global_load_lds_dwordx4 v230, s[58:59] offset:2048\n\t"
      "global_load_lds_dwordx4 v231, s[58:59] offset:3072\n\t"
      "s_waitcnt vmcnt(13)\n\t"
      "ds_read_b64_tr_b16 v[16:17], v232 offset:32768\n\t"
      "ds_read_b64_tr_b16 v[18:19], v232 offset:34816\n\t"
      "ds_read_b64_tr_b16 v[20:21], v233 offset:32768\n\t"
      "ds_read_b64_tr_b16 v[22:23], v233 offset:34816\n\t"
      "ds_read_b64_tr_b16 v[24:25], v234 offset:32768\n\t"
      "ds_read_b64_tr_b16 v[26:27], v234 offset:34816\n\t"
      "ds_read_b64_tr_b16 v[28:29], v235 offset:32768\n\t"
      "ds_read_b64_tr_b16 v[30:31], v235 offset:34816\n\t"
      "s_waitcnt lgkmcnt(0)\n\t"
      "s_lshl_b32 s37, s45, 7\n\t"
      "s_add_u32 s58, s54, s37\n\t"
      "s_addc_u32 s59, s55, 0\n\t"
      "s_add_u32 m0, s39, 32768\n\t"
      "s_nop 0\n\t"
      "global_load_lds_dwordx4 v228, s[58:59]\n\t"
      "global_load_lds_dwordx4 v229, s[58:59] offset:1024\n\t"
      "global_load_lds_dwordx4 v230, s[58:59] offset:2048\n\t"
      "global_load_lds_dwordx4 v231, s[58:59] offset:3072\n\t"
      "s_waitcnt vmcnt(13)\n\t"
      "ds_read_b64_tr_b16 v[32:33], v236\n\t"
      "ds_read_b64_tr_b16 v[34:35], v236 offset:2048\n\t"
      "ds_read_b64_tr_b16 v[36:37], v237\n\t"
      "ds_read_b64_tr_b16 v[38:39], v237 offset:2048\n\t"
      "ds_read_b64_tr_b16 v[40:41], v238\n\t"
      "ds_read_b64_tr_b16 v[42:43], v238 offset:2048\n\t"
      "ds_read_b64_tr_b16 v[44:45], v239\n\t"
      "ds_read_b64_tr_b16 v[46:47], v239 offset:2048\n\t"
      "s_waitcnt lgkmcnt(0)\n\t"
      "s_lshl_b32 s37, s46, 7\n\t"
      "s_add_u32 s58, s54, s37\n\t"
      "s_addc_u32 s59, s55, 0\n\t"
      "s_add_u32 m0, s39, 65536\n\t"
      "s_nop 0\n\t"
      "global_load_lds_dwordx4 v228, s[58:59]\n\t"
      "global_load_lds_dwordx4 v229, s[58:59] offset:1024\n\t"
      "global_load_lds_dwordx4 v230, s[58:59] offset:2048\n\t"
      "global_load_lds_dwordx4 v231, s[58:59] offset:3072\n\t"
      "s_waitcnt vmcnt(13)\n\t"
      "ds_read_b64_tr_b16 v[48:49], v236 offset:32768\n\t"
      "ds_read_b64_tr_b16 v[50:51], v236 offset:34816\n\t"
      "ds_read_b64_tr_b16 v[52:53], v237 offset:32768\n\t"
      "ds_read_b64_tr_b16 v[54:55], v237 offset:34816\n\t"
      "ds_read_b64_tr_b16 v[56:57], v238 offset:32768\n\t"
      "ds_read_b64_tr_b16 v[58:59], v238 offset:34816\n\t"
      "ds_read_b64_tr_b16 v[60:61], v239 offset:32768\n\t"
      "ds_read_b64_tr_b16 v[62:63], v239 offset:34816\n\t"
      "s_waitcnt vmcnt(8)\n\t"
      "ds_read_b64_tr_b16 v[64:65], v232\n\t"
      "ds_read_b64_tr_b16 v[66:67], v232 offset:2048\n\t"
      "ds_read_b64_tr_b16 v[68:69], v233\n\t"
      "ds_read_b64_tr_b16 v[70:71], v233 offset:2048\n\t"
      "ds_read_b64_tr_b16 v[72:73], v234\n\t"
      "ds_read_b64_tr_b16 v[74:75], v234 offset:2048\n\t"
      "ds_read_b64_tr_b16 v[76:77], v235\n\t"
      "ds_read_b64_tr_b16 v[78:79], v235 offset:2048\n\t"
      "s_waitcnt vmcnt(4)\n\t"
      "ds_read_b64_tr_b16 v[80:81], v232 offset:32768\n\t"
      "ds_read_b64_tr_b16 v[82:83], v232 offset:34816\n\t"
      "ds_read_b64_tr_b16 v[84:85], v233 offset:32768\n\t"
      "ds_read_b64_tr_b16 v[86:87], v233 offset:34816\n\t"
      "ds_read_b64_tr_b16 v[88:89], v234 offset:32768\n\t"
      "ds_read_b64_tr_b16 v[90:91], v234 offset:34816\n\t"
      "ds_read_b64_tr_b16 v[92:93], v235 offset:32768\n\t"
      "ds_read_b64_tr_b16 v[94:95], v235 offset:34816\n\t"
      "s_waitcnt vmcnt(0)\n\t"
      "ds_read_b64_tr_b16 v[96:97], v236\n\t"
      "ds_read_b64_tr_b16 v[98:99], v236 offset:2048\n\t"
      "ds_read_b64_tr_b16 v[100:101], v237\n\t"
      "ds_read_b64_tr_b16 v[102:103], v237 offset:2048\n\t"
      "ds_read_b64_tr_b16 v[104:105], v238\n\t"
      "ds_read_b64_tr_b16 v[106:107], v238 offset:2048\n\t"
      "ds_read_b64_tr_b16 v[108:109], v239\n\t"
      "ds_read_b64_tr_b16 v[110:111], v239 offset:2048\n\t"
      "v_lshlrev_b32_e32 v249, 16, v249\n\t"
      "s_waitcnt lgkmcnt(0)\n\t"
      "v_min_u32_e32 v240, 48, v244\n\t"
      "v_mul_u32_u24_e32 v240, 7, v240\n\t"
      "s_lshl_b32 s34, s29, 4\n\t"
      "v_add_u32_e32 v240, s34, v240\n\t"
      "v_subrev_u32_e32 v241, 448, v240\n\t"
      "v_min_u32_e32 v240, v240, v241\n\t"
      "v_subrev_u32_e32 v241, 448, v240\n\t"
      "v_min_u32_e32 v240, v240, v241\n\t"
      "s_and_b32 s34, s29, 7\n\t"
      "s_lshl_b32 s34, s34, 8\n\t"
      "v_lshl_add_u32 v253, v240, 7, s34\n\t"
      "v_add_u32_e32 v143, 32768, v143\n\t"
      "s_cmp_lg_u32 s24, 3\n\t"
      "s_cbranch_scc1 SK18_%=\n\t"
      "global_load_dwordx2 v[138:139], v143, s[60:61] sc1\n\t"
      "SK18_%=:\n\t"
      "s_cmp_lg_u32 s24, 3\n\t"
      "s_cbranch_scc1 LF5B_%=\n\t"
      "s_waitcnt vmcnt(0)\n\t"
      "v_cmp_ne_u64_e32 vcc, s[22:23], v[138:139]\n\t"
      "s_cbranch_vccnz LF5R_%=\n\t"
      "LF5B_%=:\n\t"
      "s_barrier\n\t"
      "s_cmp_lg_u32 s24, 3\n\t"
      "s_cbranch_scc1 HF4_%=\n\t"
      "s_lshl3_add_u32 s34, s25, s26\n\t"
      "s_lshl_b32 s34, s34, 7\n\t"
      "s_add_u32 s34, s34, 98304\n\t"
      "v_mov_b32_e32 v238, s34\n\t"
      "v_mov_b32_e32 v236, s22\n\t"
      "v_mov_b32_e32 v237, s23\n\t"
      "s_mov_b64 exec, 1\n\t"
      "global_store_dwordx2 v238, v[236:237], s[60:61] sc1\n\t"
      "s_mov_b64 exec, -1\n\t"
      "HF4_%=:\n\t"
      "s_and_b32 s34, s29, 30\n\t"
      "s_lshl_b32 s34, s34, 10\n\t"
      "s_add_u32 s34, s34, 98304\n\t"
      "s_lshr_b32 s35, s30, 2\n\t"
      "s_add_u32 s36, s30, 6\n\t"
      "s_lshr_b32 s36, s36, 2\n\t"
      "v_lshrrev_b32_e32 v143, 5, v244\n\t"
      "v_add_u32_e32 v143, s35, v143\n\t"
      "v_min_u32_e32 v143, s36, v143\n\t"
      "v_lshl_add_u32 v143, v143, 7, s34\n\t"
      "v_and_b32_e32 v142, 16, v244\n\t"
      "v_lshl_add_u32 v143, v142, 6, v143\n\t"
      "s_cmp_eq_u32 s24, 0\n\t"
      "s_cbranch_scc1 PG0R_%=\n\t"
      "PG0B_%=:\n\t"
      "s_barrier\n\t"
      "s_cmp_lg_u32 s24, 3\n\t"
      "s_cbranch_scc1 SK19_%=\n\t"
      "global_load_dwordx2 v[200:201], v143, s[60:61] sc1\n\t"
      "SK19_%=:\n\t"
      "v_mov_b32_e32 v202, v143\n\t"
      "s_add_u32 s58, s64, s40\n\t"
      "s_addc_u32 s59, s65, 0\n\t"
      "global_load_dwordx4 v[128:131], v244, s[58:59] sc1\n\t"
      "s_add_u32 s58, s64, s41\n\t"
      "s_addc_u32 s59, s65, 0\n\t"
      "global_load_dwordx4 v[132:135], v244, s[58:59] sc1\n\t"
      "s_add_u32 s58, s64, s42\n\t"
      "s_addc_u32 s59, s65, 0\n\t"
      "global_load_dwordx4 v[136:139], v244, s[58:59] sc1\n\t"
      "s_add_u32 s58, s64, s43\n\t"
      "s_addc_u32 s59, s65, 0\n\t"
      "global_load_dwordx4 v[140:143], v244, s[58:59] sc1\n\t"
      "s_add_u32 s58, s64, s44\n\t"
      "s_addc_u32 s59, s65, 0\n\t"
      "global_load_dwordx4 v[144:147], v244, s[58:59] sc1\n\t"
      "s_add_u32 s58, s64, s45\n\t"
      "s_addc_u32 s59, s65, 0\n\t"
      "global_load_dwordx4 v[148:151], v244, s[58:59] sc1\n\t"
      "s_add_u32 s58, s64, s46\n\t"
      "s_addc_u32 s59, s65, 0\n\t"
      "global_load_dwordx4 v[152:155], v244, s[58:59] sc1\n\t"
      "s_cmp_lg_u32 s24, 3\n\t"
      "s_cbranch_scc1 LF8B_%=\n\t"
      "s_waitcnt vmcnt(7)\n\t"
      "v_cmp_ne_u64_e32 vcc, s[22:23], v[200:201]\n\t"
      "s_cbranch_vccnz LF8R_%=\n\t"
      "LF8B_%=:\n\t"
      "s_cmp_eq_u32 s24, 0\n\t"
      "s_cbranch_scc1 PG1R_%=\n\t"
      "PG1B_%=:\n\t"
      "s_barrier\n\t"
      "global_load_dword v112, v248, s[50:51] offset:-4096 sc1\n\t"
      "global_load_dword v113, v248, s[50:51] offset:-2048 sc1\n\t"
      "global_load_dword v114, v248, s[50:51] sc1\n\t"
      "global_load_dword v115, v248, s[50:51] offset:2048 sc1\n\t"
      "s_add_u32 s50, s50, 8192\n\t"
      "s_addc_u32 s51, s51, 0\n\t"
      "global_load_dword v116, v248, s[50:51] offset:-4096 sc1\n\t"
      "global_load_dword v117, v248, s[50:51] offset:-2048 sc1\n\t"
      "global_load_dword v118, v248, s[50:51] sc1\n\t"
      "v_mov_b32_e32 v254, v249\n\t"
      "s_waitcnt vmcnt(13)\n\t"
      "v_mfma_f32_16x16x32_bf16 v[208:211], v[128:131], v[0:3], 0\n\t"
      "s_add_u32 s58, s66, s40\n\t"
      "s_addc_u32 s59, s67, 0\n\t"
      "global_load_dwordx4 v[156:159], v244, s[58:59] sc1\n\t"
      "s_waitcnt vmcnt(13)\n\t"
      "v_mfma_f32_16x16x32_bf16 v[208:211], v[132:135], v[16:19], v[208:211]\n\t"
      "s_waitcnt vmcnt(12)\n\t"
      "v_mfma_f32_16x16x32_bf16 v[208:211], v[136:139], v[32:35], v[208:211]\n\t"
      "s_add_u32 s58, s66, s41\n\t"
      "s_addc_u32 s59, s67, 0\n\t"
      "global_load_dwordx4 v[160:163], v244, s[58:59] sc1\n\t"
      "s_waitcnt vmcnt(12)\n\t"
      "v_mfma_f32_16x16x32_bf16 v[208:211], v[140:143], v[48:51], v[208:211]\n\t"
      "s_waitcnt vmcnt(11)\n\t"
      "v_mfma_f32_16x16x32_bf16 v[208:211], v[144:147], v[64:67], v[208:211]\n\t"
      "s_add_u32 s58, s66, s42\n\t"
      "s_addc_u32 s59, s67, 0\n\t"
      "global_load_dwordx4 v[164:167], v244, s[58:59] sc1\n\t"
      "s_waitcnt vmcnt(11)\n\t"
      "v_mfma_f32_16x16x32_bf16 v[208:211], v[148:151], v[80:83], v[208:211]\n\t"
      "s_waitcnt vmcnt(10)\n\t"
      "v_mfma_f32_16x16x32_bf16 v[208:211], v[152:155], v[96:99], v[208:211]\n\t"
      "s_add_u32 s58, s66, s43\n\t"
      "s_addc_u32 s59, s67, 0\n\t"
      "global_load_dwordx4 v[168:171], v244, s[58:59] sc1\n\t"
      "v_mfma_f32_16x16x32_bf16 v[212:215], v[128:131], v[4:7], 0\n\t"
      "v_mfma_f32_16x16x32_bf16 v[212:215], v[132:135], v[20:23], v[212:215]\n\t"
      "s_add_u32 s58, s66, s44\n\t"
      "s_addc_u32 s59, s67, 0\n\t"
      "global_load_dwordx4 v[172:175], v244, s[58:59] sc1\n\t"
      "v_mfma_f32_16x16x32_bf16 v[212:215], v[136:139], v[36:39], v[212:215]\n\t"
      "v_add_f32_e32 v251, v208, v209\n\t"
      "v_add_f32_e32 v251, v251, v210\n\t"
      "v_cmp_u_f32_e32 vcc, v251, v211\n\t"
      "s_cbranch_vccnz LA40R_%=\n\t"
      "LA40B_%=:\n\t"
      "v_mfma_f32_16x16x32_bf16 v[212:215], v[140:143], v[52:55], v[212:215]\n\t"
      "s_add_u32 s58, s66, s45\n\t"
      "s_addc_u32 s59, s67, 0\n\t"
      "global_load_dwordx4 v[176:179], v244, s[58:59] sc1\n\t"
      "v_mfma_f32_16x16x32_bf16 v[212:215], v[144:147], v[68:71], v[212:215]\n\t"
      "v_mfma_f32_16x16x32_bf16 v[212:215], v[148:151], v[84:87], v[212:215]\n\t"
      "ds_write_b128 v245, v[208:211]\n\t"
      "s_add_u32 s58, s66, s46\n\t"
      "s_addc_u32 s59, s67, 0\n\t"
      "global_load_dwordx4 v[180:183], v244, s[58:59] sc1\n\t"
      "v_mfma_f32_16x16x32_bf16 v[212:215], v[152:155], v[100:103], v[212:215]\n\t"
      "v_mfma_f32_16x16x32_bf16 v[216:219], v[128:131], v[8:11], 0\n\t"
      "v_mfma_f32_16x16x32_bf16 v[216:219], v[132:135], v[24:27], v[216:219]\n\t"
      "v_mfma_f32_16x16x32_bf16 v[216:219], v[136:139], v[40:43], v[216:219]\n\t"
      "v_mfma_f32_16x16x32_bf16 v[216:219], v[140:143], v[56:59], v[216:219]\n\t"
      "s_add_u32 s58, s68, s40\n\t"
      "s_addc_u32 s59, s69, 0\n\t"
      "global_load_dwordx4 v[184:187], v244, s[58:59] sc1\n\t"
      "v_mfma_f32_16x16x32_bf16 v[216:219], v[144:147], v[72:75], v[216:219]\n\t"
      "v_mfma_f32_16x16x32_bf16 v[216:219], v[148:151], v[88:91], v[216:219]\n\t"
      "ds_write_b128 v245, v[212:215] offset:1024\n\t"
      "v_mfma_f32_16x16x32_bf16 v[216:219], v[152:155], v[104:107], v[216:219]\n\t"
      "v_mfma_f32_16x16x32_bf16 v[220:223], v[128:131], v[12:15], 0\n\t"
      "s_add_u32 s58, s68, s41\n\t"
      "s_addc_u32 s59, s69, 0\n\t"
      "global_load_dwordx4 v[188:191], v244, s[58:59] sc1\n\t"
      "v_mfma_f32_16x16x32_bf16 v[220:223], v[132:135], v[28:31], v[220:223]\n\t"
      "v_mfma_f32_16x16x32_bf16 v[220:223], v[136:139], v[44:47], v[220:223]\n\t"
      "v_mfma_f32_16x16x32_bf16 v[220:223], v[140:143], v[60:63], v[220:223]\n\t"
      "v_mfma_f32_16x16x32_bf16 v[220:223], v[144:147], v[76:79], v[220:223]\n\t"
      "s_add_u32 s58, s68, s42\n\t"
      "s_addc_u32 s59, s69, 0\n\t"
      "global_load_dwordx4 v[192:195], v244, s[58:59] sc1\n\t"
      "v_mfma_f32_16x16x32_bf16 v[220:223], v[148:151], v[92:95], v[220:223]\n\t"
      "ds_write_b128 v245, v[216:219] offset:2048\n\t"
      "v_mfma_f32_16x16x32_bf16 v[220:223], v[152:155], v[108:111], v[220:223]\n\t"
      "s_waitcnt vmcnt(9)\n\t"
      "v_mfma_f32_16x16x32_bf16 v[224:227], v[156:159], v[0:3], 0\n\t"
      "s_waitcnt vmcnt(8)\n\t"
      "v_mfma_f32_16x16x32_bf16 v[224:227], v[160:163], v[16:19], v[224:227]\n\t"
      "s_add_u32 s58, s68, s43\n\t"
      "s_addc_u32 s59, s69, 0\n\t"
      "global_load_dwordx4 v[196:199], v244, s[58:59] sc1\n\t"
      "s_nop 0\n\t"
      "ds_write_b128 v245, v[220:223] offset:3072\n\t"
      "s_waitcnt vmcnt(8)\n\t"
      "v_mfma_f32_16x16x32_bf16 v[224:227], v[164:167], v[32:35], v[224:227]\n\t"
      "s_waitcnt vmcnt(7)\n\t"
      "v_mfma_f32_16x16x32_bf16 v[224:227], v[168:171], v[48:51], v[224:227]\n\t"
      "s_waitcnt vmcnt(6)\n\t"
      "v_mfma_f32_16x16x32_bf16 v[224:227], v[172:175], v[64:67], v[224:227]\n\t"
      "s_waitcnt lgkmcnt(0)\n\t"
      "s_barrier\n\t"
      "ds_read_b64 v[228:229], v246\n\t"
      "ds_read_b64 v[230:231], v246 offset:4096\n\t"
      "s_waitcnt vmcnt(5)\n\t"
      "v_mfma_f32_16x16x32_bf16 v[224:227], v[176:179], v[80:83], v[224:227]\n\t"
      "s_add_u32 s58, s68, s44\n\t"
      "s_addc_u32 s59, s69, 0\n\t"
      "global_load_dwordx4 v[200:203], v244, s[58:59] sc1\n\t"
      "ds_read_b64 v[232:233], v246 offset:8192\n\t"
      "ds_read_b64 v[234:235], v246 offset:12288\n\t"
      "s_waitcnt vmcnt(5)\n\t"
      "v_mfma_f32_16x16x32_bf16 v[224:227], v[180:183], v[96:99], v[224:227]\n\t"
      "ds_read_b64 v[236:237], v246 offset:16384\n\t"
      "ds_read_b64 v[238:239], v246 offset:20480\n\t"
      "v_mfma_f32_16x16x32_bf16 v[208:211], v[156:159], v[4:7], 0\n\t"
      "ds_read_b64 v[240:241], v246 offset:24576\n\t"
      "ds_read_b64 v[242:243], v246 offset:28672\n\t"
      "v_lshlrev_b32_e32 v248, 16, v112\n\t"
      "v_mfma_f32_16x16x32_bf16 v[208:211], v[160:163], v[20:23], v[208:211]\n\t"
      "v_and_b32_e32 v249, 0xffff0000, v112\n\t"
      "v_mfma_f32_16x16x32_bf16 v[208:211], v[164:167], v[36:39], v[208:211]\n\t"
      "s_add_u32 s58, s68, s45\n\t"
      "s_addc_u32 s59, s69, 0\n\t"
      "global_load_dwordx4 v[204:207], v244, s[58:59] sc1\n\t"
      "v_add_f32_e32 v248, v254, v248\n\t"
      "v_add_f32_e32 v251, v224, v225\n\t"
      "v_add_f32_e32 v251, v251, v226\n\t"
      "v_cmp_u_f32_e32 vcc, v251, v227\n\t"
      "s_cbranch_vccnz LA41R_%=\n\t"
      "LA41B_%=:\n\t"
      "v_mfma_f32_16x16x32_bf16 v[208:211], v[168:171], v[52:55], v[208:211]\n\t"
      "v_add_f32_e32 v249, v254, v249\n\t"
      "s_waitcnt lgkmcnt(4)\n\t"
      "v_mfma_f32_16x16x32_bf16 v[208:211], v[172:175], v[68:71], v[208:211]\n\t"
      "v_add_f32_e32 v228, v228, v230\n\t"
      "v_mfma_f32_16x16x32_bf16 v[208:211], v[176:179], v[84:87], v[208:211]\n\t"
      "v_add_f32_e32 v232, v232, v234\n\t"
      "ds_write_b128 v245, v[224:227] offset:32768\n\t"
      "v_mfma_f32_16x16x32_bf16 v[208:211], v[180:183], v[100:103], v[208:211]\n\t"
      "s_add_u32 s58, s68, s46\n\t"
      "s_addc_u32 s59, s69, 0\n\t"
      "global_load_dwordx4 v[128:131], v244, s[58:59] sc1\n\t"
      "v_add_f32_e32 v229, v229, v231\n\t"
      "v_add_f32_e32 v233, v233, v235\n\t"
      "v_mfma_f32_16x16x32_bf16 v[212:215], v[156:159], v[8:11], 0\n\t"
      "s_waitcnt lgkmcnt(1)\n\t"
      "v_mfma_f32_16x16x32_bf16 v[212:215], v[160:163], v[24:27], v[212:215]\n\t"
      "v_add_f32_e32 v236, v236, v238\n\t"
      "v_mfma_f32_16x16x32_bf16 v[212:215], v[164:167], v[40:43], v[212:215]\n\t"
      "v_add_f32_e32 v240, v240, v242\n\t"
      "v_mfma_f32_16x16x32_bf16 v[212:215], v[168:171], v[56:59], v[212:215]\n\t"
      "v_add_f32_e32 v237, v237, v239\n\t"
      "v_add_f32_e32 v241, v241, v243\n\t"
      "s_add_u32 s58, s70, s40\n\t"
      "s_addc_u32 s59, s71, 0\n\t"
      "global_load_dwordx4 v[132:135], v244, s[58:59] sc1\n\t"
      "v_mfma_f32_16x16x32_bf16 v[212:215], v[172:175], v[72:75], v[212:215]\n\t"
      "v_add_f32_e32 v228, v228, v232\n\t"
      "v_mfma_f32_16x16x32_bf16 v[212:215], v[176:179], v[88:91], v[212:215]\n\t"
      "v_add_f32_e32 v236, v236, v240\n\t"
      "ds_write_b128 v245, v[208:211] offset:33792\n\t"
      "v_mfma_f32_16x16x32_bf16 v[212:215], v[180:183], v[104:107], v[212:215]\n\t"
      "v_add_f32_e32 v229, v229, v233\n\t"
      "v_add_f32_e32 v237, v237, v241\n\t"
      "v_mfma_f32_16x16x32_bf16 v[216:219], v[156:159], v[12:15], 0\n\t"
      "v_add_f32_e32 v228, v228, v248\n\t"
      "s_add_u32 s58, s70, s41\n\t"
      "s_addc_u32 s59, s71, 0\n\t"
      "global_load_dwordx4 v[136:139], v244, s[58:59] sc1\n\t"
      "v_mfma_f32_16x16x32_bf16 v[216:219], v[160:163], v[28:31], v[216:219]\n\t"
      "v_add_f32_e32 v229, v229, v249\n\t"
      "v_mfma_f32_16x16x32_bf16 v[216:219], v[164:167], v[44:47], v[216:219]\n\t"
      "v_add_f32_e32 v228, v228, v236\n\t"
      "v_add_f32_e32 v229, v229, v237\n\t"
      "v_mfma_f32_16x16x32_bf16 v[216:219], v[168:171], v[60:63], v[216:219]\n\t"
      "v_cvt_pk_bf16_f32 v232, v228, v229\n\t"
      "v_mfma_f32_16x16x32_bf16 v[216:219], v[172:175], v[76:79], v[216:219]\n\t"
      "s_mov_b32 vcc_lo, 0xeeeeeeee\n\t"
      "s_mov_b32 vcc_hi, 0xeeeeeeee\n\t"
      "v_cndmask_b32_dpp v120, v232, v120, vcc quad_perm:[0,0,0,0] row_mask:0xf bank_mask:0xf\n\t"
      "v_cndmask_b32_dpp v121, v232, v121, vcc quad_perm:[1,1,1,1] row_mask:0xf bank_mask:0xf\n\t"
      "v_cndmask_b32_dpp v122, v232, v122, vcc quad_perm:[2,2,2,2] row_mask:0xf bank_mask:0xf\n\t"
      "v_cndmask_b32_dpp v123, v232, v123, vcc quad_perm:[3,3,3,3] row_mask:0xf bank_mask:0xf\n\t"
      "s_add_u32 s58, s70, s42\n\t"
      "s_addc_u32 s59, s71, 0\n\t"
      "global_load_dwordx4 v[140:143], v244, s[58:59] sc1\n\t"
      "v_mfma_f32_16x16x32_bf16 v[216:219], v[176:179], v[92:95], v[216:219]\n\t"
      "ds_write_b128 v245, v[212:215] offset:34816\n\t"
      "v_mfma_f32_16x16x32_bf16 v[216:219], v[180:183], v[108:111], v[216:219]\n\t"
      "s_waitcnt vmcnt(9)\n\t"
      "v_mfma_f32_16x16x32_bf16 v[220:223], v[184:187], v[0:3], 0\n\t"
      "s_waitcnt vmcnt(8)\n\t"
      "v_mfma_f32_16x16x32_bf16 v[220:223], v[188:191], v[16:19], v[220:223]\n\t"
      "s_add_u32 s58, s70, s43\n\t"
      "s_addc_u32 s59, s71, 0\n\t"
      "global_load_dwordx4 v[144:147], v244, s[58:59] sc1\n\t"
      "s_nop 0\n\t"
      "ds_write_b128 v245, v[216:219] offset:35840\n\t"
      "s_waitcnt vmcnt(8)\n\t"
      "v_mfma_f32_16x16x32_bf16 v[220:223], v[192:195], v[32:35], v[220:223]\n\t"
      "s_waitcnt vmcnt(7)\n\t"
      "v_mfma_f32_16x16x32_bf16 v[220:223], v[196:199], v[48:51], v[220:223]\n\t"
      "s_waitcnt vmcnt(6)\n\t"
      "v_mfma_f32_16x16x32_bf16 v[220:223], v[200:203], v[64:67], v[220:223]\n\t"
      "s_waitcnt lgkmcnt(0)\n\t"
      "s_barrier\n\t"
      "ds_read_b64 v[228:229], v246 offset:32768\n\t"
      "ds_read_b64 v[230:231], v246 offset:36864\n\t"
      "s_waitcnt vmcnt(5)\n\t"
      "v_mfma_f32_16x16x32_bf16 v[220:223], v[204:207], v[80:83], v[220:223]\n\t"
      "s_add_u32 s58, s70, s44\n\t"
      "s_addc_u32 s59, s71, 0\n\t"
      "global_load_dwordx4 v[148:151], v244, s[58:59] sc1\n\t"
      "ds_read_b64 v[232:233], v246 offset:40960\n\t"
      "ds_read_b64 v[234:235], v246 offset:45056\n\t"
      "s_waitcnt vmcnt(5)\n\t"
      "v_mfma_f32_16x16x32_bf16 v[220:223], v[128:131], v[96:99], v[220:223]\n\t"
      "ds_read_b64 v[236:237], v246 offset:49152\n\t"
      "ds_read_b64 v[238:239], v246 offset:53248\n\t"
      "v_mfma_f32_16x16x32_bf16 v[224:227], v[184:187], v[4:7], 0\n\t"
      "ds_read_b64 v[240:241], v246 offset:57344\n\t"
      "ds_read_b64 v[242:243], v246 offset:61440\n\t"
      "v_lshlrev_b32_e32 v248, 16, v113\n\t"
      "v_mfma_f32_16x16x32_bf16 v[224:227], v[188:191], v[20:23], v[224:227]\n\t"
      "v_and_b32_e32 v249, 0xffff0000, v113\n\t"
      "v_mfma_f32_16x16x32_bf16 v[224:227], v[192:195], v[36:39], v[224:227]\n\t"
      "s_add_u32 s58, s70, s45\n\t"
      "s_addc_u32 s59, s71, 0\n\t"
      "global_load_dwordx4 v[152:155], v244, s[58:59] sc1\n\t"
      "v_add_f32_e32 v248, v254, v248\n\t"
      "v_add_f32_e32 v251, v220, v221\n\t"
      "v_add_f32_e32 v251, v251, v222\n\t"
      "v_cmp_u_f32_e32 vcc, v251, v223\n\t"
      "s_cbranch_vccnz LA42R_%=\n\t"
      "LA42B_%=:\n\t"
      "v_mfma_f32_16x16x32_bf16 v[224:227], v[196:199], v[52:55], v[224:227]\n\t"
      "v_add_f32_e32 v249, v254, v249\n\t"
      "s_waitcnt lgkmcnt(4)\n\t"
      "v_mfma_f32_16x16x32_bf16 v[224:227], v[200:203], v[68:71], v[224:227]\n\t"
      "v_add_f32_e32 v228, v228, v230\n\t"
      "v_mfma_f32_16x16x32_bf16 v[224:227], v[204:207], v[84:87], v[224:227]\n\t"
      "v_add_f32_e32 v232, v232, v234\n\t"
      "ds_write_b128 v245, v[220:223]\n\t"
      "v_mfma_f32_16x16x32_bf16 v[224:227], v[128:131], v[100:103], v[224:227]\n\t"
      "s_add_u32 s58, s70, s46\n\t"
      "s_addc_u32 s59, s71, 0\n\t"
      "global_load_dwordx4 v[156:159], v244, s[58:59] sc1\n\t"
      "v_add_f32_e32 v229, v229, v231\n\t"
      "v_add_f32_e32 v233, v233, v235\n\t"
      "v_mfma_f32_16x16x32_bf16 v[208:211], v[184:187], v[8:11], 0\n\t"
      "s_waitcnt lgkmcnt(1)\n\t"
      "v_mfma_f32_16x16x32_bf16 v[208:211], v[188:191], v[24:27], v[208:211]\n\t"
      "v_add_f32_e32 v236, v236, v238\n\t"
      "v_mfma_f32_16x16x32_bf16 v[208:211], v[192:195], v[40:43], v[208:211]\n\t"
      "v_add_f32_e32 v240, v240, v242\n\t"
      "v_mfma_f32_16x16x32_bf16 v[208:211], v[196:199], v[56:59], v[208:211]\n\t"
      "v_add_f32_e32 v237, v237, v239\n\t"
      "v_add_f32_e32 v241, v241, v243\n\t"
      "s_add_u32 s58, s72, s40\n\t"
      "s_addc_u32 s59, s73, 0\n\t"
      "global_load_dwordx4 v[160:163], v244, s[58:59] sc1\n\t"
      "v_mfma_f32_16x16x32_bf16 v[208:211], v[200:203], v[72:75], v[208:211]\n\t"
      "v_add_f32_e32 v228, v228, v232\n\t"
      "v_mfma_f32_16x16x32_bf16 v[208:211], v[204:207], v[88:91], v[208:211]\n\t"
      "v_add_f32_e32 v236, v236, v240\n\t"
      "ds_write_b128 v245, v[224:227] offset:1024\n\t"
      "v_mfma_f32_16x16x32_bf16 v[208:211], v[128:131], v[104:107], v[208:211]\n\t"
      "v_add_f32_e32 v229, v229, v233\n\t"
      "v_add_f32_e32 v237, v237, v241\n\t"
      "v_mfma_f32_16x16x32_bf16 v[212:215], v[184:187], v[12:15], 0\n\t"
      "v_add_f32_e32 v228, v228, v248\n\t"
      "s_add_u32 s58, s72, s41\n\t"
      "s_addc_u32 s59, s73, 0\n\t"
      "global_load_dwordx4 v[164:167], v244, s[58:59] sc1\n\t"
      "v_mfma_f32_16x16x32_bf16 v[212:215], v[188:191], v[28:31], v[212:215]\n\t"
      "v_add_f32_e32 v229, v229, v249\n\t"
      "v_mfma_f32_16x16x32_bf16 v[212:215], v[192:195], v[44:47], v[212:215]\n\t"
      "v_add_f32_e32 v228, v228, v236\n\t"
      "v_add_f32_e32 v229, v229, v237\n\t"
      "v_mfma_f32_16x16x32_bf16 v[212:215], v[196:199], v[60:63], v[212:215]\n\t"
      "v_cvt_pk_bf16_f32 v232, v228, v229\n\t"
      "v_mfma_f32_16x16x32_bf16 v[212:215], v[200:203], v[76:79], v[212:215]\n\t"
      "s_mov_b32 vcc_lo, 0xdddddddd\n\t"
      "s_mov_b32 vcc_hi, 0xdddddddd\n\t"
      "v_cndmask_b32_dpp v120, v232, v120, vcc quad_perm:[0,0,0,0] row_mask:0xf bank_mask:0xf\n\t"
      "v_cndmask_b32_dpp v121, v232, v121, vcc quad_perm:[1,1,1,1] row_mask:0xf bank_mask:0xf\n\t"
      "v_cndmask_b32_dpp v122, v232, v122, vcc quad_perm:[2,2,2,2] row_mask:0xf bank_mask:0xf\n\t"
      "v_cndmask_b32_dpp v123, v232, v123, vcc quad_perm:[3,3,3,3] row_mask:0xf bank_mask:0xf\n\t"
      "s_add_u32 s58, s72, s42\n\t"
      "s_addc_u32 s59, s73, 0\n\t"
      "global_load_dwordx4 v[168:171], v244, s[58:59] sc1\n\t"
      "v_mfma_f32_16x16x32_bf16 v[212:215], v[204:207], v[92:95], v[212:215]\n\t"
      "ds_write_b128 v245, v[208:211] offset:2048\n\t"
      "v_mfma_f32_16x16x32_bf16 v[212:215], v[128:131], v[108:111], v[212:215]\n\t"
      "s_waitcnt vmcnt(9)\n\t"
      "v_mfma_f32_16x16x32_bf16 v[216:219], v[132:135], v[0:3], 0\n\t"
      "s_waitcnt vmcnt(8)\n\t"
      "v_mfma_f32_16x16x32_bf16 v[216:219], v[136:139], v[16:19], v[216:219]\n\t"
      "s_add_u32 s58, s72, s43\n\t"
      "s_addc_u32 s59, s73, 0\n\t"
      "global_load_dwordx4 v[172:175], v244, s[58:59] sc1\n\t"
      "s_nop 0\n\t"
      "ds_write_b128 v245, v[212:215] offset:3072\n\t"
      "s_waitcnt vmcnt(8)\n\t"
      "v_mfma_f32_16x16x32_bf16 v[216:219], v[140:143], v[32:35], v[216:219]\n\t"
      "s_waitcnt vmcnt(7)\n\t"
      "v_mfma_f32_16x16x32_bf16 v[216:219], v[144:147], v[48:51], v[216:219]\n\t"
      "s_waitcnt vmcnt(6)\n\t"
      "v_mfma_f32_16x16x32_bf16 v[216:219], v[148:151], v[64:67], v[216:219]\n\t"
      "s_waitcnt lgkmcnt(0)\n\t"
      "s_barrier\n\t"
      "ds_read_b64 v[228:229], v246\n\t"
      "ds_read_b64 v[230:231], v246 offset:4096\n\t"
      "s_waitcnt vmcnt(5)\n\t"
      "v_mfma_f32_16x16x32_bf16 v[216:219], v[152:155], v[80:83], v[216:219]\n\t"
      "s_add_u32 s58, s72, s44\n\t"
      "s_addc_u32 s59, s73, 0\n\t"
      "global_load_dwordx4 v[176:179], v244, s[58:59] sc1\n\t"
      "ds_read_b64 v[232:233], v246 offset:8192\n\t"
      "ds_read_b64 v[234:235], v246 offset:12288\n\t"
      "s_waitcnt vmcnt(5)\n\t"
      "v_mfma_f32_16x16x32_bf16 v[216:219], v[156:159], v[96:99], v[216:219]\n\t"
      "ds_read_b64 v[236:237], v246 offset:16384\n\t"
      "ds_read_b64 v[238:239], v246 offset:20480\n\t"
      "v_mfma_f32_16x16x32_bf16 v[220:223], v[132:135], v[4:7], 0\n\t"
      "ds_read_b64 v[240:241], v246 offset:24576\n\t"
      "ds_read_b64 v[242:243], v246 offset:28672\n\t"
      "v_lshlrev_b32_e32 v248, 16, v114\n\t"
      "v_mfma_f32_16x16x32_bf16 v[220:223], v[136:139], v[20:23], v[220:223]\n\t"
      "v_and_b32_e32 v249, 0xffff0000, v114\n\t"
      "v_mfma_f32_16x16x32_bf16 v[220:223], v[140:143], v[36:39], v[220:223]\n\t"
      "s_add_u32 s58, s72, s45\n\t"
      "s_addc_u32 s59, s73, 0\n\t"
      "global_load_dwordx4 v[180:183], v244, s[58:59] sc1\n\t"
      "v_add_f32_e32 v248, v254, v248\n\t"
      "v_add_f32_e32 v251, v216, v217\n\t"
      "v_add_f32_e32 v251, v251, v218\n\t"
      "v_cmp_u_f32_e32 vcc, v251, v219\n\t"
      "s_cbranch_vccnz LA43R_%=\n\t"
      "LA43B_%=:\n\t"
      "v_mfma_f32_16x16x32_bf16 v[220:223], v[144:147], v[52:55], v[220:223]\n\t"
      "v_add_f32_e32 v249, v254, v249\n\t"
      "s_waitcnt lgkmcnt(4)\n\t"
      "v_mfma_f32_16x16x32_bf16 v[220:223], v[148:151], v[68:71], v[220:223]\n\t"
      "v_add_f32_e32 v228, v228, v230\n\t"
      "v_mfma_f32_16x16x32_bf16 v[220:223], v[152:155], v[84:87], v[220:223]\n\t"
      "v_add_f32_e32 v232, v232, v234\n\t"
      "ds_write_b128 v245, v[216:219] offset:32768\n\t"
      "v_mfma_f32_16x16x32_bf16 v[220:223], v[156:159], v[100:103], v[220:223]\n\t"
      "s_add_u32 s58, s72, s46\n\t"
      "s_addc_u32 s59, s73, 0\n\t"
      "global_load_dwordx4 v[184:187], v244, s[58:59] sc1\n\t"
      "v_add_f32_e32 v229, v229, v231\n\t"
      "v_add_f32_e32 v233, v233, v235\n\t"
      "v_mfma_f32_16x16x32_bf16 v[224:227], v[132:135], v[8:11], 0\n\t"
      "s_waitcnt lgkmcnt(1)\n\t"
      "v_mfma_f32_16x16x32_bf16 v[224:227], v[136:139], v[24:27], v[224:227]\n\t"
      "v_add_f32_e32 v236, v236, v238\n\t"
      "v_mfma_f32_16x16x32_bf16 v[224:227], v[140:143], v[40:43], v[224:227]\n\t"
      "v_add_f32_e32 v240, v240, v242\n\t"
      "v_mfma_f32_16x16x32_bf16 v[224:227], v[144:147], v[56:59], v[224:227]\n\t"
      "v_add_f32_e32 v237, v237, v239\n\t"
      "v_add_f32_e32 v241, v241, v243\n\t"
      "s_add_u32 s58, s74, s40\n\t"
      "s_addc_u32 s59, s75, 0\n\t"
      "global_load_dwordx4 v[188:191], v244, s[58:59] sc1\n\t"
      "v_mfma_f32_16x16x32_bf16 v[224:227], v[148:151], v[72:75], v[224:227]\n\t"
      "v_add_f32_e32 v228, v228, v232\n\t"
      "v_mfma_f32_16x16x32_bf16 v[224:227], v[152:155], v[88:91], v[224:227]\n\t"
      "v_add_f32_e32 v236, v236, v240\n\t"
      "ds_write_b128 v245, v[220:223] offset:33792\n\t"
      "v_mfma_f32_16x16x32_bf16 v[224:227], v[156:159], v[104:107], v[224:227]\n\t"
      "v_add_f32_e32 v229, v229, v233\n\t"
      "v_add_f32_e32 v237, v237, v241\n\t"
      "v_mfma_f32_16x16x32_bf16 v[208:211], v[132:135], v[12:15], 0\n\t"
      "v_add_f32_e32 v228, v228, v248\n\t"
      "s_add_u32 s58, s74, s41\n\t"
      "s_addc_u32 s59, s75, 0\n\t"
      "global_load_dwordx4 v[192:195], v244, s[58:59] sc1\n\t"
      "v_mfma_f32_16x16x32_bf16 v[208:211], v[136:139], v[28:31], v[208:211]\n\t"
      "v_add_f32_e32 v229, v229, v249\n\t"
      "v_mfma_f32_16x16x32_bf16 v[208:211], v[140:143], v[44:47], v[208:211]\n\t"
      "v_add_f32_e32 v228, v228, v236\n\t"
      "v_add_f32_e32 v229, v229, v237\n\t"
      "v_mfma_f32_16x16x32_bf16 v[208:211], v[144:147], v[60:63], v[208:211]\n\t"
      "v_cvt_pk_bf16_f32 v232, v228, v229\n\t"
      "v_mfma_f32_16x16x32_bf16 v[208:211], v[148:151], v[76:79], v[208:211]\n\t"
      "s_mov_b32 vcc_lo, 0xbbbbbbbb\n\t"
      "s_mov_b32 vcc_hi, 0xbbbbbbbb\n\t"
      "v_cndmask_b32_dpp v120, v232, v120, vcc quad_perm:[0,0,0,0] row_mask:0xf bank_mask:0xf\n\t"
      "v_cndmask_b32_dpp v121, v232, v121, vcc quad_perm:[1,1,1,1] row_mask:0xf bank_mask:0xf\n\t"
      "v_cndmask_b32_dpp v122, v232, v122, vcc quad_perm:[2,2,2,2] row_mask:0xf bank_mask:0xf\n\t"
      "v_cndmask_b32_dpp v123, v232, v123, vcc quad_perm:[3,3,3,3] row_mask:0xf bank_mask:0xf\n\t"
      "s_add_u32 s58, s74, s42\n\t"
      "s_addc_u32 s59, s75, 0\n\t"
      "global_load_dwordx4 v[196:199], v244, s[58:59] sc1\n\t"
      "v_mfma_f32_16x16x32_bf16 v[208:211], v[152:155], v[92:95], v[208:211]\n\t"
      "ds_write_b128 v245, v[224:227] offset:34816\n\t"
      "v_mfma_f32_16x16x32_bf16 v[208:211], v[156:159], v[108:111], v[208:211]\n\t"
      "s_waitcnt vmcnt(9)\n\t"
      "v_mfma_f32_16x16x32_bf16 v[212:215], v[160:163], v[0:3], 0\n\t"
      "s_waitcnt vmcnt(8)\n\t"
      "v_mfma_f32_16x16x32_bf16 v[212:215], v[164:167], v[16:19], v[212:215]\n\t"
      "s_add_u32 s58, s74, s43\n\t"
      "s_addc_u32 s59, s75, 0\n\t"
      "global_load_dwordx4 v[200:203], v244, s[58:59] sc1\n\t"
      "s_nop 0\n\t"
      "ds_write_b128 v245, v[208:211] offset:35840\n\t"
      "s_waitcnt vmcnt(8)\n\t"
      "v_mfma_f32_16x16x32_bf16 v[212:215], v[168:171], v[32:35], v[212:215]\n\t"
      "s_waitcnt vmcnt(7)\n\t"
      "v_mfma_f32_16x16x32_bf16 v[212:215], v[172:175], v[48:51], v[212:215]\n\t"
      "s_waitcnt vmcnt(6)\n\t"
      "v_mfma_f32_16x16x32_bf16 v[212:215], v[176:179], v[64:67], v[212:215]\n\t"
      "s_waitcnt lgkmcnt(0)\n\t"
      "s_barrier\n\t"
      "ds_read_b64 v[228:229], v246 offset:32768\n\t"
      "ds_read_b64 v[230:231], v246 offset:36864\n\t"
      "s_waitcnt vmcnt(5)\n\t"
      "v_mfma_f32_16x16x32_bf16 v[212:215], v[180:183], v[80:83], v[212:215]\n\t"
      "s_add_u32 s58, s74, s44\n\t"
      "s_addc_u32 s59, s75, 0\n\t"
      "global_load_dwordx4 v[204:207], v244, s[58:59] sc1\n\t"
      "ds_read_b64 v[232:233], v246 offset:40960\n\t"
      "ds_read_b64 v[234:235], v246 offset:45056\n\t"
      "s_waitcnt vmcnt(5)\n\t"
      "v_mfma_f32_16x16x32_bf16 v[212:215], v[184:187], v[96:99], v[212:215]\n\t"
      "ds_read_b64 v[236:237], v246 offset:49152\n\t"
      "ds_read_b64 v[238:239], v246 offset:53248\n\t"
      "v_mfma_f32_16x16x32_bf16 v[216:219], v[160:163], v[4:7], 0\n\t"
      "ds_read_b64 v[240:241], v246 offset:57344\n\t"
      "ds_read_b64 v[242:243], v246 offset:61440\n\t"
      "v_lshlrev_b32_e32 v248, 16, v115\n\t"
      "v_mfma_f32_16x16x32_bf16 v[216:219], v[164:167], v[20:23], v[216:219]\n\t"
      "v_and_b32_e32 v249, 0xffff0000, v115\n\t"
      "v_mfma_f32_16x16x32_bf16 v[216:219], v[168:171], v[36:39], v[216:219]\n\t"
      "s_add_u32 s58, s74, s45\n\t"
      "s_addc_u32 s59, s75, 0\n\t"
      "global_load_dwordx4 v[128:131], v244, s[58:59] sc1\n\t"
      "v_add_f32_e32 v248, v254, v248\n\t"
      "v_add_f32_e32 v251, v212, v213\n\t"
      "v_add_f32_e32 v251, v251, v214\n\t"
      "v_cmp_u_f32_e32 vcc, v251, v215\n\t"
      "s_cbranch_vccnz LA44R_%=\n\t"
      "LA44B_%=:\n\t"
      "v_mfma_f32_16x16x32_bf16 v[216:219], v[172:175], v[52:55], v[216:219]\n\t"
      "v_add_f32_e32 v249, v254, v249\n\t"
      "s_waitcnt lgkmcnt(4)\n\t"
      "v_mfma_f32_16x16x32_bf16 v[216:219], v[176:179], v[68:71], v[216:219]\n\t"
      "v_add_f32_e32 v228, v228, v230\n\t"
      "v_mfma_f32_16x16x32_bf16 v[216:219], v[180:183], v[84:87], v[216:219]\n\t"
      "v_add_f32_e32 v232, v232, v234\n\t"
      "ds_write_b128 v245, v[212:215]\n\t"
      "v_mfma_f32_16x16x32_bf16 v[216:219], v[184:187], v[100:103], v[216:219]\n\t"
      "s_add_u32 s58, s74, s46\n\t"
      "s_addc_u32 s59, s75, 0\n\t"
      "global_load_dwordx4 v[132:135], v244, s[58:59] sc1\n\t"
      "v_add_f32_e32 v229, v229, v231\n\t"
      "v_add_f32_e32 v233, v233, v235\n\t"
      "v_mfma_f32_16x16x32_bf16 v[220:223], v[160:163], v[8:11], 0\n\t"
      "s_waitcnt lgkmcnt(1)\n\t"
      "v_mfma_f32_16x16x32_bf16 v[220:223], v[164:167], v[24:27], v[220:223]\n\t"
      "v_add_f32_e32 v236, v236, v238\n\t"
      "v_mfma_f32_16x16x32_bf16 v[220:223], v[168:171], v[40:43], v[220:223]\n\t"
      "v_add_f32_e32 v240, v240, v242\n\t"
      "v_mfma_f32_16x16x32_bf16 v[220:223], v[172:175], v[56:59], v[220:223]\n\t"
      "v_add_f32_e32 v237, v237, v239\n\t"
      "v_add_f32_e32 v241, v241, v243\n\t"
      "s_add_u32 s58, s76, s40\n\t"
      "s_addc_u32 s59, s77, 0\n\t"
      "global_load_dwordx4 v[136:139], v244, s[58:59] sc1\n\t"
      "v_mfma_f32_16x16x32_bf16 v[220:223], v[176:179], v[72:75], v[220:223]\n\t"
      "v_add_f32_e32 v228, v228, v232\n\t"
      "v_mfma_f32_16x16x32_bf16 v[220:223], v[180:183], v[88:91], v[220:223]\n\t"
      "v_add_f32_e32 v236, v236, v240\n\t"
      "ds_write_b128 v245, v[216:219] offset:1024\n\t"
      "v_mfma_f32_16x16x32_bf16 v[220:223], v[184:187], v[104:107], v[220:223]\n\t"
      "v_add_f32_e32 v229, v229, v233\n\t"
      "v_add_f32_e32 v237, v237, v241\n\t"
      "v_mfma_f32_16x16x32_bf16 v[224:227], v[160:163], v[12:15], 0\n\t"
      "v_add_f32_e32 v228, v228, v248\n\t"
      "s_add_u32 s58, s76, s41\n\t"
      "s_addc_u32 s59, s77, 0\n\t"
      "global_load_dwordx4 v[140:143], v244, s[58:59] sc1\n\t"
      "v_mfma_f32_16x16x32_bf16 v[224:227], v[164:167], v[28:31], v[224:227]\n\t"
      "v_add_f32_e32 v229, v229, v249\n\t"
      "v_mfma_f32_16x16x32_bf16 v[224:227], v[168:171], v[44:47], v[224:227]\n\t"
      "v_add_f32_e32 v228, v228, v236\n\t"
      "v_add_f32_e32 v229, v229, v237\n\t"
      "v_mfma_f32_16x16x32_bf16 v[224:227], v[172:175], v[60:63], v[224:227]\n\t"
      "v_cvt_pk_bf16_f32 v232, v228, v229\n\t"
      "v_mfma_f32_16x16x32_bf16 v[224:227], v[176:179], v[76:79], v[224:227]\n\t"
      "s_mov_b32 vcc_lo, 0x77777777\n\t"
      "s_mov_b32 vcc_hi, 0x77777777\n\t"
      "v_cndmask_b32_dpp v120, v232, v120, vcc quad_perm:[0,0,0,0] row_mask:0xf bank_mask:0xf\n\t"
      "v_cndmask_b32_dpp v121, v232, v121, vcc quad_perm:[1,1,1,1] row_mask:0xf bank_mask:0xf\n\t"
      "v_cndmask_b32_dpp v122, v232, v122, vcc quad_perm:[2,2,2,2] row_mask:0xf bank_mask:0xf\n\t"
      "v_cndmask_b32_dpp v123, v232, v123, vcc quad_perm:[3,3,3,3] row_mask:0xf bank_mask:0xf\n\t"
      "s_add_u32 s58, s76, s42\n\t"
      "s_addc_u32 s59, s77, 0\n\t"
      "global_load_dwordx4 v[144:147], v244, s[58:59] sc1\n\t"
      "v_mfma_f32_16x16x32_bf16 v[224:227], v[180:183], v[92:95], v[224:227]\n\t"
      "ds_write_b128 v245, v[220:223] offset:2048\n\t"
      "v_mfma_f32_16x16x32_bf16 v[224:227], v[184:187], v[108:111], v[224:227]\n\t"
      "s_waitcnt vmcnt(9)\n\t"
      "v_mfma_f32_16x16x32_bf16 v[208:211], v[188:191], v[0:3], 0\n\t"
      "s_waitcnt vmcnt(8)\n\t"
      "v_mfma_f32_16x16x32_bf16 v[208:211], v[192:195], v[16:19], v[208:211]\n\t"
      "s_add_u32 s58, s76, s43\n\t"
      "s_addc_u32 s59, s77, 0\n\t"
      "global_load_dwordx4 v[148:151], v244, s[58:59] sc1\n\t"
      "s_nop 0\n\t"
      "ds_write_b128 v245, v[224:227] offset:3072\n\t"
      "s_waitcnt vmcnt(8)\n\t"
      "v_mfma_f32_16x16x32_bf16 v[208:211], v[196:199], v[32:35], v[208:211]\n\t"
      "s_waitcnt vmcnt(7)\n\t"
      "v_mfma_f32_16x16x32_bf16 v[208:211], v[200:203], v[48:51], v[208:211]\n\t"
      "s_waitcnt vmcnt(6)\n\t"
      "v_mfma_f32_16x16x32_bf16 v[208:211], v[204:207], v[64:67], v[208:211]\n\t"
      "s_waitcnt lgkmcnt(0)\n\t"
      "s_barrier\n\t"
      "ds_read_b64 v[228:229], v246\n\t"
      "ds_read_b64 v[230:231], v246 offset:4096\n\t"
      "s_waitcnt vmcnt(5)\n\t"
      "v_mfma_f32_16x16x32_bf16 v[208:211], v[128:131], v[80:83], v[208:211]\n\t"
      "s_add_u32 s58, s76, s44\n\t"
      "s_addc_u32 s59, s77, 0\n\t"
      "global_load_dwordx4 v[152:155], v244, s[58:59] sc1\n\t"
      "ds_read_b64 v[232:233], v246 offset:8192\n\t"
      "ds_read_b64 v[234:235], v246 offset:12288\n\t"
      "s_waitcnt vmcnt(5)\n\t"
      "v_mfma_f32_16x16x32_bf16 v[208:211], v[132:135], v[96:99], v[208:211]\n\t"
      "ds_read_b64 v[236:237], v246 offset:16384\n\t"
      "ds_read_b64 v[238:239], v246 offset:20480\n\t"
      "v_mfma_f32_16x16x32_bf16 v[212:215], v[188:191], v[4:7], 0\n\t"
      "ds_read_b64 v[240:241], v246 offset:24576\n\t"
      "ds_read_b64 v[242:243], v246 offset:28672\n\t"
      "v_lshlrev_b32_e32 v248, 16, v116\n\t"
      "v_mfma_f32_16x16x32_bf16 v[212:215], v[192:195], v[20:23], v[212:215]\n\t"
      "v_and_b32_e32 v249, 0xffff0000, v116\n\t"
      "v_mfma_f32_16x16x32_bf16 v[212:215], v[196:199], v[36:39], v[212:215]\n\t"
      "s_add_u32 s58, s76, s45\n\t"
      "s_addc_u32 s59, s77, 0\n\t"
      "global_load_dwordx4 v[156:159], v244, s[58:59] sc1\n\t"
      "v_add_f32_e32 v248, v254, v248\n\t"
      "v_add_f32_e32 v251, v208, v209\n\t"
      "v_add_f32_e32 v251, v251, v210\n\t"
      "v_cmp_u_f32_e32 vcc, v251, v211\n\t"
      "s_cbranch_vccnz LA45R_%=\n\t"
      "LA45B_%=:\n\t"
      "v_mfma_f32_16x16x32_bf16 v[212:215], v[200:203], v[52:55], v[212:215]\n\t"
      "v_add_f32_e32 v249, v254, v249\n\t"
      "s_waitcnt lgkmcnt(4)\n\t"
      "v_mfma_f32_16x16x32_bf16 v[212:215], v[204:207], v[68:71], v[212:215]\n\t"
      "v_add_f32_e32 v228, v228, v230\n\t"
      "v_mfma_f32_16x16x32_bf16 v[212:215], v[128:131], v[84:87], v[212:215]\n\t"
      "v_add_f32_e32 v232, v232, v234\n\t"
      "ds_write_b128 v245, v[208:211] offset:32768\n\t"
      "v_mfma_f32_16x16x32_bf16 v[212:215], v[132:135], v[100:103], v[212:215]\n\t"
      "s_add_u32 s58, s76, s46\n\t"
      "s_addc_u32 s59, s77, 0\n\t"
      "global_load_dwordx4 v[160:163], v244, s[58:59] sc1\n\t"
      "v_add_f32_e32 v229, v229, v231\n\t"
      "v_add_f32_e32 v233, v233, v235\n\t"
      "v_mfma_f32_16x16x32_bf16 v[216:219], v[188:191], v[8:11], 0\n\t"
      "s_waitcnt lgkmcnt(1)\n\t"
      "v_mfma_f32_16x16x32_bf16 v[216:219], v[192:195], v[24:27], v[216:219]\n\t"
      "v_add_f32_e32 v236, v236, v238\n\t"
      "v_mfma_f32_16x16x32_bf16 v[216:219], v[196:199], v[40:43], v[216:219]\n\t"
      "v_add_f32_e32 v240, v240, v242\n\t"
      "v_mfma_f32_16x16x32_bf16 v[216:219], v[200:203], v[56:59], v[216:219]\n\t"
      "v_add_f32_e32 v237, v237, v239\n\t"
      "v_add_f32_e32 v241, v241, v243\n\t"
      "v_mfma_f32_16x16x32_bf16 v[216:219], v[204:207], v[72:75], v[216:219]\n\t"
      "v_add_f32_e32 v228, v228, v232\n\t"
      "v_mfma_f32_16x16x32_bf16 v[216:219], v[128:131], v[88:91], v[216:219]\n\t"
      "v_add_f32_e32 v236, v236, v240\n\t"
      "ds_write_b128 v245, v[212:215] offset:33792\n\t"
      "v_mfma_f32_16x16x32_bf16 v[216:219], v[132:135], v[104:107], v[216:219]\n\t"
      "v_add_f32_e32 v229, v229, v233\n\t"
      "v_add_f32_e32 v237, v237, v241\n\t"
      "v_mfma_f32_16x16x32_bf16 v[220:223], v[188:191], v[12:15], 0\n\t"
      "v_add_f32_e32 v228, v228, v248\n\t"
      "v_mfma_f32_16x16x32_bf16 v[220:223], v[192:195], v[28:31], v[220:223]\n\t"
      "v_add_f32_e32 v229, v229, v249\n\t"
      "v_mfma_f32_16x16x32_bf16 v[220:223], v[196:199], v[44:47], v[220:223]\n\t"
      "v_add_f32_e32 v228, v228, v236\n\t"
      "v_add_f32_e32 v229, v229, v237\n\t"
      "v_mfma_f32_16x16x32_bf16 v[220:223], v[200:203], v[60:63], v[220:223]\n\t"
      "v_cvt_pk_bf16_f32 v232, v228, v229\n\t"
      "v_mfma_f32_16x16x32_bf16 v[220:223], v[204:207], v[76:79], v[220:223]\n\t"
      "s_mov_b32 vcc_lo, 0xeeeeeeee\n\t"
      "s_mov_b32 vcc_hi, 0xeeeeeeee\n\t"
      "v_cndmask_b32_dpp v124, v232, v124, vcc quad_perm:[0,0,0,0] row_mask:0xf bank_mask:0xf\n\t"
      "v_cndmask_b32_dpp v125, v232, v125, vcc quad_perm:[1,1,1,1] row_mask:0xf bank_mask:0xf\n\t"
      "v_cndmask_b32_dpp v126, v232, v126, vcc quad_perm:[2,2,2,2] row_mask:0xf bank_mask:0xf\n\t"
      "v_cndmask_b32_dpp v127, v232, v127, vcc quad_perm:[3,3,3,3] row_mask:0xf bank_mask:0xf\n\t"
      "v_mfma_f32_16x16x32_bf16 v[220:223], v[128:131], v[92:95], v[220:223]\n\t"
      "ds_write_b128 v245, v[216:219] offset:34816\n\t"
      "v_mfma_f32_16x16x32_bf16 v[220:223], v[132:135], v[108:111], v[220:223]\n\t"
      "s_waitcnt vmcnt(6)\n\t"
      "v_mfma_f32_16x16x32_bf16 v[224:227], v[136:139], v[0:3], 0\n\t"
      "s_waitcnt vmcnt(5)\n\t"
      "v_mfma_f32_16x16x32_bf16 v[224:227], v[140:143], v[16:19], v[224:227]\n\t"
      "s_nop 3\n\t"
      "ds_write_b128 v245, v[220:223] offset:35840\n\t"
      "s_waitcnt vmcnt(4)\n\t"
      "v_mfma_f32_16x16x32_bf16 v[224:227], v[144:147], v[32:35], v[224:227]\n\t"
      "s_waitcnt vmcnt(3)\n\t"
      "v_mfma_f32_16x16x32_bf16 v[224:227], v[148:151], v[48:51], v[224:227]\n\t"
      "s_waitcnt vmcnt(2)\n\t"
      "v_mfma_f32_16x16x32_bf16 v[224:227], v[152:155], v[64:67], v[224:227]\n\t"
      "s_waitcnt lgkmcnt(0)\n\t"
      "s_barrier\n\t"
      "ds_read_b64 v[228:229], v246 offset:32768\n\t"
      "ds_read_b64 v[230:231], v246 offset:36864\n\t"
      "s_waitcnt vmcnt(1)\n\t"
      "v_mfma_f32_16x16x32_bf16 v[224:227], v[156:159], v[80:83], v[224:227]\n\t"
      "ds_read_b64 v[232:233], v246 offset:40960\n\t"
      "ds_read_b64 v[234:235], v246 offset:45056\n\t"
      "s_waitcnt vmcnt(0)\n\t"
      "v_mfma_f32_16x16x32_bf16 v[224:227], v[160:163], v[96:99], v[224:227]\n\t"
      "ds_read_b64 v[236:237], v246 offset:49152\n\t"
      "ds_read_b64 v[238:239], v246 offset:53248\n\t"
      "v_mfma_f32_16x16x32_bf16 v[208:211], v[136:139], v[4:7], 0\n\t"
      "ds_read_b64 v[240:241], v246 offset:57344\n\t"
      "ds_read_b64 v[242:243], v246 offset:61440\n\t"
      "v_lshlrev_b32_e32 v248, 16, v117\n\t"
      "v_and_b32_e32 v249, 0xffff0000, v117\n\t"
      "v_mfma_f32_16x16x32_bf16 v[208:211], v[140:143], v[20:23], v[208:211]\n\t"
      "v_add_f32_e32 v248, v254, v248\n\t"
      "v_add_f32_e32 v249, v254, v249\n\t"
      "v_mfma_f32_16x16x32_bf16 v[208:211], v[144:147], v[36:39], v[208:211]\n\t"
      "s_waitcnt lgkmcnt(4)\n\t"
      "v_add_f32_e32 v228, v228, v230\n\t"
      "v_add_f32_e32 v232, v232, v234\n\t"
      "v_add_f32_e32 v251, v224, v225\n\t"
      "v_add_f32_e32 v251, v251, v226\n\t"
      "v_cmp_u_f32_e32 vcc, v251, v227\n\t"
      "s_cbranch_vccnz LA46R_%=\n\t"
      "LA46B_%=:\n\t"
      "v_mfma_f32_16x16x32_bf16 v[208:211], v[148:151], v[52:55], v[208:211]\n\t"
      "v_add_f32_e32 v229, v229, v231\n\t"
      "v_add_f32_e32 v233, v233, v235\n\t"
      "v_mfma_f32_16x16x32_bf16 v[208:211], v[152:155], v[68:71], v[208:211]\n\t"
      "s_waitcnt lgkmcnt(0)\n\t"
      "v_add_f32_e32 v236, v236, v238\n\t"
      "v_add_f32_e32 v240, v240, v242\n\t"
      "v_mfma_f32_16x16x32_bf16 v[208:211], v[156:159], v[84:87], v[208:211]\n\t"
      "v_add_f32_e32 v237, v237, v239\n\t"
      "v_add_f32_e32 v241, v241, v243\n\t"
      "ds_write_b128 v245, v[224:227]\n\t"
      "v_mfma_f32_16x16x32_bf16 v[208:211], v[160:163], v[100:103], v[208:211]\n\t"
      "v_add_f32_e32 v228, v228, v232\n\t"
      "v_add_f32_e32 v236, v236, v240\n\t"
      "v_add_f32_e32 v229, v229, v233\n\t"
      "v_mfma_f32_16x16x32_bf16 v[212:215], v[136:139], v[8:11], 0\n\t"
      "v_add_f32_e32 v237, v237, v241\n\t"
      "v_add_f32_e32 v228, v228, v248\n\t"
      "v_mfma_f32_16x16x32_bf16 v[212:215], v[140:143], v[24:27], v[212:215]\n\t"
      "v_add_f32_e32 v229, v229, v249\n\t"
      "v_add_f32_e32 v228, v228, v236\n\t"
      "v_add_f32_e32 v229, v229, v237\n\t"
      "v_mfma_f32_16x16x32_bf16 v[212:215], v[144:147], v[40:43], v[212:215]\n\t"
      "v_cvt_pk_bf16_f32 v232, v228, v229\n\t"
      "s_mov_b32 vcc_lo, 0xdddddddd\n\t"
      "s_mov_b32 vcc_hi, 0xdddddddd\n\t"
      "v_cndmask_b32_dpp v124, v232, v124, vcc quad_perm:[0,0,0,0] row_mask:0xf bank_mask:0xf\n\t"
      "v_cndmask_b32_dpp v125, v232, v125, vcc quad_perm:[1,1,1,1] row_mask:0xf bank_mask:0xf\n\t"
      "v_cndmask_b32_dpp v126, v232, v126, vcc quad_perm:[2,2,2,2] row_mask:0xf bank_mask:0xf\n\t"
      "v_cndmask_b32_dpp v127, v232, v127, vcc quad_perm:[3,3,3,3] row_mask:0xf bank_mask:0xf\n\t"
      "v_mfma_f32_16x16x32_bf16 v[212:215], v[148:151], v[56:59], v[212:215]\n\t"
      "v_mfma_f32_16x16x32_bf16 v[212:215], v[152:155], v[72:75], v[212:215]\n\t"
      "v_mfma_f32_16x16x32_bf16 v[212:215], v[156:159], v[88:91], v[212:215]\n\t"
      "ds_write_b128 v245, v[208:211] offset:1024\n\t"
      "v_mfma_f32_16x16x32_bf16 v[212:215], v[160:163], v[104:107], v[212:215]\n\t"
      "v_mfma_f32_16x16x32_bf16 v[216:219], v[136:139], v[12:15], 0\n\t"
      "v_mfma_f32_16x16x32_bf16 v[216:219], v[140:143], v[28:31], v[216:219]\n\t"
      "v_mfma_f32_16x16x32_bf16 v[216:219], v[144:147], v[44:47], v[216:219]\n\t"
      "v_mfma_f32_16x16x32_bf16 v[216:219], v[148:151], v[60:63], v[216:219]\n\t"
      "v_mfma_f32_16x16x32_bf16 v[216:219], v[152:155], v[76:79], v[216:219]\n\t"
      "v_mfma_f32_16x16x32_bf16 v[216:219], v[156:159], v[92:95], v[216:219]\n\t"
      "s_nop 1\n\t"
      "ds_write_b128 v245, v[212:215] offset:2048\n\t"
      "v_mfma_f32_16x16x32_bf16 v[216:219], v[160:163], v[108:111], v[216:219]\n\t"
      "global_store_dwordx4 v247, v[120:123], s[62:63] sc1\n\t"
      "s_nop 6\n\t"
      "ds_write_b128 v245, v[216:219] offset:3072\n\t"
      "s_waitcnt lgkmcnt(0)\n\t"
      "s_barrier\n\t"
      "ds_read_b64 v[228:229], v246\n\t"
      "ds_read_b64 v[230:231], v246 offset:4096\n\t"
      "ds_read_b64 v[232:233], v246 offset:8192\n\t"
      "ds_read_b64 v[234:235], v246 offset:12288\n\t"
      "ds_read_b64 v[236:237], v246 offset:16384\n\t"
      "ds_read_b64 v[238:239], v246 offset:20480\n\t"
      "ds_read_b64 v[240:241], v246 offset:24576\n\t"
      "ds_read_b64 v[242:243], v246 offset:28672\n\t"
      "v_lshlrev_b32_e32 v248, 16, v118\n\t"
      "v_and_b32_e32 v249, 0xffff0000, v118\n\t"
      "v_add_f32_e32 v248, v254, v248\n\t"
      "v_add_f32_e32 v249, v254, v249\n\t"
      "s_waitcnt lgkmcnt(4)\n\t"
      "v_add_f32_e32 v228, v228, v230\n\t"
      "v_add_f32_e32 v232, v232, v234\n\t"
      "v_add_f32_e32 v229, v229, v231\n\t"
      "v_add_f32_e32 v233, v233, v235\n\t"
      "s_waitcnt lgkmcnt(0)\n\t"
      "v_add_f32_e32 v236, v236, v238\n\t"
      "v_add_f32_e32 v240, v240, v242\n\t"
      "v_add_f32_e32 v237, v237, v239\n\t"
      "v_add_f32_e32 v241, v241, v243\n\t"
      "v_add_f32_e32 v228, v228, v232\n\t"
      "v_add_f32_e32 v236, v236, v240\n\t"
      "v_add_f32_e32 v229, v229, v233\n\t"
      "v_add_f32_e32 v237, v237, v241\n\t"
      "v_add_f32_e32 v228, v228, v248\n\t"
      "v_add_f32_e32 v229, v229, v249\n\t"
      "v_add_f32_e32 v228, v228, v236\n\t"
      "v_add_f32_e32 v229, v229, v237\n\t"
      "v_cvt_pk_bf16_f32 v232, v228, v229\n\t"
      "s_mov_b32 vcc_lo, 0xbbbbbbbb\n\t"
      "s_mov_b32 vcc_hi, 0xbbbbbbbb\n\t"
      "v_cndmask_b32_dpp v124, v232, v124, vcc quad_perm:[0,0,0,0] row_mask:0xf bank_mask:0xf\n\t"
      "v_cndmask_b32_dpp v125, v232, v125, vcc quad_perm:[1,1,1,1] row_mask:0xf bank_mask:0xf\n\t"
      "v_cndmask_b32_dpp v126, v232, v126, vcc quad_perm:[2,2,2,2] row_mask:0xf bank_mask:0xf\n\t"
      "v_cndmask_b32_dpp v127, v232, v127, vcc quad_perm:[3,3,3,3] row_mask:0xf bank_mask:0xf\n\t"
      "s_mov_b64 exec, s[82:83]\n\t"
      "global_store_dwordx4 v247, v[124:127], s[62:63] offset:128 sc1\n\t"
      "s_mov_b64 exec, -1\n\t"
      "s_endpgm\n\t"
      "LO0R_%=:\n\t"
      "s_mov_b32 s80, 0\n\t"
      "LO0L_%=:\n\t"
      "s_add_u32 s80, s80, 1\n\t"
      "s_cmp_lt_u32 s80, 262144\n\t"
      "s_cbranch_scc0 LO0X_%=\n\t"
      "s_waitcnt vmcnt(0)\n\t"
      "s_sleep 1\n\t"
      "global_load_dwordx4 v[128:131], v244, s[42:43] offset:-4096 sc1\n\t"
      "global_load_dwordx4 v[132:135], v244, s[42:43] offset:-3072 sc1\n\t"
      "global_load_dwordx4 v[136:139], v244, s[42:43] offset:-2048 sc1\n\t"
      "global_load_dwordx4 v[140:143], v244, s[42:43] offset:-1024 sc1\n\t"
      "global_load_dwordx4 v[144:147], v244, s[42:43] sc1\n\t"
      "global_load_dwordx4 v[148:151], v244, s[42:43] offset:1024 sc1\n\t"
      "global_load_dwordx4 v[152:155], v244, s[42:43] offset:2048 sc1\n\t"
      "global_load_dwordx4 v[156:159], v244, s[42:43] offset:3072 sc1\n\t"
      "global_load_dwordx4 v[160:163], v244, s[44:45] offset:-4096 sc1\n\t"
      "global_load_dwordx4 v[164:167], v244, s[44:45] offset:-3072 sc1\n\t"
      "global_load_dwordx4 v[168:171], v244, s[44:45] offset:-2048 sc1\n\t"
      "global_load_dwordx4 v[172:175], v244, s[44:45] offset:-1024 sc1\n\t"
      "global_load_dwordx4 v[176:179], v244, s[44:45] sc1\n\t"
      "global_load_dwordx4 v[180:183], v244, s[44:45] offset:1024 sc1\n\t"
      "global_load_dwordx4 v[184:187], v244, s[44:45] offset:2048 sc1\n\t"
      "global_load_dwordx4 v[188:191], v244, s[44:45] offset:3072 sc1\n\t"
      "s_waitcnt vmcnt(0)\n\t"
      "v_max3_u32 v232, v128, v129, v130\n\t"
      "v_max3_u32 v232, v232, v131, v132\n\t"
      "v_max3_u32 v232, v232, v133, v134\n\t"
      "v_max_u32_e32 v232, v232, v135\n\t"
      "v_cmp_eq_u32_e32 vcc, -1, v232\n\t"
      "s_cbranch_vccnz LO0L_%=\n\t"
      "LO0X_%=:\n\t"
      "s_branch LO0B_%=\n\t"
      "LO2R_%=:\n\t"
      "s_mov_b32 s80, 0\n\t"
      "LO2L_%=:\n\t"
      "s_add_u32 s80, s80, 1\n\t"
      "s_cmp_lt_u32 s80, 262144\n\t"
      "s_cbranch_scc0 LO2X_%=\n\t"
      "s_waitcnt vmcnt(0)\n\t"
      "s_sleep 1\n\t"
      "global_load_dwordx4 v[136:139], v244, s[42:43] offset:-2048 sc1\n\t"
      "global_load_dwordx4 v[140:143], v244, s[42:43] offset:-1024 sc1\n\t"
      "global_load_dwordx4 v[144:147], v244, s[42:43] sc1\n\t"
      "global_load_dwordx4 v[148:151], v244, s[42:43] offset:1024 sc1\n\t"
      "global_load_dwordx4 v[152:155], v244, s[42:43] offset:2048 sc1\n\t"
      "global_load_dwordx4 v[156:159], v244, s[42:43] offset:3072 sc1\n\t"
      "global_load_dwordx4 v[160:163], v244, s[44:45] offset:-4096 sc1\n\t"
      "global_load_dwordx4 v[164:167], v244, s[44:45] offset:-3072 sc1\n\t"
      "global_load_dwordx4 v[168:171], v244, s[44:45] offset:-2048 sc1\n\t"
      "global_load_dwordx4 v[172:175], v244, s[44:45] offset:-1024 sc1\n\t"
      "global_load_dwordx4 v[176:179], v244, s[44:45] sc1\n\t"
      "global_load_dwordx4 v[180:183], v244, s[44:45] offset:1024 sc1\n\t"
      "global_load_dwordx4 v[184:187], v244, s[44:45] offset:2048 sc1\n\t"
      "global_load_dwordx4 v[188:191], v244, s[44:45] offset:3072 sc1\n\t"
      "s_waitcnt vmcnt(0)\n\t"
      "v_max3_u32 v232, v136, v137, v138\n\t"
      "v_max3_u32 v232, v232, v139, v140\n\t"
      "v_max3_u32 v232, v232, v141, v142\n\t"
      "v_max_u32_e32 v232, v232, v143\n\t"
      "v_cmp_eq_u32_e32 vcc, -1, v232\n\t"
      "s_cbranch_vccnz LO2L_%=\n\t"
      "LO2X_%=:\n\t"
      "s_branch LO2B_%=\n\t"
      "LO4R_%=:\n\t"
      "s_mov_b32 s80, 0\n\t"
      "LO4L_%=:\n\t"
      "s_add_u32 s80, s80, 1\n\t"
      "s_cmp_lt_u32 s80, 262144\n\t"
      "s_cbranch_scc0 LO4X_%=\n\t"
      "s_waitcnt vmcnt(0)\n\t"
      "s_sleep 1\n\t"
      "global_load_dwordx4 v[144:147], v244, s[42:43] sc1\n\t"
      "global_load_dwordx4 v[148:151], v244, s[42:43] offset:1024 sc1\n\t"
      "global_load_dwordx4 v[152:155], v244, s[42:43] offset:2048 sc1\n\t"
      "global_load_dwordx4 v[156:159], v244, s[42:43] offset:3072 sc1\n\t"
      "global_load_dwordx4 v[160:163], v244, s[44:45] offset:-4096 sc1\n\t"
      "global_load_dwordx4 v[164:167], v244, s[44:45] offset:-3072 sc1\n\t"
      "global_load_dwordx4 v[168:171], v244, s[44:45] offset:-2048 sc1\n\t"
      "global_load_dwordx4 v[172:175], v244, s[44:45] offset:-1024 sc1\n\t"
      "global_load_dwordx4 v[176:179], v244, s[44:45] sc1\n\t"
      "global_load_dwordx4 v[180:183], v244, s[44:45] offset:1024 sc1\n\t"
      "global_load_dwordx4 v[184:187], v244, s[44:45] offset:2048 sc1\n\t"
      "global_load_dwordx4 v[188:191], v244, s[44:45] offset:3072 sc1\n\t"
      "s_waitcnt vmcnt(0)\n\t"
      "v_max3_u32 v232, v144, v145, v146\n\t"
      "v_max3_u32 v232, v232, v147, v148\n\t"
      "v_max3_u32 v232, v232, v149, v150\n\t"
      "v_max_u32_e32 v232, v232, v151\n\t"
      "v_cmp_eq_u32_e32 vcc, -1, v232\n\t"
      "s_cbranch_vccnz LO4L_%=\n\t"
      "LO4X_%=:\n\t"
      "s_branch LO4B_%=\n\t"
      "LO6R_%=:\n\t"
      "s_mov_b32 s80, 0\n\t"
      "LO6L_%=:\n\t"
      "s_add_u32 s80, s80, 1\n\t"
      "s_cmp_lt_u32 s80, 262144\n\t"
      "s_cbranch_scc0 LO6X_%=\n\t"
      "s_waitcnt vmcnt(0)\n\t"
      "s_sleep 1\n\t"
      "global_load_dwordx4 v[152:155], v244, s[42:43] offset:2048 sc1\n\t"
      "global_load_dwordx4 v[156:159], v244, s[42:43] offset:3072 sc1\n\t"
      "global_load_dwordx4 v[160:163], v244, s[44:45] offset:-4096 sc1\n\t"
      "global_load_dwordx4 v[164:167], v244, s[44:45] offset:-3072 sc1\n\t"
      "global_load_dwordx4 v[168:171], v244, s[44:45] offset:-2048 sc1\n\t"
      "global_load_dwordx4 v[172:175], v244, s[44:45] offset:-1024 sc1\n\t"
      "global_load_dwordx4 v[176:179], v244, s[44:45] sc1\n\t"
      "global_load_dwordx4 v[180:183], v244, s[44:45] offset:1024 sc1\n\t"
      "global_load_dwordx4 v[184:187], v244, s[44:45] offset:2048 sc1\n\t"
      "global_load_dwordx4 v[188:191], v244, s[44:45] offset:3072 sc1\n\t"
      "s_waitcnt vmcnt(0)\n\t"
      "v_max3_u32 v232, v152, v153, v154\n\t"
      "v_max3_u32 v232, v232, v155, v156\n\t"
      "v_max3_u32 v232, v232, v157, v158\n\t"
      "v_max_u32_e32 v232, v232, v159\n\t"
      "v_cmp_eq_u32_e32 vcc, -1, v232\n\t"
      "s_cbranch_vccnz LO6L_%=\n\t"
      "LO6X_%=:\n\t"
      "s_branch LO6B_%=\n\t"
      "LO8R_%=:\n\t"
      "s_mov_b32 s80, 0\n\t"
      "LO8L_%=:\n\t"
      "s_add_u32 s80, s80, 1\n\t"
      "s_cmp_lt_u32 s80, 262144\n\t"
      "s_cbranch_scc0 LO8X_%=\n\t"
      "s_waitcnt vmcnt(0)\n\t"
      "s_sleep 1\n\t"
      "global_load_dwordx4 v[160:163], v244, s[44:45] offset:-4096 sc1\n\t"
      "global_load_dwordx4 v[164:167], v244, s[44:45] offset:-3072 sc1\n\t"
      "global_load_dwordx4 v[168:171], v244, s[44:45] offset:-2048 sc1\n\t"
      "global_load_dwordx4 v[172:175], v244, s[44:45] offset:-1024 sc1\n\t"
      "global_load_dwordx4 v[176:179], v244, s[44:45] sc1\n\t"
      "global_load_dwordx4 v[180:183], v244, s[44:45] offset:1024 sc1\n\t"
      "global_load_dwordx4 v[184:187], v244, s[44:45] offset:2048 sc1\n\t"
      "global_load_dwordx4 v[188:191], v244, s[44:45] offset:3072 sc1\n\t"
      "s_waitcnt vmcnt(0)\n\t"
      "v_max3_u32 v232, v160, v161, v162\n\t"
      "v_max3_u32 v232, v232, v163, v164\n\t"
      "v_max3_u32 v232, v232, v165, v166\n\t"
      "v_max_u32_e32 v232, v232, v167\n\t"
      "v_cmp_eq_u32_e32 vcc, -1, v232\n\t"
      "s_cbranch_vccnz LO8L_%=\n\t"
      "LO8X_%=:\n\t"
      "s_branch LO8B_%=\n\t"
      "LO10R_%=:\n\t"
      "s_mov_b32 s80, 0\n\t"
      "LO10L_%=:\n\t"
      "s_add_u32 s80, s80, 1\n\t"
      "s_cmp_lt_u32 s80, 262144\n\t"
      "s_cbranch_scc0 LO10X_%=\n\t"
      "s_waitcnt vmcnt(0)\n\t"
      "s_sleep 1\n\t"
      "global_load_dwordx4 v[168:171], v244, s[44:45] offset:-2048 sc1\n\t"
      "global_load_dwordx4 v[172:175], v244, s[44:45] offset:-1024 sc1\n\t"
      "global_load_dwordx4 v[176:179], v244, s[44:45] sc1\n\t"
      "global_load_dwordx4 v[180:183], v244, s[44:45] offset:1024 sc1\n\t"
      "global_load_dwordx4 v[184:187], v244, s[44:45] offset:2048 sc1\n\t"
      "global_load_dwordx4 v[188:191], v244, s[44:45] offset:3072 sc1\n\t"
      "s_waitcnt vmcnt(0)\n\t"
      "v_max3_u32 v232, v168, v169, v170\n\t"
      "v_max3_u32 v232, v232, v171, v172\n\t"
      "v_max3_u32 v232, v232, v173, v174\n\t"
      "v_max_u32_e32 v232, v232, v175\n\t"
      "v_cmp_eq_u32_e32 vcc, -1, v232\n\t"
      "s_cbranch_vccnz LO10L_%=\n\t"
      "LO10X_%=:\n\t"
      "s_branch LO10B_%=\n\t"
      "LO12R_%=:\n\t"
      "s_mov_b32 s80, 0\n\t"
      "LO12L_%=:\n\t"
      "s_add_u32 s80, s80, 1\n\t"
      "s_cmp_lt_u32 s80, 262144\n\t"
      "s_cbranch_scc0 LO12X_%=\n\t"
      "s_waitcnt vmcnt(0)\n\t"
      "s_sleep 1\n\t"
      "global_load_dwordx4 v[176:179], v244, s[44:45] sc1\n\t"
      "global_load_dwordx4 v[180:183], v244, s[44:45] offset:1024 sc1\n\t"
      "global_load_dwordx4 v[184:187], v244, s[44:45] offset:2048 sc1\n\t"
      "global_load_dwordx4 v[188:191], v244, s[44:45] offset:3072 sc1\n\t"
      "s_waitcnt vmcnt(0)\n\t"
      "v_max3_u32 v232, v176, v177, v178\n\t"
      "v_max3_u32 v232, v232, v179, v180\n\t"
      "v_max3_u32 v232, v232, v181, v182\n\t"
      "v_max_u32_e32 v232, v232, v183\n\t"
      "v_cmp_eq_u32_e32 vcc, -1, v232\n\t"
      "s_cbranch_vccnz LO12L_%=\n\t"
      "LO12X_%=:\n\t"
      "s_branch LO12B_%=\n\t"
      "LO14R_%=:\n\t"
      "s_mov_b32 s80, 0\n\t"
      "LO14L_%=:\n\t"
      "s_add_u32 s80, s80, 1\n\t"
      "s_cmp_lt_u32 s80, 262144\n\t"
      "s_cbranch_scc0 LO14X_%=\n\t"
      "s_waitcnt vmcnt(0)\n\t"
      "s_sleep 1\n\t"
      "global_load_dwordx4 v[184:187], v244, s[44:45] offset:2048 sc1\n\t"
      "global_load_dwordx4 v[188:191], v244, s[44:45] offset:3072 sc1\n\t"
      "s_waitcnt vmcnt(0)\n\t"
      "v_max3_u32 v232, v184, v185, v186\n\t"
      "v_max3_u32 v232, v232, v187, v188\n\t"
      "v_max3_u32 v232, v232, v189, v190\n\t"
      "v_max_u32_e32 v232, v232, v191\n\t"
      "v_cmp_eq_u32_e32 vcc, -1, v232\n\t"
      "s_cbranch_vccnz LO14L_%=\n\t"
      "LO14X_%=:\n\t"
      "s_branch LO14B_%=\n\t"
      "LGX_%=:\n\t"
      "v_max3_u32 v232, v128, v129, v130\n\t"
      "v_max3_u32 v232, v232, v131, v132\n\t"
      "v_max3_u32 v232, v232, v133, v134\n\t"
      "v_max3_u32 v232, v232, v135, v136\n\t"
      "v_max3_u32 v232, v232, v137, v138\n\t"
      "v_max3_u32 v232, v232, v139, v140\n\t"
      "v_max3_u32 v232, v232, v141, v142\n\t"
      "v_max3_u32 v232, v232, v143, v144\n\t"
      "v_max3_u32 v232, v232, v145, v146\n\t"
      "v_max3_u32 v232, v232, v147, v148\n\t"
      "v_max3_u32 v232, v232, v149, v150\n\t"
      "v_max3_u32 v232, v232, v151, v152\n\t"
      "v_max3_u32 v232, v232, v153, v154\n\t"
      "v_max3_u32 v232, v232, v155, v156\n\t"
      "v_max3_u32 v232, v232, v157, v158\n\t"
      "v_max3_u32 v232, v232, v159, v160\n\t"
      "v_max3_u32 v232, v232, v161, v162\n\t"
      "v_max3_u32 v232, v232, v163, v164\n\t"
      "v_max3_u32 v232, v232, v165, v166\n\t"
      "v_max3_u32 v232, v232, v167, v168\n\t"
      "v_max3_u32 v232, v232, v169, v170\n\t"
      "v_max3_u32 v232, v232, v171, v172\n\t"
      "v_max3_u32 v232, v232, v173, v174\n\t"
      "v_max3_u32 v232, v232, v175, v176\n\t"
      "v_max3_u32 v232, v232, v177, v178\n\t"
      "v_max3_u32 v232, v232, v179, v180\n\t"
      "v_max3_u32 v232, v232, v181, v182\n\t"
      "v_max3_u32 v232, v232, v183, v184\n\t"
      "v_max3_u32 v232, v232, v185, v186\n\t"
      "v_max3_u32 v232, v232, v187, v188\n\t"
      "v_max3_u32 v232, v232, v189, v190\n\t"
      "v_max_u32_e32 v232, v232, v191\n\t"
      "v_cmp_eq_u32_e32 vcc, -1, v232\n\t"
      "s_cbranch_vccz LGB_%=\n\t"
      "LGYR_%=:\n\t"
      "s_mov_b32 s80, 0\n\t"
      "LGYL_%=:\n\t"
      "s_add_u32 s80, s80, 1\n\t"
      "s_cmp_lt_u32 s80, 262144\n\t"
      "s_cbranch_scc0 LGYX_%=\n\t"
      "s_sleep 1\n\t"
      "global_load_dwordx4 v[128:131], v244, s[42:43] offset:-4096 sc1\n\t"
      "global_load_dwordx4 v[132:135], v244, s[42:43] offset:-3072 sc1\n\t"
      "global_load_dwordx4 v[136:139], v244, s[42:43] offset:-2048 sc1\n\t"
      "global_load_dwordx4 v[140:143], v244, s[42:43] offset:-1024 sc1\n\t"
      "global_load_dwordx4 v[144:147], v244, s[42:43] sc1\n\t"
      "global_load_dwordx4 v[148:151], v244, s[42:43] offset:1024 sc1\n\t"
      "global_load_dwordx4 v[152:155], v244, s[42:43] offset:2048 sc1\n\t"
      "global_load_dwordx4 v[156:159], v244, s[42:43] offset:3072 sc1\n\t"
      "global_load_dwordx4 v[160:163], v244, s[44:45] offset:-4096 sc1\n\t"
      "global_load_dwordx4 v[164:167], v244, s[44:45] offset:-3072 sc1\n\t"
      "global_load_dwordx4 v[168:171], v244, s[44:45] offset:-2048 sc1\n\t"
      "global_load_dwordx4 v[172:175], v244, s[44:45] offset:-1024 sc1\n\t"
      "global_load_dwordx4 v[176:179], v244, s[44:45] sc1\n\t"
      "global_load_dwordx4 v[180:183], v244, s[44:45] offset:1024 sc1\n\t"
      "global_load_dwordx4 v[184:187], v244, s[44:45] offset:2048 sc1\n\t"
      "global_load_dwordx4 v[188:191], v244, s[44:45] offset:3072 sc1\n\t"
      "s_waitcnt vmcnt(0)\n\t"
      "v_max3_u32 v232, v128, v129, v130\n\t"
      "v_max3_u32 v232, v232, v131, v132\n\t"
      "v_max3_u32 v232, v232, v133, v134\n\t"
      "v_max3_u32 v232, v232, v135, v136\n\t"
      "v_max3_u32 v232, v232, v137, v138\n\t"
      "v_max3_u32 v232, v232, v139, v140\n\t"
      "v_max3_u32 v232, v232, v141, v142\n\t"
      "v_max3_u32 v232, v232, v143, v144\n\t"
      "v_max3_u32 v232, v232, v145, v146\n\t"
      "v_max3_u32 v232, v232, v147, v148\n\t"
      "v_max3_u32 v232, v232, v149, v150\n\t"
      "v_max3_u32 v232, v232, v151, v152\n\t"
      "v_max3_u32 v232, v232, v153, v154\n\t"
      "v_max3_u32 v232, v232, v155, v156\n\t"
      "v_max3_u32 v232, v232, v157, v158\n\t"
      "v_max3_u32 v232, v232, v159, v160\n\t"
      "v_max3_u32 v232, v232, v161, v162\n\t"
      "v_max3_u32 v232, v232, v163, v164\n\t"
      "v_max3_u32 v232, v232, v165, v166\n\t"
      "v_max3_u32 v232, v232, v167, v168\n\t"
      "v_max3_u32 v232, v232, v169, v170\n\t"
      "v_max3_u32 v232, v232, v171, v172\n\t"
      "v_max3_u32 v232, v232, v173, v174\n\t"
      "v_max3_u32 v232, v232, v175, v176\n\t"
      "v_max3_u32 v232, v232, v177, v178\n\t"
      "v_max3_u32 v232, v232, v179, v180\n\t"
      "v_max3_u32 v232, v232, v181, v182\n\t"
      "v_max3_u32 v232, v232, v183, v184\n\t"
      "v_max3_u32 v232, v232, v185, v186\n\t"
      "v_max3_u32 v232, v232, v187, v188\n\t"
      "v_max3_u32 v232, v232, v189, v190\n\t"
      "v_max_u32_e32 v232, v232, v191\n\t"
      "v_cmp_eq_u32_e32 vcc, -1, v232\n\t"
      "s_cbranch_vccnz LGYL_%=\n\t"
      "LGYX_%=:\n\t"
      "s_cbranch_vccnz LGB_%=\n\t"
      "ds_read_b64_tr_b16 v[0:1], v216\n\t"
      "ds_read_b64_tr_b16 v[2:3], v220\n\t"
      "ds_read_b64_tr_b16 v[4:5], v217\n\t"
      "ds_read_b64_tr_b16 v[6:7], v221\n\t"
      "ds_read_b64_tr_b16 v[8:9], v218\n\t"
      "ds_read_b64_tr_b16 v[10:11], v222\n\t"
      "ds_read_b64_tr_b16 v[12:13], v219\n\t"
      "ds_read_b64_tr_b16 v[14:15], v223\n\t"
      "ds_read_b64_tr_b16 v[16:17], v218 offset:3584\n\t"
      "ds_read_b64_tr_b16 v[18:19], v222 offset:3584\n\t"
      "ds_read_b64_tr_b16 v[20:21], v219 offset:3584\n\t"
      "ds_read_b64_tr_b16 v[22:23], v223 offset:3584\n\t"
      "ds_read_b64_tr_b16 v[24:25], v216 offset:3584\n\t"
      "ds_read_b64_tr_b16 v[26:27], v220 offset:3584\n\t"
      "ds_read_b64_tr_b16 v[28:29], v217 offset:3584\n\t"
      "ds_read_b64_tr_b16 v[30:31], v221 offset:3584\n\t"
      "ds_read_b64_tr_b16 v[32:33], v216 offset:7168\n\t"
      "ds_read_b64_tr_b16 v[34:35], v220 offset:7168\n\t"
      "ds_read_b64_tr_b16 v[36:37], v217 offset:7168\n\t"
      "ds_read_b64_tr_b16 v[38:39], v221 offset:7168\n\t"
      "ds_read_b64_tr_b16 v[40:41], v218 offset:7168\n\t"
      "ds_read_b64_tr_b16 v[42:43], v222 offset:7168\n\t"
      "ds_read_b64_tr_b16 v[44:45], v219 offset:7168\n\t"
      "ds_read_b64_tr_b16 v[46:47], v223 offset:7168\n\t"
      "ds_read_b64_tr_b16 v[48:49], v218 offset:10752\n\t"
      "ds_read_b64_tr_b16 v[50:51], v222 offset:10752\n\t"
      "ds_read_b64_tr_b16 v[52:53], v219 offset:10752\n\t"
      "ds_read_b64_tr_b16 v[54:55], v223 offset:10752\n\t"
      "ds_read_b64_tr_b16 v[56:57], v216 offset:10752\n\t"
      "ds_read_b64_tr_b16 v[58:59], v220 offset:10752\n\t"
      "ds_read_b64_tr_b16 v[60:61], v217 offset:10752\n\t"
      "ds_read_b64_tr_b16 v[62:63], v221 offset:10752\n\t"
      "ds_read_b64_tr_b16 v[64:65], v216 offset:14336\n\t"
      "ds_read_b64_tr_b16 v[66:67], v220 offset:14336\n\t"
      "ds_read_b64_tr_b16 v[68:69], v217 offset:14336\n\t"
      "ds_read_b64_tr_b16 v[70:71], v221 offset:14336\n\t"
      "ds_read_b64_tr_b16 v[72:73], v218 offset:14336\n\t"
      "ds_read_b64_tr_b16 v[74:75], v222 offset:14336\n\t"
      "ds_read_b64_tr_b16 v[76:77], v219 offset:14336\n\t"
      "ds_read_b64_tr_b16 v[78:79], v223 offset:14336\n\t"
      "ds_read_b64_tr_b16 v[80:81], v218 offset:17920\n\t"
      "ds_read_b64_tr_b16 v[82:83], v222 offset:17920\n\t"
      "ds_read_b64_tr_b16 v[84:85], v219 offset:17920\n\t"
      "ds_read_b64_tr_b16 v[86:87], v223 offset:17920\n\t"
      "ds_read_b64_tr_b16 v[88:89], v216 offset:17920\n\t"
      "ds_read_b64_tr_b16 v[90:91], v220 offset:17920\n\t"
      "ds_read_b64_tr_b16 v[92:93], v217 offset:17920\n\t"
      "ds_read_b64_tr_b16 v[94:95], v221 offset:17920\n\t"
      "ds_read_b64_tr_b16 v[96:97], v216 offset:21504\n\t"
      "ds_read_b64_tr_b16 v[98:99], v220 offset:21504\n\t"
      "ds_read_b64_tr_b16 v[100:101], v217 offset:21504\n\t"
      "ds_read_b64_tr_b16 v[102:103], v221 offset:21504\n\t"
      "ds_read_b64_tr_b16 v[104:105], v218 offset:21504\n\t"
      "ds_read_b64_tr_b16 v[106:107], v222 offset:21504\n\t"
      "ds_read_b64_tr_b16 v[108:109], v219 offset:21504\n\t"
      "ds_read_b64_tr_b16 v[110:111], v223 offset:21504\n\t"
      "ds_read_b64_tr_b16 v[112:113], v218 offset:25088\n\t"
      "ds_read_b64_tr_b16 v[114:115], v222 offset:25088\n\t"
      "ds_read_b64_tr_b16 v[116:117], v219 offset:25088\n\t"
      "ds_read_b64_tr_b16 v[118:119], v223 offset:25088\n\t"
      "ds_read_b64_tr_b16 v[120:121], v216 offset:25088\n\t"
      "ds_read_b64_tr_b16 v[122:123], v220 offset:25088\n\t"
      "ds_read_b64_tr_b16 v[124:125], v217 offset:25088\n\t"
      "ds_read_b64_tr_b16 v[126:127], v221 offset:25088\n\t"
      "v_lshlrev_b32_e32 v192, 16, v208\n\t"
      "v_and_b32_e32 v193, 0xffff0000, v208\n\t"
      "v_lshlrev_b32_e32 v194, 16, v209\n\t"
      "v_and_b32_e32 v195, 0xffff0000, v209\n\t"
      "v_lshlrev_b32_e32 v196, 16, v210\n\t"
      "v_and_b32_e32 v197, 0xffff0000, v210\n\t"
      "v_lshlrev_b32_e32 v198, 16, v211\n\t"
      "v_and_b32_e32 v199, 0xffff0000, v211\n\t"
      "v_lshlrev_b32_e32 v200, 16, v212\n\t"
      "v_and_b32_e32 v201, 0xffff0000, v212\n\t"
      "v_lshlrev_b32_e32 v202, 16, v213\n\t"
      "v_and_b32_e32 v203, 0xffff0000, v213\n\t"
      "v_lshlrev_b32_e32 v204, 16, v214\n\t"
      "v_and_b32_e32 v205, 0xffff0000, v214\n\t"
      "v_lshlrev_b32_e32 v206, 16, v215\n\t"
      "v_and_b32_e32 v207, 0xffff0000, v215\n\t"
      "s_waitcnt lgkmcnt(0)\n\t"
      "s_branch LGM_%=\n\t"
      "LSR_%=:\n\t"
      "s_mov_b32 s80, 0\n\t"
      "LSL_%=:\n\t"
      "s_add_u32 s80, s80, 1\n\t"
      "s_cmp_lt_u32 s80, 262144\n\t"
      "s_cbranch_scc0 LSX_%=\n\t"
      "s_sleep 1\n\t"
      "global_load_dwordx2 v[148:149], v231, s[46:47] sc1\n\t"
      "global_load_dwordx2 v[150:151], v231, s[46:47] offset:384 sc1\n\t"
      "s_waitcnt vmcnt(0)\n\t"
      "v_max3_u32 v232, v148, v149, v150\n\t"
      "v_max_u32_e32 v232, v232, v151\n\t"
      "v_cmp_eq_u32_e32 vcc, -1, v232\n\t"
      "s_cbranch_vccnz LSL_%=\n\t"
      "LSX_%=:\n\t"
      "s_branch LSB_%=\n\t"
      "LF1R_%=:\n\t"
      "s_mov_b32 s80, 0\n\t"
      "LF1L_%=:\n\t"
      "s_add_u32 s80, s80, 1\n\t"
      "s_cmp_lt_u32 s80, 262144\n\t"
      "s_cbranch_scc0 LF1X_%=\n\t"
      "s_sleep 2\n\t"
      "global_load_dwordx2 v[138:139], v136, s[60:61] sc1\n\t"
      "s_waitcnt vmcnt(0)\n\t"
      "v_cmp_ne_u64_e32 vcc, s[22:23], v[138:139]\n\t"
      "s_cbranch_vccnz LF1L_%=\n\t"
      "LF1X_%=:\n\t"
      "s_branch LF1B_%=\n\t"
      "LF4R_%=:\n\t"
      "s_mov_b32 s80, 0\n\t"
      "LF4L_%=:\n\t"
      "s_add_u32 s80, s80, 1\n\t"
      "s_cmp_lt_u32 s80, 262144\n\t"
      "s_cbranch_scc0 LF4X_%=\n\t"
      "s_sleep 2\n\t"
      "global_load_dwordx2 v[138:139], v137, s[60:61] sc1\n\t"
      "s_waitcnt vmcnt(0)\n\t"
      "v_cmp_ne_u64_e32 vcc, s[22:23], v[138:139]\n\t"
      "s_cbranch_vccnz LF4L_%=\n\t"
      "LF4X_%=:\n\t"
      "s_branch LF4B_%=\n\t"
      "L4_%=:\n\t"
      "s_waitcnt vmcnt(0)\n\t"
      "s_barrier\n\t"
      "s_barrier\n\t"
      "s_lshl2_add_u32 s72, s31, s24\n\t"
      "s_sub_u32 s72, s72, 56\n\t"
      "s_mov_b32 m0, 64512\n\t"
      "s_cmp_ge_u32 s72, 64\n\t"
      "s_cselect_b32 s73, 8, 0\n\t"
      "s_sub_u32 s73, s72, s73\n\t"
      "s_and_b32 s73, s73, 0x38\n\t"
      "s_add_u32 s72, s72, s73\n\t"
      "s_add_u32 s72, s72, 8\n\t"
      "s_mul_i32 s72, s72, 57344\n\t"
      "s_and_b32 s73, s2, 7\n\t"
      "s_mul_i32 s73, s73, 448\n\t"
      "s_and_b32 s73, s73, 0xffffff80\n\t"
      "s_add_u32 s72, s72, s73\n\t"
      "v_lshrrev_b32_e32 v141, 2, v192\n\t"
      "v_mul_u32_u24_e32 v141, 3584, v141\n\t"
      "v_lshl_add_u32 v141, v196, 7, v141\n\t"
      "v_add_u32_e32 v141, s72, v141\n\t"
      "global_load_lds_dword v141, s[10:11]\n\t"
      "s_barrier\n\t"
      "s_cmp_lg_u32 s24, 3\n\t"
      "s_cbranch_scc1 SK12_%=\n\t"
      "global_load_dwordx2 v[138:139], v137, s[60:61] sc1\n\t"
      "SK12_%=:\n\t"
      "s_cmp_lg_u32 s24, 3\n\t"
      "s_cbranch_scc1 LF4B_%=\n\t"
      "s_waitcnt vmcnt(0)\n\t"
      "v_cmp_ne_u64_e32 vcc, s[22:23], v[138:139]\n\t"
      "s_cbranch_vccnz LF4R_%=\n\t"
      "LF4B_%=:\n\t"
      "s_barrier\n\t"
      "s_waitcnt vmcnt(0)\n\t"
      "s_barrier\n\t"
      "s_branch G4_%=\n\t"
      "LF2R_%=:\n\t"
      "s_mov_b32 s80, 0\n\t"
      "LF2L_%=:\n\t"
      "s_add_u32 s80, s80, 1\n\t"
      "s_cmp_lt_u32 s80, 262144\n\t"
      "s_cbranch_scc0 LF2X_%=\n\t"
      "s_sleep 2\n\t"
      "global_load_dwordx2 v[138:139], v137, s[60:61] sc1\n\t"
      "s_waitcnt vmcnt(0)\n\t"
      "v_cmp_ne_u64_e32 vcc, s[22:23], v[138:139]\n\t"
      "s_cbranch_vccnz LF2L_%=\n\t"
      "LF2X_%=:\n\t"
      "s_branch LF2B_%=\n\t"
      "LF3R_%=:\n\t"
      "s_mov_b32 s80, 0\n\t"
      "LF3L_%=:\n\t"
      "s_add_u32 s80, s80, 1\n\t"
      "s_cmp_lt_u32 s80, 262144\n\t"
      "s_cbranch_scc0 LF3X_%=\n\t"
      "s_sleep 2\n\t"
      "global_load_dwordx2 v[138:139], v143, s[60:61] sc1\n\t"
      "s_waitcnt vmcnt(0)\n\t"
      "v_cmp_ne_u64_e32 vcc, s[22:23], v[138:139]\n\t"
      "s_cbranch_vccnz LF3L_%=\n\t"
      "LF3X_%=:\n\t"
      "s_branch LF3B_%=\n\t"
      "LF70R_%=:\n\t"
      "s_mov_b32 s80, 0\n\t"
      "LF70L_%=:\n\t"
      "s_add_u32 s80, s80, 1\n\t"
      "s_cmp_lt_u32 s80, 262144\n\t"
      "s_cbranch_scc0 LF70X_%=\n\t"
      "s_sleep 4\n\t"
      "global_load_dwordx2 v[138:139], v136, s[60:61] sc1\n\t"
      "s_waitcnt vmcnt(0)\n\t"
      "v_cmp_ne_u64_e32 vcc, s[22:23], v[138:139]\n\t"
      "s_cbranch_vccnz LF70L_%=\n\t"
      "LF70X_%=:\n\t"
      "s_branch LF70B_%=\n\t"
      "LF71R_%=:\n\t"
      "s_mov_b32 s80, 0\n\t"
      "LF71L_%=:\n\t"
      "s_add_u32 s80, s80, 1\n\t"
      "s_cmp_lt_u32 s80, 262144\n\t"
      "s_cbranch_scc0 LF71X_%=\n\t"
      "s_sleep 4\n\t"
      "global_load_dwordx2 v[138:139], v136, s[60:61] sc1\n\t"
      "s_waitcnt vmcnt(0)\n\t"
      "v_cmp_ne_u64_e32 vcc, s[22:23], v[138:139]\n\t"
      "s_cbranch_vccnz LF71L_%=\n\t"
      "LF71X_%=:\n\t"
      "s_branch LF71B_%=\n\t"
      "SPARE_%=:\n\t"
      "v_mov_b32_e32 v132, -1\n\t"
      "v_mov_b32_e32 v133, -1\n\t"
      "v_mov_b32_e32 v134, -1\n\t"
      "v_mov_b32_e32 v135, -1\n\t"
      "s_and_b32 s34, s25, 3\n\t"
      "s_lshl_b32 s34, s34, 15\n\t"
      "s_mul_i32 s35, s24, 917504\n\t"
      "s_add_u32 s34, s34, s35\n\t"
      "s_and_b32 s35, s2, 7\n\t"
      "s_lshl_b32 s35, s35, 9\n\t"
      "s_add_u32 s34, s34, s35\n\t"
      "v_and_b32_e32 v141, 0x1c0, v244\n\t"
      "v_lshlrev_b32_e32 v141, 6, v141\n\t"
      "v_and_b32_e32 v142, 0x30, v244\n\t"
      "v_lshl_add_u32 v141, v142, 3, v141\n\t"
      "v_add_u32_e32 v141, s34, v141\n\t"
      "s_mov_b32 m0, 64512\n\t"
      "s_lshr_b32 s34, s24, 1\n\t"
      "s_lshl_b32 s34, s34, 5\n\t"
      "s_add_u32 s34, s34, s25\n\t"
      "s_and_b32 s35, s24, 1\n\t"
      "s_lshl1_add_u32 s34, s34, s35\n\t"
      "s_lshl_b32 s34, s34, 10\n\t"
      "s_add_u32 s34, s34, 8388608\n\t"
      "s_add_u32 s40, s20, s34\n\t"
      "s_addc_u32 s41, s21, 0\n\t"
      "global_store_dwordx4 v244, v[132:135], s[40:41] sc1\n\t"
      "s_add_u32 s40, s40, 262144\n\t"
      "s_addc_u32 s41, s41, 0\n\t"
      "global_store_dwordx4 v244, v[132:135], s[40:41] sc1\n\t"
      "s_add_u32 s40, s40, 262144\n\t"
      "s_addc_u32 s41, s41, 0\n\t"
      "global_store_dwordx4 v244, v[132:135], s[40:41] sc1\n\t"
      "s_add_u32 s40, s40, 262144\n\t"
      "s_addc_u32 s41, s41, 0\n\t"
      "global_store_dwordx4 v244, v[132:135], s[40:41] sc1\n\t"
      "s_add_u32 s40, s40, 262144\n\t"
      "s_addc_u32 s41, s41, 0\n\t"
      "global_store_dwordx4 v244, v[132:135], s[40:41] sc1\n\t"
      "s_add_u32 s40, s40, 262144\n\t"
      "s_addc_u32 s41, s41, 0\n\t"
      "global_store_dwordx4 v244, v[132:135], s[40:41] sc1\n\t"
      "s_add_u32 s40, s40, 262144\n\t"
      "s_addc_u32 s41, s41, 0\n\t"
      "global_store_dwordx4 v244, v[132:135], s[40:41] sc1\n\t"
      "s_add_u32 s40, s40, 262144\n\t"
      "s_addc_u32 s41, s41, 0\n\t"
      "s_lshl_b32 s34, s25, 7\n\t"
      "s_add_u32 s34, s34, 16384\n\t"
      "v_mov_b32_e32 v131, s34\n\t"
      "s_waitcnt vmcnt(0)\n\t"
      "s_barrier\n\t"
      "s_cmp_lg_u32 s24, 0\n\t"
      "s_cbranch_scc1 SP0_%=\n\t"
      "s_mov_b64 exec, 1\n\t"
      "global_store_dwordx2 v131, v[128:129], s[60:61] sc1\n\t"
      "s_mov_b64 exec, -1\n\t"
      "SP0_%=:\n\t"
      "s_and_b32 s34, s25, 3\n\t"
      "s_lshl3_add_u32 s34, s34, s24\n\t"
      "s_cmp_lg_u32 s34, 19\n\t"
      "s_cbranch_scc1 SQ0_%=\n\t"
      "v_and_b32_e32 v136, 0x1f0, v244\n\t"
      "v_lshlrev_b32_e32 v131, 6, v136\n\t"
      "v_lshlrev_b32_e32 v136, 3, v136\n\t"
      "v_add_u32_e32 v136, 16384, v136\n\t"
      "s_and_b32 s34, s2, 7\n\t"
      "s_lshl_b32 s34, s34, 7\n\t"
      "s_add_u32 s34, s34, 32768\n\t"
      "v_add_u32_e32 v131, s34, v131\n\t"
      "s_cmp_lg_u32 s24, 3\n\t"
      "s_cbranch_scc1 SK15_%=\n\t"
      "global_load_dwordx2 v[138:139], v136, s[60:61] sc1\n\t"
      "SK15_%=:\n\t"
      "s_cmp_lg_u32 s24, 3\n\t"
      "s_cbranch_scc1 LF70B_%=\n\t"
      "s_waitcnt vmcnt(0)\n\t"
      "v_cmp_ne_u64_e32 vcc, s[22:23], v[138:139]\n\t"
      "s_cbranch_vccnz LF70R_%=\n\t"
      "LF70B_%=:\n\t"
      "global_store_dwordx2 v131, v[128:129], s[60:61] sc1\n\t"
      "SQ0_%=:\n\t"
      "s_barrier\n\t"
      "s_lshl3_add_u32 s34, s25, s24\n\t"
      "s_mul_i32 s34, s34, 7168\n\t"
      "s_add_u32 s34, s34, 10227712\n\t"
      "s_add_u32 s40, s20, s34\n\t"
      "s_addc_u32 s41, s21, 0\n\t"
      "global_store_dwordx4 v244, v[132:135], s[40:41] offset:-4096 sc1\n\t"
      "global_store_dwordx4 v244, v[132:135], s[40:41] offset:-3072 sc1\n\t"
      "global_store_dwordx4 v244, v[132:135], s[40:41] offset:-2048 sc1\n\t"
      "global_store_dwordx4 v244, v[132:135], s[40:41] offset:-1024 sc1\n\t"
      "global_store_dwordx4 v244, v[132:135], s[40:41] sc1\n\t"
      "global_store_dwordx4 v244, v[132:135], s[40:41] offset:1024 sc1\n\t"
      "global_store_dwordx4 v244, v[132:135], s[40:41] offset:2048 sc1\n\t"
      "s_lshl_b32 s34, s25, 7\n\t"
      "s_add_u32 s34, s34, 24576\n\t"
      "v_mov_b32_e32 v131, s34\n\t"
      "s_waitcnt vmcnt(0)\n\t"
      "s_barrier\n\t"
      "s_cmp_lg_u32 s24, 0\n\t"
      "s_cbranch_scc1 SP1_%=\n\t"
      "s_mov_b64 exec, 1\n\t"
      "global_store_dwordx2 v131, v[128:129], s[60:61] sc1\n\t"
      "s_mov_b64 exec, -1\n\t"
      "SP1_%=:\n\t"
      "s_and_b32 s34, s25, 3\n\t"
      "s_lshl3_add_u32 s34, s34, s24\n\t"
      "s_cmp_lg_u32 s34, 27\n\t"
      "s_cbranch_scc1 SQ1_%=\n\t"
      "v_and_b32_e32 v136, 0x1f0, v244\n\t"
      "v_lshlrev_b32_e32 v131, 6, v136\n\t"
      "v_lshlrev_b32_e32 v136, 3, v136\n\t"
      "v_add_u32_e32 v136, 24576, v136\n\t"
      "s_and_b32 s34, s2, 7\n\t"
      "s_lshl_b32 s34, s34, 7\n\t"
      "s_add_u32 s34, s34, 65536\n\t"
      "v_add_u32_e32 v131, s34, v131\n\t"
      "s_cmp_lg_u32 s24, 3\n\t"
      "s_cbranch_scc1 SK16_%=\n\t"
      "global_load_dwordx2 v[138:139], v136, s[60:61] sc1\n\t"
      "SK16_%=:\n\t"
      "s_cmp_lg_u32 s24, 3\n\t"
      "s_cbranch_scc1 LF71B_%=\n\t"
      "s_waitcnt vmcnt(0)\n\t"
      "v_cmp_ne_u64_e32 vcc, s[22:23], v[138:139]\n\t"
      "s_cbranch_vccnz LF71R_%=\n\t"
      "LF71B_%=:\n\t"
      "global_store_dwordx2 v131, v[128:129], s[60:61] sc1\n\t"
      "SQ1_%=:\n\t"
      "s_barrier\n\t"
      "s_lshr_b32 s35, s2, 6\n\t"
      "s_lshl2_add_u32 s31, s26, s35\n\t"
      "s_and_b32 s35, s2, 7\n\t"
      "s_mul_i32 s34, s35, 14\n\t"
      "s_lshl4_add_u32 s35, s35, -14\n\t"
      "s_cmp_lt_u32 s31, 14\n\t"
      "s_cselect_b32 s34, s34, s35\n\t"
      "s_cselect_b32 s36, 1, 0\n\t"
      "s_or_b32 s27, s27, s36\n\t"
      "s_add_u32 s34, s34, s31\n\t"
      "s_lshr_b32 s29, s34, 2\n\t"
      "s_and_b32 s30, s34, 3\n\t"
      "s_mul_i32 s30, s30, 7\n\t"
      "v_and_b32_e32 v199, 60, v192\n\t"
      "v_and_b32_e32 v200, 24, v192\n\t"
      "v_lshlrev_b32_e32 v202, 3, v196\n\t"
      "v_lshl_or_b32 v202, v200, 2, v202\n\t"
      "v_lshl_or_b32 v202, v195, 7, v202\n\t"
      "v_lshl_or_b32 v202, v193, 10, v202\n\t"
      "v_add_u32_e32 v224, s39, v202\n\t"
      "v_xor_b32_e32 v225, 32, v224\n\t"
      "v_xor_b32_e32 v226, 64, v224\n\t"
      "v_xor_b32_e32 v227, 96, v224\n\t"
      "s_lshl_b32 s34, s24, 12\n\t"
      "s_add_u32 s34, s34, 98304\n\t"
      "v_add_u32_e32 v245, s34, v244\n\t"
      "v_lshlrev_b32_e32 v199, 2, v199\n\t"
      "s_lshl_b32 s34, s24, 8\n\t"
      "v_lshl_add_u32 v247, v192, 2, s34\n\t"
      "v_and_b32_e32 v200, 2, v192\n\t"
      "v_mul_u32_u24_e32 v202, 576, v200\n\t"
      "v_xor_b32_e32 v199, v202, v199\n\t"
      "v_and_b32_e32 v202, 1, v192\n\t"
      "v_lshl_or_b32 v199, v202, 3, v199\n\t"
      "s_and_b32 s35, s34, 0x400\n\t"
      "s_add_u32 s34, s34, s35\n\t"
      "s_add_u32 s34, s34, 98304\n\t"
      "v_add_u32_e32 v246, s34, v199\n\t"
      "v_lshlrev_b32_e32 v199, 2, v202\n\t"
      "v_lshl_or_b32 v199, v200, 4, v199\n\t"
      "s_min_u32 s34, s29, 27\n\t"
      "s_lshl_b32 s34, s34, 7\n\t"
      "s_lshr_b32 s35, s24, 2\n\t"
      "s_lshl_b32 s35, s35, 6\n\t"
      "s_add_u32 s34, s34, s35\n\t"
      "s_lshr_b32 s35, s39, 9\n\t"
      "s_add_u32 s34, s34, s35\n\t"
      "v_add_u32_e32 v248, s34, v199\n\t"
      "s_add_u32 s48, s20, 8388608\n\t"
      "s_addc_u32 s49, s21, 0\n\t"
      "s_cmp_ge_u32 s31, 30\n\t"
      "s_cbranch_scc1 BYE_%=\n\t"
      "s_setprio 0\n\t"
      "s_waitcnt vmcnt(0)\n\t"
      "s_barrier\n\t"
      "s_branch G4_%=\n\t"
      "BYE_%=:\n\t"
      "s_endpgm\n\t"
      "LA30R_%=:\n\t"
      "s_mov_b32 s80, 0\n\t"
      "LA30L_%=:\n\t"
      "s_add_u32 s80, s80, 1\n\t"
      "s_cmp_lt_u32 s80, 262144\n\t"
      "s_cbranch_scc0 LA30X_%=\n\t"
      "s_waitcnt vmcnt(0)\n\t"
      "s_sleep 1\n\t"
      "global_load_dwordx4 v[128:131], v250, s[64:65] offset:-4096 sc1\n\t"
      "global_load_dwordx4 v[132:135], v250, s[64:65] offset:-3072 sc1\n\t"
      "global_load_dwordx4 v[136:139], v250, s[64:65] offset:-2048 sc1\n\t"
      "global_load_dwordx4 v[140:143], v250, s[64:65] offset:-1024 sc1\n\t"
      "global_load_dwordx4 v[144:147], v250, s[64:65] sc1\n\t"
      "global_load_dwordx4 v[148:151], v250, s[64:65] offset:1024 sc1\n\t"
      "global_load_dwordx4 v[152:155], v250, s[64:65] offset:2048 sc1\n\t"
      "global_load_dwordx4 v[156:159], v250, s[64:65] offset:3072 sc1\n\t"
      "s_waitcnt vmcnt(0)\n\t"
      "v_max3_u32 v251, v128, v129, v130\n\t"
      "v_max3_u32 v251, v251, v131, v132\n\t"
      "v_max3_u32 v251, v251, v133, v134\n\t"
      "v_max3_u32 v251, v251, v135, v136\n\t"
      "v_max3_u32 v251, v251, v137, v138\n\t"
      "v_max3_u32 v251, v251, v139, v140\n\t"
      "v_max3_u32 v251, v251, v141, v142\n\t"
      "v_max3_u32 v251, v251, v143, v144\n\t"
      "v_max3_u32 v251, v251, v145, v146\n\t"
      "v_max3_u32 v251, v251, v147, v148\n\t"
      "v_max3_u32 v251, v251, v149, v150\n\t"
      "v_max3_u32 v251, v251, v151, v152\n\t"
      "v_max3_u32 v251, v251, v153, v154\n\t"
      "v_max3_u32 v251, v251, v155, v156\n\t"
      "v_max3_u32 v251, v251, v157, v158\n\t"
      "v_max_u32_e32 v251, v251, v159\n\t"
      "v_cmp_eq_u32_e32 vcc, -1, v251\n\t"
      "s_cbranch_vccnz LA30L_%=\n\t"
      "LA30X_%=:\n\t"
      "s_branch LA30B_%=\n\t"
      "LF6R_%=:\n\t"
      "s_mov_b32 s80, 0\n\t"
      "LF6L_%=:\n\t"
      "s_add_u32 s80, s80, 1\n\t"
      "s_cmp_lt_u32 s80, 262144\n\t"
      "s_cbranch_scc0 LF6X_%=\n\t"
      "s_sleep 2\n\t"
      "global_load_dwordx2 v[228:229], v230, s[60:61] sc1\n\t"
      "s_waitcnt vmcnt(0)\n\t"
      "v_cmp_ne_u64_e32 vcc, s[22:23], v[228:229]\n\t"
      "s_cbranch_vccnz LF6L_%=\n\t"
      "LF6X_%=:\n\t"
      "s_branch LF6B_%=\n\t"
      "LA31R_%=:\n\t"
      "s_mov_b32 s80, 0\n\t"
      "LA31L_%=:\n\t"
      "s_add_u32 s80, s80, 1\n\t"
      "s_cmp_lt_u32 s80, 262144\n\t"
      "s_cbranch_scc0 LA31X_%=\n\t"
      "s_waitcnt vmcnt(0)\n\t"
      "s_sleep 1\n\t"
      "global_load_dwordx4 v[160:163], v250, s[66:67] offset:-4096 sc1\n\t"
      "global_load_dwordx4 v[164:167], v250, s[66:67] offset:-3072 sc1\n\t"
      "global_load_dwordx4 v[168:171], v250, s[66:67] offset:-2048 sc1\n\t"
      "global_load_dwordx4 v[172:175], v250, s[66:67] offset:-1024 sc1\n\t"
      "global_load_dwordx4 v[176:179], v250, s[66:67] sc1\n\t"
      "global_load_dwordx4 v[180:183], v250, s[66:67] offset:1024 sc1\n\t"
      "global_load_dwordx4 v[184:187], v250, s[66:67] offset:2048 sc1\n\t"
      "global_load_dwordx4 v[188:191], v250, s[66:67] offset:3072 sc1\n\t"
      "s_waitcnt vmcnt(0)\n\t"
      "v_max3_u32 v251, v160, v161, v162\n\t"
      "v_max3_u32 v251, v251, v163, v164\n\t"
      "v_max3_u32 v251, v251, v165, v166\n\t"
      "v_max3_u32 v251, v251, v167, v168\n\t"
      "v_max3_u32 v251, v251, v169, v170\n\t"
      "v_max3_u32 v251, v251, v171, v172\n\t"
      "v_max3_u32 v251, v251, v173, v174\n\t"
      "v_max3_u32 v251, v251, v175, v176\n\t"
      "v_max3_u32 v251, v251, v177, v178\n\t"
      "v_max3_u32 v251, v251, v179, v180\n\t"
      "v_max3_u32 v251, v251, v181, v182\n\t"
      "v_max3_u32 v251, v251, v183, v184\n\t"
      "v_max3_u32 v251, v251, v185, v186\n\t"
      "v_max3_u32 v251, v251, v187, v188\n\t"
      "v_max3_u32 v251, v251, v189, v190\n\t"
      "v_max_u32_e32 v251, v251, v191\n\t"
      "v_cmp_eq_u32_e32 vcc, -1, v251\n\t"
      "s_cbranch_vccnz LA31L_%=\n\t"
      "LA31X_%=:\n\t"
      "v_mfma_f32_16x16x32_bf16 v[224:227], v[0:3], v[160:163], 0\n\t"
      "v_mfma_f32_16x16x32_bf16 v[224:227], v[16:19], v[164:167], v[224:227]\n\t"
      "v_mfma_f32_16x16x32_bf16 v[224:227], v[32:35], v[168:171], v[224:227]\n\t"
      "v_mfma_f32_16x16x32_bf16 v[224:227], v[48:51], v[172:175], v[224:227]\n\t"
      "v_mfma_f32_16x16x32_bf16 v[224:227], v[64:67], v[176:179], v[224:227]\n\t"
      "v_mfma_f32_16x16x32_bf16 v[224:227], v[80:83], v[180:183], v[224:227]\n\t"
      "v_mfma_f32_16x16x32_bf16 v[224:227], v[96:99], v[184:187], v[224:227]\n\t"
      "v_mfma_f32_16x16x32_bf16 v[224:227], v[112:115], v[188:191], v[224:227]\n\t"
      "v_mfma_f32_16x16x32_bf16 v[208:211], v[4:7], v[160:163], 0\n\t"
      "v_mfma_f32_16x16x32_bf16 v[208:211], v[20:23], v[164:167], v[208:211]\n\t"
      "v_mfma_f32_16x16x32_bf16 v[208:211], v[36:39], v[168:171], v[208:211]\n\t"
      "s_nop 7\n\t"
      "s_branch LA31B_%=\n\t"
      "LA32R_%=:\n\t"
      "s_mov_b32 s80, 0\n\t"
      "LA32L_%=:\n\t"
      "s_add_u32 s80, s80, 1\n\t"
      "s_cmp_lt_u32 s80, 262144\n\t"
      "s_cbranch_scc0 LA32X_%=\n\t"
      "s_waitcnt vmcnt(0)\n\t"
      "s_sleep 1\n\t"
      "global_load_dwordx4 v[192:195], v250, s[68:69] offset:-4096 sc1\n\t"
      "global_load_dwordx4 v[196:199], v250, s[68:69] offset:-3072 sc1\n\t"
      "global_load_dwordx4 v[200:203], v250, s[68:69] offset:-2048 sc1\n\t"
      "global_load_dwordx4 v[204:207], v250, s[68:69] offset:-1024 sc1\n\t"
      "global_load_dwordx4 v[128:131], v250, s[68:69] sc1\n\t"
      "global_load_dwordx4 v[132:135], v250, s[68:69] offset:1024 sc1\n\t"
      "global_load_dwordx4 v[136:139], v250, s[68:69] offset:2048 sc1\n\t"
      "global_load_dwordx4 v[140:143], v250, s[68:69] offset:3072 sc1\n\t"
      "s_waitcnt vmcnt(0)\n\t"
      "v_max3_u32 v251, v192, v193, v194\n\t"
      "v_max3_u32 v251, v251, v195, v196\n\t"
      "v_max3_u32 v251, v251, v197, v198\n\t"
      "v_max3_u32 v251, v251, v199, v200\n\t"
      "v_max3_u32 v251, v251, v201, v202\n\t"
      "v_max3_u32 v251, v251, v203, v204\n\t"
      "v_max3_u32 v251, v251, v205, v206\n\t"
      "v_max3_u32 v251, v251, v207, v128\n\t"
      "v_max3_u32 v251, v251, v129, v130\n\t"
      "v_max3_u32 v251, v251, v131, v132\n\t"
      "v_max3_u32 v251, v251, v133, v134\n\t"
      "v_max3_u32 v251, v251, v135, v136\n\t"
      "v_max3_u32 v251, v251, v137, v138\n\t"
      "v_max3_u32 v251, v251, v139, v140\n\t"
      "v_max3_u32 v251, v251, v141, v142\n\t"
      "v_max_u32_e32 v251, v251, v143\n\t"
      "v_cmp_eq_u32_e32 vcc, -1, v251\n\t"
      "s_cbranch_vccnz LA32L_%=\n\t"
      "LA32X_%=:\n\t"
      "v_mfma_f32_16x16x32_bf16 v[220:223], v[0:3], v[192:195], 0\n\t"
      "v_mfma_f32_16x16x32_bf16 v[220:223], v[16:19], v[196:199], v[220:223]\n\t"
      "v_mfma_f32_16x16x32_bf16 v[220:223], v[32:35], v[200:203], v[220:223]\n\t"
      "v_mfma_f32_16x16x32_bf16 v[220:223], v[48:51], v[204:207], v[220:223]\n\t"
      "v_mfma_f32_16x16x32_bf16 v[220:223], v[64:67], v[128:131], v[220:223]\n\t"
      "v_mfma_f32_16x16x32_bf16 v[220:223], v[80:83], v[132:135], v[220:223]\n\t"
      "v_mfma_f32_16x16x32_bf16 v[220:223], v[96:99], v[136:139], v[220:223]\n\t"
      "v_mfma_f32_16x16x32_bf16 v[220:223], v[112:115], v[140:143], v[220:223]\n\t"
      "v_mfma_f32_16x16x32_bf16 v[224:227], v[4:7], v[192:195], 0\n\t"
      "v_mfma_f32_16x16x32_bf16 v[224:227], v[20:23], v[196:199], v[224:227]\n\t"
      "v_mfma_f32_16x16x32_bf16 v[224:227], v[36:39], v[200:203], v[224:227]\n\t"
      "s_nop 7\n\t"
      "s_branch LA32B_%=\n\t"
      "LA33R_%=:\n\t"
      "s_mov_b32 s80, 0\n\t"
      "LA33L_%=:\n\t"
      "s_add_u32 s80, s80, 1\n\t"
      "s_cmp_lt_u32 s80, 262144\n\t"
      "s_cbranch_scc0 LA33X_%=\n\t"
      "s_waitcnt vmcnt(0)\n\t"
      "s_sleep 1\n\t"
      "global_load_dwordx4 v[144:147], v250, s[70:71] offset:-4096 sc1\n\t"
      "global_load_dwordx4 v[148:151], v250, s[70:71] offset:-3072 sc1\n\t"
      "global_load_dwordx4 v[152:155], v250, s[70:71] offset:-2048 sc1\n\t"
      "global_load_dwordx4 v[156:159], v250, s[70:71] offset:-1024 sc1\n\t"
      "global_load_dwordx4 v[160:163], v250, s[70:71] sc1\n\t"
      "global_load_dwordx4 v[164:167], v250, s[70:71] offset:1024 sc1\n\t"
      "global_load_dwordx4 v[168:171], v250, s[70:71] offset:2048 sc1\n\t"
      "global_load_dwordx4 v[172:175], v250, s[70:71] offset:3072 sc1\n\t"
      "s_waitcnt vmcnt(0)\n\t"
      "v_max3_u32 v251, v144, v145, v146\n\t"
      "v_max3_u32 v251, v251, v147, v148\n\t"
      "v_max3_u32 v251, v251, v149, v150\n\t"
      "v_max3_u32 v251, v251, v151, v152\n\t"
      "v_max3_u32 v251, v251, v153, v154\n\t"
      "v_max3_u32 v251, v251, v155, v156\n\t"
      "v_max3_u32 v251, v251, v157, v158\n\t"
      "v_max3_u32 v251, v251, v159, v160\n\t"
      "v_max3_u32 v251, v251, v161, v162\n\t"
      "v_max3_u32 v251, v251, v163, v164\n\t"
      "v_max3_u32 v251, v251, v165, v166\n\t"
      "v_max3_u32 v251, v251, v167, v168\n\t"
      "v_max3_u32 v251, v251, v169, v170\n\t"
      "v_max3_u32 v251, v251, v171, v172\n\t"
      "v_max3_u32 v251, v251, v173, v174\n\t"
      "v_max_u32_e32 v251, v251, v175\n\t"
      "v_cmp_eq_u32_e32 vcc, -1, v251\n\t"
      "s_cbranch_vccnz LA33L_%=\n\t"
      "LA33X_%=:\n\t"
      "v_mfma_f32_16x16x32_bf16 v[216:219], v[0:3], v[144:147], 0\n\t"
      "v_mfma_f32_16x16x32_bf16 v[216:219], v[16:19], v[148:151], v[216:219]\n\t"
      "v_mfma_f32_16x16x32_bf16 v[216:219], v[32:35], v[152:155], v[216:219]\n\t"
      "v_mfma_f32_16x16x32_bf16 v[216:219], v[48:51], v[156:159], v[216:219]\n\t"
      "v_mfma_f32_16x16x32_bf16 v[216:219], v[64:67], v[160:163], v[216:219]\n\t"
      "v_mfma_f32_16x16x32_bf16 v[216:219], v[80:83], v[164:167], v[216:219]\n\t"
      "v_mfma_f32_16x16x32_bf16 v[216:219], v[96:99], v[168:171], v[216:219]\n\t"
      "v_mfma_f32_16x16x32_bf16 v[216:219], v[112:115], v[172:175], v[216:219]\n\t"
      "v_mfma_f32_16x16x32_bf16 v[220:223], v[4:7], v[144:147], 0\n\t"
      "v_mfma_f32_16x16x32_bf16 v[220:223], v[20:23], v[148:151], v[220:223]\n\t"
      "v_mfma_f32_16x16x32_bf16 v[220:223], v[36:39], v[152:155], v[220:223]\n\t"
      "s_nop 7\n\t"
      "s_branch LA33B_%=\n\t"
      "LA34R_%=:\n\t"
      "s_mov_b32 s80, 0\n\t"
      "LA34L_%=:\n\t"
      "s_add_u32 s80, s80, 1\n\t"
      "s_cmp_lt_u32 s80, 262144\n\t"
      "s_cbranch_scc0 LA34X_%=\n\t"
      "s_waitcnt vmcnt(0)\n\t"
      "s_sleep 1\n\t"
      "global_load_dwordx4 v[176:179], v250, s[72:73] offset:-4096 sc1\n\t"
      "global_load_dwordx4 v[180:183], v250, s[72:73] offset:-3072 sc1\n\t"
      "global_load_dwordx4 v[184:187], v250, s[72:73] offset:-2048 sc1\n\t"
      "global_load_dwordx4 v[188:191], v250, s[72:73] offset:-1024 sc1\n\t"
      "global_load_dwordx4 v[192:195], v250, s[72:73] sc1\n\t"
      "global_load_dwordx4 v[196:199], v250, s[72:73] offset:1024 sc1\n\t"
      "global_load_dwordx4 v[200:203], v250, s[72:73] offset:2048 sc1\n\t"
      "global_load_dwordx4 v[204:207], v250, s[72:73] offset:3072 sc1\n\t"
      "s_waitcnt vmcnt(0)\n\t"
      "v_max3_u32 v251, v176, v177, v178\n\t"
      "v_max3_u32 v251, v251, v179, v180\n\t"
      "v_max3_u32 v251, v251, v181, v182\n\t"
      "v_max3_u32 v251, v251, v183, v184\n\t"
      "v_max3_u32 v251, v251, v185, v186\n\t"
      "v_max3_u32 v251, v251, v187, v188\n\t"
      "v_max3_u32 v251, v251, v189, v190\n\t"
      "v_max3_u32 v251, v251, v191, v192\n\t"
      "v_max3_u32 v251, v251, v193, v194\n\t"
      "v_max3_u32 v251, v251, v195, v196\n\t"
      "v_max3_u32 v251, v251, v197, v198\n\t"
      "v_max3_u32 v251, v251, v199, v200\n\t"
      "v_max3_u32 v251, v251, v201, v202\n\t"
      "v_max3_u32 v251, v251, v203, v204\n\t"
      "v_max3_u32 v251, v251, v205, v206\n\t"
      "v_max_u32_e32 v251, v251, v207\n\t"
      "v_cmp_eq_u32_e32 vcc, -1, v251\n\t"
      "s_cbranch_vccnz LA34L_%=\n\t"
      "LA34X_%=:\n\t"
      "v_mfma_f32_16x16x32_bf16 v[212:215], v[0:3], v[176:179], 0\n\t"
      "v_mfma_f32_16x16x32_bf16 v[212:215], v[16:19], v[180:183], v[212:215]\n\t"
      "v_mfma_f32_16x16x32_bf16 v[212:215], v[32:35], v[184:187], v[212:215]\n\t"
      "v_mfma_f32_16x16x32_bf16 v[212:215], v[48:51], v[188:191], v[212:215]\n\t"
      "v_mfma_f32_16x16x32_bf16 v[212:215], v[64:67], v[192:195], v[212:215]\n\t"
      "v_mfma_f32_16x16x32_bf16 v[212:215], v[80:83], v[196:199], v[212:215]\n\t"
      "v_mfma_f32_16x16x32_bf16 v[212:215], v[96:99], v[200:203], v[212:215]\n\t"
      "v_mfma_f32_16x16x32_bf16 v[212:215], v[112:115], v[204:207], v[212:215]\n\t"
      "v_mfma_f32_16x16x32_bf16 v[216:219], v[4:7], v[176:179], 0\n\t"
      "v_mfma_f32_16x16x32_bf16 v[216:219], v[20:23], v[180:183], v[216:219]\n\t"
      "v_mfma_f32_16x16x32_bf16 v[216:219], v[36:39], v[184:187], v[216:219]\n\t"
      "s_nop 7\n\t"
      "s_branch LA34B_%=\n\t"
      "LA35R_%=:\n\t"
      "s_mov_b32 s80, 0\n\t"
      "LA35L_%=:\n\t"
      "s_add_u32 s80, s80, 1\n\t"
      "s_cmp_lt_u32 s80, 262144\n\t"
      "s_cbranch_scc0 LA35X_%=\n\t"
      "s_waitcnt vmcnt(0)\n\t"
      "s_sleep 1\n\t"
      "global_load_dwordx4 v[128:131], v250, s[74:75] offset:-4096 sc1\n\t"
      "global_load_dwordx4 v[132:135], v250, s[74:75] offset:-3072 sc1\n\t"
      "global_load_dwordx4 v[136:139], v250, s[74:75] offset:-2048 sc1\n\t"
      "global_load_dwordx4 v[140:143], v250, s[74:75] offset:-1024 sc1\n\t"
      "global_load_dwordx4 v[144:147], v250, s[74:75] sc1\n\t"
      "global_load_dwordx4 v[148:151], v250, s[74:75] offset:1024 sc1\n\t"
      "global_load_dwordx4 v[152:155], v250, s[74:75] offset:2048 sc1\n\t"
      "global_load_dwordx4 v[156:159], v250, s[74:75] offset:3072 sc1\n\t"
      "s_waitcnt vmcnt(0)\n\t"
      "v_max3_u32 v251, v128, v129, v130\n\t"
      "v_max3_u32 v251, v251, v131, v132\n\t"
      "v_max3_u32 v251, v251, v133, v134\n\t"
      "v_max3_u32 v251, v251, v135, v136\n\t"
      "v_max3_u32 v251, v251, v137, v138\n\t"
      "v_max3_u32 v251, v251, v139, v140\n\t"
      "v_max3_u32 v251, v251, v141, v142\n\t"
      "v_max3_u32 v251, v251, v143, v144\n\t"
      "v_max3_u32 v251, v251, v145, v146\n\t"
      "v_max3_u32 v251, v251, v147, v148\n\t"
      "v_max3_u32 v251, v251, v149, v150\n\t"
      "v_max3_u32 v251, v251, v151, v152\n\t"
      "v_max3_u32 v251, v251, v153, v154\n\t"
      "v_max3_u32 v251, v251, v155, v156\n\t"
      "v_max3_u32 v251, v251, v157, v158\n\t"
      "v_max_u32_e32 v251, v251, v159\n\t"
      "v_cmp_eq_u32_e32 vcc, -1, v251\n\t"
      "s_cbranch_vccnz LA35L_%=\n\t"
      "LA35X_%=:\n\t"
      "v_mfma_f32_16x16x32_bf16 v[208:211], v[0:3], v[128:131], 0\n\t"
      "v_mfma_f32_16x16x32_bf16 v[208:211], v[16:19], v[132:135], v[208:211]\n\t"
      "v_mfma_f32_16x16x32_bf16 v[208:211], v[32:35], v[136:139], v[208:211]\n\t"
      "v_mfma_f32_16x16x32_bf16 v[208:211], v[48:51], v[140:143], v[208:211]\n\t"
      "v_mfma_f32_16x16x32_bf16 v[208:211], v[64:67], v[144:147], v[208:211]\n\t"
      "v_mfma_f32_16x16x32_bf16 v[208:211], v[80:83], v[148:151], v[208:211]\n\t"
      "v_mfma_f32_16x16x32_bf16 v[208:211], v[96:99], v[152:155], v[208:211]\n\t"
      "v_mfma_f32_16x16x32_bf16 v[208:211], v[112:115], v[156:159], v[208:211]\n\t"
      "v_mfma_f32_16x16x32_bf16 v[212:215], v[4:7], v[128:131], 0\n\t"
      "v_mfma_f32_16x16x32_bf16 v[212:215], v[20:23], v[132:135], v[212:215]\n\t"
      "v_mfma_f32_16x16x32_bf16 v[212:215], v[36:39], v[136:139], v[212:215]\n\t"
      "s_nop 7\n\t"
      "s_branch LA35B_%=\n\t"
      "LA36R_%=:\n\t"
      "s_mov_b32 s80, 0\n\t"
      "LA36L_%=:\n\t"
      "s_add_u32 s80, s80, 1\n\t"
      "s_cmp_lt_u32 s80, 262144\n\t"
      "s_cbranch_scc0 LA36X_%=\n\t"
      "s_waitcnt vmcnt(0)\n\t"
      "s_sleep 1\n\t"
      "global_load_dwordx4 v[160:163], v250, s[76:77] offset:-4096 sc1\n\t"
      "global_load_dwordx4 v[164:167], v250, s[76:77] offset:-3072 sc1\n\t"
      "global_load_dwordx4 v[168:171], v250, s[76:77] offset:-2048 sc1\n\t"
      "global_load_dwordx4 v[172:175], v250, s[76:77] offset:-1024 sc1\n\t"
      "global_load_dwordx4 v[176:179], v250, s[76:77] sc1\n\t"
      "global_load_dwordx4 v[180:183], v250, s[76:77] offset:1024 sc1\n\t"
      "global_load_dwordx4 v[184:187], v250, s[76:77] offset:2048 sc1\n\t"
      "global_load_dwordx4 v[188:191], v250, s[76:77] offset:3072 sc1\n\t"
      "s_waitcnt vmcnt(0)\n\t"
      "v_max3_u32 v251, v160, v161, v162\n\t"
      "v_max3_u32 v251, v251, v163, v164\n\t"
      "v_max3_u32 v251, v251, v165, v166\n\t"
      "v_max3_u32 v251, v251, v167, v168\n\t"
      "v_max3_u32 v251, v251, v169, v170\n\t"
      "v_max3_u32 v251, v251, v171, v172\n\t"
      "v_max3_u32 v251, v251, v173, v174\n\t"
      "v_max3_u32 v251, v251, v175, v176\n\t"
      "v_max3_u32 v251, v251, v177, v178\n\t"
      "v_max3_u32 v251, v251, v179, v180\n\t"
      "v_max3_u32 v251, v251, v181, v182\n\t"
      "v_max3_u32 v251, v251, v183, v184\n\t"
      "v_max3_u32 v251, v251, v185, v186\n\t"
      "v_max3_u32 v251, v251, v187, v188\n\t"
      "v_max3_u32 v251, v251, v189, v190\n\t"
      "v_max_u32_e32 v251, v251, v191\n\t"
      "v_cmp_eq_u32_e32 vcc, -1, v251\n\t"
      "s_cbranch_vccnz LA36L_%=\n\t"
      "LA36X_%=:\n\t"
      "v_mfma_f32_16x16x32_bf16 v[224:227], v[0:3], v[160:163], 0\n\t"
      "v_mfma_f32_16x16x32_bf16 v[224:227], v[16:19], v[164:167], v[224:227]\n\t"
      "v_mfma_f32_16x16x32_bf16 v[224:227], v[32:35], v[168:171], v[224:227]\n\t"
      "v_mfma_f32_16x16x32_bf16 v[224:227], v[48:51], v[172:175], v[224:227]\n\t"
      "v_mfma_f32_16x16x32_bf16 v[224:227], v[64:67], v[176:179], v[224:227]\n\t"
      "v_mfma_f32_16x16x32_bf16 v[224:227], v[80:83], v[180:183], v[224:227]\n\t"
      "v_mfma_f32_16x16x32_bf16 v[224:227], v[96:99], v[184:187], v[224:227]\n\t"
      "v_mfma_f32_16x16x32_bf16 v[224:227], v[112:115], v[188:191], v[224:227]\n\t"
      "v_mfma_f32_16x16x32_bf16 v[208:211], v[4:7], v[160:163], 0\n\t"
      "v_mfma_f32_16x16x32_bf16 v[208:211], v[20:23], v[164:167], v[208:211]\n\t"
      "v_mfma_f32_16x16x32_bf16 v[208:211], v[36:39], v[168:171], v[208:211]\n\t"
      "s_nop 7\n\t"
      "s_branch LA36B_%=\n\t"
      "LF5R_%=:\n\t"
      "s_mov_b32 s80, 0\n\t"
      "LF5L_%=:\n\t"
      "s_add_u32 s80, s80, 1\n\t"
      "s_cmp_lt_u32 s80, 262144\n\t"
      "s_cbranch_scc0 LF5X_%=\n\t"
      "s_sleep 2\n\t"
      "global_load_dwordx2 v[138:139], v143, s[60:61] sc1\n\t"
      "s_waitcnt vmcnt(0)\n\t"
      "v_cmp_ne_u64_e32 vcc, s[22:23], v[138:139]\n\t"
      "s_cbranch_vccnz LF5L_%=\n\t"
      "LF5X_%=:\n\t"
      "s_branch LF5B_%=\n\t"
      "PG0R_%=:\n\t"
      "s_mov_b32 s80, 0\n\t"
      "PG0L_%=:\n\t"
      "s_add_u32 s80, s80, 1\n\t"
      "s_cmp_lt_u32 s80, 262144\n\t"
      "s_cbranch_scc0 PG0X_%=\n\t"
      "s_sleep 2\n\t"
      "global_load_dword v251, v253, s[66:67] offset:252 sc1\n\t"
      "s_waitcnt vmcnt(0)\n\t"
      "v_cmp_eq_u32_e32 vcc, -1, v251\n\t"
      "s_cbranch_vccnz PG0L_%=\n\t"
      "PG0X_%=:\n\t"
      "s_branch PG0B_%=\n\t"
      "LF8R_%=:\n\t"
      "s_mov_b32 s80, 0\n\t"
      "LF8L_%=:\n\t"
      "s_add_u32 s80, s80, 1\n\t"
      "s_cmp_lt_u32 s80, 262144\n\t"
      "s_cbranch_scc0 LF8X_%=\n\t"
      "s_sleep 2\n\t"
      "global_load_dwordx2 v[200:201], v202, s[60:61] sc1\n\t"
      "s_waitcnt vmcnt(0)\n\t"
      "v_cmp_ne_u64_e32 vcc, s[22:23], v[200:201]\n\t"
      "s_cbranch_vccnz LF8L_%=\n\t"
      "LF8X_%=:\n\t"
      "s_branch LF8B_%=\n\t"
      "PG1R_%=:\n\t"
      "s_mov_b32 s80, 0\n\t"
      "PG1L_%=:\n\t"
      "s_add_u32 s80, s80, 1\n\t"
      "s_cmp_lt_u32 s80, 262144\n\t"
      "s_cbranch_scc0 PG1X_%=\n\t"
      "s_sleep 2\n\t"
      "global_load_dword v251, v253, s[66:67] offset:252 sc1\n\t"
      "s_waitcnt vmcnt(0)\n\t"
      "v_cmp_eq_u32_e32 vcc, -1, v251\n\t"
      "s_cbranch_vccnz PG1L_%=\n\t"
      "PG1X_%=:\n\t"
      "s_branch PG1B_%=\n\t"
      "LA40R_%=:\n\t"
      "s_mov_b32 s80, 0\n\t"
      "LA40L_%=:\n\t"
      "s_add_u32 s80, s80, 1\n\t"
      "s_cmp_lt_u32 s80, 262144\n\t"
      "s_cbranch_scc0 LA40X_%=\n\t"
      "s_waitcnt vmcnt(0)\n\t"
      "PQ0R_%=:\n\t"
      "PQ0L_%=:\n\t"
      "s_add_u32 s80, s80, 1\n\t"
      "s_cmp_lt_u32 s80, 262144\n\t"
      "s_cbranch_scc0 PQ0X_%=\n\t"
      "s_sleep 1\n\t"
      "global_load_dword v251, v252, s[64:65] offset:252 sc1\n\t"
      "s_waitcnt vmcnt(0)\n\t"
      "v_cmp_eq_u32_e32 vcc, -1, v251\n\t"
      "s_cbranch_vccnz PQ0L_%=\n\t"
      "PQ0X_%=:\n\t"
      "s_add_u32 s58, s64, s40\n\t"
      "s_addc_u32 s59, s65, 0\n\t"
      "global_load_dwordx4 v[128:131], v244, s[58:59] sc1\n\t"
      "s_add_u32 s58, s64, s41\n\t"
      "s_addc_u32 s59, s65, 0\n\t"
      "global_load_dwordx4 v[132:135], v244, s[58:59] sc1\n\t"
      "s_add_u32 s58, s64, s42\n\t"
      "s_addc_u32 s59, s65, 0\n\t"
      "global_load_dwordx4 v[136:139], v244, s[58:59] sc1\n\t"
      "s_add_u32 s58, s64, s43\n\t"
      "s_addc_u32 s59, s65, 0\n\t"
      "global_load_dwordx4 v[140:143], v244, s[58:59] sc1\n\t"
      "s_add_u32 s58, s64, s44\n\t"
      "s_addc_u32 s59, s65, 0\n\t"
      "global_load_dwordx4 v[144:147], v244, s[58:59] sc1\n\t"
      "s_add_u32 s58, s64, s45\n\t"
      "s_addc_u32 s59, s65, 0\n\t"
      "global_load_dwordx4 v[148:151], v244, s[58:59] sc1\n\t"
      "s_add_u32 s58, s64, s46\n\t"
      "s_addc_u32 s59, s65, 0\n\t"
      "global_load_dwordx4 v[152:155], v244, s[58:59] sc1\n\t"
      "s_waitcnt vmcnt(0)\n\t"
      "v_max3_u32 v251, v128, v129, v130\n\t"
      "v_max3_u32 v251, v251, v131, v132\n\t"
      "v_max3_u32 v251, v251, v133, v134\n\t"
      "v_max3_u32 v251, v251, v135, v136\n\t"
      "v_max3_u32 v251, v251, v137, v138\n\t"
      "v_max3_u32 v251, v251, v139, v140\n\t"
      "v_max3_u32 v251, v251, v141, v142\n\t"
      "v_max3_u32 v251, v251, v143, v144\n\t"
      "v_max3_u32 v251, v251, v145, v146\n\t"
      "v_max3_u32 v251, v251, v147, v148\n\t"
      "v_max3_u32 v251, v251, v149, v150\n\t"
      "v_max3_u32 v251, v251, v151, v152\n\t"
      "v_max3_u32 v251, v251, v153, v154\n\t"
      "v_max_u32_e32 v251, v251, v155\n\t"
      "v_cmp_eq_u32_e32 vcc, -1, v251\n\t"
      "s_cbranch_vccnz LA40L_%=\n\t"
      "LA40X_%=:\n\t"
      "v_mfma_f32_16x16x32_bf16 v[208:211], v[128:131], v[0:3], 0\n\t"
      "v_mfma_f32_16x16x32_bf16 v[208:211], v[132:135], v[16:19], v[208:211]\n\t"
      "v_mfma_f32_16x16x32_bf16 v[208:211], v[136:139], v[32:35], v[208:211]\n\t"
      "v_mfma_f32_16x16x32_bf16 v[208:211], v[140:143], v[48:51], v[208:211]\n\t"
      "v_mfma_f32_16x16x32_bf16 v[208:211], v[144:147], v[64:67], v[208:211]\n\t"
      "v_mfma_f32_16x16x32_bf16 v[208:211], v[148:151], v[80:83], v[208:211]\n\t"
      "v_mfma_f32_16x16x32_bf16 v[208:211], v[152:155], v[96:99], v[208:211]\n\t"
      "v_mfma_f32_16x16x32_bf16 v[212:215], v[128:131], v[4:7], 0\n\t"
      "v_mfma_f32_16x16x32_bf16 v[212:215], v[132:135], v[20:23], v[212:215]\n\t"
      "v_mfma_f32_16x16x32_bf16 v[212:215], v[136:139], v[36:39], v[212:215]\n\t"
      "s_nop 7\n\t"
      "s_branch LA40B_%=\n\t"
      "LA41R_%=:\n\t"
      "s_mov_b32 s80, 0\n\t"
      "LA41L_%=:\n\t"
      "s_add_u32 s80, s80, 1\n\t"
      "s_cmp_lt_u32 s80, 262144\n\t"
      "s_cbranch_scc0 LA41X_%=\n\t"
      "s_waitcnt vmcnt(0)\n\t"
      "PQ1R_%=:\n\t"
      "PQ1L_%=:\n\t"
      "s_add_u32 s80, s80, 1\n\t"
      "s_cmp_lt_u32 s80, 262144\n\t"
      "s_cbranch_scc0 PQ1X_%=\n\t"
      "s_sleep 1\n\t"
      "global_load_dword v251, v252, s[66:67] offset:252 sc1\n\t"
      "s_waitcnt vmcnt(0)\n\t"
      "v_cmp_eq_u32_e32 vcc, -1, v251\n\t"
      "s_cbranch_vccnz PQ1L_%=\n\t"
      "PQ1X_%=:\n\t"
      "s_add_u32 s58, s66, s40\n\t"
      "s_addc_u32 s59, s67, 0\n\t"
      "global_load_dwordx4 v[156:159], v244, s[58:59] sc1\n\t"
      "s_add_u32 s58, s66, s41\n\t"
      "s_addc_u32 s59, s67, 0\n\t"
      "global_load_dwordx4 v[160:163], v244, s[58:59] sc1\n\t"
      "s_add_u32 s58, s66, s42\n\t"
      "s_addc_u32 s59, s67, 0\n\t"
      "global_load_dwordx4 v[164:167], v244, s[58:59] sc1\n\t"
      "s_add_u32 s58, s66, s43\n\t"
      "s_addc_u32 s59, s67, 0\n\t"
      "global_load_dwordx4 v[168:171], v244, s[58:59] sc1\n\t"
      "s_add_u32 s58, s66, s44\n\t"
      "s_addc_u32 s59, s67, 0\n\t"
      "global_load_dwordx4 v[172:175], v244, s[58:59] sc1\n\t"
      "s_add_u32 s58, s66, s45\n\t"
      "s_addc_u32 s59, s67, 0\n\t"
      "global_load_dwordx4 v[176:179], v244, s[58:59] sc1\n\t"
      "s_add_u32 s58, s66, s46\n\t"
      "s_addc_u32 s59, s67, 0\n\t"
      "global_load_dwordx4 v[180:183], v244, s[58:59] sc1\n\t"
      "s_waitcnt vmcnt(0)\n\t"
      "v_max3_u32 v251, v156, v157, v158\n\t"
      "v_max3_u32 v251, v251, v159, v160\n\t"
      "v_max3_u32 v251, v251, v161, v162\n\t"
      "v_max3_u32 v251, v251, v163, v164\n\t"
      "v_max3_u32 v251, v251, v165, v166\n\t"
      "v_max3_u32 v251, v251, v167, v168\n\t"
      "v_max3_u32 v251, v251, v169, v170\n\t"
      "v_max3_u32 v251, v251, v171, v172\n\t"
      "v_max3_u32 v251, v251, v173, v174\n\t"
      "v_max3_u32 v251, v251, v175, v176\n\t"
      "v_max3_u32 v251, v251, v177, v178\n\t"
      "v_max3_u32 v251, v251, v179, v180\n\t"
      "v_max3_u32 v251, v251, v181, v182\n\t"
      "v_max_u32_e32 v251, v251, v183\n\t"
      "v_cmp_eq_u32_e32 vcc, -1, v251\n\t"
      "s_cbranch_vccnz LA41L_%=\n\t"
      "LA41X_%=:\n\t"
      "v_mfma_f32_16x16x32_bf16 v[224:227], v[156:159], v[0:3], 0\n\t"
      "v_mfma_f32_16x16x32_bf16 v[224:227], v[160:163], v[16:19], v[224:227]\n\t"
      "v_mfma_f32_16x16x32_bf16 v[224:227], v[164:167], v[32:35], v[224:227]\n\t"
      "v_mfma_f32_16x16x32_bf16 v[224:227], v[168:171], v[48:51], v[224:227]\n\t"
      "v_mfma_f32_16x16x32_bf16 v[224:227], v[172:175], v[64:67], v[224:227]\n\t"
      "v_mfma_f32_16x16x32_bf16 v[224:227], v[176:179], v[80:83], v[224:227]\n\t"
      "v_mfma_f32_16x16x32_bf16 v[224:227], v[180:183], v[96:99], v[224:227]\n\t"
      "v_mfma_f32_16x16x32_bf16 v[208:211], v[156:159], v[4:7], 0\n\t"
      "v_mfma_f32_16x16x32_bf16 v[208:211], v[160:163], v[20:23], v[208:211]\n\t"
      "v_mfma_f32_16x16x32_bf16 v[208:211], v[164:167], v[36:39], v[208:211]\n\t"
      "s_nop 7\n\t"
      "s_branch LA41B_%=\n\t"
      "LA42R_%=:\n\t"
      "s_mov_b32 s80, 0\n\t"
      "LA42L_%=:\n\t"
      "s_add_u32 s80, s80, 1\n\t"
      "s_cmp_lt_u32 s80, 262144\n\t"
      "s_cbranch_scc0 LA42X_%=\n\t"
      "s_waitcnt vmcnt(0)\n\t"
      "PQ2R_%=:\n\t"
      "PQ2L_%=:\n\t"
      "s_add_u32 s80, s80, 1\n\t"
      "s_cmp_lt_u32 s80, 262144\n\t"
      "s_cbranch_scc0 PQ2X_%=\n\t"
      "s_sleep 1\n\t"
      "global_load_dword v251, v252, s[68:69] offset:252 sc1\n\t"
      "s_waitcnt vmcnt(0)\n\t"
      "v_cmp_eq_u32_e32 vcc, -1, v251\n\t"
      "s_cbranch_vccnz PQ2L_%=\n\t"
      "PQ2X_%=:\n\t"
      "s_add_u32 s58, s68, s40\n\t"
      "s_addc_u32 s59, s69, 0\n\t"
      "global_load_dwordx4 v[184:187], v244, s[58:59] sc1\n\t"
      "s_add_u32 s58, s68, s41\n\t"
      "s_addc_u32 s59, s69, 0\n\t"
      "global_load_dwordx4 v[188:191], v244, s[58:59] sc1\n\t"
      "s_add_u32 s58, s68, s42\n\t"
      "s_addc_u32 s59, s69, 0\n\t"
      "global_load_dwordx4 v[192:195], v244, s[58:59] sc1\n\t"
      "s_add_u32 s58, s68, s43\n\t"
      "s_addc_u32 s59, s69, 0\n\t"
      "global_load_dwordx4 v[196:199], v244, s[58:59] sc1\n\t"
      "s_add_u32 s58, s68, s44\n\t"
      "s_addc_u32 s59, s69, 0\n\t"
      "global_load_dwordx4 v[200:203], v244, s[58:59] sc1\n\t"
      "s_add_u32 s58, s68, s45\n\t"
      "s_addc_u32 s59, s69, 0\n\t"
      "global_load_dwordx4 v[204:207], v244, s[58:59] sc1\n\t"
      "s_add_u32 s58, s68, s46\n\t"
      "s_addc_u32 s59, s69, 0\n\t"
      "global_load_dwordx4 v[128:131], v244, s[58:59] sc1\n\t"
      "s_waitcnt vmcnt(0)\n\t"
      "v_max3_u32 v251, v184, v185, v186\n\t"
      "v_max3_u32 v251, v251, v187, v188\n\t"
      "v_max3_u32 v251, v251, v189, v190\n\t"
      "v_max3_u32 v251, v251, v191, v192\n\t"
      "v_max3_u32 v251, v251, v193, v194\n\t"
      "v_max3_u32 v251, v251, v195, v196\n\t"
      "v_max3_u32 v251, v251, v197, v198\n\t"
      "v_max3_u32 v251, v251, v199, v200\n\t"
      "v_max3_u32 v251, v251, v201, v202\n\t"
      "v_max3_u32 v251, v251, v203, v204\n\t"
      "v_max3_u32 v251, v251, v205, v206\n\t"
      "v_max3_u32 v251, v251, v207, v128\n\t"
      "v_max3_u32 v251, v251, v129, v130\n\t"
      "v_max_u32_e32 v251, v251, v131\n\t"
      "v_cmp_eq_u32_e32 vcc, -1, v251\n\t"
      "s_cbranch_vccnz LA42L_%=\n\t"
      "LA42X_%=:\n\t"
      "v_mfma_f32_16x16x32_bf16 v[220:223], v[184:187], v[0:3], 0\n\t"
      "v_mfma_f32_16x16x32_bf16 v[220:223], v[188:191], v[16:19], v[220:223]\n\t"
      "v_mfma_f32_16x16x32_bf16 v[220:223], v[192:195], v[32:35], v[220:223]\n\t"
      "v_mfma_f32_16x16x32_bf16 v[220:223], v[196:199], v[48:51], v[220:223]\n\t"
      "v_mfma_f32_16x16x32_bf16 v[220:223], v[200:203], v[64:67], v[220:223]\n\t"
      "v_mfma_f32_16x16x32_bf16 v[220:223], v[204:207], v[80:83], v[220:223]\n\t"
      "v_mfma_f32_16x16x32_bf16 v[220:223], v[128:131], v[96:99], v[220:223]\n\t"
      "v_mfma_f32_16x16x32_bf16 v[224:227], v[184:187], v[4:7], 0\n\t"
      "v_mfma_f32_16x16x32_bf16 v[224:227], v[188:191], v[20:23], v[224:227]\n\t"
      "v_mfma_f32_16x16x32_bf16 v[224:227], v[192:195], v[36:39], v[224:227]\n\t"
      "s_nop 7\n\t"
      "s_branch LA42B_%=\n\t"
      "LA43R_%=:\n\t"
      "s_mov_b32 s80, 0\n\t"
      "LA43L_%=:\n\t"
      "s_add_u32 s80, s80, 1\n\t"
      "s_cmp_lt_u32 s80, 262144\n\t"
      "s_cbranch_scc0 LA43X_%=\n\t"
      "s_waitcnt vmcnt(0)\n\t"
      "PQ3R_%=:\n\t"
      "PQ3L_%=:\n\t"
      "s_add_u32 s80, s80, 1\n\t"
      "s_cmp_lt_u32 s80, 262144\n\t"
      "s_cbranch_scc0 PQ3X_%=\n\t"
      "s_sleep 1\n\t"
      "global_load_dword v251, v252, s[70:71] offset:252 sc1\n\t"
      "s_waitcnt vmcnt(0)\n\t"
      "v_cmp_eq_u32_e32 vcc, -1, v251\n\t"
      "s_cbranch_vccnz PQ3L_%=\n\t"
      "PQ3X_%=:\n\t"
      "s_add_u32 s58, s70, s40\n\t"
      "s_addc_u32 s59, s71, 0\n\t"
      "global_load_dwordx4 v[132:135], v244, s[58:59] sc1\n\t"
      "s_add_u32 s58, s70, s41\n\t"
      "s_addc_u32 s59, s71, 0\n\t"
      "global_load_dwordx4 v[136:139], v244, s[58:59] sc1\n\t"
      "s_add_u32 s58, s70, s42\n\t"
      "s_addc_u32 s59, s71, 0\n\t"
      "global_load_dwordx4 v[140:143], v244, s[58:59] sc1\n\t"
      "s_add_u32 s58, s70, s43\n\t"
      "s_addc_u32 s59, s71, 0\n\t"
      "global_load_dwordx4 v[144:147], v244, s[58:59] sc1\n\t"
      "s_add_u32 s58, s70, s44\n\t"
      "s_addc_u32 s59, s71, 0\n\t"
      "global_load_dwordx4 v[148:151], v244, s[58:59] sc1\n\t"
      "s_add_u32 s58, s70, s45\n\t"
      "s_addc_u32 s59, s71, 0\n\t"
      "global_load_dwordx4 v[152:155], v244, s[58:59] sc1\n\t"
      "s_add_u32 s58, s70, s46\n\t"
      "s_addc_u32 s59, s71, 0\n\t"
      "global_load_dwordx4 v[156:159], v244, s[58:59] sc1\n\t"
      "s_waitcnt vmcnt(0)\n\t"
      "v_max3_u32 v251, v132, v133, v134\n\t"
      "v_max3_u32 v251, v251, v135, v136\n\t"
      "v_max3_u32 v251, v251, v137, v138\n\t"
      "v_max3_u32 v251, v251, v139, v140\n\t"
      "v_max3_u32 v251, v251, v141, v142\n\t"
      "v_max3_u32 v251, v251, v143, v144\n\t"
      "v_max3_u32 v251, v251, v145, v146\n\t"
      "v_max3_u32 v251, v251, v147, v148\n\t"
      "v_max3_u32 v251, v251, v149, v150\n\t"
      "v_max3_u32 v251, v251, v151, v152\n\t"
      "v_max3_u32 v251, v251, v153, v154\n\t"
      "v_max3_u32 v251, v251, v155, v156\n\t"
      "v_max3_u32 v251, v251, v157, v158\n\t"
      "v_max_u32_e32 v251, v251, v159\n\t"
      "v_cmp_eq_u32_e32 vcc, -1, v251\n\t"
      "s_cbranch_vccnz LA43L_%=\n\t"
      "LA43X_%=:\n\t"
      "v_mfma_f32_16x16x32_bf16 v[216:219], v[132:135], v[0:3], 0\n\t"
      "v_mfma_f32_16x16x32_bf16 v[216:219], v[136:139], v[16:19], v[216:219]\n\t"
      "v_mfma_f32_16x16x32_bf16 v[216:219], v[140:143], v[32:35], v[216:219]\n\t"
      "v_mfma_f32_16x16x32_bf16 v[216:219], v[144:147], v[48:51], v[216:219]\n\t"
      "v_mfma_f32_16x16x32_bf16 v[216:219], v[148:151], v[64:67], v[216:219]\n\t"
      "v_mfma_f32_16x16x32_bf16 v[216:219], v[152:155], v[80:83], v[216:219]\n\t"
      "v_mfma_f32_16x16x32_bf16 v[216:219], v[156:159], v[96:99], v[216:219]\n\t"
      "v_mfma_f32_16x16x32_bf16 v[220:223], v[132:135], v[4:7], 0\n\t"
      "v_mfma_f32_16x16x32_bf16 v[220:223], v[136:139], v[20:23], v[220:223]\n\t"
      "v_mfma_f32_16x16x32_bf16 v[220:223], v[140:143], v[36:39], v[220:223]\n\t"
      "s_nop 7\n\t"
      "s_branch LA43B_%=\n\t"
      "LA44R_%=:\n\t"
      "s_mov_b32 s80, 0\n\t"
      "LA44L_%=:\n\t"
      "s_add_u32 s80, s80, 1\n\t"
      "s_cmp_lt_u32 s80, 262144\n\t"
      "s_cbranch_scc0 LA44X_%=\n\t"
      "s_waitcnt vmcnt(0)\n\t"
      "PQ4R_%=:\n\t"
      "PQ4L_%=:\n\t"
      "s_add_u32 s80, s80, 1\n\t"
      "s_cmp_lt_u32 s80, 262144\n\t"
      "s_cbranch_scc0 PQ4X_%=\n\t"
      "s_sleep 1\n\t"
      "global_load_dword v251, v252, s[72:73] offset:252 sc1\n\t"
      "s_waitcnt vmcnt(0)\n\t"
      "v_cmp_eq_u32_e32 vcc, -1, v251\n\t"
      "s_cbranch_vccnz PQ4L_%=\n\t"
      "PQ4X_%=:\n\t"
      "s_add_u32 s58, s72, s40\n\t"
      "s_addc_u32 s59, s73, 0\n\t"
      "global_load_dwordx4 v[160:163], v244, s[58:59] sc1\n\t"
      "s_add_u32 s58, s72, s41\n\t"
      "s_addc_u32 s59, s73, 0\n\t"
      "global_load_dwordx4 v[164:167], v244, s[58:59] sc1\n\t"
      "s_add_u32 s58, s72, s42\n\t"
      "s_addc_u32 s59, s73, 0\n\t"
      "global_load_dwordx4 v[168:171], v244, s[58:59] sc1\n\t"
      "s_add_u32 s58, s72, s43\n\t"
      "s_addc_u32 s59, s73, 0\n\t"
      "global_load_dwordx4 v[172:175], v244, s[58:59] sc1\n\t"
      "s_add_u32 s58, s72, s44\n\t"
      "s_addc_u32 s59, s73, 0\n\t"
      "global_load_dwordx4 v[176:179], v244, s[58:59] sc1\n\t"
      "s_add_u32 s58, s72, s45\n\t"
      "s_addc_u32 s59, s73, 0\n\t"
      "global_load_dwordx4 v[180:183], v244, s[58:59] sc1\n\t"
      "s_add_u32 s58, s72, s46\n\t"
      "s_addc_u32 s59, s73, 0\n\t"
      "global_load_dwordx4 v[184:187], v244, s[58:59] sc1\n\t"
      "s_waitcnt vmcnt(0)\n\t"
      "v_max3_u32 v251, v160, v161, v162\n\t"
      "v_max3_u32 v251, v251, v163, v164\n\t"
      "v_max3_u32 v251, v251, v165, v166\n\t"
      "v_max3_u32 v251, v251, v167, v168\n\t"
      "v_max3_u32 v251, v251, v169, v170\n\t"
      "v_max3_u32 v251, v251, v171, v172\n\t"
      "v_max3_u32 v251, v251, v173, v174\n\t"
      "v_max3_u32 v251, v251, v175, v176\n\t"
      "v_max3_u32 v251, v251, v177, v178\n\t"
      "v_max3_u32 v251, v251, v179, v180\n\t"
      "v_max3_u32 v251, v251, v181, v182\n\t"
      "v_max3_u32 v251, v251, v183, v184\n\t"
      "v_max3_u32 v251, v251, v185, v186\n\t"
      "v_max_u32_e32 v251, v251, v187\n\t"
      "v_cmp_eq_u32_e32 vcc, -1, v251\n\t"
      "s_cbranch_vccnz LA44L_%=\n\t"
      "LA44X_%=:\n\t"
      "v_mfma_f32_16x16x32_bf16 v[212:215], v[160:163], v[0:3], 0\n\t"
      "v_mfma_f32_16x16x32_bf16 v[212:215], v[164:167], v[16:19], v[212:215]\n\t"
      "v_mfma_f32_16x16x32_bf16 v[212:215], v[168:171], v[32:35], v[212:215]\n\t"
      "v_mfma_f32_16x16x32_bf16 v[212:215], v[172:175], v[48:51], v[212:215]\n\t"
      "v_mfma_f32_16x16x32_bf16 v[212:215], v[176:179], v[64:67], v[212:215]\n\t"
      "v_mfma_f32_16x16x32_bf16 v[212:215], v[180:183], v[80:83], v[212:215]\n\t"
      "v_mfma_f32_16x16x32_bf16 v[212:215], v[184:187], v[96:99], v[212:215]\n\t"
      "v_mfma_f32_16x16x32_bf16 v[216:219], v[160:163], v[4:7], 0\n\t"
      "v_mfma_f32_16x16x32_bf16 v[216:219], v[164:167], v[20:23], v[216:219]\n\t"
      "v_mfma_f32_16x16x32_bf16 v[216:219], v[168:171], v[36:39], v[216:219]\n\t"
      "s_nop 7\n\t"
      "s_branch LA44B_%=\n\t"
      "LA45R_%=:\n\t"
      "s_mov_b32 s80, 0\n\t"
      "LA45L_%=:\n\t"
      "s_add_u32 s80, s80, 1\n\t"
      "s_cmp_lt_u32 s80, 262144\n\t"
      "s_cbranch_scc0 LA45X_%=\n\t"
      "s_waitcnt vmcnt(0)\n\t"
      "PQ5R_%=:\n\t"
      "PQ5L_%=:\n\t"
      "s_add_u32 s80, s80, 1\n\t"
      "s_cmp_lt_u32 s80, 262144\n\t"
      "s_cbranch_scc0 PQ5X_%=\n\t"
      "s_sleep 1\n\t"
      "global_load_dword v251, v252, s[74:75] offset:252 sc1\n\t"
      "s_waitcnt vmcnt(0)\n\t"
      "v_cmp_eq_u32_e32 vcc, -1, v251\n\t"
      "s_cbranch_vccnz PQ5L_%=\n\t"
      "PQ5X_%=:\n\t"
      "s_add_u32 s58, s74, s40\n\t"
      "s_addc_u32 s59, s75, 0\n\t"
      "global_load_dwordx4 v[188:191], v244, s[58:59] sc1\n\t"
      "s_add_u32 s58, s74, s41\n\t"
      "s_addc_u32 s59, s75, 0\n\t"
      "global_load_dwordx4 v[192:195], v244, s[58:59] sc1\n\t"
      "s_add_u32 s58, s74, s42\n\t"
      "s_addc_u32 s59, s75, 0\n\t"
      "global_load_dwordx4 v[196:199], v244, s[58:59] sc1\n\t"
      "s_add_u32 s58, s74, s43\n\t"
      "s_addc_u32 s59, s75, 0\n\t"
      "global_load_dwordx4 v[200:203], v244, s[58:59] sc1\n\t"
      "s_add_u32 s58, s74, s44\n\t"
      "s_addc_u32 s59, s75, 0\n\t"
      "global_load_dwordx4 v[204:207], v244, s[58:59] sc1\n\t"
      "s_add_u32 s58, s74, s45\n\t"
      "s_addc_u32 s59, s75, 0\n\t"
      "global_load_dwordx4 v[128:131], v244, s[58:59] sc1\n\t"
      "s_add_u32 s58, s74, s46\n\t"
      "s_addc_u32 s59, s75, 0\n\t"
      "global_load_dwordx4 v[132:135], v244, s[58:59] sc1\n\t"
      "s_waitcnt vmcnt(0)\n\t"
      "v_max3_u32 v251, v188, v189, v190\n\t"
      "v_max3_u32 v251, v251, v191, v192\n\t"
      "v_max3_u32 v251, v251, v193, v194\n\t"
      "v_max3_u32 v251, v251, v195, v196\n\t"
      "v_max3_u32 v251, v251, v197, v198\n\t"
      "v_max3_u32 v251, v251, v199, v200\n\t"
      "v_max3_u32 v251, v251, v201, v202\n\t"
      "v_max3_u32 v251, v251, v203, v204\n\t"
      "v_max3_u32 v251, v251, v205, v206\n\t"
      "v_max3_u32 v251, v251, v207, v128\n\t"
      "v_max3_u32 v251, v251, v129, v130\n\t"
      "v_max3_u32 v251, v251, v131, v132\n\t"
      "v_max3_u32 v251, v251, v133, v134\n\t"
      "v_max_u32_e32 v251, v251, v135\n\t"
      "v_cmp_eq_u32_e32 vcc, -1, v251\n\t"
      "s_cbranch_vccnz LA45L_%=\n\t"
      "LA45X_%=:\n\t"
      "v_mfma_f32_16x16x32_bf16 v[208:211], v[188:191], v[0:3], 0\n\t"
      "v_mfma_f32_16x16x32_bf16 v[208:211], v[192:195], v[16:19], v[208:211]\n\t"
      "v_mfma_f32_16x16x32_bf16 v[208:211], v[196:199], v[32:35], v[208:211]\n\t"
      "v_mfma_f32_16x16x32_bf16 v[208:211], v[200:203], v[48:51], v[208:211]\n\t"
      "v_mfma_f32_16x16x32_bf16 v[208:211], v[204:207], v[64:67], v[208:211]\n\t"
      "v_mfma_f32_16x16x32_bf16 v[208:211], v[128:131], v[80:83], v[208:211]\n\t"
      "v_mfma_f32_16x16x32_bf16 v[208:211], v[132:135], v[96:99], v[208:211]\n\t"
      "v_mfma_f32_16x16x32_bf16 v[212:215], v[188:191], v[4:7], 0\n\t"
      "v_mfma_f32_16x16x32_bf16 v[212:215], v[192:195], v[20:23], v[212:215]\n\t"
      "v_mfma_f32_16x16x32_bf16 v[212:215], v[196:199], v[36:39], v[212:215]\n\t"
      "s_nop 7\n\t"
      "s_branch LA45B_%=\n\t"
      "LA46R_%=:\n\t"
      "s_mov_b32 s80, 0\n\t"
      "LA46L_%=:\n\t"
      "s_add_u32 s80, s80, 1\n\t"
      "s_cmp_lt_u32 s80, 262144\n\t"
      "s_cbranch_scc0 LA46X_%=\n\t"
      "s_waitcnt vmcnt(0)\n\t"
      "PQ6R_%=:\n\t"
      "PQ6L_%=:\n\t"
      "s_add_u32 s80, s80, 1\n\t"
      "s_cmp_lt_u32 s80, 262144\n\t"
      "s_cbranch_scc0 PQ6X_%=\n\t"
      "s_sleep 1\n\t"
      "global_load_dword v251, v252, s[76:77] offset:252 sc1\n\t"
      "s_waitcnt vmcnt(0)\n\t"
      "v_cmp_eq_u32_e32 vcc, -1, v251\n\t"
      "s_cbranch_vccnz PQ6L_%=\n\t"
      "PQ6X_%=:\n\t"
      "s_add_u32 s58, s76, s40\n\t"
      "s_addc_u32 s59, s77, 0\n\t"
      "global_load_dwordx4 v[136:139], v244, s[58:59] sc1\n\t"
      "s_add_u32 s58, s76, s41\n\t"
      "s_addc_u32 s59, s77, 0\n\t"
      "global_load_dwordx4 v[140:143], v244, s[58:59] sc1\n\t"
      "s_add_u32 s58, s76, s42\n\t"
      "s_addc_u32 s59, s77, 0\n\t"
      "global_load_dwordx4 v[144:147], v244, s[58:59] sc1\n\t"
      "s_add_u32 s58, s76, s43\n\t"
      "s_addc_u32 s59, s77, 0\n\t"
      "global_load_dwordx4 v[148:151], v244, s[58:59] sc1\n\t"
      "s_add_u32 s58, s76, s44\n\t"
      "s_addc_u32 s59, s77, 0\n\t"
      "global_load_dwordx4 v[152:155], v244, s[58:59] sc1\n\t"
      "s_add_u32 s58, s76, s45\n\t"
      "s_addc_u32 s59, s77, 0\n\t"
      "global_load_dwordx4 v[156:159], v244, s[58:59] sc1\n\t"
      "s_add_u32 s58, s76, s46\n\t"
      "s_addc_u32 s59, s77, 0\n\t"
      "global_load_dwordx4 v[160:163], v244, s[58:59] sc1\n\t"
      "s_waitcnt vmcnt(0)\n\t"
      "v_max3_u32 v251, v136, v137, v138\n\t"
      "v_max3_u32 v251, v251, v139, v140\n\t"
      "v_max3_u32 v251, v251, v141, v142\n\t"
      "v_max3_u32 v251, v251, v143, v144\n\t"
      "v_max3_u32 v251, v251, v145, v146\n\t"
      "v_max3_u32 v251, v251, v147, v148\n\t"
      "v_max3_u32 v251, v251, v149, v150\n\t"
      "v_max3_u32 v251, v251, v151, v152\n\t"
      "v_max3_u32 v251, v251, v153, v154\n\t"
      "v_max3_u32 v251, v251, v155, v156\n\t"
      "v_max3_u32 v251, v251, v157, v158\n\t"
      "v_max3_u32 v251, v251, v159, v160\n\t"
      "v_max3_u32 v251, v251, v161, v162\n\t"
      "v_max_u32_e32 v251, v251, v163\n\t"
      "v_cmp_eq_u32_e32 vcc, -1, v251\n\t"
      "s_cbranch_vccnz LA46L_%=\n\t"
      "LA46X_%=:\n\t"
      "v_mfma_f32_16x16x32_bf16 v[224:227], v[136:139], v[0:3], 0\n\t"
      "v_mfma_f32_16x16x32_bf16 v[224:227], v[140:143], v[16:19], v[224:227]\n\t"
      "v_mfma_f32_16x16x32_bf16 v[224:227], v[144:147], v[32:35], v[224:227]\n\t"
      "v_mfma_f32_16x16x32_bf16 v[224:227], v[148:151], v[48:51], v[224:227]\n\t"
      "v_mfma_f32_16x16x32_bf16 v[224:227], v[152:155], v[64:67], v[224:227]\n\t"
      "v_mfma_f32_16x16x32_bf16 v[224:227], v[156:159], v[80:83], v[224:227]\n\t"
      "v_mfma_f32_16x16x32_bf16 v[224:227], v[160:163], v[96:99], v[224:227]\n\t"
      "v_mfma_f32_16x16x32_bf16 v[208:211], v[136:139], v[4:7], 0\n\t"
      "v_mfma_f32_16x16x32_bf16 v[208:211], v[140:143], v[20:23], v[208:211]\n\t"
      "v_mfma_f32_16x16x32_bf16 v[208:211], v[144:147], v[36:39], v[208:211]\n\t"
      "s_nop 7\n\t"
      "s_branch LA46B_%=\n\t"
      : "+{s[0:1]}"(ka), "+{v0}"(tid)
      : "{s2}"(blockIdx.x)
      : "memory", "vcc", "scc", "m0",
        "v1", "v2", "v3", "v4", "v5", "v6", "v7", "v8", "v9", "v10", "v11", "v12", "v13", "v14", "v15", "v16", "v17",
        "v18", "v19", "v20", "v21", "v22", "v23", "v24", "v25", "v26", "v27", "v28", "v29", "v30", "v31", "v32", "v33",
        "v34", "v35", "v36", "v37", "v38", "v39", "v40", "v41", "v42", "v43", "v44", "v45", "v46", "v47", "v48", "v49",
        "v50", "v51", "v52", "v53", "v54", "v55", "v56", "v57", "v58", "v59", "v60", "v61", "v62", "v63", "v64", "v65",
        "v66", "v67", "v68", "v69", "v70", "v71", "v72", "v73", "v74", "v75", "v76", "v77", "v78", "v79", "v80", "v81",
        "v82", "v83", "v84", "v85", "v86", "v87", "v88", "v89", "v90", "v91", "v92", "v93", "v94", "v95", "v96", "v97",
        "v98", "v99", "v100", "v101", "v102", "v103", "v104", "v105", "v106", "v107", "v108", "v109", "v110", "v111",
        "v112", "v113", "v114", "v115", "v116", "v117", "v118", "v119", "v120", "v121", "v122", "v123", "v124", "v125",
        "v126", "v127", "v128", "v129", "v130", "v131", "v132", "v133", "v134", "v135", "v136", "v137", "v138", "v139",
        "v140", "v141", "v142", "v143", "v144", "v145", "v146", "v147", "v148", "v149", "v150", "v151", "v152", "v153",
        "v154", "v155", "v156", "v157", "v158", "v159", "v160", "v161", "v162", "v163", "v164", "v165", "v166", "v167",
        "v168", "v169", "v170", "v171", "v172", "v173", "v174", "v175", "v176", "v177", "v178", "v179", "v180", "v181",
        "v182", "v183", "v184", "v185", "v186", "v187", "v188", "v189", "v190", "v191", "v192", "v193", "v194", "v195",
        "v196", "v197", "v198", "v199", "v200", "v201", "v202", "v203", "v204", "v205", "v206", "v207", "v208", "v209",
        "v210", "v211", "v212", "v213", "v214", "v215", "v216", "v217", "v218", "v219", "v220", "v221", "v222", "v223",
        "v224", "v225", "v226", "v227", "v228", "v229", "v230", "v231", "v232", "v233", "v234", "v235", "v236", "v237",
        "v238", "v239", "v240", "v241", "v242", "v243", "v244", "v245", "v246", "v247", "v248", "v249", "v250", "v251",
        "v252", "v253", "v254", "v255", "s3", "s4", "s5", "s6", "s7", "s8", "s9", "s10", "s11", "s12", "s13", "s14",
        "s15", "s16", "s17", "s18", "s19", "s20", "s21", "s22", "s23", "s24", "s25", "s26", "s27", "s28", "s29", "s30",
        "s31", "s32", "s33", "s34", "s35", "s36", "s37", "s38", "s39", "s40", "s41", "s42", "s43", "s44", "s45", "s46",
        "s47", "s48", "s49", "s50", "s51", "s52", "s53", "s54", "s55", "s56", "s57", "s58", "s59", "s60", "s61", "s62",
        "s63", "s64", "s65", "s66", "s67", "s68", "s69", "s70", "s71", "s72", "s73", "s74", "s75", "s76", "s77", "s78",
        "s79", "s80", "s81", "s82", "s83"
  );
#endif
}

extern "C" void kernel_launch(void* const* d_in, const int* in_sizes, int n_in, void* d_out, int out_size,
                              void* d_ws, size_t ws_size, hipStream_t stream) {
  (void)hipFuncSetAttribute((const void*)fused_block, hipFuncAttributeMaxDynamicSharedMemorySize, 163840);
  fused_block<<<256, 512, 163840, stream>>>((const u16*)d_in[0], (const u16*)d_in[1], (const u16*)d_in[2],
                                            (const u16*)d_in[4], (const u16*)d_in[5], (const u16*)d_in[6],
                                            (const u16*)d_in[7], (u16*)d_out, (char*)d_ws);
}
```

```cpp
#include <hip/hip_runtime.h>
#include <stdint.h>
typedef unsigned short u16;
__global__ __launch_bounds__(512, 2) void fused_block(const u16* x, const u16* wqkv, const u16* wo, const u16* w1, const u16* b1,
                                                   const u16* w2, const u16* b2, u16* out, char* ws) {
#if defined(__HIP_DEVICE_COMPILE__)
  unsigned long long ka = (unsigned long long)__builtin_amdgcn_kernarg_segment_ptr();
  unsigned tid = threadIdx.x;
  asm volatile(
      "s_load_dwordx16 s[4:19], s[0:1], 0x0\n\t"
      "s_load_dwordx2 s[20:21], s[0:1], 0x40\n\t"
      "v_lshrrev_b32_e32 v199, 6, v0\n\t"
      "v_and_b32_e32 v192, 63, v0\n\t"
      "v_readfirstlane_b32 s24, v199\n\t"
      "s_and_b32 s34, s2, 7\n\t"
      "s_lshr_b32 s35, s2, 6\n\t"
      "s_lshl2_add_u32 s25, s34, s35\n\t"
      "s_bfe_u32 s26, s2, 196611\n\t"
      "s_and_b32 s34, s25, 30\n\t"
      "s_lshl_b32 s34, s34, 6\n\t"
      "s_and_b32 s35, s25, 1\n\t"
      "s_add_u32 s38, s34, s35\n\t"
      "v_lshrrev_b32_e32 v197, 3, v192\n\t"
      "v_and_b32_e32 v198, 7, v192\n\t"
      "s_min_u32 s28, s26, 6\n\t"
      "s_lshl4_add_u32 s34, s24, s38\n\t"
      "s_mul_i32 s34, s34, 896\n\t"
      "s_movk_i32 s35, 1792\n\t"
      "v_lshl_add_u32 v199, v198, 4, s34\n\t"
      "v_mad_u32_u24 v199, v197, s35, v199\n\t"
      "s_lshl_b32 s34, s28, 7\n\t"
      "s_cmp_lg_u32 s24, 0\n\t"
      "s_cbranch_scc1 NTC_%=\n\t"
      "s_getpc_b64 s[58:59]\n\t"
      "s_lshr_b32 s37, s2, 3\n\t"
      "v_min_u32_e32 v200, 8, v192\n\t"
      "v_lshl_add_u32 v200, v200, 5, s37\n\t"
      "s_mov_b32 m0, 64512\n\t"
      "v_min_u32_e32 v200, 286, v200\n\t"
      "v_lshlrev_b32_e32 v200, 7, v200\n\t"
      "global_load_lds_dword v200, s[58:59]\n\t"
      "NTC_%=:\n\t"
      "v_add_u32_e32 v120, s34, v199\n\t"
      "s_add_u32 s35, s34, 128\n\t"
      "s_sub_u32 s36, s35, 896\n\t"
      "s_min_u32 s35, s35, s36\n\t"
      "v_add_u32_e32 v121, s35, v199\n\t"
      "s_waitcnt lgkmcnt(0)\n\t"
      "global_load_dwordx4 v[204:207], v120, s[4:5]\n\t"
      "s_add_u32 s35, s34, 256\n\t"
      "s_sub_u32 s36, s35, 896\n\t"
      "s_min_u32 s35, s35, s36\n\t"
      "v_add_u32_e32 v122, s35, v199\n\t"
      "global_load_dwordx4 v[4:7], v121, s[4:5]\n\t"
      "s_add_u32 s35, s34, 384\n\t"
      "s_sub_u32 s36, s35, 896\n\t"
      "s_min_u32 s35, s35, s36\n\t"
      "v_add_u32_e32 v123, s35, v199\n\t"
      "global_load_dwordx4 v[8:11], v122, s[4:5]\n\t"
      "s_add_u32 s35, s34, 512\n\t"
      "s_sub_u32 s36, s35, 896\n\t"
      "s_min_u32 s35, s35, s36\n\t"
      "v_add_u32_e32 v124, s35, v199\n\t"
      "global_load_dwordx4 v[12:15], v123, s[4:5]\n\t"
      "s_add_u32 s35, s34, 640\n\t"
      "s_sub_u32 s36, s35, 896\n\t"
      "s_min_u32 s35, s35, s36\n\t"
      "v_add_u32_e32 v125, s35, v199\n\t"
      "global_load_dwordx4 v[16:19], v124, s[4:5]\n\t"
      "s_add_u32 s35, s34, 768\n\t"
      "s_sub_u32 s36, s35, 896\n\t"
      "s_min_u32 s35, s35, s36\n\t"
      "v_add_u32_e32 v126, s35, v199\n\t"
      "global_load_dwordx4 v[20:23], v125, s[4:5]\n\t"
      "s_cmp_lt_u32 s25, 2\n\t"
      "s_cselect_b32 s34, 0, 16\n\t"
      "s_sub_u32 s34, s38, s34\n\t"
      "s_lshl1_add_u32 s34, s24, s34\n\t"
      "s_mul_i32 s34, s34, 896\n\t"
      "s_and_b32 s33, s25, 3\n\t"
      "s_lshl_b32 s33, s33, 1\n\t"
      "s_and_b32 s35, s26, 1\n\t"
      "s_add_u32 s33, s33, s35\n\t"
      "s_cmp_eq_u32 s33, 7\n\t"
      "s_cselect_b32 s33, 0, s33\n\t"
      "v_min_u32_e32 v200, 55, v192\n\t"
      "s_lshl_b32 s35, s33, 3\n\t"
      "v_add_u32_e32 v201, s35, v200\n\t"
      "v_subrev_u32_e32 v202, 56, v201\n\t"
      "v_min_u32_e32 v201, v201, v202\n\t"
      "v_lshl_add_u32 v127, v201, 4, s34\n\t"
      "s_and_b32 s39, s24, 3\n\t"
      "s_lshl_b32 s39, s39, 12\n\t"
      "s_cmp_lt_u32 s24, 4\n\t"
      "s_cselect_b32 s27, 4, 0\n\t"
      "v_lshrrev_b32_e32 v193, 4, v192\n\t"
      "v_and_b32_e32 v194, 15, v192\n\t"
      "v_bfe_u32 v195, v192, 2, 2\n\t"
      "v_and_b32_e32 v196, 3, v192\n\t"
      "v_lshlrev_b32_e32 v244, 4, v192\n\t"
      "s_lshr_b32 s34, s24, 1\n\t"
      "s_mul_i32 s34, s34, 14336\n\t"
      "s_and_b32 s35, s24, 1\n\t"
      "s_lshl_b32 s35, s35, 7\n\t"
      "s_add_u32 s34, s34, s35\n\t"
      "v_xor_b32_e32 v199, v197, v198\n\t"
      "v_lshl_add_u32 v199, v199, 4, s34\n\t"
      "v_lshl_add_u32 v199, v198, 8, v199\n\t"
      "s_sub_u32 s35, s28, s33\n\t"
      "s_cselect_b32 s36, 7, 0\n\t"
      "s_add_u32 s35, s35, s36\n\t"
      "s_lshl_b32 s35, s35, 11\n\t"
      "s_add_u32 s34, s35, 0\n\t"
      "s_sub_u32 s36, s34, 14336\n\t"
      "s_min_u32 s34, s34, s36\n\t"
      "v_add_u32_e32 v112, s34, v199\n\t"
      "s_add_u32 s34, s35, 2048\n\t"
      "s_sub_u32 s36, s34, 14336\n\t"
      "s_min_u32 s34, s34, s36\n\t"
      "v_add_u32_e32 v113, s34, v199\n\t"
      "s_add_u32 s34, s35, 4096\n\t"
      "s_sub_u32 s36, s34, 14336\n\t"
      "s_min_u32 s34, s34, s36\n\t"
      "v_add_u32_e32 v114, s34, v199\n\t"
      "s_add_u32 s34, s35, 6144\n\t"
      "s_sub_u32 s36, s34, 14336\n\t"
      "s_min_u32 s34, s34, s36\n\t"
      "v_add_u32_e32 v115, s34, v199\n\t"
      "s_add_u32 s34, s35, 8192\n\t"
      "s_sub_u32 s36, s34, 14336\n\t"
      "s_min_u32 s34, s34, s36\n\t"
      "v_add_u32_e32 v116, s34, v199\n\t"
      "s_add_u32 s34, s35, 10240\n\t"
      "s_sub_u32 s36, s34, 14336\n\t"
      "s_min_u32 s34, s34, s36\n\t"
      "v_add_u32_e32 v117, s34, v199\n\t"
      "s_add_u32 s34, s35, 12288\n\t"
      "s_sub_u32 s36, s34, 14336\n\t"
      "s_min_u32 s34, s34, s36\n\t"
      "v_add_u32_e32 v118, s34, v199\n\t"
      "v_min_u32_e32 v200, 55, v192\n\t"
      "v_lshrrev_b32_e32 v201, 2, v200\n\t"
      "v_lshlrev_b32_e32 v119, 10, v201\n\t"
      "v_cmp_lt_u32_e32 vcc, 6, v201\n\t"
      "v_mov_b32_e32 v202, 57344\n\t"
      "v_mov_b32_e32 v201, 50688\n\t"
      "v_cndmask_b32_e32 v202, v202, v201, vcc\n\t"
      "v_and_b32_e32 v201, 3, v200\n\t"
      "v_lshl_add_u32 v202, v201, 7, v202\n\t"
      "v_and_b32_e32 v200, 7, v200\n\t"
      "v_xor_b32_e32 v200, s24, v200\n\t"
      "v_lshl_add_u32 v202, v200, 4, v202\n\t"
      "v_add_u32_e32 v119, v119, v202\n\t"
      "s_bitcmp1_b32 s27, 2\n\t"
      "s_cbranch_scc1 LSET_%=\n\t"
      "v_xor_b32_e32 v199, v194, v193\n\t"
      "v_lshlrev_b32_e32 v199, 4, v199\n\t"
      "v_lshl_or_b32 v200, v193, 8, v199\n\t"
      "s_and_b32 s34, s24, 1\n\t"
      "s_mul_i32 s35, s34, 28672\n\t"
      "v_add_u32_e32 v160, s35, v200\n\t"
      "v_xor_b32_e32 v161, 64, v160\n\t"
      "v_and_b32_e32 v199, 0x70, v199\n\t"
      "v_lshl_or_b32 v199, v193, 7, v199\n\t"
      "v_add_u32_e32 v199, 57344, v199\n\t"
      "v_add_u32_e32 v200, 14336, v200\n\t"
      "s_cmp_eq_u32 s34, 1\n\t"
      "s_cselect_b64 vcc, -1, 0\n\t"
      "s_cselect_b32 s35, 0, 6656\n\t"
      "v_cndmask_b32_e32 v162, v199, v200, vcc\n\t"
      "v_xor_b32_e32 v163, 64, v162\n\t"
      "v_subrev_u32_e32 v164, s35, v162\n\t"
      "v_subrev_u32_e32 v165, s35, v163\n\t"
      "s_bfe_u32 s35, s24, 65537\n\t"
      "s_mul_i32 s35, s35, 7\n\t"
      "v_and_b32_e32 v201, 24, v192\n\t"
      "v_lshlrev_b32_e32 v201, 2, v201\n\t"
      "v_lshlrev_b32_e32 v202, 7, v195\n\t"
      "v_lshl_or_b32 v202, v193, 12, v202\n\t"
      "v_add_u32_e32 v202, 65536, v202\n\t"
      "v_add_lshl_u32 v199, v196, s35, 3\n\t"
      "v_xor_b32_e32 v199, v199, v201\n\t"
      "v_add_u32_e32 v166, v199, v202\n\t"
      "v_min_u32_e32 v199, 2, v196\n\t"
      "s_add_u32 s35, s35, 4\n\t"
      "v_add_lshl_u32 v199, v199, s35, 3\n\t"
      "v_xor_b32_e32 v199, v199, v201\n\t"
      "v_add_u32_e32 v167, v199, v202\n\t"
      "v_add_u32_e32 v168, 0x10000, v166\n\t"
      "v_add_u32_e32 v169, 0x10000, v167\n\t"
      "v_lshlrev_b32_e32 v199, 11, v193\n\t"
      "v_sub_u32_e32 v170, v166, v199\n\t"
      "v_sub_u32_e32 v171, v167, v199\n\t"
      "s_branch NORM_%=\n\t"
      "LSET_%=:\n\t"
      "s_add_u32 s60, s20, 16777216\n\t"
      "s_addc_u32 s61, s21, 0\n\t"
      "s_load_dwordx2 s[22:23], s[60:61], 0x0\n\t"
      "s_lshr_b32 s34, s39, 9\n\t"
      "v_add_u32_e32 v199, s34, v197\n\t"
      "s_and_b32 s34, s24, 1\n\t"
      "s_lshl_b32 s34, s34, 2\n\t"
      "v_and_b32_e32 v200, 2, v197\n\t"
      "v_or_b32_e32 v200, s34, v200\n\t"
      "v_xor_b32_e32 v200, v200, v198\n\t"
      "v_min_u32_e32 v200, 6, v200\n\t"
      "v_lshlrev_b32_e32 v200, 4, v200\n\t"
      "s_movk_i32 s34, 2688\n\t"
      "v_mad_u32_u24 v149, v199, s34, v200\n\t"
      "v_add_u32_e32 v148, 128, v149\n\t"
      "v_add_u32_e32 v150, 84096, v149\n\t"
      "v_add_u32_e32 v151, 83968, v149\n\t"
      "v_add_u32_e32 v152, 85120, v149\n\t"
      "s_sub_u32 s6, s6, 128\n\t"
      "s_subb_u32 s7, s7, 0\n\t"
      "s_mul_i32 s37, s26, 112\n\t"
      "s_mov_b32 s81, s33\n\t"
      "s_mov_b32 s82, s33\n\t"
      "s_and_b32 s35, s25, 3\n\t"
      "s_lshl2_add_u32 s31, s26, s35\n\t"
      "s_and_b32 s35, s2, 7\n\t"
      "s_mul_i32 s34, s35, 14\n\t"
      "s_lshl4_add_u32 s35, s35, -14\n\t"
      "s_cmp_lt_u32 s31, 14\n\t"
      "s_cselect_b32 s34, s34, s35\n\t"
      "s_cselect_b32 s36, 1, 0\n\t"
      "s_or_b32 s27, s27, s36\n\t"
      "s_add_u32 s34, s34, s31\n\t"
      "s_lshr_b32 s29, s34, 2\n\t"
      "s_and_b32 s30, s34, 3\n\t"
      "s_mul_i32 s30, s30, 7\n\t"
      "v_and_b32_e32 v199, 60, v192\n\t"
      "v_and_b32_e32 v200, 24, v192\n\t"
      "v_lshlrev_b32_e32 v202, 3, v196\n\t"
      "v_lshl_or_b32 v202, v200, 2, v202\n\t"
      "v_lshl_or_b32 v202, v195, 7, v202\n\t"
      "v_lshl_or_b32 v202, v193, 10, v202\n\t"
      "v_add_u32_e32 v224, s39, v202\n\t"
      "v_xor_b32_e32 v225, 32, v224\n\t"
      "v_xor_b32_e32 v226, 64, v224\n\t"
      "v_xor_b32_e32 v227, 96, v224\n\t"
      "s_lshl_b32 s34, s24, 12\n\t"
      "s_add_u32 s34, s34, 98304\n\t"
      "v_add_u32_e32 v245, s34, v244\n\t"
      "v_lshlrev_b32_e32 v199, 2, v199\n\t"
      "s_lshl_b32 s34, s24, 8\n\t"
      "v_lshl_add_u32 v247, v192, 2, s34\n\t"
      "v_and_b32_e32 v200, 2, v192\n\t"
      "v_mul_u32_u24_e32 v202, 576, v200\n\t"
      "v_xor_b32_e32 v199, v202, v199\n\t"
      "v_and_b32_e32 v202, 1, v192\n\t"
      "v_lshl_or_b32 v199, v202, 3, v199\n\t"
      "s_and_b32 s35, s34, 0x400\n\t"
      "s_add_u32 s34, s34, s35\n\t"
      "s_add_u32 s34, s34, 98304\n\t"
      "v_add_u32_e32 v246, s34, v199\n\t"
      "v_lshlrev_b32_e32 v199, 2, v202\n\t"
      "v_lshl_or_b32 v199, v200, 4, v199\n\t"
      "s_min_u32 s34, s29, 27\n\t"
      "s_lshl_b32 s34, s34, 7\n\t"
      "s_lshr_b32 s35, s24, 2\n\t"
      "s_lshl_b32 s35, s35, 6\n\t"
      "s_add_u32 s34, s34, s35\n\t"
      "s_lshr_b32 s35, s39, 9\n\t"
      "s_add_u32 s34, s34, s35\n\t"
      "v_add_u32_e32 v248, s34, v199\n\t"
      "s_add_u32 s48, s20, 8388608\n\t"
      "s_addc_u32 s49, s21, 0\n\t"
      "v_and_b32_e32 v199, 2, v197\n\t"
      "v_xor_b32_e32 v199, v199, v198\n\t"
      "v_lshlrev_b32_e32 v199, 4, v199\n\t"
      "s_movk_i32 s34, 7168\n\t"
      "v_mad_u32_u24 v144, v197, s34, v199\n\t"
      "v_xor_b32_e32 v199, 64, v144\n\t"
      "v_add_u32_e32 v145, 56320, v199\n\t"
      "v_add_u32_e32 v146, 112640, v144\n\t"
      "v_add_u32_e32 v147, 168960, v199\n\t"
      "v_and_b32_e32 v199, 6, v197\n\t"
      "v_xor_b32_e32 v199, v199, v198\n\t"
      "v_lshlrev_b32_e32 v199, 4, v199\n\t"
      "s_movk_i32 s35, 896\n\t"
      "v_mad_u32_u24 v154, v197, s35, v199\n\t"
      "v_add_u32_e32 v155, 6144, v154\n\t"
      "v_add_u32_e32 v156, 12288, v154\n\t"
      "v_add_u32_e32 v157, 18432, v154\n\t"
      "s_mul_i32 s34, s24, 917504\n\t"
      "s_lshl_b32 s35, s29, 7\n\t"
      "s_add_u32 s34, s34, s35\n\t"
      "s_add_u32 s52, s10, s34\n\t"
      "s_addc_u32 s53, s11, 0\n\t"
      "s_lshl_b32 s34, s28, 7\n\t"
      "s_add_u32 s56, s8, s34\n\t"
      "s_addc_u32 s57, s9, 0\n\t"
      "s_mov_b32 s34, 0\n\t"
      "s_and_b32 s35, s34, 1\n\t"
      "s_and_b32 s34, s34, 2\n\t"
      "s_lshl_b32 s34, s34, 6\n\t"
      "s_add_u32 s34, s34, s35\n\t"
      "s_mul_i32 s76, s34, 3584\n\t"
      "s_mov_b32 s34, 1\n\t"
      "s_and_b32 s35, s34, 1\n\t"
      "s_and_b32 s34, s34, 2\n\t"
      "s_lshl_b32 s34, s34, 6\n\t"
      "s_add_u32 s34, s34, s35\n\t"
      "s_mul_i32 s77, s34, 3584\n\t"
      "s_mov_b32 s34, 2\n\t"
      "s_and_b32 s35, s34, 1\n\t"
      "s_and_b32 s34, s34, 2\n\t"
      "s_lshl_b32 s34, s34, 6\n\t"
      "s_add_u32 s34, s34, s35\n\t"
      "s_mul_i32 s78, s34, 3584\n\t"
      "s_mov_b32 s34, 3\n\t"
      "s_and_b32 s35, s34, 1\n\t"
      "s_and_b32 s34, s34, 2\n\t"
      "s_lshl_b32 s34, s34, 6\n\t"
      "s_add_u32 s34, s34, s35\n\t"
      "s_mul_i32 s79, s34, 3584\n\t"
      "NORM_%=:\n\t"
      "global_load_dwordx4 v[24:27], v126, s[4:5]\n\t"
      "global_load_dwordx4 v[28:31], v127, s[4:5]\n\t"
      "v_mov_b32_e32 v199, 0x3f803f80\n\t"
      "s_waitcnt vmcnt(7)\n\t"
      "v_dot2_f32_bf16 v88, v204, v199, 0\n\t"
      "v_dot2_f32_bf16 v92, v204, v204, 0\n\t"
      "v_lshlrev_b32_e32 v32, 16, v204\n\t"
      "v_and_b32_e32 v33, 0xffff0000, v204\n\t"
      "v_dot2_f32_bf16 v89, v205, v199, 0\n\t"
      "v_dot2_f32_bf16 v93, v205, v205, 0\n\t"
      "v_lshlrev_b32_e32 v34, 16, v205\n\t"
      "v_and_b32_e32 v35, 0xffff0000, v205\n\t"
      "v_dot2_f32_bf16 v90, v206, v199, 0\n\t"
      "v_dot2_f32_bf16 v94, v206, v206, 0\n\t"
      "v_lshlrev_b32_e32 v36, 16, v206\n\t"
      "v_and_b32_e32 v37, 0xffff0000, v206\n\t"
      "v_dot2_f32_bf16 v91, v207, v199, 0\n\t"
      "v_dot2_f32_bf16 v95, v207, v207, 0\n\t"
      "v_lshlrev_b32_e32 v38, 16, v207\n\t"
      "v_and_b32_e32 v39, 0xffff0000, v207\n\t"
      "s_waitcnt vmcnt(6)\n\t"
      "v_dot2_f32_bf16 v88, v4, v199, v88\n\t"
      "v_dot2_f32_bf16 v92, v4, v4, v92\n\t"
      "v_lshlrev_b32_e32 v40, 16, v4\n\t"
      "v_and_b32_e32 v41, 0xffff0000, v4\n\t"
      "v_dot2_f32_bf16 v89, v5, v199, v89\n\t"
      "v_dot2_f32_bf16 v93, v5, v5, v93\n\t"
      "v_lshlrev_b32_e32 v42, 16, v5\n\t"
      "v_and_b32_e32 v43, 0xffff0000, v5\n\t"
      "v_dot2_f32_bf16 v90, v6, v199, v90\n\t"
      "v_dot2_f32_bf16 v94, v6, v6, v94\n\t"
      "v_lshlrev_b32_e32 v44, 16, v6\n\t"
      "v_and_b32_e32 v45, 0xffff0000, v6\n\t"
      "v_dot2_f32_bf16 v91, v7, v199, v91\n\t"
      "v_dot2_f32_bf16 v95, v7, v7, v95\n\t"
      "v_lshlrev_b32_e32 v46, 16, v7\n\t"
      "v_and_b32_e32 v47, 0xffff0000, v7\n\t"
      "s_waitcnt vmcnt(5)\n\t"
      "v_dot2_f32_bf16 v88, v8, v199, v88\n\t"
      "v_dot2_f32_bf16 v92, v8, v8, v92\n\t"
      "v_lshlrev_b32_e32 v48, 16, v8\n\t"
      "v_and_b32_e32 v49, 0xffff0000, v8\n\t"
      "v_dot2_f32_bf16 v89, v9, v199, v89\n\t"
      "v_dot2_f32_bf16 v93, v9, v9, v93\n\t"
      "v_lshlrev_b32_e32 v50, 16, v9\n\t"
      "v_and_b32_e32 v51, 0xffff0000, v9\n\t"
      "v_dot2_f32_bf16 v90, v10, v199, v90\n\t"
      "v_dot2_f32_bf16 v94, v10, v10, v94\n\t"
      "v_lshlrev_b32_e32 v52, 16, v10\n\t"
      "v_and_b32_e32 v53, 0xffff0000, v10\n\t"
      "v_dot2_f32_bf16 v91, v11, v199, v91\n\t"
      "v_dot2_f32_bf16 v95, v11, v11, v95\n\t"
      "v_lshlrev_b32_e32 v54, 16, v11\n\t"
      "v_and_b32_e32 v55, 0xffff0000, v11\n\t"
      "s_waitcnt vmcnt(4)\n\t"
      "v_dot2_f32_bf16 v88, v12, v199, v88\n\t"
      "v_dot2_f32_bf16 v92, v12, v12, v92\n\t"
      "v_lshlrev_b32_e32 v56, 16, v12\n\t"
      "v_and_b32_e32 v57, 0xffff0000, v12\n\t"
      "v_dot2_f32_bf16 v89, v13, v199, v89\n\t"
      "v_dot2_f32_bf16 v93, v13, v13, v93\n\t"
      "v_lshlrev_b32_e32 v58, 16, v13\n\t"
      "v_and_b32_e32 v59, 0xffff0000, v13\n\t"
      "v_dot2_f32_bf16 v90, v14, v199, v90\n\t"
      "v_dot2_f32_bf16 v94, v14, v14, v94\n\t"
      "v_lshlrev_b32_e32 v60, 16, v14\n\t"
      "v_and_b32_e32 v61, 0xffff0000, v14\n\t"
      "v_dot2_f32_bf16 v91, v15, v199, v91\n\t"
      "v_dot2_f32_bf16 v95, v15, v15, v95\n\t"
      "v_lshlrev_b32_e32 v62, 16, v15\n\t"
      "v_and_b32_e32 v63, 0xffff0000, v15\n\t"
      "s_waitcnt vmcnt(3)\n\t"
      "v_dot2_f32_bf16 v88, v16, v199, v88\n\t"
      "v_dot2_f32_bf16 v92, v16, v16, v92\n\t"
      "v_lshlrev_b32_e32 v64, 16, v16\n\t"
      "v_and_b32_e32 v65, 0xffff0000, v16\n\t"
      "v_dot2_f32_bf16 v89, v17, v199, v89\n\t"
      "v_dot2_f32_bf16 v93, v17, v17, v93\n\t"
      "v_lshlrev_b32_e32 v66, 16, v17\n\t"
      "v_and_b32_e32 v67, 0xffff0000, v17\n\t"
      "v_dot2_f32_bf16 v90, v18, v199, v90\n\t"
      "v_dot2_f32_bf16 v94, v18, v18, v94\n\t"
      "v_lshlrev_b32_e32 v68, 16, v18\n\t"
      "v_and_b32_e32 v69, 0xffff0000, v18\n\t"
      "v_dot2_f32_bf16 v91, v19, v199, v91\n\t"
      "v_dot2_f32_bf16 v95, v19, v19, v95\n\t"
      "v_lshlrev_b32_e32 v70, 16, v19\n\t"
      "v_and_b32_e32 v71, 0xffff0000, v19\n\t"
      "s_waitcnt vmcnt(2)\n\t"
      "v_dot2_f32_bf16 v88, v20, v199, v88\n\t"
      "v_dot2_f32_bf16 v92, v20, v20, v92\n\t"
      "v_lshlrev_b32_e32 v72, 16, v20\n\t"
      "v_and_b32_e32 v73, 0xffff0000, v20\n\t"
      "v_dot2_f32_bf16 v89, v21, v199, v89\n\t"
      "v_dot2_f32_bf16 v93, v21, v21, v93\n\t"
      "v_lshlrev_b32_e32 v74, 16, v21\n\t"
      "v_and_b32_e32 v75, 0xffff0000, v21\n\t"
      "v_dot2_f32_bf16 v90, v22, v199, v90\n\t"
      "v_dot2_f32_bf16 v94, v22, v22, v94\n\t"
      "v_lshlrev_b32_e32 v76, 16, v22\n\t"
      "v_and_b32_e32 v77, 0xffff0000, v22\n\t"
      "v_dot2_f32_bf16 v91, v23, v199, v91\n\t"
      "v_dot2_f32_bf16 v95, v23, v23, v95\n\t"
      "v_lshlrev_b32_e32 v78, 16, v23\n\t"
      "v_and_b32_e32 v79, 0xffff0000, v23\n\t"
      "s_waitcnt vmcnt(1)\n\t"
      "v_dot2_f32_bf16 v88, v24, v199, v88\n\t"
      "v_dot2_f32_bf16 v92, v24, v24, v92\n\t"
      "v_lshlrev_b32_e32 v80, 16, v24\n\t"
      "v_and_b32_e32 v81, 0xffff0000, v24\n\t"
      "v_dot2_f32_bf16 v89, v25, v199, v89\n\t"
      "v_dot2_f32_bf16 v93, v25, v25, v93\n\t"
      "v_lshlrev_b32_e32 v82, 16, v25\n\t"
      "v_and_b32_e32 v83, 0xffff0000, v25\n\t"
      "v_dot2_f32_bf16 v90, v26, v199, v90\n\t"
      "v_dot2_f32_bf16 v94, v26, v26, v94\n\t"
      "v_lshlrev_b32_e32 v84, 16, v26\n\t"
      "v_and_b32_e32 v85, 0xffff0000, v26\n\t"
      "v_dot2_f32_bf16 v91, v27, v199, v91\n\t"
      "v_dot2_f32_bf16 v95, v27, v27, v95\n\t"
      "v_lshlrev_b32_e32 v86, 16, v27\n\t"
      "v_and_b32_e32 v87, 0xffff0000, v27\n\t"
      "s_waitcnt vmcnt(0)\n\t"
      "s_bitcmp0_b32 s27, 2\n\t"
      "s_cbranch_scc1 SK1_%=\n\t"
      "s_mul_i32 s72, s81, 172032\n\t"
      "s_add_u32 s72, s72, s37\n\t"
      "s_add_u32 s40, s6, s72\n\t"
      "s_addc_u32 s41, s7, 0\n\t"
      "s_add_u32 s81, s81, 1\n\t"
      "s_cmp_eq_u32 s81, 7\n\t"
      "s_cselect_b32 s81, 0, s81\n\t"
      "s_add_u32 m0, s39, 131072\n\t"
      "s_nop 0\n\t"
      "global_load_lds_dwordx4 v148, s[40:41]\n\t"
      "global_load_lds_dwordx4 v149, s[40:41] offset:1024\n\t"
      "global_load_lds_dwordx4 v150, s[40:41] offset:2048\n\t"
      "global_load_lds_dwordx4 v151, s[40:41] offset:3072\n\t"
      "SK1_%=:\n\t"
      "v_dot2_f32_bf16 v96, v28, v199, 0\n\t"
      "v_dot2_f32_bf16 v97, v28, v28, 0\n\t"
      "v_dot2_f32_bf16 v96, v29, v199, v96\n\t"
      "v_dot2_f32_bf16 v97, v29, v29, v97\n\t"
      "v_dot2_f32_bf16 v96, v30, v199, v96\n\t"
      "v_dot2_f32_bf16 v97, v30, v30, v97\n\t"
      "v_dot2_f32_bf16 v96, v31, v199, v96\n\t"
      "v_dot2_f32_bf16 v97, v31, v31, v97\n\t"
      "v_cmp_gt_u32_e32 vcc, 56, v192\n\t"
      "v_pk_add_f32 v[88:89], v[88:89], v[90:91]\n\t"
      "v_pk_add_f32 v[92:93], v[92:93], v[94:95]\n\t"
      "v_cndmask_b32_e32 v96, 0, v96, vcc\n\t"
      "v_cndmask_b32_e32 v97, 0, v97, vcc\n\t"
      "v_add_f32_e32 v88, v88, v89\n\t"
      "v_add_f32_e32 v92, v92, v93\n\t"
      "v_add_f32_dpp v96, v96, v96 quad_perm:[1,0,3,2] row_mask:0xf bank_mask:0xf\n\t"
      "v_add_f32_dpp v97, v97, v97 quad_perm:[1,0,3,2] row_mask:0xf bank_mask:0xf\n\t"
      "v_add_f32_dpp v88, v88, v88 quad_perm:[1,0,3,2] row_mask:0xf bank_mask:0xf\n\t"
      "v_add_f32_dpp v92, v92, v92 quad_perm:[1,0,3,2] row_mask:0xf bank_mask:0xf\n\t"
      "v_add_f32_dpp v96, v96, v96 quad_perm:[2,3,0,1] row_mask:0xf bank_mask:0xf\n\t"
      "v_add_f32_dpp v97, v97, v97 quad_perm:[2,3,0,1] row_mask:0xf bank_mask:0xf\n\t"
      "v_add_f32_dpp v88, v88, v88 quad_perm:[2,3,0,1] row_mask:0xf bank_mask:0xf\n\t"
      "v_add_f32_dpp v92, v92, v92 quad_perm:[2,3,0,1] row_mask:0xf bank_mask:0xf\n\t"
      "v_add_f32_dpp v96, v96, v96 row_half_mirror row_mask:0xf bank_mask:0xf\n\t"
      "v_add_f32_dpp v97, v97, v97 row_half_mirror row_mask:0xf bank_mask:0xf\n\t"
      "v_add_f32_dpp v88, v88, v88 row_half_mirror row_mask:0xf bank_mask:0xf\n\t"
      "v_add_f32_dpp v92, v92, v92 row_half_mirror row_mask:0xf bank_mask:0xf\n\t"
      "v_mul_f32_e32 v88, 0xbb124925, v88\n\t"
      "v_add_f32_dpp v96, v96, v96 row_mirror row_mask:0xf bank_mask:0xf\n\t"
      "v_mul_f32_e32 v92, 0x3b124925, v92\n\t"
      "s_bitcmp0_b32 s27, 2\n\t"
      "s_cbranch_scc1 SK2_%=\n\t"
      "s_mul_i32 s72, s81, 172032\n\t"
      "s_add_u32 s72, s72, s37\n\t"
      "s_add_u32 s40, s6, s72\n\t"
      "s_addc_u32 s41, s7, 0\n\t"
      "s_add_u32 s81, s81, 1\n\t"
      "s_cmp_eq_u32 s81, 7\n\t"
      "s_cselect_b32 s81, 0, s81\n\t"
      "s_add_u32 m0, s39, 147456\n\t"
      "s_nop 0\n\t"
      "global_load_lds_dwordx4 v148, s[40:41]\n\t"
      "global_load_lds_dwordx4 v149, s[40:41] offset:1024\n\t"
      "SK2_%=:\n\t"
      "v_add_f32_dpp v97, v97, v97 row_mirror row_mask:0xf bank_mask:0xf\n\t"
      "v_fma_f32 v92, -v88, v88, v92\n\t"
      "s_nop 0\n\t"
      "v_permlane16_swap_b32_e32 v96, v97\n\t"
      "v_max_f32_e32 v92, 0, v92\n\t"
      "v_add_f32_e32 v96, v96, v97\n\t"
      "v_add_f32_e32 v92, 0x3727c5ac, v92\n\t"
      "v_mov_b32_e32 v97, v96\n\t"
      "v_rsq_f32_e32 v92, v92\n\t"
      "s_nop 0\n\t"
      "v_permlane32_swap_b32_e32 v96, v97\n\t"
      "v_add_f32_e32 v96, v96, v97\n\t"
      "v_mul_f32_e32 v88, v88, v92\n\t"
      "v_mov_b32_e32 v97, v96\n\t"
      "v_pk_fma_f32 v[32:33], v[32:33], v[92:93], v[88:89] op_sel_hi:[1,0,0]\n\t"
      "v_pk_fma_f32 v[34:35], v[34:35], v[92:93], v[88:89] op_sel_hi:[1,0,0]\n\t"
      "v_permlane16_swap_b32_e32 v96, v97\n\t"
      "v_pk_fma_f32 v[36:37], v[36:37], v[92:93], v[88:89] op_sel_hi:[1,0,0]\n\t"
      "v_pk_fma_f32 v[38:39], v[38:39], v[92:93], v[88:89] op_sel_hi:[1,0,0]\n\t"
      "v_mul_f32_e32 v96, 0xbb124925, v96\n\t"
      "v_cvt_pk_bf16_f32 v0, v32, v33\n\t"
      "v_cvt_pk_bf16_f32 v1, v34, v35\n\t"
      "v_cvt_pk_bf16_f32 v2, v36, v37\n\t"
      "v_cvt_pk_bf16_f32 v3, v38, v39\n\t"
      "ds_write_b128 v112, v[0:3]\n\t"
      "v_pk_fma_f32 v[40:41], v[40:41], v[92:93], v[88:89] op_sel_hi:[1,0,0]\n\t"
      "v_pk_fma_f32 v[42:43], v[42:43], v[92:93], v[88:89] op_sel_hi:[1,0,0]\n\t"
      "v_mul_f32_e32 v97, 0x3b124925, v97\n\t"
      "v_pk_fma_f32 v[44:45], v[44:45], v[92:93], v[88:89] op_sel_hi:[1,0,0]\n\t"
      "v_pk_fma_f32 v[46:47], v[46:47], v[92:93], v[88:89] op_sel_hi:[1,0,0]\n\t"
      "v_fma_f32 v97, -v96, v96, v97\n\t"
      "v_cvt_pk_bf16_f32 v4, v40, v41\n\t"
      "s_bitcmp0_b32 s27, 2\n\t"
      "s_cbranch_scc1 SK3_%=\n\t"
      "global_load_lds_dwordx4 v150, s[40:41] offset:2048\n\t"
      "global_load_lds_dwordx4 v151, s[40:41] offset:3072\n\t"
      "SK3_%=:\n\t"
      "v_cvt_pk_bf16_f32 v5, v42, v43\n\t"
      "v_cvt_pk_bf16_f32 v6, v44, v45\n\t"
      "v_cvt_pk_bf16_f32 v7, v46, v47\n\t"
      "ds_write_b128 v113, v[4:7]\n\t"
      "v_pk_fma_f32 v[48:49], v[48:49], v[92:93], v[88:89] op_sel_hi:[1,0,0]\n\t"
      "v_pk_fma_f32 v[50:51], v[50:51], v[92:93], v[88:89] op_sel_hi:[1,0,0]\n\t"
      "v_max_f32_e32 v97, 0, v97\n\t"
      "v_pk_fma_f32 v[52:53], v[52:53], v[92:93], v[88:89] op_sel_hi:[1,0,0]\n\t"
      "v_pk_fma_f32 v[54:55], v[54:55], v[92:93], v[88:89] op_sel_hi:[1,0,0]\n\t"
      "v_add_f32_e32 v97, 0x3727c5ac, v97\n\t"
      "v_cvt_pk_bf16_f32 v8, v48, v49\n\t"
      "v_cvt_pk_bf16_f32 v9, v50, v51\n\t"
      "v_cvt_pk_bf16_f32 v10, v52, v53\n\t"
      "v_cvt_pk_bf16_f32 v11, v54, v55\n\t"
      "ds_write_b128 v114, v[8:11]\n\t"
      "v_pk_fma_f32 v[56:57], v[56:57], v[92:93], v[88:89] op_sel_hi:[1,0,0]\n\t"
      "v_pk_fma_f32 v[58:59], v[58:59], v[92:93], v[88:89] op_sel_hi:[1,0,0]\n\t"
      "v_rsq_f32_e32 v97, v97\n\t"
      "v_pk_fma_f32 v[60:61], v[60:61], v[92:93], v[88:89] op_sel_hi:[1,0,0]\n\t"
      "v_pk_fma_f32 v[62:63], v[62:63], v[92:93], v[88:89] op_sel_hi:[1,0,0]\n\t"
      "v_cvt_pk_bf16_f32 v12, v56, v57\n\t"
      "v_cvt_pk_bf16_f32 v13, v58, v59\n\t"
      "v_cvt_pk_bf16_f32 v14, v60, v61\n\t"
      "v_cvt_pk_bf16_f32 v15, v62, v63\n\t"
      "ds_write_b128 v115, v[12:15]\n\t"
      "v_pk_fma_f32 v[64:65], v[64:65], v[92:93], v[88:89] op_sel_hi:[1,0,0]\n\t"
      "v_pk_fma_f32 v[66:67], v[66:67], v[92:93], v[88:89] op_sel_hi:[1,0,0]\n\t"
      "v_mul_f32_e32 v96, v96, v97\n\t"
      "v_pk_fma_f32 v[68:69], v[68:69], v[92:93], v[88:89] op_sel_hi:[1,0,0]\n\t"
      "v_pk_fma_f32 v[70:71], v[70:71], v[92:93], v[88:89] op_sel_hi:[1,0,0]\n\t"
      "s_bitcmp0_b32 s27, 2\n\t"
      "s_cbranch_scc1 SK4_%=\n\t"
      "s_mul_i32 s72, s82, 172032\n\t"
      "s_add_u32 s72, s72, s37\n\t"
      "s_add_u32 s72, s72, 1792\n\t"
      "s_add_u32 s40, s6, s72\n\t"
      "s_addc_u32 s41, s7, 0\n\t"
      "s_add_u32 s82, s82, 1\n\t"
      "s_cmp_eq_u32 s82, 7\n\t"
      "s_cselect_b32 s82, 0, s82\n\t"
      "s_add_u32 m0, s39, 65536\n\t"
      "s_nop 0\n\t"
      "global_load_lds_dwordx4 v148, s[40:41]\n\t"
      "global_load_lds_dwordx4 v152, s[40:41] offset:1024\n\t"
      "SK4_%=:\n\t"
      "v_cvt_pk_bf16_f32 v16, v64, v65\n\t"
      "v_cvt_pk_bf16_f32 v17, v66, v67\n\t"
      "v_cvt_pk_bf16_f32 v18, v68, v69\n\t"
      "v_cvt_pk_bf16_f32 v19, v70, v71\n\t"
      "ds_write_b128 v116, v[16:19]\n\t"
      "v_pk_fma_f32 v[72:73], v[72:73], v[92:93], v[88:89] op_sel_hi:[1,0,0]\n\t"
      "v_pk_fma_f32 v[74:75], v[74:75], v[92:93], v[88:89] op_sel_hi:[1,0,0]\n\t"
      "v_pk_fma_f32 v[76:77], v[76:77], v[92:93], v[88:89] op_sel_hi:[1,0,0]\n\t"
      "v_pk_fma_f32 v[78:79], v[78:79], v[92:93], v[88:89] op_sel_hi:[1,0,0]\n\t"
      "v_cvt_pk_bf16_f32 v20, v72, v73\n\t"
      "v_cvt_pk_bf16_f32 v21, v74, v75\n\t"
      "v_cvt_pk_bf16_f32 v22, v76, v77\n\t"
      "v_cvt_pk_bf16_f32 v23, v78, v79\n\t"
      "ds_write_b128 v117, v[20:23]\n\t"
      "v_pk_fma_f32 v[80:81], v[80:81], v[92:93], v[88:89] op_sel_hi:[1,0,0]\n\t"
      "v_pk_fma_f32 v[82:83], v[82:83], v[92:93], v[88:89] op_sel_hi:[1,0,0]\n\t"
      "v_pk_fma_f32 v[84:85], v[84:85], v[92:93], v[88:89] op_sel_hi:[1,0,0]\n\t"
      "v_pk_fma_f32 v[86:87], v[86:87], v[92:93], v[88:89] op_sel_hi:[1,0,0]\n\t"
      "v_cvt_pk_bf16_f32 v24, v80, v81\n\t"
      "v_cvt_pk_bf16_f32 v25, v82, v83\n\t"
      "v_cvt_pk_bf16_f32 v26, v84, v85\n\t"
      "v_cvt_pk_bf16_f32 v27, v86, v87\n\t"
      "ds_write_b128 v118, v[24:27]\n\t"
      "v_lshlrev_b32_e32 v32, 16, v28\n\t"
      "v_and_b32_e32 v33, 0xffff0000, v28\n\t"
      "v_lshlrev_b32_e32 v34, 16, v29\n\t"
      "v_and_b32_e32 v35, 0xffff0000, v29\n\t"
      "v_lshlrev_b32_e32 v36, 16, v30\n\t"
      "v_and_b32_e32 v37, 0xffff0000, v30\n\t"
      "v_lshlrev_b32_e32 v38, 16, v31\n\t"
      "s_bitcmp0_b32 s27, 2\n\t"
      "s_cbranch_scc1 SK5_%=\n\t"
      "s_mul_i32 s72, s82, 172032\n\t"
      "s_add_u32 s72, s72, s37\n\t"
      "s_add_u32 s72, s72, 1792\n\t"
      "s_add_u32 s40, s6, s72\n\t"
      "s_addc_u32 s41, s7, 0\n\t"
      "s_add_u32 s82, s82, 1\n\t"
      "s_cmp_eq_u32 s82, 7\n\t"
      "s_cselect_b32 s82, 0, s82\n\t"
      "s_add_u32 m0, s39, 67584\n\t"
      "s_nop 0\n\t"
      "global_load_lds_dwordx4 v148, s[40:41]\n\t"
      "global_load_lds_dwordx4 v152, s[40:41] offset:1024\n\t"
      "SK5_%=:\n\t"
      "v_and_b32_e32 v39, 0xffff0000, v31\n\t"
      "v_pk_fma_f32 v[32:33], v[32:33], v[96:97], v[96:97] op_sel:[0,1,0] op_sel_hi:[1,1,0]\n\t"
      "v_pk_fma_f32 v[34:35], v[34:35], v[96:97], v[96:97] op_sel:[0,1,0] op_sel_hi:[1,1,0]\n\t"
      "v_pk_fma_f32 v[36:37], v[36:37], v[96:97], v[96:97] op_sel:[0,1,0] op_sel_hi:[1,1,0]\n\t"
      "v_pk_fma_f32 v[38:39], v[38:39], v[96:97], v[96:97] op_sel:[0,1,0] op_sel_hi:[1,1,0]\n\t"
      "v_cvt_pk_bf16_f32 v28, v32, v33\n\t"
      "v_cvt_pk_bf16_f32 v29, v34, v35\n\t"
      "v_cvt_pk_bf16_f32 v30, v36, v37\n\t"
      "v_cvt_pk_bf16_f32 v31, v38, v39\n\t"
      "ds_write_b128 v119, v[28:31]\n\t"
      "s_waitcnt lgkmcnt(0)\n\t"
      "s_bitcmp1_b32 s27, 2\n\t"
      "s_cbranch_scc1 LPATH_%=\n\t"
      "s_setprio 2\n\t"
      "s_barrier\n\t"
      "ds_read_b64_tr_b16 v[76:77], v168\n\t"
      "ds_read_b64_tr_b16 v[78:79], v168 offset:512\n\t"
      "ds_read_b128 v[68:71], v160\n\t"
      "ds_read_b128 v[72:75], v160 offset:14336\n\t"
      "ds_read_b64_tr_b16 v[80:81], v169\n\t"
      "ds_read_b64_tr_b16 v[82:83], v169 offset:512\n\t"
      "ds_read_b64_tr_b16 v[84:85], v168 offset:1024\n\t"
      "ds_read_b64_tr_b16 v[86:87], v168 offset:1536\n\t"
      "ds_read_b128 v[64:67], v162\n\t"
      "ds_read_b64_tr_b16 v[88:89], v169 offset:1024\n\t"
      "ds_read_b64_tr_b16 v[90:91], v169 offset:1536\n\t"
      "ds_read_b64_tr_b16 v[104:105], v168 offset:2048\n\t"
      "ds_read_b64_tr_b16 v[106:107], v168 offset:2560\n\t"
      "ds_read_b128 v[96:99], v161 offset:1024\n\t"
      "ds_read_b128 v[100:103], v161 offset:15360\n\t"
      "ds_read_b64_tr_b16 v[108:109], v169 offset:2048\n\t"
      "ds_read_b64_tr_b16 v[110:111], v169 offset:2560\n\t"
      "ds_read_b64_tr_b16 v[112:113], v168 offset:3072\n\t"
      "ds_read_b64_tr_b16 v[114:115], v168 offset:3584\n\t"
      "ds_read_b128 v[92:95], v163 offset:1024\n\t"
      "ds_read_b64_tr_b16 v[116:117], v169 offset:3072\n\t"
      "ds_read_b64_tr_b16 v[118:119], v169 offset:3584\n\t"
      "s_waitcnt vmcnt(0)\n\t"
      "s_barrier\n\t"
      "s_waitcnt lgkmcnt(14)\n\t"
      "v_mfma_f32_16x16x32_bf16 v[0:3], v[76:79], v[68:71], 0\n\t"
      "ds_read_b64_tr_b16 v[132:133], v168 offset:16384\n\t"
      "ds_read_b64_tr_b16 v[134:135], v168 offset:16896\n\t"
      "v_mfma_f32_16x16x32_bf16 v[8:11], v[76:79], v[72:75], 0\n\t"
      "ds_read_b128 v[124:127], v160 offset:2048\n\t"
      "v_mfma_f32_16x16x32_bf16 v[4:7], v[80:83], v[68:71], 0\n\t"
      "ds_read_b128 v[128:131], v160 offset:16384\n\t"
      "v_mfma_f32_16x16x32_bf16 v[12:15], v[80:83], v[72:75], 0\n\t"
      "ds_read_b64_tr_b16 v[136:137], v169 offset:16384\n\t"
      "v_mfma_f32_16x16x32_bf16 v[24:27], v[84:87], v[68:71], 0\n\t"
      "ds_read_b64_tr_b16 v[138:139], v169 offset:16896\n\t"
      "v_mfma_f32_16x16x32_bf16 v[32:35], v[84:87], v[72:75], 0\n\t"
      "ds_read_b64_tr_b16 v[140:141], v168 offset:17408\n\t"
      "s_waitcnt lgkmcnt(14)\n\t"
      "v_mfma_f32_16x16x32_bf16 v[16:19], v[84:87], v[64:67], 0\n\t"
      "ds_read_b64_tr_b16 v[142:143], v168 offset:17920\n\t"
      "v_mfma_f32_16x16x32_bf16 v[28:31], v[88:91], v[68:71], 0\n\t"
      "ds_read_b128 v[120:123], v162 offset:2048\n\t"
      "v_mfma_f32_16x16x32_bf16 v[36:39], v[88:91], v[72:75], 0\n\t"
      "ds_read_b64_tr_b16 v[144:145], v169 offset:17408\n\t"
      "v_mfma_f32_16x16x32_bf16 v[20:23], v[88:91], v[64:67], 0\n\t"
      "ds_read_b64_tr_b16 v[146:147], v169 offset:17920\n\t"
      "s_waitcnt lgkmcnt(11)\n\t"
      "v_mfma_f32_16x16x32_bf16 v[0:3], v[104:107], v[96:99], v[0:3]\n\t"
      "ds_read_b64_tr_b16 v[76:77], v168 offset:18432\n\t"
      "ds_read_b64_tr_b16 v[78:79], v168 offset:18944\n\t"
      "v_mfma_f32_16x16x32_bf16 v[8:11], v[104:107], v[100:103], v[8:11]\n\t"
      "ds_read_b128 v[68:71], v161 offset:3072\n\t"
      "v_mfma_f32_16x16x32_bf16 v[4:7], v[108:111], v[96:99], v[4:7]\n\t"
      "ds_read_b128 v[72:75], v161 offset:17408\n\t"
      "v_mfma_f32_16x16x32_bf16 v[12:15], v[108:111], v[100:103], v[12:15]\n\t"
      "ds_read_b64_tr_b16 v[80:81], v169 offset:18432\n\t"
      "v_mfma_f32_16x16x32_bf16 v[24:27], v[112:115], v[96:99], v[24:27]\n\t"
      "ds_read_b64_tr_b16 v[82:83], v169 offset:18944\n\t"
      "v_mfma_f32_16x16x32_bf16 v[32:35], v[112:115], v[100:103], v[32:35]\n\t"
      "ds_read_b64_tr_b16 v[84:85], v168 offset:19456\n\t"
      "v_mfma_f32_16x16x32_bf16 v[16:19], v[112:115], v[92:95], v[16:19]\n\t"
      "ds_read_b64_tr_b16 v[86:87], v168 offset:19968\n\t"
      "v_mfma_f32_16x16x32_bf16 v[28:31], v[116:119], v[96:99], v[28:31]\n\t"
      "ds_read_b128 v[64:67], v163 offset:3072\n\t"
      "v_mfma_f32_16x16x32_bf16 v[36:39], v[116:119], v[100:103], v[36:39]\n\t"
      "ds_read_b64_tr_b16 v[88:89], v169 offset:19456\n\t"
      "v_mfma_f32_16x16x32_bf16 v[20:23], v[116:119], v[92:95], v[20:23]\n\t"
      "ds_read_b64_tr_b16 v[90:91], v169 offset:19968\n\t"
      "s_barrier\n\t"
      "s_waitcnt lgkmcnt(11)\n\t"
      "v_mfma_f32_16x16x32_bf16 v[0:3], v[132:135], v[124:127], v[0:3]\n\t"
      "ds_read_b64_tr_b16 v[104:105], v166\n\t"
      "v_lshlrev_b32_e32 v199, 2, v193\n\t"
      "v_mfma_f32_16x16x32_bf16 v[8:11], v[132:135], v[128:131], v[8:11]\n\t"
      "ds_read_b64_tr_b16 v[106:107], v166 offset:512\n\t"
      "v_sub_u32_e32 v199, v194, v199\n\t"
      "v_mfma_f32_16x16x32_bf16 v[4:7], v[136:139], v[124:127], v[4:7]\n\t"
      "ds_read_b128 v[96:99], v160\n\t"
      "v_mov_b32_e32 v200, 0xff800000\n\t"
      "v_mfma_f32_16x16x32_bf16 v[12:15], v[136:139], v[128:131], v[12:15]\n\t"
      "ds_read_b128 v[100:103], v160 offset:14336\n\t"
      "s_cmp_lt_u32 s25, 2\n\t"
      "v_mfma_f32_16x16x32_bf16 v[24:27], v[140:143], v[124:127], v[24:27]\n\t"
      "ds_read_b128 v[92:95], v162\n\t"
      "s_cselect_b32 s35, 0xff800000, 0\n\t"
      "v_mfma_f32_16x16x32_bf16 v[32:35], v[140:143], v[128:131], v[32:35]\n\t"
      "ds_read_b64_tr_b16 v[108:109], v167\n\t"
      "s_bitcmp0_b32 s24, 0\n\t"
      "v_mfma_f32_16x16x32_bf16 v[16:19], v[140:143], v[120:123], v[16:19]\n\t"
      "ds_read_b64_tr_b16 v[110:111], v167 offset:512\n\t"
      "s_cselect_b32 s35, s35, 0\n\t"
      "v_mfma_f32_16x16x32_bf16 v[28:31], v[144:147], v[124:127], v[28:31]\n\t"
      "v_cmp_ge_i32_e32 vcc, -9, v199\n\t"
      "v_mfma_f32_16x16x32_bf16 v[36:39], v[144:147], v[128:131], v[36:39]\n\t"
      "v_subrev_u32_e32 v201, 0, v199\n\t"
      "v_mfma_f32_16x16x32_bf16 v[20:23], v[144:147], v[120:123], v[20:23]\n\t"
      "v_cmp_gt_u32_e64 s[64:65], 8, v201\n\t"
      "s_waitcnt lgkmcnt(7)\n\t"
      "v_mfma_f32_16x16x32_bf16 v[0:3], v[76:79], v[68:71], v[0:3]\n\t"
      "ds_read_b64_tr_b16 v[132:133], v166 offset:1024\n\t"
      "v_cndmask_b32_e64 v172, v200, 0, vcc\n\t"
      "v_mfma_f32_16x16x32_bf16 v[8:11], v[76:79], v[72:75], v[8:11]\n\t"
      "ds_read_b64_tr_b16 v[134:135], v166 offset:1536\n\t"
      "v_cndmask_b32_e64 v176, v200, 0, s[64:65]\n\t"
      "v_mfma_f32_16x16x32_bf16 v[4:7], v[80:83], v[68:71], v[4:7]\n\t"
      "ds_read_b128 v[124:127], v161 offset:1024\n\t"
      "v_or_b32_e32 v180, s35, v172\n\t"
      "v_mfma_f32_16x16x32_bf16 v[12:15], v[80:83], v[72:75], v[12:15]\n\t"
      "ds_read_b128 v[128:131], v161 offset:15360\n\t"
      "v_cmp_ge_i32_e32 vcc, -8, v199\n\t"
      "v_mfma_f32_16x16x32_bf16 v[24:27], v[84:87], v[68:71], v[24:27]\n\t"
      "ds_read_b128 v[120:123], v163 offset:1024\n\t"
      "v_subrev_u32_e32 v201, 1, v199\n\t"
      "v_mfma_f32_16x16x32_bf16 v[32:35], v[84:87], v[72:75], v[32:35]\n\t"
      "ds_read_b64_tr_b16 v[136:137], v167 offset:1024\n\t"
      "v_cmp_gt_u32_e64 s[64:65], 8, v201\n\t"
      "v_mfma_f32_16x16x32_bf16 v[16:19], v[84:87], v[64:67], v[16:19]\n\t"
      "ds_read_b64_tr_b16 v[138:139], v167 offset:1536\n\t"
      "v_cndmask_b32_e64 v173, v200, 0, vcc\n\t"
      "v_mfma_f32_16x16x32_bf16 v[28:31], v[88:91], v[68:71], v[28:31]\n\t"
      "v_cndmask_b32_e64 v177, v200, 0, s[64:65]\n\t"
      "v_mfma_f32_16x16x32_bf16 v[36:39], v[88:91], v[72:75], v[36:39]\n\t"
      "v_or_b32_e32 v181, s35, v173\n\t"
      "v_mfma_f32_16x16x32_bf16 v[20:23], v[88:91], v[64:67], v[20:23]\n\t"
      "v_cmp_ge_i32_e32 vcc, -7, v199\n\t"
      "s_waitcnt lgkmcnt(7)\n\t"
      "v_mfma_f32_16x16x32_bf16 v[48:51], v[96:99], v[104:107], 0\n\t"
      "ds_read_b64_tr_b16 v[76:77], v166 offset:2048\n\t"
      "ds_read_b64_tr_b16 v[78:79], v166 offset:2560\n\t"
      "v_subrev_u32_e32 v201, 2, v199\n\t"
      "v_mfma_f32_16x16x32_bf16 v[56:59], v[100:103], v[104:107], 0\n\t"
      "ds_read_b128 v[68:71], v160 offset:2048\n\t"
      "v_cmp_gt_u32_e64 s[64:65], 8, v201\n\t"
      "v_mfma_f32_16x16x32_bf16 v[40:43], v[92:95], v[104:107], 0\n\t"
      "ds_read_b128 v[72:75], v160 offset:16384\n\t"
      "v_cndmask_b32_e64 v174, v200, 0, vcc\n\t"
      "v_mfma_f32_16x16x32_bf16 v[52:55], v[96:99], v[108:111], 0\n\t"
      "ds_read_b128 v[64:67], v162 offset:2048\n\t"
      "v_cndmask_b32_e64 v178, v200, 0, s[64:65]\n\t"
      "v_mfma_f32_16x16x32_bf16 v[60:63], v[100:103], v[108:111], 0\n\t"
      "ds_read_b64_tr_b16 v[80:81], v167 offset:2048\n\t"
      "v_or_b32_e32 v182, s35, v174\n\t"
      "v_mfma_f32_16x16x32_bf16 v[44:47], v[92:95], v[108:111], 0\n\t"
      "ds_read_b64_tr_b16 v[82:83], v167 offset:2560\n\t"
      "v_cmp_ge_i32_e32 vcc, -6, v199\n\t"
      "s_waitcnt lgkmcnt(7)\n\t"
      "v_mfma_f32_16x16x32_bf16 v[48:51], v[124:127], v[132:135], v[48:51]\n\t"
      "ds_read_b64_tr_b16 v[104:105], v166 offset:3072\n\t"
      "ds_read_b64_tr_b16 v[106:107], v166 offset:3584\n\t"
      "v_subrev_u32_e32 v201, 3, v199\n\t"
      "v_mfma_f32_16x16x32_bf16 v[56:59], v[128:131], v[132:135], v[56:59]\n\t"
      "ds_read_b128 v[96:99], v161 offset:3072\n\t"
      "v_cmp_gt_u32_e64 s[64:65], 8, v201\n\t"
      "v_mfma_f32_16x16x32_bf16 v[40:43], v[120:123], v[132:135], v[40:43]\n\t"
      "ds_read_b128 v[100:103], v161 offset:17408\n\t"
      "v_cndmask_b32_e64 v175, v200, 0, vcc\n\t"
      "v_mfma_f32_16x16x32_bf16 v[52:55], v[124:127], v[136:139], v[52:55]\n\t"
      "ds_read_b128 v[92:95], v163 offset:3072\n\t"
      "v_cndmask_b32_e64 v179, v200, 0, s[64:65]\n\t"
      "v_mfma_f32_16x16x32_bf16 v[60:63], v[128:131], v[136:139], v[60:63]\n\t"
      "ds_read_b64_tr_b16 v[108:109], v167 offset:3072\n\t"
      "v_or_b32_e32 v183, s35, v175\n\t"
      "v_mfma_f32_16x16x32_bf16 v[44:47], v[120:123], v[136:139], v[44:47]\n\t"
      "ds_read_b64_tr_b16 v[110:111], v167 offset:3584\n\t"
      "v_lshrrev_b32_e32 v201, 31, v172\n\t"
      "s_barrier\n\t"
      "s_waitcnt lgkmcnt(7)\n\t"
      "v_mfma_f32_16x16x32_bf16 v[48:51], v[68:71], v[76:79], v[48:51]\n\t"
      "ds_read_b64_tr_b16 v[132:133], v166 offset:16384\n\t"
      "ds_read_b64_tr_b16 v[134:135], v166 offset:16896\n\t"
      "ds_read_b128 v[124:127], v160 offset:4096\n\t"
      "ds_read_b128 v[128:131], v160 offset:18432\n\t"
      "ds_read_b64_tr_b16 v[136:137], v167 offset:16384\n\t"
      "ds_read_b64_tr_b16 v[138:139], v167 offset:16896\n\t"
      "v_lshrrev_b32_e32 v202, 31, v173\n\t"
      "v_mfma_f32_16x16x32_bf16 v[56:59], v[72:75], v[76:79], v[56:59]\n\t"
      "ds_read_b64_tr_b16 v[140:141], v166 offset:17408\n\t"
      "v_lshl_or_b32 v201, v202, 16, v201\n\t"
      "v_mfma_f32_16x16x32_bf16 v[40:43], v[64:67], v[76:79], v[40:43]\n\t"
      "ds_read_b64_tr_b16 v[142:143], v166 offset:17920\n\t"
      "v_mul_u32_u24_e32 v184, 0xffff, v201\n\t"
      "v_mfma_f32_16x16x32_bf16 v[52:55], v[68:71], v[80:83], v[52:55]\n\t"
      "ds_read_b128 v[120:123], v162 offset:4096\n\t"
      "v_lshrrev_b32_e32 v201, 31, v174\n\t"
      "v_mfma_f32_16x16x32_bf16 v[60:63], v[72:75], v[80:83], v[60:63]\n\t"
      "ds_read_b64_tr_b16 v[144:145], v167 offset:17408\n\t"
      "v_lshrrev_b32_e32 v202, 31, v175\n\t"
      "v_mfma_f32_16x16x32_bf16 v[44:47], v[64:67], v[80:83], v[44:47]\n\t"
      "ds_read_b64_tr_b16 v[146:147], v167 offset:17920\n\t"
      "v_lshl_or_b32 v201, v202, 16, v201\n\t"
      "s_waitcnt lgkmcnt(11)\n\t"
      "v_mfma_f32_16x16x32_bf16 v[48:51], v[96:99], v[104:107], v[48:51]\n\t"
      "ds_read_b64_tr_b16 v[76:77], v166 offset:18432\n\t"
      "ds_read_b64_tr_b16 v[78:79], v166 offset:18944\n\t"
      "ds_read_b128 v[68:71], v161 offset:5120\n\t"
      "ds_read_b128 v[72:75], v161 offset:19456\n\t"
      "ds_read_b64_tr_b16 v[80:81], v167 offset:18432\n\t"
      "ds_read_b64_tr_b16 v[82:83], v167 offset:18944\n\t"
      "v_mul_u32_u24_e32 v185, 0xffff, v201\n\t"
      "v_mfma_f32_16x16x32_bf16 v[56:59], v[100:103], v[104:107], v[56:59]\n\t"
      "ds_read_b64_tr_b16 v[84:85], v166 offset:19456\n\t"
      "s_mov_b32 s68, 0x0\n\t"
      "v_mfma_f32_16x16x32_bf16 v[40:43], v[92:95], v[104:107], v[40:43]\n\t"
      "ds_read_b64_tr_b16 v[86:87], v166 offset:19968\n\t"
      "s_mov_b32 s69, 0xffff0000\n\t"
      "v_mfma_f32_16x16x32_bf16 v[52:55], v[96:99], v[108:111], v[52:55]\n\t"
      "ds_read_b128 v[64:67], v163 offset:5120\n\t"
      "s_mov_b32 s70, 0xffff\n\t"
      "v_mfma_f32_16x16x32_bf16 v[60:63], v[100:103], v[108:111], v[60:63]\n\t"
      "ds_read_b64_tr_b16 v[88:89], v167 offset:19456\n\t"
      "s_mov_b32 s71, 0x0\n\t"
      "v_mfma_f32_16x16x32_bf16 v[44:47], v[92:95], v[108:111], v[44:47]\n\t"
      "ds_read_b64_tr_b16 v[90:91], v167 offset:19968\n\t"
      "v_lshlrev_b32_e32 v231, 3, v192\n\t"
      "s_barrier\n\t"
      "s_waitcnt lgkmcnt(11)\n\t"
      "v_mfma_f32_16x16x32_bf16 v[0:3], v[132:135], v[124:127], v[0:3]\n\t"
      "ds_read_b64_tr_b16 v[104:105], v166 offset:32768\n\t"
      "ds_read_b64_tr_b16 v[106:107], v166 offset:33280\n\t"
      "s_lshl_b32 s34, s25, 12\n\t"
      "v_mfma_f32_16x16x32_bf16 v[8:11], v[132:135], v[128:131], v[8:11]\n\t"
      "ds_read_b128 v[96:99], v160 offset:6144\n\t"
      "s_lshr_b32 s35, s39, 2\n\t"
      "v_mfma_f32_16x16x32_bf16 v[4:7], v[136:139], v[124:127], v[4:7]\n\t"
      "ds_read_b128 v[100:103], v160 offset:20480\n\t"
      "s_add_u32 s34, s34, s35\n\t"
      "v_mfma_f32_16x16x32_bf16 v[12:15], v[136:139], v[128:131], v[12:15]\n\t"
      "ds_read_b64_tr_b16 v[108:109], v167 offset:32768\n\t"
      "s_add_u32 s34, s34, 12582912\n\t"
      "v_mfma_f32_16x16x32_bf16 v[24:27], v[140:143], v[124:127], v[24:27]\n\t"
      "ds_read_b64_tr_b16 v[110:111], v167 offset:33280\n\t"
      "s_add_u32 s46, s20, s34\n\t"
      "v_mfma_f32_16x16x32_bf16 v[32:35], v[140:143], v[128:131], v[32:35]\n\t"
      "ds_read_b64_tr_b16 v[112:113], v166 offset:33792\n\t"
      "s_addc_u32 s47, s21, 0\n\t"
      "v_mfma_f32_16x16x32_bf16 v[16:19], v[140:143], v[120:123], v[16:19]\n\t"
      "ds_read_b64_tr_b16 v[114:115], v166 offset:34304\n\t"
      "s_lshl_b32 s34, s28, 7\n\t"
      "v_mfma_f32_16x16x32_bf16 v[28:31], v[144:147], v[124:127], v[28:31]\n\t"
      "ds_read_b128 v[92:95], v162 offset:6144\n\t"
      "s_add_u32 s58, s46, s34\n\t"
      "v_mfma_f32_16x16x32_bf16 v[36:39], v[144:147], v[128:131], v[36:39]\n\t"
      "ds_read_b64_tr_b16 v[116:117], v167 offset:33792\n\t"
      "s_addc_u32 s59, s47, 0\n\t"
      "v_mfma_f32_16x16x32_bf16 v[20:23], v[144:147], v[120:123], v[20:23]\n\t"
      "ds_read_b64_tr_b16 v[118:119], v167 offset:34304\n\t"
      "s_and_b32 s34, s24, 1\n\t"
      "s_waitcnt lgkmcnt(11)\n\t"
      "v_mfma_f32_16x16x32_bf16 v[0:3], v[76:79], v[68:71], v[0:3]\n\t"
      "ds_read_b64_tr_b16 v[132:133], v166 offset:34816\n\t"
      "ds_read_b64_tr_b16 v[134:135], v166 offset:35328\n\t"
      "s_lshl1_add_u32 s34, s25, s34\n\t"
      "v_mfma_f32_16x16x32_bf16 v[8:11], v[76:79], v[72:75], v[8:11]\n\t"
      "ds_read_b128 v[124:127], v161 offset:7168\n\t"
      "s_lshl_b32 s34, s34, 5\n\t"
      "v_mfma_f32_16x16x32_bf16 v[4:7], v[80:83], v[68:71], v[4:7]\n\t"
      "ds_read_b128 v[128:131], v161 offset:21504\n\t"
      "s_lshl1_add_u32 s34, s26, s34\n\t"
      "v_mfma_f32_16x16x32_bf16 v[12:15], v[80:83], v[72:75], v[12:15]\n\t"
      "ds_read_b64_tr_b16 v[136:137], v167 offset:34816\n\t"
      "s_bfe_u32 s35, s24, 65537\n\t"
      "v_mfma_f32_16x16x32_bf16 v[24:27], v[84:87], v[68:71], v[24:27]\n\t"
      "ds_read_b64_tr_b16 v[138:139], v167 offset:35328\n\t"
      "s_add_u32 s34, s34, s35\n\t"
      "v_mfma_f32_16x16x32_bf16 v[32:35], v[84:87], v[72:75], v[32:35]\n\t"
      "ds_read_b64_tr_b16 v[140:141], v166 offset:35840\n\t"
      "s_lshl_b32 s34, s34, 10\n\t"
      "v_mfma_f32_16x16x32_bf16 v[16:19], v[84:87], v[64:67], v[16:19]\n\t"
      "ds_read_b64_tr_b16 v[142:143], v166 offset:36352\n\t"
      "s_add_u32 s42, s20, s34\n\t"
      "v_mfma_f32_16x16x32_bf16 v[28:31], v[88:91], v[68:71], v[28:31]\n\t"
      "ds_read_b128 v[120:123], v165 offset:7168\n\t"
      "s_addc_u32 s43, s21, 0\n\t"
      "v_mfma_f32_16x16x32_bf16 v[36:39], v[88:91], v[72:75], v[36:39]\n\t"
      "ds_read_b64_tr_b16 v[144:145], v167 offset:35840\n\t"
      "s_add_u32 s44, s42, 16384\n\t"
      "v_mfma_f32_16x16x32_bf16 v[20:23], v[88:91], v[64:67], v[20:23]\n\t"
      "ds_read_b64_tr_b16 v[146:147], v167 offset:36352\n\t"
      "s_addc_u32 s45, s43, 0\n\t"
      "s_barrier\n\t"
      "s_waitcnt lgkmcnt(11)\n\t"
      "v_mfma_f32_16x16x32_bf16 v[0:3], v[104:107], v[96:99], v[0:3]\n\t"
      "ds_read_b64_tr_b16 v[76:77], v166 offset:49152\n\t"
      "ds_read_b64_tr_b16 v[78:79], v166 offset:49664\n\t"
      "v_and_b32_e32 v199, 60, v192\n\t"
      "v_mfma_f32_16x16x32_bf16 v[8:11], v[104:107], v[100:103], v[8:11]\n\t"
      "ds_read_b128 v[68:71], v160 offset:8192\n\t"
      "v_and_b32_e32 v200, 24, v192\n\t"
      "v_mfma_f32_16x16x32_bf16 v[4:7], v[108:111], v[96:99], v[4:7]\n\t"
      "ds_read_b128 v[72:75], v160 offset:22528\n\t"
      "v_lshlrev_b32_e32 v202, 3, v196\n\t"
      "v_mfma_f32_16x16x32_bf16 v[12:15], v[108:111], v[100:103], v[12:15]\n\t"
      "ds_read_b64_tr_b16 v[80:81], v167 offset:49152\n\t"
      "v_lshl_or_b32 v202, v200, 2, v202\n\t"
      "v_mfma_f32_16x16x32_bf16 v[24:27], v[112:115], v[96:99], v[24:27]\n\t"
      "ds_read_b64_tr_b16 v[82:83], v167 offset:49664\n\t"
      "v_lshl_or_b32 v201, v199, 5, v202\n\t"
      "v_mfma_f32_16x16x32_bf16 v[32:35], v[112:115], v[100:103], v[32:35]\n\t"
      "ds_read_b64_tr_b16 v[84:85], v166 offset:50176\n\t"
      "v_add_u32_e32 v216, 106496, v201\n\t"
      "v_mfma_f32_16x16x32_bf16 v[16:19], v[112:115], v[92:95], v[16:19]\n\t"
      "ds_read_b64_tr_b16 v[86:87], v166 offset:50688\n\t"
      "v_mov_b32_e32 v199, 2048\n\t"
      "v_mfma_f32_16x16x32_bf16 v[28:31], v[116:119], v[96:99], v[28:31]\n\t"
      "ds_read_b128 v[64:67], v164 offset:8192\n\t"
      "v_cndmask_b32_e64 v199, v199, 0, s[68:69]\n\t"
      "v_mfma_f32_16x16x32_bf16 v[36:39], v[116:119], v[100:103], v[36:39]\n\t"
      "ds_read_b64_tr_b16 v[88:89], v167 offset:50176\n\t"
      "v_add_u32_e32 v220, v216, v199\n\t"
      "v_mfma_f32_16x16x32_bf16 v[20:23], v[116:119], v[92:95], v[20:23]\n\t"
      "ds_read_b64_tr_b16 v[90:91], v167 offset:50688\n\t"
      "v_xor_b32_e32 v217, 32, v216\n\t"
      "s_waitcnt lgkmcnt(11)\n\t"
      "v_mfma_f32_16x16x32_bf16 v[0:3], v[132:135], v[124:127], v[0:3]\n\t"
      "ds_read_b64_tr_b16 v[104:105], v166 offset:51200\n\t"
      "ds_read_b64_tr_b16 v[106:107], v166 offset:51712\n\t"
      "v_xor_b32_e32 v218, 64, v216\n\t"
      "v_mfma_f32_16x16x32_bf16 v[8:11], v[132:135], v[128:131], v[8:11]\n\t"
      "ds_read_b128 v[96:99], v161 offset:9216\n\t"
      "v_xor_b32_e32 v219, 96, v216\n\t"
      "v_mfma_f32_16x16x32_bf16 v[4:7], v[136:139], v[124:127], v[4:7]\n\t"
      "ds_read_b128 v[100:103], v161 offset:23552\n\t"
      "v_xor_b32_e32 v221, 32, v220\n\t"
      "v_mfma_f32_16x16x32_bf16 v[12:15], v[136:139], v[128:131], v[12:15]\n\t"
      "ds_read_b64_tr_b16 v[108:109], v167 offset:51200\n\t"
      "v_xor_b32_e32 v222, 64, v220\n\t"
      "v_mfma_f32_16x16x32_bf16 v[24:27], v[140:143], v[124:127], v[24:27]\n\t"
      "ds_read_b64_tr_b16 v[110:111], v167 offset:51712\n\t"
      "v_xor_b32_e32 v223, 96, v220\n\t"
      "v_mfma_f32_16x16x32_bf16 v[32:35], v[140:143], v[128:131], v[32:35]\n\t"
      "ds_read_b64_tr_b16 v[112:113], v166 offset:52224\n\t"
      "s_lshr_b32 s34, s39, 1\n\t"
      "v_mfma_f32_16x16x32_bf16 v[16:19], v[140:143], v[120:123], v[16:19]\n\t"
      "ds_read_b64_tr_b16 v[114:115], v166 offset:52736\n\t"
      "s_add_u32 s34, s34, 98304\n\t"
      "v_mfma_f32_16x16x32_bf16 v[28:31], v[144:147], v[124:127], v[28:31]\n\t"
      "ds_read_b128 v[92:95], v165 offset:9216\n\t"
      "v_lshrrev_b32_e32 v199, 1, v193\n\t"
      "v_mfma_f32_16x16x32_bf16 v[36:39], v[144:147], v[128:131], v[36:39]\n\t"
      "ds_read_b64_tr_b16 v[116:117], v167 offset:52224\n\t"
      "v_xor_b32_e32 v199, v199, v198\n\t"
      "v_mfma_f32_16x16x32_bf16 v[20:23], v[144:147], v[120:123], v[20:23]\n\t"
      "ds_read_b64_tr_b16 v[118:119], v167 offset:52736\n\t"
      "v_lshlrev_b32_e32 v199, 4, v199\n\t"
      "s_barrier\n\t"
      "s_waitcnt lgkmcnt(11)\n\t"
      "v_mfma_f32_16x16x32_bf16 v[0:3], v[76:79], v[68:71], v[0:3]\n\t"
      "ds_read_b64_tr_b16 v[132:133], v168\n\t"
      "ds_read_b64_tr_b16 v[134:135], v168 offset:512\n\t"
      "v_lshl_or_b32 v199, v194, 7, v199\n\t"
      "v_mfma_f32_16x16x32_bf16 v[8:11], v[76:79], v[72:75], v[8:11]\n\t"
      "ds_read_b128 v[124:127], v160 offset:10240\n\t"
      "v_and_b32_e32 v200, 1, v193\n\t"
      "v_mfma_f32_16x16x32_bf16 v[4:7], v[80:83], v[68:71], v[4:7]\n\t"
      "ds_read_b128 v[128:131], v160 offset:24576\n\t"
      "v_lshl_or_b32 v199, v200, 3, v199\n\t"
      "v_mfma_f32_16x16x32_bf16 v[12:15], v[80:83], v[72:75], v[12:15]\n\t"
      "ds_read_b64_tr_b16 v[136:137], v169\n\t"
      "v_add_u32_e32 v228, s34, v199\n\t"
      "v_mfma_f32_16x16x32_bf16 v[24:27], v[84:87], v[68:71], v[24:27]\n\t"
      "ds_read_b64_tr_b16 v[138:139], v169 offset:512\n\t"
      "v_mfma_f32_16x16x32_bf16 v[32:35], v[84:87], v[72:75], v[32:35]\n\t"
      "ds_read_b64_tr_b16 v[140:141], v168 offset:1024\n\t"
      "v_mfma_f32_16x16x32_bf16 v[16:19], v[84:87], v[64:67], v[16:19]\n\t"
      "ds_read_b64_tr_b16 v[142:143], v168 offset:1536\n\t"
      "v_mfma_f32_16x16x32_bf16 v[28:31], v[88:91], v[68:71], v[28:31]\n\t"
      "ds_read_b128 v[120:123], v164 offset:10240\n\t"
      "v_mfma_f32_16x16x32_bf16 v[36:39], v[88:91], v[72:75], v[36:39]\n\t"
      "ds_read_b64_tr_b16 v[144:145], v169 offset:1024\n\t"
      "v_mfma_f32_16x16x32_bf16 v[20:23], v[88:91], v[64:67], v[20:23]\n\t"
      "ds_read_b64_tr_b16 v[146:147], v169 offset:1536\n\t"
      "s_waitcnt lgkmcnt(11)\n\t"
      "v_mfma_f32_16x16x32_bf16 v[0:3], v[104:107], v[96:99], v[0:3]\n\t"
      "ds_read_b64_tr_b16 v[76:77], v168 offset:2048\n\t"
      "ds_read_b64_tr_b16 v[78:79], v168 offset:2560\n\t"
      "v_mfma_f32_16x16x32_bf16 v[8:11], v[104:107], v[100:103], v[8:11]\n\t"
      "ds_read_b128 v[68:71], v161 offset:11264\n\t"
      "v_mfma_f32_16x16x32_bf16 v[4:7], v[108:111], v[96:99], v[4:7]\n\t"
      "ds_read_b128 v[72:75], v161 offset:25600\n\t"
      "v_mfma_f32_16x16x32_bf16 v[12:15], v[108:111], v[100:103], v[12:15]\n\t"
      "ds_read_b64_tr_b16 v[80:81], v169 offset:2048\n\t"
      "v_mfma_f32_16x16x32_bf16 v[24:27], v[112:115], v[96:99], v[24:27]\n\t"
      "ds_read_b64_tr_b16 v[82:83], v169 offset:2560\n\t"
      "v_mfma_f32_16x16x32_bf16 v[32:35], v[112:115], v[100:103], v[32:35]\n\t"
      "ds_read_b64_tr_b16 v[84:85], v168 offset:3072\n\t"
      "v_mfma_f32_16x16x32_bf16 v[16:19], v[112:115], v[92:95], v[16:19]\n\t"
      "ds_read_b64_tr_b16 v[86:87], v168 offset:3584\n\t"
      "v_mfma_f32_16x16x32_bf16 v[28:31], v[116:119], v[96:99], v[28:31]\n\t"
      "ds_read_b128 v[64:67], v165 offset:11264\n\t"
      "v_mfma_f32_16x16x32_bf16 v[36:39], v[116:119], v[100:103], v[36:39]\n\t"
      "ds_read_b64_tr_b16 v[88:89], v169 offset:3072\n\t"
      "v_mfma_f32_16x16x32_bf16 v[20:23], v[116:119], v[92:95], v[20:23]\n\t"
      "ds_read_b64_tr_b16 v[90:91], v169 offset:3584\n\t"
      "s_barrier\n\t"
      "s_waitcnt lgkmcnt(11)\n\t"
      "v_mfma_f32_16x16x32_bf16 v[0:3], v[132:135], v[124:127], v[0:3]\n\t"
      "ds_read_b64_tr_b16 v[104:105], v168 offset:16384\n\t"
      "ds_read_b64_tr_b16 v[106:107], v168 offset:16896\n\t"
      "v_mfma_f32_16x16x32_bf16 v[8:11], v[132:135], v[128:131], v[8:11]\n\t"
      "ds_read_b128 v[96:99], v160 offset:12288\n\t"
      "v_mfma_f32_16x16x32_bf16 v[4:7], v[136:139], v[124:127], v[4:7]\n\t"
      "ds_read_b128 v[100:103], v160 offset:26624\n\t"
      "v_mfma_f32_16x16x32_bf16 v[12:15], v[136:139], v[128:131], v[12:15]\n\t"
      "ds_read_b64_tr_b16 v[108:109], v169 offset:16384\n\t"
      "v_mfma_f32_16x16x32_bf16 v[24:27], v[140:143], v[124:127], v[24:27]\n\t"
      "ds_read_b64_tr_b16 v[110:111], v169 offset:16896\n\t"
      "v_mfma_f32_16x16x32_bf16 v[32:35], v[140:143], v[128:131], v[32:35]\n\t"
      "ds_read_b64_tr_b16 v[112:113], v168 offset:17408\n\t"
      "v_mfma_f32_16x16x32_bf16 v[16:19], v[140:143], v[120:123], v[16:19]\n\t"
      "ds_read_b64_tr_b16 v[114:115], v168 offset:17920\n\t"
      "v_mfma_f32_16x16x32_bf16 v[28:31], v[144:147], v[124:127], v[28:31]\n\t"
      "ds_read_b128 v[92:95], v164 offset:12288\n\t"
      "v_mfma_f32_16x16x32_bf16 v[36:39], v[144:147], v[128:131], v[36:39]\n\t"
      "ds_read_b64_tr_b16 v[116:117], v169 offset:17408\n\t"
      "v_mfma_f32_16x16x32_bf16 v[20:23], v[144:147], v[120:123], v[20:23]\n\t"
      "ds_read_b64_tr_b16 v[118:119], v169 offset:17920\n\t"
      "s_waitcnt lgkmcnt(11)\n\t"
      "v_mfma_f32_16x16x32_bf16 v[0:3], v[76:79], v[68:71], v[0:3]\n\t"
      "ds_read_b64_tr_b16 v[132:133], v168 offset:18432\n\t"
      "ds_read_b64_tr_b16 v[134:135], v168 offset:18944\n\t"
      "v_mfma_f32_16x16x32_bf16 v[8:11], v[76:79], v[72:75], v[8:11]\n\t"
      "ds_read_b128 v[124:127], v161 offset:13312\n\t"
      "v_mfma_f32_16x16x32_bf16 v[4:7], v[80:83], v[68:71], v[4:7]\n\t"
      "ds_read_b128 v[128:131], v161 offset:27648\n\t"
      "v_mfma_f32_16x16x32_bf16 v[12:15], v[80:83], v[72:75], v[12:15]\n\t"
      "ds_read_b64_tr_b16 v[136:137], v169 offset:18432\n\t"
      "v_mfma_f32_16x16x32_bf16 v[24:27], v[84:87], v[68:71], v[24:27]\n\t"
      "ds_read_b64_tr_b16 v[138:139], v169 offset:18944\n\t"
      "v_mfma_f32_16x16x32_bf16 v[32:35], v[84:87], v[72:75], v[32:35]\n\t"
      "ds_read_b64_tr_b16 v[140:141], v168 offset:19456\n\t"
      "v_mfma_f32_16x16x32_bf16 v[16:19], v[84:87], v[64:67], v[16:19]\n\t"
      "ds_read_b64_tr_b16 v[142:143], v168 offset:19968\n\t"
      "v_mfma_f32_16x16x32_bf16 v[28:31], v[88:91], v[68:71], v[28:31]\n\t"
      "ds_read_b128 v[120:123], v165 offset:13312\n\t"
      "v_mfma_f32_16x16x32_bf16 v[36:39], v[88:91], v[72:75], v[36:39]\n\t"
      "ds_read_b64_tr_b16 v[144:145], v169 offset:19456\n\t"
      "v_mfma_f32_16x16x32_bf16 v[20:23], v[88:91], v[64:67], v[20:23]\n\t"
      "ds_read_b64_tr_b16 v[146:147], v169 offset:19968\n\t"
      "s_barrier\n\t"
      "s_waitcnt lgkmcnt(11)\n\t"
      "v_mfma_f32_16x16x32_bf16 v[0:3], v[104:107], v[96:99], v[0:3]\n\t"
      "ds_read_b64_tr_b16 v[76:77], v166\n\t"
      "v_mfma_f32_16x16x32_bf16 v[8:11], v[104:107], v[100:103], v[8:11]\n\t"
      "ds_read_b64_tr_b16 v[78:79], v166 offset:512\n\t"
      "v_mfma_f32_16x16x32_bf16 v[4:7], v[108:111], v[96:99], v[4:7]\n\t"
      "ds_read_b128 v[68:71], v160 offset:4096\n\t"
      "v_mfma_f32_16x16x32_bf16 v[12:15], v[108:111], v[100:103], v[12:15]\n\t"
      "ds_read_b128 v[72:75], v160 offset:18432\n\t"
      "v_mfma_f32_16x16x32_bf16 v[24:27], v[112:115], v[96:99], v[24:27]\n\t"
      "ds_read_b128 v[64:67], v162 offset:4096\n\t"
      "v_mfma_f32_16x16x32_bf16 v[32:35], v[112:115], v[100:103], v[32:35]\n\t"
      "ds_read_b64_tr_b16 v[80:81], v167\n\t"
      "v_mfma_f32_16x16x32_bf16 v[16:19], v[112:115], v[92:95], v[16:19]\n\t"
      "ds_read_b64_tr_b16 v[82:83], v167 offset:512\n\t"
      "v_mfma_f32_16x16x32_bf16 v[28:31], v[116:119], v[96:99], v[28:31]\n\t"
      "v_mfma_f32_16x16x32_bf16 v[36:39], v[116:119], v[100:103], v[36:39]\n\t"
      "v_mfma_f32_16x16x32_bf16 v[20:23], v[116:119], v[92:95], v[20:23]\n\t"
      "s_waitcnt lgkmcnt(7)\n\t"
      "v_mfma_f32_16x16x32_bf16 v[0:3], v[132:135], v[124:127], v[0:3]\n\t"
      "ds_read_b64_tr_b16 v[104:105], v166 offset:1024\n\t"
      "v_mfma_f32_16x16x32_bf16 v[8:11], v[132:135], v[128:131], v[8:11]\n\t"
      "ds_read_b64_tr_b16 v[106:107], v166 offset:1536\n\t"
      "v_mfma_f32_16x16x32_bf16 v[4:7], v[136:139], v[124:127], v[4:7]\n\t"
      "ds_read_b128 v[96:99], v161 offset:5120\n\t"
      "v_mfma_f32_16x16x32_bf16 v[12:15], v[136:139], v[128:131], v[12:15]\n\t"
      "ds_read_b128 v[100:103], v161 offset:19456\n\t"
      "v_mfma_f32_16x16x32_bf16 v[24:27], v[140:143], v[124:127], v[24:27]\n\t"
      "ds_read_b128 v[92:95], v163 offset:5120\n\t"
      "v_mfma_f32_16x16x32_bf16 v[32:35], v[140:143], v[128:131], v[32:35]\n\t"
      "ds_read_b64_tr_b16 v[108:109], v167 offset:1024\n\t"
      "v_mfma_f32_16x16x32_bf16 v[16:19], v[140:143], v[120:123], v[16:19]\n\t"
      "ds_read_b64_tr_b16 v[110:111], v167 offset:1536\n\t"
      "v_mfma_f32_16x16x32_bf16 v[28:31], v[144:147], v[124:127], v[28:31]\n\t"
      "v_mfma_f32_16x16x32_bf16 v[36:39], v[144:147], v[128:131], v[36:39]\n\t"
      "v_mfma_f32_16x16x32_bf16 v[20:23], v[144:147], v[120:123], v[20:23]\n\t"
      "s_waitcnt lgkmcnt(7)\n\t"
      "v_mfma_f32_16x16x32_bf16 v[48:51], v[68:71], v[76:79], v[48:51]\n\t"
      "ds_read_b64_tr_b16 v[132:133], v166 offset:2048\n\t"
      "ds_read_b64_tr_b16 v[134:135], v166 offset:2560\n\t"
      "v_mfma_f32_16x16x32_bf16 v[56:59], v[72:75], v[76:79], v[56:59]\n\t"
      "ds_read_b128 v[124:127], v160 offset:6144\n\t"
      "v_mfma_f32_16x16x32_bf16 v[40:43], v[64:67], v[76:79], v[40:43]\n\t"
      "ds_read_b128 v[128:131], v160 offset:20480\n\t"
      "v_cvt_pk_bf16_f32 v84, v0, v1\n\t"
      "v_cvt_pk_bf16_f32 v85, v2, v3\n\t"
      "v_cvt_pk_bf16_f32 v86, v4, v5\n\t"
      "v_mfma_f32_16x16x32_bf16 v[52:55], v[68:71], v[80:83], v[52:55]\n\t"
      "ds_read_b128 v[120:123], v162 offset:6144\n\t"
      "v_cvt_pk_bf16_f32 v87, v6, v7\n\t"
      "v_cvt_pk_bf16_f32 v88, v8, v9\n\t"
      "v_cvt_pk_bf16_f32 v89, v10, v11\n\t"
      "v_mfma_f32_16x16x32_bf16 v[60:63], v[72:75], v[80:83], v[60:63]\n\t"
      "ds_read_b64_tr_b16 v[136:137], v167 offset:2048\n\t"
      "v_cvt_pk_bf16_f32 v90, v12, v13\n\t"
      "v_cvt_pk_bf16_f32 v91, v14, v15\n\t"
      "v_cvt_pk_bf16_f32 v152, v24, v25\n\t"
      "v_mfma_f32_16x16x32_bf16 v[44:47], v[64:67], v[80:83], v[44:47]\n\t"
      "ds_read_b64_tr_b16 v[138:139], v167 offset:2560\n\t"
      "v_cvt_pk_bf16_f32 v153, v26, v27\n\t"
      "v_cvt_pk_bf16_f32 v154, v28, v29\n\t"
      "v_cvt_pk_bf16_f32 v155, v30, v31\n\t"
      "s_waitcnt lgkmcnt(7)\n\t"
      "v_mfma_f32_16x16x32_bf16 v[48:51], v[96:99], v[104:107], v[48:51]\n\t"
      "ds_read_b64_tr_b16 v[76:77], v166 offset:3072\n\t"
      "ds_read_b64_tr_b16 v[78:79], v166 offset:3584\n\t"
      "v_cndmask_b32_e64 v86, v86, 0, s[68:69]\n\t"
      "v_cndmask_b32_e64 v87, v87, 0, s[68:69]\n\t"
      "v_cvt_pk_bf16_f32 v156, v32, v33\n\t"
      "v_mfma_f32_16x16x32_bf16 v[56:59], v[100:103], v[104:107], v[56:59]\n\t"
      "ds_read_b128 v[68:71], v161 offset:7168\n\t"
      "v_cvt_pk_bf16_f32 v157, v34, v35\n\t"
      "v_cvt_pk_bf16_f32 v158, v36, v37\n\t"
      "v_cvt_pk_bf16_f32 v159, v38, v39\n\t"
      "v_mfma_f32_16x16x32_bf16 v[40:43], v[92:95], v[104:107], v[40:43]\n\t"
      "ds_read_b128 v[72:75], v161 offset:21504\n\t"
      "v_cndmask_b32_e64 v90, v90, 0, s[68:69]\n\t"
      "v_cndmask_b32_e64 v91, v91, 0, s[68:69]\n\t"
      "v_cvt_pk_bf16_f32 v148, v16, v17\n\t"
      "v_mfma_f32_16x16x32_bf16 v[52:55], v[96:99], v[108:111], v[52:55]\n\t"
      "ds_read_b128 v[64:67], v165 offset:7168\n\t"
      "v_cvt_pk_bf16_f32 v149, v18, v19\n\t"
      "v_cvt_pk_bf16_f32 v150, v20, v21\n\t"
      "v_cvt_pk_bf16_f32 v151, v22, v23\n\t"
      "v_mfma_f32_16x16x32_bf16 v[60:63], v[100:103], v[108:111], v[60:63]\n\t"
      "ds_read_b64_tr_b16 v[80:81], v167 offset:3072\n\t"
      "v_mov_b32_e32 v34, 0x3e8b97f6\n\t"
      "v_mfma_f32_16x16x32_bf16 v[28:31], v[156:159], v[88:91], v[176:179]\n\t"
      "v_mfma_f32_16x16x32_bf16 v[24:27], v[152:155], v[88:91], v[172:175]\n\t"
      "v_mfma_f32_16x16x32_bf16 v[44:47], v[92:95], v[108:111], v[44:47]\n\t"
      "ds_read_b64_tr_b16 v[82:83], v167 offset:3584\n\t"
      "v_mfma_f32_16x16x32_bf16 v[20:23], v[152:155], v[84:87], v[176:179]\n\t"
      "v_mfma_f32_16x16x32_bf16 v[16:19], v[148:151], v[84:87], v[180:183]\n\t"
      "s_barrier\n\t"
      "s_waitcnt lgkmcnt(7)\n\t"
      "v_mfma_f32_16x16x32_bf16 v[48:51], v[124:127], v[132:135], v[48:51]\n\t"
      "ds_read_b64_tr_b16 v[104:105], v166 offset:16384\n\t"
      "ds_read_b64_tr_b16 v[106:107], v166 offset:16896\n\t"
      "v_mfma_f32_16x16x32_bf16 v[56:59], v[128:131], v[132:135], v[56:59]\n\t"
      "ds_read_b128 v[96:99], v160 offset:8192\n\t"
      "v_max_f32_e32 v24, v24, v28\n\t"
      "v_mfma_f32_16x16x32_bf16 v[40:43], v[120:123], v[132:135], v[40:43]\n\t"
      "ds_read_b128 v[100:103], v160 offset:22528\n\t"
      "v_max_f32_e32 v25, v25, v29\n\t"
      "v_max_f32_e32 v26, v26, v30\n\t"
      "v_max_f32_e32 v27, v27, v31\n\t"
      "v_mfma_f32_16x16x32_bf16 v[52:55], v[124:127], v[136:139], v[52:55]\n\t"
      "ds_read_b128 v[92:95], v164 offset:8192\n\t"
      "v_max_f32_e32 v16, v16, v20\n\t"
      "v_max_f32_e32 v17, v17, v21\n\t"
      "v_max_f32_e32 v18, v18, v22\n\t"
      "v_mfma_f32_16x16x32_bf16 v[60:63], v[128:131], v[136:139], v[60:63]\n\t"
      "ds_read_b64_tr_b16 v[108:109], v167 offset:16384\n\t"
      "v_max_f32_e32 v19, v19, v23\n\t"
      "v_max3_f32 v33, v24, v25, v26\n\t"
      "v_max3_f32 v32, v16, v17, v18\n\t"
      "v_mfma_f32_16x16x32_bf16 v[44:47], v[120:123], v[136:139], v[44:47]\n\t"
      "ds_read_b64_tr_b16 v[110:111], v167 offset:16896\n\t"
      "v_max_f32_e32 v33, v33, v27\n\t"
      "v_max_f32_e32 v32, v32, v19\n\t"
      "s_waitcnt lgkmcnt(7)\n\t"
      "v_mfma_f32_16x16x32_bf16 v[48:51], v[68:71], v[76:79], v[48:51]\n\t"
      "ds_read_b64_tr_b16 v[132:133], v166 offset:17408\n\t"
      "ds_read_b64_tr_b16 v[134:135], v166 offset:17920\n\t"
      "v_permlane16_swap_b32_e32 v32, v33\n\t"
      "v_max_f32_e32 v32, v32, v33\n\t"
      "v_mov_b32_e32 v33, v32\n\t"
      "v_mfma_f32_16x16x32_bf16 v[56:59], v[72:75], v[76:79], v[56:59]\n\t"
      "ds_read_b128 v[124:127], v161 offset:9216\n\t"
      "v_permlane32_swap_b32_e32 v32, v33\n\t"
      "v_max_f32_e32 v32, v32, v33\n\t"
      "v_mfma_f32_16x16x32_bf16 v[40:43], v[64:67], v[76:79], v[40:43]\n\t"
      "ds_read_b128 v[128:131], v161 offset:23552\n\t"
      "v_mov_b32_e32 v33, v32\n\t"
      "s_nop 1\n\t"
      "v_permlane16_swap_b32_e32 v32, v33\n\t"
      "v_mfma_f32_16x16x32_bf16 v[52:55], v[68:71], v[80:83], v[52:55]\n\t"
      "ds_read_b128 v[120:123], v165 offset:9216\n\t"
      "v_mul_f32_e64 v33, -v33, v34\n\t"
      "v_mul_f32_e64 v32, -v32, v34\n\t"
      "v_fma_f32 v24, v24, v34, v33\n\t"
      "v_mfma_f32_16x16x32_bf16 v[60:63], v[72:75], v[80:83], v[60:63]\n\t"
      "ds_read_b64_tr_b16 v[136:137], v167 offset:17408\n\t"
      "v_fma_f32 v25, v25, v34, v33\n\t"
      "v_fma_f32 v26, v26, v34, v33\n\t"
      "v_fma_f32 v27, v27, v34, v33\n\t"
      "v_mfma_f32_16x16x32_bf16 v[44:47], v[64:67], v[80:83], v[44:47]\n\t"
      "ds_read_b64_tr_b16 v[138:139], v167 offset:17920\n\t"
      "v_fma_f32 v16, v16, v34, v32\n\t"
      "v_fma_f32 v17, v17, v34, v32\n\t"
      "v_fma_f32 v18, v18, v34, v32\n\t"
      "s_waitcnt lgkmcnt(7)\n\t"
      "v_mfma_f32_16x16x32_bf16 v[48:51], v[96:99], v[104:107], v[48:51]\n\t"
      "ds_read_b64_tr_b16 v[76:77], v166 offset:18432\n\t"
      "ds_read_b64_tr_b16 v[78:79], v166 offset:18944\n\t"
      "v_fma_f32 v19, v19, v34, v32\n\t"
      "v_exp_f32_e32 v24, v24\n\t"
      "v_mfma_f32_16x16x32_bf16 v[56:59], v[100:103], v[104:107], v[56:59]\n\t"
      "ds_read_b128 v[68:71], v160 offset:10240\n\t"
      "v_exp_f32_e32 v25, v25\n\t"
      "v_exp_f32_e32 v26, v26\n\t"
      "v_mfma_f32_16x16x32_bf16 v[40:43], v[92:95], v[104:107], v[40:43]\n\t"
      "ds_read_b128 v[72:75], v160 offset:24576\n\t"
      "v_exp_f32_e32 v27, v27\n\t"
      "v_mfma_f32_16x16x32_bf16 v[52:55], v[96:99], v[108:111], v[52:55]\n\t"
      "ds_read_b128 v[64:67], v164 offset:10240\n\t"
      "v_exp_f32_e32 v16, v16\n\t"
      "v_exp_f32_e32 v17, v17\n\t"
      "v_mfma_f32_16x16x32_bf16 v[60:63], v[100:103], v[108:111], v[60:63]\n\t"
      "ds_read_b64_tr_b16 v[80:81], v167 offset:18432\n\t"
      "v_exp_f32_e32 v18, v18\n\t"
      "v_mfma_f32_16x16x32_bf16 v[44:47], v[92:95], v[108:111], v[44:47]\n\t"
      "ds_read_b64_tr_b16 v[82:83], v167 offset:18944\n\t"
      "v_exp_f32_e32 v19, v19\n\t"
      "v_add_f32_e32 v33, v24, v25\n\t"
      "s_waitcnt lgkmcnt(7)\n\t"
      "v_mfma_f32_16x16x32_bf16 v[48:51], v[124:127], v[132:135], v[48:51]\n\t"
      "ds_read_b64_tr_b16 v[104:105], v166 offset:19456\n\t"
      "ds_read_b64_tr_b16 v[106:107], v166 offset:19968\n\t"
      "v_add_f32_e32 v28, v26, v27\n\t"
      "v_add_f32_e32 v32, v16, v17\n\t"
      "v_add_f32_e32 v20, v18, v19\n\t"
      "v_mfma_f32_16x16x32_bf16 v[56:59], v[128:131], v[132:135], v[56:59]\n\t"
      "ds_read_b128 v[96:99], v161 offset:11264\n\t"
      "v_add_f32_e32 v33, v33, v28\n\t"
      "v_add_f32_e32 v32, v32, v20\n\t"
      "v_mfma_f32_16x16x32_bf16 v[40:43], v[120:123], v[132:135], v[40:43]\n\t"
      "ds_read_b128 v[100:103], v161 offset:25600\n\t"
      "v_permlane16_swap_b32_e32 v32, v33\n\t"
      "v_add_f32_e32 v32, v32, v33\n\t"
      "v_mov_b32_e32 v33, v32\n\t"
      "v_mfma_f32_16x16x32_bf16 v[52:55], v[124:127], v[136:139], v[52:55]\n\t"
      "ds_read_b128 v[92:95], v165 offset:11264\n\t"
      "v_permlane32_swap_b32_e32 v32, v33\n\t"
      "v_add_f32_e32 v32, v32, v33\n\t"
      "v_mfma_f32_16x16x32_bf16 v[60:63], v[128:131], v[136:139], v[60:63]\n\t"
      "ds_read_b64_tr_b16 v[108:109], v167 offset:19456\n\t"
      "v_mov_b32_e32 v33, v32\n\t"
      "s_nop 1\n\t"
      "v_permlane16_swap_b32_e32 v32, v33\n\t"
      "v_mfma_f32_16x16x32_bf16 v[44:47], v[120:123], v[136:139], v[44:47]\n\t"
      "ds_read_b64_tr_b16 v[110:111], v167 offset:19968\n\t"
      "v_rcp_f32_e32 v33, v33\n\t"
      "v_rcp_f32_e32 v32, v32\n\t"
      "s_barrier\n\t"
      "s_waitcnt lgkmcnt(7)\n\t"
      "v_mfma_f32_16x16x32_bf16 v[48:51], v[68:71], v[76:79], v[48:51]\n\t"
      "ds_read_b64_tr_b16 v[132:133], v170 offset:32768\n\t"
      "ds_read_b64_tr_b16 v[134:135], v170 offset:33280\n\t"
      "v_mul_f32_e32 v24, v24, v33\n\t"
      "v_mul_f32_e32 v25, v25, v33\n\t"
      "v_mfma_f32_16x16x32_bf16 v[56:59], v[72:75], v[76:79], v[56:59]\n\t"
      "ds_read_b128 v[124:127], v160 offset:12288\n\t"
      "v_mul_f32_e32 v26, v26, v33\n\t"
      "v_mul_f32_e32 v27, v27, v33\n\t"
      "v_mul_f32_e32 v16, v16, v32\n\t"
      "v_mfma_f32_16x16x32_bf16 v[40:43], v[64:67], v[76:79], v[40:43]\n\t"
      "ds_read_b128 v[128:131], v160 offset:26624\n\t"
      "v_mul_f32_e32 v17, v17, v32\n\t"
      "v_mul_f32_e32 v18, v18, v32\n\t"
      "v_mul_f32_e32 v19, v19, v32\n\t"
      "v_mfma_f32_16x16x32_bf16 v[52:55], v[68:71], v[80:83], v[52:55]\n\t"
      "ds_read_b128 v[120:123], v164 offset:12288\n\t"
      "v_cvt_pk_bf16_f32 v4, v24, v25\n\t"
      "v_cvt_pk_bf16_f32 v5, v26, v27\n\t"
      "v_cvt_pk_bf16_f32 v0, v16, v17\n\t"
      "v_mfma_f32_16x16x32_bf16 v[60:63], v[72:75], v[80:83], v[60:63]\n\t"
      "ds_read_b64_tr_b16 v[136:137], v171 offset:32768\n\t"
      "v_cvt_pk_bf16_f32 v1, v18, v19\n\t"
      "v_and_b32_e32 v6, v184, v4\n\t"
      "v_and_b32_e32 v7, v185, v5\n\t"
      "v_mfma_f32_16x16x32_bf16 v[44:47], v[64:67], v[80:83], v[44:47]\n\t"
      "ds_read_b64_tr_b16 v[138:139], v171 offset:33280\n\t"
      "v_and_b32_e32 v2, v184, v0\n\t"
      "v_and_b32_e32 v3, v185, v1\n\t"
      "v_xor_b32_e32 v4, v4, v6\n\t"
      "s_waitcnt lgkmcnt(7)\n\t"
      "v_mfma_f32_16x16x32_bf16 v[48:51], v[96:99], v[104:107], v[48:51]\n\t"
      "ds_read_b64_tr_b16 v[76:77], v170 offset:33792\n\t"
      "ds_read_b64_tr_b16 v[78:79], v170 offset:34304\n\t"
      "v_xor_b32_e32 v5, v5, v7\n\t"
      "v_xor_b32_e32 v0, v0, v2\n\t"
      "v_xor_b32_e32 v1, v1, v3\n\t"
      "v_mfma_f32_16x16x32_bf16 v[56:59], v[100:103], v[104:107], v[56:59]\n\t"
      "ds_read_b128 v[68:71], v161 offset:13312\n\t"
      "v_mfma_f32_16x16x32_bf16 v[40:43], v[92:95], v[104:107], v[40:43]\n\t"
      "ds_read_b128 v[72:75], v161 offset:27648\n\t"
      "v_mfma_f32_16x16x32_bf16 v[52:55], v[96:99], v[108:111], v[52:55]\n\t"
      "ds_read_b128 v[64:67], v165 offset:13312\n\t"
      "v_mfma_f32_16x16x32_bf16 v[60:63], v[100:103], v[108:111], v[60:63]\n\t"
      "ds_read_b64_tr_b16 v[80:81], v171 offset:33792\n\t"
      "v_mfma_f32_16x16x32_bf16 v[44:47], v[92:95], v[108:111], v[44:47]\n\t"
      "ds_read_b64_tr_b16 v[82:83], v171 offset:34304\n\t"
      "s_waitcnt lgkmcnt(7)\n\t"
      "v_mfma_f32_16x16x32_bf16 v[48:51], v[124:127], v[132:135], v[48:51]\n\t"
      "v_mfma_f32_16x16x32_bf16 v[56:59], v[128:131], v[132:135], v[56:59]\n\t"
      "v_mfma_f32_16x16x32_bf16 v[40:43], v[120:123], v[132:135], v[40:43]\n\t"
      "v_mfma_f32_16x16x32_bf16 v[52:55], v[124:127], v[136:139], v[52:55]\n\t"
      "v_mfma_f32_16x16x32_bf16 v[60:63], v[128:131], v[136:139], v[60:63]\n\t"
      "v_mfma_f32_16x16x32_bf16 v[44:47], v[120:123], v[136:139], v[44:47]\n\t"
      "s_waitcnt lgkmcnt(0)\n\t"
      "v_mfma_f32_16x16x32_bf16 v[48:51], v[68:71], v[76:79], v[48:51]\n\t"
      "v_mfma_f32_16x16x32_bf16 v[56:59], v[72:75], v[76:79], v[56:59]\n\t"
      "v_mfma_f32_16x16x32_bf16 v[40:43], v[64:67], v[76:79], v[40:43]\n\t"
      "v_mfma_f32_16x16x32_bf16 v[52:55], v[68:71], v[80:83], v[52:55]\n\t"
      "v_mfma_f32_16x16x32_bf16 v[60:63], v[72:75], v[80:83], v[60:63]\n\t"
      "v_mfma_f32_16x16x32_bf16 v[44:47], v[64:67], v[80:83], v[44:47]\n\t"
      "s_nop 2\n\t"
      "v_cvt_pk_bf16_f32 v66, v48, v49\n\t"
      "v_cvt_pk_bf16_f32 v67, v50, v51\n\t"
      "v_cvt_pk_bf16_f32 v68, v56, v57\n\t"
      "v_cvt_pk_bf16_f32 v69, v58, v59\n\t"
      "v_cvt_pk_bf16_f32 v64, v40, v41\n\t"
      "v_cvt_pk_bf16_f32 v65, v42, v43\n\t"
      "v_mfma_f32_16x16x32_bf16 v[88:91], v[66:69], v[4:7], 0\n\t"
      "s_nop 0\n\t"
      "v_mfma_f32_16x16x32_bf16 v[80:83], v[64:67], v[0:3], 0\n\t"
      "v_cvt_pk_bf16_f32 v72, v52, v53\n\t"
      "v_cvt_pk_bf16_f32 v73, v54, v55\n\t"
      "v_cvt_pk_bf16_f32 v74, v60, v61\n\t"
      "v_cvt_pk_bf16_f32 v75, v62, v63\n\t"
      "v_cvt_pk_bf16_f32 v70, v44, v45\n\t"
      "v_cvt_pk_bf16_f32 v71, v46, v47\n\t"
      "v_mfma_f32_16x16x32_bf16 v[92:95], v[72:75], v[4:7], 0\n\t"
      "s_nop 0\n\t"
      "v_mfma_f32_16x16x32_bf16 v[84:87], v[70:73], v[0:3], 0\n\t"
      "v_cvt_pk_bf16_f32 v152, v88, v89\n\t"
      "v_cvt_pk_bf16_f32 v153, v90, v91\n\t"
      "v_min_u32_e32 v152, -2, v152\n\t"
      "v_min_u32_e32 v153, -2, v153\n\t"
      "v_cvt_pk_bf16_f32 v148, v80, v81\n\t"
      "v_cvt_pk_bf16_f32 v149, v82, v83\n\t"
      "v_min_u32_e32 v148, -2, v148\n\t"
      "v_min_u32_e32 v149, -2, v149\n\t"
      "v_cvt_pk_bf16_f32 v154, v92, v93\n\t"
      "v_cvt_pk_bf16_f32 v155, v94, v95\n\t"
      "v_cndmask_b32_e64 v154, v154, 0, s[68:69]\n\t"
      "v_min_u32_e32 v154, -2, v154\n\t"
      "v_cndmask_b32_e64 v155, v155, 0, s[68:69]\n\t"
      "v_min_u32_e32 v155, -2, v155\n\t"
      "global_store_dwordx4 v244, v[152:155], s[44:45] sc1\n\t"
      "v_cvt_pk_bf16_f32 v150, v84, v85\n\t"
      "v_cvt_pk_bf16_f32 v151, v86, v87\n\t"
      "v_cndmask_b32_e64 v150, v150, 0, s[68:69]\n\t"
      "v_min_u32_e32 v150, -2, v150\n\t"
      "v_cndmask_b32_e64 v151, v151, 0, s[68:69]\n\t"
      "v_min_u32_e32 v151, -2, v151\n\t"
      "global_store_dwordx4 v244, v[148:151], s[42:43] sc1\n\t"
      "s_barrier\n\t"
      "s_cmp_eq_u32 s26, 7\n\t"
      "s_cbranch_scc1 SPARE_%=\n\t"
      "s_lshl_b32 s34, s25, 16\n\t"
      "s_lshl2_add_u32 s34, s39, s34\n\t"
      "s_add_u32 s34, s34, 4096\n\t"
      "s_add_u32 s42, s20, s34\n\t"
      "s_addc_u32 s43, s21, 0\n\t"
      "s_add_u32 s44, s42, 8192\n\t"
      "s_addc_u32 s45, s43, 0\n\t"
      "s_lshl_b32 s72, s24, 10\n\t"
      "s_add_u32 s72, s72, 98304\n\t"
      "v_xor_b32_e32 v203, v197, v198\n\t"
      "v_lshl_add_u32 v203, v203, 4, s72\n\t"
      "v_lshl_add_u32 v203, v197, 7, v203\n\t"
      "ds_write_b128 v203, v[204:207]\n\t"
      "s_and_b32 s35, s25, 3\n\t"
      "s_lshl2_add_u32 s31, s26, s35\n\t"
      "s_and_b32 s35, s2, 7\n\t"
      "s_mul_i32 s34, s35, 14\n\t"
      "s_lshl4_add_u32 s35, s35, -14\n\t"
      "s_cmp_lt_u32 s31, 14\n\t"
      "s_cselect_b32 s34, s34, s35\n\t"
      "s_cselect_b32 s36, 1, 0\n\t"
      "s_or_b32 s27, s27, s36\n\t"
      "s_add_u32 s34, s34, s31\n\t"
      "s_lshr_b32 s29, s34, 2\n\t"
      "s_and_b32 s30, s34, 3\n\t"
      "s_mul_i32 s30, s30, 7\n\t"
      "v_and_b32_e32 v199, 60, v192\n\t"
      "v_and_b32_e32 v200, 24, v192\n\t"
      "v_lshlrev_b32_e32 v202, 3, v196\n\t"
      "v_lshl_or_b32 v202, v200, 2, v202\n\t"
      "v_lshl_or_b32 v202, v195, 7, v202\n\t"
      "v_lshl_or_b32 v202, v193, 10, v202\n\t"
      "v_add_u32_e32 v224, s39, v202\n\t"
      "v_xor_b32_e32 v225, 32, v224\n\t"
      "v_xor_b32_e32 v226, 64, v224\n\t"
      "v_xor_b32_e32 v227, 96, v224\n\t"
      "s_lshl_b32 s34, s24, 12\n\t"
      "s_add_u32 s34, s34, 98304\n\t"
      "v_add_u32_e32 v245, s34, v244\n\t"
      "v_lshlrev_b32_e32 v199, 2, v199\n\t"
      "s_lshl_b32 s34, s24, 8\n\t"
      "v_lshl_add_u32 v247, v192, 2, s34\n\t"
      "v_and_b32_e32 v200, 2, v192\n\t"
      "v_mul_u32_u24_e32 v202, 576, v200\n\t"
      "v_xor_b32_e32 v199, v202, v199\n\t"
      "v_and_b32_e32 v202, 1, v192\n\t"
      "v_lshl_or_b32 v199, v202, 3, v199\n\t"
      "s_and_b32 s35, s34, 0x400\n\t"
      "s_add_u32 s34, s34, s35\n\t"
      "s_add_u32 s34, s34, 98304\n\t"
      "v_add_u32_e32 v246, s34, v199\n\t"
      "v_lshlrev_b32_e32 v199, 2, v202\n\t"
      "v_lshl_or_b32 v199, v200, 4, v199\n\t"
      "s_waitcnt lgkmcnt(0)\n\t"
      "s_barrier\n\t"
      "v_xor_b32_e32 v210, 32, v228\n\t"
      "v_xor_b32_e32 v212, 64, v228\n\t"
      "v_xor_b32_e32 v214, 96, v228\n\t"
      "ds_read_b64 v[208:209], v228\n\t"
      "ds_read_b64 v[210:211], v210\n\t"
      "ds_read_b64 v[212:213], v212\n\t"
      "ds_read_b64 v[214:215], v214\n\t"
      "ds_read_b64_tr_b16 v[0:1], v216\n\t"
      "ds_read_b64_tr_b16 v[2:3], v220\n\t"
      "ds_read_b64_tr_b16 v[4:5], v217\n\t"
      "ds_read_b64_tr_b16 v[6:7], v221\n\t"
      "ds_read_b64_tr_b16 v[8:9], v218\n\t"
      "ds_read_b64_tr_b16 v[10:11], v222\n\t"
      "ds_read_b64_tr_b16 v[12:13], v219\n\t"
      "ds_read_b64_tr_b16 v[14:15], v223\n\t"
      "ds_read_b64_tr_b16 v[16:17], v218 offset:3584\n\t"
      "ds_read_b64_tr_b16 v[18:19], v222 offset:3584\n\t"
      "ds_read_b64_tr_b16 v[20:21], v219 offset:3584\n\t"
      "ds_read_b64_tr_b16 v[22:23], v223 offset:3584\n\t"
      "ds_read_b64_tr_b16 v[24:25], v216 offset:3584\n\t"
      "ds_read_b64_tr_b16 v[26:27], v220 offset:3584\n\t"
      "ds_read_b64_tr_b16 v[28:29], v217 offset:3584\n\t"
      "ds_read_b64_tr_b16 v[30:31], v221 offset:3584\n\t"
      "ds_read_b64_tr_b16 v[32:33], v216 offset:7168\n\t"
      "ds_read_b64_tr_b16 v[34:35], v220 offset:7168\n\t"
      "ds_read_b64_tr_b16 v[36:37], v217 offset:7168\n\t"
      "ds_read_b64_tr_b16 v[38:39], v221 offset:7168\n\t"
      "ds_read_b64_tr_b16 v[40:41], v218 offset:7168\n\t"
      "ds_read_b64_tr_b16 v[42:43], v222 offset:7168\n\t"
      "ds_read_b64_tr_b16 v[44:45], v219 offset:7168\n\t"
      "ds_read_b64_tr_b16 v[46:47], v223 offset:7168\n\t"
      "ds_read_b64_tr_b16 v[48:49], v218 offset:10752\n\t"
      "ds_read_b64_tr_b16 v[50:51], v222 offset:10752\n\t"
      "ds_read_b64_tr_b16 v[52:53], v219 offset:10752\n\t"
      "ds_read_b64_tr_b16 v[54:55], v223 offset:10752\n\t"
      "ds_read_b64_tr_b16 v[56:57], v216 offset:10752\n\t"
      "ds_read_b64_tr_b16 v[58:59], v220 offset:10752\n\t"
      "ds_read_b64_tr_b16 v[60:61], v217 offset:10752\n\t"
      "ds_read_b64_tr_b16 v[62:63], v221 offset:10752\n\t"
      "ds_read_b64_tr_b16 v[64:65], v216 offset:14336\n\t"
      "ds_read_b64_tr_b16 v[66:67], v220 offset:14336\n\t"
      "ds_read_b64_tr_b16 v[68:69], v217 offset:14336\n\t"
      "ds_read_b64_tr_b16 v[70:71], v221 offset:14336\n\t"
      "ds_read_b64_tr_b16 v[72:73], v218 offset:14336\n\t"
      "ds_read_b64_tr_b16 v[74:75], v222 offset:14336\n\t"
      "ds_read_b64_tr_b16 v[76:77], v219 offset:14336\n\t"
      "ds_read_b64_tr_b16 v[78:79], v223 offset:14336\n\t"
      "ds_read_b64_tr_b16 v[80:81], v218 offset:17920\n\t"
      "ds_read_b64_tr_b16 v[82:83], v222 offset:17920\n\t"
      "ds_read_b64_tr_b16 v[84:85], v219 offset:17920\n\t"
      "ds_read_b64_tr_b16 v[86:87], v223 offset:17920\n\t"
      "ds_read_b64_tr_b16 v[88:89], v216 offset:17920\n\t"
      "ds_read_b64_tr_b16 v[90:91], v220 offset:17920\n\t"
      "ds_read_b64_tr_b16 v[92:93], v217 offset:17920\n\t"
      "ds_read_b64_tr_b16 v[94:95], v221 offset:17920\n\t"
      "ds_read_b64_tr_b16 v[96:97], v216 offset:21504\n\t"
      "ds_read_b64_tr_b16 v[98:99], v220 offset:21504\n\t"
      "ds_read_b64_tr_b16 v[100:101], v217 offset:21504\n\t"
      "ds_read_b64_tr_b16 v[102:103], v221 offset:21504\n\t"
      "ds_read_b64_tr_b16 v[104:105], v218 offset:21504\n\t"
      "ds_read_b64_tr_b16 v[106:107], v222 offset:21504\n\t"
      "ds_read_b64_tr_b16 v[108:109], v219 offset:21504\n\t"
      "ds_read_b64_tr_b16 v[110:111], v223 offset:21504\n\t"
      "ds_read_b64_tr_b16 v[112:113], v218 offset:25088\n\t"
      "ds_read_b64_tr_b16 v[114:115], v222 offset:25088\n\t"
      "ds_read_b64_tr_b16 v[116:117], v219 offset:25088\n\t"
      "ds_read_b64_tr_b16 v[118:119], v223 offset:25088\n\t"
      "ds_read_b64_tr_b16 v[120:121], v216 offset:25088\n\t"
      "ds_read_b64_tr_b16 v[122:123], v220 offset:25088\n\t"
      "ds_read_b64_tr_b16 v[124:125], v217 offset:25088\n\t"
      "ds_read_b64_tr_b16 v[126:127], v221 offset:25088\n\t"
      "global_load_dwordx4 v[128:131], v244, s[42:43] offset:-4096 sc1\n\t"
      "global_load_dwordx4 v[132:135], v244, s[42:43] offset:-3072 sc1\n\t"
      "global_load_dwordx4 v[136:139], v244, s[42:43] offset:-2048 sc1\n\t"
      "global_load_dwordx4 v[140:143], v244, s[42:43] offset:-1024 sc1\n\t"
      "global_load_dwordx4 v[144:147], v244, s[42:43] sc1\n\t"
      "global_load_dwordx4 v[148:151], v244, s[42:43] offset:1024 sc1\n\t"
      "global_load_dwordx4 v[152:155], v244, s[42:43] offset:2048 sc1\n\t"
      "global_load_dwordx4 v[156:159], v244, s[42:43] offset:3072 sc1\n\t"
      "global_load_dwordx4 v[160:163], v244, s[44:45] offset:-4096 sc1\n\t"
      "global_load_dwordx4 v[164:167], v244, s[44:45] offset:-3072 sc1\n\t"
      "s_min_u32 s34, s29, 27\n\t"
      "s_lshl_b32 s34, s34, 7\n\t"
      "s_lshr_b32 s35, s24, 2\n\t"
      "s_lshl_b32 s35, s35, 6\n\t"
      "s_add_u32 s34, s34, s35\n\t"
      "s_lshr_b32 s35, s39, 9\n\t"
      "s_add_u32 s34, s34, s35\n\t"
      "v_add_u32_e32 v248, s34, v199\n\t"
      "s_add_u32 s48, s20, 8388608\n\t"
      "s_addc_u32 s49, s21, 0\n\t"
      "v_and_b32_e32 v199, 16, v192\n\t"
      "v_lshlrev_b32_e32 v199, 1, v199\n\t"
      "v_and_b32_e32 v200, 32, v192\n\t"
      "s_lshl_b32 s34, s28, 18\n\t"
      "s_lshl_b32 s35, s25, 11\n\t"
      "s_add_u32 s34, s34, s35\n\t"
      "s_lshr_b32 s35, s39, 3\n\t"
      "s_add_u32 s34, s34, s35\n\t"
      "v_and_b32_e32 v201, 0x1f0, v244\n\t"
      "v_lshl_or_b32 v201, v200, 11, v201\n\t"
      "v_add_u32_e32 v229, s34, v201\n\t"
      "v_add_u32_e32 v230, 131072, v229\n\t"
      "s_and_b32 s34, s25, 30\n\t"
      "s_lshr_b32 s35, s39, 13\n\t"
      "s_add_u32 s34, s34, s35\n\t"
      "s_mul_i32 s34, s34, 7\n\t"
      "s_add_u32 s34, s34, s28\n\t"
      "s_lshl_b32 s34, s34, 3\n\t"
      "s_and_b32 s35, s25, 1\n\t"
      "s_add_u32 s34, s34, s35\n\t"
      "s_lshl_b32 s34, s34, 10\n\t"
      "s_and_b32 s35, s24, 1\n\t"
      "s_lshl_b32 s35, s35, 9\n\t"
      "s_add_u32 s34, s34, s35\n\t"
      "s_add_u32 s34, s34, 6293504\n\t"
      "v_lshrrev_b32_e32 v201, 1, v200\n\t"
      "v_lshl_or_b32 v201, v199, 6, v201\n\t"
      "v_lshl_or_b32 v201, v194, 5, v201\n\t"
      "v_add_u32_e32 v241, s34, v201\n\t"
      "s_lshl_b32 s34, s24, 12\n\t"
      "s_add_u32 s34, s34, 131072\n\t"
      "v_xor_b32_e32 v201, v193, v194\n\t"
      "v_lshlrev_b32_e32 v201, 3, v201\n\t"
      "v_lshl_or_b32 v201, v194, 7, v201\n\t"
      "v_add_u32_e32 v233, s34, v201\n\t"
      "v_lshrrev_b32_e32 v199, 2, v199\n\t"
      "v_or_b32_e32 v199, v199, v195\n\t"
      "v_lshrrev_b32_e32 v200, 3, v200\n\t"
      "v_or_b32_e32 v200, v196, v200\n\t"
      "v_xor_b32_e32 v200, v200, v199\n\t"
      "v_lshlrev_b32_e32 v200, 3, v200\n\t"
      "v_lshl_or_b32 v200, v199, 7, v200\n\t"
      "v_add_u32_e32 v237, s34, v200\n\t"
      "v_xor_b32_e32 v234, 32, v233\n\t"
      "v_xor_b32_e32 v238, 32, v237\n\t"
      "v_xor_b32_e32 v235, 64, v233\n\t"
      "v_xor_b32_e32 v239, 64, v237\n\t"
      "v_xor_b32_e32 v236, 96, v233\n\t"
      "v_xor_b32_e32 v240, 96, v237\n\t"
      "s_waitcnt lgkmcnt(14)\n\t"
      "v_lshlrev_b32_e32 v192, 16, v208\n\t"
      "v_and_b32_e32 v193, 0xffff0000, v208\n\t"
      "v_lshlrev_b32_e32 v194, 16, v209\n\t"
      "v_and_b32_e32 v195, 0xffff0000, v209\n\t"
      "v_lshlrev_b32_e32 v196, 16, v210\n\t"
      "v_and_b32_e32 v197, 0xffff0000, v210\n\t"
      "v_lshlrev_b32_e32 v198, 16, v211\n\t"
      "v_and_b32_e32 v199, 0xffff0000, v211\n\t"
      "v_lshlrev_b32_e32 v200, 16, v212\n\t"
      "v_and_b32_e32 v201, 0xffff0000, v212\n\t"
      "v_lshlrev_b32_e32 v202, 16, v213\n\t"
      "v_and_b32_e32 v203, 0xffff0000, v213\n\t"
      "v_lshlrev_b32_e32 v204, 16, v214\n\t"
      "v_and_b32_e32 v205, 0xffff0000, v214\n\t"
      "v_lshlrev_b32_e32 v206, 16, v215\n\t"
      "v_and_b32_e32 v207, 0xffff0000, v215\n\t"
      "global_load_dwordx4 v[168:171], v244, s[44:45] offset:-2048 sc1\n\t"
      "global_load_dwordx4 v[172:175], v244, s[44:45] offset:-1024 sc1\n\t"
      "global_load_dwordx4 v[176:179], v244, s[44:45] sc1\n\t"
      "global_load_dwordx4 v[180:183], v244, s[44:45] offset:1024 sc1\n\t"
      "global_load_dwordx4 v[184:187], v244, s[44:45] offset:2048 sc1\n\t"
      "global_load_dwordx4 v[188:191], v244, s[44:45] offset:3072 sc1\n\t"
      "global_load_dword v249, v248, s[12:13]\n\t"
      "s_barrier\n\t"
      "s_waitcnt lgkmcnt(0)\n\t"
      "LGM_%=:\n\t"
      "s_waitcnt vmcnt(15)\n\t"
      "v_max_u32_e32 v232, v128, v132\n\t"
      "v_cmp_eq_u32_e32 vcc, -1, v232\n\t"
      "s_cbranch_vccnz LO0R_%=\n\t"
      "LO0B_%=:\n\t"
      "v_mfma_f32_16x16x32_bf16 v[192:195], v[0:3], v[128:131], v[192:195]\n\t"
      "ds_read_b64_tr_b16 v[0:1], v216 offset:28672\n\t"
      "ds_read_b64_tr_b16 v[2:3], v220 offset:28672\n\t"
      "v_mfma_f32_16x16x32_bf16 v[196:199], v[4:7], v[128:131], v[196:199]\n\t"
      "ds_read_b64_tr_b16 v[4:5], v217 offset:28672\n\t"
      "v_mfma_f32_16x16x32_bf16 v[200:203], v[8:11], v[128:131], v[200:203]\n\t"
      "ds_read_b64_tr_b16 v[6:7], v221 offset:28672\n\t"
      "v_mfma_f32_16x16x32_bf16 v[204:207], v[12:15], v[128:131], v[204:207]\n\t"
      "ds_read_b64_tr_b16 v[8:9], v218 offset:28672\n\t"
      "ds_read_b64_tr_b16 v[10:11], v222 offset:28672\n\t"
      "v_mfma_f32_16x16x32_bf16 v[192:195], v[16:19], v[132:135], v[192:195]\n\t"
      "ds_read_b64_tr_b16 v[12:13], v219 offset:28672\n\t"
      "v_mfma_f32_16x16x32_bf16 v[196:199], v[20:23], v[132:135], v[196:199]\n\t"
      "ds_read_b64_tr_b16 v[14:15], v223 offset:28672\n\t"
      "v_mfma_f32_16x16x32_bf16 v[200:203], v[24:27], v[132:135], v[200:203]\n\t"
      "ds_read_b64_tr_b16 v[16:17], v218 offset:32256\n\t"
      "ds_read_b64_tr_b16 v[18:19], v222 offset:32256\n\t"
      "v_mfma_f32_16x16x32_bf16 v[204:207], v[28:31], v[132:135], v[204:207]\n\t"
      "ds_read_b64_tr_b16 v[20:21], v219 offset:32256\n\t"
      "s_waitcnt vmcnt(13)\n\t"
      "v_max_u32_e32 v232, v136, v140\n\t"
      "v_cmp_eq_u32_e32 vcc, -1, v232\n\t"
      "s_cbranch_vccnz LO2R_%=\n\t"
      "LO2B_%=:\n\t"
      "v_mfma_f32_16x16x32_bf16 v[192:195], v[32:35], v[136:139], v[192:195]\n\t"
      "ds_read_b64_tr_b16 v[22:23], v223 offset:32256\n\t"
      "v_mfma_f32_16x16x32_bf16 v[196:199], v[36:39], v[136:139], v[196:199]\n\t"
      "ds_read_b64_tr_b16 v[24:25], v216 offset:32256\n\t"
      "ds_read_b64_tr_b16 v[26:27], v220 offset:32256\n\t"
      "v_mfma_f32_16x16x32_bf16 v[200:203], v[40:43], v[136:139], v[200:203]\n\t"
      "ds_read_b64_tr_b16 v[28:29], v217 offset:32256\n\t"
      "v_mfma_f32_16x16x32_bf16 v[204:207], v[44:47], v[136:139], v[204:207]\n\t"
      "ds_read_b64_tr_b16 v[30:31], v221 offset:32256\n\t"
      "v_mfma_f32_16x16x32_bf16 v[192:195], v[48:51], v[140:143], v[192:195]\n\t"
      "ds_read_b64_tr_b16 v[32:33], v216 offset:35840\n\t"
      "ds_read_b64_tr_b16 v[34:35], v220 offset:35840\n\t"
      "v_mfma_f32_16x16x32_bf16 v[196:199], v[52:55], v[140:143], v[196:199]\n\t"
      "ds_read_b64_tr_b16 v[36:37], v217 offset:35840\n\t"
      "v_mfma_f32_16x16x32_bf16 v[200:203], v[56:59], v[140:143], v[200:203]\n\t"
      "ds_read_b64_tr_b16 v[38:39], v221 offset:35840\n\t"
      "v_mfma_f32_16x16x32_bf16 v[204:207], v[60:63], v[140:143], v[204:207]\n\t"
      "ds_read_b64_tr_b16 v[40:41], v218 offset:35840\n\t"
      "ds_read_b64_tr_b16 v[42:43], v222 offset:35840\n\t"
      "s_waitcnt vmcnt(11)\n\t"
      "v_max_u32_e32 v232, v144, v148\n\t"
      "v_cmp_eq_u32_e32 vcc, -1, v232\n\t"
      "s_cbranch_vccnz LO4R_%=\n\t"
      "LO4B_%=:\n\t"
      "v_mfma_f32_16x16x32_bf16 v[192:195], v[64:67], v[144:147], v[192:195]\n\t"
      "ds_read_b64_tr_b16 v[44:45], v219 offset:35840\n\t"
      "v_mfma_f32_16x16x32_bf16 v[196:199], v[68:71], v[144:147], v[196:199]\n\t"
      "ds_read_b64_tr_b16 v[46:47], v223 offset:35840\n\t"
      "v_mfma_f32_16x16x32_bf16 v[200:203], v[72:75], v[144:147], v[200:203]\n\t"
      "ds_read_b64_tr_b16 v[48:49], v218 offset:39424\n\t"
      "ds_read_b64_tr_b16 v[50:51], v222 offset:39424\n\t"
      "v_mfma_f32_16x16x32_bf16 v[204:207], v[76:79], v[144:147], v[204:207]\n\t"
      "ds_read_b64_tr_b16 v[52:53], v219 offset:39424\n\t"
      "v_mfma_f32_16x16x32_bf16 v[192:195], v[80:83], v[148:151], v[192:195]\n\t"
      "ds_read_b64_tr_b16 v[54:55], v223 offset:39424\n\t"
      "v_mfma_f32_16x16x32_bf16 v[196:199], v[84:87], v[148:151], v[196:199]\n\t"
      "ds_read_b64_tr_b16 v[56:57], v216 offset:39424\n\t"
      "ds_read_b64_tr_b16 v[58:59], v220 offset:39424\n\t"
      "v_mfma_f32_16x16x32_bf16 v[200:203], v[88:91], v[148:151], v[200:203]\n\t"
      "ds_read_b64_tr_b16 v[60:61], v217 offset:39424\n\t"
      "v_mfma_f32_16x16x32_bf16 v[204:207], v[92:95], v[148:151], v[204:207]\n\t"
      "ds_read_b64_tr_b16 v[62:63], v221 offset:39424\n\t"
      "s_waitcnt vmcnt(9)\n\t"
      "v_max_u32_e32 v232, v152, v156\n\t"
      "v_cmp_eq_u32_e32 vcc, -1, v232\n\t"
      "s_cbranch_vccnz LO6R_%=\n\t"
      "LO6B_%=:\n\t"
      "v_mfma_f32_16x16x32_bf16 v[192:195], v[96:99], v[152:155], v[192:195]\n\t"
      "ds_read_b64_tr_b16 v[64:65], v216 offset:43008\n\t"
      "ds_read_b64_tr_b16 v[66:67], v220 offset:43008\n\t"
      "v_mfma_f32_16x16x32_bf16 v[196:199], v[100:103], v[152:155], v[196:199]\n\t"
      "ds_read_b64_tr_b16 v[68:69], v217 offset:43008\n\t"
      "v_mfma_f32_16x16x32_bf16 v[200:203], v[104:107], v[152:155], v[200:203]\n\t"
      "ds_read_b64_tr_b16 v[70:71], v221 offset:43008\n\t"
      "v_mfma_f32_16x16x32_bf16 v[204:207], v[108:111], v[152:155], v[204:207]\n\t"
      "ds_read_b64_tr_b16 v[72:73], v218 offset:43008\n\t"
      "ds_read_b64_tr_b16 v[74:75], v222 offset:43008\n\t"
      "v_mfma_f32_16x16x32_bf16 v[192:195], v[112:115], v[156:159], v[192:195]\n\t"
      "ds_read_b64_tr_b16 v[76:77], v219 offset:43008\n\t"
      "v_mfma_f32_16x16x32_bf16 v[196:199], v[116:119], v[156:159], v[196:199]\n\t"
      "ds_read_b64_tr_b16 v[78:79], v223 offset:43008\n\t"
      "v_mfma_f32_16x16x32_bf16 v[200:203], v[120:123], v[156:159], v[200:203]\n\t"
      "ds_read_b64_tr_b16 v[80:81], v218 offset:46592\n\t"
      "ds_read_b64_tr_b16 v[82:83], v222 offset:46592\n\t"
      "v_mfma_f32_16x16x32_bf16 v[204:207], v[124:127], v[156:159], v[204:207]\n\t"
      "ds_read_b64_tr_b16 v[84:85], v219 offset:46592\n\t"
      "s_waitcnt vmcnt(7)\n\t"
      "v_max_u32_e32 v232, v160, v164\n\t"
      "v_cmp_eq_u32_e32 vcc, -1, v232\n\t"
      "s_cbranch_vccnz LO8R_%=\n\t"
      "LO8B_%=:\n\t"
      "s_waitcnt lgkmcnt(14)\n\t"
      "v_mfma_f32_16x16x32_bf16 v[192:195], v[0:3], v[160:163], v[192:195]\n\t"
      "ds_read_b64_tr_b16 v[86:87], v223 offset:46592\n\t"
      "v_mfma_f32_16x16x32_bf16 v[196:199], v[4:7], v[160:163], v[196:199]\n\t"
      "ds_read_b64_tr_b16 v[88:89], v216 offset:46592\n\t"
      "ds_read_b64_tr_b16 v[90:91], v220 offset:46592\n\t"
      "v_mfma_f32_16x16x32_bf16 v[200:203], v[8:11], v[160:163], v[200:203]\n\t"
      "ds_read_b64_tr_b16 v[92:93], v217 offset:46592\n\t"
      "v_mfma_f32_16x16x32_bf16 v[204:207], v[12:15], v[160:163], v[204:207]\n\t"
      "ds_read_b64_tr_b16 v[94:95], v221 offset:46592\n\t"
      "v_mfma_f32_16x16x32_bf16 v[192:195], v[16:19], v[164:167], v[192:195]\n\t"
      "ds_read_b64_tr_b16 v[96:97], v216 offset:50176\n\t"
      "ds_read_b64_tr_b16 v[98:99], v220 offset:50176\n\t"
      "v_mfma_f32_16x16x32_bf16 v[196:199], v[20:23], v[164:167], v[196:199]\n\t"
      "ds_read_b64_tr_b16 v[100:101], v217 offset:50176\n\t"
      "v_mfma_f32_16x16x32_bf16 v[200:203], v[24:27], v[164:167], v[200:203]\n\t"
      "ds_read_b64_tr_b16 v[102:103], v221 offset:50176\n\t"
      "v_mfma_f32_16x16x32_bf16 v[204:207], v[28:31], v[164:167], v[204:207]\n\t"
      "ds_read_b64_tr_b16 v[104:105], v218 offset:50176\n\t"
      "ds_read_b64_tr_b16 v[106:107], v222 offset:50176\n\t"
      "s_waitcnt vmcnt(5)\n\t"
      "v_max_u32_e32 v232, v168, v172\n\t"
      "v_cmp_eq_u32_e32 vcc, -1, v232\n\t"
      "s_cbranch_vccnz LO10R_%=\n\t"
      "LO10B_%=:\n\t"
      "v_mfma_f32_16x16x32_bf16 v[192:195], v[32:35], v[168:171], v[192:195]\n\t"
      "ds_read_b64_tr_b16 v[108:109], v219 offset:50176\n\t"
      "v_mfma_f32_16x16x32_bf16 v[196:199], v[36:39], v[168:171], v[196:199]\n\t"
      "ds_read_b64_tr_b16 v[110:111], v223 offset:50176\n\t"
      "v_mfma_f32_16x16x32_bf16 v[200:203], v[40:43], v[168:171], v[200:203]\n\t"
      "ds_read_b64_tr_b16 v[112:113], v218 offset:53760\n\t"
      "ds_read_b64_tr_b16 v[114:115], v222 offset:53760\n\t"
      "v_mfma_f32_16x16x32_bf16 v[204:207], v[44:47], v[168:171], v[204:207]\n\t"
      "ds_read_b64_tr_b16 v[116:117], v219 offset:53760\n\t"
      "s_waitcnt lgkmcnt(14)\n\t"
      "v_mfma_f32_16x16x32_bf16 v[192:195], v[48:51], v[172:175], v[192:195]\n\t"
      "ds_read_b64_tr_b16 v[118:119], v223 offset:53760\n\t"
      "v_mfma_f32_16x16x32_bf16 v[196:199], v[52:55], v[172:175], v[196:199]\n\t"
      "ds_read_b64_tr_b16 v[120:121], v216 offset:53760\n\t"
      "ds_read_b64_tr_b16 v[122:123], v220 offset:53760\n\t"
      "v_mfma_f32_16x16x32_bf16 v[200:203], v[56:59], v[172:175], v[200:203]\n\t"
      "ds_read_b64_tr_b16 v[124:125], v217 offset:53760\n\t"
      "v_mfma_f32_16x16x32_bf16 v[204:207], v[60:63], v[172:175], v[204:207]\n\t"
      "ds_read_b64_tr_b16 v[126:127], v221 offset:53760\n\t"
      "s_waitcnt vmcnt(3)\n\t"
      "v_max_u32_e32 v232, v176, v180\n\t"
      "v_cmp_eq_u32_e32 vcc, -1, v232\n\t"
      "s_cbranch_vccnz LO12R_%=\n\t"
      "LO12B_%=:\n\t"
      "v_mfma_f32_16x16x32_bf16 v[192:195], v[64:67], v[176:179], v[192:195]\n\t"
      "v_mfma_f32_16x16x32_bf16 v[196:199], v[68:71], v[176:179], v[196:199]\n\t"
      "v_mfma_f32_16x16x32_bf16 v[200:203], v[72:75], v[176:179], v[200:203]\n\t"
      "v_mfma_f32_16x16x32_bf16 v[204:207], v[76:79], v[176:179], v[204:207]\n\t"
      "s_waitcnt lgkmcnt(14)\n\t"
      "v_mfma_f32_16x16x32_bf16 v[192:195], v[80:83], v[180:183], v[192:195]\n\t"
      "v_mfma_f32_16x16x32_bf16 v[196:199], v[84:87], v[180:183], v[196:199]\n\t"
      "v_mfma_f32_16x16x32_bf16 v[200:203], v[88:91], v[180:183], v[200:203]\n\t"
      "v_mfma_f32_16x16x32_bf16 v[204:207], v[92:95], v[180:183], v[204:207]\n\t"
      "s_waitcnt vmcnt(1)\n\t"
      "v_max_u32_e32 v232, v184, v188\n\t"
      "v_cmp_eq_u32_e32 vcc, -1, v232\n\t"
      "s_cbranch_vccnz LO14R_%=\n\t"
      "LO14B_%=:\n\t"
      "s_waitcnt lgkmcnt(8)\n\t"
      "v_mfma_f32_16x16x32_bf16 v[192:195], v[96:99], v[184:187], v[192:195]\n\t"
      "v_mfma_f32_16x16x32_bf16 v[196:199], v[100:103], v[184:187], v[196:199]\n\t"
      "v_mfma_f32_16x16x32_bf16 v[200:203], v[104:107], v[184:187], v[200:203]\n\t"
      "v_mfma_f32_16x16x32_bf16 v[204:207], v[108:111], v[184:187], v[204:207]\n\t"
      "s_waitcnt lgkmcnt(0)\n\t"
      "v_mfma_f32_16x16x32_bf16 v[192:195], v[112:115], v[188:191], v[192:195]\n\t"
      "v_mfma_f32_16x16x32_bf16 v[196:199], v[116:119], v[188:191], v[196:199]\n\t"
      "v_mfma_f32_16x16x32_bf16 v[200:203], v[120:123], v[188:191], v[200:203]\n\t"
      "v_mfma_f32_16x16x32_bf16 v[204:207], v[124:127], v[188:191], v[204:207]\n\t"
      "s_nop 4\n\t"
      "v_cmp_u_f32_e32 vcc, v192, v192\n\t"
      "s_cbranch_vccnz LGX_%=\n\t"
      "LGB_%=:\n\t"
      "v_cvt_pk_bf16_f32 v208, v192, v193\n\t"
      "v_cvt_pk_bf16_f32 v209, v194, v195\n\t"
      "v_cvt_pk_bf16_f32 v210, v196, v197\n\t"
      "v_cvt_pk_bf16_f32 v211, v198, v199\n\t"
      "v_cvt_pk_bf16_f32 v212, v200, v201\n\t"
      "v_cvt_pk_bf16_f32 v213, v202, v203\n\t"
      "v_cvt_pk_bf16_f32 v214, v204, v205\n\t"
      "v_cvt_pk_bf16_f32 v215, v206, v207\n\t"
      "v_mov_b32_e32 v148, 0x3f803f80\n\t"
      "v_dot2_f32_bf16 v144, v208, v148, 0\n\t"
      "v_dot2_f32_bf16 v145, v208, v208, 0\n\t"
      "v_dot2_f32_bf16 v146, v209, v148, 0\n\t"
      "v_dot2_f32_bf16 v147, v209, v209, 0\n\t"
      "v_dot2_f32_bf16 v144, v210, v148, v144\n\t"
      "v_dot2_f32_bf16 v145, v210, v210, v145\n\t"
      "v_dot2_f32_bf16 v146, v211, v148, v146\n\t"
      "v_dot2_f32_bf16 v147, v211, v211, v147\n\t"
      "v_dot2_f32_bf16 v144, v212, v148, v144\n\t"
      "v_dot2_f32_bf16 v145, v212, v212, v145\n\t"
      "v_dot2_f32_bf16 v146, v213, v148, v146\n\t"
      "v_dot2_f32_bf16 v147, v213, v213, v147\n\t"
      "v_dot2_f32_bf16 v144, v214, v148, v144\n\t"
      "v_dot2_f32_bf16 v145, v214, v214, v145\n\t"
      "v_dot2_f32_bf16 v146, v215, v148, v146\n\t"
      "v_dot2_f32_bf16 v147, v215, v215, v147\n\t"
      "v_lshlrev_b32_e32 v128, 16, v208\n\t"
      "v_and_b32_e32 v129, 0xffff0000, v208\n\t"
      "v_lshlrev_b32_e32 v130, 16, v209\n\t"
      "v_pk_add_f32 v[144:145], v[144:145], v[146:147]\n\t"
      "v_and_b32_e32 v131, 0xffff0000, v209\n\t"
      "v_lshlrev_b32_e32 v132, 16, v210\n\t"
      "v_permlane16_swap_b32_e32 v144, v145\n\t"
      "v_add_f32_e32 v144, v144, v145\n\t"
      "v_mov_b32_e32 v145, v144\n\t"
      "v_and_b32_e32 v133, 0xffff0000, v210\n\t"
      "v_lshlrev_b32_e32 v134, 16, v211\n\t"
      "v_permlane32_swap_b32_e32 v144, v145\n\t"
      "v_add_f32_e32 v144, v144, v145\n\t"
      "v_mov_b32_e32 v145, v144\n\t"
      "v_and_b32_e32 v135, 0xffff0000, v211\n\t"
      "v_lshlrev_b32_e32 v136, 16, v212\n\t"
      "v_permlane16_swap_b32_e32 v144, v145\n\t"
      "s_mov_b64 exec, s[70:71]\n\t"
      "v_min_u32_e32 v144, -2, v144\n\t"
      "v_min_u32_e32 v145, -2, v145\n\t"
      "global_store_dwordx2 v231, v[144:145], s[58:59] sc1\n\t"
      "s_mov_b64 exec, -1\n\t"
      "s_barrier\n\t"
      "v_and_b32_e32 v137, 0xffff0000, v212\n\t"
      "v_lshlrev_b32_e32 v138, 16, v213\n\t"
      "v_and_b32_e32 v139, 0xffff0000, v213\n\t"
      "v_lshlrev_b32_e32 v140, 16, v214\n\t"
      "v_and_b32_e32 v141, 0xffff0000, v214\n\t"
      "v_lshlrev_b32_e32 v142, 16, v215\n\t"
      "v_and_b32_e32 v143, 0xffff0000, v215\n\t"
      "v_permlane16_swap_b32_e32 v208, v210\n\t"
      "v_permlane16_swap_b32_e32 v209, v211\n\t"
      "v_permlane16_swap_b32_e32 v212, v214\n\t"
      "v_permlane16_swap_b32_e32 v213, v215\n\t"
      "s_bitcmp0_b32 s27, 0\n\t"
      "s_cbranch_scc1 NTK0_%=\n\t"
      "ds_read_b64_tr_b16 v[0:1], v224 offset:16384\n\t"
      "ds_read_b64_tr_b16 v[2:3], v224 offset:16896\n\t"
      "ds_read_b64_tr_b16 v[4:5], v225 offset:16384\n\t"
      "ds_read_b64_tr_b16 v[6:7], v225 offset:16896\n\t"
      "ds_read_b64_tr_b16 v[8:9], v226 offset:16384\n\t"
      "ds_read_b64_tr_b16 v[10:11], v226 offset:16896\n\t"
      "ds_read_b64_tr_b16 v[12:13], v227 offset:16384\n\t"
      "ds_read_b64_tr_b16 v[14:15], v227 offset:16896\n\t"
      "ds_read_b64_tr_b16 v[16:17], v224 offset:49152\n\t"
      "ds_read_b64_tr_b16 v[18:19], v224 offset:49664\n\t"
      "ds_read_b64_tr_b16 v[20:21], v225 offset:49152\n\t"
      "ds_read_b64_tr_b16 v[22:23], v225 offset:49664\n\t"
      "ds_read_b64_tr_b16 v[24:25], v226 offset:49152\n\t"
      "ds_read_b64_tr_b16 v[26:27], v226 offset:49664\n\t"
      "ds_read_b64_tr_b16 v[28:29], v227 offset:49152\n\t"
      "ds_read_b64_tr_b16 v[30:31], v227 offset:49664\n\t"
      "ds_read_b64_tr_b16 v[32:33], v224\n\t"
      "ds_read_b64_tr_b16 v[34:35], v224 offset:512\n\t"
      "ds_read_b64_tr_b16 v[36:37], v225\n\t"
      "ds_read_b64_tr_b16 v[38:39], v225 offset:512\n\t"
      "ds_read_b64_tr_b16 v[40:41], v226\n\t"
      "ds_read_b64_tr_b16 v[42:43], v226 offset:512\n\t"
      "ds_read_b64_tr_b16 v[44:45], v227\n\t"
      "ds_read_b64_tr_b16 v[46:47], v227 offset:512\n\t"
      "ds_read_b64_tr_b16 v[48:49], v224 offset:32768\n\t"
      "ds_read_b64_tr_b16 v[50:51], v224 offset:33280\n\t"
      "ds_read_b64_tr_b16 v[52:53], v225 offset:32768\n\t"
      "ds_read_b64_tr_b16 v[54:55], v225 offset:33280\n\t"
      "ds_read_b64_tr_b16 v[56:57], v226 offset:32768\n\t"
      "ds_read_b64_tr_b16 v[58:59], v226 offset:33280\n\t"
      "ds_read_b64_tr_b16 v[60:61], v227 offset:32768\n\t"
      "ds_read_b64_tr_b16 v[62:63], v227 offset:33280\n\t"
      "NTK0_%=:\n\t"
      "s_sleep 6\n\t"
      "global_load_dwordx2 v[148:149], v231, s[46:47] sc1\n\t"
      "global_load_dwordx2 v[150:151], v231, s[46:47] offset:384 sc1\n\t"
      "s_waitcnt lgkmcnt(0)\n\t"
      "s_barrier\n\t"
      "s_waitcnt vmcnt(0)\n\t"
      "v_max3_u32 v232, v148, v149, v150\n\t"
      "v_max_u32_e32 v232, v232, v151\n\t"
      "v_cmp_eq_u32_e32 vcc, -1, v232\n\t"
      "s_cbranch_vccnz LSR_%=\n\t"
      "LSB_%=:\n\t"
      "v_cndmask_b32_e64 v150, v150, 0, s[70:71]\n\t"
      "v_cndmask_b32_e64 v151, v151, 0, s[70:71]\n\t"
      "v_pk_add_f32 v[144:145], v[148:149], v[150:151]\n\t"
      "s_nop 1\n\t"
      "v_permlane16_swap_b32_e32 v144, v145\n\t"
      "v_add_f32_e32 v144, v144, v145\n\t"
      "v_mov_b32_e32 v145, v144\n\t"
      "s_nop 1\n\t"
      "v_permlane32_swap_b32_e32 v144, v145\n\t"
      "v_add_f32_e32 v144, v144, v145\n\t"
      "v_mov_b32_e32 v145, v144\n\t"
      "s_nop 1\n\t"
      "v_permlane16_swap_b32_e32 v144, v145\n\t"
      "v_mul_f32_e32 v144, 0xbb124925, v144\n\t"
      "v_mul_f32_e32 v145, 0x3b124925, v145\n\t"
      "v_fma_f32 v145, -v144, v144, v145\n\t"
      "v_max_f32_e32 v145, 0, v145\n\t"
      "v_add_f32_e32 v146, 0x3727c5ac, v145\n\t"
      "v_fmac_f32_e32 v145, 0x3727c5ac, v146\n\t"
      "v_rsq_f32_e32 v145, v145\n\t"
      "s_nop 0\n\t"
      "v_mul_f32_e32 v144, v144, v145\n\t"
      "v_pk_fma_f32 v[128:129], v[128:129], v[144:145], v[144:145] op_sel:[0,1,0] op_sel_hi:[1,1,0]\n\t"
      "v_pk_fma_f32 v[130:131], v[130:131], v[144:145], v[144:145] op_sel:[0,1,0] op_sel_hi:[1,1,0]\n\t"
      "v_cvt_pk_bf16_f32 v152, v128, v129\n\t"
      "v_cvt_pk_bf16_f32 v153, v130, v131\n\t"
      "ds_write_b64 v233, v[152:153]\n\t"
      "v_pk_fma_f32 v[132:133], v[132:133], v[144:145], v[144:145] op_sel:[0,1,0] op_sel_hi:[1,1,0]\n\t"
      "v_pk_fma_f32 v[134:135], v[134:135], v[144:145], v[144:145] op_sel:[0,1,0] op_sel_hi:[1,1,0]\n\t"
      "v_cvt_pk_bf16_f32 v154, v132, v133\n\t"
      "v_cvt_pk_bf16_f32 v155, v134, v135\n\t"
      "ds_write_b64 v234, v[154:155]\n\t"
      "ds_read_b64_tr_b16 v[160:161], v237\n\t"
      "ds_read_b64_tr_b16 v[162:163], v238 offset:512\n\t"
      "v_pk_fma_f32 v[136:137], v[136:137], v[144:145], v[144:145] op_sel:[0,1,0] op_sel_hi:[1,1,0]\n\t"
      "v_pk_fma_f32 v[138:139], v[138:139], v[144:145], v[144:145] op_sel:[0,1,0] op_sel_hi:[1,1,0]\n\t"
      "v_cvt_pk_bf16_f32 v156, v136, v137\n\t"
      "v_cvt_pk_bf16_f32 v157, v138, v139\n\t"
      "ds_write_b64 v235, v[156:157]\n\t"
      "v_pk_fma_f32 v[140:141], v[140:141], v[144:145], v[144:145] op_sel:[0,1,0] op_sel_hi:[1,1,0]\n\t"
      "v_pk_fma_f32 v[142:143], v[142:143], v[144:145], v[144:145] op_sel:[0,1,0] op_sel_hi:[1,1,0]\n\t"
      "v_cvt_pk_bf16_f32 v158, v140, v141\n\t"
      "v_cvt_pk_bf16_f32 v159, v142, v143\n\t"
      "ds_write_b64 v236, v[158:159]\n\t"
      "ds_read_b64_tr_b16 v[164:165], v239\n\t"
      "ds_read_b64_tr_b16 v[166:167], v240 offset:512\n\t"
      "s_waitcnt lgkmcnt(4)\n\t"
      "v_min_u32_e32 v160, -2, v160\n\t"
      "v_min_u32_e32 v161, -2, v161\n\t"
      "v_min_u32_e32 v162, -2, v162\n\t"
      "v_min_u32_e32 v163, -2, v163\n\t"
      "global_store_dwordx4 v229, v[160:163], s[48:49] sc1\n\t"
      "s_waitcnt lgkmcnt(0)\n\t"
      "v_min_u32_e32 v164, -2, v164\n\t"
      "v_min_u32_e32 v165, -2, v165\n\t"
      "v_min_u32_e32 v166, -2, v166\n\t"
      "v_min_u32_e32 v167, -2, v167\n\t"
      "global_store_dwordx4 v230, v[164:167], s[48:49] sc1\n\t"
      "s_setprio 0\n\t"
      "s_barrier\n\t"
      "s_bitcmp0_b32 s27, 0\n\t"
      "s_cbranch_scc1 G4C_%=\n\t"
      "s_mul_i32 s34, s30, 65536\n\t"
      "s_mul_i32 s35, s24, 8192\n\t"
      "s_add_u32 s34, s34, s35\n\t"
      "s_add_u32 s64, s48, s34\n\t"
      "s_addc_u32 s65, s49, 0\n\t"
      "v_add_u32_e32 v250, 4096, v244\n\t"
      "global_load_dwordx4 v[128:131], v250, s[64:65] offset:-4096 sc1\n\t"
      "global_load_dwordx4 v[132:135], v250, s[64:65] offset:-3072 sc1\n\t"
      "global_load_dwordx4 v[136:139], v250, s[64:65] offset:-2048 sc1\n\t"
      "global_load_dwordx4 v[140:143], v250, s[64:65] offset:-1024 sc1\n\t"
      "global_load_dwordx4 v[144:147], v250, s[64:65] sc1\n\t"
      "global_load_dwordx4 v[148:151], v250, s[64:65] offset:1024 sc1\n\t"
      "global_load_dwordx4 v[152:155], v250, s[64:65] offset:2048 sc1\n\t"
      "global_load_dwordx4 v[156:159], v250, s[64:65] offset:3072 sc1\n\t"
      "global_store_dwordx4 v241, v[208:211], s[20:21] offset:-2048 sc1\n\t"
      "global_store_dwordx4 v241, v[212:215], s[20:21] offset:2048 sc1\n\t"
      "ds_read_b64_tr_b16 v[64:65], v224 offset:16384\n\t"
      "ds_read_b64_tr_b16 v[66:67], v224 offset:16896\n\t"
      "ds_read_b64_tr_b16 v[68:69], v225 offset:16384\n\t"
      "ds_read_b64_tr_b16 v[70:71], v225 offset:16896\n\t"
      "ds_read_b64_tr_b16 v[72:73], v226 offset:16384\n\t"
      "ds_read_b64_tr_b16 v[74:75], v226 offset:16896\n\t"
      "ds_read_b64_tr_b16 v[76:77], v227 offset:16384\n\t"
      "ds_read_b64_tr_b16 v[78:79], v227 offset:16896\n\t"
      "ds_read_b64_tr_b16 v[80:81], v224 offset:49152\n\t"
      "ds_read_b64_tr_b16 v[82:83], v224 offset:49664\n\t"
      "ds_read_b64_tr_b16 v[84:85], v225 offset:49152\n\t"
      "ds_read_b64_tr_b16 v[86:87], v225 offset:49664\n\t"
      "ds_read_b64_tr_b16 v[88:89], v226 offset:49152\n\t"
      "ds_read_b64_tr_b16 v[90:91], v226 offset:49664\n\t"
      "ds_read_b64_tr_b16 v[92:93], v227 offset:49152\n\t"
      "ds_read_b64_tr_b16 v[94:95], v227 offset:49664\n\t"
      "ds_read_b64_tr_b16 v[96:97], v224\n\t"
      "ds_read_b64_tr_b16 v[98:99], v224 offset:512\n\t"
      "ds_read_b64_tr_b16 v[100:101], v225\n\t"
      "ds_read_b64_tr_b16 v[102:103], v225 offset:512\n\t"
      "ds_read_b64_tr_b16 v[104:105], v226\n\t"
      "ds_read_b64_tr_b16 v[106:107], v226 offset:512\n\t"
      "ds_read_b64_tr_b16 v[108:109], v227\n\t"
      "ds_read_b64_tr_b16 v[110:111], v227 offset:512\n\t"
      "ds_read_b64_tr_b16 v[112:113], v224 offset:32768\n\t"
      "ds_read_b64_tr_b16 v[114:115], v224 offset:33280\n\t"
      "ds_read_b64_tr_b16 v[116:117], v225 offset:32768\n\t"
      "ds_read_b64_tr_b16 v[118:119], v225 offset:33280\n\t"
      "ds_read_b64_tr_b16 v[120:121], v226 offset:32768\n\t"
      "ds_read_b64_tr_b16 v[122:123], v226 offset:33280\n\t"
      "ds_read_b64_tr_b16 v[124:125], v227 offset:32768\n\t"
      "ds_read_b64_tr_b16 v[126:127], v227 offset:33280\n\t"
      "s_waitcnt vmcnt(2) lgkmcnt(0)\n\t"
      "GST_%=:\n\t"
      "s_add_u32 s66, s64, 65536\n\t"
      "s_addc_u32 s67, s65, 0\n\t"
      "s_add_u32 s68, s66, 65536\n\t"
      "s_addc_u32 s69, s67, 0\n\t"
      "s_add_u32 s70, s68, 65536\n\t"
      "s_addc_u32 s71, s69, 0\n\t"
      "s_add_u32 s72, s70, 65536\n\t"
      "s_addc_u32 s73, s71, 0\n\t"
      "s_add_u32 s74, s72, 65536\n\t"
      "s_addc_u32 s75, s73, 0\n\t"
      "s_add_u32 s76, s74, 65536\n\t"
      "s_addc_u32 s77, s75, 0\n\t"
      "s_mul_i32 s34, s30, 28\n\t"
      "s_add_u32 s34, s34, s29\n\t"
      "s_lshl_b32 s34, s34, 11\n\t"
      "s_add_u32 s34, s34, 10223616\n\t"
      "s_add_u32 s62, s20, s34\n\t"
      "s_addc_u32 s63, s21, 0\n\t"
      "v_lshlrev_b32_e32 v248, 16, v249\n\t"
      "v_and_b32_e32 v249, 0xffff0000, v249\n\t"
      "v_mov_b32_e32 v252, 0xbdd2d3e8\n\t"
      "v_xor_b32_e32 v253, 128, v245\n\t"
      "v_max3_u32 v251, v128, v129, v130\n\t"
      "v_max3_u32 v251, v251, v131, v132\n\t"
      "v_max3_u32 v251, v251, v133, v134\n\t"
      "v_max3_u32 v251, v251, v135, v136\n\t"
      "v_max3_u32 v251, v251, v137, v138\n\t"
      "v_max3_u32 v251, v251, v139, v140\n\t"
      "v_max3_u32 v251, v251, v141, v142\n\t"
      "v_max3_u32 v251, v251, v143, v144\n\t"
      "v_max3_u32 v251, v251, v145, v146\n\t"
      "v_max3_u32 v251, v251, v147, v148\n\t"
      "v_max3_u32 v251, v251, v149, v150\n\t"
      "v_max3_u32 v251, v251, v151, v152\n\t"
      "v_max3_u32 v251, v251, v153, v154\n\t"
      "v_max3_u32 v251, v251, v155, v156\n\t"
      "v_max3_u32 v251, v251, v157, v158\n\t"
      "v_max_u32_e32 v251, v251, v159\n\t"
      "v_cmp_eq_u32_e32 vcc, -1, v251\n\t"
      "s_cbranch_vccnz LA30R_%=\n\t"
      "LA30B_%=:\n\t"
      "v_mfma_f32_16x16x32_bf16 v[208:211], v[0:3], v[128:131], 0\n\t"
      "global_load_dwordx4 v[160:163], v250, s[66:67] offset:-4096 sc1\n\t"
      "v_mfma_f32_16x16x32_bf16 v[208:211], v[16:19], v[132:135], v[208:211]\n\t"
      "v_mfma_f32_16x16x32_bf16 v[208:211], v[32:35], v[136:139], v[208:211]\n\t"
      "global_load_dwordx4 v[164:167], v250, s[66:67] offset:-3072 sc1\n\t"
      "v_mfma_f32_16x16x32_bf16 v[208:211], v[48:51], v[140:143], v[208:211]\n\t"
      "v_mfma_f32_16x16x32_bf16 v[208:211], v[64:67], v[144:147], v[208:211]\n\t"
      "global_load_dwordx4 v[168:171], v250, s[66:67] offset:-2048 sc1\n\t"
      "v_mfma_f32_16x16x32_bf16 v[208:211], v[80:83], v[148:151], v[208:211]\n\t"
      "s_barrier\n\t"
      "v_mfma_f32_16x16x32_bf16 v[208:211], v[96:99], v[152:155], v[208:211]\n\t"
      "s_lshl_b32 s34, s2, 7\n\t"
      "s_add_u32 s34, s34, 65536\n\t"
      "v_mov_b32_e32 v230, s34\n\t"
      "s_cmp_lg_u32 s24, 3\n\t"
      "s_cbranch_scc1 SK17_%=\n\t"
      "global_load_dwordx2 v[228:229], v230, s[60:61] sc1\n\t"
      "SK17_%=:\n\t"
      "global_load_dwordx4 v[172:175], v250, s[66:67] offset:-1024 sc1\n\t"
      "v_mfma_f32_16x16x32_bf16 v[208:211], v[112:115], v[156:159], v[208:211]\n\t"
      "v_mfma_f32_16x16x32_bf16 v[212:215], v[4:7], v[128:131], 0\n\t"
      "global_load_dwordx4 v[176:179], v250, s[66:67] sc1\n\t"
      "v_mfma_f32_16x16x32_bf16 v[212:215], v[20:23], v[132:135], v[212:215]\n\t"
      "v_mfma_f32_16x16x32_bf16 v[212:215], v[36:39], v[136:139], v[212:215]\n\t"
      "global_load_dwordx4 v[180:183], v250, s[66:67] offset:1024 sc1\n\t"
      "v_mfma_f32_16x16x32_bf16 v[212:215], v[52:55], v[140:143], v[212:215]\n\t"
      "v_mfma_f32_16x16x32_bf16 v[212:215], v[68:71], v[144:147], v[212:215]\n\t"
      "global_load_dwordx4 v[184:187], v250, s[66:67] offset:2048 sc1\n\t"
      "v_mfma_f32_16x16x32_bf16 v[212:215], v[84:87], v[148:151], v[212:215]\n\t"
      "v_mfma_f32_16x16x32_bf16 v[212:215], v[100:103], v[152:155], v[212:215]\n\t"
      "ds_write_b128 v245, v[208:211]\n\t"
      "global_load_dwordx4 v[188:191], v250, s[66:67] offset:3072 sc1\n\t"
      "v_mfma_f32_16x16x32_bf16 v[212:215], v[116:119], v[156:159], v[212:215]\n\t"
      "v_mfma_f32_16x16x32_bf16 v[216:219], v[8:11], v[128:131], 0\n\t"
      "global_load_dwordx4 v[192:195], v250, s[68:69] offset:-4096 sc1\n\t"
      "v_mfma_f32_16x16x32_bf16 v[216:219], v[24:27], v[132:135], v[216:219]\n\t"
      "v_mfma_f32_16x16x32_bf16 v[216:219], v[40:43], v[136:139], v[216:219]\n\t"
      "global_load_dwordx4 v[196:199], v250, s[68:69] offset:-3072 sc1\n\t"
      "v_mfma_f32_16x16x32_bf16 v[216:219], v[56:59], v[140:143], v[216:219]\n\t"
      "v_mfma_f32_16x16x32_bf16 v[216:219], v[72:75], v[144:147], v[216:219]\n\t"
      "global_load_dwordx4 v[200:203], v250, s[68:69] offset:-2048 sc1\n\t"
      "v_mfma_f32_16x16x32_bf16 v[216:219], v[88:91], v[148:151], v[216:219]\n\t"
      "v_mfma_f32_16x16x32_bf16 v[216:219], v[104:107], v[152:155], v[216:219]\n\t"
      "ds_write_b128 v253, v[212:215] offset:1024\n\t"
      "global_load_dwordx4 v[204:207], v250, s[68:69] offset:-1024 sc1\n\t"
      "v_mfma_f32_16x16x32_bf16 v[216:219], v[120:123], v[156:159], v[216:219]\n\t"
      "v_mfma_f32_16x16x32_bf16 v[220:223], v[12:15], v[128:131], 0\n\t"
      "global_load_dwordx4 v[128:131], v250, s[68:69] sc1\n\t"
      "v_mfma_f32_16x16x32_bf16 v[220:223], v[28:31], v[132:135], v[220:223]\n\t"
      "v_mfma_f32_16x16x32_bf16 v[220:223], v[44:47], v[136:139], v[220:223]\n\t"
      "v_mfma_f32_16x16x32_bf16 v[220:223], v[60:63], v[140:143], v[220:223]\n\t"
      "v_mfma_f32_16x16x32_bf16 v[220:223], v[76:79], v[144:147], v[220:223]\n\t"
      "global_load_dwordx4 v[132:135], v250, s[68:69] offset:1024 sc1\n\t"
      "v_mfma_f32_16x16x32_bf16 v[220:223], v[92:95], v[148:151], v[220:223]\n\t"
      "v_mfma_f32_16x16x32_bf16 v[220:223], v[108:111], v[152:155], v[220:223]\n\t"
      "ds_write_b128 v245, v[216:219] offset:2048\n\t"
      "v_mfma_f32_16x16x32_bf16 v[220:223], v[124:127], v[156:159], v[220:223]\n\t"
      "s_waitcnt vmcnt(10)\n\t"
      "v_mfma_f32_16x16x32_bf16 v[224:227], v[0:3], v[160:163], 0\n\t"
      "global_load_dwordx4 v[136:139], v250, s[68:69] offset:2048 sc1\n\t"
      "v_mfma_f32_16x16x32_bf16 v[224:227], v[16:19], v[164:167], v[224:227]\n\t"
      "s_nop 3\n\t"
      "ds_write_b128 v253, v[220:223] offset:3072\n\t"
      "v_mfma_f32_16x16x32_bf16 v[224:227], v[32:35], v[168:171], v[224:227]\n\t"
      "v_mfma_f32_16x16x32_bf16 v[224:227], v[48:51], v[172:175], v[224:227]\n\t"
      "s_waitcnt vmcnt(7)\n\t"
      "v_mfma_f32_16x16x32_bf16 v[224:227], v[64:67], v[176:179], v[224:227]\n\t"
      "global_load_dwordx4 v[140:143], v250, s[68:69] offset:3072 sc1\n\t"
      "v_mfma_f32_16x16x32_bf16 v[224:227], v[80:83], v[180:183], v[224:227]\n\t"
      "s_cmp_lg_u32 s24, 3\n\t"
      "s_cbranch_scc1 LF6B_%=\n\t"
      "v_cmp_ne_u64_e32 vcc, s[22:23], v[228:229]\n\t"
      "s_cbranch_vccnz LF6R_%=\n\t"
      "LF6B_%=:\n\t"
      "s_waitcnt lgkmcnt(0)\n\t"
      "s_barrier\n\t"
      "s_cmp_lg_u32 s24, 3\n\t"
      "s_cbranch_scc1 HF3_%=\n\t"
      "s_lshl3_add_u32 s34, s25, s26\n\t"
      "s_lshl_b32 s34, s34, 7\n\t"
      "s_add_u32 s34, s34, 98304\n\t"
      "v_mov_b32_e32 v238, s34\n\t"
      "v_mov_b32_e32 v236, s22\n\t"
      "v_mov_b32_e32 v237, s23\n\t"
      "s_mov_b64 exec, 1\n\t"
      "global_store_dwordx2 v238, v[236:237], s[60:61] sc1\n\t"
      "s_mov_b64 exec, -1\n\t"
      "HF3_%=:\n\t"
      "ds_read_b64 v[228:229], v246\n\t"
      "ds_read_b64 v[230:231], v246 offset:4096\n\t"
      "v_mfma_f32_16x16x32_bf16 v[224:227], v[96:99], v[184:187], v[224:227]\n\t"
      "ds_read_b64 v[232:233], v246 offset:8192\n\t"
      "ds_read_b64 v[234:235], v246 offset:12288\n\t"
      "v_mfma_f32_16x16x32_bf16 v[224:227], v[112:115], v[188:191], v[224:227]\n\t"
      "ds_read_b64 v[236:237], v246 offset:16384\n\t"
      "ds_read_b64 v[238:239], v246 offset:20480\n\t"
      "v_mfma_f32_16x16x32_bf16 v[208:211], v[4:7], v[160:163], 0\n\t"
      "ds_read_b64 v[240:241], v246 offset:24576\n\t"
      "ds_read_b64 v[242:243], v246 offset:28672\n\t"
      "global_load_dwordx4 v[144:147], v250, s[70:71] offset:-4096 sc1\n\t"
      "v_mfma_f32_16x16x32_bf16 v[208:211], v[20:23], v[164:167], v[208:211]\n\t"
      "v_mfma_f32_16x16x32_bf16 v[208:211], v[36:39], v[168:171], v[208:211]\n\t"
      "s_waitcnt lgkmcnt(4)\n\t"
      "v_add_f32_e32 v228, v228, v230\n\t"
      "v_add_f32_e32 v232, v232, v234\n\t"
      "v_cmp_u_f32_e32 vcc, v224, v224\n\t"
      "s_cbranch_vccnz LA31R_%=\n\t"
      "LA31B_%=:\n\t"
      "v_mfma_f32_16x16x32_bf16 v[208:211], v[52:55], v[172:175], v[208:211]\n\t"
      "v_add_f32_e32 v229, v229, v231\n\t"
      "v_add_f32_e32 v233, v233, v235\n\t"
      "v_mfma_f32_16x16x32_bf16 v[208:211], v[68:71], v[176:179], v[208:211]\n\t"
      "s_waitcnt lgkmcnt(0)\n\t"
      "v_add_f32_e32 v236, v236, v238\n\t"
      "v_add_f32_e32 v240, v240, v242\n\t"
      "global_load_dwordx4 v[148:151], v250, s[70:71] offset:-3072 sc1\n\t"
      "v_mfma_f32_16x16x32_bf16 v[208:211], v[84:87], v[180:183], v[208:211]\n\t"
      "v_add_f32_e32 v237, v237, v239\n\t"
      "v_add_f32_e32 v241, v241, v243\n\t"
      "v_mfma_f32_16x16x32_bf16 v[208:211], v[100:103], v[184:187], v[208:211]\n\t"
      "v_add_f32_e32 v228, v228, v232\n\t"
      "v_add_f32_e32 v236, v236, v240\n\t"
      "v_add_f32_e32 v229, v229, v233\n\t"
      "ds_write_b128 v245, v[224:227] offset:32768\n\t"
      "v_mfma_f32_16x16x32_bf16 v[208:211], v[116:119], v[188:191], v[208:211]\n\t"
      "v_add_f32_e32 v237, v237, v241\n\t"
      "v_add_f32_e32 v228, v228, v248\n\t"
      "v_mfma_f32_16x16x32_bf16 v[212:215], v[8:11], v[160:163], 0\n\t"
      "v_add_f32_e32 v229, v229, v249\n\t"
      "v_add_f32_e32 v228, v228, v236\n\t"
      "v_add_f32_e32 v229, v229, v237\n\t"
      "global_load_dwordx4 v[152:155], v250, s[70:71] offset:-2048 sc1\n\t"
      "v_mfma_f32_16x16x32_bf16 v[212:215], v[24:27], v[164:167], v[212:215]\n\t"
      "v_mul_f32_e32 v230, v228, v228\n\t"
      "v_mul_f32_e32 v231, v229, v229\n\t"
      "v_mfma_f32_16x16x32_bf16 v[212:215], v[40:43], v[168:171], v[212:215]\n\t"
      "v_fmaak_f32 v230, v252, v230, 0xc0135761\n\t"
      "v_fmaak_f32 v231, v252, v231, 0xc0135761\n\t"
      "v_mul_f32_e32 v230, v228, v230\n\t"
      "v_mfma_f32_16x16x32_bf16 v[212:215], v[56:59], v[172:175], v[212:215]\n\t"
      "v_mul_f32_e32 v231, v229, v231\n\t"
      "v_exp_f32_e32 v230, v230\n\t"
      "v_mfma_f32_16x16x32_bf16 v[212:215], v[72:75], v[176:179], v[212:215]\n\t"
      "v_exp_f32_e32 v231, v231\n\t"
      "v_add_f32_e32 v230, 1.0, v230\n\t"
      "v_add_f32_e32 v231, 1.0, v231\n\t"
      "global_load_dwordx4 v[156:159], v250, s[70:71] offset:-1024 sc1\n\t"
      "v_mfma_f32_16x16x32_bf16 v[212:215], v[88:91], v[180:183], v[212:215]\n\t"
      "v_rcp_f32_e32 v230, v230\n\t"
      "v_rcp_f32_e32 v231, v231\n\t"
      "v_mfma_f32_16x16x32_bf16 v[212:215], v[104:107], v[184:187], v[212:215]\n\t"
      "v_mul_f32_e32 v228, v228, v230\n\t"
      "v_mul_f32_e32 v229, v229, v231\n\t"
      "v_cvt_pk_bf16_f32 v232, v228, v229\n\t"
      "ds_write_b128 v253, v[208:211] offset:33792\n\t"
      "v_mfma_f32_16x16x32_bf16 v[212:215], v[120:123], v[188:191], v[212:215]\n\t"
      "v_min_u32_e32 v254, -2, v232\n\t"
      "global_store_dword v247, v254, s[62:63] sc1\n\t"
      "s_add_u32 s62, s62, 57344\n\t"
      "s_addc_u32 s63, s63, 0\n\t"
      "v_mfma_f32_16x16x32_bf16 v[216:219], v[12:15], v[160:163], 0\n\t"
      "global_load_dwordx4 v[160:163], v250, s[70:71] sc1\n\t"
      "v_mfma_f32_16x16x32_bf16 v[216:219], v[28:31], v[164:167], v[216:219]\n\t"
      "v_mfma_f32_16x16x32_bf16 v[216:219], v[44:47], v[168:171], v[216:219]\n\t"
      "v_mfma_f32_16x16x32_bf16 v[216:219], v[60:63], v[172:175], v[216:219]\n\t"
      "v_mfma_f32_16x16x32_bf16 v[216:219], v[76:79], v[176:179], v[216:219]\n\t"
      "global_load_dwordx4 v[164:167], v250, s[70:71] offset:1024 sc1\n\t"
      "v_mfma_f32_16x16x32_bf16 v[216:219], v[92:95], v[180:183], v[216:219]\n\t"
      "v_mfma_f32_16x16x32_bf16 v[216:219], v[108:111], v[184:187], v[216:219]\n\t"
      "ds_write_b128 v245, v[212:215] offset:34816\n\t"
      "v_mfma_f32_16x16x32_bf16 v[216:219], v[124:127], v[188:191], v[216:219]\n\t"
      "s_waitcnt vmcnt(11)\n\t"
      "v_mfma_f32_16x16x32_bf16 v[220:223], v[0:3], v[192:195], 0\n\t"
      "global_load_dwordx4 v[168:171], v250, s[70:71] offset:2048 sc1\n\t"
      "v_mfma_f32_16x16x32_bf16 v[220:223], v[16:19], v[196:199], v[220:223]\n\t"
      "s_nop 3\n\t"
      "ds_write_b128 v253, v[216:219] offset:35840\n\t"
      "v_mfma_f32_16x16x32_bf16 v[220:223], v[32:35], v[200:203], v[220:223]\n\t"
      "v_mfma_f32_16x16x32_bf16 v[220:223], v[48:51], v[204:207], v[220:223]\n\t"
      "s_waitcnt vmcnt(8)\n\t"
      "v_mfma_f32_16x16x32_bf16 v[220:223], v[64:67], v[128:131], v[220:223]\n\t"
      "global_load_dwordx4 v[172:175], v250, s[70:71] offset:3072 sc1\n\t"
      "v_mfma_f32_16x16x32_bf16 v[220:223], v[80:83], v[132:135], v[220:223]\n\t"
      "s_waitcnt lgkmcnt(0)\n\t"
      "s_barrier\n\t"
      "ds_read_b64 v[228:229], v246 offset:32768\n\t"
      "ds_read_b64 v[230:231], v246 offset:36864\n\t"
      "v_mfma_f32_16x16x32_bf16 v[220:223], v[96:99], v[136:139], v[220:223]\n\t"
      "ds_read_b64 v[232:233], v246 offset:40960\n\t"
      "ds_read_b64 v[234:235], v246 offset:45056\n\t"
      "v_mfma_f32_16x16x32_bf16 v[220:223], v[112:115], v[140:143], v[220:223]\n\t"
      "ds_read_b64 v[236:237], v246 offset:49152\n\t"
      "ds_read_b64 v[238:239], v246 offset:53248\n\t"
      "v_mfma_f32_16x16x32_bf16 v[224:227], v[4:7], v[192:195], 0\n\t"
      "ds_read_b64 v[240:241], v246 offset:57344\n\t"
      "ds_read_b64 v[242:243], v246 offset:61440\n\t"
      "global_load_dwordx4 v[176:179], v250, s[72:73] offset:-4096 sc1\n\t"
      "v_mfma_f32_16x16x32_bf16 v[224:227], v[20:23], v[196:199], v[224:227]\n\t"
      "v_mfma_f32_16x16x32_bf16 v[224:227], v[36:39], v[200:203], v[224:227]\n\t"
      "s_waitcnt lgkmcnt(4)\n\t"
      "v_add_f32_e32 v228, v228, v230\n\t"
      "v_add_f32_e32 v232, v232, v234\n\t"
      "v_cmp_u_f32_e32 vcc, v220, v220\n\t"
      "s_cbranch_vccnz LA32R_%=\n\t"
      "LA32B_%=:\n\t"
      "v_mfma_f32_16x16x32_bf16 v[224:227], v[52:55], v[204:207], v[224:227]\n\t"
      "v_add_f32_e32 v229, v229, v231\n\t"
      "v_add_f32_e32 v233, v233, v235\n\t"
      "v_mfma_f32_16x16x32_bf16 v[224:227], v[68:71], v[128:131], v[224:227]\n\t"
      "s_waitcnt lgkmcnt(0)\n\t"
      "v_add_f32_e32 v236, v236, v238\n\t"
      "v_add_f32_e32 v240, v240, v242\n\t"
      "global_load_dwordx4 v[180:183], v250, s[72:73] offset:-3072 sc1\n\t"
      "v_mfma_f32_16x16x32_bf16 v[224:227], v[84:87], v[132:135], v[224:227]\n\t"
      "v_add_f32_e32 v237, v237, v239\n\t"
      "v_add_f32_e32 v241, v241, v243\n\t"
      "v_mfma_f32_16x16x32_bf16 v[224:227], v[100:103], v[136:139], v[224:227]\n\t"
      "v_add_f32_e32 v228, v228, v232\n\t"
      "v_add_f32_e32 v236, v236, v240\n\t"
      "v_add_f32_e32 v229, v229, v233\n\t"
      "ds_write_b128 v245, v[220:223]\n\t"
      "v_mfma_f32_16x16x32_bf16 v[224:227], v[116:119], v[140:143], v[224:227]\n\t"
      "v_add_f32_e32 v237, v237, v241\n\t"
      "v_add_f32_e32 v228, v228, v248\n\t"
      "v_mfma_f32_16x16x32_bf16 v[208:211], v[8:11], v[192:195], 0\n\t"
      "v_add_f32_e32 v229, v229, v249\n\t"
      "v_add_f32_e32 v228, v228, v236\n\t"
      "v_add_f32_e32 v229, v229, v237\n\t"
      "global_load_dwordx4 v[184:187], v250, s[72:73] offset:-2048 sc1\n\t"
      "v_mfma_f32_16x16x32_bf16 v[208:211], v[24:27], v[196:199], v[208:211]\n\t"
      "v_mul_f32_e32 v230, v228, v228\n\t"
      "v_mul_f32_e32 v231, v229, v229\n\t"
      "v_mfma_f32_16x16x32_bf16 v[208:211], v[40:43], v[200:203], v[208:211]\n\t"
      "v_fmaak_f32 v230, v252, v230, 0xc0135761\n\t"
      "v_fmaak_f32 v231, v252, v231, 0xc0135761\n\t"
      "v_mul_f32_e32 v230, v228, v230\n\t"
      "v_mfma_f32_16x16x32_bf16 v[208:211], v[56:59], v[204:207], v[208:211]\n\t"
      "v_mul_f32_e32 v231, v229, v231\n\t"
      "v_exp_f32_e32 v230, v230\n\t"
      "v_mfma_f32_16x16x32_bf16 v[208:211], v[72:75], v[128:131], v[208:211]\n\t"
      "v_exp_f32_e32 v231, v231\n\t"
      "v_add_f32_e32 v230, 1.0, v230\n\t"
      "v_add_f32_e32 v231, 1.0, v231\n\t"
      "global_load_dwordx4 v[188:191], v250, s[72:73] offset:-1024 sc1\n\t"
      "v_mfma_f32_16x16x32_bf16 v[208:211], v[88:91], v[132:135], v[208:211]\n\t"
      "v_rcp_f32_e32 v230, v230\n\t"
      "v_rcp_f32_e32 v231, v231\n\t"
      "v_mfma_f32_16x16x32_bf16 v[208:211], v[104:107], v[136:139], v[208:211]\n\t"
      "v_mul_f32_e32 v228, v228, v230\n\t"
      "v_mul_f32_e32 v229, v229, v231\n\t"
      "v_cvt_pk_bf16_f32 v232, v228, v229\n\t"
      "ds_write_b128 v253, v[224:227] offset:1024\n\t"
      "v_mfma_f32_16x16x32_bf16 v[208:211], v[120:123], v[140:143], v[208:211]\n\t"
      "v_min_u32_e32 v254, -2, v232\n\t"
      "global_store_dword v247, v254, s[62:63] sc1\n\t"
      "s_add_u32 s62, s62, 57344\n\t"
      "s_addc_u32 s63, s63, 0\n\t"
      "v_mfma_f32_16x16x32_bf16 v[212:215], v[12:15], v[192:195], 0\n\t"
      "global_load_dwordx4 v[192:195], v250, s[72:73] sc1\n\t"
      "v_mfma_f32_16x16x32_bf16 v[212:215], v[28:31], v[196:199], v[212:215]\n\t"
      "v_mfma_f32_16x16x32_bf16 v[212:215], v[44:47], v[200:203], v[212:215]\n\t"
      "v_mfma_f32_16x16x32_bf16 v[212:215], v[60:63], v[204:207], v[212:215]\n\t"
      "v_mfma_f32_16x16x32_bf16 v[212:215], v[76:79], v[128:131], v[212:215]\n\t"
      "global_load_dwordx4 v[196:199], v250, s[72:73] offset:1024 sc1\n\t"
      "v_mfma_f32_16x16x32_bf16 v[212:215], v[92:95], v[132:135], v[212:215]\n\t"
      "v_mfma_f32_16x16x32_bf16 v[212:215], v[108:111], v[136:139], v[212:215]\n\t"
      "ds_write_b128 v245, v[208:211] offset:2048\n\t"
      "v_mfma_f32_16x16x32_bf16 v[212:215], v[124:127], v[140:143], v[212:215]\n\t"
      "s_waitcnt vmcnt(12)\n\t"
      "v_mfma_f32_16x16x32_bf16 v[216:219], v[0:3], v[144:147], 0\n\t"
      "global_load_dwordx4 v[200:203], v250, s[72:73] offset:2048 sc1\n\t"
      "v_mfma_f32_16x16x32_bf16 v[216:219], v[16:19], v[148:151], v[216:219]\n\t"
      "s_nop 3\n\t"
      "ds_write_b128 v253, v[212:215] offset:3072\n\t"
      "v_mfma_f32_16x16x32_bf16 v[216:219], v[32:35], v[152:155], v[216:219]\n\t"
      "v_mfma_f32_16x16x32_bf16 v[216:219], v[48:51], v[156:159], v[216:219]\n\t"
      "s_waitcnt vmcnt(8)\n\t"
      "v_mfma_f32_16x16x32_bf16 v[216:219], v[64:67], v[160:163], v[216:219]\n\t"
      "global_load_dwordx4 v[204:207], v250, s[72:73] offset:3072 sc1\n\t"
      "v_mfma_f32_16x16x32_bf16 v[216:219], v[80:83], v[164:167], v[216:219]\n\t"
      "s_waitcnt lgkmcnt(0)\n\t"
      "s_barrier\n\t"
      "ds_read_b64 v[228:229], v246\n\t"
      "ds_read_b64 v[230:231], v246 offset:4096\n\t"
      "v_mfma_f32_16x16x32_bf16 v[216:219], v[96:99], v[168:171], v[216:219]\n\t"
      "ds_read_b64 v[232:233], v246 offset:8192\n\t"
      "ds_read_b64 v[234:235], v246 offset:12288\n\t"
      "v_mfma_f32_16x16x32_bf16 v[216:219], v[112:115], v[172:175], v[216:219]\n\t"
      "ds_read_b64 v[236:237], v246 offset:16384\n\t"
      "ds_read_b64 v[238:239], v246 offset:20480\n\t"
      "v_mfma_f32_16x16x32_bf16 v[220:223], v[4:7], v[144:147], 0\n\t"
      "ds_read_b64 v[240:241], v246 offset:24576\n\t"
      "ds_read_b64 v[242:243], v246 offset:28672\n\t"
      "global_load_dwordx4 v[128:131], v250, s[74:75] offset:-4096 sc1\n\t"
      "v_mfma_f32_16x16x32_bf16 v[220:223], v[20:23], v[148:151], v[220:223]\n\t"
      "v_mfma_f32_16x16x32_bf16 v[220:223], v[36:39], v[152:155], v[220:223]\n\t"
      "s_waitcnt lgkmcnt(4)\n\t"
      "v_add_f32_e32 v228, v228, v230\n\t"
      "v_add_f32_e32 v232, v232, v234\n\t"
      "v_cmp_u_f32_e32 vcc, v216, v216\n\t"
      "s_cbranch_vccnz LA33R_%=\n\t"
      "LA33B_%=:\n\t"
      "v_mfma_f32_16x16x32_bf16 v[220:223], v[52:55], v[156:159], v[220:223]\n\t"
      "v_add_f32_e32 v229, v229, v231\n\t"
      "v_add_f32_e32 v233, v233, v235\n\t"
      "v_mfma_f32_16x16x32_bf16 v[220:223], v[68:71], v[160:163], v[220:223]\n\t"
      "s_waitcnt lgkmcnt(0)\n\t"
      "v_add_f32_e32 v236, v236, v238\n\t"
      "v_add_f32_e32 v240, v240, v242\n\t"
      "global_load_dwordx4 v[132:135], v250, s[74:75] offset:-3072 sc1\n\t"
      "v_mfma_f32_16x16x32_bf16 v[220:223], v[84:87], v[164:167], v[220:223]\n\t"
      "v_add_f32_e32 v237, v237, v239\n\t"
      "v_add_f32_e32 v241, v241, v243\n\t"
      "v_mfma_f32_16x16x32_bf16 v[220:223], v[100:103], v[168:171], v[220:223]\n\t"
      "v_add_f32_e32 v228, v228, v232\n\t"
      "v_add_f32_e32 v236, v236, v240\n\t"
      "v_add_f32_e32 v229, v229, v233\n\t"
      "ds_write_b128 v245, v[216:219] offset:32768\n\t"
      "v_mfma_f32_16x16x32_bf16 v[220:223], v[116:119], v[172:175], v[220:223]\n\t"
      "v_add_f32_e32 v237, v237, v241\n\t"
      "v_add_f32_e32 v228, v228, v248\n\t"
      "v_mfma_f32_16x16x32_bf16 v[224:227], v[8:11], v[144:147], 0\n\t"
      "v_add_f32_e32 v229, v229, v249\n\t"
      "v_add_f32_e32 v228, v228, v236\n\t"
      "v_add_f32_e32 v229, v229, v237\n\t"
      "global_load_dwordx4 v[136:139], v250, s[74:75] offset:-2048 sc1\n\t"
      "v_mfma_f32_16x16x32_bf16 v[224:227], v[24:27], v[148:151], v[224:227]\n\t"
      "v_mul_f32_e32 v230, v228, v228\n\t"
      "v_mul_f32_e32 v231, v229, v229\n\t"
      "v_mfma_f32_16x16x32_bf16 v[224:227], v[40:43], v[152:155], v[224:227]\n\t"
      "v_fmaak_f32 v230, v252, v230, 0xc0135761\n\t"
      "v_fmaak_f32 v231, v252, v231, 0xc0135761\n\t"
      "v_mul_f32_e32 v230, v228, v230\n\t"
      "v_mfma_f32_16x16x32_bf16 v[224:227], v[56:59], v[156:159], v[224:227]\n\t"
      "v_mul_f32_e32 v231, v229, v231\n\t"
      "v_exp_f32_e32 v230, v230\n\t"
      "v_mfma_f32_16x16x32_bf16 v[224:227], v[72:75], v[160:163], v[224:227]\n\t"
      "v_exp_f32_e32 v231, v231\n\t"
      "v_add_f32_e32 v230, 1.0, v230\n\t"
      "v_add_f32_e32 v231, 1.0, v231\n\t"
      "global_load_dwordx4 v[140:143], v250, s[74:75] offset:-1024 sc1\n\t"
      "v_mfma_f32_16x16x32_bf16 v[224:227], v[88:91], v[164:167], v[224:227]\n\t"
      "v_rcp_f32_e32 v230, v230\n\t"
      "v_rcp_f32_e32 v231, v231\n\t"
      "v_mfma_f32_16x16x32_bf16 v[224:227], v[104:107], v[168:171], v[224:227]\n\t"
      "v_mul_f32_e32 v228, v228, v230\n\t"
      "v_mul_f32_e32 v229, v229, v231\n\t"
      "v_cvt_pk_bf16_f32 v232, v228, v229\n\t"
      "ds_write_b128 v253, v[220:223] offset:33792\n\t"
      "v_mfma_f32_16x16x32_bf16 v[224:227], v[120:123], v[172:175], v[224:227]\n\t"
      "v_min_u32_e32 v254, -2, v232\n\t"
      "global_store_dword v247, v254, s[62:63] sc1\n\t"
      "s_add_u32 s62, s62, 57344\n\t"
      "s_addc_u32 s63, s63, 0\n\t"
      "v_mfma_f32_16x16x32_bf16 v[208:211], v[12:15], v[144:147], 0\n\t"
      "global_load_dwordx4 v[144:147], v250, s[74:75] sc1\n\t"
      "v_mfma_f32_16x16x32_bf16 v[208:211], v[28:31], v[148:151], v[208:211]\n\t"
      "v_mfma_f32_16x16x32_bf16 v[208:211], v[44:47], v[152:155], v[208:211]\n\t"
      "v_mfma_f32_16x16x32_bf16 v[208:211], v[60:63], v[156:159], v[208:211]\n\t"
      "v_mfma_f32_16x16x32_bf16 v[208:211], v[76:79], v[160:163], v[208:211]\n\t"
      "global_load_dwordx4 v[148:151], v250, s[74:75] offset:1024 sc1\n\t"
      "v_mfma_f32_16x16x32_bf16 v[208:211], v[92:95], v[164:167], v[208:211]\n\t"
      "v_mfma_f32_16x16x32_bf16 v[208:211], v[108:111], v[168:171], v[208:211]\n\t"
      "ds_write_b128 v245, v[224:227] offset:34816\n\t"
      "v_mfma_f32_16x16x32_bf16 v[208:211], v[124:127], v[172:175], v[208:211]\n\t"
      "s_waitcnt vmcnt(12)\n\t"
      "v_mfma_f32_16x16x32_bf16 v[212:215], v[0:3], v[176:179], 0\n\t"
      "global_load_dwordx4 v[152:155], v250, s[74:75] offset:2048 sc1\n\t"
      "v_mfma_f32_16x16x32_bf16 v[212:215], v[16:19], v[180:183], v[212:215]\n\t"
      "s_nop 3\n\t"
      "ds_write_b128 v253, v[208:211] offset:35840\n\t"
      "v_mfma_f32_16x16x32_bf16 v[212:215], v[32:35], v[184:187], v[212:215]\n\t"
      "v_mfma_f32_16x16x32_bf16 v[212:215], v[48:51], v[188:191], v[212:215]\n\t"
      "s_waitcnt vmcnt(8)\n\t"
      "v_mfma_f32_16x16x32_bf16 v[212:215], v[64:67], v[192:195], v[212:215]\n\t"
      "global_load_dwordx4 v[156:159], v250, s[74:75] offset:3072 sc1\n\t"
      "v_mfma_f32_16x16x32_bf16 v[212:215], v[80:83], v[196:199], v[212:215]\n\t"
      "s_waitcnt lgkmcnt(0)\n\t"
      "s_barrier\n\t"
      "ds_read_b64 v[228:229], v246 offset:32768\n\t"
      "ds_read_b64 v[230:231], v246 offset:36864\n\t"
      "v_mfma_f32_16x16x32_bf16 v[212:215], v[96:99], v[200:203], v[212:215]\n\t"
      "ds_read_b64 v[232:233], v246 offset:40960\n\t"
      "ds_read_b64 v[234:235], v246 offset:45056\n\t"
      "v_mfma_f32_16x16x32_bf16 v[212:215], v[112:115], v[204:207], v[212:215]\n\t"
      "ds_read_b64 v[236:237], v246 offset:49152\n\t"
      "ds_read_b64 v[238:239], v246 offset:53248\n\t"
      "v_mfma_f32_16x16x32_bf16 v[216:219], v[4:7], v[176:179], 0\n\t"
      "ds_read_b64 v[240:241], v246 offset:57344\n\t"
      "ds_read_b64 v[242:243], v246 offset:61440\n\t"
      "global_load_dwordx4 v[160:163], v250, s[76:77] offset:-4096 sc1\n\t"
      "v_mfma_f32_16x16x32_bf16 v[216:219], v[20:23], v[180:183], v[216:219]\n\t"
      "v_mfma_f32_16x16x32_bf16 v[216:219], v[36:39], v[184:187], v[216:219]\n\t"
      "s_waitcnt lgkmcnt(4)\n\t"
      "v_add_f32_e32 v228, v228, v230\n\t"
      "v_add_f32_e32 v232, v232, v234\n\t"
      "v_cmp_u_f32_e32 vcc, v212, v212\n\t"
      "s_cbranch_vccnz LA34R_%=\n\t"
      "LA34B_%=:\n\t"
      "v_mfma_f32_16x16x32_bf16 v[216:219], v[52:55], v[188:191], v[216:219]\n\t"
      "v_add_f32_e32 v229, v229, v231\n\t"
      "v_add_f32_e32 v233, v233, v235\n\t"
      "v_mfma_f32_16x16x32_bf16 v[216:219], v[68:71], v[192:195], v[216:219]\n\t"
      "s_waitcnt lgkmcnt(0)\n\t"
      "v_add_f32_e32 v236, v236, v238\n\t"
      "v_add_f32_e32 v240, v240, v242\n\t"
      "global_load_dwordx4 v[164:167], v250, s[76:77] offset:-3072 sc1\n\t"
      "v_mfma_f32_16x16x32_bf16 v[216:219], v[84:87], v[196:199], v[216:219]\n\t"
      "v_add_f32_e32 v237, v237, v239\n\t"
      "v_add_f32_e32 v241, v241, v243\n\t"
      "v_mfma_f32_16x16x32_bf16 v[216:219], v[100:103], v[200:203], v[216:219]\n\t"
      "v_add_f32_e32 v228, v228, v232\n\t"
      "v_add_f32_e32 v236, v236, v240\n\t"
      "v_add_f32_e32 v229, v229, v233\n\t"
      "ds_write_b128 v245, v[212:215]\n\t"
      "v_mfma_f32_16x16x32_bf16 v[216:219], v[116:119], v[204:207], v[216:219]\n\t"
      "v_add_f32_e32 v237, v237, v241\n\t"
      "v_add_f32_e32 v228, v228, v248\n\t"
      "v_mfma_f32_16x16x32_bf16 v[220:223], v[8:11], v[176:179], 0\n\t"
      "v_add_f32_e32 v229, v229, v249\n\t"
      "v_add_f32_e32 v228, v228, v236\n\t"
      "v_add_f32_e32 v229, v229, v237\n\t"
      "global_load_dwordx4 v[168:171], v250, s[76:77] offset:-2048 sc1\n\t"
      "v_mfma_f32_16x16x32_bf16 v[220:223], v[24:27], v[180:183], v[220:223]\n\t"
      "v_mul_f32_e32 v230, v228, v228\n\t"
      "v_mul_f32_e32 v231, v229, v229\n\t"
      "v_mfma_f32_16x16x32_bf16 v[220:223], v[40:43], v[184:187], v[220:223]\n\t"
      "v_fmaak_f32 v230, v252, v230, 0xc0135761\n\t"
      "v_fmaak_f32 v231, v252, v231, 0xc0135761\n\t"
      "v_mul_f32_e32 v230, v228, v230\n\t"
      "v_mfma_f32_16x16x32_bf16 v[220:223], v[56:59], v[188:191], v[220:223]\n\t"
      "v_mul_f32_e32 v231, v229, v231\n\t"
      "v_exp_f32_e32 v230, v230\n\t"
      "v_mfma_f32_16x16x32_bf16 v[220:223], v[72:75], v[192:195], v[220:223]\n\t"
      "v_exp_f32_e32 v231, v231\n\t"
      "v_add_f32_e32 v230, 1.0, v230\n\t"
      "v_add_f32_e32 v231, 1.0, v231\n\t"
      "global_load_dwordx4 v[172:175], v250, s[76:77] offset:-1024 sc1\n\t"
      "v_mfma_f32_16x16x32_bf16 v[220:223], v[88:91], v[196:199], v[220:223]\n\t"
      "v_rcp_f32_e32 v230, v230\n\t"
      "v_rcp_f32_e32 v231, v231\n\t"
      "v_mfma_f32_16x16x32_bf16 v[220:223], v[104:107], v[200:203], v[220:223]\n\t"
      "v_mul_f32_e32 v228, v228, v230\n\t"
      "v_mul_f32_e32 v229, v229, v231\n\t"
      "v_cvt_pk_bf16_f32 v232, v228, v229\n\t"
      "ds_write_b128 v253, v[216:219] offset:1024\n\t"
      "v_mfma_f32_16x16x32_bf16 v[220:223], v[120:123], v[204:207], v[220:223]\n\t"
      "v_min_u32_e32 v254, -2, v232\n\t"
      "global_store_dword v247, v254, s[62:63] sc1\n\t"
      "s_add_u32 s62, s62, 57344\n\t"
      "s_addc_u32 s63, s63, 0\n\t"
      "v_mfma_f32_16x16x32_bf16 v[224:227], v[12:15], v[176:179], 0\n\t"
      "global_load_dwordx4 v[176:179], v250, s[76:77] sc1\n\t"
      "v_mfma_f32_16x16x32_bf16 v[224:227], v[28:31], v[180:183], v[224:227]\n\t"
      "v_mfma_f32_16x16x32_bf16 v[224:227], v[44:47], v[184:187], v[224:227]\n\t"
      "v_mfma_f32_16x16x32_bf16 v[224:227], v[60:63], v[188:191], v[224:227]\n\t"
      "v_mfma_f32_16x16x32_bf16 v[224:227], v[76:79], v[192:195], v[224:227]\n\t"
      "global_load_dwordx4 v[180:183], v250, s[76:77] offset:1024 sc1\n\t"
      "v_mfma_f32_16x16x32_bf16 v[224:227], v[92:95], v[196:199], v[224:227]\n\t"
      "v_mfma_f32_16x16x32_bf16 v[224:227], v[108:111], v[200:203], v[224:227]\n\t"
      "ds_write_b128 v245, v[220:223] offset:2048\n\t"
      "v_mfma_f32_16x16x32_bf16 v[224:227], v[124:127], v[204:207], v[224:227]\n\t"
      "s_waitcnt vmcnt(12)\n\t"
      "v_mfma_f32_16x16x32_bf16 v[208:211], v[0:3], v[128:131], 0\n\t"
      "global_load_dwordx4 v[184:187], v250, s[76:77] offset:2048 sc1\n\t"
      "v_mfma_f32_16x16x32_bf16 v[208:211], v[16:19], v[132:135], v[208:211]\n\t"
      "s_nop 3\n\t"
      "ds_write_b128 v253, v[224:227] offset:3072\n\t"
      "v_mfma_f32_16x16x32_bf16 v[208:211], v[32:35], v[136:139], v[208:211]\n\t"
      "v_mfma_f32_16x16x32_bf16 v[208:211], v[48:51], v[140:143], v[208:211]\n\t"
      "s_waitcnt vmcnt(8)\n\t"
      "v_mfma_f32_16x16x32_bf16 v[208:211], v[64:67], v[144:147], v[208:211]\n\t"
      "global_load_dwordx4 v[188:191], v250, s[76:77] offset:3072 sc1\n\t"
      "v_mfma_f32_16x16x32_bf16 v[208:211], v[80:83], v[148:151], v[208:211]\n\t"
      "s_waitcnt lgkmcnt(0)\n\t"
      "s_barrier\n\t"
      "ds_read_b64 v[228:229], v246\n\t"
      "ds_read_b64 v[230:231], v246 offset:4096\n\t"
      "v_mfma_f32_16x16x32_bf16 v[208:211], v[96:99], v[152:155], v[208:211]\n\t"
      "ds_read_b64 v[232:233], v246 offset:8192\n\t"
      "ds_read_b64 v[234:235], v246 offset:12288\n\t"
      "v_mfma_f32_16x16x32_bf16 v[208:211], v[112:115], v[156:159], v[208:211]\n\t"
      "ds_read_b64 v[236:237], v246 offset:16384\n\t"
      "ds_read_b64 v[238:239], v246 offset:20480\n\t"
      "v_mfma_f32_16x16x32_bf16 v[212:215], v[4:7], v[128:131], 0\n\t"
      "ds_read_b64 v[240:241], v246 offset:24576\n\t"
      "ds_read_b64 v[242:243], v246 offset:28672\n\t"
      "v_mfma_f32_16x16x32_bf16 v[212:215], v[20:23], v[132:135], v[212:215]\n\t"
      "v_mfma_f32_16x16x32_bf16 v[212:215], v[36:39], v[136:139], v[212:215]\n\t"
      "s_waitcnt lgkmcnt(4)\n\t"
      "v_add_f32_e32 v228, v228, v230\n\t"
      "v_add_f32_e32 v232, v232, v234\n\t"
      "v_cmp_u_f32_e32 vcc, v208, v208\n\t"
      "s_cbranch_vccnz LA35R_%=\n\t"
      "LA35B_%=:\n\t"
      "v_mfma_f32_16x16x32_bf16 v[212:215], v[52:55], v[140:143], v[212:215]\n\t"
      "v_add_f32_e32 v229, v229, v231\n\t"
      "v_add_f32_e32 v233, v233, v235\n\t"
      "v_mfma_f32_16x16x32_bf16 v[212:215], v[68:71], v[144:147], v[212:215]\n\t"
      "s_waitcnt lgkmcnt(0)\n\t"
      "v_add_f32_e32 v236, v236, v238\n\t"
      "v_add_f32_e32 v240, v240, v242\n\t"
      "v_mfma_f32_16x16x32_bf16 v[212:215], v[84:87], v[148:151], v[212:215]\n\t"
      "v_add_f32_e32 v237, v237, v239\n\t"
      "v_add_f32_e32 v241, v241, v243\n\t"
      "v_mfma_f32_16x16x32_bf16 v[212:215], v[100:103], v[152:155], v[212:215]\n\t"
      "v_add_f32_e32 v228, v228, v232\n\t"
      "v_add_f32_e32 v236, v236, v240\n\t"
      "v_add_f32_e32 v229, v229, v233\n\t"
      "ds_write_b128 v245, v[208:211] offset:32768\n\t"
      "v_mfma_f32_16x16x32_bf16 v[212:215], v[116:119], v[156:159], v[212:215]\n\t"
      "v_add_f32_e32 v237, v237, v241\n\t"
      "v_add_f32_e32 v228, v228, v248\n\t"
      "v_mfma_f32_16x16x32_bf16 v[216:219], v[8:11], v[128:131], 0\n\t"
      "v_add_f32_e32 v229, v229, v249\n\t"
      "v_add_f32_e32 v228, v228, v236\n\t"
      "v_add_f32_e32 v229, v229, v237\n\t"
      "v_mfma_f32_16x16x32_bf16 v[216:219], v[24:27], v[132:135], v[216:219]\n\t"
      "v_mul_f32_e32 v230, v228, v228\n\t"
      "v_mul_f32_e32 v231, v229, v229\n\t"
      "v_mfma_f32_16x16x32_bf16 v[216:219], v[40:43], v[136:139], v[216:219]\n\t"
      "v_fmaak_f32 v230, v252, v230, 0xc0135761\n\t"
      "v_fmaak_f32 v231, v252, v231, 0xc0135761\n\t"
      "v_mul_f32_e32 v230, v228, v230\n\t"
      "v_mfma_f32_16x16x32_bf16 v[216:219], v[56:59], v[140:143], v[216:219]\n\t"
      "v_mul_f32_e32 v231, v229, v231\n\t"
      "v_exp_f32_e32 v230, v230\n\t"
      "v_mfma_f32_16x16x32_bf16 v[216:219], v[72:75], v[144:147], v[216:219]\n\t"
      "v_exp_f32_e32 v231, v231\n\t"
      "v_add_f32_e32 v230, 1.0, v230\n\t"
      "v_add_f32_e32 v231, 1.0, v231\n\t"
      "v_mfma_f32_16x16x32_bf16 v[216:219], v[88:91], v[148:151], v[216:219]\n\t"
      "v_rcp_f32_e32 v230, v230\n\t"
      "v_rcp_f32_e32 v231, v231\n\t"
      "v_mfma_f32_16x16x32_bf16 v[216:219], v[104:107], v[152:155], v[216:219]\n\t"
      "v_mul_f32_e32 v228, v228, v230\n\t"
      "v_mul_f32_e32 v229, v229, v231\n\t"
      "v_cvt_pk_bf16_f32 v232, v228, v229\n\t"
      "ds_write_b128 v253, v[212:215] offset:33792\n\t"
      "v_mfma_f32_16x16x32_bf16 v[216:219], v[120:123], v[156:159], v[216:219]\n\t"
      "v_min_u32_e32 v254, -2, v232\n\t"
      "global_store_dword v247, v254, s[62:63] sc1\n\t"
      "s_add_u32 s62, s62, 57344\n\t"
      "s_addc_u32 s63, s63, 0\n\t"
      "v_mfma_f32_16x16x32_bf16 v[220:223], v[12:15], v[128:131], 0\n\t"
      "v_mfma_f32_16x16x32_bf16 v[220:223], v[28:31], v[132:135], v[220:223]\n\t"
      "v_mfma_f32_16x16x32_bf16 v[220:223], v[44:47], v[136:139], v[220:223]\n\t"
      "v_mfma_f32_16x16x32_bf16 v[220:223], v[60:63], v[140:143], v[220:223]\n\t"
      "v_mfma_f32_16x16x32_bf16 v[220:223], v[76:79], v[144:147], v[220:223]\n\t"
      "v_mfma_f32_16x16x32_bf16 v[220:223], v[92:95], v[148:151], v[220:223]\n\t"
      "v_mfma_f32_16x16x32_bf16 v[220:223], v[108:111], v[152:155], v[220:223]\n\t"
      "ds_write_b128 v245, v[216:219] offset:34816\n\t"
      "v_mfma_f32_16x16x32_bf16 v[220:223], v[124:127], v[156:159], v[220:223]\n\t"
      "s_waitcnt vmcnt(6)\n\t"
      "v_mfma_f32_16x16x32_bf16 v[224:227], v[0:3], v[160:163], 0\n\t"
      "v_mfma_f32_16x16x32_bf16 v[224:227], v[16:19], v[164:167], v[224:227]\n\t"
      "s_nop 4\n\t"
      "ds_write_b128 v253, v[220:223] offset:35840\n\t"
      "v_mfma_f32_16x16x32_bf16 v[224:227], v[32:35], v[168:171], v[224:227]\n\t"
      "v_mfma_f32_16x16x32_bf16 v[224:227], v[48:51], v[172:175], v[224:227]\n\t"
      "s_waitcnt vmcnt(1)\n\t"
      "v_mfma_f32_16x16x32_bf16 v[224:227], v[64:67], v[176:179], v[224:227]\n\t"
      "v_mfma_f32_16x16x32_bf16 v[224:227], v[80:83], v[180:183], v[224:227]\n\t"
      "s_waitcnt lgkmcnt(0)\n\t"
      "s_barrier\n\t"
      "ds_read_b64 v[228:229], v246 offset:32768\n\t"
      "ds_read_b64 v[230:231], v246 offset:36864\n\t"
      "v_mfma_f32_16x16x32_bf16 v[224:227], v[96:99], v[184:187], v[224:227]\n\t"
      "ds_read_b64 v[232:233], v246 offset:40960\n\t"
      "ds_read_b64 v[234:235], v246 offset:45056\n\t"
      "v_mfma_f32_16x16x32_bf16 v[224:227], v[112:115], v[188:191], v[224:227]\n\t"
      "ds_read_b64 v[236:237], v246 offset:49152\n\t"
      "ds_read_b64 v[238:239], v246 offset:53248\n\t"
      "v_mfma_f32_16x16x32_bf16 v[208:211], v[4:7], v[160:163], 0\n\t"
      "ds_read_b64 v[240:241], v246 offset:57344\n\t"
      "ds_read_b64 v[242:243], v246 offset:61440\n\t"
      "v_mfma_f32_16x16x32_bf16 v[208:211], v[20:23], v[164:167], v[208:211]\n\t"
      "v_mfma_f32_16x16x32_bf16 v[208:211], v[36:39], v[168:171], v[208:211]\n\t"
      "s_waitcnt lgkmcnt(4)\n\t"
      "v_add_f32_e32 v228, v228, v230\n\t"
      "v_add_f32_e32 v232, v232, v234\n\t"
      "v_cmp_u_f32_e32 vcc, v224, v224\n\t"
      "s_cbranch_vccnz LA36R_%=\n\t"
      "LA36B_%=:\n\t"
      "v_mfma_f32_16x16x32_bf16 v[208:211], v[52:55], v[172:175], v[208:211]\n\t"
      "v_add_f32_e32 v229, v229, v231\n\t"
      "v_add_f32_e32 v233, v233, v235\n\t"
      "v_mfma_f32_16x16x32_bf16 v[208:211], v[68:71], v[176:179], v[208:211]\n\t"
      "s_waitcnt lgkmcnt(0)\n\t"
      "v_add_f32_e32 v236, v236, v238\n\t"
      "v_add_f32_e32 v240, v240, v242\n\t"
      "v_mfma_f32_16x16x32_bf16 v[208:211], v[84:87], v[180:183], v[208:211]\n\t"
      "v_add_f32_e32 v237, v237, v239\n\t"
      "v_add_f32_e32 v241, v241, v243\n\t"
      "v_mfma_f32_16x16x32_bf16 v[208:211], v[100:103], v[184:187], v[208:211]\n\t"
      "v_add_f32_e32 v228, v228, v232\n\t"
      "v_add_f32_e32 v236, v236, v240\n\t"
      "v_add_f32_e32 v229, v229, v233\n\t"
      "ds_write_b128 v245, v[224:227]\n\t"
      "v_mfma_f32_16x16x32_bf16 v[208:211], v[116:119], v[188:191], v[208:211]\n\t"
      "v_add_f32_e32 v237, v237, v241\n\t"
      "v_add_f32_e32 v228, v228, v248\n\t"
      "v_mfma_f32_16x16x32_bf16 v[212:215], v[8:11], v[160:163], 0\n\t"
      "v_add_f32_e32 v229, v229, v249\n\t"
      "v_add_f32_e32 v228, v228, v236\n\t"
      "v_add_f32_e32 v229, v229, v237\n\t"
      "v_mfma_f32_16x16x32_bf16 v[212:215], v[24:27], v[164:167], v[212:215]\n\t"
      "v_mul_f32_e32 v230, v228, v228\n\t"
      "v_mul_f32_e32 v231, v229, v229\n\t"
      "v_mfma_f32_16x16x32_bf16 v[212:215], v[40:43], v[168:171], v[212:215]\n\t"
      "v_fmaak_f32 v230, v252, v230, 0xc0135761\n\t"
      "v_fmaak_f32 v231, v252, v231, 0xc0135761\n\t"
      "v_mul_f32_e32 v230, v228, v230\n\t"
      "v_mfma_f32_16x16x32_bf16 v[212:215], v[56:59], v[172:175], v[212:215]\n\t"
      "v_mul_f32_e32 v231, v229, v231\n\t"
      "v_exp_f32_e32 v230, v230\n\t"
      "v_mfma_f32_16x16x32_bf16 v[212:215], v[72:75], v[176:179], v[212:215]\n\t"
      "v_exp_f32_e32 v231, v231\n\t"
      "v_add_f32_e32 v230, 1.0, v230\n\t"
      "v_add_f32_e32 v231, 1.0, v231\n\t"
      "v_mfma_f32_16x16x32_bf16 v[212:215], v[88:91], v[180:183], v[212:215]\n\t"
      "v_rcp_f32_e32 v230, v230\n\t"
      "v_rcp_f32_e32 v231, v231\n\t"
      "v_mfma_f32_16x16x32_bf16 v[212:215], v[104:107], v[184:187], v[212:215]\n\t"
      "v_mul_f32_e32 v228, v228, v230\n\t"
      "v_mul_f32_e32 v229, v229, v231\n\t"
      "v_cvt_pk_bf16_f32 v232, v228, v229\n\t"
      "ds_write_b128 v253, v[208:211] offset:1024\n\t"
      "v_mfma_f32_16x16x32_bf16 v[212:215], v[120:123], v[188:191], v[212:215]\n\t"
      "v_min_u32_e32 v254, -2, v232\n\t"
      "global_store_dword v247, v254, s[62:63] sc1\n\t"
      "s_add_u32 s62, s62, 57344\n\t"
      "s_addc_u32 s63, s63, 0\n\t"
      "v_mfma_f32_16x16x32_bf16 v[216:219], v[12:15], v[160:163], 0\n\t"
      "v_mfma_f32_16x16x32_bf16 v[216:219], v[28:31], v[164:167], v[216:219]\n\t"
      "v_mfma_f32_16x16x32_bf16 v[216:219], v[44:47], v[168:171], v[216:219]\n\t"
      "v_mfma_f32_16x16x32_bf16 v[216:219], v[60:63], v[172:175], v[216:219]\n\t"
      "v_mfma_f32_16x16x32_bf16 v[216:219], v[76:79], v[176:179], v[216:219]\n\t"
      "v_mfma_f32_16x16x32_bf16 v[216:219], v[92:95], v[180:183], v[216:219]\n\t"
      "v_mfma_f32_16x16x32_bf16 v[216:219], v[108:111], v[184:187], v[216:219]\n\t"
      "ds_write_b128 v245, v[212:215] offset:2048\n\t"
      "v_mfma_f32_16x16x32_bf16 v[216:219], v[124:127], v[188:191], v[216:219]\n\t"
      "s_nop 7\n\t"
      "ds_write_b128 v253, v[216:219] offset:3072\n\t"
      "s_waitcnt lgkmcnt(0)\n\t"
      "s_barrier\n\t"
      "ds_read_b64 v[228:229], v246\n\t"
      "ds_read_b64 v[230:231], v246 offset:4096\n\t"
      "ds_read_b64 v[232:233], v246 offset:8192\n\t"
      "ds_read_b64 v[234:235], v246 offset:12288\n\t"
      "ds_read_b64 v[236:237], v246 offset:16384\n\t"
      "ds_read_b64 v[238:239], v246 offset:20480\n\t"
      "ds_read_b64 v[240:241], v246 offset:24576\n\t"
      "ds_read_b64 v[242:243], v246 offset:28672\n\t"
      "s_waitcnt lgkmcnt(4)\n\t"
      "v_add_f32_e32 v228, v228, v230\n\t"
      "v_add_f32_e32 v232, v232, v234\n\t"
      "v_add_f32_e32 v229, v229, v231\n\t"
      "v_add_f32_e32 v233, v233, v235\n\t"
      "s_waitcnt lgkmcnt(0)\n\t"
      "v_add_f32_e32 v236, v236, v238\n\t"
      "v_add_f32_e32 v240, v240, v242\n\t"
      "v_add_f32_e32 v237, v237, v239\n\t"
      "v_add_f32_e32 v241, v241, v243\n\t"
      "v_add_f32_e32 v228, v228, v232\n\t"
      "v_add_f32_e32 v236, v236, v240\n\t"
      "v_add_f32_e32 v229, v229, v233\n\t"
      "v_add_f32_e32 v237, v237, v241\n\t"
      "v_add_f32_e32 v228, v228, v248\n\t"
      "v_add_f32_e32 v229, v229, v249\n\t"
      "v_add_f32_e32 v228, v228, v236\n\t"
      "v_add_f32_e32 v229, v229, v237\n\t"
      "v_mul_f32_e32 v230, v228, v228\n\t"
      "v_mul_f32_e32 v231, v229, v229\n\t"
      "v_fmaak_f32 v230, v252, v230, 0xc0135761\n\t"
      "v_fmaak_f32 v231, v252, v231, 0xc0135761\n\t"
      "v_mul_f32_e32 v230, v228, v230\n\t"
      "v_mul_f32_e32 v231, v229, v231\n\t"
      "v_exp_f32_e32 v230, v230\n\t"
      "v_exp_f32_e32 v231, v231\n\t"
      "v_add_f32_e32 v230, 1.0, v230\n\t"
      "v_add_f32_e32 v231, 1.0, v231\n\t"
      "v_rcp_f32_e32 v230, v230\n\t"
      "v_rcp_f32_e32 v231, v231\n\t"
      "v_mul_f32_e32 v228, v228, v230\n\t"
      "v_mul_f32_e32 v229, v229, v231\n\t"
      "v_cvt_pk_bf16_f32 v232, v228, v229\n\t"
      "v_min_u32_e32 v254, -2, v232\n\t"
      "global_store_dword v247, v254, s[62:63] sc1\n\t"
      "s_add_u32 s62, s62, 57344\n\t"
      "s_addc_u32 s63, s63, 0\n\t"
      "s_or_b32 s34, s2, s24\n\t"
      "s_cbranch_scc1 EPB_%=\n\t"
      "v_mov_b32_e32 v228, s22\n\t"
      "v_mov_b32_e32 v229, s23\n\t"
      "v_mov_b32_e32 v230, 0\n\t"
      "s_mov_b64 exec, 1\n\t"
      "global_store_dwordx2 v230, v[228:229], s[60:61] sc1\n\t"
      "s_mov_b64 exec, -1\n\t"
      "EPB_%=:\n\t"
      "s_endpgm\n\t"
      "LPATH_%=:\n\t"
      "s_add_u32 s22, s22, 1\n\t"
      "s_addc_u32 s23, s23, 0\n\t"
      "s_add_u32 s72, s26, 1\n\t"
      "s_and_b32 s72, s72, 6\n\t"
      "s_cselect_b32 s72, 0, 2\n\t"
      "s_or_b32 s27, s27, s72\n\t"
      "s_mul_i32 s72, s81, 172032\n\t"
      "s_add_u32 s72, s72, s37\n\t"
      "s_add_u32 s40, s6, s72\n\t"
      "s_addc_u32 s41, s7, 0\n\t"
      "s_add_u32 s81, s81, 1\n\t"
      "s_cmp_eq_u32 s81, 7\n\t"
      "s_cselect_b32 s81, 0, s81\n\t"
      "s_add_u32 m0, s39, 81920\n\t"
      "s_nop 0\n\t"
      "global_load_lds_dwordx4 v148, s[40:41]\n\t"
      "global_load_lds_dwordx4 v149, s[40:41] offset:1024\n\t"
      "global_load_lds_dwordx4 v150, s[40:41] offset:2048\n\t"
      "global_load_lds_dwordx4 v151, s[40:41] offset:3072\n\t"
      "s_mul_i32 s72, s81, 172032\n\t"
      "s_add_u32 s72, s72, s37\n\t"
      "s_add_u32 s40, s6, s72\n\t"
      "s_addc_u32 s41, s7, 0\n\t"
      "s_add_u32 s81, s81, 1\n\t"
      "s_cmp_eq_u32 s81, 7\n\t"
      "s_cselect_b32 s81, 0, s81\n\t"
      "s_add_u32 m0, s39, 98304\n\t"
      "s_nop 0\n\t"
      "global_load_lds_dwordx4 v148, s[40:41]\n\t"
      "global_load_lds_dwordx4 v149, s[40:41] offset:1024\n\t"
      "s_waitcnt vmcnt(14)\n\t"
      "s_barrier\n\t"
      "v_add_u32_e32 v172, 0x10000, v224\n\t"
      "v_add_u32_e32 v173, 0x10000, v225\n\t"
      "v_add_u32_e32 v174, 0x10000, v226\n\t"
      "v_add_u32_e32 v175, 0x10000, v227\n\t"
      "s_lshl_b32 s34, s25, 8\n\t"
      "s_add_u32 s34, s34, 4096\n\t"
      "v_and_b32_e32 v136, 1, v192\n\t"
      "v_lshl_add_u32 v136, v136, 7, s34\n\t"
      "s_lshl_b32 s34, s25, 7\n\t"
      "s_add_u32 s34, s34, 16384\n\t"
      "v_mov_b32_e32 v137, s34\n\t"
      "s_lshl_b32 s34, s2, 7\n\t"
      "s_add_u32 s34, s34, 32768\n\t"
      "v_mov_b32_e32 v143, s34\n\t"
      "s_bitcmp0_b32 s27, 1\n\t"
      "s_cbranch_scc1 SK6_%=\n\t"
      "s_lshr_b32 s73, s26, 2\n\t"
      "s_lshl1_add_u32 s73, s25, s73\n\t"
      "s_lshl_b32 s72, s73, 7\n\t"
      "s_add_u32 s72, s72, 4096\n\t"
      "v_mov_b32_e32 v130, s72\n\t"
      "s_lshl2_add_u32 s75, s73, s24\n\t"
      "v_mov_b32_e32 v128, s22\n\t"
      "v_mov_b32_e32 v129, s23\n\t"
      "v_lshrrev_b32_e32 v140, 1, v244\n\t"
      "s_lshl_b32 s72, s75, 9\n\t"
      "s_add_u32 s72, s72, 12582912\n\t"
      "v_mov_b32_e32 v132, -1\n\t"
      "v_mov_b32_e32 v133, -1\n\t"
      "v_mov_b32_e32 v134, -1\n\t"
      "v_mov_b32_e32 v135, -1\n\t"
      "s_add_u32 s42, s20, s72\n\t"
      "s_addc_u32 s43, s21, 0\n\t"
      "global_store_dwordx2 v140, v[132:133], s[42:43] sc1\n\t"
      "s_lshl_b32 s72, s75, 13\n\t"
      "s_add_u32 s72, s72, 4096\n\t"
      "s_add_u32 s42, s20, s72\n\t"
      "s_addc_u32 s43, s21, 0\n\t"
      "global_store_dwordx4 v244, v[132:135], s[42:43] offset:-4096 sc1\n\t"
      "global_store_dwordx4 v244, v[132:135], s[42:43] offset:-3072 sc1\n\t"
      "global_store_dwordx4 v244, v[132:135], s[42:43] offset:-2048 sc1\n\t"
      "SK6_%=:\n\t"
      "s_waitcnt vmcnt(10)\n\t"
      "s_barrier\n\t"
      "global_load_lds_dwordx4 v150, s[40:41] offset:2048\n\t"
      "global_load_lds_dwordx4 v151, s[40:41] offset:3072\n\t"
      "s_bitcmp0_b32 s27, 1\n\t"
      "s_cbranch_scc1 SK7_%=\n\t"
      "global_store_dwordx4 v244, v[132:135], s[42:43] offset:-1024 sc1\n\t"
      "global_store_dwordx4 v244, v[132:135], s[42:43] sc1\n\t"
      "global_store_dwordx4 v244, v[132:135], s[42:43] offset:1024 sc1\n\t"
      "SK7_%=:\n\t"
      "s_waitcnt vmcnt(8)\n\t"
      "s_barrier\n\t"
      "s_mul_i32 s72, s81, 172032\n\t"
      "s_add_u32 s72, s72, s37\n\t"
      "s_add_u32 s40, s6, s72\n\t"
      "s_addc_u32 s41, s7, 0\n\t"
      "s_add_u32 s81, s81, 1\n\t"
      "s_cmp_eq_u32 s81, 7\n\t"
      "s_cselect_b32 s81, 0, s81\n\t"
      "s_add_u32 m0, s39, 114688\n\t"
      "s_nop 0\n\t"
      "global_load_lds_dwordx4 v148, s[40:41]\n\t"
      "global_load_lds_dwordx4 v149, s[40:41] offset:1024\n\t"
      "global_load_lds_dwordx4 v150, s[40:41] offset:2048\n\t"
      "global_load_lds_dwordx4 v151, s[40:41] offset:3072\n\t"
      "s_bitcmp0_b32 s27, 1\n\t"
      "s_cbranch_scc1 SK8_%=\n\t"
      "global_store_dwordx4 v244, v[132:135], s[42:43] offset:2048 sc1\n\t"
      "global_store_dwordx4 v244, v[132:135], s[42:43] offset:3072 sc1\n\t"
      "SK8_%=:\n\t"
      "s_waitcnt vmcnt(8)\n\t"
      "s_barrier\n\t"
      "s_mul_i32 s72, s81, 172032\n\t"
      "s_add_u32 s72, s72, s37\n\t"
      "s_add_u32 s40, s6, s72\n\t"
      "s_addc_u32 s41, s7, 0\n\t"
      "s_add_u32 s81, s81, 1\n\t"
      "s_cmp_eq_u32 s81, 7\n\t"
      "s_cselect_b32 s81, 0, s81\n\t"
      "s_add_u32 m0, s39, 131072\n\t"
      "s_nop 0\n\t"
      "global_load_lds_dwordx4 v148, s[40:41]\n\t"
      "global_load_lds_dwordx4 v149, s[40:41] offset:1024\n\t"
      "global_load_lds_dwordx4 v150, s[40:41] offset:2048\n\t"
      "global_load_lds_dwordx4 v151, s[40:41] offset:3072\n\t"
      "s_waitcnt vmcnt(8)\n\t"
      "s_bitcmp0_b32 s27, 1\n\t"
      "s_cbranch_scc1 SK9_%=\n\t"
      "s_waitcnt vmcnt(4)\n\t"
      "SK9_%=:\n\t"
      "s_barrier\n\t"
      "s_mul_i32 s72, s81, 172032\n\t"
      "s_add_u32 s72, s72, s37\n\t"
      "s_add_u32 s40, s6, s72\n\t"
      "s_addc_u32 s41, s7, 0\n\t"
      "s_add_u32 s81, s81, 1\n\t"
      "s_cmp_eq_u32 s81, 7\n\t"
      "s_cselect_b32 s81, 0, s81\n\t"
      "s_add_u32 m0, s39, 147456\n\t"
      "s_nop 0\n\t"
      "global_load_lds_dwordx4 v148, s[40:41]\n\t"
      "global_load_lds_dwordx4 v149, s[40:41] offset:1024\n\t"
      "global_load_lds_dwordx4 v150, s[40:41] offset:2048\n\t"
      "global_load_lds_dwordx4 v151, s[40:41] offset:3072\n\t"
      "s_bitcmp0_b32 s27, 1\n\t"
      "s_cbranch_scc1 SK10_%=\n\t"
      "s_cmp_eq_u32 s24, 0\n\t"
      "s_cselect_b64 exec, 1, 0\n\t"
      "global_store_dwordx2 v130, v[128:129], s[60:61] sc1\n\t"
      "s_mov_b64 exec, -1\n\t"
      "s_mov_b32 m0, 64512\n\t"
      "s_mul_i32 s72, s75, 13\n\t"
      "v_min_u32_e32 v141, 12, v192\n\t"
      "v_add_u32_e32 v141, s72, v141\n\t"
      "v_min_u32_e32 v141, 3135, v141\n\t"
      "v_lshlrev_b32_e32 v141, 7, v141\n\t"
      "global_load_lds_dword v141, s[8:9]\n\t"
      "SK10_%=:\n\t"
      "s_waitcnt vmcnt(8)\n\t"
      "s_barrier\n\t"
      "s_mul_i32 s72, s82, 172032\n\t"
      "s_add_u32 s72, s72, s37\n\t"
      "s_add_u32 s72, s72, 1792\n\t"
      "s_add_u32 s40, s6, s72\n\t"
      "s_addc_u32 s41, s7, 0\n\t"
      "s_add_u32 s82, s82, 1\n\t"
      "s_cmp_eq_u32 s82, 7\n\t"
      "s_cselect_b32 s82, 0, s82\n\t"
      "s_add_u32 m0, s39, 65536\n\t"
      "s_nop 0\n\t"
      "global_load_lds_dwordx4 v148, s[40:41]\n\t"
      "global_load_lds_dwordx4 v152, s[40:41] offset:1024\n\t"
      "s_mul_i32 s72, s82, 172032\n\t"
      "s_add_u32 s72, s72, s37\n\t"
      "s_add_u32 s72, s72, 1792\n\t"
      "s_add_u32 s40, s6, s72\n\t"
      "s_addc_u32 s41, s7, 0\n\t"
      "s_add_u32 s82, s82, 1\n\t"
      "s_cmp_eq_u32 s82, 7\n\t"
      "s_cselect_b32 s82, 0, s82\n\t"
      "s_add_u32 m0, s39, 67584\n\t"
      "s_nop 0\n\t"
      "global_load_lds_dwordx4 v148, s[40:41]\n\t"
      "global_load_lds_dwordx4 v152, s[40:41] offset:1024\n\t"
      "s_waitcnt vmcnt(8)\n\t"
      "s_barrier\n\t"
      "s_mul_i32 s72, s82, 172032\n\t"
      "s_add_u32 s72, s72, s37\n\t"
      "s_add_u32 s72, s72, 1792\n\t"
      "s_add_u32 s40, s6, s72\n\t"
      "s_addc_u32 s41, s7, 0\n\t"
      "s_add_u32 s82, s82, 1\n\t"
      "s_cmp_eq_u32 s82, 7\n\t"
      "s_cselect_b32 s82, 0, s82\n\t"
      "s_add_u32 m0, s39, 81920\n\t"
      "s_nop 0\n\t"
      "global_load_lds_dwordx4 v148, s[40:41]\n\t"
      "global_load_lds_dwordx4 v152, s[40:41] offset:1024\n\t"
      "s_mul_i32 s72, s82, 172032\n\t"
      "s_add_u32 s72, s72, s37\n\t"
      "s_add_u32 s72, s72, 1792\n\t"
      "s_add_u32 s40, s6, s72\n\t"
      "s_addc_u32 s41, s7, 0\n\t"
      "s_add_u32 s82, s82, 1\n\t"
      "s_cmp_eq_u32 s82, 7\n\t"
      "s_cselect_b32 s82, 0, s82\n\t"
      "s_add_u32 m0, s39, 83968\n\t"
      "s_nop 0\n\t"
      "global_load_lds_dwordx4 v148, s[40:41]\n\t"
      "global_load_lds_dwordx4 v152, s[40:41] offset:1024\n\t"
      "s_waitcnt vmcnt(8)\n\t"
      "s_barrier\n\t"
      "s_cmp_lg_u32 s24, 3\n\t"
      "s_cbranch_scc1 SK11_%=\n\t"
      "global_load_dwordx2 v[138:139], v136, s[60:61] sc1\n\t"
      "SK11_%=:\n\t"
      "s_mul_i32 s72, s82, 172032\n\t"
      "s_add_u32 s72, s72, s37\n\t"
      "s_add_u32 s72, s72, 1792\n\t"
      "s_add_u32 s40, s6, s72\n\t"
      "s_addc_u32 s41, s7, 0\n\t"
      "s_add_u32 s82, s82, 1\n\t"
      "s_cmp_eq_u32 s82, 7\n\t"
      "s_cselect_b32 s82, 0, s82\n\t"
      "s_lshr_b32 s72, s39, 1\n\t"
      "s_add_u32 m0, s72, 98304\n\t"
      "s_nop 0\n\t"
      "global_load_lds_dwordx4 v148, s[40:41]\n\t"
      "global_load_lds_dwordx4 v152, s[40:41] offset:1024\n\t"
      "global_load_dword v249, v248, s[12:13]\n\t"
      "s_lshl2_add_u32 s72, s31, s24\n\t"
      "s_and_b32 s73, s72, 0x70\n\t"
      "s_add_u32 s72, s72, s73\n\t"
      "s_mov_b32 m0, 64512\n\t"
      "s_mul_i32 s72, s72, 28672\n\t"
      "s_and_b32 s73, s2, 7\n\t"
      "s_mul_i32 s73, s73, 448\n\t"
      "s_and_b32 s73, s73, 0xffffff80\n\t"
      "s_add_u32 s72, s72, s73\n\t"
      "v_bfe_u32 v141, v192, 2, 3\n\t"
      "s_cmp_ge_u32 s31, 28\n\t"
      "s_cselect_b32 s73, 32, 0\n\t"
      "v_and_b32_e32 v142, s73, v192\n\t"
      "v_lshl_or_b32 v141, v142, 2, v141\n\t"
      "v_mul_u32_u24_e32 v141, 3584, v141\n\t"
      "v_lshl_add_u32 v141, v196, 7, v141\n\t"
      "v_add_u32_e32 v141, s72, v141\n\t"
      "global_load_lds_dword v141, s[10:11]\n\t"
      "s_waitcnt vmcnt(8)\n\t"
      "s_barrier\n\t"
      "s_mul_i32 s72, s24, 14336\n\t"
      "s_add_u32 s40, s56, s72\n\t"
      "s_addc_u32 s41, s57, 0\n\t"
      "s_lshr_b32 s72, s39, 1\n\t"
      "s_add_u32 m0, s72, 106496\n\t"
      "s_nop 0\n\t"
      "global_load_lds_dwordx4 v154, s[40:41]\n\t"
      "global_load_lds_dwordx4 v155, s[40:41] offset:1024\n\t"
      "s_waitcnt vmcnt(6)\n\t"
      "s_barrier\n\t"
      "s_mul_i32 s72, s24, 28672\n\t"
      "s_add_u32 s72, s72, 57344\n\t"
      "s_add_u32 s40, s56, s72\n\t"
      "s_addc_u32 s41, s57, 0\n\t"
      "s_lshr_b32 s72, s39, 0\n\t"
      "s_add_u32 m0, s72, 114688\n\t"
      "s_nop 0\n\t"
      "global_load_lds_dwordx4 v154, s[40:41]\n\t"
      "global_load_lds_dwordx4 v155, s[40:41] offset:1024\n\t"
      "global_load_lds_dwordx4 v156, s[40:41] offset:2048\n\t"
      "global_load_lds_dwordx4 v157, s[40:41] offset:3072\n\t"
      "s_waitcnt vmcnt(8)\n\t"
      "s_cmp_lg_u32 s24, 3\n\t"
      "s_cbranch_scc1 LF1B_%=\n\t"
      "v_cmp_ne_u64_e32 vcc, s[22:23], v[138:139]\n\t"
      "s_cbranch_vccnz LF1R_%=\n\t"
      "LF1B_%=:\n\t"
      "s_barrier\n\t"
      "s_mul_i32 s72, s24, 28672\n\t"
      "s_add_u32 s72, s72, 172032\n\t"
      "s_add_u32 s40, s56, s72\n\t"
      "s_addc_u32 s41, s57, 0\n\t"
      "s_lshr_b32 s72, s39, 0\n\t"
      "s_add_u32 m0, s72, 131072\n\t"
      "s_nop 0\n\t"
      "global_load_lds_dwordx4 v154, s[40:41]\n\t"
      "global_load_lds_dwordx4 v155, s[40:41] offset:1024\n\t"
      "global_load_lds_dwordx4 v156, s[40:41] offset:2048\n\t"
      "global_load_lds_dwordx4 v157, s[40:41] offset:3072\n\t"
      "s_mul_i32 s72, s24, 28672\n\t"
      "s_add_u32 s72, s72, 286720\n\t"
      "s_add_u32 s40, s56, s72\n\t"
      "s_addc_u32 s41, s57, 0\n\t"
      "s_lshr_b32 s72, s39, 0\n\t"
      "s_add_u32 m0, s72, 147456\n\t"
      "s_nop 0\n\t"
      "global_load_lds_dwordx4 v154, s[40:41]\n\t"
      "global_load_lds_dwordx4 v155, s[40:41] offset:1024\n\t"
      "global_load_lds_dwordx4 v156, s[40:41] offset:2048\n\t"
      "global_load_lds_dwordx4 v157, s[40:41] offset:3072\n\t"
      "s_barrier\n\t"
      "s_cmp_eq_u32 s26, 7\n\t"
      "s_cbranch_scc1 SPARE_%=\n\t"
      "s_lshl_b32 s72, s24, 10\n\t"
      "s_add_u32 s72, s72, 98304\n\t"
      "v_xor_b32_e32 v203, v197, v198\n\t"
      "v_lshl_add_u32 v203, v203, 4, s72\n\t"
      "v_lshl_add_u32 v203, v197, 7, v203\n\t"
      "ds_write_b128 v203, v[204:207]\n\t"
      "s_waitcnt lgkmcnt(0)\n\t"
      "s_bitcmp0_b32 s27, 0\n\t"
      "s_cbranch_scc1 L4_%=\n\t"
      "s_add_u32 s72, s76, 0\n\t"
      "s_add_u32 s40, s52, s72\n\t"
      "s_addc_u32 s41, s53, 0\n\t"
      "s_add_u32 m0, s39, 65536\n\t"
      "s_nop 0\n\t"
      "global_load_lds_dwordx4 v144, s[40:41]\n\t"
      "global_load_lds_dwordx4 v145, s[40:41] offset:1024\n\t"
      "global_load_lds_dwordx4 v146, s[40:41] offset:2048\n\t"
      "global_load_lds_dwordx4 v147, s[40:41] offset:3072\n\t"
      "s_add_u32 s72, s76, 229376\n\t"
      "s_add_u32 s40, s52, s72\n\t"
      "s_addc_u32 s41, s53, 0\n\t"
      "s_add_u32 m0, s39, 81920\n\t"
      "s_nop 0\n\t"
      "global_load_lds_dwordx4 v144, s[40:41]\n\t"
      "global_load_lds_dwordx4 v145, s[40:41] offset:1024\n\t"
      "global_load_lds_dwordx4 v146, s[40:41] offset:2048\n\t"
      "global_load_lds_dwordx4 v147, s[40:41] offset:3072\n\t"
      "s_waitcnt vmcnt(8)\n\t"
      "s_barrier\n\t"
      "s_add_u32 s72, s76, 3670016\n\t"
      "s_add_u32 s40, s52, s72\n\t"
      "s_addc_u32 s41, s53, 0\n\t"
      "s_add_u32 m0, s39, 16384\n\t"
      "s_nop 0\n\t"
      "global_load_lds_dwordx4 v144, s[40:41]\n\t"
      "global_load_lds_dwordx4 v145, s[40:41] offset:1024\n\t"
      "global_load_lds_dwordx4 v146, s[40:41] offset:2048\n\t"
      "global_load_lds_dwordx4 v147, s[40:41] offset:3072\n\t"
      "s_barrier\n\t"
      "s_add_u32 s72, s76, 3899392\n\t"
      "s_add_u32 s40, s52, s72\n\t"
      "s_addc_u32 s41, s53, 0\n\t"
      "s_add_u32 m0, s39, 49152\n\t"
      "s_nop 0\n\t"
      "global_load_lds_dwordx4 v144, s[40:41]\n\t"
      "global_load_lds_dwordx4 v145, s[40:41] offset:1024\n\t"
      "global_load_lds_dwordx4 v146, s[40:41] offset:2048\n\t"
      "global_load_lds_dwordx4 v147, s[40:41] offset:3072\n\t"
      "s_waitcnt vmcnt(12)\n\t"
      "ds_read_b64_tr_b16 v[0:1], v172\n\t"
      "ds_read_b64_tr_b16 v[2:3], v172 offset:512\n\t"
      "ds_read_b64_tr_b16 v[4:5], v173\n\t"
      "ds_read_b64_tr_b16 v[6:7], v173 offset:512\n\t"
      "ds_read_b64_tr_b16 v[8:9], v174\n\t"
      "ds_read_b64_tr_b16 v[10:11], v174 offset:512\n\t"
      "ds_read_b64_tr_b16 v[12:13], v175\n\t"
      "ds_read_b64_tr_b16 v[14:15], v175 offset:512\n\t"
      "s_waitcnt lgkmcnt(0)\n\t"
      "s_add_u32 s72, s77, 0\n\t"
      "s_add_u32 s40, s52, s72\n\t"
      "s_addc_u32 s41, s53, 0\n\t"
      "s_add_u32 m0, s39, 65536\n\t"
      "s_nop 0\n\t"
      "global_load_lds_dwordx4 v144, s[40:41]\n\t"
      "global_load_lds_dwordx4 v145, s[40:41] offset:1024\n\t"
      "global_load_lds_dwordx4 v146, s[40:41] offset:2048\n\t"
      "global_load_lds_dwordx4 v147, s[40:41] offset:3072\n\t"
      "s_waitcnt vmcnt(12)\n\t"
      "ds_read_b64_tr_b16 v[16:17], v172 offset:16384\n\t"
      "ds_read_b64_tr_b16 v[18:19], v172 offset:16896\n\t"
      "ds_read_b64_tr_b16 v[20:21], v173 offset:16384\n\t"
      "ds_read_b64_tr_b16 v[22:23], v173 offset:16896\n\t"
      "ds_read_b64_tr_b16 v[24:25], v174 offset:16384\n\t"
      "ds_read_b64_tr_b16 v[26:27], v174 offset:16896\n\t"
      "ds_read_b64_tr_b16 v[28:29], v175 offset:16384\n\t"
      "ds_read_b64_tr_b16 v[30:31], v175 offset:16896\n\t"
      "s_waitcnt lgkmcnt(0)\n\t"
      "s_add_u32 s72, s77, 229376\n\t"
      "s_add_u32 s40, s52, s72\n\t"
      "s_addc_u32 s41, s53, 0\n\t"
      "s_add_u32 m0, s39, 81920\n\t"
      "s_nop 0\n\t"
      "global_load_lds_dwordx4 v144, s[40:41]\n\t"
      "global_load_lds_dwordx4 v145, s[40:41] offset:1024\n\t"
      "global_load_lds_dwordx4 v146, s[40:41] offset:2048\n\t"
      "global_load_lds_dwordx4 v147, s[40:41] offset:3072\n\t"
      "s_add_u32 s72, s77, 3670016\n\t"
      "s_add_u32 s40, s52, s72\n\t"
      "s_addc_u32 s41, s53, 0\n\t"
      "s_add_u32 m0, s39, 0\n\t"
      "s_nop 0\n\t"
      "global_load_lds_dwordx4 v144, s[40:41]\n\t"
      "global_load_lds_dwordx4 v145, s[40:41] offset:1024\n\t"
      "global_load_lds_dwordx4 v146, s[40:41] offset:2048\n\t"
      "global_load_lds_dwordx4 v147, s[40:41] offset:3072\n\t"
      "s_add_u32 s72, s77, 3899392\n\t"
      "s_add_u32 s40, s52, s72\n\t"
      "s_addc_u32 s41, s53, 0\n\t"
      "s_add_u32 m0, s39, 32768\n\t"
      "s_nop 0\n\t"
      "global_load_lds_dwordx4 v144, s[40:41]\n\t"
      "global_load_lds_dwordx4 v145, s[40:41] offset:1024\n\t"
      "global_load_lds_dwordx4 v146, s[40:41] offset:2048\n\t"
      "global_load_lds_dwordx4 v147, s[40:41] offset:3072\n\t"
      "s_waitcnt vmcnt(12)\n\t"
      "ds_read_b64_tr_b16 v[32:33], v172\n\t"
      "ds_read_b64_tr_b16 v[34:35], v172 offset:512\n\t"
      "ds_read_b64_tr_b16 v[36:37], v173\n\t"
      "ds_read_b64_tr_b16 v[38:39], v173 offset:512\n\t"
      "ds_read_b64_tr_b16 v[40:41], v174\n\t"
      "ds_read_b64_tr_b16 v[42:43], v174 offset:512\n\t"
      "ds_read_b64_tr_b16 v[44:45], v175\n\t"
      "ds_read_b64_tr_b16 v[46:47], v175 offset:512\n\t"
      "s_waitcnt lgkmcnt(0)\n\t"
      "s_add_u32 s72, s78, 0\n\t"
      "s_add_u32 s40, s52, s72\n\t"
      "s_addc_u32 s41, s53, 0\n\t"
      "s_add_u32 m0, s39, 65536\n\t"
      "s_nop 0\n\t"
      "global_load_lds_dwordx4 v144, s[40:41]\n\t"
      "global_load_lds_dwordx4 v145, s[40:41] offset:1024\n\t"
      "global_load_lds_dwordx4 v146, s[40:41] offset:2048\n\t"
      "global_load_lds_dwordx4 v147, s[40:41] offset:3072\n\t"
      "s_waitcnt vmcnt(12)\n\t"
      "ds_read_b64_tr_b16 v[48:49], v172 offset:16384\n\t"
      "ds_read_b64_tr_b16 v[50:51], v172 offset:16896\n\t"
      "ds_read_b64_tr_b16 v[52:53], v173 offset:16384\n\t"
      "ds_read_b64_tr_b16 v[54:55], v173 offset:16896\n\t"
      "ds_read_b64_tr_b16 v[56:57], v174 offset:16384\n\t"
      "ds_read_b64_tr_b16 v[58:59], v174 offset:16896\n\t"
      "ds_read_b64_tr_b16 v[60:61], v175 offset:16384\n\t"
      "ds_read_b64_tr_b16 v[62:63], v175 offset:16896\n\t"
      "s_waitcnt lgkmcnt(0)\n\t"
      "s_add_u32 s72, s78, 229376\n\t"
      "s_add_u32 s40, s52, s72\n\t"
      "s_addc_u32 s41, s53, 0\n\t"
      "s_add_u32 m0, s39, 81920\n\t"
      "s_nop 0\n\t"
      "global_load_lds_dwordx4 v144, s[40:41]\n\t"
      "global_load_lds_dwordx4 v145, s[40:41] offset:1024\n\t"
      "global_load_lds_dwordx4 v146, s[40:41] offset:2048\n\t"
      "global_load_lds_dwordx4 v147, s[40:41] offset:3072\n\t"
      "s_waitcnt vmcnt(8)\n\t"
      "s_barrier\n\t"
      "s_cmp_lg_u32 s24, 3\n\t"
      "s_cbranch_scc1 SK13_%=\n\t"
      "global_load_dwordx2 v[138:139], v137, s[60:61] sc1\n\t"
      "SK13_%=:\n\t"
      "s_cmp_lg_u32 s24, 3\n\t"
      "s_cbranch_scc1 LF2B_%=\n\t"
      "s_waitcnt vmcnt(0)\n\t"
      "v_cmp_ne_u64_e32 vcc, s[22:23], v[138:139]\n\t"
      "s_cbranch_vccnz LF2R_%=\n\t"
      "LF2B_%=:\n\t"
      "s_barrier\n\t"
      "s_add_u32 s72, s78, 3670016\n\t"
      "s_add_u32 s40, s52, s72\n\t"
      "s_addc_u32 s41, s53, 0\n\t"
      "s_add_u32 m0, s39, 16384\n\t"
      "s_nop 0\n\t"
      "global_load_lds_dwordx4 v144, s[40:41]\n\t"
      "global_load_lds_dwordx4 v145, s[40:41] offset:1024\n\t"
      "global_load_lds_dwordx4 v146, s[40:41] offset:2048\n\t"
      "global_load_lds_dwordx4 v147, s[40:41] offset:3072\n\t"
      "s_add_u32 s72, s78, 3899392\n\t"
      "s_add_u32 s40, s52, s72\n\t"
      "s_addc_u32 s41, s53, 0\n\t"
      "s_add_u32 m0, s39, 49152\n\t"
      "s_nop 0\n\t"
      "global_load_lds_dwordx4 v144, s[40:41]\n\t"
      "global_load_lds_dwordx4 v145, s[40:41] offset:1024\n\t"
      "global_load_lds_dwordx4 v146, s[40:41] offset:2048\n\t"
      "global_load_lds_dwordx4 v147, s[40:41] offset:3072\n\t"
      "s_add_u32 s72, s79, 3670016\n\t"
      "s_add_u32 s40, s52, s72\n\t"
      "s_addc_u32 s41, s53, 0\n\t"
      "s_add_u32 m0, s39, 0\n\t"
      "s_nop 0\n\t"
      "global_load_lds_dwordx4 v144, s[40:41]\n\t"
      "global_load_lds_dwordx4 v145, s[40:41] offset:1024\n\t"
      "global_load_lds_dwordx4 v146, s[40:41] offset:2048\n\t"
      "global_load_lds_dwordx4 v147, s[40:41] offset:3072\n\t"
      "s_add_u32 s72, s79, 3899392\n\t"
      "s_add_u32 s40, s52, s72\n\t"
      "s_addc_u32 s41, s53, 0\n\t"
      "s_add_u32 m0, s39, 32768\n\t"
      "s_nop 0\n\t"
      "global_load_lds_dwordx4 v144, s[40:41]\n\t"
      "global_load_lds_dwordx4 v145, s[40:41] offset:1024\n\t"
      "global_load_lds_dwordx4 v146, s[40:41] offset:2048\n\t"
      "global_load_lds_dwordx4 v147, s[40:41] offset:3072\n\t"
      "s_waitcnt vmcnt(20)\n\t"
      "ds_read_b64_tr_b16 v[64:65], v172\n\t"
      "ds_read_b64_tr_b16 v[66:67], v172 offset:512\n\t"
      "ds_read_b64_tr_b16 v[68:69], v173\n\t"
      "ds_read_b64_tr_b16 v[70:71], v173 offset:512\n\t"
      "ds_read_b64_tr_b16 v[72:73], v174\n\t"
      "ds_read_b64_tr_b16 v[74:75], v174 offset:512\n\t"
      "ds_read_b64_tr_b16 v[76:77], v175\n\t"
      "ds_read_b64_tr_b16 v[78:79], v175 offset:512\n\t"
      "s_waitcnt vmcnt(16)\n\t"
      "ds_read_b64_tr_b16 v[80:81], v172 offset:16384\n\t"
      "ds_read_b64_tr_b16 v[82:83], v172 offset:16896\n\t"
      "ds_read_b64_tr_b16 v[84:85], v173 offset:16384\n\t"
      "ds_read_b64_tr_b16 v[86:87], v173 offset:16896\n\t"
      "ds_read_b64_tr_b16 v[88:89], v174 offset:16384\n\t"
      "ds_read_b64_tr_b16 v[90:91], v174 offset:16896\n\t"
      "ds_read_b64_tr_b16 v[92:93], v175 offset:16384\n\t"
      "ds_read_b64_tr_b16 v[94:95], v175 offset:16896\n\t"
      "s_cmp_lg_u32 s24, 3\n\t"
      "s_cbranch_scc1 SK14_%=\n\t"
      "global_load_dwordx2 v[138:139], v143, s[60:61] sc1\n\t"
      "SK14_%=:\n\t"
      "s_waitcnt vmcnt(0)\n\t"
      "s_cmp_lg_u32 s24, 3\n\t"
      "s_cbranch_scc1 LF3B_%=\n\t"
      "s_waitcnt vmcnt(0)\n\t"
      "v_cmp_ne_u64_e32 vcc, s[22:23], v[138:139]\n\t"
      "s_cbranch_vccnz LF3R_%=\n\t"
      "LF3B_%=:\n\t"
      "s_barrier\n\t"
      "s_waitcnt lgkmcnt(0)\n\t"
      "s_add_u32 s72, s79, 0\n\t"
      "s_add_u32 s40, s52, s72\n\t"
      "s_addc_u32 s41, s53, 0\n\t"
      "s_add_u32 m0, s39, 65536\n\t"
      "s_nop 0\n\t"
      "global_load_lds_dwordx4 v144, s[40:41]\n\t"
      "global_load_lds_dwordx4 v145, s[40:41] offset:1024\n\t"
      "global_load_lds_dwordx4 v146, s[40:41] offset:2048\n\t"
      "global_load_lds_dwordx4 v147, s[40:41] offset:3072\n\t"
      "s_add_u32 s72, s79, 229376\n\t"
      "s_add_u32 s40, s52, s72\n\t"
      "s_addc_u32 s41, s53, 0\n\t"
      "s_add_u32 m0, s39, 81920\n\t"
      "s_nop 0\n\t"
      "global_load_lds_dwordx4 v144, s[40:41]\n\t"
      "global_load_lds_dwordx4 v145, s[40:41] offset:1024\n\t"
      "global_load_lds_dwordx4 v146, s[40:41] offset:2048\n\t"
      "global_load_lds_dwordx4 v147, s[40:41] offset:3072\n\t"
      "s_mul_i32 s34, s30, 65536\n\t"
      "s_mul_i32 s35, s24, 8192\n\t"
      "s_add_u32 s34, s34, s35\n\t"
      "s_add_u32 s64, s48, s34\n\t"
      "s_addc_u32 s65, s49, 0\n\t"
      "v_add_u32_e32 v250, 4096, v244\n\t"
      "global_load_dwordx4 v[128:131], v250, s[64:65] offset:-4096 sc1\n\t"
      "global_load_dwordx4 v[132:135], v250, s[64:65] offset:-3072 sc1\n\t"
      "global_load_dwordx4 v[136:139], v250, s[64:65] offset:-2048 sc1\n\t"
      "global_load_dwordx4 v[140:143], v250, s[64:65] offset:-1024 sc1\n\t"
      "global_load_dwordx4 v[144:147], v250, s[64:65] sc1\n\t"
      "global_load_dwordx4 v[148:151], v250, s[64:65] offset:1024 sc1\n\t"
      "global_load_dwordx4 v[152:155], v250, s[64:65] offset:2048 sc1\n\t"
      "global_load_dwordx4 v[156:159], v250, s[64:65] offset:3072 sc1\n\t"
      "s_waitcnt vmcnt(12)\n\t"
      "ds_read_b64_tr_b16 v[96:97], v172\n\t"
      "ds_read_b64_tr_b16 v[98:99], v172 offset:512\n\t"
      "ds_read_b64_tr_b16 v[100:101], v173\n\t"
      "ds_read_b64_tr_b16 v[102:103], v173 offset:512\n\t"
      "ds_read_b64_tr_b16 v[104:105], v174\n\t"
      "ds_read_b64_tr_b16 v[106:107], v174 offset:512\n\t"
      "ds_read_b64_tr_b16 v[108:109], v175\n\t"
      "ds_read_b64_tr_b16 v[110:111], v175 offset:512\n\t"
      "s_waitcnt vmcnt(8)\n\t"
      "ds_read_b64_tr_b16 v[112:113], v172 offset:16384\n\t"
      "ds_read_b64_tr_b16 v[114:115], v172 offset:16896\n\t"
      "ds_read_b64_tr_b16 v[116:117], v173 offset:16384\n\t"
      "ds_read_b64_tr_b16 v[118:119], v173 offset:16896\n\t"
      "ds_read_b64_tr_b16 v[120:121], v174 offset:16384\n\t"
      "ds_read_b64_tr_b16 v[122:123], v174 offset:16896\n\t"
      "ds_read_b64_tr_b16 v[124:125], v175 offset:16384\n\t"
      "ds_read_b64_tr_b16 v[126:127], v175 offset:16896\n\t"
      "s_waitcnt vmcnt(0) lgkmcnt(0)\n\t"
      "s_branch GST_%=\n\t"
      "G4C_%=:\n\t"
      "global_store_dwordx4 v241, v[208:211], s[20:21] offset:-2048 sc1\n\t"
      "global_store_dwordx4 v241, v[212:215], s[20:21] offset:2048 sc1\n\t"
      "G4_%=:\n\t"
      "s_mul_i32 s34, s30, 57344\n\t"
      "s_mul_i32 s35, s24, 7168\n\t"
      "s_add_u32 s34, s34, s35\n\t"
      "s_add_u32 s34, s34, 1835008\n\t"
      "s_add_u32 s64, s48, s34\n\t"
      "s_addc_u32 s65, s49, 0\n\t"
      "s_and_b32 s34, s30, 3\n\t"
      "s_lshl_b32 s34, s34, 11\n\t"
      "s_add_u32 s40, s34, 0\n\t"
      "s_sub_u32 s35, s40, 7168\n\t"
      "s_min_u32 s40, s40, s35\n\t"
      "s_add_u32 s41, s34, 1024\n\t"
      "s_sub_u32 s35, s41, 7168\n\t"
      "s_min_u32 s41, s41, s35\n\t"
      "s_add_u32 s42, s34, 2048\n\t"
      "s_sub_u32 s35, s42, 7168\n\t"
      "s_min_u32 s42, s42, s35\n\t"
      "s_add_u32 s43, s34, 3072\n\t"
      "s_sub_u32 s35, s43, 7168\n\t"
      "s_min_u32 s43, s43, s35\n\t"
      "s_add_u32 s44, s34, 4096\n\t"
      "s_sub_u32 s35, s44, 7168\n\t"
      "s_min_u32 s44, s44, s35\n\t"
      "s_add_u32 s45, s34, 5120\n\t"
      "s_sub_u32 s35, s45, 7168\n\t"
      "s_min_u32 s45, s45, s35\n\t"
      "s_add_u32 s46, s34, 6144\n\t"
      "s_sub_u32 s35, s46, 7168\n\t"
      "s_min_u32 s46, s46, s35\n\t"
      "s_add_u32 s66, s64, 57344\n\t"
      "s_addc_u32 s67, s65, 0\n\t"
      "s_add_u32 s68, s66, 57344\n\t"
      "s_addc_u32 s69, s67, 0\n\t"
      "s_add_u32 s70, s68, 57344\n\t"
      "s_addc_u32 s71, s69, 0\n\t"
      "s_add_u32 s72, s70, 57344\n\t"
      "s_addc_u32 s73, s71, 0\n\t"
      "s_add_u32 s74, s72, 57344\n\t"
      "s_addc_u32 s75, s73, 0\n\t"
      "s_add_u32 s76, s74, 57344\n\t"
      "s_addc_u32 s77, s75, 0\n\t"
      "v_lshrrev_b32_e32 v240, 7, v244\n\t"
      "v_and_b32_e32 v241, 0x70, v244\n\t"
      "v_and_b32_e32 v242, 6, v240\n\t"
      "v_lshlrev_b32_e32 v242, 4, v242\n\t"
      "v_xor_b32_e32 v242, v242, v241\n\t"
      "v_lshl_or_b32 v228, v240, 12, v242\n\t"
      "s_mul_i32 s34, s24, 917504\n\t"
      "s_lshl_b32 s35, s29, 7\n\t"
      "s_add_u32 s34, s34, s35\n\t"
      "s_lshl_b32 s39, s24, 12\n\t"
      "s_add_u32 s54, s14, s34\n\t"
      "s_addc_u32 s55, s15, 0\n\t"
      "v_add_u32_e32 v229, 31744, v228\n\t"
      "v_add_u32_e32 v230, 63488, v228\n\t"
      "v_add_u32_e32 v231, 95232, v228\n\t"
      "s_lshl_b32 s37, s40, 7\n\t"
      "s_add_u32 s58, s54, s37\n\t"
      "s_addc_u32 s59, s55, 0\n\t"
      "s_add_u32 m0, s39, 0\n\t"
      "s_nop 0\n\t"
      "global_load_lds_dwordx4 v228, s[58:59]\n\t"
      "global_load_lds_dwordx4 v229, s[58:59] offset:1024\n\t"
      "global_load_lds_dwordx4 v230, s[58:59] offset:2048\n\t"
      "global_load_lds_dwordx4 v231, s[58:59] offset:3072\n\t"
      "s_lshl_b32 s37, s41, 7\n\t"
      "s_add_u32 s58, s54, s37\n\t"
      "s_addc_u32 s59, s55, 0\n\t"
      "s_add_u32 m0, s39, 32768\n\t"
      "s_nop 0\n\t"
      "global_load_lds_dwordx4 v228, s[58:59]\n\t"
      "global_load_lds_dwordx4 v229, s[58:59] offset:1024\n\t"
      "global_load_lds_dwordx4 v230, s[58:59] offset:2048\n\t"
      "global_load_lds_dwordx4 v231, s[58:59] offset:3072\n\t"
      "s_lshl_b32 s37, s42, 7\n\t"
      "s_add_u32 s58, s54, s37\n\t"
      "s_addc_u32 s59, s55, 0\n\t"
      "s_add_u32 m0, s39, 65536\n\t"
      "s_nop 0\n\t"
      "global_load_lds_dwordx4 v228, s[58:59]\n\t"
      "global_load_lds_dwordx4 v229, s[58:59] offset:1024\n\t"
      "global_load_lds_dwordx4 v230, s[58:59] offset:2048\n\t"
      "global_load_lds_dwordx4 v231, s[58:59] offset:3072\n\t"
      "s_lshl_b32 s37, s43, 7\n\t"
      "s_add_u32 s58, s54, s37\n\t"
      "s_addc_u32 s59, s55, 0\n\t"
      "s_add_u32 m0, s39, 98304\n\t"
      "s_nop 0\n\t"
      "global_load_lds_dwordx4 v228, s[58:59]\n\t"
      "global_load_lds_dwordx4 v229, s[58:59] offset:1024\n\t"
      "global_load_lds_dwordx4 v230, s[58:59] offset:2048\n\t"
      "global_load_lds_dwordx4 v231, s[58:59] offset:3072\n\t"
      "s_lshr_b32 s34, s24, 1\n\t"
      "s_lshl2_add_u32 s35, s29, s34\n\t"
      "s_and_b32 s36, s24, 1\n\t"
      "s_lshl4_add_u32 s35, s35, s36\n\t"
      "v_lshl_add_u32 v242, v240, 1, s35\n\t"
      "v_lshlrev_b32_e32 v248, 1, v242\n\t"
      "global_load_ushort v249, v248, s[16:17]\n\t"
      "s_movk_i32 s35, 896\n\t"
      "v_and_b32_e32 v247, 64, v244\n\t"
      "v_lshrrev_b32_e32 v247, 2, v247\n\t"
      "v_mad_u32_u24 v247, v242, s35, v247\n\t"
      "v_and_b32_e32 v242, 0x30, v244\n\t"
      "v_lshl_add_u32 v247, v242, 1, v247\n\t"
      "s_mov_b32 s82, 0x77777777\n\t"
      "s_mov_b32 s83, 0x77777777\n\t"
      "s_lshl_b32 s35, s30, 5\n\t"
      "s_add_u32 s62, s18, s35\n\t"
      "s_addc_u32 s63, s19, 0\n\t"
      "s_mul_i32 s35, s29, 28\n\t"
      "s_add_u32 s35, s35, s30\n\t"
      "s_lshl1_add_u32 s35, s35, s36\n\t"
      "s_lshl2_add_u32 s35, s35, s34\n\t"
      "s_lshl_b32 s35, s35, 8\n\t"
      "s_add_u32 s35, s35, 6295552\n\t"
      "s_add_u32 s50, s20, s35\n\t"
      "s_addc_u32 s51, s21, 0\n\t"
      "v_lshrrev_b32_e32 v248, 2, v244\n\t"
      "v_bfe_u32 v242, v244, 8, 1\n\t"
      "v_lshlrev_b32_e32 v242, 4, v242\n\t"
      "v_xor_b32_e32 v245, v244, v242\n\t"
      "v_bfe_u32 v242, v244, 9, 1\n\t"
      "v_lshlrev_b32_e32 v242, 7, v242\n\t"
      "v_xor_b32_e32 v245, v245, v242\n\t"
      "s_add_u32 s35, s39, 98304\n\t"
      "v_add_u32_e32 v245, s35, v245\n\t"
      "v_lshl_or_b32 v246, v240, 1, s36\n\t"
      "v_bfe_u32 v242, v244, 5, 1\n\t"
      "v_xor_b32_e32 v246, v246, v242\n\t"
      "v_bfe_u32 v242, v244, 6, 1\n\t"
      "v_lshlrev_b32_e32 v242, 3, v242\n\t"
      "v_xor_b32_e32 v246, v246, v242\n\t"
      "v_lshlrev_b32_e32 v246, 4, v246\n\t"
      "v_and_b32_e32 v242, 0x60, v244\n\t"
      "v_lshl_or_b32 v246, v242, 3, v246\n\t"
      "v_bfe_u32 v242, v244, 4, 1\n\t"
      "v_lshl_or_b32 v246, v242, 3, v246\n\t"
      "s_lshl_b32 s34, s34, 10\n\t"
      "s_add_u32 s34, s34, 98304\n\t"
      "v_add_u32_e32 v246, s34, v246\n\t"
      "v_and_b32_e32 v240, 0x3c0, v244\n\t"
      "v_lshlrev_b32_e32 v240, 1, v240\n\t"
      "v_and_b32_e32 v241, 0x180, v244\n\t"
      "v_lshrrev_b32_e32 v241, 2, v241\n\t"
      "v_and_b32_e32 v242, 0x30, v244\n\t"
      "v_lshrrev_b32_e32 v242, 1, v242\n\t"
      "v_or3_b32 v232, v240, v241, v242\n\t"
      "v_add_u32_e32 v232, s39, v232\n\t"
      "v_min_u32_e32 v252, 432, v244\n\t"
      "v_lshlrev_b32_e32 v252, 4, v252\n\t"
      "v_xor_b32_e32 v233, 32, v232\n\t"
      "v_xor_b32_e32 v234, 64, v232\n\t"
      "v_xor_b32_e32 v235, 96, v232\n\t"
      "v_xor_b32_e32 v236, 65536, v232\n\t"
      "v_xor_b32_e32 v237, 65568, v232\n\t"
      "v_xor_b32_e32 v238, 65600, v232\n\t"
      "v_xor_b32_e32 v239, 65632, v232\n\t"
      "s_waitcnt vmcnt(13)\n\t"
      "ds_read_b64_tr_b16 v[0:1], v232\n\t"
      "ds_read_b64_tr_b16 v[2:3], v232 offset:2048\n\t"
      "ds_read_b64_tr_b16 v[4:5], v233\n\t"
      "ds_read_b64_tr_b16 v[6:7], v233 offset:2048\n\t"
      "ds_read_b64_tr_b16 v[8:9], v234\n\t"
      "ds_read_b64_tr_b16 v[10:11], v234 offset:2048\n\t"
      "ds_read_b64_tr_b16 v[12:13], v235\n\t"
      "ds_read_b64_tr_b16 v[14:15], v235 offset:2048\n\t"
      "s_waitcnt lgkmcnt(0)\n\t"
      "s_lshl_b32 s37, s44, 7\n\t"
      "s_add_u32 s58, s54, s37\n\t"
      "s_addc_u32 s59, s55, 0\n\t"
      "s_add_u32 m0, s39, 0\n\t"
      "s_nop 0\n\t"
      "global_load_lds_dwordx4 v228, s[58:59]\n\t"
      "global_load_lds_dwordx4 v229, s[58:59] offset:1024\n\t"
      "global_load_lds_dwordx4 v230, s[58:59] offset:2048\n\t"
      "global_load_lds_dwordx4 v231, s[58:59] offset:3072\n\t"
      "s_waitcnt vmcnt(13)\n\t"
      "ds_read_b64_tr_b16 v[16:17], v232 offset:32768\n\t"
      "ds_read_b64_tr_b16 v[18:19], v232 offset:34816\n\t"
      "ds_read_b64_tr_b16 v[20:21], v233 offset:32768\n\t"
      "ds_read_b64_tr_b16 v[22:23], v233 offset:34816\n\t"
      "ds_read_b64_tr_b16 v[24:25], v234 offset:32768\n\t"
      "ds_read_b64_tr_b16 v[26:27], v234 offset:34816\n\t"
      "ds_read_b64_tr_b16 v[28:29], v235 offset:32768\n\t"
      "ds_read_b64_tr_b16 v[30:31], v235 offset:34816\n\t"
      "s_waitcnt lgkmcnt(0)\n\t"
      "s_lshl_b32 s37, s45, 7\n\t"
      "s_add_u32 s58, s54, s37\n\t"
      "s_addc_u32 s59, s55, 0\n\t"
      "s_add_u32 m0, s39, 32768\n\t"
      "s_nop 0\n\t"
      "global_load_lds_dwordx4 v228, s[58:59]\n\t"
      "global_load_lds_dwordx4 v229, s[58:59] offset:1024\n\t"
      "global_load_lds_dwordx4 v230, s[58:59] offset:2048\n\t"
      "global_load_lds_dwordx4 v231, s[58:59] offset:3072\n\t"
      "s_waitcnt vmcnt(13)\n\t"
      "ds_read_b64_tr_b16 v[32:33], v236\n\t"
      "ds_read_b64_tr_b16 v[34:35], v236 offset:2048\n\t"
      "ds_read_b64_tr_b16 v[36:37], v237\n\t"
      "ds_read_b64_tr_b16 v[38:39], v237 offset:2048\n\t"
      "ds_read_b64_tr_b16 v[40:41], v238\n\t"
      "ds_read_b64_tr_b16 v[42:43], v238 offset:2048\n\t"
      "ds_read_b64_tr_b16 v[44:45], v239\n\t"
      "ds_read_b64_tr_b16 v[46:47], v239 offset:2048\n\t"
      "s_waitcnt lgkmcnt(0)\n\t"
      "s_lshl_b32 s37, s46, 7\n\t"
      "s_add_u32 s58, s54, s37\n\t"
      "s_addc_u32 s59, s55, 0\n\t"
      "s_add_u32 m0, s39, 65536\n\t"
      "s_nop 0\n\t"
      "global_load_lds_dwordx4 v228, s[58:59]\n\t"
      "global_load_lds_dwordx4 v229, s[58:59] offset:1024\n\t"
      "global_load_lds_dwordx4 v230, s[58:59] offset:2048\n\t"
      "global_load_lds_dwordx4 v231, s[58:59] offset:3072\n\t"
      "s_waitcnt vmcnt(13)\n\t"
      "ds_read_b64_tr_b16 v[48:49], v236 offset:32768\n\t"
      "ds_read_b64_tr_b16 v[50:51], v236 offset:34816\n\t"
      "ds_read_b64_tr_b16 v[52:53], v237 offset:32768\n\t"
      "ds_read_b64_tr_b16 v[54:55], v237 offset:34816\n\t"
      "ds_read_b64_tr_b16 v[56:57], v238 offset:32768\n\t"
      "ds_read_b64_tr_b16 v[58:59], v238 offset:34816\n\t"
      "ds_read_b64_tr_b16 v[60:61], v239 offset:32768\n\t"
      "ds_read_b64_tr_b16 v[62:63], v239 offset:34816\n\t"
      "s_waitcnt vmcnt(8)\n\t"
      "ds_read_b64_tr_b16 v[64:65], v232\n\t"
      "ds_read_b64_tr_b16 v[66:67], v232 offset:2048\n\t"
      "ds_read_b64_tr_b16 v[68:69], v233\n\t"
      "ds_read_b64_tr_b16 v[70:71], v233 offset:2048\n\t"
      "ds_read_b64_tr_b16 v[72:73], v234\n\t"
      "ds_read_b64_tr_b16 v[74:75], v234 offset:2048\n\t"
      "ds_read_b64_tr_b16 v[76:77], v235\n\t"
      "ds_read_b64_tr_b16 v[78:79], v235 offset:2048\n\t"
      "s_waitcnt vmcnt(4)\n\t"
      "ds_read_b64_tr_b16 v[80:81], v232 offset:32768\n\t"
      "ds_read_b64_tr_b16 v[82:83], v232 offset:34816\n\t"
      "ds_read_b64_tr_b16 v[84:85], v233 offset:32768\n\t"
      "ds_read_b64_tr_b16 v[86:87], v233 offset:34816\n\t"
      "ds_read_b64_tr_b16 v[88:89], v234 offset:32768\n\t"
      "ds_read_b64_tr_b16 v[90:91], v234 offset:34816\n\t"
      "ds_read_b64_tr_b16 v[92:93], v235 offset:32768\n\t"
      "ds_read_b64_tr_b16 v[94:95], v235 offset:34816\n\t"
      "s_waitcnt vmcnt(0)\n\t"
      "ds_read_b64_tr_b16 v[96:97], v236\n\t"
      "ds_read_b64_tr_b16 v[98:99], v236 offset:2048\n\t"
      "ds_read_b64_tr_b16 v[100:101], v237\n\t"
      "ds_read_b64_tr_b16 v[102:103], v237 offset:2048\n\t"
      "ds_read_b64_tr_b16 v[104:105], v238\n\t"
      "ds_read_b64_tr_b16 v[106:107], v238 offset:2048\n\t"
      "ds_read_b64_tr_b16 v[108:109], v239\n\t"
      "ds_read_b64_tr_b16 v[110:111], v239 offset:2048\n\t"
      "v_lshlrev_b32_e32 v249, 16, v249\n\t"
      "s_waitcnt lgkmcnt(0)\n\t"
      "v_min_u32_e32 v240, 48, v244\n\t"
      "v_mul_u32_u24_e32 v240, 7, v240\n\t"
      "s_lshl_b32 s34, s29, 4\n\t"
      "v_add_u32_e32 v240, s34, v240\n\t"
      "v_subrev_u32_e32 v241, 448, v240\n\t"
      "v_min_u32_e32 v240, v240, v241\n\t"
      "v_subrev_u32_e32 v241, 448, v240\n\t"
      "v_min_u32_e32 v240, v240, v241\n\t"
      "s_and_b32 s34, s29, 7\n\t"
      "s_lshl_b32 s34, s34, 8\n\t"
      "v_lshl_add_u32 v253, v240, 7, s34\n\t"
      "v_add_u32_e32 v143, 32768, v143\n\t"
      "s_cmp_lg_u32 s24, 3\n\t"
      "s_cbranch_scc1 SK18_%=\n\t"
      "global_load_dwordx2 v[138:139], v143, s[60:61] sc1\n\t"
      "SK18_%=:\n\t"
      "s_cmp_lg_u32 s24, 3\n\t"
      "s_cbranch_scc1 LF5B_%=\n\t"
      "s_waitcnt vmcnt(0)\n\t"
      "v_cmp_ne_u64_e32 vcc, s[22:23], v[138:139]\n\t"
      "s_cbranch_vccnz LF5R_%=\n\t"
      "LF5B_%=:\n\t"
      "s_barrier\n\t"
      "s_cmp_lg_u32 s24, 3\n\t"
      "s_cbranch_scc1 HF4_%=\n\t"
      "s_lshl3_add_u32 s34, s25, s26\n\t"
      "s_lshl_b32 s34, s34, 7\n\t"
      "s_add_u32 s34, s34, 98304\n\t"
      "v_mov_b32_e32 v238, s34\n\t"
      "v_mov_b32_e32 v236, s22\n\t"
      "v_mov_b32_e32 v237, s23\n\t"
      "s_mov_b64 exec, 1\n\t"
      "global_store_dwordx2 v238, v[236:237], s[60:61] sc1\n\t"
      "s_mov_b64 exec, -1\n\t"
      "HF4_%=:\n\t"
      "s_and_b32 s34, s29, 30\n\t"
      "s_lshl_b32 s34, s34, 10\n\t"
      "s_add_u32 s34, s34, 98304\n\t"
      "s_lshr_b32 s35, s30, 2\n\t"
      "s_add_u32 s36, s30, 6\n\t"
      "s_lshr_b32 s36, s36, 2\n\t"
      "v_lshrrev_b32_e32 v143, 5, v244\n\t"
      "v_add_u32_e32 v143, s35, v143\n\t"
      "v_min_u32_e32 v143, s36, v143\n\t"
      "v_lshl_add_u32 v143, v143, 7, s34\n\t"
      "v_and_b32_e32 v142, 16, v244\n\t"
      "v_lshl_add_u32 v143, v142, 6, v143\n\t"
      "s_cmp_eq_u32 s24, 0\n\t"
      "s_cbranch_scc1 PG0R_%=\n\t"
      "PG0B_%=:\n\t"
      "s_sleep 8\n\t"
      "s_barrier\n\t"
      "s_cmp_lg_u32 s24, 3\n\t"
      "s_cbranch_scc1 SK19_%=\n\t"
      "global_load_dwordx2 v[240:241], v143, s[60:61] sc1\n\t"
      "SK19_%=:\n\t"
      "v_mov_b32_e32 v242, v143\n\t"
      "s_add_u32 s58, s64, s40\n\t"
      "s_addc_u32 s59, s65, 0\n\t"
      "global_load_dwordx4 v[128:131], v244, s[58:59] sc1\n\t"
      "s_add_u32 s58, s64, s41\n\t"
      "s_addc_u32 s59, s65, 0\n\t"
      "global_load_dwordx4 v[132:135], v244, s[58:59] sc1\n\t"
      "s_add_u32 s58, s64, s42\n\t"
      "s_addc_u32 s59, s65, 0\n\t"
      "global_load_dwordx4 v[136:139], v244, s[58:59] sc1\n\t"
      "s_add_u32 s58, s64, s43\n\t"
      "s_addc_u32 s59, s65, 0\n\t"
      "global_load_dwordx4 v[140:143], v244, s[58:59] sc1\n\t"
      "s_add_u32 s58, s64, s44\n\t"
      "s_addc_u32 s59, s65, 0\n\t"
      "global_load_dwordx4 v[144:147], v244, s[58:59] sc1\n\t"
      "s_add_u32 s58, s64, s45\n\t"
      "s_addc_u32 s59, s65, 0\n\t"
      "global_load_dwordx4 v[148:151], v244, s[58:59] sc1\n\t"
      "s_add_u32 s58, s64, s46\n\t"
      "s_addc_u32 s59, s65, 0\n\t"
      "global_load_dwordx4 v[152:155], v244, s[58:59] sc1\n\t"
      "v_mov_b32_e32 v254, v249\n\t"
      "s_waitcnt vmcnt(6)\n\t"
      "v_mfma_f32_16x16x32_bf16 v[196:199], v[128:131], v[0:3], 0\n\t"
      "v_mfma_f32_16x16x32_bf16 v[200:203], v[128:131], v[4:7], 0\n\t"
      "v_mfma_f32_16x16x32_bf16 v[204:207], v[128:131], v[8:11], 0\n\t"
      "v_mfma_f32_16x16x32_bf16 v[208:211], v[128:131], v[12:15], 0\n\t"
      "s_waitcnt vmcnt(5)\n\t"
      "v_mfma_f32_16x16x32_bf16 v[196:199], v[132:135], v[16:19], v[196:199]\n\t"
      "v_mfma_f32_16x16x32_bf16 v[200:203], v[132:135], v[20:23], v[200:203]\n\t"
      "v_mfma_f32_16x16x32_bf16 v[204:207], v[132:135], v[24:27], v[204:207]\n\t"
      "s_cmp_lg_u32 s24, 3\n\t"
      "s_cbranch_scc1 LF8B_%=\n\t"
      "v_cmp_ne_u64_e32 vcc, s[22:23], v[240:241]\n\t"
      "s_cbranch_vccnz LF8R_%=\n\t"
      "LF8B_%=:\n\t"
      "s_barrier\n\t"
      "v_mfma_f32_16x16x32_bf16 v[208:211], v[132:135], v[28:31], v[208:211]\n\t"
      "global_load_dword v112, v248, s[50:51] offset:-4096 sc1\n\t"
      "global_load_dword v113, v248, s[50:51] offset:-2048 sc1\n\t"
      "s_waitcnt vmcnt(6)\n\t"
      "v_mfma_f32_16x16x32_bf16 v[196:199], v[136:139], v[32:35], v[196:199]\n\t"
      "global_load_dword v114, v248, s[50:51] sc1\n\t"
      "global_load_dword v115, v248, s[50:51] offset:2048 sc1\n\t"
      "s_add_u32 s58, s66, s40\n\t"
      "s_addc_u32 s59, s67, 0\n\t"
      "global_load_dwordx4 v[156:159], v244, s[58:59] sc1\n\t"
      "v_mfma_f32_16x16x32_bf16 v[200:203], v[136:139], v[36:39], v[200:203]\n\t"
      "s_add_u32 s50, s50, 8192\n\t"
      "s_addc_u32 s51, s51, 0\n\t"
      "global_load_dword v116, v248, s[50:51] offset:-4096 sc1\n\t"
      "global_load_dword v117, v248, s[50:51] offset:-2048 sc1\n\t"
      "v_mfma_f32_16x16x32_bf16 v[204:207], v[136:139], v[40:43], v[204:207]\n\t"
      "global_load_dword v118, v248, s[50:51] sc1\n\t"
      "v_mfma_f32_16x16x32_bf16 v[208:211], v[136:139], v[44:47], v[208:211]\n\t"
      "s_waitcnt vmcnt(11)\n\t"
      "v_mfma_f32_16x16x32_bf16 v[196:199], v[140:143], v[48:51], v[196:199]\n\t"
      "s_add_u32 s58, s66, s41\n\t"
      "s_addc_u32 s59, s67, 0\n\t"
      "global_load_dwordx4 v[160:163], v244, s[58:59] sc1\n\t"
      "v_mfma_f32_16x16x32_bf16 v[200:203], v[140:143], v[52:55], v[200:203]\n\t"
      "v_mfma_f32_16x16x32_bf16 v[204:207], v[140:143], v[56:59], v[204:207]\n\t"
      "v_mfma_f32_16x16x32_bf16 v[208:211], v[140:143], v[60:63], v[208:211]\n\t"
      "s_waitcnt vmcnt(11)\n\t"
      "v_mfma_f32_16x16x32_bf16 v[196:199], v[144:147], v[64:67], v[196:199]\n\t"
      "s_add_u32 s58, s66, s42\n\t"
      "s_addc_u32 s59, s67, 0\n\t"
      "global_load_dwordx4 v[164:167], v244, s[58:59] sc1\n\t"
      "v_mfma_f32_16x16x32_bf16 v[200:203], v[144:147], v[68:71], v[200:203]\n\t"
      "v_mfma_f32_16x16x32_bf16 v[204:207], v[144:147], v[72:75], v[204:207]\n\t"
      "v_mfma_f32_16x16x32_bf16 v[208:211], v[144:147], v[76:79], v[208:211]\n\t"
      "s_waitcnt vmcnt(11)\n\t"
      "v_mfma_f32_16x16x32_bf16 v[196:199], v[148:151], v[80:83], v[196:199]\n\t"
      "s_add_u32 s58, s66, s43\n\t"
      "s_addc_u32 s59, s67, 0\n\t"
      "global_load_dwordx4 v[168:171], v244, s[58:59] sc1\n\t"
      "s_waitcnt vmcnt(11)\n\t"
      "v_mfma_f32_16x16x32_bf16 v[196:199], v[152:155], v[96:99], v[196:199]\n\t"
      "v_mfma_f32_16x16x32_bf16 v[200:203], v[148:151], v[84:87], v[200:203]\n\t"
      "v_mfma_f32_16x16x32_bf16 v[200:203], v[152:155], v[100:103], v[200:203]\n\t"
      "v_mfma_f32_16x16x32_bf16 v[204:207], v[148:151], v[88:91], v[204:207]\n\t"
      "s_nop 4\n\t"
      "v_add_f32_e32 v251, v196, v197\n\t"
      "v_add_f32_e32 v251, v251, v198\n\t"
      "v_cmp_u_f32_e32 vcc, v251, v199\n\t"
      "s_cbranch_vccnz LA40R_%=\n\t"
      "LA40B_%=:\n\t"
      "s_add_u32 s58, s66, s44\n\t"
      "s_addc_u32 s59, s67, 0\n\t"
      "global_load_dwordx4 v[172:175], v244, s[58:59] sc1\n\t"
      "v_mfma_f32_16x16x32_bf16 v[204:207], v[152:155], v[104:107], v[204:207]\n\t"
      "ds_write_b128 v245, v[196:199]\n\t"
      "v_mfma_f32_16x16x32_bf16 v[208:211], v[148:151], v[92:95], v[208:211]\n\t"
      "v_mfma_f32_16x16x32_bf16 v[208:211], v[152:155], v[108:111], v[208:211]\n\t"
      "ds_write_b128 v245, v[200:203] offset:1024\n\t"
      "s_waitcnt vmcnt(7)\n\t"
      "v_mfma_f32_16x16x32_bf16 v[212:215], v[156:159], v[0:3], 0\n\t"
      "s_add_u32 s58, s66, s45\n\t"
      "s_addc_u32 s59, s67, 0\n\t"
      "global_load_dwordx4 v[176:179], v244, s[58:59] sc1\n\t"
      "v_mfma_f32_16x16x32_bf16 v[216:219], v[156:159], v[4:7], 0\n\t"
      "ds_write_b128 v245, v[204:207] offset:2048\n\t"
      "s_add_u32 s58, s66, s46\n\t"
      "s_addc_u32 s59, s67, 0\n\t"
      "global_load_dwordx4 v[180:183], v244, s[58:59] sc1\n\t"
      "v_mfma_f32_16x16x32_bf16 v[220:223], v[156:159], v[8:11], 0\n\t"
      "v_mfma_f32_16x16x32_bf16 v[224:227], v[156:159], v[12:15], 0\n\t"
      "ds_write_b128 v245, v[208:211] offset:3072\n\t"
      "s_waitcnt vmcnt(5)\n\t"
      "v_mfma_f32_16x16x32_bf16 v[212:215], v[160:163], v[16:19], v[212:215]\n\t"
      "s_waitcnt lgkmcnt(0)\n\t"
      "s_barrier\n\t"
      "ds_read_b64 v[228:229], v246\n\t"
      "ds_read_b64 v[230:231], v246 offset:4096\n\t"
      "v_mfma_f32_16x16x32_bf16 v[216:219], v[160:163], v[20:23], v[216:219]\n\t"
      "ds_read_b64 v[232:233], v246 offset:8192\n\t"
      "ds_read_b64 v[234:235], v246 offset:12288\n\t"
      "v_mfma_f32_16x16x32_bf16 v[220:223], v[160:163], v[24:27], v[220:223]\n\t"
      "ds_read_b64 v[236:237], v246 offset:16384\n\t"
      "ds_read_b64 v[238:239], v246 offset:20480\n\t"
      "s_add_u32 s58, s68, s40\n\t"
      "s_addc_u32 s59, s69, 0\n\t"
      "global_load_dwordx4 v[184:187], v244, s[58:59] sc1\n\t"
      "v_mfma_f32_16x16x32_bf16 v[224:227], v[160:163], v[28:31], v[224:227]\n\t"
      "ds_read_b64 v[240:241], v246 offset:24576\n\t"
      "ds_read_b64 v[242:243], v246 offset:28672\n\t"
      "v_lshlrev_b32_e32 v248, 16, v112\n\t"
      "s_waitcnt vmcnt(5)\n\t"
      "v_mfma_f32_16x16x32_bf16 v[212:215], v[164:167], v[32:35], v[212:215]\n\t"
      "v_and_b32_e32 v249, 0xffff0000, v112\n\t"
      "v_mfma_f32_16x16x32_bf16 v[216:219], v[164:167], v[36:39], v[216:219]\n\t"
      "v_add_f32_e32 v248, v254, v248\n\t"
      "v_mfma_f32_16x16x32_bf16 v[220:223], v[164:167], v[40:43], v[220:223]\n\t"
      "v_add_f32_e32 v249, v254, v249\n\t"
      "s_waitcnt lgkmcnt(4)\n\t"
      "s_add_u32 s58, s68, s41\n\t"
      "s_addc_u32 s59, s69, 0\n\t"
      "global_load_dwordx4 v[188:191], v244, s[58:59] sc1\n\t"
      "v_mfma_f32_16x16x32_bf16 v[224:227], v[164:167], v[44:47], v[224:227]\n\t"
      "v_add_f32_e32 v228, v228, v230\n\t"
      "s_waitcnt vmcnt(5)\n\t"
      "v_mfma_f32_16x16x32_bf16 v[212:215], v[168:171], v[48:51], v[212:215]\n\t"
      "v_add_f32_e32 v232, v232, v234\n\t"
      "v_mfma_f32_16x16x32_bf16 v[216:219], v[168:171], v[52:55], v[216:219]\n\t"
      "v_add_f32_e32 v229, v229, v231\n\t"
      "v_add_f32_e32 v233, v233, v235\n\t"
      "v_mfma_f32_16x16x32_bf16 v[220:223], v[168:171], v[56:59], v[220:223]\n\t"
      "s_waitcnt lgkmcnt(0)\n\t"
      "s_add_u32 s58, s68, s42\n\t"
      "s_addc_u32 s59, s69, 0\n\t"
      "global_load_dwordx4 v[192:195], v244, s[58:59] sc1\n\t"
      "v_mfma_f32_16x16x32_bf16 v[224:227], v[168:171], v[60:63], v[224:227]\n\t"
      "v_add_f32_e32 v236, v236, v238\n\t"
      "s_waitcnt vmcnt(5)\n\t"
      "v_mfma_f32_16x16x32_bf16 v[212:215], v[172:175], v[64:67], v[212:215]\n\t"
      "v_add_f32_e32 v240, v240, v242\n\t"
      "v_mfma_f32_16x16x32_bf16 v[216:219], v[172:175], v[68:71], v[216:219]\n\t"
      "v_add_f32_e32 v237, v237, v239\n\t"
      "v_add_f32_e32 v241, v241, v243\n\t"
      "v_mfma_f32_16x16x32_bf16 v[220:223], v[172:175], v[72:75], v[220:223]\n\t"
      "v_add_f32_e32 v228, v228, v232\n\t"
      "s_add_u32 s58, s68, s43\n\t"
      "s_addc_u32 s59, s69, 0\n\t"
      "global_load_dwordx4 v[128:131], v244, s[58:59] sc1\n\t"
      "v_mfma_f32_16x16x32_bf16 v[224:227], v[172:175], v[76:79], v[224:227]\n\t"
      "v_add_f32_e32 v236, v236, v240\n\t"
      "s_waitcnt vmcnt(5)\n\t"
      "v_mfma_f32_16x16x32_bf16 v[212:215], v[176:179], v[80:83], v[212:215]\n\t"
      "v_add_f32_e32 v229, v229, v233\n\t"
      "v_add_f32_e32 v237, v237, v241\n\t"
      "s_waitcnt vmcnt(4)\n\t"
      "v_mfma_f32_16x16x32_bf16 v[212:215], v[180:183], v[96:99], v[212:215]\n\t"
      "v_add_f32_e32 v228, v228, v248\n\t"
      "v_mfma_f32_16x16x32_bf16 v[216:219], v[176:179], v[84:87], v[216:219]\n\t"
      "v_add_f32_e32 v229, v229, v249\n\t"
      "s_add_u32 s58, s68, s44\n\t"
      "s_addc_u32 s59, s69, 0\n\t"
      "global_load_dwordx4 v[132:135], v244, s[58:59] sc1\n\t"
      "v_mfma_f32_16x16x32_bf16 v[216:219], v[180:183], v[100:103], v[216:219]\n\t"
      "v_add_f32_e32 v228, v228, v236\n\t"
      "v_add_f32_e32 v229, v229, v237\n\t"
      "v_mfma_f32_16x16x32_bf16 v[220:223], v[176:179], v[88:91], v[220:223]\n\t"
      "v_cvt_pk_bf16_f32 v232, v228, v229\n\t"
      "v_add_f32_e32 v251, v212, v213\n\t"
      "v_add_f32_e32 v251, v251, v214\n\t"
      "v_cmp_u_f32_e32 vcc, v251, v215\n\t"
      "s_cbranch_vccnz LA41R_%=\n\t"
      "LA41B_%=:\n\t"
      "v_mfma_f32_16x16x32_bf16 v[220:223], v[180:183], v[104:107], v[220:223]\n\t"
      "s_mov_b32 vcc_lo, 0xeeeeeeee\n\t"
      "s_mov_b32 vcc_hi, 0xeeeeeeee\n\t"
      "v_cndmask_b32_dpp v120, v232, v120, vcc quad_perm:[0,0,0,0] row_mask:0xf bank_mask:0xf\n\t"
      "v_cndmask_b32_dpp v121, v232, v121, vcc quad_perm:[1,1,1,1] row_mask:0xf bank_mask:0xf\n\t"
      "v_cndmask_b32_dpp v122, v232, v122, vcc quad_perm:[2,2,2,2] row_mask:0xf bank_mask:0xf\n\t"
      "v_cndmask_b32_dpp v123, v232, v123, vcc quad_perm:[3,3,3,3] row_mask:0xf bank_mask:0xf\n\t"
      "ds_write_b128 v245, v[212:215] offset:32768\n\t"
      "v_mfma_f32_16x16x32_bf16 v[224:227], v[176:179], v[92:95], v[224:227]\n\t"
      "s_add_u32 s58, s68, s45\n\t"
      "s_addc_u32 s59, s69, 0\n\t"
      "global_load_dwordx4 v[136:139], v244, s[58:59] sc1\n\t"
      "v_mfma_f32_16x16x32_bf16 v[224:227], v[180:183], v[108:111], v[224:227]\n\t"
      "ds_write_b128 v245, v[216:219] offset:33792\n\t"
      "s_add_u32 s58, s68, s46\n\t"
      "s_addc_u32 s59, s69, 0\n\t"
      "global_load_dwordx4 v[140:143], v244, s[58:59] sc1\n\t"
      "s_waitcnt vmcnt(6)\n\t"
      "v_mfma_f32_16x16x32_bf16 v[196:199], v[184:187], v[0:3], 0\n\t"
      "v_mfma_f32_16x16x32_bf16 v[200:203], v[184:187], v[4:7], 0\n\t"
      "ds_write_b128 v245, v[220:223] offset:34816\n\t"
      "v_mfma_f32_16x16x32_bf16 v[204:207], v[184:187], v[8:11], 0\n\t"
      "v_mfma_f32_16x16x32_bf16 v[208:211], v[184:187], v[12:15], 0\n\t"
      "ds_write_b128 v245, v[224:227] offset:35840\n\t"
      "s_waitcnt vmcnt(5)\n\t"
      "v_mfma_f32_16x16x32_bf16 v[196:199], v[188:191], v[16:19], v[196:199]\n\t"
      "s_waitcnt lgkmcnt(0)\n\t"
      "s_barrier\n\t"
      "ds_read_b64 v[228:229], v246 offset:32768\n\t"
      "ds_read_b64 v[230:231], v246 offset:36864\n\t"
      "v_mfma_f32_16x16x32_bf16 v[200:203], v[188:191], v[20:23], v[200:203]\n\t"
      "ds_read_b64 v[232:233], v246 offset:40960\n\t"
      "ds_read_b64 v[234:235], v246 offset:45056\n\t"
      "v_mfma_f32_16x16x32_bf16 v[204:207], v[188:191], v[24:27], v[204:207]\n\t"
      "ds_read_b64 v[236:237], v246 offset:49152\n\t"
      "ds_read_b64 v[238:239], v246 offset:53248\n\t"
      "s_add_u32 s58, s70, s40\n\t"
      "s_addc_u32 s59, s71, 0\n\t"
      "global_load_dwordx4 v[144:147], v244, s[58:59] sc1\n\t"
      "v_mfma_f32_16x16x32_bf16 v[208:211], v[188:191], v[28:31], v[208:211]\n\t"
      "ds_read_b64 v[240:241], v246 offset:57344\n\t"
      "ds_read_b64 v[242:243], v246 offset:61440\n\t"
      "v_lshlrev_b32_e32 v248, 16, v113\n\t"
      "s_waitcnt vmcnt(5)\n\t"
      "v_mfma_f32_16x16x32_bf16 v[196:199], v[192:195], v[32:35], v[196:199]\n\t"
      "v_and_b32_e32 v249, 0xffff0000, v113\n\t"
      "v_mfma_f32_16x16x32_bf16 v[200:203], v[192:195], v[36:39], v[200:203]\n\t"
      "v_add_f32_e32 v248, v254, v248\n\t"
      "v_mfma_f32_16x16x32_bf16 v[204:207], v[192:195], v[40:43], v[204:207]\n\t"
      "v_add_f32_e32 v249, v254, v249\n\t"
      "s_waitcnt lgkmcnt(4)\n\t"
      "s_add_u32 s58, s70, s41\n\t"
      "s_addc_u32 s59, s71, 0\n\t"
      "global_load_dwordx4 v[148:151], v244, s[58:59] sc1\n\t"
      "v_mfma_f32_16x16x32_bf16 v[208:211], v[192:195], v[44:47], v[208:211]\n\t"
      "v_add_f32_e32 v228, v228, v230\n\t"
      "s_waitcnt vmcnt(5)\n\t"
      "v_mfma_f32_16x16x32_bf16 v[196:199], v[128:131], v[48:51], v[196:199]\n\t"
      "v_add_f32_e32 v232, v232, v234\n\t"
      "v_mfma_f32_16x16x32_bf16 v[200:203], v[128:131], v[52:55], v[200:203]\n\t"
      "v_add_f32_e32 v229, v229, v231\n\t"
      "v_add_f32_e32 v233, v233, v235\n\t"
      "v_mfma_f32_16x16x32_bf16 v[204:207], v[128:131], v[56:59], v[204:207]\n\t"
      "s_waitcnt lgkmcnt(0)\n\t"
      "s_add_u32 s58, s70, s42\n\t"
      "s_addc_u32 s59, s71, 0\n\t"
      "global_load_dwordx4 v[152:155], v244, s[58:59] sc1\n\t"
      "v_mfma_f32_16x16x32_bf16 v[208:211], v[128:131], v[60:63], v[208:211]\n\t"
      "v_add_f32_e32 v236, v236, v238\n\t"
      "s_waitcnt vmcnt(5)\n\t"
      "v_mfma_f32_16x16x32_bf16 v[196:199], v[132:135], v[64:67], v[196:199]\n\t"
      "v_add_f32_e32 v240, v240, v242\n\t"
      "v_mfma_f32_16x16x32_bf16 v[200:203], v[132:135], v[68:71], v[200:203]\n\t"
      "v_add_f32_e32 v237, v237, v239\n\t"
      "v_add_f32_e32 v241, v241, v243\n\t"
      "v_mfma_f32_16x16x32_bf16 v[204:207], v[132:135], v[72:75], v[204:207]\n\t"
      "v_add_f32_e32 v228, v228, v232\n\t"
      "s_add_u32 s58, s70, s43\n\t"
      "s_addc_u32 s59, s71, 0\n\t"
      "global_load_dwordx4 v[156:159], v244, s[58:59] sc1\n\t"
      "v_mfma_f32_16x16x32_bf16 v[208:211], v[132:135], v[76:79], v[208:211]\n\t"
      "v_add_f32_e32 v236, v236, v240\n\t"
      "s_waitcnt vmcnt(5)\n\t"
      "v_mfma_f32_16x16x32_bf16 v[196:199], v[136:139], v[80:83], v[196:199]\n\t"
      "v_add_f32_e32 v229, v229, v233\n\t"
      "v_add_f32_e32 v237, v237, v241\n\t"
      "s_waitcnt vmcnt(4)\n\t"
      "v_mfma_f32_16x16x32_bf16 v[196:199], v[140:143], v[96:99], v[196:199]\n\t"
      "v_add_f32_e32 v228, v228, v248\n\t"
      "v_mfma_f32_16x16x32_bf16 v[200:203], v[136:139], v[84:87], v[200:203]\n\t"
      "v_add_f32_e32 v229, v229, v249\n\t"
      "s_add_u32 s58, s70, s44\n\t"
      "s_addc_u32 s59, s71, 0\n\t"
      "global_load_dwordx4 v[160:163], v244, s[58:59] sc1\n\t"
      "v_mfma_f32_16x16x32_bf16 v[200:203], v[140:143], v[100:103], v[200:203]\n\t"
      "v_add_f32_e32 v228, v228, v236\n\t"
      "v_add_f32_e32 v229, v229, v237\n\t"
      "v_mfma_f32_16x16x32_bf16 v[204:207], v[136:139], v[88:91], v[204:207]\n\t"
      "v_cvt_pk_bf16_f32 v232, v228, v229\n\t"
      "v_add_f32_e32 v251, v196, v197\n\t"
      "v_add_f32_e32 v251, v251, v198\n\t"
      "v_cmp_u_f32_e32 vcc, v251, v199\n\t"
      "s_cbranch_vccnz LA42R_%=\n\t"
      "LA42B_%=:\n\t"
      "v_mfma_f32_16x16x32_bf16 v[204:207], v[140:143], v[104:107], v[204:207]\n\t"
      "s_mov_b32 vcc_lo, 0xdddddddd\n\t"
      "s_mov_b32 vcc_hi, 0xdddddddd\n\t"
      "v_cndmask_b32_dpp v120, v232, v120, vcc quad_perm:[0,0,0,0] row_mask:0xf bank_mask:0xf\n\t"
      "v_cndmask_b32_dpp v121, v232, v121, vcc quad_perm:[1,1,1,1] row_mask:0xf bank_mask:0xf\n\t"
      "v_cndmask_b32_dpp v122, v232, v122, vcc quad_perm:[2,2,2,2] row_mask:0xf bank_mask:0xf\n\t"
      "v_cndmask_b32_dpp v123, v232, v123, vcc quad_perm:[3,3,3,3] row_mask:0xf bank_mask:0xf\n\t"
      "ds_write_b128 v245, v[196:199]\n\t"
      "v_mfma_f32_16x16x32_bf16 v[208:211], v[136:139], v[92:95], v[208:211]\n\t"
      "s_add_u32 s58, s70, s45\n\t"
      "s_addc_u32 s59, s71, 0\n\t"
      "global_load_dwordx4 v[164:167], v244, s[58:59] sc1\n\t"
      "v_mfma_f32_16x16x32_bf16 v[208:211], v[140:143], v[108:111], v[208:211]\n\t"
      "ds_write_b128 v245, v[200:203] offset:1024\n\t"
      "s_add_u32 s58, s70, s46\n\t"
      "s_addc_u32 s59, s71, 0\n\t"
      "global_load_dwordx4 v[168:171], v244, s[58:59] sc1\n\t"
      "s_waitcnt vmcnt(6)\n\t"
      "v_mfma_f32_16x16x32_bf16 v[212:215], v[144:147], v[0:3], 0\n\t"
      "v_mfma_f32_16x16x32_bf16 v[216:219], v[144:147], v[4:7], 0\n\t"
      "ds_write_b128 v245, v[204:207] offset:2048\n\t"
      "v_mfma_f32_16x16x32_bf16 v[220:223], v[144:147], v[8:11], 0\n\t"
      "v_mfma_f32_16x16x32_bf16 v[224:227], v[144:147], v[12:15], 0\n\t"
      "ds_write_b128 v245, v[208:211] offset:3072\n\t"
      "s_waitcnt vmcnt(5)\n\t"
      "v_mfma_f32_16x16x32_bf16 v[212:215], v[148:151], v[16:19], v[212:215]\n\t"
      "s_waitcnt lgkmcnt(0)\n\t"
      "s_barrier\n\t"
      "ds_read_b64 v[228:229], v246\n\t"
      "ds_read_b64 v[230:231], v246 offset:4096\n\t"
      "v_mfma_f32_16x16x32_bf16 v[216:219], v[148:151], v[20:23], v[216:219]\n\t"
      "ds_read_b64 v[232:233], v246 offset:8192\n\t"
      "ds_read_b64 v[234:235], v246 offset:12288\n\t"
      "v_mfma_f32_16x16x32_bf16 v[220:223], v[148:151], v[24:27], v[220:223]\n\t"
      "ds_read_b64 v[236:237], v246 offset:16384\n\t"
      "ds_read_b64 v[238:239], v246 offset:20480\n\t"
      "s_add_u32 s58, s72, s40\n\t"
      "s_addc_u32 s59, s73, 0\n\t"
      "global_load_dwordx4 v[172:175], v244, s[58:59] sc1\n\t"
      "v_mfma_f32_16x16x32_bf16 v[224:227], v[148:151], v[28:31], v[224:227]\n\t"
      "ds_read_b64 v[240:241], v246 offset:24576\n\t"
      "ds_read_b64 v[242:243], v246 offset:28672\n\t"
      "v_lshlrev_b32_e32 v248, 16, v114\n\t"
      "s_waitcnt vmcnt(5)\n\t"
      "v_mfma_f32_16x16x32_bf16 v[212:215], v[152:155], v[32:35], v[212:215]\n\t"
      "v_and_b32_e32 v249, 0xffff0000, v114\n\t"
      "v_mfma_f32_16x16x32_bf16 v[216:219], v[152:155], v[36:39], v[216:219]\n\t"
      "v_add_f32_e32 v248, v254, v248\n\t"
      "v_mfma_f32_16x16x32_bf16 v[220:223], v[152:155], v[40:43], v[220:223]\n\t"
      "v_add_f32_e32 v249, v254, v249\n\t"
      "s_waitcnt lgkmcnt(4)\n\t"
      "s_add_u32 s58, s72, s41\n\t"
      "s_addc_u32 s59, s73, 0\n\t"
      "global_load_dwordx4 v[176:179], v244, s[58:59] sc1\n\t"
      "v_mfma_f32_16x16x32_bf16 v[224:227], v[152:155], v[44:47], v[224:227]\n\t"
      "v_add_f32_e32 v228, v228, v230\n\t"
      "s_waitcnt vmcnt(5)\n\t"
      "v_mfma_f32_16x16x32_bf16 v[212:215], v[156:159], v[48:51], v[212:215]\n\t"
      "v_add_f32_e32 v232, v232, v234\n\t"
      "v_mfma_f32_16x16x32_bf16 v[216:219], v[156:159], v[52:55], v[216:219]\n\t"
      "v_add_f32_e32 v229, v229, v231\n\t"
      "v_add_f32_e32 v233, v233, v235\n\t"
      "v_mfma_f32_16x16x32_bf16 v[220:223], v[156:159], v[56:59], v[220:223]\n\t"
      "s_waitcnt lgkmcnt(0)\n\t"
      "s_add_u32 s58, s72, s42\n\t"
      "s_addc_u32 s59, s73, 0\n\t"
      "global_load_dwordx4 v[180:183], v244, s[58:59] sc1\n\t"
      "v_mfma_f32_16x16x32_bf16 v[224:227], v[156:159], v[60:63], v[224:227]\n\t"
      "v_add_f32_e32 v236, v236, v238\n\t"
      "s_waitcnt vmcnt(5)\n\t"
      "v_mfma_f32_16x16x32_bf16 v[212:215], v[160:163], v[64:67], v[212:215]\n\t"
      "v_add_f32_e32 v240, v240, v242\n\t"
      "v_mfma_f32_16x16x32_bf16 v[216:219], v[160:163], v[68:71], v[216:219]\n\t"
      "v_add_f32_e32 v237, v237, v239\n\t"
      "v_add_f32_e32 v241, v241, v243\n\t"
      "v_mfma_f32_16x16x32_bf16 v[220:223], v[160:163], v[72:75], v[220:223]\n\t"
      "v_add_f32_e32 v228, v228, v232\n\t"
      "s_add_u32 s58, s72, s43\n\t"
      "s_addc_u32 s59, s73, 0\n\t"
      "global_load_dwordx4 v[184:187], v244, s[58:59] sc1\n\t"
      "v_mfma_f32_16x16x32_bf16 v[224:227], v[160:163], v[76:79], v[224:227]\n\t"
      "v_add_f32_e32 v236, v236, v240\n\t"
      "s_waitcnt vmcnt(5)\n\t"
      "v_mfma_f32_16x16x32_bf16 v[212:215], v[164:167], v[80:83], v[212:215]\n\t"
      "v_add_f32_e32 v229, v229, v233\n\t"
      "v_add_f32_e32 v237, v237, v241\n\t"
      "s_waitcnt vmcnt(4)\n\t"
      "v_mfma_f32_16x16x32_bf16 v[212:215], v[168:171], v[96:99], v[212:215]\n\t"
      "v_add_f32_e32 v228, v228, v248\n\t"
      "v_mfma_f32_16x16x32_bf16 v[216:219], v[164:167], v[84:87], v[216:219]\n\t"
      "v_add_f32_e32 v229, v229, v249\n\t"
      "s_add_u32 s58, s72, s44\n\t"
      "s_addc_u32 s59, s73, 0\n\t"
      "global_load_dwordx4 v[188:191], v244, s[58:59] sc1\n\t"
      "v_mfma_f32_16x16x32_bf16 v[216:219], v[168:171], v[100:103], v[216:219]\n\t"
      "v_add_f32_e32 v228, v228, v236\n\t"
      "v_add_f32_e32 v229, v229, v237\n\t"
      "v_mfma_f32_16x16x32_bf16 v[220:223], v[164:167], v[88:91], v[220:223]\n\t"
      "v_cvt_pk_bf16_f32 v232, v228, v229\n\t"
      "v_add_f32_e32 v251, v212, v213\n\t"
      "v_add_f32_e32 v251, v251, v214\n\t"
      "v_cmp_u_f32_e32 vcc, v251, v215\n\t"
      "s_cbranch_vccnz LA43R_%=\n\t"
      "LA43B_%=:\n\t"
      "v_mfma_f32_16x16x32_bf16 v[220:223], v[168:171], v[104:107], v[220:223]\n\t"
      "s_mov_b32 vcc_lo, 0xbbbbbbbb\n\t"
      "s_mov_b32 vcc_hi, 0xbbbbbbbb\n\t"
      "v_cndmask_b32_dpp v120, v232, v120, vcc quad_perm:[0,0,0,0] row_mask:0xf bank_mask:0xf\n\t"
      "v_cndmask_b32_dpp v121, v232, v121, vcc quad_perm:[1,1,1,1] row_mask:0xf bank_mask:0xf\n\t"
      "v_cndmask_b32_dpp v122, v232, v122, vcc quad_perm:[2,2,2,2] row_mask:0xf bank_mask:0xf\n\t"
      "v_cndmask_b32_dpp v123, v232, v123, vcc quad_perm:[3,3,3,3] row_mask:0xf bank_mask:0xf\n\t"
      "ds_write_b128 v245, v[212:215] offset:32768\n\t"
      "v_mfma_f32_16x16x32_bf16 v[224:227], v[164:167], v[92:95], v[224:227]\n\t"
      "s_add_u32 s58, s72, s45\n\t"
      "s_addc_u32 s59, s73, 0\n\t"
      "global_load_dwordx4 v[192:195], v244, s[58:59] sc1\n\t"
      "v_mfma_f32_16x16x32_bf16 v[224:227], v[168:171], v[108:111], v[224:227]\n\t"
      "ds_write_b128 v245, v[216:219] offset:33792\n\t"
      "s_add_u32 s58, s72, s46\n\t"
      "s_addc_u32 s59, s73, 0\n\t"
      "global_load_dwordx4 v[128:131], v244, s[58:59] sc1\n\t"
      "s_waitcnt vmcnt(6)\n\t"
      "v_mfma_f32_16x16x32_bf16 v[196:199], v[172:175], v[0:3], 0\n\t"
      "v_mfma_f32_16x16x32_bf16 v[200:203], v[172:175], v[4:7], 0\n\t"
      "ds_write_b128 v245, v[220:223] offset:34816\n\t"
      "v_mfma_f32_16x16x32_bf16 v[204:207], v[172:175], v[8:11], 0\n\t"
      "v_mfma_f32_16x16x32_bf16 v[208:211], v[172:175], v[12:15], 0\n\t"
      "ds_write_b128 v245, v[224:227] offset:35840\n\t"
      "s_waitcnt vmcnt(5)\n\t"
      "v_mfma_f32_16x16x32_bf16 v[196:199], v[176:179], v[16:19], v[196:199]\n\t"
      "s_waitcnt lgkmcnt(0)\n\t"
      "s_barrier\n\t"
      "ds_read_b64 v[228:229], v246 offset:32768\n\t"
      "ds_read_b64 v[230:231], v246 offset:36864\n\t"
      "v_mfma_f32_16x16x32_bf16 v[200:203], v[176:179], v[20:23], v[200:203]\n\t"
      "ds_read_b64 v[232:233], v246 offset:40960\n\t"
      "ds_read_b64 v[234:235], v246 offset:45056\n\t"
      "v_mfma_f32_16x16x32_bf16 v[204:207], v[176:179], v[24:27], v[204:207]\n\t"
      "ds_read_b64 v[236:237], v246 offset:49152\n\t"
      "ds_read_b64 v[238:239], v246 offset:53248\n\t"
      "s_add_u32 s58, s74, s40\n\t"
      "s_addc_u32 s59, s75, 0\n\t"
      "global_load_dwordx4 v[132:135], v244, s[58:59] sc1\n\t"
      "v_mfma_f32_16x16x32_bf16 v[208:211], v[176:179], v[28:31], v[208:211]\n\t"
      "ds_read_b64 v[240:241], v246 offset:57344\n\t"
      "ds_read_b64 v[242:243], v246 offset:61440\n\t"
      "v_lshlrev_b32_e32 v248, 16, v115\n\t"
      "s_waitcnt vmcnt(5)\n\t"
      "v_mfma_f32_16x16x32_bf16 v[196:199], v[180:183], v[32:35], v[196:199]\n\t"
      "v_and_b32_e32 v249, 0xffff0000, v115\n\t"
      "v_mfma_f32_16x16x32_bf16 v[200:203], v[180:183], v[36:39], v[200:203]\n\t"
      "v_add_f32_e32 v248, v254, v248\n\t"
      "v_mfma_f32_16x16x32_bf16 v[204:207], v[180:183], v[40:43], v[204:207]\n\t"
      "v_add_f32_e32 v249, v254, v249\n\t"
      "s_waitcnt lgkmcnt(4)\n\t"
      "s_add_u32 s58, s74, s41\n\t"
      "s_addc_u32 s59, s75, 0\n\t"
      "global_load_dwordx4 v[136:139], v244, s[58:59] sc1\n\t"
      "v_mfma_f32_16x16x32_bf16 v[208:211], v[180:183], v[44:47], v[208:211]\n\t"
      "v_add_f32_e32 v228, v228, v230\n\t"
      "s_waitcnt vmcnt(5)\n\t"
      "v_mfma_f32_16x16x32_bf16 v[196:199], v[184:187], v[48:51], v[196:199]\n\t"
      "v_add_f32_e32 v232, v232, v234\n\t"
      "v_mfma_f32_16x16x32_bf16 v[200:203], v[184:187], v[52:55], v[200:203]\n\t"
      "v_add_f32_e32 v229, v229, v231\n\t"
      "v_add_f32_e32 v233, v233, v235\n\t"
      "v_mfma_f32_16x16x32_bf16 v[204:207], v[184:187], v[56:59], v[204:207]\n\t"
      "s_waitcnt lgkmcnt(0)\n\t"
      "s_add_u32 s58, s74, s42\n\t"
      "s_addc_u32 s59, s75, 0\n\t"
      "global_load_dwordx4 v[140:143], v244, s[58:59] sc1\n\t"
      "v_mfma_f32_16x16x32_bf16 v[208:211], v[184:187], v[60:63], v[208:211]\n\t"
      "v_add_f32_e32 v236, v236, v238\n\t"
      "s_waitcnt vmcnt(5)\n\t"
      "v_mfma_f32_16x16x32_bf16 v[196:199], v[188:191], v[64:67], v[196:199]\n\t"
      "v_add_f32_e32 v240, v240, v242\n\t"
      "v_mfma_f32_16x16x32_bf16 v[200:203], v[188:191], v[68:71], v[200:203]\n\t"
      "v_add_f32_e32 v237, v237, v239\n\t"
      "v_add_f32_e32 v241, v241, v243\n\t"
      "v_mfma_f32_16x16x32_bf16 v[204:207], v[188:191], v[72:75], v[204:207]\n\t"
      "v_add_f32_e32 v228, v228, v232\n\t"
      "s_add_u32 s58, s74, s43\n\t"
      "s_addc_u32 s59, s75, 0\n\t"
      "global_load_dwordx4 v[144:147], v244, s[58:59] sc1\n\t"
      "v_mfma_f32_16x16x32_bf16 v[208:211], v[188:191], v[76:79], v[208:211]\n\t"
      "v_add_f32_e32 v236, v236, v240\n\t"
      "s_waitcnt vmcnt(5)\n\t"
      "v_mfma_f32_16x16x32_bf16 v[196:199], v[192:195], v[80:83], v[196:199]\n\t"
      "v_add_f32_e32 v229, v229, v233\n\t"
      "v_add_f32_e32 v237, v237, v241\n\t"
      "s_waitcnt vmcnt(4)\n\t"
      "v_mfma_f32_16x16x32_bf16 v[196:199], v[128:131], v[96:99], v[196:199]\n\t"
      "v_add_f32_e32 v228, v228, v248\n\t"
      "v_mfma_f32_16x16x32_bf16 v[200:203], v[192:195], v[84:87], v[200:203]\n\t"
      "v_add_f32_e32 v229, v229, v249\n\t"
      "s_add_u32 s58, s74, s44\n\t"
      "s_addc_u32 s59, s75, 0\n\t"
      "global_load_dwordx4 v[148:151], v244, s[58:59] sc1\n\t"
      "v_mfma_f32_16x16x32_bf16 v[200:203], v[128:131], v[100:103], v[200:203]\n\t"
      "v_add_f32_e32 v228, v228, v236\n\t"
      "v_add_f32_e32 v229, v229, v237\n\t"
      "v_mfma_f32_16x16x32_bf16 v[204:207], v[192:195], v[88:91], v[204:207]\n\t"
      "v_cvt_pk_bf16_f32 v232, v228, v229\n\t"
      "v_add_f32_e32 v251, v196, v197\n\t"
      "v_add_f32_e32 v251, v251, v198\n\t"
      "v_cmp_u_f32_e32 vcc, v251, v199\n\t"
      "s_cbranch_vccnz LA44R_%=\n\t"
      "LA44B_%=:\n\t"
      "v_mfma_f32_16x16x32_bf16 v[204:207], v[128:131], v[104:107], v[204:207]\n\t"
      "s_mov_b32 vcc_lo, 0x77777777\n\t"
      "s_mov_b32 vcc_hi, 0x77777777\n\t"
      "v_cndmask_b32_dpp v120, v232, v120, vcc quad_perm:[0,0,0,0] row_mask:0xf bank_mask:0xf\n\t"
      "v_cndmask_b32_dpp v121, v232, v121, vcc quad_perm:[1,1,1,1] row_mask:0xf bank_mask:0xf\n\t"
      "v_cndmask_b32_dpp v122, v232, v122, vcc quad_perm:[2,2,2,2] row_mask:0xf bank_mask:0xf\n\t"
      "v_cndmask_b32_dpp v123, v232, v123, vcc quad_perm:[3,3,3,3] row_mask:0xf bank_mask:0xf\n\t"
      "ds_write_b128 v245, v[196:199]\n\t"
      "v_mfma_f32_16x16x32_bf16 v[208:211], v[192:195], v[92:95], v[208:211]\n\t"
      "s_add_u32 s58, s74, s45\n\t"
      "s_addc_u32 s59, s75, 0\n\t"
      "global_load_dwordx4 v[152:155], v244, s[58:59] sc1\n\t"
      "v_mfma_f32_16x16x32_bf16 v[208:211], v[128:131], v[108:111], v[208:211]\n\t"
      "ds_write_b128 v245, v[200:203] offset:1024\n\t"
      "s_add_u32 s58, s74, s46\n\t"
      "s_addc_u32 s59, s75, 0\n\t"
      "global_load_dwordx4 v[156:159], v244, s[58:59] sc1\n\t"
      "s_waitcnt vmcnt(6)\n\t"
      "v_mfma_f32_16x16x32_bf16 v[212:215], v[132:135], v[0:3], 0\n\t"
      "v_mfma_f32_16x16x32_bf16 v[216:219], v[132:135], v[4:7], 0\n\t"
      "ds_write_b128 v245, v[204:207] offset:2048\n\t"
      "v_mfma_f32_16x16x32_bf16 v[220:223], v[132:135], v[8:11], 0\n\t"
      "v_mfma_f32_16x16x32_bf16 v[224:227], v[132:135], v[12:15], 0\n\t"
      "ds_write_b128 v245, v[208:211] offset:3072\n\t"
      "s_waitcnt vmcnt(5)\n\t"
      "v_mfma_f32_16x16x32_bf16 v[212:215], v[136:139], v[16:19], v[212:215]\n\t"
      "s_waitcnt lgkmcnt(0)\n\t"
      "s_barrier\n\t"
      "ds_read_b64 v[228:229], v246\n\t"
      "ds_read_b64 v[230:231], v246 offset:4096\n\t"
      "v_mfma_f32_16x16x32_bf16 v[216:219], v[136:139], v[20:23], v[216:219]\n\t"
      "ds_read_b64 v[232:233], v246 offset:8192\n\t"
      "ds_read_b64 v[234:235], v246 offset:12288\n\t"
      "v_mfma_f32_16x16x32_bf16 v[220:223], v[136:139], v[24:27], v[220:223]\n\t"
      "ds_read_b64 v[236:237], v246 offset:16384\n\t"
      "ds_read_b64 v[238:239], v246 offset:20480\n\t"
      "s_add_u32 s58, s76, s40\n\t"
      "s_addc_u32 s59, s77, 0\n\t"
      "global_load_dwordx4 v[160:163], v244, s[58:59] sc1\n\t"
      "v_mfma_f32_16x16x32_bf16 v[224:227], v[136:139], v[28:31], v[224:227]\n\t"
      "ds_read_b64 v[240:241], v246 offset:24576\n\t"
      "ds_read_b64 v[242:243], v246 offset:28672\n\t"
      "v_lshlrev_b32_e32 v248, 16, v116\n\t"
      "s_waitcnt vmcnt(5)\n\t"
      "v_mfma_f32_16x16x32_bf16 v[212:215], v[140:143], v[32:35], v[212:215]\n\t"
      "v_and_b32_e32 v249, 0xffff0000, v116\n\t"
      "v_mfma_f32_16x16x32_bf16 v[216:219], v[140:143], v[36:39], v[216:219]\n\t"
      "v_add_f32_e32 v248, v254, v248\n\t"
      "v_mfma_f32_16x16x32_bf16 v[220:223], v[140:143], v[40:43], v[220:223]\n\t"
      "v_add_f32_e32 v249, v254, v249\n\t"
      "s_waitcnt lgkmcnt(4)\n\t"
      "s_add_u32 s58, s76, s41\n\t"
      "s_addc_u32 s59, s77, 0\n\t"
      "global_load_dwordx4 v[164:167], v244, s[58:59] sc1\n\t"
      "v_mfma_f32_16x16x32_bf16 v[224:227], v[140:143], v[44:47], v[224:227]\n\t"
      "v_add_f32_e32 v228, v228, v230\n\t"
      "s_waitcnt vmcnt(5)\n\t"
      "v_mfma_f32_16x16x32_bf16 v[212:215], v[144:147], v[48:51], v[212:215]\n\t"
      "v_add_f32_e32 v232, v232, v234\n\t"
      "v_mfma_f32_16x16x32_bf16 v[216:219], v[144:147], v[52:55], v[216:219]\n\t"
      "v_add_f32_e32 v229, v229, v231\n\t"
      "v_add_f32_e32 v233, v233, v235\n\t"
      "v_mfma_f32_16x16x32_bf16 v[220:223], v[144:147], v[56:59], v[220:223]\n\t"
      "s_waitcnt lgkmcnt(0)\n\t"
      "s_add_u32 s58, s76, s42\n\t"
      "s_addc_u32 s59, s77, 0\n\t"
      "global_load_dwordx4 v[168:171], v244, s[58:59] sc1\n\t"
      "v_mfma_f32_16x16x32_bf16 v[224:227], v[144:147], v[60:63], v[224:227]\n\t"
      "v_add_f32_e32 v236, v236, v238\n\t"
      "s_waitcnt vmcnt(5)\n\t"
      "v_mfma_f32_16x16x32_bf16 v[212:215], v[148:151], v[64:67], v[212:215]\n\t"
      "v_add_f32_e32 v240, v240, v242\n\t"
      "v_mfma_f32_16x16x32_bf16 v[216:219], v[148:151], v[68:71], v[216:219]\n\t"
      "v_add_f32_e32 v237, v237, v239\n\t"
      "v_add_f32_e32 v241, v241, v243\n\t"
      "v_mfma_f32_16x16x32_bf16 v[220:223], v[148:151], v[72:75], v[220:223]\n\t"
      "v_add_f32_e32 v228, v228, v232\n\t"
      "s_add_u32 s58, s76, s43\n\t"
      "s_addc_u32 s59, s77, 0\n\t"
      "global_load_dwordx4 v[172:175], v244, s[58:59] sc1\n\t"
      "v_mfma_f32_16x16x32_bf16 v[224:227], v[148:151], v[76:79], v[224:227]\n\t"
      "v_add_f32_e32 v236, v236, v240\n\t"
      "s_waitcnt vmcnt(5)\n\t"
      "v_mfma_f32_16x16x32_bf16 v[212:215], v[152:155], v[80:83], v[212:215]\n\t"
      "v_add_f32_e32 v229, v229, v233\n\t"
      "v_add_f32_e32 v237, v237, v241\n\t"
      "s_waitcnt vmcnt(4)\n\t"
      "v_mfma_f32_16x16x32_bf16 v[212:215], v[156:159], v[96:99], v[212:215]\n\t"
      "v_add_f32_e32 v228, v228, v248\n\t"
      "v_mfma_f32_16x16x32_bf16 v[216:219], v[152:155], v[84:87], v[216:219]\n\t"
      "v_add_f32_e32 v229, v229, v249\n\t"
      "s_add_u32 s58, s76, s44\n\t"
      "s_addc_u32 s59, s77, 0\n\t"
      "global_load_dwordx4 v[176:179], v244, s[58:59] sc1\n\t"
      "v_mfma_f32_16x16x32_bf16 v[216:219], v[156:159], v[100:103], v[216:219]\n\t"
      "v_add_f32_e32 v228, v228, v236\n\t"
      "v_add_f32_e32 v229, v229, v237\n\t"
      "v_mfma_f32_16x16x32_bf16 v[220:223], v[152:155], v[88:91], v[220:223]\n\t"
      "v_cvt_pk_bf16_f32 v232, v228, v229\n\t"
      "v_add_f32_e32 v251, v212, v213\n\t"
      "v_add_f32_e32 v251, v251, v214\n\t"
      "v_cmp_u_f32_e32 vcc, v251, v215\n\t"
      "s_cbranch_vccnz LA45R_%=\n\t"
      "LA45B_%=:\n\t"
      "v_mfma_f32_16x16x32_bf16 v[220:223], v[156:159], v[104:107], v[220:223]\n\t"
      "s_mov_b32 vcc_lo, 0xeeeeeeee\n\t"
      "s_mov_b32 vcc_hi, 0xeeeeeeee\n\t"
      "v_cndmask_b32_dpp v124, v232, v124, vcc quad_perm:[0,0,0,0] row_mask:0xf bank_mask:0xf\n\t"
      "v_cndmask_b32_dpp v125, v232, v125, vcc quad_perm:[1,1,1,1] row_mask:0xf bank_mask:0xf\n\t"
      "v_cndmask_b32_dpp v126, v232, v126, vcc quad_perm:[2,2,2,2] row_mask:0xf bank_mask:0xf\n\t"
      "v_cndmask_b32_dpp v127, v232, v127, vcc quad_perm:[3,3,3,3] row_mask:0xf bank_mask:0xf\n\t"
      "ds_write_b128 v245, v[212:215] offset:32768\n\t"
      "v_mfma_f32_16x16x32_bf16 v[224:227], v[152:155], v[92:95], v[224:227]\n\t"
      "s_add_u32 s58, s76, s45\n\t"
      "s_addc_u32 s59, s77, 0\n\t"
      "global_load_dwordx4 v[180:183], v244, s[58:59] sc1\n\t"
      "v_mfma_f32_16x16x32_bf16 v[224:227], v[156:159], v[108:111], v[224:227]\n\t"
      "ds_write_b128 v245, v[216:219] offset:33792\n\t"
      "s_add_u32 s58, s76, s46\n\t"
      "s_addc_u32 s59, s77, 0\n\t"
      "global_load_dwordx4 v[184:187], v244, s[58:59] sc1\n\t"
      "s_waitcnt vmcnt(6)\n\t"
      "v_mfma_f32_16x16x32_bf16 v[196:199], v[160:163], v[0:3], 0\n\t"
      "v_mfma_f32_16x16x32_bf16 v[200:203], v[160:163], v[4:7], 0\n\t"
      "ds_write_b128 v245, v[220:223] offset:34816\n\t"
      "v_mfma_f32_16x16x32_bf16 v[204:207], v[160:163], v[8:11], 0\n\t"
      "v_mfma_f32_16x16x32_bf16 v[208:211], v[160:163], v[12:15], 0\n\t"
      "ds_write_b128 v245, v[224:227] offset:35840\n\t"
      "s_waitcnt vmcnt(5)\n\t"
      "v_mfma_f32_16x16x32_bf16 v[196:199], v[164:167], v[16:19], v[196:199]\n\t"
      "s_waitcnt lgkmcnt(0)\n\t"
      "s_barrier\n\t"
      "ds_read_b64 v[228:229], v246 offset:32768\n\t"
      "ds_read_b64 v[230:231], v246 offset:36864\n\t"
      "v_mfma_f32_16x16x32_bf16 v[200:203], v[164:167], v[20:23], v[200:203]\n\t"
      "ds_read_b64 v[232:233], v246 offset:40960\n\t"
      "ds_read_b64 v[234:235], v246 offset:45056\n\t"
      "v_mfma_f32_16x16x32_bf16 v[204:207], v[164:167], v[24:27], v[204:207]\n\t"
      "ds_read_b64 v[236:237], v246 offset:49152\n\t"
      "ds_read_b64 v[238:239], v246 offset:53248\n\t"
      "v_mfma_f32_16x16x32_bf16 v[208:211], v[164:167], v[28:31], v[208:211]\n\t"
      "ds_read_b64 v[240:241], v246 offset:57344\n\t"
      "ds_read_b64 v[242:243], v246 offset:61440\n\t"
      "v_lshlrev_b32_e32 v248, 16, v117\n\t"
      "v_and_b32_e32 v249, 0xffff0000, v117\n\t"
      "s_waitcnt vmcnt(4)\n\t"
      "v_mfma_f32_16x16x32_bf16 v[196:199], v[168:171], v[32:35], v[196:199]\n\t"
      "v_add_f32_e32 v248, v254, v248\n\t"
      "v_add_f32_e32 v249, v254, v249\n\t"
      "v_mfma_f32_16x16x32_bf16 v[200:203], v[168:171], v[36:39], v[200:203]\n\t"
      "s_waitcnt lgkmcnt(4)\n\t"
      "v_add_f32_e32 v228, v228, v230\n\t"
      "v_add_f32_e32 v232, v232, v234\n\t"
      "v_mfma_f32_16x16x32_bf16 v[204:207], v[168:171], v[40:43], v[204:207]\n\t"
      "v_add_f32_e32 v229, v229, v231\n\t"
      "v_add_f32_e32 v233, v233, v235\n\t"
      "v_mfma_f32_16x16x32_bf16 v[208:211], v[168:171], v[44:47], v[208:211]\n\t"
      "s_waitcnt lgkmcnt(0)\n\t"
      "v_add_f32_e32 v236, v236, v238\n\t"
      "v_add_f32_e32 v240, v240, v242\n\t"
      "s_waitcnt vmcnt(3)\n\t"
      "v_mfma_f32_16x16x32_bf16 v[196:199], v[172:175], v[48:51], v[196:199]\n\t"
      "v_add_f32_e32 v237, v237, v239\n\t"
      "v_add_f32_e32 v241, v241, v243\n\t"
      "v_mfma_f32_16x16x32_bf16 v[200:203], v[172:175], v[52:55], v[200:203]\n\t"
      "v_add_f32_e32 v228, v228, v232\n\t"
      "v_add_f32_e32 v236, v236, v240\n\t"
      "v_add_f32_e32 v229, v229, v233\n\t"
      "v_mfma_f32_16x16x32_bf16 v[204:207], v[172:175], v[56:59], v[204:207]\n\t"
      "v_add_f32_e32 v237, v237, v241\n\t"
      "v_add_f32_e32 v228, v228, v248\n\t"
      "v_mfma_f32_16x16x32_bf16 v[208:211], v[172:175], v[60:63], v[208:211]\n\t"
      "v_add_f32_e32 v229, v229, v249\n\t"
      "v_add_f32_e32 v228, v228, v236\n\t"
      "v_add_f32_e32 v229, v229, v237\n\t"
      "s_waitcnt vmcnt(2)\n\t"
      "v_mfma_f32_16x16x32_bf16 v[196:199], v[176:179], v[64:67], v[196:199]\n\t"
      "v_cvt_pk_bf16_f32 v232, v228, v229\n\t"
      "s_mov_b32 vcc_lo, 0xdddddddd\n\t"
      "s_mov_b32 vcc_hi, 0xdddddddd\n\t"
      "v_cndmask_b32_dpp v124, v232, v124, vcc quad_perm:[0,0,0,0] row_mask:0xf bank_mask:0xf\n\t"
      "v_cndmask_b32_dpp v125, v232, v125, vcc quad_perm:[1,1,1,1] row_mask:0xf bank_mask:0xf\n\t"
      "v_cndmask_b32_dpp v126, v232, v126, vcc quad_perm:[2,2,2,2] row_mask:0xf bank_mask:0xf\n\t"
      "v_cndmask_b32_dpp v127, v232, v127, vcc quad_perm:[3,3,3,3] row_mask:0xf bank_mask:0xf\n\t"
      "v_mfma_f32_16x16x32_bf16 v[200:203], v[176:179], v[68:71], v[200:203]\n\t"
      "v_mfma_f32_16x16x32_bf16 v[204:207], v[176:179], v[72:75], v[204:207]\n\t"
      "v_mfma_f32_16x16x32_bf16 v[208:211], v[176:179], v[76:79], v[208:211]\n\t"
      "s_waitcnt vmcnt(1)\n\t"
      "v_mfma_f32_16x16x32_bf16 v[196:199], v[180:183], v[80:83], v[196:199]\n\t"
      "s_waitcnt vmcnt(0)\n\t"
      "v_mfma_f32_16x16x32_bf16 v[196:199], v[184:187], v[96:99], v[196:199]\n\t"
      "v_mfma_f32_16x16x32_bf16 v[200:203], v[180:183], v[84:87], v[200:203]\n\t"
      "v_mfma_f32_16x16x32_bf16 v[200:203], v[184:187], v[100:103], v[200:203]\n\t"
      "v_mfma_f32_16x16x32_bf16 v[204:207], v[180:183], v[88:91], v[204:207]\n\t"
      "s_nop 4\n\t"
      "v_add_f32_e32 v251, v196, v197\n\t"
      "v_add_f32_e32 v251, v251, v198\n\t"
      "v_cmp_u_f32_e32 vcc, v251, v199\n\t"
      "s_cbranch_vccnz LA46R_%=\n\t"
      "LA46B_%=:\n\t"
      "v_mfma_f32_16x16x32_bf16 v[204:207], v[184:187], v[104:107], v[204:207]\n\t"
      "ds_write_b128 v245, v[196:199]\n\t"
      "v_mfma_f32_16x16x32_bf16 v[208:211], v[180:183], v[92:95], v[208:211]\n\t"
      "v_mfma_f32_16x16x32_bf16 v[208:211], v[184:187], v[108:111], v[208:211]\n\t"
      "ds_write_b128 v245, v[200:203] offset:1024\n\t"
      "global_store_dwordx4 v247, v[120:123], s[62:63] sc1\n\t"
      "s_nop 2\n\t"
      "ds_write_b128 v245, v[204:207] offset:2048\n\t"
      "s_nop 1\n\t"
      "ds_write_b128 v245, v[208:211] offset:3072\n\t"
      "s_waitcnt lgkmcnt(0)\n\t"
      "s_barrier\n\t"
      "ds_read_b64 v[228:229], v246\n\t"
      "ds_read_b64 v[230:231], v246 offset:4096\n\t"
      "ds_read_b64 v[232:233], v246 offset:8192\n\t"
      "ds_read_b64 v[234:235], v246 offset:12288\n\t"
      "ds_read_b64 v[236:237], v246 offset:16384\n\t"
      "ds_read_b64 v[238:239], v246 offset:20480\n\t"
      "ds_read_b64 v[240:241], v246 offset:24576\n\t"
      "ds_read_b64 v[242:243], v246 offset:28672\n\t"
      "v_lshlrev_b32_e32 v248, 16, v118\n\t"
      "v_and_b32_e32 v249, 0xffff0000, v118\n\t"
      "v_add_f32_e32 v248, v254, v248\n\t"
      "v_add_f32_e32 v249, v254, v249\n\t"
      "s_waitcnt lgkmcnt(4)\n\t"
      "v_add_f32_e32 v228, v228, v230\n\t"
      "v_add_f32_e32 v232, v232, v234\n\t"
      "v_add_f32_e32 v229, v229, v231\n\t"
      "v_add_f32_e32 v233, v233, v235\n\t"
      "s_waitcnt lgkmcnt(0)\n\t"
      "v_add_f32_e32 v236, v236, v238\n\t"
      "v_add_f32_e32 v240, v240, v242\n\t"
      "v_add_f32_e32 v237, v237, v239\n\t"
      "v_add_f32_e32 v241, v241, v243\n\t"
      "v_add_f32_e32 v228, v228, v232\n\t"
      "v_add_f32_e32 v236, v236, v240\n\t"
      "v_add_f32_e32 v229, v229, v233\n\t"
      "v_add_f32_e32 v237, v237, v241\n\t"
      "v_add_f32_e32 v228, v228, v248\n\t"
      "v_add_f32_e32 v229, v229, v249\n\t"
      "v_add_f32_e32 v228, v228, v236\n\t"
      "v_add_f32_e32 v229, v229, v237\n\t"
      "v_cvt_pk_bf16_f32 v232, v228, v229\n\t"
      "s_mov_b32 vcc_lo, 0xbbbbbbbb\n\t"
      "s_mov_b32 vcc_hi, 0xbbbbbbbb\n\t"
      "v_cndmask_b32_dpp v124, v232, v124, vcc quad_perm:[0,0,0,0] row_mask:0xf bank_mask:0xf\n\t"
      "v_cndmask_b32_dpp v125, v232, v125, vcc quad_perm:[1,1,1,1] row_mask:0xf bank_mask:0xf\n\t"
      "v_cndmask_b32_dpp v126, v232, v126, vcc quad_perm:[2,2,2,2] row_mask:0xf bank_mask:0xf\n\t"
      "v_cndmask_b32_dpp v127, v232, v127, vcc quad_perm:[3,3,3,3] row_mask:0xf bank_mask:0xf\n\t"
      "s_mov_b64 exec, s[82:83]\n\t"
      "global_store_dwordx4 v247, v[124:127], s[62:63] offset:128 sc1\n\t"
      "s_mov_b64 exec, -1\n\t"
      "s_endpgm\n\t"
      "LO0R_%=:\n\t"
      "s_mov_b32 s80, 0\n\t"
      "LO0L_%=:\n\t"
      "s_add_u32 s80, s80, 1\n\t"
      "s_cmp_lt_u32 s80, 262144\n\t"
      "s_cbranch_scc0 LO0X_%=\n\t"
      "s_waitcnt vmcnt(0)\n\t"
      "s_sleep 1\n\t"
      "global_load_dwordx4 v[128:131], v244, s[42:43] offset:-4096 sc1\n\t"
      "global_load_dwordx4 v[132:135], v244, s[42:43] offset:-3072 sc1\n\t"
      "global_load_dwordx4 v[136:139], v244, s[42:43] offset:-2048 sc1\n\t"
      "global_load_dwordx4 v[140:143], v244, s[42:43] offset:-1024 sc1\n\t"
      "global_load_dwordx4 v[144:147], v244, s[42:43] sc1\n\t"
      "global_load_dwordx4 v[148:151], v244, s[42:43] offset:1024 sc1\n\t"
      "global_load_dwordx4 v[152:155], v244, s[42:43] offset:2048 sc1\n\t"
      "global_load_dwordx4 v[156:159], v244, s[42:43] offset:3072 sc1\n\t"
      "global_load_dwordx4 v[160:163], v244, s[44:45] offset:-4096 sc1\n\t"
      "global_load_dwordx4 v[164:167], v244, s[44:45] offset:-3072 sc1\n\t"
      "global_load_dwordx4 v[168:171], v244, s[44:45] offset:-2048 sc1\n\t"
      "global_load_dwordx4 v[172:175], v244, s[44:45] offset:-1024 sc1\n\t"
      "global_load_dwordx4 v[176:179], v244, s[44:45] sc1\n\t"
      "global_load_dwordx4 v[180:183], v244, s[44:45] offset:1024 sc1\n\t"
      "global_load_dwordx4 v[184:187], v244, s[44:45] offset:2048 sc1\n\t"
      "global_load_dwordx4 v[188:191], v244, s[44:45] offset:3072 sc1\n\t"
      "s_waitcnt vmcnt(0)\n\t"
      "v_max3_u32 v232, v128, v129, v130\n\t"
      "v_max3_u32 v232, v232, v131, v132\n\t"
      "v_max3_u32 v232, v232, v133, v134\n\t"
      "v_max_u32_e32 v232, v232, v135\n\t"
      "v_cmp_eq_u32_e32 vcc, -1, v232\n\t"
      "s_cbranch_vccnz LO0L_%=\n\t"
      "LO0X_%=:\n\t"
      "s_branch LO0B_%=\n\t"
      "LO2R_%=:\n\t"
      "s_mov_b32 s80, 0\n\t"
      "LO2L_%=:\n\t"
      "s_add_u32 s80, s80, 1\n\t"
      "s_cmp_lt_u32 s80, 262144\n\t"
      "s_cbranch_scc0 LO2X_%=\n\t"
      "s_waitcnt vmcnt(0)\n\t"
      "s_sleep 1\n\t"
      "global_load_dwordx4 v[136:139], v244, s[42:43] offset:-2048 sc1\n\t"
      "global_load_dwordx4 v[140:143], v244, s[42:43] offset:-1024 sc1\n\t"
      "global_load_dwordx4 v[144:147], v244, s[42:43] sc1\n\t"
      "global_load_dwordx4 v[148:151], v244, s[42:43] offset:1024 sc1\n\t"
      "global_load_dwordx4 v[152:155], v244, s[42:43] offset:2048 sc1\n\t"
      "global_load_dwordx4 v[156:159], v244, s[42:43] offset:3072 sc1\n\t"
      "global_load_dwordx4 v[160:163], v244, s[44:45] offset:-4096 sc1\n\t"
      "global_load_dwordx4 v[164:167], v244, s[44:45] offset:-3072 sc1\n\t"
      "global_load_dwordx4 v[168:171], v244, s[44:45] offset:-2048 sc1\n\t"
      "global_load_dwordx4 v[172:175], v244, s[44:45] offset:-1024 sc1\n\t"
      "global_load_dwordx4 v[176:179], v244, s[44:45] sc1\n\t"
      "global_load_dwordx4 v[180:183], v244, s[44:45] offset:1024 sc1\n\t"
      "global_load_dwordx4 v[184:187], v244, s[44:45] offset:2048 sc1\n\t"
      "global_load_dwordx4 v[188:191], v244, s[44:45] offset:3072 sc1\n\t"
      "s_waitcnt vmcnt(0)\n\t"
      "v_max3_u32 v232, v136, v137, v138\n\t"
      "v_max3_u32 v232, v232, v139, v140\n\t"
      "v_max3_u32 v232, v232, v141, v142\n\t"
      "v_max_u32_e32 v232, v232, v143\n\t"
      "v_cmp_eq_u32_e32 vcc, -1, v232\n\t"
      "s_cbranch_vccnz LO2L_%=\n\t"
      "LO2X_%=:\n\t"
      "s_branch LO2B_%=\n\t"
      "LO4R_%=:\n\t"
      "s_mov_b32 s80, 0\n\t"
      "LO4L_%=:\n\t"
      "s_add_u32 s80, s80, 1\n\t"
      "s_cmp_lt_u32 s80, 262144\n\t"
      "s_cbranch_scc0 LO4X_%=\n\t"
      "s_waitcnt vmcnt(0)\n\t"
      "s_sleep 1\n\t"
      "global_load_dwordx4 v[144:147], v244, s[42:43] sc1\n\t"
      "global_load_dwordx4 v[148:151], v244, s[42:43] offset:1024 sc1\n\t"
      "global_load_dwordx4 v[152:155], v244, s[42:43] offset:2048 sc1\n\t"
      "global_load_dwordx4 v[156:159], v244, s[42:43] offset:3072 sc1\n\t"
      "global_load_dwordx4 v[160:163], v244, s[44:45] offset:-4096 sc1\n\t"
      "global_load_dwordx4 v[164:167], v244, s[44:45] offset:-3072 sc1\n\t"
      "global_load_dwordx4 v[168:171], v244, s[44:45] offset:-2048 sc1\n\t"
      "global_load_dwordx4 v[172:175], v244, s[44:45] offset:-1024 sc1\n\t"
      "global_load_dwordx4 v[176:179], v244, s[44:45] sc1\n\t"
      "global_load_dwordx4 v[180:183], v244, s[44:45] offset:1024 sc1\n\t"
      "global_load_dwordx4 v[184:187], v244, s[44:45] offset:2048 sc1\n\t"
      "global_load_dwordx4 v[188:191], v244, s[44:45] offset:3072 sc1\n\t"
      "s_waitcnt vmcnt(0)\n\t"
      "v_max3_u32 v232, v144, v145, v146\n\t"
      "v_max3_u32 v232, v232, v147, v148\n\t"
      "v_max3_u32 v232, v232, v149, v150\n\t"
      "v_max_u32_e32 v232, v232, v151\n\t"
      "v_cmp_eq_u32_e32 vcc, -1, v232\n\t"
      "s_cbranch_vccnz LO4L_%=\n\t"
      "LO4X_%=:\n\t"
      "s_branch LO4B_%=\n\t"
      "LO6R_%=:\n\t"
      "s_mov_b32 s80, 0\n\t"
      "LO6L_%=:\n\t"
      "s_add_u32 s80, s80, 1\n\t"
      "s_cmp_lt_u32 s80, 262144\n\t"
      "s_cbranch_scc0 LO6X_%=\n\t"
      "s_waitcnt vmcnt(0)\n\t"
      "s_sleep 1\n\t"
      "global_load_dwordx4 v[152:155], v244, s[42:43] offset:2048 sc1\n\t"
      "global_load_dwordx4 v[156:159], v244, s[42:43] offset:3072 sc1\n\t"
      "global_load_dwordx4 v[160:163], v244, s[44:45] offset:-4096 sc1\n\t"
      "global_load_dwordx4 v[164:167], v244, s[44:45] offset:-3072 sc1\n\t"
      "global_load_dwordx4 v[168:171], v244, s[44:45] offset:-2048 sc1\n\t"
      "global_load_dwordx4 v[172:175], v244, s[44:45] offset:-1024 sc1\n\t"
      "global_load_dwordx4 v[176:179], v244, s[44:45] sc1\n\t"
      "global_load_dwordx4 v[180:183], v244, s[44:45] offset:1024 sc1\n\t"
      "global_load_dwordx4 v[184:187], v244, s[44:45] offset:2048 sc1\n\t"
      "global_load_dwordx4 v[188:191], v244, s[44:45] offset:3072 sc1\n\t"
      "s_waitcnt vmcnt(0)\n\t"
      "v_max3_u32 v232, v152, v153, v154\n\t"
      "v_max3_u32 v232, v232, v155, v156\n\t"
      "v_max3_u32 v232, v232, v157, v158\n\t"
      "v_max_u32_e32 v232, v232, v159\n\t"
      "v_cmp_eq_u32_e32 vcc, -1, v232\n\t"
      "s_cbranch_vccnz LO6L_%=\n\t"
      "LO6X_%=:\n\t"
      "s_branch LO6B_%=\n\t"
      "LO8R_%=:\n\t"
      "s_mov_b32 s80, 0\n\t"
      "LO8L_%=:\n\t"
      "s_add_u32 s80, s80, 1\n\t"
      "s_cmp_lt_u32 s80, 262144\n\t"
      "s_cbranch_scc0 LO8X_%=\n\t"
      "s_waitcnt vmcnt(0)\n\t"
      "s_sleep 1\n\t"
      "global_load_dwordx4 v[160:163], v244, s[44:45] offset:-4096 sc1\n\t"
      "global_load_dwordx4 v[164:167], v244, s[44:45] offset:-3072 sc1\n\t"
      "global_load_dwordx4 v[168:171], v244, s[44:45] offset:-2048 sc1\n\t"
      "global_load_dwordx4 v[172:175], v244, s[44:45] offset:-1024 sc1\n\t"
      "global_load_dwordx4 v[176:179], v244, s[44:45] sc1\n\t"
      "global_load_dwordx4 v[180:183], v244, s[44:45] offset:1024 sc1\n\t"
      "global_load_dwordx4 v[184:187], v244, s[44:45] offset:2048 sc1\n\t"
      "global_load_dwordx4 v[188:191], v244, s[44:45] offset:3072 sc1\n\t"
      "s_waitcnt vmcnt(0)\n\t"
      "v_max3_u32 v232, v160, v161, v162\n\t"
      "v_max3_u32 v232, v232, v163, v164\n\t"
      "v_max3_u32 v232, v232, v165, v166\n\t"
      "v_max_u32_e32 v232, v232, v167\n\t"
      "v_cmp_eq_u32_e32 vcc, -1, v232\n\t"
      "s_cbranch_vccnz LO8L_%=\n\t"
      "LO8X_%=:\n\t"
      "s_branch LO8B_%=\n\t"
      "LO10R_%=:\n\t"
      "s_mov_b32 s80, 0\n\t"
      "LO10L_%=:\n\t"
      "s_add_u32 s80, s80, 1\n\t"
      "s_cmp_lt_u32 s80, 262144\n\t"
      "s_cbranch_scc0 LO10X_%=\n\t"
      "s_waitcnt vmcnt(0)\n\t"
      "s_sleep 1\n\t"
      "global_load_dwordx4 v[168:171], v244, s[44:45] offset:-2048 sc1\n\t"
      "global_load_dwordx4 v[172:175], v244, s[44:45] offset:-1024 sc1\n\t"
      "global_load_dwordx4 v[176:179], v244, s[44:45] sc1\n\t"
      "global_load_dwordx4 v[180:183], v244, s[44:45] offset:1024 sc1\n\t"
      "global_load_dwordx4 v[184:187], v244, s[44:45] offset:2048 sc1\n\t"
      "global_load_dwordx4 v[188:191], v244, s[44:45] offset:3072 sc1\n\t"
      "s_waitcnt vmcnt(0)\n\t"
      "v_max3_u32 v232, v168, v169, v170\n\t"
      "v_max3_u32 v232, v232, v171, v172\n\t"
      "v_max3_u32 v232, v232, v173, v174\n\t"
      "v_max_u32_e32 v232, v232, v175\n\t"
      "v_cmp_eq_u32_e32 vcc, -1, v232\n\t"
      "s_cbranch_vccnz LO10L_%=\n\t"
      "LO10X_%=:\n\t"
      "s_branch LO10B_%=\n\t"
      "LO12R_%=:\n\t"
      "s_mov_b32 s80, 0\n\t"
      "LO12L_%=:\n\t"
      "s_add_u32 s80, s80, 1\n\t"
      "s_cmp_lt_u32 s80, 262144\n\t"
      "s_cbranch_scc0 LO12X_%=\n\t"
      "s_waitcnt vmcnt(0)\n\t"
      "s_sleep 1\n\t"
      "global_load_dwordx4 v[176:179], v244, s[44:45] sc1\n\t"
      "global_load_dwordx4 v[180:183], v244, s[44:45] offset:1024 sc1\n\t"
      "global_load_dwordx4 v[184:187], v244, s[44:45] offset:2048 sc1\n\t"
      "global_load_dwordx4 v[188:191], v244, s[44:45] offset:3072 sc1\n\t"
      "s_waitcnt vmcnt(0)\n\t"
      "v_max3_u32 v232, v176, v177, v178\n\t"
      "v_max3_u32 v232, v232, v179, v180\n\t"
      "v_max3_u32 v232, v232, v181, v182\n\t"
      "v_max_u32_e32 v232, v232, v183\n\t"
      "v_cmp_eq_u32_e32 vcc, -1, v232\n\t"
      "s_cbranch_vccnz LO12L_%=\n\t"
      "LO12X_%=:\n\t"
      "s_branch LO12B_%=\n\t"
      "LO14R_%=:\n\t"
      "s_mov_b32 s80, 0\n\t"
      "LO14L_%=:\n\t"
      "s_add_u32 s80, s80, 1\n\t"
      "s_cmp_lt_u32 s80, 262144\n\t"
      "s_cbranch_scc0 LO14X_%=\n\t"
      "s_waitcnt vmcnt(0)\n\t"
      "s_sleep 1\n\t"
      "global_load_dwordx4 v[184:187], v244, s[44:45] offset:2048 sc1\n\t"
      "global_load_dwordx4 v[188:191], v244, s[44:45] offset:3072 sc1\n\t"
      "s_waitcnt vmcnt(0)\n\t"
      "v_max3_u32 v232, v184, v185, v186\n\t"
      "v_max3_u32 v232, v232, v187, v188\n\t"
      "v_max3_u32 v232, v232, v189, v190\n\t"
      "v_max_u32_e32 v232, v232, v191\n\t"
      "v_cmp_eq_u32_e32 vcc, -1, v232\n\t"
      "s_cbranch_vccnz LO14L_%=\n\t"
      "LO14X_%=:\n\t"
      "s_branch LO14B_%=\n\t"
      "LGX_%=:\n\t"
      "v_max3_u32 v232, v128, v129, v130\n\t"
      "v_max3_u32 v232, v232, v131, v132\n\t"
      "v_max3_u32 v232, v232, v133, v134\n\t"
      "v_max3_u32 v232, v232, v135, v136\n\t"
      "v_max3_u32 v232, v232, v137, v138\n\t"
      "v_max3_u32 v232, v232, v139, v140\n\t"
      "v_max3_u32 v232, v232, v141, v142\n\t"
      "v_max3_u32 v232, v232, v143, v144\n\t"
      "v_max3_u32 v232, v232, v145, v146\n\t"
      "v_max3_u32 v232, v232, v147, v148\n\t"
      "v_max3_u32 v232, v232, v149, v150\n\t"
      "v_max3_u32 v232, v232, v151, v152\n\t"
      "v_max3_u32 v232, v232, v153, v154\n\t"
      "v_max3_u32 v232, v232, v155, v156\n\t"
      "v_max3_u32 v232, v232, v157, v158\n\t"
      "v_max3_u32 v232, v232, v159, v160\n\t"
      "v_max3_u32 v232, v232, v161, v162\n\t"
      "v_max3_u32 v232, v232, v163, v164\n\t"
      "v_max3_u32 v232, v232, v165, v166\n\t"
      "v_max3_u32 v232, v232, v167, v168\n\t"
      "v_max3_u32 v232, v232, v169, v170\n\t"
      "v_max3_u32 v232, v232, v171, v172\n\t"
      "v_max3_u32 v232, v232, v173, v174\n\t"
      "v_max3_u32 v232, v232, v175, v176\n\t"
      "v_max3_u32 v232, v232, v177, v178\n\t"
      "v_max3_u32 v232, v232, v179, v180\n\t"
      "v_max3_u32 v232, v232, v181, v182\n\t"
      "v_max3_u32 v232, v232, v183, v184\n\t"
      "v_max3_u32 v232, v232, v185, v186\n\t"
      "v_max3_u32 v232, v232, v187, v188\n\t"
      "v_max3_u32 v232, v232, v189, v190\n\t"
      "v_max_u32_e32 v232, v232, v191\n\t"
      "v_cmp_eq_u32_e32 vcc, -1, v232\n\t"
      "s_cbranch_vccz LGB_%=\n\t"
      "LGYR_%=:\n\t"
      "s_mov_b32 s80, 0\n\t"
      "LGYL_%=:\n\t"
      "s_add_u32 s80, s80, 1\n\t"
      "s_cmp_lt_u32 s80, 262144\n\t"
      "s_cbranch_scc0 LGYX_%=\n\t"
      "s_sleep 1\n\t"
      "global_load_dwordx4 v[128:131], v244, s[42:43] offset:-4096 sc1\n\t"
      "global_load_dwordx4 v[132:135], v244, s[42:43] offset:-3072 sc1\n\t"
      "global_load_dwordx4 v[136:139], v244, s[42:43] offset:-2048 sc1\n\t"
      "global_load_dwordx4 v[140:143], v244, s[42:43] offset:-1024 sc1\n\t"
      "global_load_dwordx4 v[144:147], v244, s[42:43] sc1\n\t"
      "global_load_dwordx4 v[148:151], v244, s[42:43] offset:1024 sc1\n\t"
      "global_load_dwordx4 v[152:155], v244, s[42:43] offset:2048 sc1\n\t"
      "global_load_dwordx4 v[156:159], v244, s[42:43] offset:3072 sc1\n\t"
      "global_load_dwordx4 v[160:163], v244, s[44:45] offset:-4096 sc1\n\t"
      "global_load_dwordx4 v[164:167], v244, s[44:45] offset:-3072 sc1\n\t"
      "global_load_dwordx4 v[168:171], v244, s[44:45] offset:-2048 sc1\n\t"
      "global_load_dwordx4 v[172:175], v244, s[44:45] offset:-1024 sc1\n\t"
      "global_load_dwordx4 v[176:179], v244, s[44:45] sc1\n\t"
      "global_load_dwordx4 v[180:183], v244, s[44:45] offset:1024 sc1\n\t"
      "global_load_dwordx4 v[184:187], v244, s[44:45] offset:2048 sc1\n\t"
      "global_load_dwordx4 v[188:191], v244, s[44:45] offset:3072 sc1\n\t"
      "s_waitcnt vmcnt(0)\n\t"
      "v_max3_u32 v232, v128, v129, v130\n\t"
      "v_max3_u32 v232, v232, v131, v132\n\t"
      "v_max3_u32 v232, v232, v133, v134\n\t"
      "v_max3_u32 v232, v232, v135, v136\n\t"
      "v_max3_u32 v232, v232, v137, v138\n\t"
      "v_max3_u32 v232, v232, v139, v140\n\t"
      "v_max3_u32 v232, v232, v141, v142\n\t"
      "v_max3_u32 v232, v232, v143, v144\n\t"
      "v_max3_u32 v232, v232, v145, v146\n\t"
      "v_max3_u32 v232, v232, v147, v148\n\t"
      "v_max3_u32 v232, v232, v149, v150\n\t"
      "v_max3_u32 v232, v232, v151, v152\n\t"
      "v_max3_u32 v232, v232, v153, v154\n\t"
      "v_max3_u32 v232, v232, v155, v156\n\t"
      "v_max3_u32 v232, v232, v157, v158\n\t"
      "v_max3_u32 v232, v232, v159, v160\n\t"
      "v_max3_u32 v232, v232, v161, v162\n\t"
      "v_max3_u32 v232, v232, v163, v164\n\t"
      "v_max3_u32 v232, v232, v165, v166\n\t"
      "v_max3_u32 v232, v232, v167, v168\n\t"
      "v_max3_u32 v232, v232, v169, v170\n\t"
      "v_max3_u32 v232, v232, v171, v172\n\t"
      "v_max3_u32 v232, v232, v173, v174\n\t"
      "v_max3_u32 v232, v232, v175, v176\n\t"
      "v_max3_u32 v232, v232, v177, v178\n\t"
      "v_max3_u32 v232, v232, v179, v180\n\t"
      "v_max3_u32 v232, v232, v181, v182\n\t"
      "v_max3_u32 v232, v232, v183, v184\n\t"
      "v_max3_u32 v232, v232, v185, v186\n\t"
      "v_max3_u32 v232, v232, v187, v188\n\t"
      "v_max3_u32 v232, v232, v189, v190\n\t"
      "v_max_u32_e32 v232, v232, v191\n\t"
      "v_cmp_eq_u32_e32 vcc, -1, v232\n\t"
      "s_cbranch_vccnz LGYL_%=\n\t"
      "LGYX_%=:\n\t"
      "s_cbranch_vccnz LGB_%=\n\t"
      "ds_read_b64_tr_b16 v[0:1], v216\n\t"
      "ds_read_b64_tr_b16 v[2:3], v220\n\t"
      "ds_read_b64_tr_b16 v[4:5], v217\n\t"
      "ds_read_b64_tr_b16 v[6:7], v221\n\t"
      "ds_read_b64_tr_b16 v[8:9], v218\n\t"
      "ds_read_b64_tr_b16 v[10:11], v222\n\t"
      "ds_read_b64_tr_b16 v[12:13], v219\n\t"
      "ds_read_b64_tr_b16 v[14:15], v223\n\t"
      "ds_read_b64_tr_b16 v[16:17], v218 offset:3584\n\t"
      "ds_read_b64_tr_b16 v[18:19], v222 offset:3584\n\t"
      "ds_read_b64_tr_b16 v[20:21], v219 offset:3584\n\t"
      "ds_read_b64_tr_b16 v[22:23], v223 offset:3584\n\t"
      "ds_read_b64_tr_b16 v[24:25], v216 offset:3584\n\t"
      "ds_read_b64_tr_b16 v[26:27], v220 offset:3584\n\t"
      "ds_read_b64_tr_b16 v[28:29], v217 offset:3584\n\t"
      "ds_read_b64_tr_b16 v[30:31], v221 offset:3584\n\t"
      "ds_read_b64_tr_b16 v[32:33], v216 offset:7168\n\t"
      "ds_read_b64_tr_b16 v[34:35], v220 offset:7168\n\t"
      "ds_read_b64_tr_b16 v[36:37], v217 offset:7168\n\t"
      "ds_read_b64_tr_b16 v[38:39], v221 offset:7168\n\t"
      "ds_read_b64_tr_b16 v[40:41], v218 offset:7168\n\t"
      "ds_read_b64_tr_b16 v[42:43], v222 offset:7168\n\t"
      "ds_read_b64_tr_b16 v[44:45], v219 offset:7168\n\t"
      "ds_read_b64_tr_b16 v[46:47], v223 offset:7168\n\t"
      "ds_read_b64_tr_b16 v[48:49], v218 offset:10752\n\t"
      "ds_read_b64_tr_b16 v[50:51], v222 offset:10752\n\t"
      "ds_read_b64_tr_b16 v[52:53], v219 offset:10752\n\t"
      "ds_read_b64_tr_b16 v[54:55], v223 offset:10752\n\t"
      "ds_read_b64_tr_b16 v[56:57], v216 offset:10752\n\t"
      "ds_read_b64_tr_b16 v[58:59], v220 offset:10752\n\t"
      "ds_read_b64_tr_b16 v[60:61], v217 offset:10752\n\t"
      "ds_read_b64_tr_b16 v[62:63], v221 offset:10752\n\t"
      "ds_read_b64_tr_b16 v[64:65], v216 offset:14336\n\t"
      "ds_read_b64_tr_b16 v[66:67], v220 offset:14336\n\t"
      "ds_read_b64_tr_b16 v[68:69], v217 offset:14336\n\t"
      "ds_read_b64_tr_b16 v[70:71], v221 offset:14336\n\t"
      "ds_read_b64_tr_b16 v[72:73], v218 offset:14336\n\t"
      "ds_read_b64_tr_b16 v[74:75], v222 offset:14336\n\t"
      "ds_read_b64_tr_b16 v[76:77], v219 offset:14336\n\t"
      "ds_read_b64_tr_b16 v[78:79], v223 offset:14336\n\t"
      "ds_read_b64_tr_b16 v[80:81], v218 offset:17920\n\t"
      "ds_read_b64_tr_b16 v[82:83], v222 offset:17920\n\t"
      "ds_read_b64_tr_b16 v[84:85], v219 offset:17920\n\t"
      "ds_read_b64_tr_b16 v[86:87], v223 offset:17920\n\t"
      "ds_read_b64_tr_b16 v[88:89], v216 offset:17920\n\t"
      "ds_read_b64_tr_b16 v[90:91], v220 offset:17920\n\t"
      "ds_read_b64_tr_b16 v[92:93], v217 offset:17920\n\t"
      "ds_read_b64_tr_b16 v[94:95], v221 offset:17920\n\t"
      "ds_read_b64_tr_b16 v[96:97], v216 offset:21504\n\t"
      "ds_read_b64_tr_b16 v[98:99], v220 offset:21504\n\t"
      "ds_read_b64_tr_b16 v[100:101], v217 offset:21504\n\t"
      "ds_read_b64_tr_b16 v[102:103], v221 offset:21504\n\t"
      "ds_read_b64_tr_b16 v[104:105], v218 offset:21504\n\t"
      "ds_read_b64_tr_b16 v[106:107], v222 offset:21504\n\t"
      "ds_read_b64_tr_b16 v[108:109], v219 offset:21504\n\t"
      "ds_read_b64_tr_b16 v[110:111], v223 offset:21504\n\t"
      "ds_read_b64_tr_b16 v[112:113], v218 offset:25088\n\t"
      "ds_read_b64_tr_b16 v[114:115], v222 offset:25088\n\t"
      "ds_read_b64_tr_b16 v[116:117], v219 offset:25088\n\t"
      "ds_read_b64_tr_b16 v[118:119], v223 offset:25088\n\t"
      "ds_read_b64_tr_b16 v[120:121], v216 offset:25088\n\t"
      "ds_read_b64_tr_b16 v[122:123], v220 offset:25088\n\t"
      "ds_read_b64_tr_b16 v[124:125], v217 offset:25088\n\t"
      "ds_read_b64_tr_b16 v[126:127], v221 offset:25088\n\t"
      "v_lshlrev_b32_e32 v192, 16, v208\n\t"
      "v_and_b32_e32 v193, 0xffff0000, v208\n\t"
      "v_lshlrev_b32_e32 v194, 16, v209\n\t"
      "v_and_b32_e32 v195, 0xffff0000, v209\n\t"
      "v_lshlrev_b32_e32 v196, 16, v210\n\t"
      "v_and_b32_e32 v197, 0xffff0000, v210\n\t"
      "v_lshlrev_b32_e32 v198, 16, v211\n\t"
      "v_and_b32_e32 v199, 0xffff0000, v211\n\t"
      "v_lshlrev_b32_e32 v200, 16, v212\n\t"
      "v_and_b32_e32 v201, 0xffff0000, v212\n\t"
      "v_lshlrev_b32_e32 v202, 16, v213\n\t"
      "v_and_b32_e32 v203, 0xffff0000, v213\n\t"
      "v_lshlrev_b32_e32 v204, 16, v214\n\t"
      "v_and_b32_e32 v205, 0xffff0000, v214\n\t"
      "v_lshlrev_b32_e32 v206, 16, v215\n\t"
      "v_and_b32_e32 v207, 0xffff0000, v215\n\t"
      "s_waitcnt lgkmcnt(0)\n\t"
      "s_branch LGM_%=\n\t"
      "LSR_%=:\n\t"
      "s_mov_b32 s80, 0\n\t"
      "LSL_%=:\n\t"
      "s_add_u32 s80, s80, 1\n\t"
      "s_cmp_lt_u32 s80, 262144\n\t"
      "s_cbranch_scc0 LSX_%=\n\t"
      "s_sleep 1\n\t"
      "global_load_dwordx2 v[148:149], v231, s[46:47] sc1\n\t"
      "global_load_dwordx2 v[150:151], v231, s[46:47] offset:384 sc1\n\t"
      "s_waitcnt vmcnt(0)\n\t"
      "v_max3_u32 v232, v148, v149, v150\n\t"
      "v_max_u32_e32 v232, v232, v151\n\t"
      "v_cmp_eq_u32_e32 vcc, -1, v232\n\t"
      "s_cbranch_vccnz LSL_%=\n\t"
      "LSX_%=:\n\t"
      "s_branch LSB_%=\n\t"
      "LF1R_%=:\n\t"
      "s_mov_b32 s80, 0\n\t"
      "LF1L_%=:\n\t"
      "s_add_u32 s80, s80, 1\n\t"
      "s_cmp_lt_u32 s80, 262144\n\t"
      "s_cbranch_scc0 LF1X_%=\n\t"
      "s_sleep 2\n\t"
      "global_load_dwordx2 v[138:139], v136, s[60:61] sc1\n\t"
      "s_waitcnt vmcnt(0)\n\t"
      "v_cmp_ne_u64_e32 vcc, s[22:23], v[138:139]\n\t"
      "s_cbranch_vccnz LF1L_%=\n\t"
      "LF1X_%=:\n\t"
      "s_branch LF1B_%=\n\t"
      "LF4R_%=:\n\t"
      "s_mov_b32 s80, 0\n\t"
      "LF4L_%=:\n\t"
      "s_add_u32 s80, s80, 1\n\t"
      "s_cmp_lt_u32 s80, 262144\n\t"
      "s_cbranch_scc0 LF4X_%=\n\t"
      "s_sleep 2\n\t"
      "global_load_dwordx2 v[138:139], v137, s[60:61] sc1\n\t"
      "s_waitcnt vmcnt(0)\n\t"
      "v_cmp_ne_u64_e32 vcc, s[22:23], v[138:139]\n\t"
      "s_cbranch_vccnz LF4L_%=\n\t"
      "LF4X_%=:\n\t"
      "s_branch LF4B_%=\n\t"
      "L4_%=:\n\t"
      "s_waitcnt vmcnt(0)\n\t"
      "s_barrier\n\t"
      "s_barrier\n\t"
      "s_lshl2_add_u32 s72, s31, s24\n\t"
      "s_sub_u32 s72, s72, 56\n\t"
      "s_mov_b32 m0, 64512\n\t"
      "s_cmp_ge_u32 s72, 64\n\t"
      "s_cselect_b32 s73, 8, 0\n\t"
      "s_sub_u32 s73, s72, s73\n\t"
      "s_and_b32 s73, s73, 0x38\n\t"
      "s_add_u32 s72, s72, s73\n\t"
      "s_add_u32 s72, s72, 8\n\t"
      "s_mul_i32 s72, s72, 57344\n\t"
      "s_and_b32 s73, s2, 7\n\t"
      "s_mul_i32 s73, s73, 448\n\t"
      "s_and_b32 s73, s73, 0xffffff80\n\t"
      "s_add_u32 s72, s72, s73\n\t"
      "v_bfe_u32 v141, v192, 2, 4\n\t"
      "v_mul_u32_u24_e32 v141, 3584, v141\n\t"
      "v_lshl_add_u32 v141, v196, 7, v141\n\t"
      "v_add_u32_e32 v141, s72, v141\n\t"
      "global_load_lds_dword v141, s[10:11]\n\t"
      "s_barrier\n\t"
      "s_cmp_lg_u32 s24, 3\n\t"
      "s_cbranch_scc1 SK12_%=\n\t"
      "global_load_dwordx2 v[138:139], v137, s[60:61] sc1\n\t"
      "SK12_%=:\n\t"
      "s_cmp_lg_u32 s24, 3\n\t"
      "s_cbranch_scc1 LF4B_%=\n\t"
      "s_waitcnt vmcnt(0)\n\t"
      "v_cmp_ne_u64_e32 vcc, s[22:23], v[138:139]\n\t"
      "s_cbranch_vccnz LF4R_%=\n\t"
      "LF4B_%=:\n\t"
      "s_barrier\n\t"
      "s_waitcnt vmcnt(0)\n\t"
      "s_barrier\n\t"
      "s_branch G4_%=\n\t"
      "LF2R_%=:\n\t"
      "s_mov_b32 s80, 0\n\t"
      "LF2L_%=:\n\t"
      "s_add_u32 s80, s80, 1\n\t"
      "s_cmp_lt_u32 s80, 262144\n\t"
      "s_cbranch_scc0 LF2X_%=\n\t"
      "s_sleep 2\n\t"
      "global_load_dwordx2 v[138:139], v137, s[60:61] sc1\n\t"
      "s_waitcnt vmcnt(0)\n\t"
      "v_cmp_ne_u64_e32 vcc, s[22:23], v[138:139]\n\t"
      "s_cbranch_vccnz LF2L_%=\n\t"
      "LF2X_%=:\n\t"
      "s_branch LF2B_%=\n\t"
      "LF3R_%=:\n\t"
      "s_mov_b32 s80, 0\n\t"
      "LF3L_%=:\n\t"
      "s_add_u32 s80, s80, 1\n\t"
      "s_cmp_lt_u32 s80, 262144\n\t"
      "s_cbranch_scc0 LF3X_%=\n\t"
      "s_sleep 2\n\t"
      "global_load_dwordx2 v[138:139], v143, s[60:61] sc1\n\t"
      "s_waitcnt vmcnt(0)\n\t"
      "v_cmp_ne_u64_e32 vcc, s[22:23], v[138:139]\n\t"
      "s_cbranch_vccnz LF3L_%=\n\t"
      "LF3X_%=:\n\t"
      "s_branch LF3B_%=\n\t"
      "LF70R_%=:\n\t"
      "s_mov_b32 s80, 0\n\t"
      "LF70L_%=:\n\t"
      "s_add_u32 s80, s80, 1\n\t"
      "s_cmp_lt_u32 s80, 262144\n\t"
      "s_cbranch_scc0 LF70X_%=\n\t"
      "s_sleep 4\n\t"
      "global_load_dwordx2 v[138:139], v136, s[60:61] sc1\n\t"
      "s_waitcnt vmcnt(0)\n\t"
      "v_cmp_ne_u64_e32 vcc, s[22:23], v[138:139]\n\t"
      "s_cbranch_vccnz LF70L_%=\n\t"
      "LF70X_%=:\n\t"
      "s_branch LF70B_%=\n\t"
      "LF71R_%=:\n\t"
      "s_mov_b32 s80, 0\n\t"
      "LF71L_%=:\n\t"
      "s_add_u32 s80, s80, 1\n\t"
      "s_cmp_lt_u32 s80, 262144\n\t"
      "s_cbranch_scc0 LF71X_%=\n\t"
      "s_sleep 4\n\t"
      "global_load_dwordx2 v[138:139], v136, s[60:61] sc1\n\t"
      "s_waitcnt vmcnt(0)\n\t"
      "v_cmp_ne_u64_e32 vcc, s[22:23], v[138:139]\n\t"
      "s_cbranch_vccnz LF71L_%=\n\t"
      "LF71X_%=:\n\t"
      "s_branch LF71B_%=\n\t"
      "SPARE_%=:\n\t"
      "v_mov_b32_e32 v132, -1\n\t"
      "v_mov_b32_e32 v133, -1\n\t"
      "v_mov_b32_e32 v134, -1\n\t"
      "v_mov_b32_e32 v135, -1\n\t"
      "s_and_b32 s34, s25, 3\n\t"
      "s_lshl_b32 s34, s34, 15\n\t"
      "s_mul_i32 s35, s24, 917504\n\t"
      "s_add_u32 s34, s34, s35\n\t"
      "s_and_b32 s35, s2, 7\n\t"
      "s_lshl_b32 s35, s35, 9\n\t"
      "s_add_u32 s34, s34, s35\n\t"
      "v_and_b32_e32 v141, 0x1c0, v244\n\t"
      "v_lshlrev_b32_e32 v141, 6, v141\n\t"
      "v_and_b32_e32 v142, 0x30, v244\n\t"
      "v_lshl_add_u32 v141, v142, 3, v141\n\t"
      "v_add_u32_e32 v141, s34, v141\n\t"
      "s_mov_b32 m0, 64512\n\t"
      "s_lshr_b32 s34, s24, 1\n\t"
      "s_lshl_b32 s34, s34, 5\n\t"
      "s_add_u32 s34, s34, s25\n\t"
      "s_and_b32 s35, s24, 1\n\t"
      "s_lshl1_add_u32 s34, s34, s35\n\t"
      "s_lshl_b32 s34, s34, 10\n\t"
      "s_add_u32 s34, s34, 8388608\n\t"
      "s_add_u32 s40, s20, s34\n\t"
      "s_addc_u32 s41, s21, 0\n\t"
      "global_store_dwordx4 v244, v[132:135], s[40:41] sc1\n\t"
      "s_add_u32 s40, s40, 262144\n\t"
      "s_addc_u32 s41, s41, 0\n\t"
      "global_store_dwordx4 v244, v[132:135], s[40:41] sc1\n\t"
      "s_add_u32 s40, s40, 262144\n\t"
      "s_addc_u32 s41, s41, 0\n\t"
      "global_store_dwordx4 v244, v[132:135], s[40:41] sc1\n\t"
      "s_add_u32 s40, s40, 262144\n\t"
      "s_addc_u32 s41, s41, 0\n\t"
      "global_store_dwordx4 v244, v[132:135], s[40:41] sc1\n\t"
      "s_add_u32 s40, s40, 262144\n\t"
      "s_addc_u32 s41, s41, 0\n\t"
      "global_store_dwordx4 v244, v[132:135], s[40:41] sc1\n\t"
      "s_add_u32 s40, s40, 262144\n\t"
      "s_addc_u32 s41, s41, 0\n\t"
      "global_store_dwordx4 v244, v[132:135], s[40:41] sc1\n\t"
      "s_add_u32 s40, s40, 262144\n\t"
      "s_addc_u32 s41, s41, 0\n\t"
      "global_store_dwordx4 v244, v[132:135], s[40:41] sc1\n\t"
      "s_add_u32 s40, s40, 262144\n\t"
      "s_addc_u32 s41, s41, 0\n\t"
      "s_lshl_b32 s34, s25, 7\n\t"
      "s_add_u32 s34, s34, 16384\n\t"
      "v_mov_b32_e32 v131, s34\n\t"
      "s_waitcnt vmcnt(0)\n\t"
      "s_barrier\n\t"
      "s_cmp_lg_u32 s24, 0\n\t"
      "s_cbranch_scc1 SP0_%=\n\t"
      "s_mov_b64 exec, 1\n\t"
      "global_store_dwordx2 v131, v[128:129], s[60:61] sc1\n\t"
      "s_mov_b64 exec, -1\n\t"
      "SP0_%=:\n\t"
      "s_and_b32 s34, s25, 3\n\t"
      "s_lshl3_add_u32 s34, s34, s24\n\t"
      "s_cmp_lg_u32 s34, 19\n\t"
      "s_cbranch_scc1 SQ0_%=\n\t"
      "v_and_b32_e32 v136, 0x1f0, v244\n\t"
      "v_lshlrev_b32_e32 v131, 6, v136\n\t"
      "v_lshlrev_b32_e32 v136, 3, v136\n\t"
      "v_add_u32_e32 v136, 16384, v136\n\t"
      "s_and_b32 s34, s2, 7\n\t"
      "s_lshl_b32 s34, s34, 7\n\t"
      "s_add_u32 s34, s34, 32768\n\t"
      "v_add_u32_e32 v131, s34, v131\n\t"
      "s_cmp_lg_u32 s24, 3\n\t"
      "s_cbranch_scc1 SK15_%=\n\t"
      "global_load_dwordx2 v[138:139], v136, s[60:61] sc1\n\t"
      "SK15_%=:\n\t"
      "s_cmp_lg_u32 s24, 3\n\t"
      "s_cbranch_scc1 LF70B_%=\n\t"
      "s_waitcnt vmcnt(0)\n\t"
      "v_cmp_ne_u64_e32 vcc, s[22:23], v[138:139]\n\t"
      "s_cbranch_vccnz LF70R_%=\n\t"
      "LF70B_%=:\n\t"
      "global_store_dwordx2 v131, v[128:129], s[60:61] sc1\n\t"
      "SQ0_%=:\n\t"
      "s_barrier\n\t"
      "s_lshl3_add_u32 s34, s25, s24\n\t"
      "s_mul_i32 s34, s34, 7168\n\t"
      "s_add_u32 s34, s34, 10227712\n\t"
      "s_add_u32 s40, s20, s34\n\t"
      "s_addc_u32 s41, s21, 0\n\t"
      "global_store_dwordx4 v244, v[132:135], s[40:41] offset:-4096 sc1\n\t"
      "global_store_dwordx4 v244, v[132:135], s[40:41] offset:-3072 sc1\n\t"
      "global_store_dwordx4 v244, v[132:135], s[40:41] offset:-2048 sc1\n\t"
      "global_store_dwordx4 v244, v[132:135], s[40:41] offset:-1024 sc1\n\t"
      "global_store_dwordx4 v244, v[132:135], s[40:41] sc1\n\t"
      "global_store_dwordx4 v244, v[132:135], s[40:41] offset:1024 sc1\n\t"
      "global_store_dwordx4 v244, v[132:135], s[40:41] offset:2048 sc1\n\t"
      "s_lshl_b32 s34, s25, 7\n\t"
      "s_add_u32 s34, s34, 24576\n\t"
      "v_mov_b32_e32 v131, s34\n\t"
      "s_waitcnt vmcnt(0)\n\t"
      "s_barrier\n\t"
      "s_cmp_lg_u32 s24, 0\n\t"
      "s_cbranch_scc1 SP1_%=\n\t"
      "s_mov_b64 exec, 1\n\t"
      "global_store_dwordx2 v131, v[128:129], s[60:61] sc1\n\t"
      "s_mov_b64 exec, -1\n\t"
      "SP1_%=:\n\t"
      "s_and_b32 s34, s25, 3\n\t"
      "s_lshl3_add_u32 s34, s34, s24\n\t"
      "s_cmp_lg_u32 s34, 27\n\t"
      "s_cbranch_scc1 SQ1_%=\n\t"
      "v_and_b32_e32 v136, 0x1f0, v244\n\t"
      "v_lshlrev_b32_e32 v131, 6, v136\n\t"
      "v_lshlrev_b32_e32 v136, 3, v136\n\t"
      "v_add_u32_e32 v136, 24576, v136\n\t"
      "s_and_b32 s34, s2, 7\n\t"
      "s_lshl_b32 s34, s34, 7\n\t"
      "s_add_u32 s34, s34, 65536\n\t"
      "v_add_u32_e32 v131, s34, v131\n\t"
      "s_cmp_lg_u32 s24, 3\n\t"
      "s_cbranch_scc1 SK16_%=\n\t"
      "global_load_dwordx2 v[138:139], v136, s[60:61] sc1\n\t"
      "SK16_%=:\n\t"
      "s_cmp_lg_u32 s24, 3\n\t"
      "s_cbranch_scc1 LF71B_%=\n\t"
      "s_waitcnt vmcnt(0)\n\t"
      "v_cmp_ne_u64_e32 vcc, s[22:23], v[138:139]\n\t"
      "s_cbranch_vccnz LF71R_%=\n\t"
      "LF71B_%=:\n\t"
      "global_store_dwordx2 v131, v[128:129], s[60:61] sc1\n\t"
      "SQ1_%=:\n\t"
      "s_barrier\n\t"
      "s_and_b32 s35, s25, 3\n\t"
      "s_lshl2_add_u32 s31, s26, s35\n\t"
      "s_and_b32 s35, s2, 7\n\t"
      "s_mul_i32 s34, s35, 14\n\t"
      "s_lshl4_add_u32 s35, s35, -14\n\t"
      "s_cmp_lt_u32 s31, 14\n\t"
      "s_cselect_b32 s34, s34, s35\n\t"
      "s_cselect_b32 s36, 1, 0\n\t"
      "s_or_b32 s27, s27, s36\n\t"
      "s_add_u32 s34, s34, s31\n\t"
      "s_lshr_b32 s29, s34, 2\n\t"
      "s_and_b32 s30, s34, 3\n\t"
      "s_mul_i32 s30, s30, 7\n\t"
      "v_and_b32_e32 v199, 60, v192\n\t"
      "v_and_b32_e32 v200, 24, v192\n\t"
      "v_lshlrev_b32_e32 v202, 3, v196\n\t"
      "v_lshl_or_b32 v202, v200, 2, v202\n\t"
      "v_lshl_or_b32 v202, v195, 7, v202\n\t"
      "v_lshl_or_b32 v202, v193, 10, v202\n\t"
      "v_add_u32_e32 v224, s39, v202\n\t"
      "v_xor_b32_e32 v225, 32, v224\n\t"
      "v_xor_b32_e32 v226, 64, v224\n\t"
      "v_xor_b32_e32 v227, 96, v224\n\t"
      "s_lshl_b32 s34, s24, 12\n\t"
      "s_add_u32 s34, s34, 98304\n\t"
      "v_add_u32_e32 v245, s34, v244\n\t"
      "v_lshlrev_b32_e32 v199, 2, v199\n\t"
      "s_lshl_b32 s34, s24, 8\n\t"
      "v_lshl_add_u32 v247, v192, 2, s34\n\t"
      "v_and_b32_e32 v200, 2, v192\n\t"
      "v_mul_u32_u24_e32 v202, 576, v200\n\t"
      "v_xor_b32_e32 v199, v202, v199\n\t"
      "v_and_b32_e32 v202, 1, v192\n\t"
      "v_lshl_or_b32 v199, v202, 3, v199\n\t"
      "s_and_b32 s35, s34, 0x400\n\t"
      "s_add_u32 s34, s34, s35\n\t"
      "s_add_u32 s34, s34, 98304\n\t"
      "v_add_u32_e32 v246, s34, v199\n\t"
      "v_lshlrev_b32_e32 v199, 2, v202\n\t"
      "v_lshl_or_b32 v199, v200, 4, v199\n\t"
      "s_min_u32 s34, s29, 27\n\t"
      "s_lshl_b32 s34, s34, 7\n\t"
      "s_lshr_b32 s35, s24, 2\n\t"
      "s_lshl_b32 s35, s35, 6\n\t"
      "s_add_u32 s34, s34, s35\n\t"
      "s_lshr_b32 s35, s39, 9\n\t"
      "s_add_u32 s34, s34, s35\n\t"
      "v_add_u32_e32 v248, s34, v199\n\t"
      "s_add_u32 s48, s20, 8388608\n\t"
      "s_addc_u32 s49, s21, 0\n\t"
      "s_cmp_ge_u32 s31, 30\n\t"
      "s_cbranch_scc1 BYE_%=\n\t"
      "s_setprio 0\n\t"
      "s_waitcnt vmcnt(0)\n\t"
      "s_barrier\n\t"
      "s_branch G4_%=\n\t"
      "BYE_%=:\n\t"
      "s_endpgm\n\t"
      "LA30R_%=:\n\t"
      "s_mov_b32 s80, 0\n\t"
      "LA30L_%=:\n\t"
      "s_add_u32 s80, s80, 1\n\t"
      "s_cmp_lt_u32 s80, 262144\n\t"
      "s_cbranch_scc0 LA30X_%=\n\t"
      "s_waitcnt vmcnt(0)\n\t"
      "s_sleep 1\n\t"
      "global_load_dwordx4 v[128:131], v250, s[64:65] offset:-4096 sc1\n\t"
      "global_load_dwordx4 v[132:135], v250, s[64:65] offset:-3072 sc1\n\t"
      "global_load_dwordx4 v[136:139], v250, s[64:65] offset:-2048 sc1\n\t"
      "global_load_dwordx4 v[140:143], v250, s[64:65] offset:-1024 sc1\n\t"
      "global_load_dwordx4 v[144:147], v250, s[64:65] sc1\n\t"
      "global_load_dwordx4 v[148:151], v250, s[64:65] offset:1024 sc1\n\t"
      "global_load_dwordx4 v[152:155], v250, s[64:65] offset:2048 sc1\n\t"
      "global_load_dwordx4 v[156:159], v250, s[64:65] offset:3072 sc1\n\t"
      "s_waitcnt vmcnt(0)\n\t"
      "v_max3_u32 v251, v128, v129, v130\n\t"
      "v_max3_u32 v251, v251, v131, v132\n\t"
      "v_max3_u32 v251, v251, v133, v134\n\t"
      "v_max3_u32 v251, v251, v135, v136\n\t"
      "v_max3_u32 v251, v251, v137, v138\n\t"
      "v_max3_u32 v251, v251, v139, v140\n\t"
      "v_max3_u32 v251, v251, v141, v142\n\t"
      "v_max3_u32 v251, v251, v143, v144\n\t"
      "v_max3_u32 v251, v251, v145, v146\n\t"
      "v_max3_u32 v251, v251, v147, v148\n\t"
      "v_max3_u32 v251, v251, v149, v150\n\t"
      "v_max3_u32 v251, v251, v151, v152\n\t"
      "v_max3_u32 v251, v251, v153, v154\n\t"
      "v_max3_u32 v251, v251, v155, v156\n\t"
      "v_max3_u32 v251, v251, v157, v158\n\t"
      "v_max_u32_e32 v251, v251, v159\n\t"
      "v_cmp_eq_u32_e32 vcc, -1, v251\n\t"
      "s_cbranch_vccnz LA30L_%=\n\t"
      "LA30X_%=:\n\t"
      "s_branch LA30B_%=\n\t"
      "LF6R_%=:\n\t"
      "s_mov_b32 s80, 0\n\t"
      "LF6L_%=:\n\t"
      "s_add_u32 s80, s80, 1\n\t"
      "s_cmp_lt_u32 s80, 262144\n\t"
      "s_cbranch_scc0 LF6X_%=\n\t"
      "s_sleep 2\n\t"
      "global_load_dwordx2 v[228:229], v230, s[60:61] sc1\n\t"
      "s_waitcnt vmcnt(0)\n\t"
      "v_cmp_ne_u64_e32 vcc, s[22:23], v[228:229]\n\t"
      "s_cbranch_vccnz LF6L_%=\n\t"
      "LF6X_%=:\n\t"
      "s_branch LF6B_%=\n\t"
      "LA31R_%=:\n\t"
      "s_mov_b32 s80, 0\n\t"
      "LA31L_%=:\n\t"
      "s_add_u32 s80, s80, 1\n\t"
      "s_cmp_lt_u32 s80, 262144\n\t"
      "s_cbranch_scc0 LA31X_%=\n\t"
      "s_waitcnt vmcnt(0)\n\t"
      "s_sleep 1\n\t"
      "global_load_dwordx4 v[160:163], v250, s[66:67] offset:-4096 sc1\n\t"
      "global_load_dwordx4 v[164:167], v250, s[66:67] offset:-3072 sc1\n\t"
      "global_load_dwordx4 v[168:171], v250, s[66:67] offset:-2048 sc1\n\t"
      "global_load_dwordx4 v[172:175], v250, s[66:67] offset:-1024 sc1\n\t"
      "global_load_dwordx4 v[176:179], v250, s[66:67] sc1\n\t"
      "global_load_dwordx4 v[180:183], v250, s[66:67] offset:1024 sc1\n\t"
      "global_load_dwordx4 v[184:187], v250, s[66:67] offset:2048 sc1\n\t"
      "global_load_dwordx4 v[188:191], v250, s[66:67] offset:3072 sc1\n\t"
      "s_waitcnt vmcnt(0)\n\t"
      "v_max3_u32 v251, v160, v161, v162\n\t"
      "v_max3_u32 v251, v251, v163, v164\n\t"
      "v_max3_u32 v251, v251, v165, v166\n\t"
      "v_max3_u32 v251, v251, v167, v168\n\t"
      "v_max3_u32 v251, v251, v169, v170\n\t"
      "v_max3_u32 v251, v251, v171, v172\n\t"
      "v_max3_u32 v251, v251, v173, v174\n\t"
      "v_max3_u32 v251, v251, v175, v176\n\t"
      "v_max3_u32 v251, v251, v177, v178\n\t"
      "v_max3_u32 v251, v251, v179, v180\n\t"
      "v_max3_u32 v251, v251, v181, v182\n\t"
      "v_max3_u32 v251, v251, v183, v184\n\t"
      "v_max3_u32 v251, v251, v185, v186\n\t"
      "v_max3_u32 v251, v251, v187, v188\n\t"
      "v_max3_u32 v251, v251, v189, v190\n\t"
      "v_max_u32_e32 v251, v251, v191\n\t"
      "v_cmp_eq_u32_e32 vcc, -1, v251\n\t"
      "s_cbranch_vccnz LA31L_%=\n\t"
      "LA31X_%=:\n\t"
      "v_mfma_f32_16x16x32_bf16 v[224:227], v[0:3], v[160:163], 0\n\t"
      "v_mfma_f32_16x16x32_bf16 v[224:227], v[16:19], v[164:167], v[224:227]\n\t"
      "v_mfma_f32_16x16x32_bf16 v[224:227], v[32:35], v[168:171], v[224:227]\n\t"
      "v_mfma_f32_16x16x32_bf16 v[224:227], v[48:51], v[172:175], v[224:227]\n\t"
      "v_mfma_f32_16x16x32_bf16 v[224:227], v[64:67], v[176:179], v[224:227]\n\t"
      "v_mfma_f32_16x16x32_bf16 v[224:227], v[80:83], v[180:183], v[224:227]\n\t"
      "v_mfma_f32_16x16x32_bf16 v[224:227], v[96:99], v[184:187], v[224:227]\n\t"
      "v_mfma_f32_16x16x32_bf16 v[224:227], v[112:115], v[188:191], v[224:227]\n\t"
      "v_mfma_f32_16x16x32_bf16 v[208:211], v[4:7], v[160:163], 0\n\t"
      "v_mfma_f32_16x16x32_bf16 v[208:211], v[20:23], v[164:167], v[208:211]\n\t"
      "v_mfma_f32_16x16x32_bf16 v[208:211], v[36:39], v[168:171], v[208:211]\n\t"
      "s_nop 7\n\t"
      "s_branch LA31B_%=\n\t"
      "LA32R_%=:\n\t"
      "s_mov_b32 s80, 0\n\t"
      "LA32L_%=:\n\t"
      "s_add_u32 s80, s80, 1\n\t"
      "s_cmp_lt_u32 s80, 262144\n\t"
      "s_cbranch_scc0 LA32X_%=\n\t"
      "s_waitcnt vmcnt(0)\n\t"
      "s_sleep 1\n\t"
      "global_load_dwordx4 v[192:195], v250, s[68:69] offset:-4096 sc1\n\t"
      "global_load_dwordx4 v[196:199], v250, s[68:69] offset:-3072 sc1\n\t"
      "global_load_dwordx4 v[200:203], v250, s[68:69] offset:-2048 sc1\n\t"
      "global_load_dwordx4 v[204:207], v250, s[68:69] offset:-1024 sc1\n\t"
      "global_load_dwordx4 v[128:131], v250, s[68:69] sc1\n\t"
      "global_load_dwordx4 v[132:135], v250, s[68:69] offset:1024 sc1\n\t"
      "global_load_dwordx4 v[136:139], v250, s[68:69] offset:2048 sc1\n\t"
      "global_load_dwordx4 v[140:143], v250, s[68:69] offset:3072 sc1\n\t"
      "s_waitcnt vmcnt(0)\n\t"
      "v_max3_u32 v251, v192, v193, v194\n\t"
      "v_max3_u32 v251, v251, v195, v196\n\t"
      "v_max3_u32 v251, v251, v197, v198\n\t"
      "v_max3_u32 v251, v251, v199, v200\n\t"
      "v_max3_u32 v251, v251, v201, v202\n\t"
      "v_max3_u32 v251, v251, v203, v204\n\t"
      "v_max3_u32 v251, v251, v205, v206\n\t"
      "v_max3_u32 v251, v251, v207, v128\n\t"
      "v_max3_u32 v251, v251, v129, v130\n\t"
      "v_max3_u32 v251, v251, v131, v132\n\t"
      "v_max3_u32 v251, v251, v133, v134\n\t"
      "v_max3_u32 v251, v251, v135, v136\n\t"
      "v_max3_u32 v251, v251, v137, v138\n\t"
      "v_max3_u32 v251, v251, v139, v140\n\t"
      "v_max3_u32 v251, v251, v141, v142\n\t"
      "v_max_u32_e32 v251, v251, v143\n\t"
      "v_cmp_eq_u32_e32 vcc, -1, v251\n\t"
      "s_cbranch_vccnz LA32L_%=\n\t"
      "LA32X_%=:\n\t"
      "v_mfma_f32_16x16x32_bf16 v[220:223], v[0:3], v[192:195], 0\n\t"
      "v_mfma_f32_16x16x32_bf16 v[220:223], v[16:19], v[196:199], v[220:223]\n\t"
      "v_mfma_f32_16x16x32_bf16 v[220:223], v[32:35], v[200:203], v[220:223]\n\t"
      "v_mfma_f32_16x16x32_bf16 v[220:223], v[48:51], v[204:207], v[220:223]\n\t"
      "v_mfma_f32_16x16x32_bf16 v[220:223], v[64:67], v[128:131], v[220:223]\n\t"
      "v_mfma_f32_16x16x32_bf16 v[220:223], v[80:83], v[132:135], v[220:223]\n\t"
      "v_mfma_f32_16x16x32_bf16 v[220:223], v[96:99], v[136:139], v[220:223]\n\t"
      "v_mfma_f32_16x16x32_bf16 v[220:223], v[112:115], v[140:143], v[220:223]\n\t"
      "v_mfma_f32_16x16x32_bf16 v[224:227], v[4:7], v[192:195], 0\n\t"
      "v_mfma_f32_16x16x32_bf16 v[224:227], v[20:23], v[196:199], v[224:227]\n\t"
      "v_mfma_f32_16x16x32_bf16 v[224:227], v[36:39], v[200:203], v[224:227]\n\t"
      "s_nop 7\n\t"
      "s_branch LA32B_%=\n\t"
      "LA33R_%=:\n\t"
      "s_mov_b32 s80, 0\n\t"
      "LA33L_%=:\n\t"
      "s_add_u32 s80, s80, 1\n\t"
      "s_cmp_lt_u32 s80, 262144\n\t"
      "s_cbranch_scc0 LA33X_%=\n\t"
      "s_waitcnt vmcnt(0)\n\t"
      "s_sleep 1\n\t"
      "global_load_dwordx4 v[144:147], v250, s[70:71] offset:-4096 sc1\n\t"
      "global_load_dwordx4 v[148:151], v250, s[70:71] offset:-3072 sc1\n\t"
      "global_load_dwordx4 v[152:155], v250, s[70:71] offset:-2048 sc1\n\t"
      "global_load_dwordx4 v[156:159], v250, s[70:71] offset:-1024 sc1\n\t"
      "global_load_dwordx4 v[160:163], v250, s[70:71] sc1\n\t"
      "global_load_dwordx4 v[164:167], v250, s[70:71] offset:1024 sc1\n\t"
      "global_load_dwordx4 v[168:171], v250, s[70:71] offset:2048 sc1\n\t"
      "global_load_dwordx4 v[172:175], v250, s[70:71] offset:3072 sc1\n\t"
      "s_waitcnt vmcnt(0)\n\t"
      "v_max3_u32 v251, v144, v145, v146\n\t"
      "v_max3_u32 v251, v251, v147, v148\n\t"
      "v_max3_u32 v251, v251, v149, v150\n\t"
      "v_max3_u32 v251, v251, v151, v152\n\t"
      "v_max3_u32 v251, v251, v153, v154\n\t"
      "v_max3_u32 v251, v251, v155, v156\n\t"
      "v_max3_u32 v251, v251, v157, v158\n\t"
      "v_max3_u32 v251, v251, v159, v160\n\t"
      "v_max3_u32 v251, v251, v161, v162\n\t"
      "v_max3_u32 v251, v251, v163, v164\n\t"
      "v_max3_u32 v251, v251, v165, v166\n\t"
      "v_max3_u32 v251, v251, v167, v168\n\t"
      "v_max3_u32 v251, v251, v169, v170\n\t"
      "v_max3_u32 v251, v251, v171, v172\n\t"
      "v_max3_u32 v251, v251, v173, v174\n\t"
      "v_max_u32_e32 v251, v251, v175\n\t"
      "v_cmp_eq_u32_e32 vcc, -1, v251\n\t"
      "s_cbranch_vccnz LA33L_%=\n\t"
      "LA33X_%=:\n\t"
      "v_mfma_f32_16x16x32_bf16 v[216:219], v[0:3], v[144:147], 0\n\t"
      "v_mfma_f32_16x16x32_bf16 v[216:219], v[16:19], v[148:151], v[216:219]\n\t"
      "v_mfma_f32_16x16x32_bf16 v[216:219], v[32:35], v[152:155], v[216:219]\n\t"
      "v_mfma_f32_16x16x32_bf16 v[216:219], v[48:51], v[156:159], v[216:219]\n\t"
      "v_mfma_f32_16x16x32_bf16 v[216:219], v[64:67], v[160:163], v[216:219]\n\t"
      "v_mfma_f32_16x16x32_bf16 v[216:219], v[80:83], v[164:167], v[216:219]\n\t"
      "v_mfma_f32_16x16x32_bf16 v[216:219], v[96:99], v[168:171], v[216:219]\n\t"
      "v_mfma_f32_16x16x32_bf16 v[216:219], v[112:115], v[172:175], v[216:219]\n\t"
      "v_mfma_f32_16x16x32_bf16 v[220:223], v[4:7], v[144:147], 0\n\t"
      "v_mfma_f32_16x16x32_bf16 v[220:223], v[20:23], v[148:151], v[220:223]\n\t"
      "v_mfma_f32_16x16x32_bf16 v[220:223], v[36:39], v[152:155], v[220:223]\n\t"
      "s_nop 7\n\t"
      "s_branch LA33B_%=\n\t"
      "LA34R_%=:\n\t"
      "s_mov_b32 s80, 0\n\t"
      "LA34L_%=:\n\t"
      "s_add_u32 s80, s80, 1\n\t"
      "s_cmp_lt_u32 s80, 262144\n\t"
      "s_cbranch_scc0 LA34X_%=\n\t"
      "s_waitcnt vmcnt(0)\n\t"
      "s_sleep 1\n\t"
      "global_load_dwordx4 v[176:179], v250, s[72:73] offset:-4096 sc1\n\t"
      "global_load_dwordx4 v[180:183], v250, s[72:73] offset:-3072 sc1\n\t"
      "global_load_dwordx4 v[184:187], v250, s[72:73] offset:-2048 sc1\n\t"
      "global_load_dwordx4 v[188:191], v250, s[72:73] offset:-1024 sc1\n\t"
      "global_load_dwordx4 v[192:195], v250, s[72:73] sc1\n\t"
      "global_load_dwordx4 v[196:199], v250, s[72:73] offset:1024 sc1\n\t"
      "global_load_dwordx4 v[200:203], v250, s[72:73] offset:2048 sc1\n\t"
      "global_load_dwordx4 v[204:207], v250, s[72:73] offset:3072 sc1\n\t"
      "s_waitcnt vmcnt(0)\n\t"
      "v_max3_u32 v251, v176, v177, v178\n\t"
      "v_max3_u32 v251, v251, v179, v180\n\t"
      "v_max3_u32 v251, v251, v181, v182\n\t"
      "v_max3_u32 v251, v251, v183, v184\n\t"
      "v_max3_u32 v251, v251, v185, v186\n\t"
      "v_max3_u32 v251, v251, v187, v188\n\t"
      "v_max3_u32 v251, v251, v189, v190\n\t"
      "v_max3_u32 v251, v251, v191, v192\n\t"
      "v_max3_u32 v251, v251, v193, v194\n\t"
      "v_max3_u32 v251, v251, v195, v196\n\t"
      "v_max3_u32 v251, v251, v197, v198\n\t"
      "v_max3_u32 v251, v251, v199, v200\n\t"
      "v_max3_u32 v251, v251, v201, v202\n\t"
      "v_max3_u32 v251, v251, v203, v204\n\t"
      "v_max3_u32 v251, v251, v205, v206\n\t"
      "v_max_u32_e32 v251, v251, v207\n\t"
      "v_cmp_eq_u32_e32 vcc, -1, v251\n\t"
      "s_cbranch_vccnz LA34L_%=\n\t"
      "LA34X_%=:\n\t"
      "v_mfma_f32_16x16x32_bf16 v[212:215], v[0:3], v[176:179], 0\n\t"
      "v_mfma_f32_16x16x32_bf16 v[212:215], v[16:19], v[180:183], v[212:215]\n\t"
      "v_mfma_f32_16x16x32_bf16 v[212:215], v[32:35], v[184:187], v[212:215]\n\t"
      "v_mfma_f32_16x16x32_bf16 v[212:215], v[48:51], v[188:191], v[212:215]\n\t"
      "v_mfma_f32_16x16x32_bf16 v[212:215], v[64:67], v[192:195], v[212:215]\n\t"
      "v_mfma_f32_16x16x32_bf16 v[212:215], v[80:83], v[196:199], v[212:215]\n\t"
      "v_mfma_f32_16x16x32_bf16 v[212:215], v[96:99], v[200:203], v[212:215]\n\t"
      "v_mfma_f32_16x16x32_bf16 v[212:215], v[112:115], v[204:207], v[212:215]\n\t"
      "v_mfma_f32_16x16x32_bf16 v[216:219], v[4:7], v[176:179], 0\n\t"
      "v_mfma_f32_16x16x32_bf16 v[216:219], v[20:23], v[180:183], v[216:219]\n\t"
      "v_mfma_f32_16x16x32_bf16 v[216:219], v[36:39], v[184:187], v[216:219]\n\t"
      "s_nop 7\n\t"
      "s_branch LA34B_%=\n\t"
      "LA35R_%=:\n\t"
      "s_mov_b32 s80, 0\n\t"
      "LA35L_%=:\n\t"
      "s_add_u32 s80, s80, 1\n\t"
      "s_cmp_lt_u32 s80, 262144\n\t"
      "s_cbranch_scc0 LA35X_%=\n\t"
      "s_waitcnt vmcnt(0)\n\t"
      "s_sleep 1\n\t"
      "global_load_dwordx4 v[128:131], v250, s[74:75] offset:-4096 sc1\n\t"
      "global_load_dwordx4 v[132:135], v250, s[74:75] offset:-3072 sc1\n\t"
      "global_load_dwordx4 v[136:139], v250, s[74:75] offset:-2048 sc1\n\t"
      "global_load_dwordx4 v[140:143], v250, s[74:75] offset:-1024 sc1\n\t"
      "global_load_dwordx4 v[144:147], v250, s[74:75] sc1\n\t"
      "global_load_dwordx4 v[148:151], v250, s[74:75] offset:1024 sc1\n\t"
      "global_load_dwordx4 v[152:155], v250, s[74:75] offset:2048 sc1\n\t"
      "global_load_dwordx4 v[156:159], v250, s[74:75] offset:3072 sc1\n\t"
      "s_waitcnt vmcnt(0)\n\t"
      "v_max3_u32 v251, v128, v129, v130\n\t"
      "v_max3_u32 v251, v251, v131, v132\n\t"
      "v_max3_u32 v251, v251, v133, v134\n\t"
      "v_max3_u32 v251, v251, v135, v136\n\t"
      "v_max3_u32 v251, v251, v137, v138\n\t"
      "v_max3_u32 v251, v251, v139, v140\n\t"
      "v_max3_u32 v251, v251, v141, v142\n\t"
      "v_max3_u32 v251, v251, v143, v144\n\t"
      "v_max3_u32 v251, v251, v145, v146\n\t"
      "v_max3_u32 v251, v251, v147, v148\n\t"
      "v_max3_u32 v251, v251, v149, v150\n\t"
      "v_max3_u32 v251, v251, v151, v152\n\t"
      "v_max3_u32 v251, v251, v153, v154\n\t"
      "v_max3_u32 v251, v251, v155, v156\n\t"
      "v_max3_u32 v251, v251, v157, v158\n\t"
      "v_max_u32_e32 v251, v251, v159\n\t"
      "v_cmp_eq_u32_e32 vcc, -1, v251\n\t"
      "s_cbranch_vccnz LA35L_%=\n\t"
      "LA35X_%=:\n\t"
      "v_mfma_f32_16x16x32_bf16 v[208:211], v[0:3], v[128:131], 0\n\t"
      "v_mfma_f32_16x16x32_bf16 v[208:211], v[16:19], v[132:135], v[208:211]\n\t"
      "v_mfma_f32_16x16x32_bf16 v[208:211], v[32:35], v[136:139], v[208:211]\n\t"
      "v_mfma_f32_16x16x32_bf16 v[208:211], v[48:51], v[140:143], v[208:211]\n\t"
      "v_mfma_f32_16x16x32_bf16 v[208:211], v[64:67], v[144:147], v[208:211]\n\t"
      "v_mfma_f32_16x16x32_bf16 v[208:211], v[80:83], v[148:151], v[208:211]\n\t"
      "v_mfma_f32_16x16x32_bf16 v[208:211], v[96:99], v[152:155], v[208:211]\n\t"
      "v_mfma_f32_16x16x32_bf16 v[208:211], v[112:115], v[156:159], v[208:211]\n\t"
      "v_mfma_f32_16x16x32_bf16 v[212:215], v[4:7], v[128:131], 0\n\t"
      "v_mfma_f32_16x16x32_bf16 v[212:215], v[20:23], v[132:135], v[212:215]\n\t"
      "v_mfma_f32_16x16x32_bf16 v[212:215], v[36:39], v[136:139], v[212:215]\n\t"
      "s_nop 7\n\t"
      "s_branch LA35B_%=\n\t"
      "LA36R_%=:\n\t"
      "s_mov_b32 s80, 0\n\t"
      "LA36L_%=:\n\t"
      "s_add_u32 s80, s80, 1\n\t"
      "s_cmp_lt_u32 s80, 262144\n\t"
      "s_cbranch_scc0 LA36X_%=\n\t"
      "s_waitcnt vmcnt(0)\n\t"
      "s_sleep 1\n\t"
      "global_load_dwordx4 v[160:163], v250, s[76:77] offset:-4096 sc1\n\t"
      "global_load_dwordx4 v[164:167], v250, s[76:77] offset:-3072 sc1\n\t"
      "global_load_dwordx4 v[168:171], v250, s[76:77] offset:-2048 sc1\n\t"
      "global_load_dwordx4 v[172:175], v250, s[76:77] offset:-1024 sc1\n\t"
      "global_load_dwordx4 v[176:179], v250, s[76:77] sc1\n\t"
      "global_load_dwordx4 v[180:183], v250, s[76:77] offset:1024 sc1\n\t"
      "global_load_dwordx4 v[184:187], v250, s[76:77] offset:2048 sc1\n\t"
      "global_load_dwordx4 v[188:191], v250, s[76:77] offset:3072 sc1\n\t"
      "s_waitcnt vmcnt(0)\n\t"
      "v_max3_u32 v251, v160, v161, v162\n\t"
      "v_max3_u32 v251, v251, v163, v164\n\t"
      "v_max3_u32 v251, v251, v165, v166\n\t"
      "v_max3_u32 v251, v251, v167, v168\n\t"
      "v_max3_u32 v251, v251, v169, v170\n\t"
      "v_max3_u32 v251, v251, v171, v172\n\t"
      "v_max3_u32 v251, v251, v173, v174\n\t"
      "v_max3_u32 v251, v251, v175, v176\n\t"
      "v_max3_u32 v251, v251, v177, v178\n\t"
      "v_max3_u32 v251, v251, v179, v180\n\t"
      "v_max3_u32 v251, v251, v181, v182\n\t"
      "v_max3_u32 v251, v251, v183, v184\n\t"
      "v_max3_u32 v251, v251, v185, v186\n\t"
      "v_max3_u32 v251, v251, v187, v188\n\t"
      "v_max3_u32 v251, v251, v189, v190\n\t"
      "v_max_u32_e32 v251, v251, v191\n\t"
      "v_cmp_eq_u32_e32 vcc, -1, v251\n\t"
      "s_cbranch_vccnz LA36L_%=\n\t"
      "LA36X_%=:\n\t"
      "v_mfma_f32_16x16x32_bf16 v[224:227], v[0:3], v[160:163], 0\n\t"
      "v_mfma_f32_16x16x32_bf16 v[224:227], v[16:19], v[164:167], v[224:227]\n\t"
      "v_mfma_f32_16x16x32_bf16 v[224:227], v[32:35], v[168:171], v[224:227]\n\t"
      "v_mfma_f32_16x16x32_bf16 v[224:227], v[48:51], v[172:175], v[224:227]\n\t"
      "v_mfma_f32_16x16x32_bf16 v[224:227], v[64:67], v[176:179], v[224:227]\n\t"
      "v_mfma_f32_16x16x32_bf16 v[224:227], v[80:83], v[180:183], v[224:227]\n\t"
      "v_mfma_f32_16x16x32_bf16 v[224:227], v[96:99], v[184:187], v[224:227]\n\t"
      "v_mfma_f32_16x16x32_bf16 v[224:227], v[112:115], v[188:191], v[224:227]\n\t"
      "v_mfma_f32_16x16x32_bf16 v[208:211], v[4:7], v[160:163], 0\n\t"
      "v_mfma_f32_16x16x32_bf16 v[208:211], v[20:23], v[164:167], v[208:211]\n\t"
      "v_mfma_f32_16x16x32_bf16 v[208:211], v[36:39], v[168:171], v[208:211]\n\t"
      "s_nop 7\n\t"
      "s_branch LA36B_%=\n\t"
      "LF5R_%=:\n\t"
      "s_mov_b32 s80, 0\n\t"
      "LF5L_%=:\n\t"
      "s_add_u32 s80, s80, 1\n\t"
      "s_cmp_lt_u32 s80, 262144\n\t"
      "s_cbranch_scc0 LF5X_%=\n\t"
      "s_sleep 2\n\t"
      "global_load_dwordx2 v[138:139], v143, s[60:61] sc1\n\t"
      "s_waitcnt vmcnt(0)\n\t"
      "v_cmp_ne_u64_e32 vcc, s[22:23], v[138:139]\n\t"
      "s_cbranch_vccnz LF5L_%=\n\t"
      "LF5X_%=:\n\t"
      "s_branch LF5B_%=\n\t"
      "PG0R_%=:\n\t"
      "s_mov_b32 s80, 0\n\t"
      "PG0L_%=:\n\t"
      "s_add_u32 s80, s80, 1\n\t"
      "s_cmp_lt_u32 s80, 262144\n\t"
      "s_cbranch_scc0 PG0X_%=\n\t"
      "s_sleep 2\n\t"
      "global_load_dword v251, v253, s[64:65] offset:252 sc1\n\t"
      "s_waitcnt vmcnt(0)\n\t"
      "v_cmp_eq_u32_e32 vcc, -1, v251\n\t"
      "s_cbranch_vccnz PG0L_%=\n\t"
      "PG0X_%=:\n\t"
      "s_branch PG0B_%=\n\t"
      "LF8R_%=:\n\t"
      "s_mov_b32 s80, 0\n\t"
      "LF8L_%=:\n\t"
      "s_add_u32 s80, s80, 1\n\t"
      "s_cmp_lt_u32 s80, 262144\n\t"
      "s_cbranch_scc0 LF8X_%=\n\t"
      "s_sleep 2\n\t"
      "global_load_dwordx2 v[240:241], v242, s[60:61] sc1\n\t"
      "s_waitcnt vmcnt(0)\n\t"
      "v_cmp_ne_u64_e32 vcc, s[22:23], v[240:241]\n\t"
      "s_cbranch_vccnz LF8L_%=\n\t"
      "LF8X_%=:\n\t"
      "s_branch LF8B_%=\n\t"
      "LA40R_%=:\n\t"
      "s_mov_b32 s80, 0\n\t"
      "LA40L_%=:\n\t"
      "s_add_u32 s80, s80, 1\n\t"
      "s_cmp_lt_u32 s80, 262144\n\t"
      "s_cbranch_scc0 LA40X_%=\n\t"
      "s_waitcnt vmcnt(0)\n\t"
      "PQ0R_%=:\n\t"
      "PQ0L_%=:\n\t"
      "s_add_u32 s80, s80, 1\n\t"
      "s_cmp_lt_u32 s80, 262144\n\t"
      "s_cbranch_scc0 PQ0X_%=\n\t"
      "s_sleep 1\n\t"
      "global_load_dword v251, v252, s[64:65] offset:252 sc1\n\t"
      "s_waitcnt vmcnt(0)\n\t"
      "v_cmp_eq_u32_e32 vcc, -1, v251\n\t"
      "s_cbranch_vccnz PQ0L_%=\n\t"
      "PQ0X_%=:\n\t"
      "s_add_u32 s58, s64, s40\n\t"
      "s_addc_u32 s59, s65, 0\n\t"
      "global_load_dwordx4 v[128:131], v244, s[58:59] sc1\n\t"
      "s_add_u32 s58, s64, s41\n\t"
      "s_addc_u32 s59, s65, 0\n\t"
      "global_load_dwordx4 v[132:135], v244, s[58:59] sc1\n\t"
      "s_add_u32 s58, s64, s42\n\t"
      "s_addc_u32 s59, s65, 0\n\t"
      "global_load_dwordx4 v[136:139], v244, s[58:59] sc1\n\t"
      "s_add_u32 s58, s64, s43\n\t"
      "s_addc_u32 s59, s65, 0\n\t"
      "global_load_dwordx4 v[140:143], v244, s[58:59] sc1\n\t"
      "s_add_u32 s58, s64, s44\n\t"
      "s_addc_u32 s59, s65, 0\n\t"
      "global_load_dwordx4 v[144:147], v244, s[58:59] sc1\n\t"
      "s_add_u32 s58, s64, s45\n\t"
      "s_addc_u32 s59, s65, 0\n\t"
      "global_load_dwordx4 v[148:151], v244, s[58:59] sc1\n\t"
      "s_add_u32 s58, s64, s46\n\t"
      "s_addc_u32 s59, s65, 0\n\t"
      "global_load_dwordx4 v[152:155], v244, s[58:59] sc1\n\t"
      "s_waitcnt vmcnt(0)\n\t"
      "v_max3_u32 v251, v128, v129, v130\n\t"
      "v_max3_u32 v251, v251, v131, v132\n\t"
      "v_max3_u32 v251, v251, v133, v134\n\t"
      "v_max3_u32 v251, v251, v135, v136\n\t"
      "v_max3_u32 v251, v251, v137, v138\n\t"
      "v_max3_u32 v251, v251, v139, v140\n\t"
      "v_max3_u32 v251, v251, v141, v142\n\t"
      "v_max3_u32 v251, v251, v143, v144\n\t"
      "v_max3_u32 v251, v251, v145, v146\n\t"
      "v_max3_u32 v251, v251, v147, v148\n\t"
      "v_max3_u32 v251, v251, v149, v150\n\t"
      "v_max3_u32 v251, v251, v151, v152\n\t"
      "v_max3_u32 v251, v251, v153, v154\n\t"
      "v_max_u32_e32 v251, v251, v155\n\t"
      "v_cmp_eq_u32_e32 vcc, -1, v251\n\t"
      "s_cbranch_vccnz LA40L_%=\n\t"
      "LA40X_%=:\n\t"
      "v_mfma_f32_16x16x32_bf16 v[196:199], v[128:131], v[0:3], 0\n\t"
      "v_mfma_f32_16x16x32_bf16 v[200:203], v[128:131], v[4:7], 0\n\t"
      "v_mfma_f32_16x16x32_bf16 v[204:207], v[128:131], v[8:11], 0\n\t"
      "v_mfma_f32_16x16x32_bf16 v[208:211], v[128:131], v[12:15], 0\n\t"
      "v_mfma_f32_16x16x32_bf16 v[196:199], v[132:135], v[16:19], v[196:199]\n\t"
      "v_mfma_f32_16x16x32_bf16 v[200:203], v[132:135], v[20:23], v[200:203]\n\t"
      "v_mfma_f32_16x16x32_bf16 v[204:207], v[132:135], v[24:27], v[204:207]\n\t"
      "v_mfma_f32_16x16x32_bf16 v[208:211], v[132:135], v[28:31], v[208:211]\n\t"
      "v_mfma_f32_16x16x32_bf16 v[196:199], v[136:139], v[32:35], v[196:199]\n\t"
      "v_mfma_f32_16x16x32_bf16 v[200:203], v[136:139], v[36:39], v[200:203]\n\t"
      "v_mfma_f32_16x16x32_bf16 v[204:207], v[136:139], v[40:43], v[204:207]\n\t"
      "v_mfma_f32_16x16x32_bf16 v[208:211], v[136:139], v[44:47], v[208:211]\n\t"
      "v_mfma_f32_16x16x32_bf16 v[196:199], v[140:143], v[48:51], v[196:199]\n\t"
      "v_mfma_f32_16x16x32_bf16 v[200:203], v[140:143], v[52:55], v[200:203]\n\t"
      "v_mfma_f32_16x16x32_bf16 v[204:207], v[140:143], v[56:59], v[204:207]\n\t"
      "v_mfma_f32_16x16x32_bf16 v[208:211], v[140:143], v[60:63], v[208:211]\n\t"
      "v_mfma_f32_16x16x32_bf16 v[196:199], v[144:147], v[64:67], v[196:199]\n\t"
      "v_mfma_f32_16x16x32_bf16 v[200:203], v[144:147], v[68:71], v[200:203]\n\t"
      "v_mfma_f32_16x16x32_bf16 v[204:207], v[144:147], v[72:75], v[204:207]\n\t"
      "v_mfma_f32_16x16x32_bf16 v[208:211], v[144:147], v[76:79], v[208:211]\n\t"
      "v_mfma_f32_16x16x32_bf16 v[196:199], v[148:151], v[80:83], v[196:199]\n\t"
      "v_mfma_f32_16x16x32_bf16 v[196:199], v[152:155], v[96:99], v[196:199]\n\t"
      "v_mfma_f32_16x16x32_bf16 v[200:203], v[148:151], v[84:87], v[200:203]\n\t"
      "v_mfma_f32_16x16x32_bf16 v[200:203], v[152:155], v[100:103], v[200:203]\n\t"
      "v_mfma_f32_16x16x32_bf16 v[204:207], v[148:151], v[88:91], v[204:207]\n\t"
      "s_nop 7\n\t"
      "s_branch LA40B_%=\n\t"
      "LA41R_%=:\n\t"
      "s_mov_b32 s80, 0\n\t"
      "LA41L_%=:\n\t"
      "s_add_u32 s80, s80, 1\n\t"
      "s_cmp_lt_u32 s80, 262144\n\t"
      "s_cbranch_scc0 LA41X_%=\n\t"
      "s_waitcnt vmcnt(0)\n\t"
      "PQ1R_%=:\n\t"
      "PQ1L_%=:\n\t"
      "s_add_u32 s80, s80, 1\n\t"
      "s_cmp_lt_u32 s80, 262144\n\t"
      "s_cbranch_scc0 PQ1X_%=\n\t"
      "s_sleep 1\n\t"
      "global_load_dword v251, v252, s[66:67] offset:252 sc1\n\t"
      "s_waitcnt vmcnt(0)\n\t"
      "v_cmp_eq_u32_e32 vcc, -1, v251\n\t"
      "s_cbranch_vccnz PQ1L_%=\n\t"
      "PQ1X_%=:\n\t"
      "s_add_u32 s58, s66, s40\n\t"
      "s_addc_u32 s59, s67, 0\n\t"
      "global_load_dwordx4 v[156:159], v244, s[58:59] sc1\n\t"
      "s_add_u32 s58, s66, s41\n\t"
      "s_addc_u32 s59, s67, 0\n\t"
      "global_load_dwordx4 v[160:163], v244, s[58:59] sc1\n\t"
      "s_add_u32 s58, s66, s42\n\t"
      "s_addc_u32 s59, s67, 0\n\t"
      "global_load_dwordx4 v[164:167], v244, s[58:59] sc1\n\t"
      "s_add_u32 s58, s66, s43\n\t"
      "s_addc_u32 s59, s67, 0\n\t"
      "global_load_dwordx4 v[168:171], v244, s[58:59] sc1\n\t"
      "s_add_u32 s58, s66, s44\n\t"
      "s_addc_u32 s59, s67, 0\n\t"
      "global_load_dwordx4 v[172:175], v244, s[58:59] sc1\n\t"
      "s_add_u32 s58, s66, s45\n\t"
      "s_addc_u32 s59, s67, 0\n\t"
      "global_load_dwordx4 v[176:179], v244, s[58:59] sc1\n\t"
      "s_add_u32 s58, s66, s46\n\t"
      "s_addc_u32 s59, s67, 0\n\t"
      "global_load_dwordx4 v[180:183], v244, s[58:59] sc1\n\t"
      "s_waitcnt vmcnt(0)\n\t"
      "v_max3_u32 v251, v156, v157, v158\n\t"
      "v_max3_u32 v251, v251, v159, v160\n\t"
      "v_max3_u32 v251, v251, v161, v162\n\t"
      "v_max3_u32 v251, v251, v163, v164\n\t"
      "v_max3_u32 v251, v251, v165, v166\n\t"
      "v_max3_u32 v251, v251, v167, v168\n\t"
      "v_max3_u32 v251, v251, v169, v170\n\t"
      "v_max3_u32 v251, v251, v171, v172\n\t"
      "v_max3_u32 v251, v251, v173, v174\n\t"
      "v_max3_u32 v251, v251, v175, v176\n\t"
      "v_max3_u32 v251, v251, v177, v178\n\t"
      "v_max3_u32 v251, v251, v179, v180\n\t"
      "v_max3_u32 v251, v251, v181, v182\n\t"
      "v_max_u32_e32 v251, v251, v183\n\t"
      "v_cmp_eq_u32_e32 vcc, -1, v251\n\t"
      "s_cbranch_vccnz LA41L_%=\n\t"
      "LA41X_%=:\n\t"
      "v_mfma_f32_16x16x32_bf16 v[212:215], v[156:159], v[0:3], 0\n\t"
      "v_mfma_f32_16x16x32_bf16 v[216:219], v[156:159], v[4:7], 0\n\t"
      "v_mfma_f32_16x16x32_bf16 v[220:223], v[156:159], v[8:11], 0\n\t"
      "v_mfma_f32_16x16x32_bf16 v[224:227], v[156:159], v[12:15], 0\n\t"
      "v_mfma_f32_16x16x32_bf16 v[212:215], v[160:163], v[16:19], v[212:215]\n\t"
      "v_mfma_f32_16x16x32_bf16 v[216:219], v[160:163], v[20:23], v[216:219]\n\t"
      "v_mfma_f32_16x16x32_bf16 v[220:223], v[160:163], v[24:27], v[220:223]\n\t"
      "v_mfma_f32_16x16x32_bf16 v[224:227], v[160:163], v[28:31], v[224:227]\n\t"
      "v_mfma_f32_16x16x32_bf16 v[212:215], v[164:167], v[32:35], v[212:215]\n\t"
      "v_mfma_f32_16x16x32_bf16 v[216:219], v[164:167], v[36:39], v[216:219]\n\t"
      "v_mfma_f32_16x16x32_bf16 v[220:223], v[164:167], v[40:43], v[220:223]\n\t"
      "v_mfma_f32_16x16x32_bf16 v[224:227], v[164:167], v[44:47], v[224:227]\n\t"
      "v_mfma_f32_16x16x32_bf16 v[212:215], v[168:171], v[48:51], v[212:215]\n\t"
      "v_mfma_f32_16x16x32_bf16 v[216:219], v[168:171], v[52:55], v[216:219]\n\t"
      "v_mfma_f32_16x16x32_bf16 v[220:223], v[168:171], v[56:59], v[220:223]\n\t"
      "v_mfma_f32_16x16x32_bf16 v[224:227], v[168:171], v[60:63], v[224:227]\n\t"
      "v_mfma_f32_16x16x32_bf16 v[212:215], v[172:175], v[64:67], v[212:215]\n\t"
      "v_mfma_f32_16x16x32_bf16 v[216:219], v[172:175], v[68:71], v[216:219]\n\t"
      "v_mfma_f32_16x16x32_bf16 v[220:223], v[172:175], v[72:75], v[220:223]\n\t"
      "v_mfma_f32_16x16x32_bf16 v[224:227], v[172:175], v[76:79], v[224:227]\n\t"
      "v_mfma_f32_16x16x32_bf16 v[212:215], v[176:179], v[80:83], v[212:215]\n\t"
      "v_mfma_f32_16x16x32_bf16 v[212:215], v[180:183], v[96:99], v[212:215]\n\t"
      "v_mfma_f32_16x16x32_bf16 v[216:219], v[176:179], v[84:87], v[216:219]\n\t"
      "v_mfma_f32_16x16x32_bf16 v[216:219], v[180:183], v[100:103], v[216:219]\n\t"
      "v_mfma_f32_16x16x32_bf16 v[220:223], v[176:179], v[88:91], v[220:223]\n\t"
      "s_nop 7\n\t"
      "s_branch LA41B_%=\n\t"
      "LA42R_%=:\n\t"
      "s_mov_b32 s80, 0\n\t"
      "LA42L_%=:\n\t"
      "s_add_u32 s80, s80, 1\n\t"
      "s_cmp_lt_u32 s80, 262144\n\t"
      "s_cbranch_scc0 LA42X_%=\n\t"
      "s_waitcnt vmcnt(0)\n\t"
      "PQ2R_%=:\n\t"
      "PQ2L_%=:\n\t"
      "s_add_u32 s80, s80, 1\n\t"
      "s_cmp_lt_u32 s80, 262144\n\t"
      "s_cbranch_scc0 PQ2X_%=\n\t"
      "s_sleep 1\n\t"
      "global_load_dword v251, v252, s[68:69] offset:252 sc1\n\t"
      "s_waitcnt vmcnt(0)\n\t"
      "v_cmp_eq_u32_e32 vcc, -1, v251\n\t"
      "s_cbranch_vccnz PQ2L_%=\n\t"
      "PQ2X_%=:\n\t"
      "s_add_u32 s58, s68, s40\n\t"
      "s_addc_u32 s59, s69, 0\n\t"
      "global_load_dwordx4 v[184:187], v244, s[58:59] sc1\n\t"
      "s_add_u32 s58, s68, s41\n\t"
      "s_addc_u32 s59, s69, 0\n\t"
      "global_load_dwordx4 v[188:191], v244, s[58:59] sc1\n\t"
      "s_add_u32 s58, s68, s42\n\t"
      "s_addc_u32 s59, s69, 0\n\t"
      "global_load_dwordx4 v[192:195], v244, s[58:59] sc1\n\t"
      "s_add_u32 s58, s68, s43\n\t"
      "s_addc_u32 s59, s69, 0\n\t"
      "global_load_dwordx4 v[128:131], v244, s[58:59] sc1\n\t"
      "s_add_u32 s58, s68, s44\n\t"
      "s_addc_u32 s59, s69, 0\n\t"
      "global_load_dwordx4 v[132:135], v244, s[58:59] sc1\n\t"
      "s_add_u32 s58, s68, s45\n\t"
      "s_addc_u32 s59, s69, 0\n\t"
      "global_load_dwordx4 v[136:139], v244, s[58:59] sc1\n\t"
      "s_add_u32 s58, s68, s46\n\t"
      "s_addc_u32 s59, s69, 0\n\t"
      "global_load_dwordx4 v[140:143], v244, s[58:59] sc1\n\t"
      "s_waitcnt vmcnt(0)\n\t"
      "v_max3_u32 v251, v184, v185, v186\n\t"
      "v_max3_u32 v251, v251, v187, v188\n\t"
      "v_max3_u32 v251, v251, v189, v190\n\t"
      "v_max3_u32 v251, v251, v191, v192\n\t"
      "v_max3_u32 v251, v251, v193, v194\n\t"
      "v_max3_u32 v251, v251, v195, v128\n\t"
      "v_max3_u32 v251, v251, v129, v130\n\t"
      "v_max3_u32 v251, v251, v131, v132\n\t"
      "v_max3_u32 v251, v251, v133, v134\n\t"
      "v_max3_u32 v251, v251, v135, v136\n\t"
      "v_max3_u32 v251, v251, v137, v138\n\t"
      "v_max3_u32 v251, v251, v139, v140\n\t"
      "v_max3_u32 v251, v251, v141, v142\n\t"
      "v_max_u32_e32 v251, v251, v143\n\t"
      "v_cmp_eq_u32_e32 vcc, -1, v251\n\t"
      "s_cbranch_vccnz LA42L_%=\n\t"
      "LA42X_%=:\n\t"
      "v_mfma_f32_16x16x32_bf16 v[196:199], v[184:187], v[0:3], 0\n\t"
      "v_mfma_f32_16x16x32_bf16 v[200:203], v[184:187], v[4:7], 0\n\t"
      "v_mfma_f32_16x16x32_bf16 v[204:207], v[184:187], v[8:11], 0\n\t"
      "v_mfma_f32_16x16x32_bf16 v[208:211], v[184:187], v[12:15], 0\n\t"
      "v_mfma_f32_16x16x32_bf16 v[196:199], v[188:191], v[16:19], v[196:199]\n\t"
      "v_mfma_f32_16x16x32_bf16 v[200:203], v[188:191], v[20:23], v[200:203]\n\t"
      "v_mfma_f32_16x16x32_bf16 v[204:207], v[188:191], v[24:27], v[204:207]\n\t"
      "v_mfma_f32_16x16x32_bf16 v[208:211], v[188:191], v[28:31], v[208:211]\n\t"
      "v_mfma_f32_16x16x32_bf16 v[196:199], v[192:195], v[32:35], v[196:199]\n\t"
      "v_mfma_f32_16x16x32_bf16 v[200:203], v[192:195], v[36:39], v[200:203]\n\t"
      "v_mfma_f32_16x16x32_bf16 v[204:207], v[192:195], v[40:43], v[204:207]\n\t"
      "v_mfma_f32_16x16x32_bf16 v[208:211], v[192:195], v[44:47], v[208:211]\n\t"
      "v_mfma_f32_16x16x32_bf16 v[196:199], v[128:131], v[48:51], v[196:199]\n\t"
      "v_mfma_f32_16x16x32_bf16 v[200:203], v[128:131], v[52:55], v[200:203]\n\t"
      "v_mfma_f32_16x16x32_bf16 v[204:207], v[128:131], v[56:59], v[204:207]\n\t"
      "v_mfma_f32_16x16x32_bf16 v[208:211], v[128:131], v[60:63], v[208:211]\n\t"
      "v_mfma_f32_16x16x32_bf16 v[196:199], v[132:135], v[64:67], v[196:199]\n\t"
      "v_mfma_f32_16x16x32_bf16 v[200:203], v[132:135], v[68:71], v[200:203]\n\t"
      "v_mfma_f32_16x16x32_bf16 v[204:207], v[132:135], v[72:75], v[204:207]\n\t"
      "v_mfma_f32_16x16x32_bf16 v[208:211], v[132:135], v[76:79], v[208:211]\n\t"
      "v_mfma_f32_16x16x32_bf16 v[196:199], v[136:139], v[80:83], v[196:199]\n\t"
      "v_mfma_f32_16x16x32_bf16 v[196:199], v[140:143], v[96:99], v[196:199]\n\t"
      "v_mfma_f32_16x16x32_bf16 v[200:203], v[136:139], v[84:87], v[200:203]\n\t"
      "v_mfma_f32_16x16x32_bf16 v[200:203], v[140:143], v[100:103], v[200:203]\n\t"
      "v_mfma_f32_16x16x32_bf16 v[204:207], v[136:139], v[88:91], v[204:207]\n\t"
      "s_nop 7\n\t"
      "s_branch LA42B_%=\n\t"
      "LA43R_%=:\n\t"
      "s_mov_b32 s80, 0\n\t"
      "LA43L_%=:\n\t"
      "s_add_u32 s80, s80, 1\n\t"
      "s_cmp_lt_u32 s80, 262144\n\t"
      "s_cbranch_scc0 LA43X_%=\n\t"
      "s_waitcnt vmcnt(0)\n\t"
      "PQ3R_%=:\n\t"
      "PQ3L_%=:\n\t"
      "s_add_u32 s80, s80, 1\n\t"
      "s_cmp_lt_u32 s80, 262144\n\t"
      "s_cbranch_scc0 PQ3X_%=\n\t"
      "s_sleep 1\n\t"
      "global_load_dword v251, v252, s[70:71] offset:252 sc1\n\t"
      "s_waitcnt vmcnt(0)\n\t"
      "v_cmp_eq_u32_e32 vcc, -1, v251\n\t"
      "s_cbranch_vccnz PQ3L_%=\n\t"
      "PQ3X_%=:\n\t"
      "s_add_u32 s58, s70, s40\n\t"
      "s_addc_u32 s59, s71, 0\n\t"
      "global_load_dwordx4 v[144:147], v244, s[58:59] sc1\n\t"
      "s_add_u32 s58, s70, s41\n\t"
      "s_addc_u32 s59, s71, 0\n\t"
      "global_load_dwordx4 v[148:151], v244, s[58:59] sc1\n\t"
      "s_add_u32 s58, s70, s42\n\t"
      "s_addc_u32 s59, s71, 0\n\t"
      "global_load_dwordx4 v[152:155], v244, s[58:59] sc1\n\t"
      "s_add_u32 s58, s70, s43\n\t"
      "s_addc_u32 s59, s71, 0\n\t"
      "global_load_dwordx4 v[156:159], v244, s[58:59] sc1\n\t"
      "s_add_u32 s58, s70, s44\n\t"
      "s_addc_u32 s59, s71, 0\n\t"
      "global_load_dwordx4 v[160:163], v244, s[58:59] sc1\n\t"
      "s_add_u32 s58, s70, s45\n\t"
      "s_addc_u32 s59, s71, 0\n\t"
      "global_load_dwordx4 v[164:167], v244, s[58:59] sc1\n\t"
      "s_add_u32 s58, s70, s46\n\t"
      "s_addc_u32 s59, s71, 0\n\t"
      "global_load_dwordx4 v[168:171], v244, s[58:59] sc1\n\t"
      "s_waitcnt vmcnt(0)\n\t"
      "v_max3_u32 v251, v144, v145, v146\n\t"
      "v_max3_u32 v251, v251, v147, v148\n\t"
      "v_max3_u32 v251, v251, v149, v150\n\t"
      "v_max3_u32 v251, v251, v151, v152\n\t"
      "v_max3_u32 v251, v251, v153, v154\n\t"
      "v_max3_u32 v251, v251, v155, v156\n\t"
      "v_max3_u32 v251, v251, v157, v158\n\t"
      "v_max3_u32 v251, v251, v159, v160\n\t"
      "v_max3_u32 v251, v251, v161, v162\n\t"
      "v_max3_u32 v251, v251, v163, v164\n\t"
      "v_max3_u32 v251, v251, v165, v166\n\t"
      "v_max3_u32 v251, v251, v167, v168\n\t"
      "v_max3_u32 v251, v251, v169, v170\n\t"
      "v_max_u32_e32 v251, v251, v171\n\t"
      "v_cmp_eq_u32_e32 vcc, -1, v251\n\t"
      "s_cbranch_vccnz LA43L_%=\n\t"
      "LA43X_%=:\n\t"
      "v_mfma_f32_16x16x32_bf16 v[212:215], v[144:147], v[0:3], 0\n\t"
      "v_mfma_f32_16x16x32_bf16 v[216:219], v[144:147], v[4:7], 0\n\t"
      "v_mfma_f32_16x16x32_bf16 v[220:223], v[144:147], v[8:11], 0\n\t"
      "v_mfma_f32_16x16x32_bf16 v[224:227], v[144:147], v[12:15], 0\n\t"
      "v_mfma_f32_16x16x32_bf16 v[212:215], v[148:151], v[16:19], v[212:215]\n\t"
      "v_mfma_f32_16x16x32_bf16 v[216:219], v[148:151], v[20:23], v[216:219]\n\t"
      "v_mfma_f32_16x16x32_bf16 v[220:223], v[148:151], v[24:27], v[220:223]\n\t"
      "v_mfma_f32_16x16x32_bf16 v[224:227], v[148:151], v[28:31], v[224:227]\n\t"
      "v_mfma_f32_16x16x32_bf16 v[212:215], v[152:155], v[32:35], v[212:215]\n\t"
      "v_mfma_f32_16x16x32_bf16 v[216:219], v[152:155], v[36:39], v[216:219]\n\t"
      "v_mfma_f32_16x16x32_bf16 v[220:223], v[152:155], v[40:43], v[220:223]\n\t"
      "v_mfma_f32_16x16x32_bf16 v[224:227], v[152:155], v[44:47], v[224:227]\n\t"
      "v_mfma_f32_16x16x32_bf16 v[212:215], v[156:159], v[48:51], v[212:215]\n\t"
      "v_mfma_f32_16x16x32_bf16 v[216:219], v[156:159], v[52:55], v[216:219]\n\t"
      "v_mfma_f32_16x16x32_bf16 v[220:223], v[156:159], v[56:59], v[220:223]\n\t"
      "v_mfma_f32_16x16x32_bf16 v[224:227], v[156:159], v[60:63], v[224:227]\n\t"
      "v_mfma_f32_16x16x32_bf16 v[212:215], v[160:163], v[64:67], v[212:215]\n\t"
      "v_mfma_f32_16x16x32_bf16 v[216:219], v[160:163], v[68:71], v[216:219]\n\t"
      "v_mfma_f32_16x16x32_bf16 v[220:223], v[160:163], v[72:75], v[220:223]\n\t"
      "v_mfma_f32_16x16x32_bf16 v[224:227], v[160:163], v[76:79], v[224:227]\n\t"
      "v_mfma_f32_16x16x32_bf16 v[212:215], v[164:167], v[80:83], v[212:215]\n\t"
      "v_mfma_f32_16x16x32_bf16 v[212:215], v[168:171], v[96:99], v[212:215]\n\t"
      "v_mfma_f32_16x16x32_bf16 v[216:219], v[164:167], v[84:87], v[216:219]\n\t"
      "v_mfma_f32_16x16x32_bf16 v[216:219], v[168:171], v[100:103], v[216:219]\n\t"
      "v_mfma_f32_16x16x32_bf16 v[220:223], v[164:167], v[88:91], v[220:223]\n\t"
      "s_nop 7\n\t"
      "s_branch LA43B_%=\n\t"
      "LA44R_%=:\n\t"
      "s_mov_b32 s80, 0\n\t"
      "LA44L_%=:\n\t"
      "s_add_u32 s80, s80, 1\n\t"
      "s_cmp_lt_u32 s80, 262144\n\t"
      "s_cbranch_scc0 LA44X_%=\n\t"
      "s_waitcnt vmcnt(0)\n\t"
      "PQ4R_%=:\n\t"
      "PQ4L_%=:\n\t"
      "s_add_u32 s80, s80, 1\n\t"
      "s_cmp_lt_u32 s80, 262144\n\t"
      "s_cbranch_scc0 PQ4X_%=\n\t"
      "s_sleep 1\n\t"
      "global_load_dword v251, v252, s[72:73] offset:252 sc1\n\t"
      "s_waitcnt vmcnt(0)\n\t"
      "v_cmp_eq_u32_e32 vcc, -1, v251\n\t"
      "s_cbranch_vccnz PQ4L_%=\n\t"
      "PQ4X_%=:\n\t"
      "s_add_u32 s58, s72, s40\n\t"
      "s_addc_u32 s59, s73, 0\n\t"
      "global_load_dwordx4 v[172:175], v244, s[58:59] sc1\n\t"
      "s_add_u32 s58, s72, s41\n\t"
      "s_addc_u32 s59, s73, 0\n\t"
      "global_load_dwordx4 v[176:179], v244, s[58:59] sc1\n\t"
      "s_add_u32 s58, s72, s42\n\t"
      "s_addc_u32 s59, s73, 0\n\t"
      "global_load_dwordx4 v[180:183], v244, s[58:59] sc1\n\t"
      "s_add_u32 s58, s72, s43\n\t"
      "s_addc_u32 s59, s73, 0\n\t"
      "global_load_dwordx4 v[184:187], v244, s[58:59] sc1\n\t"
      "s_add_u32 s58, s72, s44\n\t"
      "s_addc_u32 s59, s73, 0\n\t"
      "global_load_dwordx4 v[188:191], v244, s[58:59] sc1\n\t"
      "s_add_u32 s58, s72, s45\n\t"
      "s_addc_u32 s59, s73, 0\n\t"
      "global_load_dwordx4 v[192:195], v244, s[58:59] sc1\n\t"
      "s_add_u32 s58, s72, s46\n\t"
      "s_addc_u32 s59, s73, 0\n\t"
      "global_load_dwordx4 v[128:131], v244, s[58:59] sc1\n\t"
      "s_waitcnt vmcnt(0)\n\t"
      "v_max3_u32 v251, v172, v173, v174\n\t"
      "v_max3_u32 v251, v251, v175, v176\n\t"
      "v_max3_u32 v251, v251, v177, v178\n\t"
      "v_max3_u32 v251, v251, v179, v180\n\t"
      "v_max3_u32 v251, v251, v181, v182\n\t"
      "v_max3_u32 v251, v251, v183, v184\n\t"
      "v_max3_u32 v251, v251, v185, v186\n\t"
      "v_max3_u32 v251, v251, v187, v188\n\t"
      "v_max3_u32 v251, v251, v189, v190\n\t"
      "v_max3_u32 v251, v251, v191, v192\n\t"
      "v_max3_u32 v251, v251, v193, v194\n\t"
      "v_max3_u32 v251, v251, v195, v128\n\t"
      "v_max3_u32 v251, v251, v129, v130\n\t"
      "v_max_u32_e32 v251, v251, v131\n\t"
      "v_cmp_eq_u32_e32 vcc, -1, v251\n\t"
      "s_cbranch_vccnz LA44L_%=\n\t"
      "LA44X_%=:\n\t"
      "v_mfma_f32_16x16x32_bf16 v[196:199], v[172:175], v[0:3], 0\n\t"
      "v_mfma_f32_16x16x32_bf16 v[200:203], v[172:175], v[4:7], 0\n\t"
      "v_mfma_f32_16x16x32_bf16 v[204:207], v[172:175], v[8:11], 0\n\t"
      "v_mfma_f32_16x16x32_bf16 v[208:211], v[172:175], v[12:15], 0\n\t"
      "v_mfma_f32_16x16x32_bf16 v[196:199], v[176:179], v[16:19], v[196:199]\n\t"
      "v_mfma_f32_16x16x32_bf16 v[200:203], v[176:179], v[20:23], v[200:203]\n\t"
      "v_mfma_f32_16x16x32_bf16 v[204:207], v[176:179], v[24:27], v[204:207]\n\t"
      "v_mfma_f32_16x16x32_bf16 v[208:211], v[176:179], v[28:31], v[208:211]\n\t"
      "v_mfma_f32_16x16x32_bf16 v[196:199], v[180:183], v[32:35], v[196:199]\n\t"
      "v_mfma_f32_16x16x32_bf16 v[200:203], v[180:183], v[36:39], v[200:203]\n\t"
      "v_mfma_f32_16x16x32_bf16 v[204:207], v[180:183], v[40:43], v[204:207]\n\t"
      "v_mfma_f32_16x16x32_bf16 v[208:211], v[180:183], v[44:47], v[208:211]\n\t"
      "v_mfma_f32_16x16x32_bf16 v[196:199], v[184:187], v[48:51], v[196:199]\n\t"
      "v_mfma_f32_16x16x32_bf16 v[200:203], v[184:187], v[52:55], v[200:203]\n\t"
      "v_mfma_f32_16x16x32_bf16 v[204:207], v[184:187], v[56:59], v[204:207]\n\t"
      "v_mfma_f32_16x16x32_bf16 v[208:211], v[184:187], v[60:63], v[208:211]\n\t"
      "v_mfma_f32_16x16x32_bf16 v[196:199], v[188:191], v[64:67], v[196:199]\n\t"
      "v_mfma_f32_16x16x32_bf16 v[200:203], v[188:191], v[68:71], v[200:203]\n\t"
      "v_mfma_f32_16x16x32_bf16 v[204:207], v[188:191], v[72:75], v[204:207]\n\t"
      "v_mfma_f32_16x16x32_bf16 v[208:211], v[188:191], v[76:79], v[208:211]\n\t"
      "v_mfma_f32_16x16x32_bf16 v[196:199], v[192:195], v[80:83], v[196:199]\n\t"
      "v_mfma_f32_16x16x32_bf16 v[196:199], v[128:131], v[96:99], v[196:199]\n\t"
      "v_mfma_f32_16x16x32_bf16 v[200:203], v[192:195], v[84:87], v[200:203]\n\t"
      "v_mfma_f32_16x16x32_bf16 v[200:203], v[128:131], v[100:103], v[200:203]\n\t"
      "v_mfma_f32_16x16x32_bf16 v[204:207], v[192:195], v[88:91], v[204:207]\n\t"
      "s_nop 7\n\t"
      "s_branch LA44B_%=\n\t"
      "LA45R_%=:\n\t"
      "s_mov_b32 s80, 0\n\t"
      "LA45L_%=:\n\t"
      "s_add_u32 s80, s80, 1\n\t"
      "s_cmp_lt_u32 s80, 262144\n\t"
      "s_cbranch_scc0 LA45X_%=\n\t"
      "s_waitcnt vmcnt(0)\n\t"
      "PQ5R_%=:\n\t"
      "PQ5L_%=:\n\t"
      "s_add_u32 s80, s80, 1\n\t"
      "s_cmp_lt_u32 s80, 262144\n\t"
      "s_cbranch_scc0 PQ5X_%=\n\t"
      "s_sleep 1\n\t"
      "global_load_dword v251, v252, s[74:75] offset:252 sc1\n\t"
      "s_waitcnt vmcnt(0)\n\t"
      "v_cmp_eq_u32_e32 vcc, -1, v251\n\t"
      "s_cbranch_vccnz PQ5L_%=\n\t"
      "PQ5X_%=:\n\t"
      "s_add_u32 s58, s74, s40\n\t"
      "s_addc_u32 s59, s75, 0\n\t"
      "global_load_dwordx4 v[132:135], v244, s[58:59] sc1\n\t"
      "s_add_u32 s58, s74, s41\n\t"
      "s_addc_u32 s59, s75, 0\n\t"
      "global_load_dwordx4 v[136:139], v244, s[58:59] sc1\n\t"
      "s_add_u32 s58, s74, s42\n\t"
      "s_addc_u32 s59, s75, 0\n\t"
      "global_load_dwordx4 v[140:143], v244, s[58:59] sc1\n\t"
      "s_add_u32 s58, s74, s43\n\t"
      "s_addc_u32 s59, s75, 0\n\t"
      "global_load_dwordx4 v[144:147], v244, s[58:59] sc1\n\t"
      "s_add_u32 s58, s74, s44\n\t"
      "s_addc_u32 s59, s75, 0\n\t"
      "global_load_dwordx4 v[148:151], v244, s[58:59] sc1\n\t"
      "s_add_u32 s58, s74, s45\n\t"
      "s_addc_u32 s59, s75, 0\n\t"
      "global_load_dwordx4 v[152:155], v244, s[58:59] sc1\n\t"
      "s_add_u32 s58, s74, s46\n\t"
      "s_addc_u32 s59, s75, 0\n\t"
      "global_load_dwordx4 v[156:159], v244, s[58:59] sc1\n\t"
      "s_waitcnt vmcnt(0)\n\t"
      "v_max3_u32 v251, v132, v133, v134\n\t"
      "v_max3_u32 v251, v251, v135, v136\n\t"
      "v_max3_u32 v251, v251, v137, v138\n\t"
      "v_max3_u32 v251, v251, v139, v140\n\t"
      "v_max3_u32 v251, v251, v141, v142\n\t"
      "v_max3_u32 v251, v251, v143, v144\n\t"
      "v_max3_u32 v251, v251, v145, v146\n\t"
      "v_max3_u32 v251, v251, v147, v148\n\t"
      "v_max3_u32 v251, v251, v149, v150\n\t"
      "v_max3_u32 v251, v251, v151, v152\n\t"
      "v_max3_u32 v251, v251, v153, v154\n\t"
      "v_max3_u32 v251, v251, v155, v156\n\t"
      "v_max3_u32 v251, v251, v157, v158\n\t"
      "v_max_u32_e32 v251, v251, v159\n\t"
      "v_cmp_eq_u32_e32 vcc, -1, v251\n\t"
      "s_cbranch_vccnz LA45L_%=\n\t"
      "LA45X_%=:\n\t"
      "v_mfma_f32_16x16x32_bf16 v[212:215], v[132:135], v[0:3], 0\n\t"
      "v_mfma_f32_16x16x32_bf16 v[216:219], v[132:135], v[4:7], 0\n\t"
      "v_mfma_f32_16x16x32_bf16 v[220:223], v[132:135], v[8:11], 0\n\t"
      "v_mfma_f32_16x16x32_bf16 v[224:227], v[132:135], v[12:15], 0\n\t"
      "v_mfma_f32_16x16x32_bf16 v[212:215], v[136:139], v[16:19], v[212:215]\n\t"
      "v_mfma_f32_16x16x32_bf16 v[216:219], v[136:139], v[20:23], v[216:219]\n\t"
      "v_mfma_f32_16x16x32_bf16 v[220:223], v[136:139], v[24:27], v[220:223]\n\t"
      "v_mfma_f32_16x16x32_bf16 v[224:227], v[136:139], v[28:31], v[224:227]\n\t"
      "v_mfma_f32_16x16x32_bf16 v[212:215], v[140:143], v[32:35], v[212:215]\n\t"
      "v_mfma_f32_16x16x32_bf16 v[216:219], v[140:143], v[36:39], v[216:219]\n\t"
      "v_mfma_f32_16x16x32_bf16 v[220:223], v[140:143], v[40:43], v[220:223]\n\t"
      "v_mfma_f32_16x16x32_bf16 v[224:227], v[140:143], v[44:47], v[224:227]\n\t"
      "v_mfma_f32_16x16x32_bf16 v[212:215], v[144:147], v[48:51], v[212:215]\n\t"
      "v_mfma_f32_16x16x32_bf16 v[216:219], v[144:147], v[52:55], v[216:219]\n\t"
      "v_mfma_f32_16x16x32_bf16 v[220:223], v[144:147], v[56:59], v[220:223]\n\t"
      "v_mfma_f32_16x16x32_bf16 v[224:227], v[144:147], v[60:63], v[224:227]\n\t"
      "v_mfma_f32_16x16x32_bf16 v[212:215], v[148:151], v[64:67], v[212:215]\n\t"
      "v_mfma_f32_16x16x32_bf16 v[216:219], v[148:151], v[68:71], v[216:219]\n\t"
      "v_mfma_f32_16x16x32_bf16 v[220:223], v[148:151], v[72:75], v[220:223]\n\t"
      "v_mfma_f32_16x16x32_bf16 v[224:227], v[148:151], v[76:79], v[224:227]\n\t"
      "v_mfma_f32_16x16x32_bf16 v[212:215], v[152:155], v[80:83], v[212:215]\n\t"
      "v_mfma_f32_16x16x32_bf16 v[212:215], v[156:159], v[96:99], v[212:215]\n\t"
      "v_mfma_f32_16x16x32_bf16 v[216:219], v[152:155], v[84:87], v[216:219]\n\t"
      "v_mfma_f32_16x16x32_bf16 v[216:219], v[156:159], v[100:103], v[216:219]\n\t"
      "v_mfma_f32_16x16x32_bf16 v[220:223], v[152:155], v[88:91], v[220:223]\n\t"
      "s_nop 7\n\t"
      "s_branch LA45B_%=\n\t"
      "LA46R_%=:\n\t"
      "s_mov_b32 s80, 0\n\t"
      "LA46L_%=:\n\t"
      "s_add_u32 s80, s80, 1\n\t"
      "s_cmp_lt_u32 s80, 262144\n\t"
      "s_cbranch_scc0 LA46X_%=\n\t"
      "s_waitcnt vmcnt(0)\n\t"
      "PQ6R_%=:\n\t"
      "PQ6L_%=:\n\t"
      "s_add_u32 s80, s80, 1\n\t"
      "s_cmp_lt_u32 s80, 262144\n\t"
      "s_cbranch_scc0 PQ6X_%=\n\t"
      "s_sleep 1\n\t"
      "global_load_dword v251, v252, s[76:77] offset:252 sc1\n\t"
      "s_waitcnt vmcnt(0)\n\t"
      "v_cmp_eq_u32_e32 vcc, -1, v251\n\t"
      "s_cbranch_vccnz PQ6L_%=\n\t"
      "PQ6X_%=:\n\t"
      "s_add_u32 s58, s76, s40\n\t"
      "s_addc_u32 s59, s77, 0\n\t"
      "global_load_dwordx4 v[160:163], v244, s[58:59] sc1\n\t"
      "s_add_u32 s58, s76, s41\n\t"
      "s_addc_u32 s59, s77, 0\n\t"
      "global_load_dwordx4 v[164:167], v244, s[58:59] sc1\n\t"
      "s_add_u32 s58, s76, s42\n\t"
      "s_addc_u32 s59, s77, 0\n\t"
      "global_load_dwordx4 v[168:171], v244, s[58:59] sc1\n\t"
      "s_add_u32 s58, s76, s43\n\t"
      "s_addc_u32 s59, s77, 0\n\t"
      "global_load_dwordx4 v[172:175], v244, s[58:59] sc1\n\t"
      "s_add_u32 s58, s76, s44\n\t"
      "s_addc_u32 s59, s77, 0\n\t"
      "global_load_dwordx4 v[176:179], v244, s[58:59] sc1\n\t"
      "s_add_u32 s58, s76, s45\n\t"
      "s_addc_u32 s59, s77, 0\n\t"
      "global_load_dwordx4 v[180:183], v244, s[58:59] sc1\n\t"
      "s_add_u32 s58, s76, s46\n\t"
      "s_addc_u32 s59, s77, 0\n\t"
      "global_load_dwordx4 v[184:187], v244, s[58:59] sc1\n\t"
      "s_waitcnt vmcnt(0)\n\t"
      "v_max3_u32 v251, v160, v161, v162\n\t"
      "v_max3_u32 v251, v251, v163, v164\n\t"
      "v_max3_u32 v251, v251, v165, v166\n\t"
      "v_max3_u32 v251, v251, v167, v168\n\t"
      "v_max3_u32 v251, v251, v169, v170\n\t"
      "v_max3_u32 v251, v251, v171, v172\n\t"
      "v_max3_u32 v251, v251, v173, v174\n\t"
      "v_max3_u32 v251, v251, v175, v176\n\t"
      "v_max3_u32 v251, v251, v177, v178\n\t"
      "v_max3_u32 v251, v251, v179, v180\n\t"
      "v_max3_u32 v251, v251, v181, v182\n\t"
      "v_max3_u32 v251, v251, v183, v184\n\t"
      "v_max3_u32 v251, v251, v185, v186\n\t"
      "v_max_u32_e32 v251, v251, v187\n\t"
      "v_cmp_eq_u32_e32 vcc, -1, v251\n\t"
      "s_cbranch_vccnz LA46L_%=\n\t"
      "LA46X_%=:\n\t"
      "v_mfma_f32_16x16x32_bf16 v[196:199], v[160:163], v[0:3], 0\n\t"
      "v_mfma_f32_16x16x32_bf16 v[200:203], v[160:163], v[4:7], 0\n\t"
      "v_mfma_f32_16x16x32_bf16 v[204:207], v[160:163], v[8:11], 0\n\t"
      "v_mfma_f32_16x16x32_bf16 v[208:211], v[160:163], v[12:15], 0\n\t"
      "v_mfma_f32_16x16x32_bf16 v[196:199], v[164:167], v[16:19], v[196:199]\n\t"
      "v_mfma_f32_16x16x32_bf16 v[200:203], v[164:167], v[20:23], v[200:203]\n\t"
      "v_mfma_f32_16x16x32_bf16 v[204:207], v[164:167], v[24:27], v[204:207]\n\t"
      "v_mfma_f32_16x16x32_bf16 v[208:211], v[164:167], v[28:31], v[208:211]\n\t"
      "v_mfma_f32_16x16x32_bf16 v[196:199], v[168:171], v[32:35], v[196:199]\n\t"
      "v_mfma_f32_16x16x32_bf16 v[200:203], v[168:171], v[36:39], v[200:203]\n\t"
      "v_mfma_f32_16x16x32_bf16 v[204:207], v[168:171], v[40:43], v[204:207]\n\t"
      "v_mfma_f32_16x16x32_bf16 v[208:211], v[168:171], v[44:47], v[208:211]\n\t"
      "v_mfma_f32_16x16x32_bf16 v[196:199], v[172:175], v[48:51], v[196:199]\n\t"
      "v_mfma_f32_16x16x32_bf16 v[200:203], v[172:175], v[52:55], v[200:203]\n\t"
      "v_mfma_f32_16x16x32_bf16 v[204:207], v[172:175], v[56:59], v[204:207]\n\t"
      "v_mfma_f32_16x16x32_bf16 v[208:211], v[172:175], v[60:63], v[208:211]\n\t"
      "v_mfma_f32_16x16x32_bf16 v[196:199], v[176:179], v[64:67], v[196:199]\n\t"
      "v_mfma_f32_16x16x32_bf16 v[200:203], v[176:179], v[68:71], v[200:203]\n\t"
      "v_mfma_f32_16x16x32_bf16 v[204:207], v[176:179], v[72:75], v[204:207]\n\t"
      "v_mfma_f32_16x16x32_bf16 v[208:211], v[176:179], v[76:79], v[208:211]\n\t"
      "v_mfma_f32_16x16x32_bf16 v[196:199], v[180:183], v[80:83], v[196:199]\n\t"
      "v_mfma_f32_16x16x32_bf16 v[196:199], v[184:187], v[96:99], v[196:199]\n\t"
      "v_mfma_f32_16x16x32_bf16 v[200:203], v[180:183], v[84:87], v[200:203]\n\t"
      "v_mfma_f32_16x16x32_bf16 v[200:203], v[184:187], v[100:103], v[200:203]\n\t"
      "v_mfma_f32_16x16x32_bf16 v[204:207], v[180:183], v[88:91], v[204:207]\n\t"
      "s_nop 7\n\t"
      "s_branch LA46B_%=\n\t"
      : "+{s[0:1]}"(ka), "+{v0}"(tid)
      : "{s2}"(blockIdx.x)
      : "memory", "vcc", "scc", "m0",
        "v1", "v2", "v3", "v4", "v5", "v6", "v7", "v8", "v9", "v10", "v11", "v12", "v13", "v14", "v15", "v16", "v17",
        "v18", "v19", "v20", "v21", "v22", "v23", "v24", "v25", "v26", "v27", "v28", "v29", "v30", "v31", "v32", "v33",
        "v34", "v35", "v36", "v37", "v38", "v39", "v40", "v41", "v42", "v43", "v44", "v45", "v46", "v47", "v48", "v49",
        "v50", "v51", "v52", "v53", "v54", "v55", "v56", "v57", "v58", "v59", "v60", "v61", "v62", "v63", "v64", "v65",
        "v66", "v67", "v68", "v69", "v70", "v71", "v72", "v73", "v74", "v75", "v76", "v77", "v78", "v79", "v80", "v81",
        "v82", "v83", "v84", "v85", "v86", "v87", "v88", "v89", "v90", "v91", "v92", "v93", "v94", "v95", "v96", "v97",
        "v98", "v99", "v100", "v101", "v102", "v103", "v104", "v105", "v106", "v107", "v108", "v109", "v110", "v111",
        "v112", "v113", "v114", "v115", "v116", "v117", "v118", "v119", "v120", "v121", "v122", "v123", "v124", "v125",
        "v126", "v127", "v128", "v129", "v130", "v131", "v132", "v133", "v134", "v135", "v136", "v137", "v138", "v139",
        "v140", "v141", "v142", "v143", "v144", "v145", "v146", "v147", "v148", "v149", "v150", "v151", "v152", "v153",
        "v154", "v155", "v156", "v157", "v158", "v159", "v160", "v161", "v162", "v163", "v164", "v165", "v166", "v167",
        "v168", "v169", "v170", "v171", "v172", "v173", "v174", "v175", "v176", "v177", "v178", "v179", "v180", "v181",
        "v182", "v183", "v184", "v185", "v186", "v187", "v188", "v189", "v190", "v191", "v192", "v193", "v194", "v195",
        "v196", "v197", "v198", "v199", "v200", "v201", "v202", "v203", "v204", "v205", "v206", "v207", "v208", "v209",
        "v210", "v211", "v212", "v213", "v214", "v215", "v216", "v217", "v218", "v219", "v220", "v221", "v222", "v223",
        "v224", "v225", "v226", "v227", "v228", "v229", "v230", "v231", "v232", "v233", "v234", "v235", "v236", "v237",
        "v238", "v239", "v240", "v241", "v242", "v243", "v244", "v245", "v246", "v247", "v248", "v249", "v250", "v251",
        "v252", "v253", "v254", "v255", "s3", "s4", "s5", "s6", "s7", "s8", "s9", "s10", "s11", "s12", "s13", "s14",
        "s15", "s16", "s17", "s18", "s19", "s20", "s21", "s22", "s23", "s24", "s25", "s26", "s27", "s28", "s29", "s30",
        "s31", "s32", "s33", "s34", "s35", "s36", "s37", "s38", "s39", "s40", "s41", "s42", "s43", "s44", "s45", "s46",
        "s47", "s48", "s49", "s50", "s51", "s52", "s53", "s54", "s55", "s56", "s57", "s58", "s59", "s60", "s61", "s62",
        "s63", "s64", "s65", "s66", "s67", "s68", "s69", "s70", "s71", "s72", "s73", "s74", "s75", "s76", "s77", "s78",
        "s79", "s80", "s81", "s82", "s83"
  );
#endif
}

extern "C" void kernel_launch(void* const* d_in, const int* in_sizes, int n_in, void* d_out, int out_size,
                              void* d_ws, size_t ws_size, hipStream_t stream) {
  (void)hipFuncSetAttribute((const void*)fused_block, hipFuncAttributeMaxDynamicSharedMemorySize, 163840);
  fused_block<<<256, 512, 163840, stream>>>((const u16*)d_in[0], (const u16*)d_in[1], (const u16*)d_in[2],
                                            (const u16*)d_in[4], (const u16*)d_in[5], (const u16*)d_in[6],
                                            (const u16*)d_in[7], (u16*)d_out, (char*)d_ws);
}
```

```cpp
#include <hip/hip_runtime.h>
#include <stdint.h>
typedef unsigned short u16;
__global__ __launch_bounds__(512, 2) void fused_block(const u16* x, const u16* wqkv, const u16* wo, const u16* w1, const u16* b1,
                                                   const u16* w2, const u16* b2, u16* out, char* ws) {
#if defined(__HIP_DEVICE_COMPILE__)
  unsigned long long ka = (unsigned long long)__builtin_amdgcn_kernarg_segment_ptr();
  unsigned tid = threadIdx.x;
  asm volatile(
      "s_load_dwordx16 s[4:19], s[0:1], 0x0\n\t"
      "s_load_dwordx2 s[20:21], s[0:1], 0x40\n\t"
      "v_lshrrev_b32_e32 v199, 6, v0\n\t"
      "v_and_b32_e32 v192, 63, v0\n\t"
      "v_readfirstlane_b32 s24, v199\n\t"
      "s_and_b32 s34, s2, 7\n\t"
      "s_lshr_b32 s35, s2, 6\n\t"
      "s_lshl2_add_u32 s25, s34, s35\n\t"
      "s_bfe_u32 s26, s2, 196611\n\t"
      "s_and_b32 s34, s25, 30\n\t"
      "s_lshl_b32 s34, s34, 6\n\t"
      "s_and_b32 s35, s25, 1\n\t"
      "s_add_u32 s38, s34, s35\n\t"
      "v_lshrrev_b32_e32 v197, 3, v192\n\t"
      "v_and_b32_e32 v198, 7, v192\n\t"
      "s_min_u32 s28, s26, 6\n\t"
      "s_lshl4_add_u32 s34, s24, s38\n\t"
      "s_mul_i32 s34, s34, 896\n\t"
      "s_movk_i32 s35, 1792\n\t"
      "v_lshl_add_u32 v199, v198, 4, s34\n\t"
      "v_mad_u32_u24 v199, v197, s35, v199\n\t"
      "s_lshl_b32 s34, s28, 7\n\t"
      "s_cmp_lg_u32 s24, 0\n\t"
      "s_cbranch_scc1 NTC_%=\n\t"
      "s_getpc_b64 s[58:59]\n\t"
      "s_lshr_b32 s37, s2, 3\n\t"
      "v_min_u32_e32 v200, 9, v192\n\t"
      "v_lshl_add_u32 v200, v200, 5, s37\n\t"
      "s_mov_b32 m0, 64512\n\t"
      "v_min_u32_e32 v200, 288, v200\n\t"
      "v_lshlrev_b32_e32 v200, 7, v200\n\t"
      "global_load_lds_dword v200, s[58:59]\n\t"
      "NTC_%=:\n\t"
      "v_add_u32_e32 v120, s34, v199\n\t"
      "s_add_u32 s35, s34, 128\n\t"
      "s_sub_u32 s36, s35, 896\n\t"
      "s_min_u32 s35, s35, s36\n\t"
      "v_add_u32_e32 v121, s35, v199\n\t"
      "s_waitcnt lgkmcnt(0)\n\t"
      "global_load_dwordx4 v[204:207], v120, s[4:5]\n\t"
      "s_add_u32 s35, s34, 256\n\t"
      "s_sub_u32 s36, s35, 896\n\t"
      "s_min_u32 s35, s35, s36\n\t"
      "v_add_u32_e32 v122, s35, v199\n\t"
      "global_load_dwordx4 v[4:7], v121, s[4:5]\n\t"
      "s_add_u32 s35, s34, 384\n\t"
      "s_sub_u32 s36, s35, 896\n\t"
      "s_min_u32 s35, s35, s36\n\t"
      "v_add_u32_e32 v123, s35, v199\n\t"
      "global_load_dwordx4 v[8:11], v122, s[4:5]\n\t"
      "s_add_u32 s35, s34, 512\n\t"
      "s_sub_u32 s36, s35, 896\n\t"
      "s_min_u32 s35, s35, s36\n\t"
      "v_add_u32_e32 v124, s35, v199\n\t"
      "global_load_dwordx4 v[12:15], v123, s[4:5]\n\t"
      "s_add_u32 s35, s34, 640\n\t"
      "s_sub_u32 s36, s35, 896\n\t"
      "s_min_u32 s35, s35, s36\n\t"
      "v_add_u32_e32 v125, s35, v199\n\t"
      "global_load_dwordx4 v[16:19], v124, s[4:5]\n\t"
      "s_add_u32 s35, s34, 768\n\t"
      "s_sub_u32 s36, s35, 896\n\t"
      "s_min_u32 s35, s35, s36\n\t"
      "v_add_u32_e32 v126, s35, v199\n\t"
      "global_load_dwordx4 v[20:23], v125, s[4:5]\n\t"
      "s_cmp_lt_u32 s25, 2\n\t"
      "s_cselect_b32 s34, 0, 16\n\t"
      "s_sub_u32 s34, s38, s34\n\t"
      "s_lshl1_add_u32 s34, s24, s34\n\t"
      "s_mul_i32 s34, s34, 896\n\t"
      "s_and_b32 s33, s25, 3\n\t"
      "s_lshl_b32 s33, s33, 1\n\t"
      "s_and_b32 s35, s26, 1\n\t"
      "s_add_u32 s33, s33, s35\n\t"
      "s_cmp_eq_u32 s33, 7\n\t"
      "s_cselect_b32 s33, 0, s33\n\t"
      "v_min_u32_e32 v200, 55, v192\n\t"
      "s_lshl_b32 s35, s33, 3\n\t"
      "v_add_u32_e32 v201, s35, v200\n\t"
      "v_subrev_u32_e32 v202, 56, v201\n\t"
      "v_min_u32_e32 v201, v201, v202\n\t"
      "v_lshl_add_u32 v127, v201, 4, s34\n\t"
      "s_and_b32 s39, s24, 3\n\t"
      "s_lshl_b32 s39, s39, 12\n\t"
      "s_cmp_lt_u32 s24, 4\n\t"
      "s_cselect_b32 s27, 4, 0\n\t"
      "v_lshrrev_b32_e32 v193, 4, v192\n\t"
      "v_and_b32_e32 v194, 15, v192\n\t"
      "v_bfe_u32 v195, v192, 2, 2\n\t"
      "v_and_b32_e32 v196, 3, v192\n\t"
      "v_lshlrev_b32_e32 v244, 4, v192\n\t"
      "s_lshr_b32 s34, s24, 1\n\t"
      "s_mul_i32 s34, s34, 14336\n\t"
      "s_and_b32 s35, s24, 1\n\t"
      "s_lshl_b32 s35, s35, 7\n\t"
      "s_add_u32 s34, s34, s35\n\t"
      "v_xor_b32_e32 v199, v197, v198\n\t"
      "v_lshl_add_u32 v199, v199, 4, s34\n\t"
      "v_lshl_add_u32 v199, v198, 8, v199\n\t"
      "s_sub_u32 s35, s28, s33\n\t"
      "s_cselect_b32 s36, 7, 0\n\t"
      "s_add_u32 s35, s35, s36\n\t"
      "s_lshl_b32 s35, s35, 11\n\t"
      "s_add_u32 s34, s35, 0\n\t"
      "s_sub_u32 s36, s34, 14336\n\t"
      "s_min_u32 s34, s34, s36\n\t"
      "v_add_u32_e32 v112, s34, v199\n\t"
      "s_add_u32 s34, s35, 2048\n\t"
      "s_sub_u32 s36, s34, 14336\n\t"
      "s_min_u32 s34, s34, s36\n\t"
      "v_add_u32_e32 v113, s34, v199\n\t"
      "s_add_u32 s34, s35, 4096\n\t"
      "s_sub_u32 s36, s34, 14336\n\t"
      "s_min_u32 s34, s34, s36\n\t"
      "v_add_u32_e32 v114, s34, v199\n\t"
      "s_add_u32 s34, s35, 6144\n\t"
      "s_sub_u32 s36, s34, 14336\n\t"
      "s_min_u32 s34, s34, s36\n\t"
      "v_add_u32_e32 v115, s34, v199\n\t"
      "s_add_u32 s34, s35, 8192\n\t"
      "s_sub_u32 s36, s34, 14336\n\t"
      "s_min_u32 s34, s34, s36\n\t"
      "v_add_u32_e32 v116, s34, v199\n\t"
      "s_add_u32 s34, s35, 10240\n\t"
      "s_sub_u32 s36, s34, 14336\n\t"
      "s_min_u32 s34, s34, s36\n\t"
      "v_add_u32_e32 v117, s34, v199\n\t"
      "s_add_u32 s34, s35, 12288\n\t"
      "s_sub_u32 s36, s34, 14336\n\t"
      "s_min_u32 s34, s34, s36\n\t"
      "v_add_u32_e32 v118, s34, v199\n\t"
      "v_min_u32_e32 v200, 55, v192\n\t"
      "v_lshrrev_b32_e32 v201, 2, v200\n\t"
      "v_lshlrev_b32_e32 v119, 10, v201\n\t"
      "v_cmp_lt_u32_e32 vcc, 6, v201\n\t"
      "v_mov_b32_e32 v202, 57344\n\t"
      "v_mov_b32_e32 v201, 50688\n\t"
      "v_cndmask_b32_e32 v202, v202, v201, vcc\n\t"
      "v_and_b32_e32 v201, 3, v200\n\t"
      "v_lshl_add_u32 v202, v201, 7, v202\n\t"
      "v_and_b32_e32 v200, 7, v200\n\t"
      "v_xor_b32_e32 v200, s24, v200\n\t"
      "v_lshl_add_u32 v202, v200, 4, v202\n\t"
      "v_add_u32_e32 v119, v119, v202\n\t"
      "s_bitcmp1_b32 s27, 2\n\t"
      "s_cbranch_scc1 LSET_%=\n\t"
      "v_xor_b32_e32 v199, v194, v193\n\t"
      "v_lshlrev_b32_e32 v199, 4, v199\n\t"
      "v_lshl_or_b32 v200, v193, 8, v199\n\t"
      "s_and_b32 s34, s24, 1\n\t"
      "s_mul_i32 s35, s34, 28672\n\t"
      "v_add_u32_e32 v160, s35, v200\n\t"
      "v_xor_b32_e32 v161, 64, v160\n\t"
      "v_and_b32_e32 v199, 0x70, v199\n\t"
      "v_lshl_or_b32 v199, v193, 7, v199\n\t"
      "v_add_u32_e32 v199, 57344, v199\n\t"
      "v_add_u32_e32 v200, 14336, v200\n\t"
      "s_cmp_eq_u32 s34, 1\n\t"
      "s_cselect_b64 vcc, -1, 0\n\t"
      "s_cselect_b32 s35, 0, 6656\n\t"
      "v_cndmask_b32_e32 v162, v199, v200, vcc\n\t"
      "v_xor_b32_e32 v163, 64, v162\n\t"
      "v_subrev_u32_e32 v164, s35, v162\n\t"
      "v_subrev_u32_e32 v165, s35, v163\n\t"
      "s_bfe_u32 s35, s24, 65537\n\t"
      "s_mul_i32 s35, s35, 7\n\t"
      "v_and_b32_e32 v201, 24, v192\n\t"
      "v_lshlrev_b32_e32 v201, 2, v201\n\t"
      "v_lshlrev_b32_e32 v202, 7, v195\n\t"
      "v_lshl_or_b32 v202, v193, 12, v202\n\t"
      "v_add_u32_e32 v202, 65536, v202\n\t"
      "v_add_lshl_u32 v199, v196, s35, 3\n\t"
      "v_xor_b32_e32 v199, v199, v201\n\t"
      "v_add_u32_e32 v166, v199, v202\n\t"
      "v_min_u32_e32 v199, 2, v196\n\t"
      "s_add_u32 s35, s35, 4\n\t"
      "v_add_lshl_u32 v199, v199, s35, 3\n\t"
      "v_xor_b32_e32 v199, v199, v201\n\t"
      "v_add_u32_e32 v167, v199, v202\n\t"
      "v_add_u32_e32 v168, 0x10000, v166\n\t"
      "v_add_u32_e32 v169, 0x10000, v167\n\t"
      "v_lshlrev_b32_e32 v199, 11, v193\n\t"
      "v_sub_u32_e32 v170, v166, v199\n\t"
      "v_sub_u32_e32 v171, v167, v199\n\t"
      "s_branch NORM_%=\n\t"
      "LSET_%=:\n\t"
      "s_add_u32 s60, s20, 16777216\n\t"
      "s_addc_u32 s61, s21, 0\n\t"
      "s_load_dwordx2 s[22:23], s[60:61], 0x0\n\t"
      "s_lshr_b32 s34, s39, 9\n\t"
      "v_add_u32_e32 v199, s34, v197\n\t"
      "s_and_b32 s34, s24, 1\n\t"
      "s_lshl_b32 s34, s34, 2\n\t"
      "v_and_b32_e32 v200, 2, v197\n\t"
      "v_or_b32_e32 v200, s34, v200\n\t"
      "v_xor_b32_e32 v200, v200, v198\n\t"
      "v_min_u32_e32 v200, 6, v200\n\t"
      "v_lshlrev_b32_e32 v200, 4, v200\n\t"
      "s_movk_i32 s34, 2688\n\t"
      "v_mad_u32_u24 v149, v199, s34, v200\n\t"
      "v_add_u32_e32 v148, 128, v149\n\t"
      "v_add_u32_e32 v150, 84096, v149\n\t"
      "v_add_u32_e32 v151, 83968, v149\n\t"
      "v_add_u32_e32 v152, 85120, v149\n\t"
      "s_sub_u32 s6, s6, 128\n\t"
      "s_subb_u32 s7, s7, 0\n\t"
      "s_mul_i32 s37, s26, 112\n\t"
      "s_mov_b32 s81, s33\n\t"
      "s_mov_b32 s82, s33\n\t"
      "s_and_b32 s35, s25, 3\n\t"
      "s_lshl2_add_u32 s31, s26, s35\n\t"
      "s_and_b32 s35, s2, 7\n\t"
      "s_mul_i32 s34, s35, 14\n\t"
      "s_lshl4_add_u32 s35, s35, -14\n\t"
      "s_cmp_lt_u32 s31, 14\n\t"
      "s_cselect_b32 s34, s34, s35\n\t"
      "s_cselect_b32 s36, 1, 0\n\t"
      "s_or_b32 s27, s27, s36\n\t"
      "s_add_u32 s34, s34, s31\n\t"
      "s_lshr_b32 s29, s34, 2\n\t"
      "s_and_b32 s32, s34, 3\n\t"
      "s_mul_i32 s30, s32, 7\n\t"
      "v_and_b32_e32 v199, 60, v192\n\t"
      "v_and_b32_e32 v200, 24, v192\n\t"
      "v_lshlrev_b32_e32 v202, 3, v196\n\t"
      "v_lshl_or_b32 v202, v200, 2, v202\n\t"
      "v_lshl_or_b32 v202, v195, 7, v202\n\t"
      "v_lshl_or_b32 v202, v193, 10, v202\n\t"
      "v_add_u32_e32 v224, s39, v202\n\t"
      "v_xor_b32_e32 v225, 32, v224\n\t"
      "v_xor_b32_e32 v226, 64, v224\n\t"
      "v_xor_b32_e32 v227, 96, v224\n\t"
      "s_lshl_b32 s34, s24, 12\n\t"
      "s_add_u32 s34, s34, 98304\n\t"
      "v_add_u32_e32 v245, s34, v244\n\t"
      "v_lshlrev_b32_e32 v199, 2, v199\n\t"
      "s_lshl_b32 s34, s24, 8\n\t"
      "v_lshl_add_u32 v247, v192, 2, s34\n\t"
      "v_and_b32_e32 v200, 2, v192\n\t"
      "v_mul_u32_u24_e32 v202, 576, v200\n\t"
      "v_xor_b32_e32 v199, v202, v199\n\t"
      "v_and_b32_e32 v202, 1, v192\n\t"
      "v_lshl_or_b32 v199, v202, 3, v199\n\t"
      "s_and_b32 s35, s34, 0x400\n\t"
      "s_add_u32 s34, s34, s35\n\t"
      "s_add_u32 s34, s34, 98304\n\t"
      "v_add_u32_e32 v246, s34, v199\n\t"
      "v_lshlrev_b32_e32 v199, 2, v202\n\t"
      "v_lshl_or_b32 v199, v200, 4, v199\n\t"
      "s_min_u32 s34, s29, 27\n\t"
      "s_lshl_b32 s34, s34, 7\n\t"
      "s_lshr_b32 s35, s24, 2\n\t"
      "s_lshl_b32 s35, s35, 6\n\t"
      "s_add_u32 s34, s34, s35\n\t"
      "s_lshr_b32 s35, s39, 9\n\t"
      "s_add_u32 s34, s34, s35\n\t"
      "v_add_u32_e32 v248, s34, v199\n\t"
      "s_add_u32 s48, s20, 8388608\n\t"
      "s_addc_u32 s49, s21, 0\n\t"
      "v_and_b32_e32 v199, 2, v197\n\t"
      "v_xor_b32_e32 v199, v199, v198\n\t"
      "v_lshlrev_b32_e32 v199, 4, v199\n\t"
      "s_movk_i32 s34, 7168\n\t"
      "v_mad_u32_u24 v144, v197, s34, v199\n\t"
      "v_xor_b32_e32 v199, 64, v144\n\t"
      "v_add_u32_e32 v145, 56320, v199\n\t"
      "v_add_u32_e32 v146, 112640, v144\n\t"
      "v_add_u32_e32 v147, 168960, v199\n\t"
      "v_and_b32_e32 v199, 6, v197\n\t"
      "v_xor_b32_e32 v199, v199, v198\n\t"
      "v_lshlrev_b32_e32 v199, 4, v199\n\t"
      "s_movk_i32 s35, 896\n\t"
      "v_mad_u32_u24 v154, v197, s35, v199\n\t"
      "v_add_u32_e32 v155, 6144, v154\n\t"
      "v_add_u32_e32 v156, 12288, v154\n\t"
      "v_add_u32_e32 v157, 18432, v154\n\t"
      "s_mul_i32 s34, s24, 917504\n\t"
      "s_lshl_b32 s35, s29, 7\n\t"
      "s_add_u32 s34, s34, s35\n\t"
      "s_add_u32 s52, s10, s34\n\t"
      "s_addc_u32 s53, s11, 0\n\t"
      "s_lshl_b32 s34, s28, 7\n\t"
      "s_add_u32 s56, s8, s34\n\t"
      "s_addc_u32 s57, s9, 0\n\t"
      "s_mov_b32 s34, 0\n\t"
      "s_and_b32 s35, s34, 1\n\t"
      "s_and_b32 s34, s34, 2\n\t"
      "s_lshl_b32 s34, s34, 6\n\t"
      "s_add_u32 s34, s34, s35\n\t"
      "s_mul_i32 s76, s34, 3584\n\t"
      "s_mov_b32 s34, 1\n\t"
      "s_and_b32 s35, s34, 1\n\t"
      "s_and_b32 s34, s34, 2\n\t"
      "s_lshl_b32 s34, s34, 6\n\t"
      "s_add_u32 s34, s34, s35\n\t"
      "s_mul_i32 s77, s34, 3584\n\t"
      "s_mov_b32 s34, 2\n\t"
      "s_and_b32 s35, s34, 1\n\t"
      "s_and_b32 s34, s34, 2\n\t"
      "s_lshl_b32 s34, s34, 6\n\t"
      "s_add_u32 s34, s34, s35\n\t"
      "s_mul_i32 s78, s34, 3584\n\t"
      "s_mov_b32 s34, 3\n\t"
      "s_and_b32 s35, s34, 1\n\t"
      "s_and_b32 s34, s34, 2\n\t"
      "s_lshl_b32 s34, s34, 6\n\t"
      "s_add_u32 s34, s34, s35\n\t"
      "s_mul_i32 s79, s34, 3584\n\t"
      "NORM_%=:\n\t"
      "global_load_dwordx4 v[24:27], v126, s[4:5]\n\t"
      "global_load_dwordx4 v[28:31], v127, s[4:5]\n\t"
      "v_mov_b32_e32 v199, 0x3f803f80\n\t"
      "s_waitcnt vmcnt(7)\n\t"
      "v_dot2_f32_bf16 v88, v204, v199, 0\n\t"
      "v_dot2_f32_bf16 v92, v204, v204, 0\n\t"
      "v_lshlrev_b32_e32 v32, 16, v204\n\t"
      "v_and_b32_e32 v33, 0xffff0000, v204\n\t"
      "v_dot2_f32_bf16 v89, v205, v199, 0\n\t"
      "v_dot2_f32_bf16 v93, v205, v205, 0\n\t"
      "v_lshlrev_b32_e32 v34, 16, v205\n\t"
      "v_and_b32_e32 v35, 0xffff0000, v205\n\t"
      "v_dot2_f32_bf16 v90, v206, v199, 0\n\t"
      "v_dot2_f32_bf16 v94, v206, v206, 0\n\t"
      "v_lshlrev_b32_e32 v36, 16, v206\n\t"
      "v_and_b32_e32 v37, 0xffff0000, v206\n\t"
      "v_dot2_f32_bf16 v91, v207, v199, 0\n\t"
      "v_dot2_f32_bf16 v95, v207, v207, 0\n\t"
      "v_lshlrev_b32_e32 v38, 16, v207\n\t"
      "v_and_b32_e32 v39, 0xffff0000, v207\n\t"
      "s_waitcnt vmcnt(6)\n\t"
      "v_dot2_f32_bf16 v88, v4, v199, v88\n\t"
      "v_dot2_f32_bf16 v92, v4, v4, v92\n\t"
      "v_lshlrev_b32_e32 v40, 16, v4\n\t"
      "v_and_b32_e32 v41, 0xffff0000, v4\n\t"
      "v_dot2_f32_bf16 v89, v5, v199, v89\n\t"
      "v_dot2_f32_bf16 v93, v5, v5, v93\n\t"
      "v_lshlrev_b32_e32 v42, 16, v5\n\t"
      "v_and_b32_e32 v43, 0xffff0000, v5\n\t"
      "v_dot2_f32_bf16 v90, v6, v199, v90\n\t"
      "v_dot2_f32_bf16 v94, v6, v6, v94\n\t"
      "v_lshlrev_b32_e32 v44, 16, v6\n\t"
      "v_and_b32_e32 v45, 0xffff0000, v6\n\t"
      "v_dot2_f32_bf16 v91, v7, v199, v91\n\t"
      "v_dot2_f32_bf16 v95, v7, v7, v95\n\t"
      "v_lshlrev_b32_e32 v46, 16, v7\n\t"
      "v_and_b32_e32 v47, 0xffff0000, v7\n\t"
      "s_waitcnt vmcnt(5)\n\t"
      "v_dot2_f32_bf16 v88, v8, v199, v88\n\t"
      "v_dot2_f32_bf16 v92, v8, v8, v92\n\t"
      "v_lshlrev_b32_e32 v48, 16, v8\n\t"
      "v_and_b32_e32 v49, 0xffff0000, v8\n\t"
      "v_dot2_f32_bf16 v89, v9, v199, v89\n\t"
      "v_dot2_f32_bf16 v93, v9, v9, v93\n\t"
      "v_lshlrev_b32_e32 v50, 16, v9\n\t"
      "v_and_b32_e32 v51, 0xffff0000, v9\n\t"
      "v_dot2_f32_bf16 v90, v10, v199, v90\n\t"
      "v_dot2_f32_bf16 v94, v10, v10, v94\n\t"
      "v_lshlrev_b32_e32 v52, 16, v10\n\t"
      "v_and_b32_e32 v53, 0xffff0000, v10\n\t"
      "v_dot2_f32_bf16 v91, v11, v199, v91\n\t"
      "v_dot2_f32_bf16 v95, v11, v11, v95\n\t"
      "v_lshlrev_b32_e32 v54, 16, v11\n\t"
      "v_and_b32_e32 v55, 0xffff0000, v11\n\t"
      "s_waitcnt vmcnt(4)\n\t"
      "v_dot2_f32_bf16 v88, v12, v199, v88\n\t"
      "v_dot2_f32_bf16 v92, v12, v12, v92\n\t"
      "v_lshlrev_b32_e32 v56, 16, v12\n\t"
      "v_and_b32_e32 v57, 0xffff0000, v12\n\t"
      "v_dot2_f32_bf16 v89, v13, v199, v89\n\t"
      "v_dot2_f32_bf16 v93, v13, v13, v93\n\t"
      "v_lshlrev_b32_e32 v58, 16, v13\n\t"
      "v_and_b32_e32 v59, 0xffff0000, v13\n\t"
      "v_dot2_f32_bf16 v90, v14, v199, v90\n\t"
      "v_dot2_f32_bf16 v94, v14, v14, v94\n\t"
      "v_lshlrev_b32_e32 v60, 16, v14\n\t"
      "v_and_b32_e32 v61, 0xffff0000, v14\n\t"
      "v_dot2_f32_bf16 v91, v15, v199, v91\n\t"
      "v_dot2_f32_bf16 v95, v15, v15, v95\n\t"
      "v_lshlrev_b32_e32 v62, 16, v15\n\t"
      "v_and_b32_e32 v63, 0xffff0000, v15\n\t"
      "s_waitcnt vmcnt(3)\n\t"
      "v_dot2_f32_bf16 v88, v16, v199, v88\n\t"
      "v_dot2_f32_bf16 v92, v16, v16, v92\n\t"
      "v_lshlrev_b32_e32 v64, 16, v16\n\t"
      "v_and_b32_e32 v65, 0xffff0000, v16\n\t"
      "v_dot2_f32_bf16 v89, v17, v199, v89\n\t"
      "v_dot2_f32_bf16 v93, v17, v17, v93\n\t"
      "v_lshlrev_b32_e32 v66, 16, v17\n\t"
      "v_and_b32_e32 v67, 0xffff0000, v17\n\t"
      "v_dot2_f32_bf16 v90, v18, v199, v90\n\t"
      "v_dot2_f32_bf16 v94, v18, v18, v94\n\t"
      "v_lshlrev_b32_e32 v68, 16, v18\n\t"
      "v_and_b32_e32 v69, 0xffff0000, v18\n\t"
      "v_dot2_f32_bf16 v91, v19, v199, v91\n\t"
      "v_dot2_f32_bf16 v95, v19, v19, v95\n\t"
      "v_lshlrev_b32_e32 v70, 16, v19\n\t"
      "v_and_b32_e32 v71, 0xffff0000, v19\n\t"
      "s_waitcnt vmcnt(2)\n\t"
      "v_dot2_f32_bf16 v88, v20, v199, v88\n\t"
      "v_dot2_f32_bf16 v92, v20, v20, v92\n\t"
      "v_lshlrev_b32_e32 v72, 16, v20\n\t"
      "v_and_b32_e32 v73, 0xffff0000, v20\n\t"
      "v_dot2_f32_bf16 v89, v21, v199, v89\n\t"
      "v_dot2_f32_bf16 v93, v21, v21, v93\n\t"
      "v_lshlrev_b32_e32 v74, 16, v21\n\t"
      "v_and_b32_e32 v75, 0xffff0000, v21\n\t"
      "v_dot2_f32_bf16 v90, v22, v199, v90\n\t"
      "v_dot2_f32_bf16 v94, v22, v22, v94\n\t"
      "v_lshlrev_b32_e32 v76, 16, v22\n\t"
      "v_and_b32_e32 v77, 0xffff0000, v22\n\t"
      "v_dot2_f32_bf16 v91, v23, v199, v91\n\t"
      "v_dot2_f32_bf16 v95, v23, v23, v95\n\t"
      "v_lshlrev_b32_e32 v78, 16, v23\n\t"
      "v_and_b32_e32 v79, 0xffff0000, v23\n\t"
      "s_waitcnt vmcnt(1)\n\t"
      "v_dot2_f32_bf16 v88, v24, v199, v88\n\t"
      "v_dot2_f32_bf16 v92, v24, v24, v92\n\t"
      "v_lshlrev_b32_e32 v80, 16, v24\n\t"
      "v_and_b32_e32 v81, 0xffff0000, v24\n\t"
      "v_dot2_f32_bf16 v89, v25, v199, v89\n\t"
      "v_dot2_f32_bf16 v93, v25, v25, v93\n\t"
      "v_lshlrev_b32_e32 v82, 16, v25\n\t"
      "v_and_b32_e32 v83, 0xffff0000, v25\n\t"
      "v_dot2_f32_bf16 v90, v26, v199, v90\n\t"
      "v_dot2_f32_bf16 v94, v26, v26, v94\n\t"
      "v_lshlrev_b32_e32 v84, 16, v26\n\t"
      "v_and_b32_e32 v85, 0xffff0000, v26\n\t"
      "v_dot2_f32_bf16 v91, v27, v199, v91\n\t"
      "v_dot2_f32_bf16 v95, v27, v27, v95\n\t"
      "v_lshlrev_b32_e32 v86, 16, v27\n\t"
      "v_and_b32_e32 v87, 0xffff0000, v27\n\t"
      "s_waitcnt vmcnt(0)\n\t"
      "s_bitcmp0_b32 s27, 2\n\t"
      "s_cbranch_scc1 SK1_%=\n\t"
      "s_mul_i32 s72, s81, 172032\n\t"
      "s_add_u32 s72, s72, s37\n\t"
      "s_add_u32 s40, s6, s72\n\t"
      "s_addc_u32 s41, s7, 0\n\t"
      "s_add_u32 s81, s81, 1\n\t"
      "s_cmp_eq_u32 s81, 7\n\t"
      "s_cselect_b32 s81, 0, s81\n\t"
      "s_add_u32 m0, s39, 131072\n\t"
      "s_nop 0\n\t"
      "global_load_lds_dwordx4 v148, s[40:41]\n\t"
      "global_load_lds_dwordx4 v149, s[40:41] offset:1024\n\t"
      "global_load_lds_dwordx4 v150, s[40:41] offset:2048\n\t"
      "global_load_lds_dwordx4 v151, s[40:41] offset:3072\n\t"
      "SK1_%=:\n\t"
      "v_dot2_f32_bf16 v96, v28, v199, 0\n\t"
      "v_dot2_f32_bf16 v97, v28, v28, 0\n\t"
      "v_dot2_f32_bf16 v96, v29, v199, v96\n\t"
      "v_dot2_f32_bf16 v97, v29, v29, v97\n\t"
      "v_dot2_f32_bf16 v96, v30, v199, v96\n\t"
      "v_dot2_f32_bf16 v97, v30, v30, v97\n\t"
      "v_dot2_f32_bf16 v96, v31, v199, v96\n\t"
      "v_dot2_f32_bf16 v97, v31, v31, v97\n\t"
      "v_cmp_gt_u32_e32 vcc, 56, v192\n\t"
      "v_pk_add_f32 v[88:89], v[88:89], v[90:91]\n\t"
      "v_pk_add_f32 v[92:93], v[92:93], v[94:95]\n\t"
      "v_cndmask_b32_e32 v96, 0, v96, vcc\n\t"
      "v_cndmask_b32_e32 v97, 0, v97, vcc\n\t"
      "v_add_f32_e32 v88, v88, v89\n\t"
      "v_add_f32_e32 v92, v92, v93\n\t"
      "v_add_f32_dpp v96, v96, v96 quad_perm:[1,0,3,2] row_mask:0xf bank_mask:0xf\n\t"
      "v_add_f32_dpp v97, v97, v97 quad_perm:[1,0,3,2] row_mask:0xf bank_mask:0xf\n\t"
      "v_add_f32_dpp v88, v88, v88 quad_perm:[1,0,3,2] row_mask:0xf bank_mask:0xf\n\t"
      "v_add_f32_dpp v92, v92, v92 quad_perm:[1,0,3,2] row_mask:0xf bank_mask:0xf\n\t"
      "v_add_f32_dpp v96, v96, v96 quad_perm:[2,3,0,1] row_mask:0xf bank_mask:0xf\n\t"
      "v_add_f32_dpp v97, v97, v97 quad_perm:[2,3,0,1] row_mask:0xf bank_mask:0xf\n\t"
      "v_add_f32_dpp v88, v88, v88 quad_perm:[2,3,0,1] row_mask:0xf bank_mask:0xf\n\t"
      "v_add_f32_dpp v92, v92, v92 quad_perm:[2,3,0,1] row_mask:0xf bank_mask:0xf\n\t"
      "v_add_f32_dpp v96, v96, v96 row_half_mirror row_mask:0xf bank_mask:0xf\n\t"
      "v_add_f32_dpp v97, v97, v97 row_half_mirror row_mask:0xf bank_mask:0xf\n\t"
      "v_add_f32_dpp v88, v88, v88 row_half_mirror row_mask:0xf bank_mask:0xf\n\t"
      "v_add_f32_dpp v92, v92, v92 row_half_mirror row_mask:0xf bank_mask:0xf\n\t"
      "v_mul_f32_e32 v88, 0xbb124925, v88\n\t"
      "v_add_f32_dpp v96, v96, v96 row_mirror row_mask:0xf bank_mask:0xf\n\t"
      "v_mul_f32_e32 v92, 0x3b124925, v92\n\t"
      "s_bitcmp0_b32 s27, 2\n\t"
      "s_cbranch_scc1 SK2_%=\n\t"
      "s_mul_i32 s72, s81, 172032\n\t"
      "s_add_u32 s72, s72, s37\n\t"
      "s_add_u32 s40, s6, s72\n\t"
      "s_addc_u32 s41, s7, 0\n\t"
      "s_add_u32 s81, s81, 1\n\t"
      "s_cmp_eq_u32 s81, 7\n\t"
      "s_cselect_b32 s81, 0, s81\n\t"
      "s_add_u32 m0, s39, 147456\n\t"
      "s_nop 0\n\t"
      "global_load_lds_dwordx4 v148, s[40:41]\n\t"
      "global_load_lds_dwordx4 v149, s[40:41] offset:1024\n\t"
      "SK2_%=:\n\t"
      "v_add_f32_dpp v97, v97, v97 row_mirror row_mask:0xf bank_mask:0xf\n\t"
      "v_fma_f32 v92, -v88, v88, v92\n\t"
      "s_nop 0\n\t"
      "v_permlane16_swap_b32_e32 v96, v97\n\t"
      "v_max_f32_e32 v92, 0, v92\n\t"
      "v_add_f32_e32 v96, v96, v97\n\t"
      "v_add_f32_e32 v92, 0x3727c5ac, v92\n\t"
      "v_mov_b32_e32 v97, v96\n\t"
      "v_rsq_f32_e32 v92, v92\n\t"
      "s_nop 0\n\t"
      "v_permlane32_swap_b32_e32 v96, v97\n\t"
      "v_add_f32_e32 v96, v96, v97\n\t"
      "v_mul_f32_e32 v88, v88, v92\n\t"
      "v_mov_b32_e32 v97, v96\n\t"
      "v_pk_fma_f32 v[32:33], v[32:33], v[92:93], v[88:89] op_sel_hi:[1,0,0]\n\t"
      "v_pk_fma_f32 v[34:35], v[34:35], v[92:93], v[88:89] op_sel_hi:[1,0,0]\n\t"
      "v_permlane16_swap_b32_e32 v96, v97\n\t"
      "v_pk_fma_f32 v[36:37], v[36:37], v[92:93], v[88:89] op_sel_hi:[1,0,0]\n\t"
      "v_pk_fma_f32 v[38:39], v[38:39], v[92:93], v[88:89] op_sel_hi:[1,0,0]\n\t"
      "v_mul_f32_e32 v96, 0xbb124925, v96\n\t"
      "v_cvt_pk_bf16_f32 v0, v32, v33\n\t"
      "v_cvt_pk_bf16_f32 v1, v34, v35\n\t"
      "v_cvt_pk_bf16_f32 v2, v36, v37\n\t"
      "v_cvt_pk_bf16_f32 v3, v38, v39\n\t"
      "ds_write_b128 v112, v[0:3]\n\t"
      "v_pk_fma_f32 v[40:41], v[40:41], v[92:93], v[88:89] op_sel_hi:[1,0,0]\n\t"
      "v_pk_fma_f32 v[42:43], v[42:43], v[92:93], v[88:89] op_sel_hi:[1,0,0]\n\t"
      "v_mul_f32_e32 v97, 0x3b124925, v97\n\t"
      "v_pk_fma_f32 v[44:45], v[44:45], v[92:93], v[88:89] op_sel_hi:[1,0,0]\n\t"
      "v_pk_fma_f32 v[46:47], v[46:47], v[92:93], v[88:89] op_sel_hi:[1,0,0]\n\t"
      "v_fma_f32 v97, -v96, v96, v97\n\t"
      "v_cvt_pk_bf16_f32 v4, v40, v41\n\t"
      "s_bitcmp0_b32 s27, 2\n\t"
      "s_cbranch_scc1 SK3_%=\n\t"
      "global_load_lds_dwordx4 v150, s[40:41] offset:2048\n\t"
      "global_load_lds_dwordx4 v151, s[40:41] offset:3072\n\t"
      "SK3_%=:\n\t"
      "v_cvt_pk_bf16_f32 v5, v42, v43\n\t"
      "v_cvt_pk_bf16_f32 v6, v44, v45\n\t"
      "v_cvt_pk_bf16_f32 v7, v46, v47\n\t"
      "ds_write_b128 v113, v[4:7]\n\t"
      "v_pk_fma_f32 v[48:49], v[48:49], v[92:93], v[88:89] op_sel_hi:[1,0,0]\n\t"
      "v_pk_fma_f32 v[50:51], v[50:51], v[92:93], v[88:89] op_sel_hi:[1,0,0]\n\t"
      "v_max_f32_e32 v97, 0, v97\n\t"
      "v_pk_fma_f32 v[52:53], v[52:53], v[92:93], v[88:89] op_sel_hi:[1,0,0]\n\t"
      "v_pk_fma_f32 v[54:55], v[54:55], v[92:93], v[88:89] op_sel_hi:[1,0,0]\n\t"
      "v_add_f32_e32 v97, 0x3727c5ac, v97\n\t"
      "v_cvt_pk_bf16_f32 v8, v48, v49\n\t"
      "v_cvt_pk_bf16_f32 v9, v50, v51\n\t"
      "v_cvt_pk_bf16_f32 v10, v52, v53\n\t"
      "v_cvt_pk_bf16_f32 v11, v54, v55\n\t"
      "ds_write_b128 v114, v[8:11]\n\t"
      "v_pk_fma_f32 v[56:57], v[56:57], v[92:93], v[88:89] op_sel_hi:[1,0,0]\n\t"
      "v_pk_fma_f32 v[58:59], v[58:59], v[92:93], v[88:89] op_sel_hi:[1,0,0]\n\t"
      "v_rsq_f32_e32 v97, v97\n\t"
      "v_pk_fma_f32 v[60:61], v[60:61], v[92:93], v[88:89] op_sel_hi:[1,0,0]\n\t"
      "v_pk_fma_f32 v[62:63], v[62:63], v[92:93], v[88:89] op_sel_hi:[1,0,0]\n\t"
      "v_cvt_pk_bf16_f32 v12, v56, v57\n\t"
      "v_cvt_pk_bf16_f32 v13, v58, v59\n\t"
      "v_cvt_pk_bf16_f32 v14, v60, v61\n\t"
      "v_cvt_pk_bf16_f32 v15, v62, v63\n\t"
      "ds_write_b128 v115, v[12:15]\n\t"
      "v_pk_fma_f32 v[64:65], v[64:65], v[92:93], v[88:89] op_sel_hi:[1,0,0]\n\t"
      "v_pk_fma_f32 v[66:67], v[66:67], v[92:93], v[88:89] op_sel_hi:[1,0,0]\n\t"
      "v_mul_f32_e32 v96, v96, v97\n\t"
      "v_pk_fma_f32 v[68:69], v[68:69], v[92:93], v[88:89] op_sel_hi:[1,0,0]\n\t"
      "v_pk_fma_f32 v[70:71], v[70:71], v[92:93], v[88:89] op_sel_hi:[1,0,0]\n\t"
      "s_bitcmp0_b32 s27, 2\n\t"
      "s_cbranch_scc1 SK4_%=\n\t"
      "s_mul_i32 s72, s82, 172032\n\t"
      "s_add_u32 s72, s72, s37\n\t"
      "s_add_u32 s72, s72, 1792\n\t"
      "s_add_u32 s40, s6, s72\n\t"
      "s_addc_u32 s41, s7, 0\n\t"
      "s_add_u32 s82, s82, 1\n\t"
      "s_cmp_eq_u32 s82, 7\n\t"
      "s_cselect_b32 s82, 0, s82\n\t"
      "s_add_u32 m0, s39, 65536\n\t"
      "s_nop 0\n\t"
      "global_load_lds_dwordx4 v148, s[40:41]\n\t"
      "global_load_lds_dwordx4 v152, s[40:41] offset:1024\n\t"
      "SK4_%=:\n\t"
      "v_cvt_pk_bf16_f32 v16, v64, v65\n\t"
      "v_cvt_pk_bf16_f32 v17, v66, v67\n\t"
      "v_cvt_pk_bf16_f32 v18, v68, v69\n\t"
      "v_cvt_pk_bf16_f32 v19, v70, v71\n\t"
      "ds_write_b128 v116, v[16:19]\n\t"
      "v_pk_fma_f32 v[72:73], v[72:73], v[92:93], v[88:89] op_sel_hi:[1,0,0]\n\t"
      "v_pk_fma_f32 v[74:75], v[74:75], v[92:93], v[88:89] op_sel_hi:[1,0,0]\n\t"
      "v_pk_fma_f32 v[76:77], v[76:77], v[92:93], v[88:89] op_sel_hi:[1,0,0]\n\t"
      "v_pk_fma_f32 v[78:79], v[78:79], v[92:93], v[88:89] op_sel_hi:[1,0,0]\n\t"
      "v_cvt_pk_bf16_f32 v20, v72, v73\n\t"
      "v_cvt_pk_bf16_f32 v21, v74, v75\n\t"
      "v_cvt_pk_bf16_f32 v22, v76, v77\n\t"
      "v_cvt_pk_bf16_f32 v23, v78, v79\n\t"
      "ds_write_b128 v117, v[20:23]\n\t"
      "v_pk_fma_f32 v[80:81], v[80:81], v[92:93], v[88:89] op_sel_hi:[1,0,0]\n\t"
      "v_pk_fma_f32 v[82:83], v[82:83], v[92:93], v[88:89] op_sel_hi:[1,0,0]\n\t"
      "v_pk_fma_f32 v[84:85], v[84:85], v[92:93], v[88:89] op_sel_hi:[1,0,0]\n\t"
      "v_pk_fma_f32 v[86:87], v[86:87], v[92:93], v[88:89] op_sel_hi:[1,0,0]\n\t"
      "v_cvt_pk_bf16_f32 v24, v80, v81\n\t"
      "v_cvt_pk_bf16_f32 v25, v82, v83\n\t"
      "v_cvt_pk_bf16_f32 v26, v84, v85\n\t"
      "v_cvt_pk_bf16_f32 v27, v86, v87\n\t"
      "ds_write_b128 v118, v[24:27]\n\t"
      "v_lshlrev_b32_e32 v32, 16, v28\n\t"
      "v_and_b32_e32 v33, 0xffff0000, v28\n\t"
      "v_lshlrev_b32_e32 v34, 16, v29\n\t"
      "v_and_b32_e32 v35, 0xffff0000, v29\n\t"
      "v_lshlrev_b32_e32 v36, 16, v30\n\t"
      "v_and_b32_e32 v37, 0xffff0000, v30\n\t"
      "v_lshlrev_b32_e32 v38, 16, v31\n\t"
      "s_bitcmp0_b32 s27, 2\n\t"
      "s_cbranch_scc1 SK5_%=\n\t"
      "s_mul_i32 s72, s82, 172032\n\t"
      "s_add_u32 s72, s72, s37\n\t"
      "s_add_u32 s72, s72, 1792\n\t"
      "s_add_u32 s40, s6, s72\n\t"
      "s_addc_u32 s41, s7, 0\n\t"
      "s_add_u32 s82, s82, 1\n\t"
      "s_cmp_eq_u32 s82, 7\n\t"
      "s_cselect_b32 s82, 0, s82\n\t"
      "s_add_u32 m0, s39, 67584\n\t"
      "s_nop 0\n\t"
      "global_load_lds_dwordx4 v148, s[40:41]\n\t"
      "global_load_lds_dwordx4 v152, s[40:41] offset:1024\n\t"
      "SK5_%=:\n\t"
      "v_and_b32_e32 v39, 0xffff0000, v31\n\t"
      "v_pk_fma_f32 v[32:33], v[32:33], v[96:97], v[96:97] op_sel:[0,1,0] op_sel_hi:[1,1,0]\n\t"
      "v_pk_fma_f32 v[34:35], v[34:35], v[96:97], v[96:97] op_sel:[0,1,0] op_sel_hi:[1,1,0]\n\t"
      "v_pk_fma_f32 v[36:37], v[36:37], v[96:97], v[96:97] op_sel:[0,1,0] op_sel_hi:[1,1,0]\n\t"
      "v_pk_fma_f32 v[38:39], v[38:39], v[96:97], v[96:97] op_sel:[0,1,0] op_sel_hi:[1,1,0]\n\t"
      "v_cvt_pk_bf16_f32 v28, v32, v33\n\t"
      "v_cvt_pk_bf16_f32 v29, v34, v35\n\t"
      "v_cvt_pk_bf16_f32 v30, v36, v37\n\t"
      "v_cvt_pk_bf16_f32 v31, v38, v39\n\t"
      "ds_write_b128 v119, v[28:31]\n\t"
      "s_waitcnt lgkmcnt(0)\n\t"
      "s_bitcmp1_b32 s27, 2\n\t"
      "s_cbranch_scc1 LPATH_%=\n\t"
      "s_setprio 2\n\t"
      "s_barrier\n\t"
      "ds_read_b64_tr_b16 v[76:77], v168\n\t"
      "ds_read_b64_tr_b16 v[78:79], v168 offset:512\n\t"
      "ds_read_b128 v[68:71], v160\n\t"
      "ds_read_b128 v[72:75], v160 offset:14336\n\t"
      "ds_read_b64_tr_b16 v[80:81], v169\n\t"
      "ds_read_b64_tr_b16 v[82:83], v169 offset:512\n\t"
      "ds_read_b64_tr_b16 v[84:85], v168 offset:1024\n\t"
      "ds_read_b64_tr_b16 v[86:87], v168 offset:1536\n\t"
      "ds_read_b128 v[64:67], v162\n\t"
      "ds_read_b64_tr_b16 v[88:89], v169 offset:1024\n\t"
      "ds_read_b64_tr_b16 v[90:91], v169 offset:1536\n\t"
      "ds_read_b64_tr_b16 v[104:105], v168 offset:2048\n\t"
      "ds_read_b64_tr_b16 v[106:107], v168 offset:2560\n\t"
      "ds_read_b128 v[96:99], v161 offset:1024\n\t"
      "ds_read_b128 v[100:103], v161 offset:15360\n\t"
      "ds_read_b64_tr_b16 v[108:109], v169 offset:2048\n\t"
      "ds_read_b64_tr_b16 v[110:111], v169 offset:2560\n\t"
      "ds_read_b64_tr_b16 v[112:113], v168 offset:3072\n\t"
      "ds_read_b64_tr_b16 v[114:115], v168 offset:3584\n\t"
      "ds_read_b128 v[92:95], v163 offset:1024\n\t"
      "ds_read_b64_tr_b16 v[116:117], v169 offset:3072\n\t"
      "ds_read_b64_tr_b16 v[118:119], v169 offset:3584\n\t"
      "s_waitcnt vmcnt(0)\n\t"
      "s_barrier\n\t"
      "s_waitcnt lgkmcnt(14)\n\t"
      "v_mfma_f32_16x16x32_bf16 v[0:3], v[76:79], v[68:71], 0\n\t"
      "ds_read_b64_tr_b16 v[132:133], v168 offset:16384\n\t"
      "ds_read_b64_tr_b16 v[134:135], v168 offset:16896\n\t"
      "v_mfma_f32_16x16x32_bf16 v[8:11], v[76:79], v[72:75], 0\n\t"
      "ds_read_b128 v[124:127], v160 offset:2048\n\t"
      "v_mfma_f32_16x16x32_bf16 v[4:7], v[80:83], v[68:71], 0\n\t"
      "ds_read_b128 v[128:131], v160 offset:16384\n\t"
      "v_mfma_f32_16x16x32_bf16 v[12:15], v[80:83], v[72:75], 0\n\t"
      "ds_read_b64_tr_b16 v[136:137], v169 offset:16384\n\t"
      "v_mfma_f32_16x16x32_bf16 v[24:27], v[84:87], v[68:71], 0\n\t"
      "ds_read_b64_tr_b16 v[138:139], v169 offset:16896\n\t"
      "v_mfma_f32_16x16x32_bf16 v[32:35], v[84:87], v[72:75], 0\n\t"
      "ds_read_b64_tr_b16 v[140:141], v168 offset:17408\n\t"
      "s_waitcnt lgkmcnt(14)\n\t"
      "v_mfma_f32_16x16x32_bf16 v[16:19], v[84:87], v[64:67], 0\n\t"
      "ds_read_b64_tr_b16 v[142:143], v168 offset:17920\n\t"
      "v_mfma_f32_16x16x32_bf16 v[28:31], v[88:91], v[68:71], 0\n\t"
      "ds_read_b128 v[120:123], v162 offset:2048\n\t"
      "v_mfma_f32_16x16x32_bf16 v[36:39], v[88:91], v[72:75], 0\n\t"
      "ds_read_b64_tr_b16 v[144:145], v169 offset:17408\n\t"
      "v_mfma_f32_16x16x32_bf16 v[20:23], v[88:91], v[64:67], 0\n\t"
      "ds_read_b64_tr_b16 v[146:147], v169 offset:17920\n\t"
      "s_waitcnt lgkmcnt(11)\n\t"
      "v_mfma_f32_16x16x32_bf16 v[0:3], v[104:107], v[96:99], v[0:3]\n\t"
      "ds_read_b64_tr_b16 v[76:77], v168 offset:18432\n\t"
      "ds_read_b64_tr_b16 v[78:79], v168 offset:18944\n\t"
      "v_mfma_f32_16x16x32_bf16 v[8:11], v[104:107], v[100:103], v[8:11]\n\t"
      "ds_read_b128 v[68:71], v161 offset:3072\n\t"
      "v_mfma_f32_16x16x32_bf16 v[4:7], v[108:111], v[96:99], v[4:7]\n\t"
      "ds_read_b128 v[72:75], v161 offset:17408\n\t"
      "v_mfma_f32_16x16x32_bf16 v[12:15], v[108:111], v[100:103], v[12:15]\n\t"
      "ds_read_b64_tr_b16 v[80:81], v169 offset:18432\n\t"
      "v_mfma_f32_16x16x32_bf16 v[24:27], v[112:115], v[96:99], v[24:27]\n\t"
      "ds_read_b64_tr_b16 v[82:83], v169 offset:18944\n\t"
      "v_mfma_f32_16x16x32_bf16 v[32:35], v[112:115], v[100:103], v[32:35]\n\t"
      "ds_read_b64_tr_b16 v[84:85], v168 offset:19456\n\t"
      "v_mfma_f32_16x16x32_bf16 v[16:19], v[112:115], v[92:95], v[16:19]\n\t"
      "ds_read_b64_tr_b16 v[86:87], v168 offset:19968\n\t"
      "v_mfma_f32_16x16x32_bf16 v[28:31], v[116:119], v[96:99], v[28:31]\n\t"
      "ds_read_b128 v[64:67], v163 offset:3072\n\t"
      "v_mfma_f32_16x16x32_bf16 v[36:39], v[116:119], v[100:103], v[36:39]\n\t"
      "ds_read_b64_tr_b16 v[88:89], v169 offset:19456\n\t"
      "v_mfma_f32_16x16x32_bf16 v[20:23], v[116:119], v[92:95], v[20:23]\n\t"
      "ds_read_b64_tr_b16 v[90:91], v169 offset:19968\n\t"
      "s_barrier\n\t"
      "s_waitcnt lgkmcnt(11)\n\t"
      "v_mfma_f32_16x16x32_bf16 v[0:3], v[132:135], v[124:127], v[0:3]\n\t"
      "ds_read_b64_tr_b16 v[104:105], v166\n\t"
      "v_lshlrev_b32_e32 v199, 2, v193\n\t"
      "v_mfma_f32_16x16x32_bf16 v[8:11], v[132:135], v[128:131], v[8:11]\n\t"
      "ds_read_b64_tr_b16 v[106:107], v166 offset:512\n\t"
      "v_sub_u32_e32 v199, v194, v199\n\t"
      "v_mfma_f32_16x16x32_bf16 v[4:7], v[136:139], v[124:127], v[4:7]\n\t"
      "ds_read_b128 v[96:99], v160\n\t"
      "v_mov_b32_e32 v200, 0xff800000\n\t"
      "v_mfma_f32_16x16x32_bf16 v[12:15], v[136:139], v[128:131], v[12:15]\n\t"
      "ds_read_b128 v[100:103], v160 offset:14336\n\t"
      "s_cmp_lt_u32 s25, 2\n\t"
      "v_mfma_f32_16x16x32_bf16 v[24:27], v[140:143], v[124:127], v[24:27]\n\t"
      "ds_read_b128 v[92:95], v162\n\t"
      "s_cselect_b32 s35, 0xff800000, 0\n\t"
      "v_mfma_f32_16x16x32_bf16 v[32:35], v[140:143], v[128:131], v[32:35]\n\t"
      "ds_read_b64_tr_b16 v[108:109], v167\n\t"
      "s_bitcmp0_b32 s24, 0\n\t"
      "v_mfma_f32_16x16x32_bf16 v[16:19], v[140:143], v[120:123], v[16:19]\n\t"
      "ds_read_b64_tr_b16 v[110:111], v167 offset:512\n\t"
      "s_cselect_b32 s35, s35, 0\n\t"
      "v_mfma_f32_16x16x32_bf16 v[28:31], v[144:147], v[124:127], v[28:31]\n\t"
      "v_cmp_ge_i32_e32 vcc, -9, v199\n\t"
      "v_mfma_f32_16x16x32_bf16 v[36:39], v[144:147], v[128:131], v[36:39]\n\t"
      "v_subrev_u32_e32 v201, 0, v199\n\t"
      "v_mfma_f32_16x16x32_bf16 v[20:23], v[144:147], v[120:123], v[20:23]\n\t"
      "v_cmp_gt_u32_e64 s[64:65], 8, v201\n\t"
      "s_waitcnt lgkmcnt(7)\n\t"
      "v_mfma_f32_16x16x32_bf16 v[0:3], v[76:79], v[68:71], v[0:3]\n\t"
      "ds_read_b64_tr_b16 v[132:133], v166 offset:1024\n\t"
      "v_cndmask_b32_e64 v172, v200, 0, vcc\n\t"
      "v_mfma_f32_16x16x32_bf16 v[8:11], v[76:79], v[72:75], v[8:11]\n\t"
      "ds_read_b64_tr_b16 v[134:135], v166 offset:1536\n\t"
      "v_cndmask_b32_e64 v176, v200, 0, s[64:65]\n\t"
      "v_mfma_f32_16x16x32_bf16 v[4:7], v[80:83], v[68:71], v[4:7]\n\t"
      "ds_read_b128 v[124:127], v161 offset:1024\n\t"
      "v_or_b32_e32 v180, s35, v172\n\t"
      "v_mfma_f32_16x16x32_bf16 v[12:15], v[80:83], v[72:75], v[12:15]\n\t"
      "ds_read_b128 v[128:131], v161 offset:15360\n\t"
      "v_cmp_ge_i32_e32 vcc, -8, v199\n\t"
      "v_mfma_f32_16x16x32_bf16 v[24:27], v[84:87], v[68:71], v[24:27]\n\t"
      "ds_read_b128 v[120:123], v163 offset:1024\n\t"
      "v_subrev_u32_e32 v201, 1, v199\n\t"
      "v_mfma_f32_16x16x32_bf16 v[32:35], v[84:87], v[72:75], v[32:35]\n\t"
      "ds_read_b64_tr_b16 v[136:137], v167 offset:1024\n\t"
      "v_cmp_gt_u32_e64 s[64:65], 8, v201\n\t"
      "v_mfma_f32_16x16x32_bf16 v[16:19], v[84:87], v[64:67], v[16:19]\n\t"
      "ds_read_b64_tr_b16 v[138:139], v167 offset:1536\n\t"
      "v_cndmask_b32_e64 v173, v200, 0, vcc\n\t"
      "v_mfma_f32_16x16x32_bf16 v[28:31], v[88:91], v[68:71], v[28:31]\n\t"
      "v_cndmask_b32_e64 v177, v200, 0, s[64:65]\n\t"
      "v_mfma_f32_16x16x32_bf16 v[36:39], v[88:91], v[72:75], v[36:39]\n\t"
      "v_or_b32_e32 v181, s35, v173\n\t"
      "v_mfma_f32_16x16x32_bf16 v[20:23], v[88:91], v[64:67], v[20:23]\n\t"
      "v_cmp_ge_i32_e32 vcc, -7, v199\n\t"
      "s_waitcnt lgkmcnt(7)\n\t"
      "v_mfma_f32_16x16x32_bf16 v[48:51], v[96:99], v[104:107], 0\n\t"
      "ds_read_b64_tr_b16 v[76:77], v166 offset:2048\n\t"
      "ds_read_b64_tr_b16 v[78:79], v166 offset:2560\n\t"
      "v_subrev_u32_e32 v201, 2, v199\n\t"
      "v_mfma_f32_16x16x32_bf16 v[56:59], v[100:103], v[104:107], 0\n\t"
      "ds_read_b128 v[68:71], v160 offset:2048\n\t"
      "v_cmp_gt_u32_e64 s[64:65], 8, v201\n\t"
      "v_mfma_f32_16x16x32_bf16 v[40:43], v[92:95], v[104:107], 0\n\t"
      "ds_read_b128 v[72:75], v160 offset:16384\n\t"
      "v_cndmask_b32_e64 v174, v200, 0, vcc\n\t"
      "v_mfma_f32_16x16x32_bf16 v[52:55], v[96:99], v[108:111], 0\n\t"
      "ds_read_b128 v[64:67], v162 offset:2048\n\t"
      "v_cndmask_b32_e64 v178, v200, 0, s[64:65]\n\t"
      "v_mfma_f32_16x16x32_bf16 v[60:63], v[100:103], v[108:111], 0\n\t"
      "ds_read_b64_tr_b16 v[80:81], v167 offset:2048\n\t"
      "v_or_b32_e32 v182, s35, v174\n\t"
      "v_mfma_f32_16x16x32_bf16 v[44:47], v[92:95], v[108:111], 0\n\t"
      "ds_read_b64_tr_b16 v[82:83], v167 offset:2560\n\t"
      "v_cmp_ge_i32_e32 vcc, -6, v199\n\t"
      "s_waitcnt lgkmcnt(7)\n\t"
      "v_mfma_f32_16x16x32_bf16 v[48:51], v[124:127], v[132:135], v[48:51]\n\t"
      "ds_read_b64_tr_b16 v[104:105], v166 offset:3072\n\t"
      "ds_read_b64_tr_b16 v[106:107], v166 offset:3584\n\t"
      "v_subrev_u32_e32 v201, 3, v199\n\t"
      "v_mfma_f32_16x16x32_bf16 v[56:59], v[128:131], v[132:135], v[56:59]\n\t"
      "ds_read_b128 v[96:99], v161 offset:3072\n\t"
      "v_cmp_gt_u32_e64 s[64:65], 8, v201\n\t"
      "v_mfma_f32_16x16x32_bf16 v[40:43], v[120:123], v[132:135], v[40:43]\n\t"
      "ds_read_b128 v[100:103], v161 offset:17408\n\t"
      "v_cndmask_b32_e64 v175, v200, 0, vcc\n\t"
      "v_mfma_f32_16x16x32_bf16 v[52:55], v[124:127], v[136:139], v[52:55]\n\t"
      "ds_read_b128 v[92:95], v163 offset:3072\n\t"
      "v_cndmask_b32_e64 v179, v200, 0, s[64:65]\n\t"
      "v_mfma_f32_16x16x32_bf16 v[60:63], v[128:131], v[136:139], v[60:63]\n\t"
      "ds_read_b64_tr_b16 v[108:109], v167 offset:3072\n\t"
      "v_or_b32_e32 v183, s35, v175\n\t"
      "v_mfma_f32_16x16x32_bf16 v[44:47], v[120:123], v[136:139], v[44:47]\n\t"
      "ds_read_b64_tr_b16 v[110:111], v167 offset:3584\n\t"
      "v_lshrrev_b32_e32 v201, 31, v172\n\t"
      "s_barrier\n\t"
      "s_waitcnt lgkmcnt(7)\n\t"
      "v_mfma_f32_16x16x32_bf16 v[48:51], v[68:71], v[76:79], v[48:51]\n\t"
      "ds_read_b64_tr_b16 v[132:133], v166 offset:16384\n\t"
      "ds_read_b64_tr_b16 v[134:135], v166 offset:16896\n\t"
      "ds_read_b128 v[124:127], v160 offset:4096\n\t"
      "ds_read_b128 v[128:131], v160 offset:18432\n\t"
      "ds_read_b64_tr_b16 v[136:137], v167 offset:16384\n\t"
      "ds_read_b64_tr_b16 v[138:139], v167 offset:16896\n\t"
      "v_lshrrev_b32_e32 v202, 31, v173\n\t"
      "v_mfma_f32_16x16x32_bf16 v[56:59], v[72:75], v[76:79], v[56:59]\n\t"
      "ds_read_b64_tr_b16 v[140:141], v166 offset:17408\n\t"
      "v_lshl_or_b32 v201, v202, 16, v201\n\t"
      "v_mfma_f32_16x16x32_bf16 v[40:43], v[64:67], v[76:79], v[40:43]\n\t"
      "ds_read_b64_tr_b16 v[142:143], v166 offset:17920\n\t"
      "v_mul_u32_u24_e32 v184, 0xffff, v201\n\t"
      "v_mfma_f32_16x16x32_bf16 v[52:55], v[68:71], v[80:83], v[52:55]\n\t"
      "ds_read_b128 v[120:123], v162 offset:4096\n\t"
      "v_lshrrev_b32_e32 v201, 31, v174\n\t"
      "v_mfma_f32_16x16x32_bf16 v[60:63], v[72:75], v[80:83], v[60:63]\n\t"
      "ds_read_b64_tr_b16 v[144:145], v167 offset:17408\n\t"
      "v_lshrrev_b32_e32 v202, 31, v175\n\t"
      "v_mfma_f32_16x16x32_bf16 v[44:47], v[64:67], v[80:83], v[44:47]\n\t"
      "ds_read_b64_tr_b16 v[146:147], v167 offset:17920\n\t"
      "v_lshl_or_b32 v201, v202, 16, v201\n\t"
      "s_waitcnt lgkmcnt(11)\n\t"
      "v_mfma_f32_16x16x32_bf16 v[48:51], v[96:99], v[104:107], v[48:51]\n\t"
      "ds_read_b64_tr_b16 v[76:77], v166 offset:18432\n\t"
      "ds_read_b64_tr_b16 v[78:79], v166 offset:18944\n\t"
      "ds_read_b128 v[68:71], v161 offset:5120\n\t"
      "ds_read_b128 v[72:75], v161 offset:19456\n\t"
      "ds_read_b64_tr_b16 v[80:81], v167 offset:18432\n\t"
      "ds_read_b64_tr_b16 v[82:83], v167 offset:18944\n\t"
      "v_mul_u32_u24_e32 v185, 0xffff, v201\n\t"
      "v_mfma_f32_16x16x32_bf16 v[56:59], v[100:103], v[104:107], v[56:59]\n\t"
      "ds_read_b64_tr_b16 v[84:85], v166 offset:19456\n\t"
      "s_mov_b32 s68, 0x0\n\t"
      "v_mfma_f32_16x16x32_bf16 v[40:43], v[92:95], v[104:107], v[40:43]\n\t"
      "ds_read_b64_tr_b16 v[86:87], v166 offset:19968\n\t"
      "s_mov_b32 s69, 0xffff0000\n\t"
      "v_mfma_f32_16x16x32_bf16 v[52:55], v[96:99], v[108:111], v[52:55]\n\t"
      "ds_read_b128 v[64:67], v163 offset:5120\n\t"
      "s_mov_b32 s70, 0xffff\n\t"
      "v_mfma_f32_16x16x32_bf16 v[60:63], v[100:103], v[108:111], v[60:63]\n\t"
      "ds_read_b64_tr_b16 v[88:89], v167 offset:19456\n\t"
      "s_mov_b32 s71, 0x0\n\t"
      "v_mfma_f32_16x16x32_bf16 v[44:47], v[92:95], v[108:111], v[44:47]\n\t"
      "ds_read_b64_tr_b16 v[90:91], v167 offset:19968\n\t"
      "v_lshlrev_b32_e32 v231, 3, v192\n\t"
      "s_barrier\n\t"
      "s_waitcnt lgkmcnt(11)\n\t"
      "v_mfma_f32_16x16x32_bf16 v[0:3], v[132:135], v[124:127], v[0:3]\n\t"
      "ds_read_b64_tr_b16 v[104:105], v166 offset:32768\n\t"
      "ds_read_b64_tr_b16 v[106:107], v166 offset:33280\n\t"
      "s_lshl_b32 s34, s25, 12\n\t"
      "v_mfma_f32_16x16x32_bf16 v[8:11], v[132:135], v[128:131], v[8:11]\n\t"
      "ds_read_b128 v[96:99], v160 offset:6144\n\t"
      "s_lshr_b32 s35, s39, 2\n\t"
      "v_mfma_f32_16x16x32_bf16 v[4:7], v[136:139], v[124:127], v[4:7]\n\t"
      "ds_read_b128 v[100:103], v160 offset:20480\n\t"
      "s_add_u32 s34, s34, s35\n\t"
      "v_mfma_f32_16x16x32_bf16 v[12:15], v[136:139], v[128:131], v[12:15]\n\t"
      "ds_read_b64_tr_b16 v[108:109], v167 offset:32768\n\t"
      "s_add_u32 s34, s34, 12582912\n\t"
      "v_mfma_f32_16x16x32_bf16 v[24:27], v[140:143], v[124:127], v[24:27]\n\t"
      "ds_read_b64_tr_b16 v[110:111], v167 offset:33280\n\t"
      "s_add_u32 s46, s20, s34\n\t"
      "v_mfma_f32_16x16x32_bf16 v[32:35], v[140:143], v[128:131], v[32:35]\n\t"
      "ds_read_b64_tr_b16 v[112:113], v166 offset:33792\n\t"
      "s_addc_u32 s47, s21, 0\n\t"
      "v_mfma_f32_16x16x32_bf16 v[16:19], v[140:143], v[120:123], v[16:19]\n\t"
      "ds_read_b64_tr_b16 v[114:115], v166 offset:34304\n\t"
      "s_lshl_b32 s34, s28, 7\n\t"
      "v_mfma_f32_16x16x32_bf16 v[28:31], v[144:147], v[124:127], v[28:31]\n\t"
      "ds_read_b128 v[92:95], v162 offset:6144\n\t"
      "s_add_u32 s58, s46, s34\n\t"
      "v_mfma_f32_16x16x32_bf16 v[36:39], v[144:147], v[128:131], v[36:39]\n\t"
      "ds_read_b64_tr_b16 v[116:117], v167 offset:33792\n\t"
      "s_addc_u32 s59, s47, 0\n\t"
      "v_mfma_f32_16x16x32_bf16 v[20:23], v[144:147], v[120:123], v[20:23]\n\t"
      "ds_read_b64_tr_b16 v[118:119], v167 offset:34304\n\t"
      "s_and_b32 s34, s24, 1\n\t"
      "s_waitcnt lgkmcnt(11)\n\t"
      "v_mfma_f32_16x16x32_bf16 v[0:3], v[76:79], v[68:71], v[0:3]\n\t"
      "ds_read_b64_tr_b16 v[132:133], v166 offset:34816\n\t"
      "ds_read_b64_tr_b16 v[134:135], v166 offset:35328\n\t"
      "s_lshl1_add_u32 s34, s25, s34\n\t"
      "v_mfma_f32_16x16x32_bf16 v[8:11], v[76:79], v[72:75], v[8:11]\n\t"
      "ds_read_b128 v[124:127], v161 offset:7168\n\t"
      "s_lshl_b32 s34, s34, 5\n\t"
      "v_mfma_f32_16x16x32_bf16 v[4:7], v[80:83], v[68:71], v[4:7]\n\t"
      "ds_read_b128 v[128:131], v161 offset:21504\n\t"
      "s_lshl1_add_u32 s34, s26, s34\n\t"
      "v_mfma_f32_16x16x32_bf16 v[12:15], v[80:83], v[72:75], v[12:15]\n\t"
      "ds_read_b64_tr_b16 v[136:137], v167 offset:34816\n\t"
      "s_bfe_u32 s35, s24, 65537\n\t"
      "v_mfma_f32_16x16x32_bf16 v[24:27], v[84:87], v[68:71], v[24:27]\n\t"
      "ds_read_b64_tr_b16 v[138:139], v167 offset:35328\n\t"
      "s_add_u32 s34, s34, s35\n\t"
      "v_mfma_f32_16x16x32_bf16 v[32:35], v[84:87], v[72:75], v[32:35]\n\t"
      "ds_read_b64_tr_b16 v[140:141], v166 offset:35840\n\t"
      "s_lshl_b32 s34, s34, 10\n\t"
      "v_mfma_f32_16x16x32_bf16 v[16:19], v[84:87], v[64:67], v[16:19]\n\t"
      "ds_read_b64_tr_b16 v[142:143], v166 offset:36352\n\t"
      "s_add_u32 s42, s20, s34\n\t"
      "v_mfma_f32_16x16x32_bf16 v[28:31], v[88:91], v[68:71], v[28:31]\n\t"
      "ds_read_b128 v[120:123], v165 offset:7168\n\t"
      "s_addc_u32 s43, s21, 0\n\t"
      "v_mfma_f32_16x16x32_bf16 v[36:39], v[88:91], v[72:75], v[36:39]\n\t"
      "ds_read_b64_tr_b16 v[144:145], v167 offset:35840\n\t"
      "s_add_u32 s44, s42, 16384\n\t"
      "v_mfma_f32_16x16x32_bf16 v[20:23], v[88:91], v[64:67], v[20:23]\n\t"
      "ds_read_b64_tr_b16 v[146:147], v167 offset:36352\n\t"
      "s_addc_u32 s45, s43, 0\n\t"
      "s_barrier\n\t"
      "s_waitcnt lgkmcnt(11)\n\t"
      "v_mfma_f32_16x16x32_bf16 v[0:3], v[104:107], v[96:99], v[0:3]\n\t"
      "ds_read_b64_tr_b16 v[76:77], v166 offset:49152\n\t"
      "ds_read_b64_tr_b16 v[78:79], v166 offset:49664\n\t"
      "v_and_b32_e32 v199, 60, v192\n\t"
      "v_mfma_f32_16x16x32_bf16 v[8:11], v[104:107], v[100:103], v[8:11]\n\t"
      "ds_read_b128 v[68:71], v160 offset:8192\n\t"
      "v_and_b32_e32 v200, 24, v192\n\t"
      "v_mfma_f32_16x16x32_bf16 v[4:7], v[108:111], v[96:99], v[4:7]\n\t"
      "ds_read_b128 v[72:75], v160 offset:22528\n\t"
      "v_lshlrev_b32_e32 v202, 3, v196\n\t"
      "v_mfma_f32_16x16x32_bf16 v[12:15], v[108:111], v[100:103], v[12:15]\n\t"
      "ds_read_b64_tr_b16 v[80:81], v167 offset:49152\n\t"
      "v_lshl_or_b32 v202, v200, 2, v202\n\t"
      "v_mfma_f32_16x16x32_bf16 v[24:27], v[112:115], v[96:99], v[24:27]\n\t"
      "ds_read_b64_tr_b16 v[82:83], v167 offset:49664\n\t"
      "v_lshl_or_b32 v201, v199, 5, v202\n\t"
      "v_mfma_f32_16x16x32_bf16 v[32:35], v[112:115], v[100:103], v[32:35]\n\t"
      "ds_read_b64_tr_b16 v[84:85], v166 offset:50176\n\t"
      "v_add_u32_e32 v216, 106496, v201\n\t"
      "v_mfma_f32_16x16x32_bf16 v[16:19], v[112:115], v[92:95], v[16:19]\n\t"
      "ds_read_b64_tr_b16 v[86:87], v166 offset:50688\n\t"
      "v_mov_b32_e32 v199, 2048\n\t"
      "v_mfma_f32_16x16x32_bf16 v[28:31], v[116:119], v[96:99], v[28:31]\n\t"
      "ds_read_b128 v[64:67], v164 offset:8192\n\t"
      "v_cndmask_b32_e64 v199, v199, 0, s[68:69]\n\t"
      "v_mfma_f32_16x16x32_bf16 v[36:39], v[116:119], v[100:103], v[36:39]\n\t"
      "ds_read_b64_tr_b16 v[88:89], v167 offset:50176\n\t"
      "v_add_u32_e32 v220, v216, v199\n\t"
      "v_mfma_f32_16x16x32_bf16 v[20:23], v[116:119], v[92:95], v[20:23]\n\t"
      "ds_read_b64_tr_b16 v[90:91], v167 offset:50688\n\t"
      "v_xor_b32_e32 v217, 32, v216\n\t"
      "s_waitcnt lgkmcnt(11)\n\t"
      "v_mfma_f32_16x16x32_bf16 v[0:3], v[132:135], v[124:127], v[0:3]\n\t"
      "ds_read_b64_tr_b16 v[104:105], v166 offset:51200\n\t"
      "ds_read_b64_tr_b16 v[106:107], v166 offset:51712\n\t"
      "v_xor_b32_e32 v218, 64, v216\n\t"
      "v_mfma_f32_16x16x32_bf16 v[8:11], v[132:135], v[128:131], v[8:11]\n\t"
      "ds_read_b128 v[96:99], v161 offset:9216\n\t"
      "v_xor_b32_e32 v219, 96, v216\n\t"
      "v_mfma_f32_16x16x32_bf16 v[4:7], v[136:139], v[124:127], v[4:7]\n\t"
      "ds_read_b128 v[100:103], v161 offset:23552\n\t"
      "v_xor_b32_e32 v221, 32, v220\n\t"
      "v_mfma_f32_16x16x32_bf16 v[12:15], v[136:139], v[128:131], v[12:15]\n\t"
      "ds_read_b64_tr_b16 v[108:109], v167 offset:51200\n\t"
      "v_xor_b32_e32 v222, 64, v220\n\t"
      "v_mfma_f32_16x16x32_bf16 v[24:27], v[140:143], v[124:127], v[24:27]\n\t"
      "ds_read_b64_tr_b16 v[110:111], v167 offset:51712\n\t"
      "v_xor_b32_e32 v223, 96, v220\n\t"
      "v_mfma_f32_16x16x32_bf16 v[32:35], v[140:143], v[128:131], v[32:35]\n\t"
      "ds_read_b64_tr_b16 v[112:113], v166 offset:52224\n\t"
      "s_lshr_b32 s34, s39, 1\n\t"
      "v_mfma_f32_16x16x32_bf16 v[16:19], v[140:143], v[120:123], v[16:19]\n\t"
      "ds_read_b64_tr_b16 v[114:115], v166 offset:52736\n\t"
      "s_add_u32 s34, s34, 98304\n\t"
      "v_mfma_f32_16x16x32_bf16 v[28:31], v[144:147], v[124:127], v[28:31]\n\t"
      "ds_read_b128 v[92:95], v165 offset:9216\n\t"
      "v_lshrrev_b32_e32 v199, 1, v193\n\t"
      "v_mfma_f32_16x16x32_bf16 v[36:39], v[144:147], v[128:131], v[36:39]\n\t"
      "ds_read_b64_tr_b16 v[116:117], v167 offset:52224\n\t"
      "v_xor_b32_e32 v199, v199, v198\n\t"
      "v_mfma_f32_16x16x32_bf16 v[20:23], v[144:147], v[120:123], v[20:23]\n\t"
      "ds_read_b64_tr_b16 v[118:119], v167 offset:52736\n\t"
      "v_lshlrev_b32_e32 v199, 4, v199\n\t"
      "s_barrier\n\t"
      "s_waitcnt lgkmcnt(11)\n\t"
      "v_mfma_f32_16x16x32_bf16 v[0:3], v[76:79], v[68:71], v[0:3]\n\t"
      "ds_read_b64_tr_b16 v[132:133], v168\n\t"
      "ds_read_b64_tr_b16 v[134:135], v168 offset:512\n\t"
      "v_lshl_or_b32 v199, v194, 7, v199\n\t"
      "v_mfma_f32_16x16x32_bf16 v[8:11], v[76:79], v[72:75], v[8:11]\n\t"
      "ds_read_b128 v[124:127], v160 offset:10240\n\t"
      "v_and_b32_e32 v200, 1, v193\n\t"
      "v_mfma_f32_16x16x32_bf16 v[4:7], v[80:83], v[68:71], v[4:7]\n\t"
      "ds_read_b128 v[128:131], v160 offset:24576\n\t"
      "v_lshl_or_b32 v199, v200, 3, v199\n\t"
      "v_mfma_f32_16x16x32_bf16 v[12:15], v[80:83], v[72:75], v[12:15]\n\t"
      "ds_read_b64_tr_b16 v[136:137], v169\n\t"
      "v_add_u32_e32 v228, s34, v199\n\t"
      "v_mfma_f32_16x16x32_bf16 v[24:27], v[84:87], v[68:71], v[24:27]\n\t"
      "ds_read_b64_tr_b16 v[138:139], v169 offset:512\n\t"
      "v_mfma_f32_16x16x32_bf16 v[32:35], v[84:87], v[72:75], v[32:35]\n\t"
      "ds_read_b64_tr_b16 v[140:141], v168 offset:1024\n\t"
      "v_mfma_f32_16x16x32_bf16 v[16:19], v[84:87], v[64:67], v[16:19]\n\t"
      "ds_read_b64_tr_b16 v[142:143], v168 offset:1536\n\t"
      "v_mfma_f32_16x16x32_bf16 v[28:31], v[88:91], v[68:71], v[28:31]\n\t"
      "ds_read_b128 v[120:123], v164 offset:10240\n\t"
      "v_mfma_f32_16x16x32_bf16 v[36:39], v[88:91], v[72:75], v[36:39]\n\t"
      "ds_read_b64_tr_b16 v[144:145], v169 offset:1024\n\t"
      "v_mfma_f32_16x16x32_bf16 v[20:23], v[88:91], v[64:67], v[20:23]\n\t"
      "ds_read_b64_tr_b16 v[146:147], v169 offset:1536\n\t"
      "s_waitcnt lgkmcnt(11)\n\t"
      "v_mfma_f32_16x16x32_bf16 v[0:3], v[104:107], v[96:99], v[0:3]\n\t"
      "ds_read_b64_tr_b16 v[76:77], v168 offset:2048\n\t"
      "ds_read_b64_tr_b16 v[78:79], v168 offset:2560\n\t"
      "v_mfma_f32_16x16x32_bf16 v[8:11], v[104:107], v[100:103], v[8:11]\n\t"
      "ds_read_b128 v[68:71], v161 offset:11264\n\t"
      "v_mfma_f32_16x16x32_bf16 v[4:7], v[108:111], v[96:99], v[4:7]\n\t"
      "ds_read_b128 v[72:75], v161 offset:25600\n\t"
      "v_mfma_f32_16x16x32_bf16 v[12:15], v[108:111], v[100:103], v[12:15]\n\t"
      "ds_read_b64_tr_b16 v[80:81], v169 offset:2048\n\t"
      "v_mfma_f32_16x16x32_bf16 v[24:27], v[112:115], v[96:99], v[24:27]\n\t"
      "ds_read_b64_tr_b16 v[82:83], v169 offset:2560\n\t"
      "v_mfma_f32_16x16x32_bf16 v[32:35], v[112:115], v[100:103], v[32:35]\n\t"
      "ds_read_b64_tr_b16 v[84:85], v168 offset:3072\n\t"
      "v_mfma_f32_16x16x32_bf16 v[16:19], v[112:115], v[92:95], v[16:19]\n\t"
      "ds_read_b64_tr_b16 v[86:87], v168 offset:3584\n\t"
      "v_mfma_f32_16x16x32_bf16 v[28:31], v[116:119], v[96:99], v[28:31]\n\t"
      "ds_read_b128 v[64:67], v165 offset:11264\n\t"
      "v_mfma_f32_16x16x32_bf16 v[36:39], v[116:119], v[100:103], v[36:39]\n\t"
      "ds_read_b64_tr_b16 v[88:89], v169 offset:3072\n\t"
      "v_mfma_f32_16x16x32_bf16 v[20:23], v[116:119], v[92:95], v[20:23]\n\t"
      "ds_read_b64_tr_b16 v[90:91], v169 offset:3584\n\t"
      "s_barrier\n\t"
      "s_waitcnt lgkmcnt(11)\n\t"
      "v_mfma_f32_16x16x32_bf16 v[0:3], v[132:135], v[124:127], v[0:3]\n\t"
      "ds_read_b64_tr_b16 v[104:105], v168 offset:16384\n\t"
      "ds_read_b64_tr_b16 v[106:107], v168 offset:16896\n\t"
      "v_mfma_f32_16x16x32_bf16 v[8:11], v[132:135], v[128:131], v[8:11]\n\t"
      "ds_read_b128 v[96:99], v160 offset:12288\n\t"
      "v_mfma_f32_16x16x32_bf16 v[4:7], v[136:139], v[124:127], v[4:7]\n\t"
      "ds_read_b128 v[100:103], v160 offset:26624\n\t"
      "v_mfma_f32_16x16x32_bf16 v[12:15], v[136:139], v[128:131], v[12:15]\n\t"
      "ds_read_b64_tr_b16 v[108:109], v169 offset:16384\n\t"
      "v_mfma_f32_16x16x32_bf16 v[24:27], v[140:143], v[124:127], v[24:27]\n\t"
      "ds_read_b64_tr_b16 v[110:111], v169 offset:16896\n\t"
      "v_mfma_f32_16x16x32_bf16 v[32:35], v[140:143], v[128:131], v[32:35]\n\t"
      "ds_read_b64_tr_b16 v[112:113], v168 offset:17408\n\t"
      "v_mfma_f32_16x16x32_bf16 v[16:19], v[140:143], v[120:123], v[16:19]\n\t"
      "ds_read_b64_tr_b16 v[114:115], v168 offset:17920\n\t"
      "v_mfma_f32_16x16x32_bf16 v[28:31], v[144:147], v[124:127], v[28:31]\n\t"
      "ds_read_b128 v[92:95], v164 offset:12288\n\t"
      "v_mfma_f32_16x16x32_bf16 v[36:39], v[144:147], v[128:131], v[36:39]\n\t"
      "ds_read_b64_tr_b16 v[116:117], v169 offset:17408\n\t"
      "v_mfma_f32_16x16x32_bf16 v[20:23], v[144:147], v[120:123], v[20:23]\n\t"
      "ds_read_b64_tr_b16 v[118:119], v169 offset:17920\n\t"
      "s_waitcnt lgkmcnt(11)\n\t"
      "v_mfma_f32_16x16x32_bf16 v[0:3], v[76:79], v[68:71], v[0:3]\n\t"
      "ds_read_b64_tr_b16 v[132:133], v168 offset:18432\n\t"
      "ds_read_b64_tr_b16 v[134:135], v168 offset:18944\n\t"
      "v_mfma_f32_16x16x32_bf16 v[8:11], v[76:79], v[72:75], v[8:11]\n\t"
      "ds_read_b128 v[124:127], v161 offset:13312\n\t"
      "v_mfma_f32_16x16x32_bf16 v[4:7], v[80:83], v[68:71], v[4:7]\n\t"
      "ds_read_b128 v[128:131], v161 offset:27648\n\t"
      "v_mfma_f32_16x16x32_bf16 v[12:15], v[80:83], v[72:75], v[12:15]\n\t"
      "ds_read_b64_tr_b16 v[136:137], v169 offset:18432\n\t"
      "v_mfma_f32_16x16x32_bf16 v[24:27], v[84:87], v[68:71], v[24:27]\n\t"
      "ds_read_b64_tr_b16 v[138:139], v169 offset:18944\n\t"
      "v_mfma_f32_16x16x32_bf16 v[32:35], v[84:87], v[72:75], v[32:35]\n\t"
      "ds_read_b64_tr_b16 v[140:141], v168 offset:19456\n\t"
      "v_mfma_f32_16x16x32_bf16 v[16:19], v[84:87], v[64:67], v[16:19]\n\t"
      "ds_read_b64_tr_b16 v[142:143], v168 offset:19968\n\t"
      "v_mfma_f32_16x16x32_bf16 v[28:31], v[88:91], v[68:71], v[28:31]\n\t"
      "ds_read_b128 v[120:123], v165 offset:13312\n\t"
      "v_mfma_f32_16x16x32_bf16 v[36:39], v[88:91], v[72:75], v[36:39]\n\t"
      "ds_read_b64_tr_b16 v[144:145], v169 offset:19456\n\t"
      "v_mfma_f32_16x16x32_bf16 v[20:23], v[88:91], v[64:67], v[20:23]\n\t"
      "ds_read_b64_tr_b16 v[146:147], v169 offset:19968\n\t"
      "s_barrier\n\t"
      "s_waitcnt lgkmcnt(11)\n\t"
      "v_mfma_f32_16x16x32_bf16 v[0:3], v[104:107], v[96:99], v[0:3]\n\t"
      "ds_read_b64_tr_b16 v[76:77], v166\n\t"
      "v_mfma_f32_16x16x32_bf16 v[8:11], v[104:107], v[100:103], v[8:11]\n\t"
      "ds_read_b64_tr_b16 v[78:79], v166 offset:512\n\t"
      "v_mfma_f32_16x16x32_bf16 v[4:7], v[108:111], v[96:99], v[4:7]\n\t"
      "ds_read_b128 v[68:71], v160 offset:4096\n\t"
      "v_mfma_f32_16x16x32_bf16 v[12:15], v[108:111], v[100:103], v[12:15]\n\t"
      "ds_read_b128 v[72:75], v160 offset:18432\n\t"
      "v_mfma_f32_16x16x32_bf16 v[24:27], v[112:115], v[96:99], v[24:27]\n\t"
      "ds_read_b128 v[64:67], v162 offset:4096\n\t"
      "v_mfma_f32_16x16x32_bf16 v[32:35], v[112:115], v[100:103], v[32:35]\n\t"
      "ds_read_b64_tr_b16 v[80:81], v167\n\t"
      "v_mfma_f32_16x16x32_bf16 v[16:19], v[112:115], v[92:95], v[16:19]\n\t"
      "ds_read_b64_tr_b16 v[82:83], v167 offset:512\n\t"
      "v_mfma_f32_16x16x32_bf16 v[28:31], v[116:119], v[96:99], v[28:31]\n\t"
      "v_mfma_f32_16x16x32_bf16 v[36:39], v[116:119], v[100:103], v[36:39]\n\t"
      "v_mfma_f32_16x16x32_bf16 v[20:23], v[116:119], v[92:95], v[20:23]\n\t"
      "s_waitcnt lgkmcnt(7)\n\t"
      "v_mfma_f32_16x16x32_bf16 v[0:3], v[132:135], v[124:127], v[0:3]\n\t"
      "ds_read_b64_tr_b16 v[104:105], v166 offset:1024\n\t"
      "v_mfma_f32_16x16x32_bf16 v[8:11], v[132:135], v[128:131], v[8:11]\n\t"
      "ds_read_b64_tr_b16 v[106:107], v166 offset:1536\n\t"
      "v_mfma_f32_16x16x32_bf16 v[4:7], v[136:139], v[124:127], v[4:7]\n\t"
      "ds_read_b128 v[96:99], v161 offset:5120\n\t"
      "v_mfma_f32_16x16x32_bf16 v[12:15], v[136:139], v[128:131], v[12:15]\n\t"
      "ds_read_b128 v[100:103], v161 offset:19456\n\t"
      "v_mfma_f32_16x16x32_bf16 v[24:27], v[140:143], v[124:127], v[24:27]\n\t"
      "ds_read_b128 v[92:95], v163 offset:5120\n\t"
      "v_mfma_f32_16x16x32_bf16 v[32:35], v[140:143], v[128:131], v[32:35]\n\t"
      "ds_read_b64_tr_b16 v[108:109], v167 offset:1024\n\t"
      "v_mfma_f32_16x16x32_bf16 v[16:19], v[140:143], v[120:123], v[16:19]\n\t"
      "ds_read_b64_tr_b16 v[110:111], v167 offset:1536\n\t"
      "v_mfma_f32_16x16x32_bf16 v[28:31], v[144:147], v[124:127], v[28:31]\n\t"
      "v_mfma_f32_16x16x32_bf16 v[36:39], v[144:147], v[128:131], v[36:39]\n\t"
      "v_mfma_f32_16x16x32_bf16 v[20:23], v[144:147], v[120:123], v[20:23]\n\t"
      "s_waitcnt lgkmcnt(7)\n\t"
      "v_mfma_f32_16x16x32_bf16 v[48:51], v[68:71], v[76:79], v[48:51]\n\t"
      "ds_read_b64_tr_b16 v[132:133], v166 offset:2048\n\t"
      "ds_read_b64_tr_b16 v[134:135], v166 offset:2560\n\t"
      "v_mfma_f32_16x16x32_bf16 v[56:59], v[72:75], v[76:79], v[56:59]\n\t"
      "ds_read_b128 v[124:127], v160 offset:6144\n\t"
      "v_mfma_f32_16x16x32_bf16 v[40:43], v[64:67], v[76:79], v[40:43]\n\t"
      "ds_read_b128 v[128:131], v160 offset:20480\n\t"
      "v_cvt_pk_bf16_f32 v84, v0, v1\n\t"
      "v_cvt_pk_bf16_f32 v85, v2, v3\n\t"
      "v_cvt_pk_bf16_f32 v86, v4, v5\n\t"
      "v_mfma_f32_16x16x32_bf16 v[52:55], v[68:71], v[80:83], v[52:55]\n\t"
      "ds_read_b128 v[120:123], v162 offset:6144\n\t"
      "v_cvt_pk_bf16_f32 v87, v6, v7\n\t"
      "v_cvt_pk_bf16_f32 v88, v8, v9\n\t"
      "v_cvt_pk_bf16_f32 v89, v10, v11\n\t"
      "v_mfma_f32_16x16x32_bf16 v[60:63], v[72:75], v[80:83], v[60:63]\n\t"
      "ds_read_b64_tr_b16 v[136:137], v167 offset:2048\n\t"
      "v_cvt_pk_bf16_f32 v90, v12, v13\n\t"
      "v_cvt_pk_bf16_f32 v91, v14, v15\n\t"
      "v_cvt_pk_bf16_f32 v152, v24, v25\n\t"
      "v_mfma_f32_16x16x32_bf16 v[44:47], v[64:67], v[80:83], v[44:47]\n\t"
      "ds_read_b64_tr_b16 v[138:139], v167 offset:2560\n\t"
      "v_cvt_pk_bf16_f32 v153, v26, v27\n\t"
      "v_cvt_pk_bf16_f32 v154, v28, v29\n\t"
      "v_cvt_pk_bf16_f32 v155, v30, v31\n\t"
      "s_waitcnt lgkmcnt(7)\n\t"
      "v_mfma_f32_16x16x32_bf16 v[48:51], v[96:99], v[104:107], v[48:51]\n\t"
      "ds_read_b64_tr_b16 v[76:77], v166 offset:3072\n\t"
      "ds_read_b64_tr_b16 v[78:79], v166 offset:3584\n\t"
      "v_cndmask_b32_e64 v86, v86, 0, s[68:69]\n\t"
      "v_cndmask_b32_e64 v87, v87, 0, s[68:69]\n\t"
      "v_cvt_pk_bf16_f32 v156, v32, v33\n\t"
      "v_mfma_f32_16x16x32_bf16 v[56:59], v[100:103], v[104:107], v[56:59]\n\t"
      "ds_read_b128 v[68:71], v161 offset:7168\n\t"
      "v_cvt_pk_bf16_f32 v157, v34, v35\n\t"
      "v_cvt_pk_bf16_f32 v158, v36, v37\n\t"
      "v_cvt_pk_bf16_f32 v159, v38, v39\n\t"
      "v_mfma_f32_16x16x32_bf16 v[40:43], v[92:95], v[104:107], v[40:43]\n\t"
      "ds_read_b128 v[72:75], v161 offset:21504\n\t"
      "v_cndmask_b32_e64 v90, v90, 0, s[68:69]\n\t"
      "v_cndmask_b32_e64 v91, v91, 0, s[68:69]\n\t"
      "v_cvt_pk_bf16_f32 v148, v16, v17\n\t"
      "v_mfma_f32_16x16x32_bf16 v[52:55], v[96:99], v[108:111], v[52:55]\n\t"
      "ds_read_b128 v[64:67], v165 offset:7168\n\t"
      "v_cvt_pk_bf16_f32 v149, v18, v19\n\t"
      "v_cvt_pk_bf16_f32 v150, v20, v21\n\t"
      "v_cvt_pk_bf16_f32 v151, v22, v23\n\t"
      "v_mfma_f32_16x16x32_bf16 v[60:63], v[100:103], v[108:111], v[60:63]\n\t"
      "ds_read_b64_tr_b16 v[80:81], v167 offset:3072\n\t"
      "v_mov_b32_e32 v34, 0x3e8b97f6\n\t"
      "v_mfma_f32_16x16x32_bf16 v[28:31], v[156:159], v[88:91], v[176:179]\n\t"
      "v_mfma_f32_16x16x32_bf16 v[24:27], v[152:155], v[88:91], v[172:175]\n\t"
      "v_mfma_f32_16x16x32_bf16 v[44:47], v[92:95], v[108:111], v[44:47]\n\t"
      "ds_read_b64_tr_b16 v[82:83], v167 offset:3584\n\t"
      "v_mfma_f32_16x16x32_bf16 v[20:23], v[152:155], v[84:87], v[176:179]\n\t"
      "v_mfma_f32_16x16x32_bf16 v[16:19], v[148:151], v[84:87], v[180:183]\n\t"
      "s_barrier\n\t"
      "s_waitcnt lgkmcnt(7)\n\t"
      "v_mfma_f32_16x16x32_bf16 v[48:51], v[124:127], v[132:135], v[48:51]\n\t"
      "ds_read_b64_tr_b16 v[104:105], v166 offset:16384\n\t"
      "ds_read_b64_tr_b16 v[106:107], v166 offset:16896\n\t"
      "v_mfma_f32_16x16x32_bf16 v[56:59], v[128:131], v[132:135], v[56:59]\n\t"
      "ds_read_b128 v[96:99], v160 offset:8192\n\t"
      "v_max_f32_e32 v24, v24, v28\n\t"
      "v_mfma_f32_16x16x32_bf16 v[40:43], v[120:123], v[132:135], v[40:43]\n\t"
      "ds_read_b128 v[100:103], v160 offset:22528\n\t"
      "v_max_f32_e32 v25, v25, v29\n\t"
      "v_max_f32_e32 v26, v26, v30\n\t"
      "v_max_f32_e32 v27, v27, v31\n\t"
      "v_mfma_f32_16x16x32_bf16 v[52:55], v[124:127], v[136:139], v[52:55]\n\t"
      "ds_read_b128 v[92:95], v164 offset:8192\n\t"
      "v_max_f32_e32 v16, v16, v20\n\t"
      "v_max_f32_e32 v17, v17, v21\n\t"
      "v_max_f32_e32 v18, v18, v22\n\t"
      "v_mfma_f32_16x16x32_bf16 v[60:63], v[128:131], v[136:139], v[60:63]\n\t"
      "ds_read_b64_tr_b16 v[108:109], v167 offset:16384\n\t"
      "v_max_f32_e32 v19, v19, v23\n\t"
      "v_max3_f32 v33, v24, v25, v26\n\t"
      "v_max3_f32 v32, v16, v17, v18\n\t"
      "v_mfma_f32_16x16x32_bf16 v[44:47], v[120:123], v[136:139], v[44:47]\n\t"
      "ds_read_b64_tr_b16 v[110:111], v167 offset:16896\n\t"
      "v_max_f32_e32 v33, v33, v27\n\t"
      "v_max_f32_e32 v32, v32, v19\n\t"
      "s_waitcnt lgkmcnt(7)\n\t"
      "v_mfma_f32_16x16x32_bf16 v[48:51], v[68:71], v[76:79], v[48:51]\n\t"
      "ds_read_b64_tr_b16 v[132:133], v166 offset:17408\n\t"
      "ds_read_b64_tr_b16 v[134:135], v166 offset:17920\n\t"
      "v_permlane16_swap_b32_e32 v32, v33\n\t"
      "v_max_f32_e32 v32, v32, v33\n\t"
      "v_mov_b32_e32 v33, v32\n\t"
      "v_mfma_f32_16x16x32_bf16 v[56:59], v[72:75], v[76:79], v[56:59]\n\t"
      "ds_read_b128 v[124:127], v161 offset:9216\n\t"
      "v_permlane32_swap_b32_e32 v32, v33\n\t"
      "v_max_f32_e32 v32, v32, v33\n\t"
      "v_mfma_f32_16x16x32_bf16 v[40:43], v[64:67], v[76:79], v[40:43]\n\t"
      "ds_read_b128 v[128:131], v161 offset:23552\n\t"
      "v_mov_b32_e32 v33, v32\n\t"
      "s_nop 1\n\t"
      "v_permlane16_swap_b32_e32 v32, v33\n\t"
      "v_mfma_f32_16x16x32_bf16 v[52:55], v[68:71], v[80:83], v[52:55]\n\t"
      "ds_read_b128 v[120:123], v165 offset:9216\n\t"
      "v_mul_f32_e64 v33, -v33, v34\n\t"
      "v_mul_f32_e64 v32, -v32, v34\n\t"
      "v_fma_f32 v24, v24, v34, v33\n\t"
      "v_mfma_f32_16x16x32_bf16 v[60:63], v[72:75], v[80:83], v[60:63]\n\t"
      "ds_read_b64_tr_b16 v[136:137], v167 offset:17408\n\t"
      "v_fma_f32 v25, v25, v34, v33\n\t"
      "v_fma_f32 v26, v26, v34, v33\n\t"
      "v_fma_f32 v27, v27, v34, v33\n\t"
      "v_mfma_f32_16x16x32_bf16 v[44:47], v[64:67], v[80:83], v[44:47]\n\t"
      "ds_read_b64_tr_b16 v[138:139], v167 offset:17920\n\t"
      "v_fma_f32 v16, v16, v34, v32\n\t"
      "v_fma_f32 v17, v17, v34, v32\n\t"
      "v_fma_f32 v18, v18, v34, v32\n\t"
      "s_waitcnt lgkmcnt(7)\n\t"
      "v_mfma_f32_16x16x32_bf16 v[48:51], v[96:99], v[104:107], v[48:51]\n\t"
      "ds_read_b64_tr_b16 v[76:77], v166 offset:18432\n\t"
      "ds_read_b64_tr_b16 v[78:79], v166 offset:18944\n\t"
      "v_fma_f32 v19, v19, v34, v32\n\t"
      "v_exp_f32_e32 v24, v24\n\t"
      "v_mfma_f32_16x16x32_bf16 v[56:59], v[100:103], v[104:107], v[56:59]\n\t"
      "ds_read_b128 v[68:71], v160 offset:10240\n\t"
      "v_exp_f32_e32 v25, v25\n\t"
      "v_exp_f32_e32 v26, v26\n\t"
      "v_mfma_f32_16x16x32_bf16 v[40:43], v[92:95], v[104:107], v[40:43]\n\t"
      "ds_read_b128 v[72:75], v160 offset:24576\n\t"
      "v_exp_f32_e32 v27, v27\n\t"
      "v_mfma_f32_16x16x32_bf16 v[52:55], v[96:99], v[108:111], v[52:55]\n\t"
      "ds_read_b128 v[64:67], v164 offset:10240\n\t"
      "v_exp_f32_e32 v16, v16\n\t"
      "v_exp_f32_e32 v17, v17\n\t"
      "v_mfma_f32_16x16x32_bf16 v[60:63], v[100:103], v[108:111], v[60:63]\n\t"
      "ds_read_b64_tr_b16 v[80:81], v167 offset:18432\n\t"
      "v_exp_f32_e32 v18, v18\n\t"
      "v_mfma_f32_16x16x32_bf16 v[44:47], v[92:95], v[108:111], v[44:47]\n\t"
      "ds_read_b64_tr_b16 v[82:83], v167 offset:18944\n\t"
      "v_exp_f32_e32 v19, v19\n\t"
      "v_add_f32_e32 v33, v24, v25\n\t"
      "s_waitcnt lgkmcnt(7)\n\t"
      "v_mfma_f32_16x16x32_bf16 v[48:51], v[124:127], v[132:135], v[48:51]\n\t"
      "ds_read_b64_tr_b16 v[104:105], v166 offset:19456\n\t"
      "ds_read_b64_tr_b16 v[106:107], v166 offset:19968\n\t"
      "v_add_f32_e32 v28, v26, v27\n\t"
      "v_add_f32_e32 v32, v16, v17\n\t"
      "v_add_f32_e32 v20, v18, v19\n\t"
      "v_mfma_f32_16x16x32_bf16 v[56:59], v[128:131], v[132:135], v[56:59]\n\t"
      "ds_read_b128 v[96:99], v161 offset:11264\n\t"
      "v_add_f32_e32 v33, v33, v28\n\t"
      "v_add_f32_e32 v32, v32, v20\n\t"
      "v_mfma_f32_16x16x32_bf16 v[40:43], v[120:123], v[132:135], v[40:43]\n\t"
      "ds_read_b128 v[100:103], v161 offset:25600\n\t"
      "v_permlane16_swap_b32_e32 v32, v33\n\t"
      "v_add_f32_e32 v32, v32, v33\n\t"
      "v_mov_b32_e32 v33, v32\n\t"
      "v_mfma_f32_16x16x32_bf16 v[52:55], v[124:127], v[136:139], v[52:55]\n\t"
      "ds_read_b128 v[92:95], v165 offset:11264\n\t"
      "v_permlane32_swap_b32_e32 v32, v33\n\t"
      "v_add_f32_e32 v32, v32, v33\n\t"
      "v_mfma_f32_16x16x32_bf16 v[60:63], v[128:131], v[136:139], v[60:63]\n\t"
      "ds_read_b64_tr_b16 v[108:109], v167 offset:19456\n\t"
      "v_mov_b32_e32 v33, v32\n\t"
      "s_nop 1\n\t"
      "v_permlane16_swap_b32_e32 v32, v33\n\t"
      "v_mfma_f32_16x16x32_bf16 v[44:47], v[120:123], v[136:139], v[44:47]\n\t"
      "ds_read_b64_tr_b16 v[110:111], v167 offset:19968\n\t"
      "v_rcp_f32_e32 v33, v33\n\t"
      "v_rcp_f32_e32 v32, v32\n\t"
      "s_barrier\n\t"
      "s_waitcnt lgkmcnt(7)\n\t"
      "v_mfma_f32_16x16x32_bf16 v[48:51], v[68:71], v[76:79], v[48:51]\n\t"
      "ds_read_b64_tr_b16 v[132:133], v170 offset:32768\n\t"
      "ds_read_b64_tr_b16 v[134:135], v170 offset:33280\n\t"
      "v_mul_f32_e32 v24, v24, v33\n\t"
      "v_mul_f32_e32 v25, v25, v33\n\t"
      "v_mfma_f32_16x16x32_bf16 v[56:59], v[72:75], v[76:79], v[56:59]\n\t"
      "ds_read_b128 v[124:127], v160 offset:12288\n\t"
      "v_mul_f32_e32 v26, v26, v33\n\t"
      "v_mul_f32_e32 v27, v27, v33\n\t"
      "v_mul_f32_e32 v16, v16, v32\n\t"
      "v_mfma_f32_16x16x32_bf16 v[40:43], v[64:67], v[76:79], v[40:43]\n\t"
      "ds_read_b128 v[128:131], v160 offset:26624\n\t"
      "v_mul_f32_e32 v17, v17, v32\n\t"
      "v_mul_f32_e32 v18, v18, v32\n\t"
      "v_mul_f32_e32 v19, v19, v32\n\t"
      "v_mfma_f32_16x16x32_bf16 v[52:55], v[68:71], v[80:83], v[52:55]\n\t"
      "ds_read_b128 v[120:123], v164 offset:12288\n\t"
      "v_cvt_pk_bf16_f32 v4, v24, v25\n\t"
      "v_cvt_pk_bf16_f32 v5, v26, v27\n\t"
      "v_cvt_pk_bf16_f32 v0, v16, v17\n\t"
      "v_mfma_f32_16x16x32_bf16 v[60:63], v[72:75], v[80:83], v[60:63]\n\t"
      "ds_read_b64_tr_b16 v[136:137], v171 offset:32768\n\t"
      "v_cvt_pk_bf16_f32 v1, v18, v19\n\t"
      "v_and_b32_e32 v6, v184, v4\n\t"
      "v_and_b32_e32 v7, v185, v5\n\t"
      "v_mfma_f32_16x16x32_bf16 v[44:47], v[64:67], v[80:83], v[44:47]\n\t"
      "ds_read_b64_tr_b16 v[138:139], v171 offset:33280\n\t"
      "v_and_b32_e32 v2, v184, v0\n\t"
      "v_and_b32_e32 v3, v185, v1\n\t"
      "v_xor_b32_e32 v4, v4, v6\n\t"
      "s_waitcnt lgkmcnt(7)\n\t"
      "v_mfma_f32_16x16x32_bf16 v[48:51], v[96:99], v[104:107], v[48:51]\n\t"
      "ds_read_b64_tr_b16 v[76:77], v170 offset:33792\n\t"
      "ds_read_b64_tr_b16 v[78:79], v170 offset:34304\n\t"
      "v_xor_b32_e32 v5, v5, v7\n\t"
      "v_xor_b32_e32 v0, v0, v2\n\t"
      "v_xor_b32_e32 v1, v1, v3\n\t"
      "v_mfma_f32_16x16x32_bf16 v[56:59], v[100:103], v[104:107], v[56:59]\n\t"
      "ds_read_b128 v[68:71], v161 offset:13312\n\t"
      "v_mfma_f32_16x16x32_bf16 v[40:43], v[92:95], v[104:107], v[40:43]\n\t"
      "ds_read_b128 v[72:75], v161 offset:27648\n\t"
      "v_mfma_f32_16x16x32_bf16 v[52:55], v[96:99], v[108:111], v[52:55]\n\t"
      "ds_read_b128 v[64:67], v165 offset:13312\n\t"
      "v_mfma_f32_16x16x32_bf16 v[60:63], v[100:103], v[108:111], v[60:63]\n\t"
      "ds_read_b64_tr_b16 v[80:81], v171 offset:33792\n\t"
      "v_mfma_f32_16x16x32_bf16 v[44:47], v[92:95], v[108:111], v[44:47]\n\t"
      "ds_read_b64_tr_b16 v[82:83], v171 offset:34304\n\t"
      "s_waitcnt lgkmcnt(7)\n\t"
      "v_mfma_f32_16x16x32_bf16 v[48:51], v[124:127], v[132:135], v[48:51]\n\t"
      "v_mfma_f32_16x16x32_bf16 v[56:59], v[128:131], v[132:135], v[56:59]\n\t"
      "v_mfma_f32_16x16x32_bf16 v[40:43], v[120:123], v[132:135], v[40:43]\n\t"
      "v_mfma_f32_16x16x32_bf16 v[52:55], v[124:127], v[136:139], v[52:55]\n\t"
      "v_mfma_f32_16x16x32_bf16 v[60:63], v[128:131], v[136:139], v[60:63]\n\t"
      "v_mfma_f32_16x16x32_bf16 v[44:47], v[120:123], v[136:139], v[44:47]\n\t"
      "s_waitcnt lgkmcnt(0)\n\t"
      "v_mfma_f32_16x16x32_bf16 v[48:51], v[68:71], v[76:79], v[48:51]\n\t"
      "v_mfma_f32_16x16x32_bf16 v[56:59], v[72:75], v[76:79], v[56:59]\n\t"
      "v_mfma_f32_16x16x32_bf16 v[40:43], v[64:67], v[76:79], v[40:43]\n\t"
      "v_mfma_f32_16x16x32_bf16 v[52:55], v[68:71], v[80:83], v[52:55]\n\t"
      "v_mfma_f32_16x16x32_bf16 v[60:63], v[72:75], v[80:83], v[60:63]\n\t"
      "v_mfma_f32_16x16x32_bf16 v[44:47], v[64:67], v[80:83], v[44:47]\n\t"
      "s_nop 2\n\t"
      "v_cvt_pk_bf16_f32 v66, v48, v49\n\t"
      "v_cvt_pk_bf16_f32 v67, v50, v51\n\t"
      "v_cvt_pk_bf16_f32 v68, v56, v57\n\t"
      "v_cvt_pk_bf16_f32 v69, v58, v59\n\t"
      "v_cvt_pk_bf16_f32 v64, v40, v41\n\t"
      "v_cvt_pk_bf16_f32 v65, v42, v43\n\t"
      "v_mfma_f32_16x16x32_bf16 v[88:91], v[66:69], v[4:7], 0\n\t"
      "s_nop 0\n\t"
      "v_mfma_f32_16x16x32_bf16 v[80:83], v[64:67], v[0:3], 0\n\t"
      "v_cvt_pk_bf16_f32 v72, v52, v53\n\t"
      "v_cvt_pk_bf16_f32 v73, v54, v55\n\t"
      "v_cvt_pk_bf16_f32 v74, v60, v61\n\t"
      "v_cvt_pk_bf16_f32 v75, v62, v63\n\t"
      "v_cvt_pk_bf16_f32 v70, v44, v45\n\t"
      "v_cvt_pk_bf16_f32 v71, v46, v47\n\t"
      "v_mfma_f32_16x16x32_bf16 v[92:95], v[72:75], v[4:7], 0\n\t"
      "s_nop 0\n\t"
      "v_mfma_f32_16x16x32_bf16 v[84:87], v[70:73], v[0:3], 0\n\t"
      "v_cvt_pk_bf16_f32 v152, v88, v89\n\t"
      "v_cvt_pk_bf16_f32 v153, v90, v91\n\t"
      "v_min_u32_e32 v152, -2, v152\n\t"
      "v_min_u32_e32 v153, -2, v153\n\t"
      "v_cvt_pk_bf16_f32 v148, v80, v81\n\t"
      "v_cvt_pk_bf16_f32 v149, v82, v83\n\t"
      "v_min_u32_e32 v148, -2, v148\n\t"
      "v_min_u32_e32 v149, -2, v149\n\t"
      "v_cvt_pk_bf16_f32 v154, v92, v93\n\t"
      "v_cvt_pk_bf16_f32 v155, v94, v95\n\t"
      "v_cndmask_b32_e64 v154, v154, 0, s[68:69]\n\t"
      "v_min_u32_e32 v154, -2, v154\n\t"
      "v_cndmask_b32_e64 v155, v155, 0, s[68:69]\n\t"
      "v_min_u32_e32 v155, -2, v155\n\t"
      "global_store_dwordx4 v244, v[152:155], s[44:45] sc1\n\t"
      "v_cvt_pk_bf16_f32 v150, v84, v85\n\t"
      "v_cvt_pk_bf16_f32 v151, v86, v87\n\t"
      "v_cndmask_b32_e64 v150, v150, 0, s[68:69]\n\t"
      "v_min_u32_e32 v150, -2, v150\n\t"
      "v_cndmask_b32_e64 v151, v151, 0, s[68:69]\n\t"
      "v_min_u32_e32 v151, -2, v151\n\t"
      "global_store_dwordx4 v244, v[148:151], s[42:43] sc1\n\t"
      "s_barrier\n\t"
      "s_cmp_eq_u32 s26, 7\n\t"
      "s_cbranch_scc1 SPARE_%=\n\t"
      "s_lshl_b32 s34, s25, 16\n\t"
      "s_lshl2_add_u32 s34, s39, s34\n\t"
      "s_add_u32 s34, s34, 4096\n\t"
      "s_add_u32 s42, s20, s34\n\t"
      "s_addc_u32 s43, s21, 0\n\t"
      "s_add_u32 s44, s42, 8192\n\t"
      "s_addc_u32 s45, s43, 0\n\t"
      "s_lshl_b32 s72, s24, 10\n\t"
      "s_add_u32 s72, s72, 98304\n\t"
      "v_xor_b32_e32 v203, v197, v198\n\t"
      "v_lshl_add_u32 v203, v203, 4, s72\n\t"
      "v_lshl_add_u32 v203, v197, 7, v203\n\t"
      "ds_write_b128 v203, v[204:207]\n\t"
      "s_and_b32 s35, s25, 3\n\t"
      "s_lshl2_add_u32 s31, s26, s35\n\t"
      "s_and_b32 s35, s2, 7\n\t"
      "s_mul_i32 s34, s35, 14\n\t"
      "s_lshl4_add_u32 s35, s35, -14\n\t"
      "s_cmp_lt_u32 s31, 14\n\t"
      "s_cselect_b32 s34, s34, s35\n\t"
      "s_cselect_b32 s36, 1, 0\n\t"
      "s_or_b32 s27, s27, s36\n\t"
      "s_add_u32 s34, s34, s31\n\t"
      "s_lshr_b32 s29, s34, 2\n\t"
      "s_and_b32 s32, s34, 3\n\t"
      "s_mul_i32 s30, s32, 7\n\t"
      "v_and_b32_e32 v199, 60, v192\n\t"
      "v_and_b32_e32 v200, 24, v192\n\t"
      "v_lshlrev_b32_e32 v202, 3, v196\n\t"
      "v_lshl_or_b32 v202, v200, 2, v202\n\t"
      "v_lshl_or_b32 v202, v195, 7, v202\n\t"
      "v_lshl_or_b32 v202, v193, 10, v202\n\t"
      "v_add_u32_e32 v224, s39, v202\n\t"
      "v_xor_b32_e32 v225, 32, v224\n\t"
      "v_xor_b32_e32 v226, 64, v224\n\t"
      "v_xor_b32_e32 v227, 96, v224\n\t"
      "s_lshl_b32 s34, s24, 12\n\t"
      "s_add_u32 s34, s34, 98304\n\t"
      "v_add_u32_e32 v245, s34, v244\n\t"
      "v_lshlrev_b32_e32 v199, 2, v199\n\t"
      "s_lshl_b32 s34, s24, 8\n\t"
      "v_lshl_add_u32 v247, v192, 2, s34\n\t"
      "v_and_b32_e32 v200, 2, v192\n\t"
      "v_mul_u32_u24_e32 v202, 576, v200\n\t"
      "v_xor_b32_e32 v199, v202, v199\n\t"
      "v_and_b32_e32 v202, 1, v192\n\t"
      "v_lshl_or_b32 v199, v202, 3, v199\n\t"
      "s_and_b32 s35, s34, 0x400\n\t"
      "s_add_u32 s34, s34, s35\n\t"
      "s_add_u32 s34, s34, 98304\n\t"
      "v_add_u32_e32 v246, s34, v199\n\t"
      "v_lshlrev_b32_e32 v199, 2, v202\n\t"
      "v_lshl_or_b32 v199, v200, 4, v199\n\t"
      "s_waitcnt lgkmcnt(0)\n\t"
      "s_barrier\n\t"
      "v_xor_b32_e32 v210, 32, v228\n\t"
      "v_xor_b32_e32 v212, 64, v228\n\t"
      "v_xor_b32_e32 v214, 96, v228\n\t"
      "ds_read_b64 v[208:209], v228\n\t"
      "ds_read_b64 v[210:211], v210\n\t"
      "ds_read_b64 v[212:213], v212\n\t"
      "ds_read_b64 v[214:215], v214\n\t"
      "ds_read_b64_tr_b16 v[0:1], v216\n\t"
      "ds_read_b64_tr_b16 v[2:3], v220\n\t"
      "ds_read_b64_tr_b16 v[4:5], v217\n\t"
      "ds_read_b64_tr_b16 v[6:7], v221\n\t"
      "ds_read_b64_tr_b16 v[8:9], v218\n\t"
      "ds_read_b64_tr_b16 v[10:11], v222\n\t"
      "ds_read_b64_tr_b16 v[12:13], v219\n\t"
      "ds_read_b64_tr_b16 v[14:15], v223\n\t"
      "ds_read_b64_tr_b16 v[16:17], v218 offset:3584\n\t"
      "ds_read_b64_tr_b16 v[18:19], v222 offset:3584\n\t"
      "ds_read_b64_tr_b16 v[20:21], v219 offset:3584\n\t"
      "ds_read_b64_tr_b16 v[22:23], v223 offset:3584\n\t"
      "ds_read_b64_tr_b16 v[24:25], v216 offset:3584\n\t"
      "ds_read_b64_tr_b16 v[26:27], v220 offset:3584\n\t"
      "ds_read_b64_tr_b16 v[28:29], v217 offset:3584\n\t"
      "ds_read_b64_tr_b16 v[30:31], v221 offset:3584\n\t"
      "ds_read_b64_tr_b16 v[32:33], v216 offset:7168\n\t"
      "ds_read_b64_tr_b16 v[34:35], v220 offset:7168\n\t"
      "ds_read_b64_tr_b16 v[36:37], v217 offset:7168\n\t"
      "ds_read_b64_tr_b16 v[38:39], v221 offset:7168\n\t"
      "ds_read_b64_tr_b16 v[40:41], v218 offset:7168\n\t"
      "ds_read_b64_tr_b16 v[42:43], v222 offset:7168\n\t"
      "ds_read_b64_tr_b16 v[44:45], v219 offset:7168\n\t"
      "ds_read_b64_tr_b16 v[46:47], v223 offset:7168\n\t"
      "ds_read_b64_tr_b16 v[48:49], v218 offset:10752\n\t"
      "ds_read_b64_tr_b16 v[50:51], v222 offset:10752\n\t"
      "ds_read_b64_tr_b16 v[52:53], v219 offset:10752\n\t"
      "ds_read_b64_tr_b16 v[54:55], v223 offset:10752\n\t"
      "ds_read_b64_tr_b16 v[56:57], v216 offset:10752\n\t"
      "ds_read_b64_tr_b16 v[58:59], v220 offset:10752\n\t"
      "ds_read_b64_tr_b16 v[60:61], v217 offset:10752\n\t"
      "ds_read_b64_tr_b16 v[62:63], v221 offset:10752\n\t"
      "ds_read_b64_tr_b16 v[64:65], v216 offset:14336\n\t"
      "ds_read_b64_tr_b16 v[66:67], v220 offset:14336\n\t"
      "ds_read_b64_tr_b16 v[68:69], v217 offset:14336\n\t"
      "ds_read_b64_tr_b16 v[70:71], v221 offset:14336\n\t"
      "ds_read_b64_tr_b16 v[72:73], v218 offset:14336\n\t"
      "ds_read_b64_tr_b16 v[74:75], v222 offset:14336\n\t"
      "ds_read_b64_tr_b16 v[76:77], v219 offset:14336\n\t"
      "ds_read_b64_tr_b16 v[78:79], v223 offset:14336\n\t"
      "ds_read_b64_tr_b16 v[80:81], v218 offset:17920\n\t"
      "ds_read_b64_tr_b16 v[82:83], v222 offset:17920\n\t"
      "ds_read_b64_tr_b16 v[84:85], v219 offset:17920\n\t"
      "ds_read_b64_tr_b16 v[86:87], v223 offset:17920\n\t"
      "ds_read_b64_tr_b16 v[88:89], v216 offset:17920\n\t"
      "ds_read_b64_tr_b16 v[90:91], v220 offset:17920\n\t"
      "ds_read_b64_tr_b16 v[92:93], v217 offset:17920\n\t"
      "ds_read_b64_tr_b16 v[94:95], v221 offset:17920\n\t"
      "ds_read_b64_tr_b16 v[96:97], v216 offset:21504\n\t"
      "ds_read_b64_tr_b16 v[98:99], v220 offset:21504\n\t"
      "ds_read_b64_tr_b16 v[100:101], v217 offset:21504\n\t"
      "ds_read_b64_tr_b16 v[102:103], v221 offset:21504\n\t"
      "ds_read_b64_tr_b16 v[104:105], v218 offset:21504\n\t"
      "ds_read_b64_tr_b16 v[106:107], v222 offset:21504\n\t"
      "ds_read_b64_tr_b16 v[108:109], v219 offset:21504\n\t"
      "ds_read_b64_tr_b16 v[110:111], v223 offset:21504\n\t"
      "ds_read_b64_tr_b16 v[112:113], v218 offset:25088\n\t"
      "ds_read_b64_tr_b16 v[114:115], v222 offset:25088\n\t"
      "ds_read_b64_tr_b16 v[116:117], v219 offset:25088\n\t"
      "ds_read_b64_tr_b16 v[118:119], v223 offset:25088\n\t"
      "ds_read_b64_tr_b16 v[120:121], v216 offset:25088\n\t"
      "ds_read_b64_tr_b16 v[122:123], v220 offset:25088\n\t"
      "ds_read_b64_tr_b16 v[124:125], v217 offset:25088\n\t"
      "ds_read_b64_tr_b16 v[126:127], v221 offset:25088\n\t"
      "global_load_dwordx4 v[128:131], v244, s[42:43] offset:-4096 sc1\n\t"
      "global_load_dwordx4 v[132:135], v244, s[42:43] offset:-3072 sc1\n\t"
      "global_load_dwordx4 v[136:139], v244, s[42:43] offset:-2048 sc1\n\t"
      "global_load_dwordx4 v[140:143], v244, s[42:43] offset:-1024 sc1\n\t"
      "global_load_dwordx4 v[144:147], v244, s[42:43] sc1\n\t"
      "global_load_dwordx4 v[148:151], v244, s[42:43] offset:1024 sc1\n\t"
      "global_load_dwordx4 v[152:155], v244, s[42:43] offset:2048 sc1\n\t"
      "global_load_dwordx4 v[156:159], v244, s[42:43] offset:3072 sc1\n\t"
      "global_load_dwordx4 v[160:163], v244, s[44:45] offset:-4096 sc1\n\t"
      "global_load_dwordx4 v[164:167], v244, s[44:45] offset:-3072 sc1\n\t"
      "s_min_u32 s34, s29, 27\n\t"
      "s_lshl_b32 s34, s34, 7\n\t"
      "s_lshr_b32 s35, s24, 2\n\t"
      "s_lshl_b32 s35, s35, 6\n\t"
      "s_add_u32 s34, s34, s35\n\t"
      "s_lshr_b32 s35, s39, 9\n\t"
      "s_add_u32 s34, s34, s35\n\t"
      "v_add_u32_e32 v248, s34, v199\n\t"
      "s_add_u32 s48, s20, 8388608\n\t"
      "s_addc_u32 s49, s21, 0\n\t"
      "v_and_b32_e32 v199, 16, v192\n\t"
      "v_lshlrev_b32_e32 v199, 1, v199\n\t"
      "v_and_b32_e32 v200, 32, v192\n\t"
      "s_lshl_b32 s34, s28, 18\n\t"
      "s_lshl_b32 s35, s25, 11\n\t"
      "s_add_u32 s34, s34, s35\n\t"
      "s_lshr_b32 s35, s39, 3\n\t"
      "s_add_u32 s34, s34, s35\n\t"
      "v_and_b32_e32 v201, 0x1f0, v244\n\t"
      "v_lshl_or_b32 v201, v200, 11, v201\n\t"
      "v_add_u32_e32 v229, s34, v201\n\t"
      "v_add_u32_e32 v230, 131072, v229\n\t"
      "s_and_b32 s34, s25, 30\n\t"
      "s_lshr_b32 s35, s39, 13\n\t"
      "s_add_u32 s34, s34, s35\n\t"
      "s_mul_i32 s34, s34, 7\n\t"
      "s_add_u32 s34, s34, s28\n\t"
      "s_lshl_b32 s34, s34, 3\n\t"
      "s_and_b32 s35, s25, 1\n\t"
      "s_add_u32 s34, s34, s35\n\t"
      "s_lshl_b32 s34, s34, 10\n\t"
      "s_and_b32 s35, s24, 1\n\t"
      "s_lshl_b32 s35, s35, 9\n\t"
      "s_add_u32 s34, s34, s35\n\t"
      "s_add_u32 s34, s34, 6293504\n\t"
      "v_lshrrev_b32_e32 v201, 1, v200\n\t"
      "v_lshl_or_b32 v201, v199, 6, v201\n\t"
      "v_lshl_or_b32 v201, v194, 5, v201\n\t"
      "v_add_u32_e32 v241, s34, v201\n\t"
      "s_lshl_b32 s34, s24, 12\n\t"
      "s_add_u32 s34, s34, 131072\n\t"
      "v_xor_b32_e32 v201, v193, v194\n\t"
      "v_lshlrev_b32_e32 v201, 3, v201\n\t"
      "v_lshl_or_b32 v201, v194, 7, v201\n\t"
      "v_add_u32_e32 v233, s34, v201\n\t"
      "v_lshrrev_b32_e32 v199, 2, v199\n\t"
      "v_or_b32_e32 v199, v199, v195\n\t"
      "v_lshrrev_b32_e32 v200, 3, v200\n\t"
      "v_or_b32_e32 v200, v196, v200\n\t"
      "v_xor_b32_e32 v200, v200, v199\n\t"
      "v_lshlrev_b32_e32 v200, 3, v200\n\t"
      "v_lshl_or_b32 v200, v199, 7, v200\n\t"
      "v_add_u32_e32 v237, s34, v200\n\t"
      "v_xor_b32_e32 v234, 32, v233\n\t"
      "v_xor_b32_e32 v238, 32, v237\n\t"
      "v_xor_b32_e32 v235, 64, v233\n\t"
      "v_xor_b32_e32 v239, 64, v237\n\t"
      "v_xor_b32_e32 v236, 96, v233\n\t"
      "v_xor_b32_e32 v240, 96, v237\n\t"
      "s_waitcnt lgkmcnt(14)\n\t"
      "v_lshlrev_b32_e32 v192, 16, v208\n\t"
      "v_and_b32_e32 v193, 0xffff0000, v208\n\t"
      "v_lshlrev_b32_e32 v194, 16, v209\n\t"
      "v_and_b32_e32 v195, 0xffff0000, v209\n\t"
      "v_lshlrev_b32_e32 v196, 16, v210\n\t"
      "v_and_b32_e32 v197, 0xffff0000, v210\n\t"
      "v_lshlrev_b32_e32 v198, 16, v211\n\t"
      "v_and_b32_e32 v199, 0xffff0000, v211\n\t"
      "v_lshlrev_b32_e32 v200, 16, v212\n\t"
      "v_and_b32_e32 v201, 0xffff0000, v212\n\t"
      "v_lshlrev_b32_e32 v202, 16, v213\n\t"
      "v_and_b32_e32 v203, 0xffff0000, v213\n\t"
      "v_lshlrev_b32_e32 v204, 16, v214\n\t"
      "v_and_b32_e32 v205, 0xffff0000, v214\n\t"
      "v_lshlrev_b32_e32 v206, 16, v215\n\t"
      "v_and_b32_e32 v207, 0xffff0000, v215\n\t"
      "global_load_dwordx4 v[168:171], v244, s[44:45] offset:-2048 sc1\n\t"
      "global_load_dwordx4 v[172:175], v244, s[44:45] offset:-1024 sc1\n\t"
      "global_load_dwordx4 v[176:179], v244, s[44:45] sc1\n\t"
      "global_load_dwordx4 v[180:183], v244, s[44:45] offset:1024 sc1\n\t"
      "global_load_dwordx4 v[184:187], v244, s[44:45] offset:2048 sc1\n\t"
      "global_load_dwordx4 v[188:191], v244, s[44:45] offset:3072 sc1\n\t"
      "global_load_dword v249, v248, s[12:13]\n\t"
      "s_barrier\n\t"
      "s_waitcnt lgkmcnt(0)\n\t"
      "LGM_%=:\n\t"
      "s_waitcnt vmcnt(15)\n\t"
      "v_max_u32_e32 v232, v128, v132\n\t"
      "v_cmp_eq_u32_e32 vcc, -1, v232\n\t"
      "s_cbranch_vccnz LO0R_%=\n\t"
      "LO0B_%=:\n\t"
      "v_mfma_f32_16x16x32_bf16 v[192:195], v[0:3], v[128:131], v[192:195]\n\t"
      "ds_read_b64_tr_b16 v[0:1], v216 offset:28672\n\t"
      "ds_read_b64_tr_b16 v[2:3], v220 offset:28672\n\t"
      "v_mfma_f32_16x16x32_bf16 v[196:199], v[4:7], v[128:131], v[196:199]\n\t"
      "ds_read_b64_tr_b16 v[4:5], v217 offset:28672\n\t"
      "v_mfma_f32_16x16x32_bf16 v[200:203], v[8:11], v[128:131], v[200:203]\n\t"
      "ds_read_b64_tr_b16 v[6:7], v221 offset:28672\n\t"
      "v_mfma_f32_16x16x32_bf16 v[204:207], v[12:15], v[128:131], v[204:207]\n\t"
      "ds_read_b64_tr_b16 v[8:9], v218 offset:28672\n\t"
      "ds_read_b64_tr_b16 v[10:11], v222 offset:28672\n\t"
      "v_mfma_f32_16x16x32_bf16 v[192:195], v[16:19], v[132:135], v[192:195]\n\t"
      "ds_read_b64_tr_b16 v[12:13], v219 offset:28672\n\t"
      "v_mfma_f32_16x16x32_bf16 v[196:199], v[20:23], v[132:135], v[196:199]\n\t"
      "ds_read_b64_tr_b16 v[14:15], v223 offset:28672\n\t"
      "v_mfma_f32_16x16x32_bf16 v[200:203], v[24:27], v[132:135], v[200:203]\n\t"
      "ds_read_b64_tr_b16 v[16:17], v218 offset:32256\n\t"
      "ds_read_b64_tr_b16 v[18:19], v222 offset:32256\n\t"
      "v_mfma_f32_16x16x32_bf16 v[204:207], v[28:31], v[132:135], v[204:207]\n\t"
      "ds_read_b64_tr_b16 v[20:21], v219 offset:32256\n\t"
      "s_waitcnt vmcnt(13)\n\t"
      "v_max_u32_e32 v232, v136, v140\n\t"
      "v_cmp_eq_u32_e32 vcc, -1, v232\n\t"
      "s_cbranch_vccnz LO2R_%=\n\t"
      "LO2B_%=:\n\t"
      "v_mfma_f32_16x16x32_bf16 v[192:195], v[32:35], v[136:139], v[192:195]\n\t"
      "ds_read_b64_tr_b16 v[22:23], v223 offset:32256\n\t"
      "v_mfma_f32_16x16x32_bf16 v[196:199], v[36:39], v[136:139], v[196:199]\n\t"
      "ds_read_b64_tr_b16 v[24:25], v216 offset:32256\n\t"
      "ds_read_b64_tr_b16 v[26:27], v220 offset:32256\n\t"
      "v_mfma_f32_16x16x32_bf16 v[200:203], v[40:43], v[136:139], v[200:203]\n\t"
      "ds_read_b64_tr_b16 v[28:29], v217 offset:32256\n\t"
      "v_mfma_f32_16x16x32_bf16 v[204:207], v[44:47], v[136:139], v[204:207]\n\t"
      "ds_read_b64_tr_b16 v[30:31], v221 offset:32256\n\t"
      "v_mfma_f32_16x16x32_bf16 v[192:195], v[48:51], v[140:143], v[192:195]\n\t"
      "ds_read_b64_tr_b16 v[32:33], v216 offset:35840\n\t"
      "ds_read_b64_tr_b16 v[34:35], v220 offset:35840\n\t"
      "v_mfma_f32_16x16x32_bf16 v[196:199], v[52:55], v[140:143], v[196:199]\n\t"
      "ds_read_b64_tr_b16 v[36:37], v217 offset:35840\n\t"
      "v_mfma_f32_16x16x32_bf16 v[200:203], v[56:59], v[140:143], v[200:203]\n\t"
      "ds_read_b64_tr_b16 v[38:39], v221 offset:35840\n\t"
      "v_mfma_f32_16x16x32_bf16 v[204:207], v[60:63], v[140:143], v[204:207]\n\t"
      "ds_read_b64_tr_b16 v[40:41], v218 offset:35840\n\t"
      "ds_read_b64_tr_b16 v[42:43], v222 offset:35840\n\t"
      "s_waitcnt vmcnt(11)\n\t"
      "v_max_u32_e32 v232, v144, v148\n\t"
      "v_cmp_eq_u32_e32 vcc, -1, v232\n\t"
      "s_cbranch_vccnz LO4R_%=\n\t"
      "LO4B_%=:\n\t"
      "v_mfma_f32_16x16x32_bf16 v[192:195], v[64:67], v[144:147], v[192:195]\n\t"
      "ds_read_b64_tr_b16 v[44:45], v219 offset:35840\n\t"
      "v_mfma_f32_16x16x32_bf16 v[196:199], v[68:71], v[144:147], v[196:199]\n\t"
      "ds_read_b64_tr_b16 v[46:47], v223 offset:35840\n\t"
      "v_mfma_f32_16x16x32_bf16 v[200:203], v[72:75], v[144:147], v[200:203]\n\t"
      "ds_read_b64_tr_b16 v[48:49], v218 offset:39424\n\t"
      "ds_read_b64_tr_b16 v[50:51], v222 offset:39424\n\t"
      "v_mfma_f32_16x16x32_bf16 v[204:207], v[76:79], v[144:147], v[204:207]\n\t"
      "ds_read_b64_tr_b16 v[52:53], v219 offset:39424\n\t"
      "v_mfma_f32_16x16x32_bf16 v[192:195], v[80:83], v[148:151], v[192:195]\n\t"
      "ds_read_b64_tr_b16 v[54:55], v223 offset:39424\n\t"
      "v_mfma_f32_16x16x32_bf16 v[196:199], v[84:87], v[148:151], v[196:199]\n\t"
      "ds_read_b64_tr_b16 v[56:57], v216 offset:39424\n\t"
      "ds_read_b64_tr_b16 v[58:59], v220 offset:39424\n\t"
      "v_mfma_f32_16x16x32_bf16 v[200:203], v[88:91], v[148:151], v[200:203]\n\t"
      "ds_read_b64_tr_b16 v[60:61], v217 offset:39424\n\t"
      "v_mfma_f32_16x16x32_bf16 v[204:207], v[92:95], v[148:151], v[204:207]\n\t"
      "ds_read_b64_tr_b16 v[62:63], v221 offset:39424\n\t"
      "s_waitcnt vmcnt(9)\n\t"
      "v_max_u32_e32 v232, v152, v156\n\t"
      "v_cmp_eq_u32_e32 vcc, -1, v232\n\t"
      "s_cbranch_vccnz LO6R_%=\n\t"
      "LO6B_%=:\n\t"
      "v_mfma_f32_16x16x32_bf16 v[192:195], v[96:99], v[152:155], v[192:195]\n\t"
      "ds_read_b64_tr_b16 v[64:65], v216 offset:43008\n\t"
      "ds_read_b64_tr_b16 v[66:67], v220 offset:43008\n\t"
      "v_mfma_f32_16x16x32_bf16 v[196:199], v[100:103], v[152:155], v[196:199]\n\t"
      "ds_read_b64_tr_b16 v[68:69], v217 offset:43008\n\t"
      "v_mfma_f32_16x16x32_bf16 v[200:203], v[104:107], v[152:155], v[200:203]\n\t"
      "ds_read_b64_tr_b16 v[70:71], v221 offset:43008\n\t"
      "v_mfma_f32_16x16x32_bf16 v[204:207], v[108:111], v[152:155], v[204:207]\n\t"
      "ds_read_b64_tr_b16 v[72:73], v218 offset:43008\n\t"
      "ds_read_b64_tr_b16 v[74:75], v222 offset:43008\n\t"
      "v_mfma_f32_16x16x32_bf16 v[192:195], v[112:115], v[156:159], v[192:195]\n\t"
      "ds_read_b64_tr_b16 v[76:77], v219 offset:43008\n\t"
      "v_mfma_f32_16x16x32_bf16 v[196:199], v[116:119], v[156:159], v[196:199]\n\t"
      "ds_read_b64_tr_b16 v[78:79], v223 offset:43008\n\t"
      "v_mfma_f32_16x16x32_bf16 v[200:203], v[120:123], v[156:159], v[200:203]\n\t"
      "ds_read_b64_tr_b16 v[80:81], v218 offset:46592\n\t"
      "ds_read_b64_tr_b16 v[82:83], v222 offset:46592\n\t"
      "v_mfma_f32_16x16x32_bf16 v[204:207], v[124:127], v[156:159], v[204:207]\n\t"
      "ds_read_b64_tr_b16 v[84:85], v219 offset:46592\n\t"
      "s_waitcnt vmcnt(7)\n\t"
      "v_max_u32_e32 v232, v160, v164\n\t"
      "v_cmp_eq_u32_e32 vcc, -1, v232\n\t"
      "s_cbranch_vccnz LO8R_%=\n\t"
      "LO8B_%=:\n\t"
      "s_waitcnt lgkmcnt(14)\n\t"
      "v_mfma_f32_16x16x32_bf16 v[192:195], v[0:3], v[160:163], v[192:195]\n\t"
      "ds_read_b64_tr_b16 v[86:87], v223 offset:46592\n\t"
      "v_mfma_f32_16x16x32_bf16 v[196:199], v[4:7], v[160:163], v[196:199]\n\t"
      "ds_read_b64_tr_b16 v[88:89], v216 offset:46592\n\t"
      "ds_read_b64_tr_b16 v[90:91], v220 offset:46592\n\t"
      "v_mfma_f32_16x16x32_bf16 v[200:203], v[8:11], v[160:163], v[200:203]\n\t"
      "ds_read_b64_tr_b16 v[92:93], v217 offset:46592\n\t"
      "v_mfma_f32_16x16x32_bf16 v[204:207], v[12:15], v[160:163], v[204:207]\n\t"
      "ds_read_b64_tr_b16 v[94:95], v221 offset:46592\n\t"
      "v_mfma_f32_16x16x32_bf16 v[192:195], v[16:19], v[164:167], v[192:195]\n\t"
      "ds_read_b64_tr_b16 v[96:97], v216 offset:50176\n\t"
      "ds_read_b64_tr_b16 v[98:99], v220 offset:50176\n\t"
      "v_mfma_f32_16x16x32_bf16 v[196:199], v[20:23], v[164:167], v[196:199]\n\t"
      "ds_read_b64_tr_b16 v[100:101], v217 offset:50176\n\t"
      "v_mfma_f32_16x16x32_bf16 v[200:203], v[24:27], v[164:167], v[200:203]\n\t"
      "ds_read_b64_tr_b16 v[102:103], v221 offset:50176\n\t"
      "v_mfma_f32_16x16x32_bf16 v[204:207], v[28:31], v[164:167], v[204:207]\n\t"
      "ds_read_b64_tr_b16 v[104:105], v218 offset:50176\n\t"
      "ds_read_b64_tr_b16 v[106:107], v222 offset:50176\n\t"
      "s_waitcnt vmcnt(5)\n\t"
      "v_max_u32_e32 v232, v168, v172\n\t"
      "v_cmp_eq_u32_e32 vcc, -1, v232\n\t"
      "s_cbranch_vccnz LO10R_%=\n\t"
      "LO10B_%=:\n\t"
      "v_mfma_f32_16x16x32_bf16 v[192:195], v[32:35], v[168:171], v[192:195]\n\t"
      "ds_read_b64_tr_b16 v[108:109], v219 offset:50176\n\t"
      "v_mfma_f32_16x16x32_bf16 v[196:199], v[36:39], v[168:171], v[196:199]\n\t"
      "ds_read_b64_tr_b16 v[110:111], v223 offset:50176\n\t"
      "v_mfma_f32_16x16x32_bf16 v[200:203], v[40:43], v[168:171], v[200:203]\n\t"
      "ds_read_b64_tr_b16 v[112:113], v218 offset:53760\n\t"
      "ds_read_b64_tr_b16 v[114:115], v222 offset:53760\n\t"
      "v_mfma_f32_16x16x32_bf16 v[204:207], v[44:47], v[168:171], v[204:207]\n\t"
      "ds_read_b64_tr_b16 v[116:117], v219 offset:53760\n\t"
      "s_waitcnt lgkmcnt(14)\n\t"
      "v_mfma_f32_16x16x32_bf16 v[192:195], v[48:51], v[172:175], v[192:195]\n\t"
      "ds_read_b64_tr_b16 v[118:119], v223 offset:53760\n\t"
      "v_mfma_f32_16x16x32_bf16 v[196:199], v[52:55], v[172:175], v[196:199]\n\t"
      "ds_read_b64_tr_b16 v[120:121], v216 offset:53760\n\t"
      "ds_read_b64_tr_b16 v[122:123], v220 offset:53760\n\t"
      "v_mfma_f32_16x16x32_bf16 v[200:203], v[56:59], v[172:175], v[200:203]\n\t"
      "ds_read_b64_tr_b16 v[124:125], v217 offset:53760\n\t"
      "v_mfma_f32_16x16x32_bf16 v[204:207], v[60:63], v[172:175], v[204:207]\n\t"
      "ds_read_b64_tr_b16 v[126:127], v221 offset:53760\n\t"
      "s_waitcnt vmcnt(3)\n\t"
      "v_max_u32_e32 v232, v176, v180\n\t"
      "v_cmp_eq_u32_e32 vcc, -1, v232\n\t"
      "s_cbranch_vccnz LO12R_%=\n\t"
      "LO12B_%=:\n\t"
      "v_mfma_f32_16x16x32_bf16 v[192:195], v[64:67], v[176:179], v[192:195]\n\t"
      "v_mfma_f32_16x16x32_bf16 v[196:199], v[68:71], v[176:179], v[196:199]\n\t"
      "v_mfma_f32_16x16x32_bf16 v[200:203], v[72:75], v[176:179], v[200:203]\n\t"
      "v_mfma_f32_16x16x32_bf16 v[204:207], v[76:79], v[176:179], v[204:207]\n\t"
      "s_waitcnt lgkmcnt(14)\n\t"
      "v_mfma_f32_16x16x32_bf16 v[192:195], v[80:83], v[180:183], v[192:195]\n\t"
      "v_mfma_f32_16x16x32_bf16 v[196:199], v[84:87], v[180:183], v[196:199]\n\t"
      "v_mfma_f32_16x16x32_bf16 v[200:203], v[88:91], v[180:183], v[200:203]\n\t"
      "v_mfma_f32_16x16x32_bf16 v[204:207], v[92:95], v[180:183], v[204:207]\n\t"
      "s_waitcnt vmcnt(1)\n\t"
      "v_max_u32_e32 v232, v184, v188\n\t"
      "v_cmp_eq_u32_e32 vcc, -1, v232\n\t"
      "s_cbranch_vccnz LO14R_%=\n\t"
      "LO14B_%=:\n\t"
      "s_waitcnt lgkmcnt(8)\n\t"
      "v_mfma_f32_16x16x32_bf16 v[192:195], v[96:99], v[184:187], v[192:195]\n\t"
      "v_mfma_f32_16x16x32_bf16 v[196:199], v[100:103], v[184:187], v[196:199]\n\t"
      "v_mfma_f32_16x16x32_bf16 v[200:203], v[104:107], v[184:187], v[200:203]\n\t"
      "v_mfma_f32_16x16x32_bf16 v[204:207], v[108:111], v[184:187], v[204:207]\n\t"
      "s_waitcnt lgkmcnt(0)\n\t"
      "v_mfma_f32_16x16x32_bf16 v[192:195], v[112:115], v[188:191], v[192:195]\n\t"
      "v_mfma_f32_16x16x32_bf16 v[196:199], v[116:119], v[188:191], v[196:199]\n\t"
      "v_mfma_f32_16x16x32_bf16 v[200:203], v[120:123], v[188:191], v[200:203]\n\t"
      "v_mfma_f32_16x16x32_bf16 v[204:207], v[124:127], v[188:191], v[204:207]\n\t"
      "s_nop 4\n\t"
      "v_cmp_u_f32_e32 vcc, v192, v192\n\t"
      "s_cbranch_vccnz LGX_%=\n\t"
      "LGB_%=:\n\t"
      "v_cvt_pk_bf16_f32 v208, v192, v193\n\t"
      "v_cvt_pk_bf16_f32 v209, v194, v195\n\t"
      "v_cvt_pk_bf16_f32 v210, v196, v197\n\t"
      "v_cvt_pk_bf16_f32 v211, v198, v199\n\t"
      "v_cvt_pk_bf16_f32 v212, v200, v201\n\t"
      "v_cvt_pk_bf16_f32 v213, v202, v203\n\t"
      "v_cvt_pk_bf16_f32 v214, v204, v205\n\t"
      "v_cvt_pk_bf16_f32 v215, v206, v207\n\t"
      "v_mov_b32_e32 v148, 0x3f803f80\n\t"
      "v_dot2_f32_bf16 v144, v208, v148, 0\n\t"
      "v_dot2_f32_bf16 v145, v208, v208, 0\n\t"
      "v_dot2_f32_bf16 v146, v209, v148, 0\n\t"
      "v_dot2_f32_bf16 v147, v209, v209, 0\n\t"
      "v_dot2_f32_bf16 v144, v210, v148, v144\n\t"
      "v_dot2_f32_bf16 v145, v210, v210, v145\n\t"
      "v_dot2_f32_bf16 v146, v211, v148, v146\n\t"
      "v_dot2_f32_bf16 v147, v211, v211, v147\n\t"
      "v_dot2_f32_bf16 v144, v212, v148, v144\n\t"
      "v_dot2_f32_bf16 v145, v212, v212, v145\n\t"
      "v_dot2_f32_bf16 v146, v213, v148, v146\n\t"
      "v_dot2_f32_bf16 v147, v213, v213, v147\n\t"
      "v_dot2_f32_bf16 v144, v214, v148, v144\n\t"
      "v_dot2_f32_bf16 v145, v214, v214, v145\n\t"
      "v_dot2_f32_bf16 v146, v215, v148, v146\n\t"
      "v_dot2_f32_bf16 v147, v215, v215, v147\n\t"
      "v_lshlrev_b32_e32 v128, 16, v208\n\t"
      "v_and_b32_e32 v129, 0xffff0000, v208\n\t"
      "v_lshlrev_b32_e32 v130, 16, v209\n\t"
      "v_pk_add_f32 v[144:145], v[144:145], v[146:147]\n\t"
      "v_and_b32_e32 v131, 0xffff0000, v209\n\t"
      "v_lshlrev_b32_e32 v132, 16, v210\n\t"
      "v_permlane16_swap_b32_e32 v144, v145\n\t"
      "v_add_f32_e32 v144, v144, v145\n\t"
      "v_mov_b32_e32 v145, v144\n\t"
      "v_and_b32_e32 v133, 0xffff0000, v210\n\t"
      "v_lshlrev_b32_e32 v134, 16, v211\n\t"
      "v_permlane32_swap_b32_e32 v144, v145\n\t"
      "v_add_f32_e32 v144, v144, v145\n\t"
      "v_mov_b32_e32 v145, v144\n\t"
      "v_and_b32_e32 v135, 0xffff0000, v211\n\t"
      "v_lshlrev_b32_e32 v136, 16, v212\n\t"
      "v_permlane16_swap_b32_e32 v144, v145\n\t"
      "s_mov_b64 exec, s[70:71]\n\t"
      "v_min_u32_e32 v144, -2, v144\n\t"
      "v_min_u32_e32 v145, -2, v145\n\t"
      "global_store_dwordx2 v231, v[144:145], s[58:59] sc1\n\t"
      "s_mov_b64 exec, -1\n\t"
      "s_barrier\n\t"
      "v_and_b32_e32 v137, 0xffff0000, v212\n\t"
      "v_lshlrev_b32_e32 v138, 16, v213\n\t"
      "v_and_b32_e32 v139, 0xffff0000, v213\n\t"
      "v_lshlrev_b32_e32 v140, 16, v214\n\t"
      "v_and_b32_e32 v141, 0xffff0000, v214\n\t"
      "v_lshlrev_b32_e32 v142, 16, v215\n\t"
      "v_and_b32_e32 v143, 0xffff0000, v215\n\t"
      "v_permlane16_swap_b32_e32 v208, v210\n\t"
      "v_permlane16_swap_b32_e32 v209, v211\n\t"
      "v_permlane16_swap_b32_e32 v212, v214\n\t"
      "v_permlane16_swap_b32_e32 v213, v215\n\t"
      "s_bitcmp0_b32 s27, 0\n\t"
      "s_cbranch_scc1 NTK0_%=\n\t"
      "ds_read_b64_tr_b16 v[0:1], v224 offset:16384\n\t"
      "ds_read_b64_tr_b16 v[2:3], v224 offset:16896\n\t"
      "ds_read_b64_tr_b16 v[4:5], v225 offset:16384\n\t"
      "ds_read_b64_tr_b16 v[6:7], v225 offset:16896\n\t"
      "ds_read_b64_tr_b16 v[8:9], v226 offset:16384\n\t"
      "ds_read_b64_tr_b16 v[10:11], v226 offset:16896\n\t"
      "ds_read_b64_tr_b16 v[12:13], v227 offset:16384\n\t"
      "ds_read_b64_tr_b16 v[14:15], v227 offset:16896\n\t"
      "ds_read_b64_tr_b16 v[16:17], v224 offset:49152\n\t"
      "ds_read_b64_tr_b16 v[18:19], v224 offset:49664\n\t"
      "ds_read_b64_tr_b16 v[20:21], v225 offset:49152\n\t"
      "ds_read_b64_tr_b16 v[22:23], v225 offset:49664\n\t"
      "ds_read_b64_tr_b16 v[24:25], v226 offset:49152\n\t"
      "ds_read_b64_tr_b16 v[26:27], v226 offset:49664\n\t"
      "ds_read_b64_tr_b16 v[28:29], v227 offset:49152\n\t"
      "ds_read_b64_tr_b16 v[30:31], v227 offset:49664\n\t"
      "ds_read_b64_tr_b16 v[32:33], v224\n\t"
      "ds_read_b64_tr_b16 v[34:35], v224 offset:512\n\t"
      "ds_read_b64_tr_b16 v[36:37], v225\n\t"
      "ds_read_b64_tr_b16 v[38:39], v225 offset:512\n\t"
      "ds_read_b64_tr_b16 v[40:41], v226\n\t"
      "ds_read_b64_tr_b16 v[42:43], v226 offset:512\n\t"
      "ds_read_b64_tr_b16 v[44:45], v227\n\t"
      "ds_read_b64_tr_b16 v[46:47], v227 offset:512\n\t"
      "ds_read_b64_tr_b16 v[48:49], v224 offset:32768\n\t"
      "ds_read_b64_tr_b16 v[50:51], v224 offset:33280\n\t"
      "ds_read_b64_tr_b16 v[52:53], v225 offset:32768\n\t"
      "ds_read_b64_tr_b16 v[54:55], v225 offset:33280\n\t"
      "ds_read_b64_tr_b16 v[56:57], v226 offset:32768\n\t"
      "ds_read_b64_tr_b16 v[58:59], v226 offset:33280\n\t"
      "ds_read_b64_tr_b16 v[60:61], v227 offset:32768\n\t"
      "ds_read_b64_tr_b16 v[62:63], v227 offset:33280\n\t"
      "NTK0_%=:\n\t"
      "s_waitcnt lgkmcnt(0)\n\t"
      "s_barrier\n\t"
      "global_load_dwordx2 v[148:149], v231, s[46:47] sc1\n\t"
      "global_load_dwordx2 v[150:151], v231, s[46:47] offset:384 sc1\n\t"
      "s_waitcnt vmcnt(0)\n\t"
      "v_max3_u32 v232, v148, v149, v150\n\t"
      "v_max_u32_e32 v232, v232, v151\n\t"
      "v_cmp_eq_u32_e32 vcc, -1, v232\n\t"
      "s_cbranch_vccnz LSR_%=\n\t"
      "LSB_%=:\n\t"
      "v_cndmask_b32_e64 v150, v150, 0, s[70:71]\n\t"
      "v_cndmask_b32_e64 v151, v151, 0, s[70:71]\n\t"
      "v_pk_add_f32 v[144:145], v[148:149], v[150:151]\n\t"
      "s_nop 1\n\t"
      "v_permlane16_swap_b32_e32 v144, v145\n\t"
      "v_add_f32_e32 v144, v144, v145\n\t"
      "v_mov_b32_e32 v145, v144\n\t"
      "s_nop 1\n\t"
      "v_permlane32_swap_b32_e32 v144, v145\n\t"
      "v_add_f32_e32 v144, v144, v145\n\t"
      "v_mov_b32_e32 v145, v144\n\t"
      "s_nop 1\n\t"
      "v_permlane16_swap_b32_e32 v144, v145\n\t"
      "v_mul_f32_e32 v144, 0xbb124925, v144\n\t"
      "v_mul_f32_e32 v145, 0x3b124925, v145\n\t"
      "v_fma_f32 v145, -v144, v144, v145\n\t"
      "v_max_f32_e32 v145, 0, v145\n\t"
      "v_add_f32_e32 v146, 0x3727c5ac, v145\n\t"
      "v_fmac_f32_e32 v145, 0x3727c5ac, v146\n\t"
      "v_rsq_f32_e32 v145, v145\n\t"
      "s_nop 0\n\t"
      "v_mul_f32_e32 v144, v144, v145\n\t"
      "v_pk_fma_f32 v[128:129], v[128:129], v[144:145], v[144:145] op_sel:[0,1,0] op_sel_hi:[1,1,0]\n\t"
      "v_pk_fma_f32 v[130:131], v[130:131], v[144:145], v[144:145] op_sel:[0,1,0] op_sel_hi:[1,1,0]\n\t"
      "v_cvt_pk_bf16_f32 v152, v128, v129\n\t"
      "v_cvt_pk_bf16_f32 v153, v130, v131\n\t"
      "ds_write_b64 v233, v[152:153]\n\t"
      "v_pk_fma_f32 v[132:133], v[132:133], v[144:145], v[144:145] op_sel:[0,1,0] op_sel_hi:[1,1,0]\n\t"
      "v_pk_fma_f32 v[134:135], v[134:135], v[144:145], v[144:145] op_sel:[0,1,0] op_sel_hi:[1,1,0]\n\t"
      "v_cvt_pk_bf16_f32 v154, v132, v133\n\t"
      "v_cvt_pk_bf16_f32 v155, v134, v135\n\t"
      "ds_write_b64 v234, v[154:155]\n\t"
      "ds_read_b64_tr_b16 v[160:161], v237\n\t"
      "ds_read_b64_tr_b16 v[162:163], v238 offset:512\n\t"
      "v_pk_fma_f32 v[136:137], v[136:137], v[144:145], v[144:145] op_sel:[0,1,0] op_sel_hi:[1,1,0]\n\t"
      "v_pk_fma_f32 v[138:139], v[138:139], v[144:145], v[144:145] op_sel:[0,1,0] op_sel_hi:[1,1,0]\n\t"
      "v_cvt_pk_bf16_f32 v156, v136, v137\n\t"
      "v_cvt_pk_bf16_f32 v157, v138, v139\n\t"
      "ds_write_b64 v235, v[156:157]\n\t"
      "v_pk_fma_f32 v[140:141], v[140:141], v[144:145], v[144:145] op_sel:[0,1,0] op_sel_hi:[1,1,0]\n\t"
      "v_pk_fma_f32 v[142:143], v[142:143], v[144:145], v[144:145] op_sel:[0,1,0] op_sel_hi:[1,1,0]\n\t"
      "v_cvt_pk_bf16_f32 v158, v140, v141\n\t"
      "v_cvt_pk_bf16_f32 v159, v142, v143\n\t"
      "ds_write_b64 v236, v[158:159]\n\t"
      "ds_read_b64_tr_b16 v[164:165], v239\n\t"
      "ds_read_b64_tr_b16 v[166:167], v240 offset:512\n\t"
      "s_waitcnt lgkmcnt(4)\n\t"
      "v_min_u32_e32 v160, -2, v160\n\t"
      "v_min_u32_e32 v161, -2, v161\n\t"
      "v_min_u32_e32 v162, -2, v162\n\t"
      "v_min_u32_e32 v163, -2, v163\n\t"
      "global_store_dwordx4 v229, v[160:163], s[48:49] sc1\n\t"
      "s_waitcnt lgkmcnt(0)\n\t"
      "v_min_u32_e32 v164, -2, v164\n\t"
      "v_min_u32_e32 v165, -2, v165\n\t"
      "v_min_u32_e32 v166, -2, v166\n\t"
      "v_min_u32_e32 v167, -2, v167\n\t"
      "global_store_dwordx4 v230, v[164:167], s[48:49] sc1\n\t"
      "s_barrier\n\t"
      "s_bitcmp0_b32 s27, 0\n\t"
      "s_cbranch_scc1 G4C_%=\n\t"
      "s_mul_i32 s34, s32, 196608\n\t"
      "s_mul_i32 s35, s24, 8192\n\t"
      "s_add_u32 s34, s34, s35\n\t"
      "s_add_u32 s34, s34, 1048576\n\t"
      "s_add_u32 s64, s48, s34\n\t"
      "s_addc_u32 s65, s49, 0\n\t"
      "v_add_u32_e32 v250, 4096, v244\n\t"
      "global_load_dwordx4 v[128:131], v250, s[64:65] offset:-4096 sc1\n\t"
      "global_load_dwordx4 v[132:135], v250, s[64:65] offset:-3072 sc1\n\t"
      "global_load_dwordx4 v[136:139], v250, s[64:65] offset:-2048 sc1\n\t"
      "global_load_dwordx4 v[140:143], v250, s[64:65] offset:-1024 sc1\n\t"
      "global_load_dwordx4 v[144:147], v250, s[64:65] sc1\n\t"
      "global_load_dwordx4 v[148:151], v250, s[64:65] offset:1024 sc1\n\t"
      "global_load_dwordx4 v[152:155], v250, s[64:65] offset:2048 sc1\n\t"
      "global_load_dwordx4 v[156:159], v250, s[64:65] offset:3072 sc1\n\t"
      "global_store_dwordx4 v241, v[208:211], s[20:21] offset:-2048 sc1\n\t"
      "global_store_dwordx4 v241, v[212:215], s[20:21] offset:2048 sc1\n\t"
      "s_setprio 0\n\t"
      "ds_read_b64_tr_b16 v[64:65], v224 offset:16384\n\t"
      "ds_read_b64_tr_b16 v[66:67], v224 offset:16896\n\t"
      "ds_read_b64_tr_b16 v[68:69], v225 offset:16384\n\t"
      "ds_read_b64_tr_b16 v[70:71], v225 offset:16896\n\t"
      "ds_read_b64_tr_b16 v[72:73], v226 offset:16384\n\t"
      "ds_read_b64_tr_b16 v[74:75], v226 offset:16896\n\t"
      "ds_read_b64_tr_b16 v[76:77], v227 offset:16384\n\t"
      "ds_read_b64_tr_b16 v[78:79], v227 offset:16896\n\t"
      "ds_read_b64_tr_b16 v[80:81], v224 offset:49152\n\t"
      "ds_read_b64_tr_b16 v[82:83], v224 offset:49664\n\t"
      "ds_read_b64_tr_b16 v[84:85], v225 offset:49152\n\t"
      "ds_read_b64_tr_b16 v[86:87], v225 offset:49664\n\t"
      "ds_read_b64_tr_b16 v[88:89], v226 offset:49152\n\t"
      "ds_read_b64_tr_b16 v[90:91], v226 offset:49664\n\t"
      "ds_read_b64_tr_b16 v[92:93], v227 offset:49152\n\t"
      "ds_read_b64_tr_b16 v[94:95], v227 offset:49664\n\t"
      "ds_read_b64_tr_b16 v[96:97], v224\n\t"
      "ds_read_b64_tr_b16 v[98:99], v224 offset:512\n\t"
      "ds_read_b64_tr_b16 v[100:101], v225\n\t"
      "ds_read_b64_tr_b16 v[102:103], v225 offset:512\n\t"
      "ds_read_b64_tr_b16 v[104:105], v226\n\t"
      "ds_read_b64_tr_b16 v[106:107], v226 offset:512\n\t"
      "ds_read_b64_tr_b16 v[108:109], v227\n\t"
      "ds_read_b64_tr_b16 v[110:111], v227 offset:512\n\t"
      "ds_read_b64_tr_b16 v[112:113], v224 offset:32768\n\t"
      "ds_read_b64_tr_b16 v[114:115], v224 offset:33280\n\t"
      "ds_read_b64_tr_b16 v[116:117], v225 offset:32768\n\t"
      "ds_read_b64_tr_b16 v[118:119], v225 offset:33280\n\t"
      "ds_read_b64_tr_b16 v[120:121], v226 offset:32768\n\t"
      "ds_read_b64_tr_b16 v[122:123], v226 offset:33280\n\t"
      "ds_read_b64_tr_b16 v[124:125], v227 offset:32768\n\t"
      "ds_read_b64_tr_b16 v[126:127], v227 offset:33280\n\t"
      "s_waitcnt vmcnt(2) lgkmcnt(0)\n\t"
      "GST_%=:\n\t"
      "s_add_u32 s66, s64, 65536\n\t"
      "s_addc_u32 s67, s65, 0\n\t"
      "s_add_u32 s68, s66, 65536\n\t"
      "s_addc_u32 s69, s67, 0\n\t"
      "s_lshl_b32 s34, s32, 18\n\t"
      "s_lshl_b32 s35, s24, 13\n\t"
      "s_add_u32 s34, s34, s35\n\t"
      "s_add_u32 s70, s48, s34\n\t"
      "s_addc_u32 s71, s49, 0\n\t"
      "s_add_u32 s72, s70, 65536\n\t"
      "s_addc_u32 s73, s71, 0\n\t"
      "s_add_u32 s74, s72, 65536\n\t"
      "s_addc_u32 s75, s73, 0\n\t"
      "s_add_u32 s76, s74, 65536\n\t"
      "s_addc_u32 s77, s75, 0\n\t"
      "s_mul_i32 s34, s30, 28\n\t"
      "s_add_u32 s34, s34, s29\n\t"
      "s_lshl_b32 s34, s34, 11\n\t"
      "s_add_u32 s34, s34, 10223616\n\t"
      "s_add_u32 s62, s20, s34\n\t"
      "s_addc_u32 s63, s21, 0\n\t"
      "v_lshlrev_b32_e32 v248, 16, v249\n\t"
      "v_and_b32_e32 v249, 0xffff0000, v249\n\t"
      "v_mov_b32_e32 v252, 0xbdd2d3e8\n\t"
      "v_xor_b32_e32 v253, 128, v245\n\t"
      "v_max3_u32 v251, v128, v129, v130\n\t"
      "v_max3_u32 v251, v251, v131, v132\n\t"
      "v_max3_u32 v251, v251, v133, v134\n\t"
      "v_max3_u32 v251, v251, v135, v136\n\t"
      "v_max3_u32 v251, v251, v137, v138\n\t"
      "v_max3_u32 v251, v251, v139, v140\n\t"
      "v_max3_u32 v251, v251, v141, v142\n\t"
      "v_max3_u32 v251, v251, v143, v144\n\t"
      "v_max3_u32 v251, v251, v145, v146\n\t"
      "v_max3_u32 v251, v251, v147, v148\n\t"
      "v_max3_u32 v251, v251, v149, v150\n\t"
      "v_max3_u32 v251, v251, v151, v152\n\t"
      "v_max3_u32 v251, v251, v153, v154\n\t"
      "v_max3_u32 v251, v251, v155, v156\n\t"
      "v_max3_u32 v251, v251, v157, v158\n\t"
      "v_max_u32_e32 v251, v251, v159\n\t"
      "v_cmp_eq_u32_e32 vcc, -1, v251\n\t"
      "s_cbranch_vccnz LA30R_%=\n\t"
      "LA30B_%=:\n\t"
      "v_mfma_f32_16x16x32_bf16 v[208:211], v[0:3], v[128:131], 0\n\t"
      "global_load_dwordx4 v[160:163], v250, s[66:67] offset:-4096 sc1\n\t"
      "v_mfma_f32_16x16x32_bf16 v[208:211], v[16:19], v[132:135], v[208:211]\n\t"
      "v_mfma_f32_16x16x32_bf16 v[208:211], v[32:35], v[136:139], v[208:211]\n\t"
      "global_load_dwordx4 v[164:167], v250, s[66:67] offset:-3072 sc1\n\t"
      "v_mfma_f32_16x16x32_bf16 v[208:211], v[48:51], v[140:143], v[208:211]\n\t"
      "v_mfma_f32_16x16x32_bf16 v[208:211], v[64:67], v[144:147], v[208:211]\n\t"
      "global_load_dwordx4 v[168:171], v250, s[66:67] offset:-2048 sc1\n\t"
      "v_mfma_f32_16x16x32_bf16 v[208:211], v[80:83], v[148:151], v[208:211]\n\t"
      "v_mfma_f32_16x16x32_bf16 v[208:211], v[96:99], v[152:155], v[208:211]\n\t"
      "s_lshl_b32 s34, s2, 7\n\t"
      "s_add_u32 s34, s34, 65536\n\t"
      "v_mov_b32_e32 v230, s34\n\t"
      "s_cmp_lg_u32 s24, 3\n\t"
      "s_cbranch_scc1 SK17_%=\n\t"
      "global_load_dwordx2 v[228:229], v230, s[60:61] sc1\n\t"
      "SK17_%=:\n\t"
      "global_load_dwordx4 v[172:175], v250, s[66:67] offset:-1024 sc1\n\t"
      "v_mfma_f32_16x16x32_bf16 v[208:211], v[112:115], v[156:159], v[208:211]\n\t"
      "v_mfma_f32_16x16x32_bf16 v[212:215], v[4:7], v[128:131], 0\n\t"
      "global_load_dwordx4 v[176:179], v250, s[66:67] sc1\n\t"
      "v_mfma_f32_16x16x32_bf16 v[212:215], v[20:23], v[132:135], v[212:215]\n\t"
      "v_mfma_f32_16x16x32_bf16 v[212:215], v[36:39], v[136:139], v[212:215]\n\t"
      "global_load_dwordx4 v[180:183], v250, s[66:67] offset:1024 sc1\n\t"
      "v_mfma_f32_16x16x32_bf16 v[212:215], v[52:55], v[140:143], v[212:215]\n\t"
      "v_mfma_f32_16x16x32_bf16 v[212:215], v[68:71], v[144:147], v[212:215]\n\t"
      "global_load_dwordx4 v[184:187], v250, s[66:67] offset:2048 sc1\n\t"
      "v_mfma_f32_16x16x32_bf16 v[212:215], v[84:87], v[148:151], v[212:215]\n\t"
      "v_mfma_f32_16x16x32_bf16 v[212:215], v[100:103], v[152:155], v[212:215]\n\t"
      "ds_write_b128 v245, v[208:211]\n\t"
      "global_load_dwordx4 v[188:191], v250, s[66:67] offset:3072 sc1\n\t"
      "v_mfma_f32_16x16x32_bf16 v[212:215], v[116:119], v[156:159], v[212:215]\n\t"
      "v_mfma_f32_16x16x32_bf16 v[216:219], v[8:11], v[128:131], 0\n\t"
      "global_load_dwordx4 v[192:195], v250, s[68:69] offset:-4096 sc1\n\t"
      "v_mfma_f32_16x16x32_bf16 v[216:219], v[24:27], v[132:135], v[216:219]\n\t"
      "v_mfma_f32_16x16x32_bf16 v[216:219], v[40:43], v[136:139], v[216:219]\n\t"
      "global_load_dwordx4 v[196:199], v250, s[68:69] offset:-3072 sc1\n\t"
      "v_mfma_f32_16x16x32_bf16 v[216:219], v[56:59], v[140:143], v[216:219]\n\t"
      "v_mfma_f32_16x16x32_bf16 v[216:219], v[72:75], v[144:147], v[216:219]\n\t"
      "global_load_dwordx4 v[200:203], v250, s[68:69] offset:-2048 sc1\n\t"
      "v_mfma_f32_16x16x32_bf16 v[216:219], v[88:91], v[148:151], v[216:219]\n\t"
      "v_mfma_f32_16x16x32_bf16 v[216:219], v[104:107], v[152:155], v[216:219]\n\t"
      "ds_write_b128 v253, v[212:215] offset:1024\n\t"
      "global_load_dwordx4 v[204:207], v250, s[68:69] offset:-1024 sc1\n\t"
      "v_mfma_f32_16x16x32_bf16 v[216:219], v[120:123], v[156:159], v[216:219]\n\t"
      "v_mfma_f32_16x16x32_bf16 v[220:223], v[12:15], v[128:131], 0\n\t"
      "global_load_dwordx4 v[128:131], v250, s[68:69] sc1\n\t"
      "v_mfma_f32_16x16x32_bf16 v[220:223], v[28:31], v[132:135], v[220:223]\n\t"
      "v_mfma_f32_16x16x32_bf16 v[220:223], v[44:47], v[136:139], v[220:223]\n\t"
      "v_mfma_f32_16x16x32_bf16 v[220:223], v[60:63], v[140:143], v[220:223]\n\t"
      "v_mfma_f32_16x16x32_bf16 v[220:223], v[76:79], v[144:147], v[220:223]\n\t"
      "global_load_dwordx4 v[132:135], v250, s[68:69] offset:1024 sc1\n\t"
      "v_mfma_f32_16x16x32_bf16 v[220:223], v[92:95], v[148:151], v[220:223]\n\t"
      "v_mfma_f32_16x16x32_bf16 v[220:223], v[108:111], v[152:155], v[220:223]\n\t"
      "ds_write_b128 v245, v[216:219] offset:2048\n\t"
      "v_mfma_f32_16x16x32_bf16 v[220:223], v[124:127], v[156:159], v[220:223]\n\t"
      "s_waitcnt vmcnt(10)\n\t"
      "v_mfma_f32_16x16x32_bf16 v[224:227], v[0:3], v[160:163], 0\n\t"
      "global_load_dwordx4 v[136:139], v250, s[68:69] offset:2048 sc1\n\t"
      "v_mfma_f32_16x16x32_bf16 v[224:227], v[16:19], v[164:167], v[224:227]\n\t"
      "s_nop 3\n\t"
      "ds_write_b128 v253, v[220:223] offset:3072\n\t"
      "v_mfma_f32_16x16x32_bf16 v[224:227], v[32:35], v[168:171], v[224:227]\n\t"
      "v_mfma_f32_16x16x32_bf16 v[224:227], v[48:51], v[172:175], v[224:227]\n\t"
      "s_waitcnt vmcnt(7)\n\t"
      "v_mfma_f32_16x16x32_bf16 v[224:227], v[64:67], v[176:179], v[224:227]\n\t"
      "global_load_dwordx4 v[140:143], v250, s[68:69] offset:3072 sc1\n\t"
      "v_mfma_f32_16x16x32_bf16 v[224:227], v[80:83], v[180:183], v[224:227]\n\t"
      "s_cmp_lg_u32 s24, 3\n\t"
      "s_cbranch_scc1 LF6B_%=\n\t"
      "v_cmp_ne_u64_e32 vcc, s[22:23], v[228:229]\n\t"
      "s_cbranch_vccnz LF6R_%=\n\t"
      "LF6B_%=:\n\t"
      "s_waitcnt lgkmcnt(0)\n\t"
      "s_barrier\n\t"
      "s_cmp_lg_u32 s24, 3\n\t"
      "s_cbranch_scc1 HF3_%=\n\t"
      "s_lshl3_add_u32 s34, s25, s26\n\t"
      "s_lshl_b32 s34, s34, 7\n\t"
      "s_add_u32 s34, s34, 98304\n\t"
      "v_mov_b32_e32 v238, s34\n\t"
      "v_mov_b32_e32 v236, s22\n\t"
      "v_mov_b32_e32 v237, s23\n\t"
      "s_mov_b64 exec, 1\n\t"
      "global_store_dwordx2 v238, v[236:237], s[60:61] sc1\n\t"
      "s_mov_b64 exec, -1\n\t"
      "HF3_%=:\n\t"
      "ds_read_b64 v[228:229], v246\n\t"
      "ds_read_b64 v[230:231], v246 offset:4096\n\t"
      "v_mfma_f32_16x16x32_bf16 v[224:227], v[96:99], v[184:187], v[224:227]\n\t"
      "ds_read_b64 v[232:233], v246 offset:8192\n\t"
      "ds_read_b64 v[234:235], v246 offset:12288\n\t"
      "v_mfma_f32_16x16x32_bf16 v[224:227], v[112:115], v[188:191], v[224:227]\n\t"
      "ds_read_b64 v[236:237], v246 offset:16384\n\t"
      "ds_read_b64 v[238:239], v246 offset:20480\n\t"
      "v_mfma_f32_16x16x32_bf16 v[208:211], v[4:7], v[160:163], 0\n\t"
      "ds_read_b64 v[240:241], v246 offset:24576\n\t"
      "ds_read_b64 v[242:243], v246 offset:28672\n\t"
      "global_load_dwordx4 v[144:147], v250, s[70:71] offset:-4096 sc1\n\t"
      "v_mfma_f32_16x16x32_bf16 v[208:211], v[20:23], v[164:167], v[208:211]\n\t"
      "v_mfma_f32_16x16x32_bf16 v[208:211], v[36:39], v[168:171], v[208:211]\n\t"
      "s_waitcnt lgkmcnt(4)\n\t"
      "v_add_f32_e32 v228, v228, v230\n\t"
      "v_add_f32_e32 v232, v232, v234\n\t"
      "v_cmp_u_f32_e32 vcc, v224, v224\n\t"
      "s_cbranch_vccnz LA31R_%=\n\t"
      "LA31B_%=:\n\t"
      "v_mfma_f32_16x16x32_bf16 v[208:211], v[52:55], v[172:175], v[208:211]\n\t"
      "v_add_f32_e32 v229, v229, v231\n\t"
      "v_add_f32_e32 v233, v233, v235\n\t"
      "v_mfma_f32_16x16x32_bf16 v[208:211], v[68:71], v[176:179], v[208:211]\n\t"
      "s_waitcnt lgkmcnt(0)\n\t"
      "v_add_f32_e32 v236, v236, v238\n\t"
      "v_add_f32_e32 v240, v240, v242\n\t"
      "global_load_dwordx4 v[148:151], v250, s[70:71] offset:-3072 sc1\n\t"
      "v_mfma_f32_16x16x32_bf16 v[208:211], v[84:87], v[180:183], v[208:211]\n\t"
      "v_add_f32_e32 v237, v237, v239\n\t"
      "v_add_f32_e32 v241, v241, v243\n\t"
      "v_mfma_f32_16x16x32_bf16 v[208:211], v[100:103], v[184:187], v[208:211]\n\t"
      "v_add_f32_e32 v228, v228, v232\n\t"
      "v_add_f32_e32 v236, v236, v240\n\t"
      "v_add_f32_e32 v229, v229, v233\n\t"
      "ds_write_b128 v245, v[224:227] offset:32768\n\t"
      "v_mfma_f32_16x16x32_bf16 v[208:211], v[116:119], v[188:191], v[208:211]\n\t"
      "v_add_f32_e32 v237, v237, v241\n\t"
      "v_add_f32_e32 v228, v228, v248\n\t"
      "v_mfma_f32_16x16x32_bf16 v[212:215], v[8:11], v[160:163], 0\n\t"
      "v_add_f32_e32 v229, v229, v249\n\t"
      "v_add_f32_e32 v228, v228, v236\n\t"
      "v_add_f32_e32 v229, v229, v237\n\t"
      "global_load_dwordx4 v[152:155], v250, s[70:71] offset:-2048 sc1\n\t"
      "v_mfma_f32_16x16x32_bf16 v[212:215], v[24:27], v[164:167], v[212:215]\n\t"
      "v_mul_f32_e32 v230, v228, v228\n\t"
      "v_mul_f32_e32 v231, v229, v229\n\t"
      "v_mfma_f32_16x16x32_bf16 v[212:215], v[40:43], v[168:171], v[212:215]\n\t"
      "v_fmaak_f32 v230, v252, v230, 0xc0135761\n\t"
      "v_fmaak_f32 v231, v252, v231, 0xc0135761\n\t"
      "v_mul_f32_e32 v230, v228, v230\n\t"
      "v_mfma_f32_16x16x32_bf16 v[212:215], v[56:59], v[172:175], v[212:215]\n\t"
      "v_mul_f32_e32 v231, v229, v231\n\t"
      "v_exp_f32_e32 v230, v230\n\t"
      "v_mfma_f32_16x16x32_bf16 v[212:215], v[72:75], v[176:179], v[212:215]\n\t"
      "v_exp_f32_e32 v231, v231\n\t"
      "v_add_f32_e32 v230, 1.0, v230\n\t"
      "v_add_f32_e32 v231, 1.0, v231\n\t"
      "global_load_dwordx4 v[156:159], v250, s[70:71] offset:-1024 sc1\n\t"
      "v_mfma_f32_16x16x32_bf16 v[212:215], v[88:91], v[180:183], v[212:215]\n\t"
      "v_rcp_f32_e32 v230, v230\n\t"
      "v_rcp_f32_e32 v231, v231\n\t"
      "v_mfma_f32_16x16x32_bf16 v[212:215], v[104:107], v[184:187], v[212:215]\n\t"
      "v_mul_f32_e32 v228, v228, v230\n\t"
      "v_mul_f32_e32 v229, v229, v231\n\t"
      "v_cvt_pk_bf16_f32 v232, v228, v229\n\t"
      "ds_write_b128 v253, v[208:211] offset:33792\n\t"
      "v_mfma_f32_16x16x32_bf16 v[212:215], v[120:123], v[188:191], v[212:215]\n\t"
      "v_min_u32_e32 v254, -2, v232\n\t"
      "global_store_dword v247, v254, s[62:63] sc1\n\t"
      "s_add_u32 s62, s62, 57344\n\t"
      "s_addc_u32 s63, s63, 0\n\t"
      "v_mfma_f32_16x16x32_bf16 v[216:219], v[12:15], v[160:163], 0\n\t"
      "global_load_dwordx4 v[160:163], v250, s[70:71] sc1\n\t"
      "v_mfma_f32_16x16x32_bf16 v[216:219], v[28:31], v[164:167], v[216:219]\n\t"
      "v_mfma_f32_16x16x32_bf16 v[216:219], v[44:47], v[168:171], v[216:219]\n\t"
      "v_mfma_f32_16x16x32_bf16 v[216:219], v[60:63], v[172:175], v[216:219]\n\t"
      "v_mfma_f32_16x16x32_bf16 v[216:219], v[76:79], v[176:179], v[216:219]\n\t"
      "global_load_dwordx4 v[164:167], v250, s[70:71] offset:1024 sc1\n\t"
      "v_mfma_f32_16x16x32_bf16 v[216:219], v[92:95], v[180:183], v[216:219]\n\t"
      "v_mfma_f32_16x16x32_bf16 v[216:219], v[108:111], v[184:187], v[216:219]\n\t"
      "ds_write_b128 v245, v[212:215] offset:34816\n\t"
      "v_mfma_f32_16x16x32_bf16 v[216:219], v[124:127], v[188:191], v[216:219]\n\t"
      "s_waitcnt vmcnt(11)\n\t"
      "v_mfma_f32_16x16x32_bf16 v[220:223], v[0:3], v[192:195], 0\n\t"
      "global_load_dwordx4 v[168:171], v250, s[70:71] offset:2048 sc1\n\t"
      "v_mfma_f32_16x16x32_bf16 v[220:223], v[16:19], v[196:199], v[220:223]\n\t"
      "s_nop 3\n\t"
      "ds_write_b128 v253, v[216:219] offset:35840\n\t"
      "v_mfma_f32_16x16x32_bf16 v[220:223], v[32:35], v[200:203], v[220:223]\n\t"
      "v_mfma_f32_16x16x32_bf16 v[220:223], v[48:51], v[204:207], v[220:223]\n\t"
      "s_waitcnt vmcnt(8)\n\t"
      "v_mfma_f32_16x16x32_bf16 v[220:223], v[64:67], v[128:131], v[220:223]\n\t"
      "global_load_dwordx4 v[172:175], v250, s[70:71] offset:3072 sc1\n\t"
      "v_mfma_f32_16x16x32_bf16 v[220:223], v[80:83], v[132:135], v[220:223]\n\t"
      "s_waitcnt lgkmcnt(0)\n\t"
      "s_barrier\n\t"
      "ds_read_b64 v[228:229], v246 offset:32768\n\t"
      "ds_read_b64 v[230:231], v246 offset:36864\n\t"
      "v_mfma_f32_16x16x32_bf16 v[220:223], v[96:99], v[136:139], v[220:223]\n\t"
      "ds_read_b64 v[232:233], v246 offset:40960\n\t"
      "ds_read_b64 v[234:235], v246 offset:45056\n\t"
      "v_mfma_f32_16x16x32_bf16 v[220:223], v[112:115], v[140:143], v[220:223]\n\t"
      "ds_read_b64 v[236:237], v246 offset:49152\n\t"
      "ds_read_b64 v[238:239], v246 offset:53248\n\t"
      "v_mfma_f32_16x16x32_bf16 v[224:227], v[4:7], v[192:195], 0\n\t"
      "ds_read_b64 v[240:241], v246 offset:57344\n\t"
      "ds_read_b64 v[242:243], v246 offset:61440\n\t"
      "global_load_dwordx4 v[176:179], v250, s[72:73] offset:-4096 sc1\n\t"
      "v_mfma_f32_16x16x32_bf16 v[224:227], v[20:23], v[196:199], v[224:227]\n\t"
      "v_mfma_f32_16x16x32_bf16 v[224:227], v[36:39], v[200:203], v[224:227]\n\t"
      "s_waitcnt lgkmcnt(4)\n\t"
      "v_add_f32_e32 v228, v228, v230\n\t"
      "v_add_f32_e32 v232, v232, v234\n\t"
      "v_cmp_u_f32_e32 vcc, v220, v220\n\t"
      "s_cbranch_vccnz LA32R_%=\n\t"
      "LA32B_%=:\n\t"
      "v_mfma_f32_16x16x32_bf16 v[224:227], v[52:55], v[204:207], v[224:227]\n\t"
      "v_add_f32_e32 v229, v229, v231\n\t"
      "v_add_f32_e32 v233, v233, v235\n\t"
      "v_mfma_f32_16x16x32_bf16 v[224:227], v[68:71], v[128:131], v[224:227]\n\t"
      "s_waitcnt lgkmcnt(0)\n\t"
      "v_add_f32_e32 v236, v236, v238\n\t"
      "v_add_f32_e32 v240, v240, v242\n\t"
      "global_load_dwordx4 v[180:183], v250, s[72:73] offset:-3072 sc1\n\t"
      "v_mfma_f32_16x16x32_bf16 v[224:227], v[84:87], v[132:135], v[224:227]\n\t"
      "v_add_f32_e32 v237, v237, v239\n\t"
      "v_add_f32_e32 v241, v241, v243\n\t"
      "v_mfma_f32_16x16x32_bf16 v[224:227], v[100:103], v[136:139], v[224:227]\n\t"
      "v_add_f32_e32 v228, v228, v232\n\t"
      "v_add_f32_e32 v236, v236, v240\n\t"
      "v_add_f32_e32 v229, v229, v233\n\t"
      "ds_write_b128 v245, v[220:223]\n\t"
      "v_mfma_f32_16x16x32_bf16 v[224:227], v[116:119], v[140:143], v[224:227]\n\t"
      "v_add_f32_e32 v237, v237, v241\n\t"
      "v_add_f32_e32 v228, v228, v248\n\t"
      "v_mfma_f32_16x16x32_bf16 v[208:211], v[8:11], v[192:195], 0\n\t"
      "v_add_f32_e32 v229, v229, v249\n\t"
      "v_add_f32_e32 v228, v228, v236\n\t"
      "v_add_f32_e32 v229, v229, v237\n\t"
      "global_load_dwordx4 v[184:187], v250, s[72:73] offset:-2048 sc1\n\t"
      "v_mfma_f32_16x16x32_bf16 v[208:211], v[24:27], v[196:199], v[208:211]\n\t"
      "v_mul_f32_e32 v230, v228, v228\n\t"
      "v_mul_f32_e32 v231, v229, v229\n\t"
      "v_mfma_f32_16x16x32_bf16 v[208:211], v[40:43], v[200:203], v[208:211]\n\t"
      "v_fmaak_f32 v230, v252, v230, 0xc0135761\n\t"
      "v_fmaak_f32 v231, v252, v231, 0xc0135761\n\t"
      "v_mul_f32_e32 v230, v228, v230\n\t"
      "v_mfma_f32_16x16x32_bf16 v[208:211], v[56:59], v[204:207], v[208:211]\n\t"
      "v_mul_f32_e32 v231, v229, v231\n\t"
      "v_exp_f32_e32 v230, v230\n\t"
      "v_mfma_f32_16x16x32_bf16 v[208:211], v[72:75], v[128:131], v[208:211]\n\t"
      "v_exp_f32_e32 v231, v231\n\t"
      "v_add_f32_e32 v230, 1.0, v230\n\t"
      "v_add_f32_e32 v231, 1.0, v231\n\t"
      "global_load_dwordx4 v[188:191], v250, s[72:73] offset:-1024 sc1\n\t"
      "v_mfma_f32_16x16x32_bf16 v[208:211], v[88:91], v[132:135], v[208:211]\n\t"
      "v_rcp_f32_e32 v230, v230\n\t"
      "v_rcp_f32_e32 v231, v231\n\t"
      "v_mfma_f32_16x16x32_bf16 v[208:211], v[104:107], v[136:139], v[208:211]\n\t"
      "v_mul_f32_e32 v228, v228, v230\n\t"
      "v_mul_f32_e32 v229, v229, v231\n\t"
      "v_cvt_pk_bf16_f32 v232, v228, v229\n\t"
      "ds_write_b128 v253, v[224:227] offset:1024\n\t"
      "v_mfma_f32_16x16x32_bf16 v[208:211], v[120:123], v[140:143], v[208:211]\n\t"
      "v_min_u32_e32 v254, -2, v232\n\t"
      "global_store_dword v247, v254, s[62:63] sc1\n\t"
      "s_add_u32 s62, s62, 57344\n\t"
      "s_addc_u32 s63, s63, 0\n\t"
      "v_mfma_f32_16x16x32_bf16 v[212:215], v[12:15], v[192:195], 0\n\t"
      "global_load_dwordx4 v[192:195], v250, s[72:73] sc1\n\t"
      "v_mfma_f32_16x16x32_bf16 v[212:215], v[28:31], v[196:199], v[212:215]\n\t"
      "v_mfma_f32_16x16x32_bf16 v[212:215], v[44:47], v[200:203], v[212:215]\n\t"
      "v_mfma_f32_16x16x32_bf16 v[212:215], v[60:63], v[204:207], v[212:215]\n\t"
      "v_mfma_f32_16x16x32_bf16 v[212:215], v[76:79], v[128:131], v[212:215]\n\t"
      "global_load_dwordx4 v[196:199], v250, s[72:73] offset:1024 sc1\n\t"
      "v_mfma_f32_16x16x32_bf16 v[212:215], v[92:95], v[132:135], v[212:215]\n\t"
      "v_mfma_f32_16x16x32_bf16 v[212:215], v[108:111], v[136:139], v[212:215]\n\t"
      "ds_write_b128 v245, v[208:211] offset:2048\n\t"
      "v_mfma_f32_16x16x32_bf16 v[212:215], v[124:127], v[140:143], v[212:215]\n\t"
      "s_waitcnt vmcnt(12)\n\t"
      "v_mfma_f32_16x16x32_bf16 v[216:219], v[0:3], v[144:147], 0\n\t"
      "global_load_dwordx4 v[200:203], v250, s[72:73] offset:2048 sc1\n\t"
      "v_mfma_f32_16x16x32_bf16 v[216:219], v[16:19], v[148:151], v[216:219]\n\t"
      "s_nop 3\n\t"
      "ds_write_b128 v253, v[212:215] offset:3072\n\t"
      "v_mfma_f32_16x16x32_bf16 v[216:219], v[32:35], v[152:155], v[216:219]\n\t"
      "v_mfma_f32_16x16x32_bf16 v[216:219], v[48:51], v[156:159], v[216:219]\n\t"
      "s_waitcnt vmcnt(8)\n\t"
      "v_mfma_f32_16x16x32_bf16 v[216:219], v[64:67], v[160:163], v[216:219]\n\t"
      "global_load_dwordx4 v[204:207], v250, s[72:73] offset:3072 sc1\n\t"
      "v_mfma_f32_16x16x32_bf16 v[216:219], v[80:83], v[164:167], v[216:219]\n\t"
      "s_waitcnt lgkmcnt(0)\n\t"
      "s_barrier\n\t"
      "ds_read_b64 v[228:229], v246\n\t"
      "ds_read_b64 v[230:231], v246 offset:4096\n\t"
      "v_mfma_f32_16x16x32_bf16 v[216:219], v[96:99], v[168:171], v[216:219]\n\t"
      "ds_read_b64 v[232:233], v246 offset:8192\n\t"
      "ds_read_b64 v[234:235], v246 offset:12288\n\t"
      "v_mfma_f32_16x16x32_bf16 v[216:219], v[112:115], v[172:175], v[216:219]\n\t"
      "ds_read_b64 v[236:237], v246 offset:16384\n\t"
      "ds_read_b64 v[238:239], v246 offset:20480\n\t"
      "v_mfma_f32_16x16x32_bf16 v[220:223], v[4:7], v[144:147], 0\n\t"
      "ds_read_b64 v[240:241], v246 offset:24576\n\t"
      "ds_read_b64 v[242:243], v246 offset:28672\n\t"
      "global_load_dwordx4 v[128:131], v250, s[74:75] offset:-4096 sc1\n\t"
      "v_mfma_f32_16x16x32_bf16 v[220:223], v[20:23], v[148:151], v[220:223]\n\t"
      "v_mfma_f32_16x16x32_bf16 v[220:223], v[36:39], v[152:155], v[220:223]\n\t"
      "s_waitcnt lgkmcnt(4)\n\t"
      "v_add_f32_e32 v228, v228, v230\n\t"
      "v_add_f32_e32 v232, v232, v234\n\t"
      "v_cmp_u_f32_e32 vcc, v216, v216\n\t"
      "s_cbranch_vccnz LA33R_%=\n\t"
      "LA33B_%=:\n\t"
      "v_mfma_f32_16x16x32_bf16 v[220:223], v[52:55], v[156:159], v[220:223]\n\t"
      "v_add_f32_e32 v229, v229, v231\n\t"
      "v_add_f32_e32 v233, v233, v235\n\t"
      "v_mfma_f32_16x16x32_bf16 v[220:223], v[68:71], v[160:163], v[220:223]\n\t"
      "s_waitcnt lgkmcnt(0)\n\t"
      "v_add_f32_e32 v236, v236, v238\n\t"
      "v_add_f32_e32 v240, v240, v242\n\t"
      "global_load_dwordx4 v[132:135], v250, s[74:75] offset:-3072 sc1\n\t"
      "v_mfma_f32_16x16x32_bf16 v[220:223], v[84:87], v[164:167], v[220:223]\n\t"
      "v_add_f32_e32 v237, v237, v239\n\t"
      "v_add_f32_e32 v241, v241, v243\n\t"
      "v_mfma_f32_16x16x32_bf16 v[220:223], v[100:103], v[168:171], v[220:223]\n\t"
      "v_add_f32_e32 v228, v228, v232\n\t"
      "v_add_f32_e32 v236, v236, v240\n\t"
      "v_add_f32_e32 v229, v229, v233\n\t"
      "ds_write_b128 v245, v[216:219] offset:32768\n\t"
      "v_mfma_f32_16x16x32_bf16 v[220:223], v[116:119], v[172:175], v[220:223]\n\t"
      "v_add_f32_e32 v237, v237, v241\n\t"
      "v_add_f32_e32 v228, v228, v248\n\t"
      "v_mfma_f32_16x16x32_bf16 v[224:227], v[8:11], v[144:147], 0\n\t"
      "v_add_f32_e32 v229, v229, v249\n\t"
      "v_add_f32_e32 v228, v228, v236\n\t"
      "v_add_f32_e32 v229, v229, v237\n\t"
      "global_load_dwordx4 v[136:139], v250, s[74:75] offset:-2048 sc1\n\t"
      "v_mfma_f32_16x16x32_bf16 v[224:227], v[24:27], v[148:151], v[224:227]\n\t"
      "v_mul_f32_e32 v230, v228, v228\n\t"
      "v_mul_f32_e32 v231, v229, v229\n\t"
      "v_mfma_f32_16x16x32_bf16 v[224:227], v[40:43], v[152:155], v[224:227]\n\t"
      "v_fmaak_f32 v230, v252, v230, 0xc0135761\n\t"
      "v_fmaak_f32 v231, v252, v231, 0xc0135761\n\t"
      "v_mul_f32_e32 v230, v228, v230\n\t"
      "v_mfma_f32_16x16x32_bf16 v[224:227], v[56:59], v[156:159], v[224:227]\n\t"
      "v_mul_f32_e32 v231, v229, v231\n\t"
      "v_exp_f32_e32 v230, v230\n\t"
      "v_mfma_f32_16x16x32_bf16 v[224:227], v[72:75], v[160:163], v[224:227]\n\t"
      "v_exp_f32_e32 v231, v231\n\t"
      "v_add_f32_e32 v230, 1.0, v230\n\t"
      "v_add_f32_e32 v231, 1.0, v231\n\t"
      "global_load_dwordx4 v[140:143], v250, s[74:75] offset:-1024 sc1\n\t"
      "v_mfma_f32_16x16x32_bf16 v[224:227], v[88:91], v[164:167], v[224:227]\n\t"
      "v_rcp_f32_e32 v230, v230\n\t"
      "v_rcp_f32_e32 v231, v231\n\t"
      "v_mfma_f32_16x16x32_bf16 v[224:227], v[104:107], v[168:171], v[224:227]\n\t"
      "v_mul_f32_e32 v228, v228, v230\n\t"
      "v_mul_f32_e32 v229, v229, v231\n\t"
      "v_cvt_pk_bf16_f32 v232, v228, v229\n\t"
      "ds_write_b128 v253, v[220:223] offset:33792\n\t"
      "v_mfma_f32_16x16x32_bf16 v[224:227], v[120:123], v[172:175], v[224:227]\n\t"
      "v_min_u32_e32 v254, -2, v232\n\t"
      "global_store_dword v247, v254, s[62:63] sc1\n\t"
      "s_add_u32 s62, s62, 57344\n\t"
      "s_addc_u32 s63, s63, 0\n\t"
      "v_mfma_f32_16x16x32_bf16 v[208:211], v[12:15], v[144:147], 0\n\t"
      "global_load_dwordx4 v[144:147], v250, s[74:75] sc1\n\t"
      "v_mfma_f32_16x16x32_bf16 v[208:211], v[28:31], v[148:151], v[208:211]\n\t"
      "v_mfma_f32_16x16x32_bf16 v[208:211], v[44:47], v[152:155], v[208:211]\n\t"
      "v_mfma_f32_16x16x32_bf16 v[208:211], v[60:63], v[156:159], v[208:211]\n\t"
      "v_mfma_f32_16x16x32_bf16 v[208:211], v[76:79], v[160:163], v[208:211]\n\t"
      "global_load_dwordx4 v[148:151], v250, s[74:75] offset:1024 sc1\n\t"
      "v_mfma_f32_16x16x32_bf16 v[208:211], v[92:95], v[164:167], v[208:211]\n\t"
      "v_mfma_f32_16x16x32_bf16 v[208:211], v[108:111], v[168:171], v[208:211]\n\t"
      "ds_write_b128 v245, v[224:227] offset:34816\n\t"
      "v_mfma_f32_16x16x32_bf16 v[208:211], v[124:127], v[172:175], v[208:211]\n\t"
      "s_waitcnt vmcnt(12)\n\t"
      "v_mfma_f32_16x16x32_bf16 v[212:215], v[0:3], v[176:179], 0\n\t"
      "global_load_dwordx4 v[152:155], v250, s[74:75] offset:2048 sc1\n\t"
      "v_mfma_f32_16x16x32_bf16 v[212:215], v[16:19], v[180:183], v[212:215]\n\t"
      "s_nop 3\n\t"
      "ds_write_b128 v253, v[208:211] offset:35840\n\t"
      "v_mfma_f32_16x16x32_bf16 v[212:215], v[32:35], v[184:187], v[212:215]\n\t"
      "v_mfma_f32_16x16x32_bf16 v[212:215], v[48:51], v[188:191], v[212:215]\n\t"
      "s_waitcnt vmcnt(8)\n\t"
      "v_mfma_f32_16x16x32_bf16 v[212:215], v[64:67], v[192:195], v[212:215]\n\t"
      "global_load_dwordx4 v[156:159], v250, s[74:75] offset:3072 sc1\n\t"
      "v_mfma_f32_16x16x32_bf16 v[212:215], v[80:83], v[196:199], v[212:215]\n\t"
      "s_waitcnt lgkmcnt(0)\n\t"
      "s_barrier\n\t"
      "ds_read_b64 v[228:229], v246 offset:32768\n\t"
      "ds_read_b64 v[230:231], v246 offset:36864\n\t"
      "v_mfma_f32_16x16x32_bf16 v[212:215], v[96:99], v[200:203], v[212:215]\n\t"
      "ds_read_b64 v[232:233], v246 offset:40960\n\t"
      "ds_read_b64 v[234:235], v246 offset:45056\n\t"
      "v_mfma_f32_16x16x32_bf16 v[212:215], v[112:115], v[204:207], v[212:215]\n\t"
      "ds_read_b64 v[236:237], v246 offset:49152\n\t"
      "ds_read_b64 v[238:239], v246 offset:53248\n\t"
      "v_mfma_f32_16x16x32_bf16 v[216:219], v[4:7], v[176:179], 0\n\t"
      "ds_read_b64 v[240:241], v246 offset:57344\n\t"
      "ds_read_b64 v[242:243], v246 offset:61440\n\t"
      "global_load_dwordx4 v[160:163], v250, s[76:77] offset:-4096 sc1\n\t"
      "v_mfma_f32_16x16x32_bf16 v[216:219], v[20:23], v[180:183], v[216:219]\n\t"
      "v_mfma_f32_16x16x32_bf16 v[216:219], v[36:39], v[184:187], v[216:219]\n\t"
      "s_waitcnt lgkmcnt(4)\n\t"
      "v_add_f32_e32 v228, v228, v230\n\t"
      "v_add_f32_e32 v232, v232, v234\n\t"
      "v_cmp_u_f32_e32 vcc, v212, v212\n\t"
      "s_cbranch_vccnz LA34R_%=\n\t"
      "LA34B_%=:\n\t"
      "v_mfma_f32_16x16x32_bf16 v[216:219], v[52:55], v[188:191], v[216:219]\n\t"
      "v_add_f32_e32 v229, v229, v231\n\t"
      "v_add_f32_e32 v233, v233, v235\n\t"
      "v_mfma_f32_16x16x32_bf16 v[216:219], v[68:71], v[192:195], v[216:219]\n\t"
      "s_waitcnt lgkmcnt(0)\n\t"
      "v_add_f32_e32 v236, v236, v238\n\t"
      "v_add_f32_e32 v240, v240, v242\n\t"
      "global_load_dwordx4 v[164:167], v250, s[76:77] offset:-3072 sc1\n\t"
      "v_mfma_f32_16x16x32_bf16 v[216:219], v[84:87], v[196:199], v[216:219]\n\t"
      "v_add_f32_e32 v237, v237, v239\n\t"
      "v_add_f32_e32 v241, v241, v243\n\t"
      "v_mfma_f32_16x16x32_bf16 v[216:219], v[100:103], v[200:203], v[216:219]\n\t"
      "v_add_f32_e32 v228, v228, v232\n\t"
      "v_add_f32_e32 v236, v236, v240\n\t"
      "v_add_f32_e32 v229, v229, v233\n\t"
      "ds_write_b128 v245, v[212:215]\n\t"
      "v_mfma_f32_16x16x32_bf16 v[216:219], v[116:119], v[204:207], v[216:219]\n\t"
      "v_add_f32_e32 v237, v237, v241\n\t"
      "v_add_f32_e32 v228, v228, v248\n\t"
      "v_mfma_f32_16x16x32_bf16 v[220:223], v[8:11], v[176:179], 0\n\t"
      "v_add_f32_e32 v229, v229, v249\n\t"
      "v_add_f32_e32 v228, v228, v236\n\t"
      "v_add_f32_e32 v229, v229, v237\n\t"
      "global_load_dwordx4 v[168:171], v250, s[76:77] offset:-2048 sc1\n\t"
      "v_mfma_f32_16x16x32_bf16 v[220:223], v[24:27], v[180:183], v[220:223]\n\t"
      "v_mul_f32_e32 v230, v228, v228\n\t"
      "v_mul_f32_e32 v231, v229, v229\n\t"
      "v_mfma_f32_16x16x32_bf16 v[220:223], v[40:43], v[184:187], v[220:223]\n\t"
      "v_fmaak_f32 v230, v252, v230, 0xc0135761\n\t"
      "v_fmaak_f32 v231, v252, v231, 0xc0135761\n\t"
      "v_mul_f32_e32 v230, v228, v230\n\t"
      "v_mfma_f32_16x16x32_bf16 v[220:223], v[56:59], v[188:191], v[220:223]\n\t"
      "v_mul_f32_e32 v231, v229, v231\n\t"
      "v_exp_f32_e32 v230, v230\n\t"
      "v_mfma_f32_16x16x32_bf16 v[220:223], v[72:75], v[192:195], v[220:223]\n\t"
      "v_exp_f32_e32 v231, v231\n\t"
      "v_add_f32_e32 v230, 1.0, v230\n\t"
      "v_add_f32_e32 v231, 1.0, v231\n\t"
      "global_load_dwordx4 v[172:175], v250, s[76:77] offset:-1024 sc1\n\t"
      "v_mfma_f32_16x16x32_bf16 v[220:223], v[88:91], v[196:199], v[220:223]\n\t"
      "v_rcp_f32_e32 v230, v230\n\t"
      "v_rcp_f32_e32 v231, v231\n\t"
      "v_mfma_f32_16x16x32_bf16 v[220:223], v[104:107], v[200:203], v[220:223]\n\t"
      "v_mul_f32_e32 v228, v228, v230\n\t"
      "v_mul_f32_e32 v229, v229, v231\n\t"
      "v_cvt_pk_bf16_f32 v232, v228, v229\n\t"
      "ds_write_b128 v253, v[216:219] offset:1024\n\t"
      "v_mfma_f32_16x16x32_bf16 v[220:223], v[120:123], v[204:207], v[220:223]\n\t"
      "v_min_u32_e32 v254, -2, v232\n\t"
      "global_store_dword v247, v254, s[62:63] sc1\n\t"
      "s_add_u32 s62, s62, 57344\n\t"
      "s_addc_u32 s63, s63, 0\n\t"
      "v_mfma_f32_16x16x32_bf16 v[224:227], v[12:15], v[176:179], 0\n\t"
      "global_load_dwordx4 v[176:179], v250, s[76:77] sc1\n\t"
      "v_mfma_f32_16x16x32_bf16 v[224:227], v[28:31], v[180:183], v[224:227]\n\t"
      "v_mfma_f32_16x16x32_bf16 v[224:227], v[44:47], v[184:187], v[224:227]\n\t"
      "v_mfma_f32_16x16x32_bf16 v[224:227], v[60:63], v[188:191], v[224:227]\n\t"
      "v_mfma_f32_16x16x32_bf16 v[224:227], v[76:79], v[192:195], v[224:227]\n\t"
      "global_load_dwordx4 v[180:183], v250, s[76:77] offset:1024 sc1\n\t"
      "v_mfma_f32_16x16x32_bf16 v[224:227], v[92:95], v[196:199], v[224:227]\n\t"
      "v_mfma_f32_16x16x32_bf16 v[224:227], v[108:111], v[200:203], v[224:227]\n\t"
      "ds_write_b128 v245, v[220:223] offset:2048\n\t"
      "v_mfma_f32_16x16x32_bf16 v[224:227], v[124:127], v[204:207], v[224:227]\n\t"
      "s_waitcnt vmcnt(12)\n\t"
      "v_mfma_f32_16x16x32_bf16 v[208:211], v[0:3], v[128:131], 0\n\t"
      "global_load_dwordx4 v[184:187], v250, s[76:77] offset:2048 sc1\n\t"
      "v_mfma_f32_16x16x32_bf16 v[208:211], v[16:19], v[132:135], v[208:211]\n\t"
      "s_nop 3\n\t"
      "ds_write_b128 v253, v[224:227] offset:3072\n\t"
      "v_mfma_f32_16x16x32_bf16 v[208:211], v[32:35], v[136:139], v[208:211]\n\t"
      "v_mfma_f32_16x16x32_bf16 v[208:211], v[48:51], v[140:143], v[208:211]\n\t"
      "s_waitcnt vmcnt(8)\n\t"
      "v_mfma_f32_16x16x32_bf16 v[208:211], v[64:67], v[144:147], v[208:211]\n\t"
      "global_load_dwordx4 v[188:191], v250, s[76:77] offset:3072 sc1\n\t"
      "v_mfma_f32_16x16x32_bf16 v[208:211], v[80:83], v[148:151], v[208:211]\n\t"
      "s_waitcnt lgkmcnt(0)\n\t"
      "s_barrier\n\t"
      "ds_read_b64 v[228:229], v246\n\t"
      "ds_read_b64 v[230:231], v246 offset:4096\n\t"
      "v_mfma_f32_16x16x32_bf16 v[208:211], v[96:99], v[152:155], v[208:211]\n\t"
      "ds_read_b64 v[232:233], v246 offset:8192\n\t"
      "ds_read_b64 v[234:235], v246 offset:12288\n\t"
      "v_mfma_f32_16x16x32_bf16 v[208:211], v[112:115], v[156:159], v[208:211]\n\t"
      "ds_read_b64 v[236:237], v246 offset:16384\n\t"
      "ds_read_b64 v[238:239], v246 offset:20480\n\t"
      "v_mfma_f32_16x16x32_bf16 v[212:215], v[4:7], v[128:131], 0\n\t"
      "ds_read_b64 v[240:241], v246 offset:24576\n\t"
      "ds_read_b64 v[242:243], v246 offset:28672\n\t"
      "v_mfma_f32_16x16x32_bf16 v[212:215], v[20:23], v[132:135], v[212:215]\n\t"
      "v_mfma_f32_16x16x32_bf16 v[212:215], v[36:39], v[136:139], v[212:215]\n\t"
      "s_waitcnt lgkmcnt(4)\n\t"
      "v_add_f32_e32 v228, v228, v230\n\t"
      "v_add_f32_e32 v232, v232, v234\n\t"
      "v_cmp_u_f32_e32 vcc, v208, v208\n\t"
      "s_cbranch_vccnz LA35R_%=\n\t"
      "LA35B_%=:\n\t"
      "v_mfma_f32_16x16x32_bf16 v[212:215], v[52:55], v[140:143], v[212:215]\n\t"
      "v_add_f32_e32 v229, v229, v231\n\t"
      "v_add_f32_e32 v233, v233, v235\n\t"
      "v_mfma_f32_16x16x32_bf16 v[212:215], v[68:71], v[144:147], v[212:215]\n\t"
      "s_waitcnt lgkmcnt(0)\n\t"
      "v_add_f32_e32 v236, v236, v238\n\t"
      "v_add_f32_e32 v240, v240, v242\n\t"
      "v_mfma_f32_16x16x32_bf16 v[212:215], v[84:87], v[148:151], v[212:215]\n\t"
      "v_add_f32_e32 v237, v237, v239\n\t"
      "v_add_f32_e32 v241, v241, v243\n\t"
      "v_mfma_f32_16x16x32_bf16 v[212:215], v[100:103], v[152:155], v[212:215]\n\t"
      "v_add_f32_e32 v228, v228, v232\n\t"
      "v_add_f32_e32 v236, v236, v240\n\t"
      "v_add_f32_e32 v229, v229, v233\n\t"
      "ds_write_b128 v245, v[208:211] offset:32768\n\t"
      "v_mfma_f32_16x16x32_bf16 v[212:215], v[116:119], v[156:159], v[212:215]\n\t"
      "v_add_f32_e32 v237, v237, v241\n\t"
      "v_add_f32_e32 v228, v228, v248\n\t"
      "v_mfma_f32_16x16x32_bf16 v[216:219], v[8:11], v[128:131], 0\n\t"
      "v_add_f32_e32 v229, v229, v249\n\t"
      "v_add_f32_e32 v228, v228, v236\n\t"
      "v_add_f32_e32 v229, v229, v237\n\t"
      "v_mfma_f32_16x16x32_bf16 v[216:219], v[24:27], v[132:135], v[216:219]\n\t"
      "v_mul_f32_e32 v230, v228, v228\n\t"
      "v_mul_f32_e32 v231, v229, v229\n\t"
      "v_mfma_f32_16x16x32_bf16 v[216:219], v[40:43], v[136:139], v[216:219]\n\t"
      "v_fmaak_f32 v230, v252, v230, 0xc0135761\n\t"
      "v_fmaak_f32 v231, v252, v231, 0xc0135761\n\t"
      "v_mul_f32_e32 v230, v228, v230\n\t"
      "v_mfma_f32_16x16x32_bf16 v[216:219], v[56:59], v[140:143], v[216:219]\n\t"
      "v_mul_f32_e32 v231, v229, v231\n\t"
      "v_exp_f32_e32 v230, v230\n\t"
      "v_mfma_f32_16x16x32_bf16 v[216:219], v[72:75], v[144:147], v[216:219]\n\t"
      "v_exp_f32_e32 v231, v231\n\t"
      "v_add_f32_e32 v230, 1.0, v230\n\t"
      "v_add_f32_e32 v231, 1.0, v231\n\t"
      "v_mfma_f32_16x16x32_bf16 v[216:219], v[88:91], v[148:151], v[216:219]\n\t"
      "v_rcp_f32_e32 v230, v230\n\t"
      "v_rcp_f32_e32 v231, v231\n\t"
      "v_mfma_f32_16x16x32_bf16 v[216:219], v[104:107], v[152:155], v[216:219]\n\t"
      "v_mul_f32_e32 v228, v228, v230\n\t"
      "v_mul_f32_e32 v229, v229, v231\n\t"
      "v_cvt_pk_bf16_f32 v232, v228, v229\n\t"
      "ds_write_b128 v253, v[212:215] offset:33792\n\t"
      "v_mfma_f32_16x16x32_bf16 v[216:219], v[120:123], v[156:159], v[216:219]\n\t"
      "v_min_u32_e32 v254, -2, v232\n\t"
      "global_store_dword v247, v254, s[62:63] sc1\n\t"
      "s_add_u32 s62, s62, 57344\n\t"
      "s_addc_u32 s63, s63, 0\n\t"
      "v_mfma_f32_16x16x32_bf16 v[220:223], v[12:15], v[128:131], 0\n\t"
      "v_mfma_f32_16x16x32_bf16 v[220:223], v[28:31], v[132:135], v[220:223]\n\t"
      "v_mfma_f32_16x16x32_bf16 v[220:223], v[44:47], v[136:139], v[220:223]\n\t"
      "v_mfma_f32_16x16x32_bf16 v[220:223], v[60:63], v[140:143], v[220:223]\n\t"
      "v_mfma_f32_16x16x32_bf16 v[220:223], v[76:79], v[144:147], v[220:223]\n\t"
      "v_mfma_f32_16x16x32_bf16 v[220:223], v[92:95], v[148:151], v[220:223]\n\t"
      "v_mfma_f32_16x16x32_bf16 v[220:223], v[108:111], v[152:155], v[220:223]\n\t"
      "ds_write_b128 v245, v[216:219] offset:34816\n\t"
      "v_mfma_f32_16x16x32_bf16 v[220:223], v[124:127], v[156:159], v[220:223]\n\t"
      "s_waitcnt vmcnt(6)\n\t"
      "v_mfma_f32_16x16x32_bf16 v[224:227], v[0:3], v[160:163], 0\n\t"
      "v_mfma_f32_16x16x32_bf16 v[224:227], v[16:19], v[164:167], v[224:227]\n\t"
      "s_nop 4\n\t"
      "ds_write_b128 v253, v[220:223] offset:35840\n\t"
      "v_mfma_f32_16x16x32_bf16 v[224:227], v[32:35], v[168:171], v[224:227]\n\t"
      "v_mfma_f32_16x16x32_bf16 v[224:227], v[48:51], v[172:175], v[224:227]\n\t"
      "s_waitcnt vmcnt(1)\n\t"
      "v_mfma_f32_16x16x32_bf16 v[224:227], v[64:67], v[176:179], v[224:227]\n\t"
      "v_mfma_f32_16x16x32_bf16 v[224:227], v[80:83], v[180:183], v[224:227]\n\t"
      "s_waitcnt lgkmcnt(0)\n\t"
      "s_barrier\n\t"
      "ds_read_b64 v[228:229], v246 offset:32768\n\t"
      "ds_read_b64 v[230:231], v246 offset:36864\n\t"
      "v_mfma_f32_16x16x32_bf16 v[224:227], v[96:99], v[184:187], v[224:227]\n\t"
      "ds_read_b64 v[232:233], v246 offset:40960\n\t"
      "ds_read_b64 v[234:235], v246 offset:45056\n\t"
      "v_mfma_f32_16x16x32_bf16 v[224:227], v[112:115], v[188:191], v[224:227]\n\t"
      "ds_read_b64 v[236:237], v246 offset:49152\n\t"
      "ds_read_b64 v[238:239], v246 offset:53248\n\t"
      "v_mfma_f32_16x16x32_bf16 v[208:211], v[4:7], v[160:163], 0\n\t"
      "ds_read_b64 v[240:241], v246 offset:57344\n\t"
      "ds_read_b64 v[242:243], v246 offset:61440\n\t"
      "v_mfma_f32_16x16x32_bf16 v[208:211], v[20:23], v[164:167], v[208:211]\n\t"
      "v_mfma_f32_16x16x32_bf16 v[208:211], v[36:39], v[168:171], v[208:211]\n\t"
      "s_waitcnt lgkmcnt(4)\n\t"
      "v_add_f32_e32 v228, v228, v230\n\t"
      "v_add_f32_e32 v232, v232, v234\n\t"
      "v_cmp_u_f32_e32 vcc, v224, v224\n\t"
      "s_cbranch_vccnz LA36R_%=\n\t"
      "LA36B_%=:\n\t"
      "v_mfma_f32_16x16x32_bf16 v[208:211], v[52:55], v[172:175], v[208:211]\n\t"
      "v_add_f32_e32 v229, v229, v231\n\t"
      "v_add_f32_e32 v233, v233, v235\n\t"
      "v_mfma_f32_16x16x32_bf16 v[208:211], v[68:71], v[176:179], v[208:211]\n\t"
      "s_waitcnt lgkmcnt(0)\n\t"
      "v_add_f32_e32 v236, v236, v238\n\t"
      "v_add_f32_e32 v240, v240, v242\n\t"
      "v_mfma_f32_16x16x32_bf16 v[208:211], v[84:87], v[180:183], v[208:211]\n\t"
      "v_add_f32_e32 v237, v237, v239\n\t"
      "v_add_f32_e32 v241, v241, v243\n\t"
      "v_mfma_f32_16x16x32_bf16 v[208:211], v[100:103], v[184:187], v[208:211]\n\t"
      "v_add_f32_e32 v228, v228, v232\n\t"
      "v_add_f32_e32 v236, v236, v240\n\t"
      "v_add_f32_e32 v229, v229, v233\n\t"
      "ds_write_b128 v245, v[224:227]\n\t"
      "v_mfma_f32_16x16x32_bf16 v[208:211], v[116:119], v[188:191], v[208:211]\n\t"
      "v_add_f32_e32 v237, v237, v241\n\t"
      "v_add_f32_e32 v228, v228, v248\n\t"
      "v_mfma_f32_16x16x32_bf16 v[212:215], v[8:11], v[160:163], 0\n\t"
      "v_add_f32_e32 v229, v229, v249\n\t"
      "v_add_f32_e32 v228, v228, v236\n\t"
      "v_add_f32_e32 v229, v229, v237\n\t"
      "v_mfma_f32_16x16x32_bf16 v[212:215], v[24:27], v[164:167], v[212:215]\n\t"
      "v_mul_f32_e32 v230, v228, v228\n\t"
      "v_mul_f32_e32 v231, v229, v229\n\t"
      "v_mfma_f32_16x16x32_bf16 v[212:215], v[40:43], v[168:171], v[212:215]\n\t"
      "v_fmaak_f32 v230, v252, v230, 0xc0135761\n\t"
      "v_fmaak_f32 v231, v252, v231, 0xc0135761\n\t"
      "v_mul_f32_e32 v230, v228, v230\n\t"
      "v_mfma_f32_16x16x32_bf16 v[212:215], v[56:59], v[172:175], v[212:215]\n\t"
      "v_mul_f32_e32 v231, v229, v231\n\t"
      "v_exp_f32_e32 v230, v230\n\t"
      "v_mfma_f32_16x16x32_bf16 v[212:215], v[72:75], v[176:179], v[212:215]\n\t"
      "v_exp_f32_e32 v231, v231\n\t"
      "v_add_f32_e32 v230, 1.0, v230\n\t"
      "v_add_f32_e32 v231, 1.0, v231\n\t"
      "v_mfma_f32_16x16x32_bf16 v[212:215], v[88:91], v[180:183], v[212:215]\n\t"
      "v_rcp_f32_e32 v230, v230\n\t"
      "v_rcp_f32_e32 v231, v231\n\t"
      "v_mfma_f32_16x16x32_bf16 v[212:215], v[104:107], v[184:187], v[212:215]\n\t"
      "v_mul_f32_e32 v228, v228, v230\n\t"
      "v_mul_f32_e32 v229, v229, v231\n\t"
      "v_cvt_pk_bf16_f32 v232, v228, v229\n\t"
      "ds_write_b128 v253, v[208:211] offset:1024\n\t"
      "v_mfma_f32_16x16x32_bf16 v[212:215], v[120:123], v[188:191], v[212:215]\n\t"
      "v_min_u32_e32 v254, -2, v232\n\t"
      "global_store_dword v247, v254, s[62:63] sc1\n\t"
      "s_add_u32 s62, s62, 57344\n\t"
      "s_addc_u32 s63, s63, 0\n\t"
      "v_mfma_f32_16x16x32_bf16 v[216:219], v[12:15], v[160:163], 0\n\t"
      "v_mfma_f32_16x16x32_bf16 v[216:219], v[28:31], v[164:167], v[216:219]\n\t"
      "v_mfma_f32_16x16x32_bf16 v[216:219], v[44:47], v[168:171], v[216:219]\n\t"
      "v_mfma_f32_16x16x32_bf16 v[216:219], v[60:63], v[172:175], v[216:219]\n\t"
      "v_mfma_f32_16x16x32_bf16 v[216:219], v[76:79], v[176:179], v[216:219]\n\t"
      "v_mfma_f32_16x16x32_bf16 v[216:219], v[92:95], v[180:183], v[216:219]\n\t"
      "v_mfma_f32_16x16x32_bf16 v[216:219], v[108:111], v[184:187], v[216:219]\n\t"
      "ds_write_b128 v245, v[212:215] offset:2048\n\t"
      "v_mfma_f32_16x16x32_bf16 v[216:219], v[124:127], v[188:191], v[216:219]\n\t"
      "s_nop 7\n\t"
      "ds_write_b128 v253, v[216:219] offset:3072\n\t"
      "s_waitcnt lgkmcnt(0)\n\t"
      "s_barrier\n\t"
      "ds_read_b64 v[228:229], v246\n\t"
      "ds_read_b64 v[230:231], v246 offset:4096\n\t"
      "ds_read_b64 v[232:233], v246 offset:8192\n\t"
      "ds_read_b64 v[234:235], v246 offset:12288\n\t"
      "ds_read_b64 v[236:237], v246 offset:16384\n\t"
      "ds_read_b64 v[238:239], v246 offset:20480\n\t"
      "ds_read_b64 v[240:241], v246 offset:24576\n\t"
      "ds_read_b64 v[242:243], v246 offset:28672\n\t"
      "s_waitcnt lgkmcnt(4)\n\t"
      "v_add_f32_e32 v228, v228, v230\n\t"
      "v_add_f32_e32 v232, v232, v234\n\t"
      "v_add_f32_e32 v229, v229, v231\n\t"
      "v_add_f32_e32 v233, v233, v235\n\t"
      "s_waitcnt lgkmcnt(0)\n\t"
      "v_add_f32_e32 v236, v236, v238\n\t"
      "v_add_f32_e32 v240, v240, v242\n\t"
      "v_add_f32_e32 v237, v237, v239\n\t"
      "v_add_f32_e32 v241, v241, v243\n\t"
      "v_add_f32_e32 v228, v228, v232\n\t"
      "v_add_f32_e32 v236, v236, v240\n\t"
      "v_add_f32_e32 v229, v229, v233\n\t"
      "v_add_f32_e32 v237, v237, v241\n\t"
      "v_add_f32_e32 v228, v228, v248\n\t"
      "v_add_f32_e32 v229, v229, v249\n\t"
      "v_add_f32_e32 v228, v228, v236\n\t"
      "v_add_f32_e32 v229, v229, v237\n\t"
      "v_mul_f32_e32 v230, v228, v228\n\t"
      "v_mul_f32_e32 v231, v229, v229\n\t"
      "v_fmaak_f32 v230, v252, v230, 0xc0135761\n\t"
      "v_fmaak_f32 v231, v252, v231, 0xc0135761\n\t"
      "v_mul_f32_e32 v230, v228, v230\n\t"
      "v_mul_f32_e32 v231, v229, v231\n\t"
      "v_exp_f32_e32 v230, v230\n\t"
      "v_exp_f32_e32 v231, v231\n\t"
      "v_add_f32_e32 v230, 1.0, v230\n\t"
      "v_add_f32_e32 v231, 1.0, v231\n\t"
      "v_rcp_f32_e32 v230, v230\n\t"
      "v_rcp_f32_e32 v231, v231\n\t"
      "v_mul_f32_e32 v228, v228, v230\n\t"
      "v_mul_f32_e32 v229, v229, v231\n\t"
      "v_cvt_pk_bf16_f32 v232, v228, v229\n\t"
      "v_min_u32_e32 v254, -2, v232\n\t"
      "global_store_dword v247, v254, s[62:63] sc1\n\t"
      "s_add_u32 s62, s62, 57344\n\t"
      "s_addc_u32 s63, s63, 0\n\t"
      "s_or_b32 s34, s2, s24\n\t"
      "s_cbranch_scc1 EPB_%=\n\t"
      "v_mov_b32_e32 v228, s22\n\t"
      "v_mov_b32_e32 v229, s23\n\t"
      "v_mov_b32_e32 v230, 0\n\t"
      "s_mov_b64 exec, 1\n\t"
      "global_store_dwordx2 v230, v[228:229], s[60:61] sc1\n\t"
      "s_mov_b64 exec, -1\n\t"
      "EPB_%=:\n\t"
      "s_endpgm\n\t"
      "LPATH_%=:\n\t"
      "s_add_u32 s22, s22, 1\n\t"
      "s_addc_u32 s23, s23, 0\n\t"
      "s_add_u32 s72, s26, 1\n\t"
      "s_and_b32 s72, s72, 6\n\t"
      "s_cselect_b32 s72, 0, 2\n\t"
      "s_or_b32 s27, s27, s72\n\t"
      "s_mul_i32 s72, s81, 172032\n\t"
      "s_add_u32 s72, s72, s37\n\t"
      "s_add_u32 s40, s6, s72\n\t"
      "s_addc_u32 s41, s7, 0\n\t"
      "s_add_u32 s81, s81, 1\n\t"
      "s_cmp_eq_u32 s81, 7\n\t"
      "s_cselect_b32 s81, 0, s81\n\t"
      "s_add_u32 m0, s39, 81920\n\t"
      "s_nop 0\n\t"
      "global_load_lds_dwordx4 v148, s[40:41]\n\t"
      "global_load_lds_dwordx4 v149, s[40:41] offset:1024\n\t"
      "global_load_lds_dwordx4 v150, s[40:41] offset:2048\n\t"
      "global_load_lds_dwordx4 v151, s[40:41] offset:3072\n\t"
      "s_mul_i32 s72, s81, 172032\n\t"
      "s_add_u32 s72, s72, s37\n\t"
      "s_add_u32 s40, s6, s72\n\t"
      "s_addc_u32 s41, s7, 0\n\t"
      "s_add_u32 s81, s81, 1\n\t"
      "s_cmp_eq_u32 s81, 7\n\t"
      "s_cselect_b32 s81, 0, s81\n\t"
      "s_add_u32 m0, s39, 98304\n\t"
      "s_nop 0\n\t"
      "global_load_lds_dwordx4 v148, s[40:41]\n\t"
      "global_load_lds_dwordx4 v149, s[40:41] offset:1024\n\t"
      "s_waitcnt vmcnt(14)\n\t"
      "s_barrier\n\t"
      "v_add_u32_e32 v172, 0x10000, v224\n\t"
      "v_add_u32_e32 v173, 0x10000, v225\n\t"
      "v_add_u32_e32 v174, 0x10000, v226\n\t"
      "v_add_u32_e32 v175, 0x10000, v227\n\t"
      "s_lshl_b32 s34, s25, 8\n\t"
      "s_add_u32 s34, s34, 4096\n\t"
      "v_and_b32_e32 v136, 1, v192\n\t"
      "v_lshl_add_u32 v136, v136, 7, s34\n\t"
      "s_lshl_b32 s34, s25, 7\n\t"
      "s_add_u32 s34, s34, 16384\n\t"
      "v_mov_b32_e32 v137, s34\n\t"
      "s_lshl_b32 s34, s2, 7\n\t"
      "s_add_u32 s34, s34, 32768\n\t"
      "v_mov_b32_e32 v143, s34\n\t"
      "s_bitcmp0_b32 s27, 1\n\t"
      "s_cbranch_scc1 SK6_%=\n\t"
      "s_lshr_b32 s73, s26, 2\n\t"
      "s_lshl1_add_u32 s73, s25, s73\n\t"
      "s_lshl_b32 s72, s73, 7\n\t"
      "s_add_u32 s72, s72, 4096\n\t"
      "v_mov_b32_e32 v130, s72\n\t"
      "s_lshl2_add_u32 s75, s73, s24\n\t"
      "v_mov_b32_e32 v128, s22\n\t"
      "v_mov_b32_e32 v129, s23\n\t"
      "v_lshrrev_b32_e32 v140, 1, v244\n\t"
      "s_lshl_b32 s72, s75, 9\n\t"
      "s_add_u32 s72, s72, 12582912\n\t"
      "v_mov_b32_e32 v132, -1\n\t"
      "v_mov_b32_e32 v133, -1\n\t"
      "v_mov_b32_e32 v134, -1\n\t"
      "v_mov_b32_e32 v135, -1\n\t"
      "s_add_u32 s42, s20, s72\n\t"
      "s_addc_u32 s43, s21, 0\n\t"
      "global_store_dwordx2 v140, v[132:133], s[42:43] sc1\n\t"
      "s_lshl_b32 s72, s75, 13\n\t"
      "s_add_u32 s72, s72, 4096\n\t"
      "s_add_u32 s42, s20, s72\n\t"
      "s_addc_u32 s43, s21, 0\n\t"
      "global_store_dwordx4 v244, v[132:135], s[42:43] offset:-4096 sc1\n\t"
      "global_store_dwordx4 v244, v[132:135], s[42:43] offset:-3072 sc1\n\t"
      "global_store_dwordx4 v244, v[132:135], s[42:43] offset:-2048 sc1\n\t"
      "SK6_%=:\n\t"
      "s_waitcnt vmcnt(10)\n\t"
      "s_barrier\n\t"
      "global_load_lds_dwordx4 v150, s[40:41] offset:2048\n\t"
      "global_load_lds_dwordx4 v151, s[40:41] offset:3072\n\t"
      "s_bitcmp0_b32 s27, 1\n\t"
      "s_cbranch_scc1 SK7_%=\n\t"
      "global_store_dwordx4 v244, v[132:135], s[42:43] offset:-1024 sc1\n\t"
      "global_store_dwordx4 v244, v[132:135], s[42:43] sc1\n\t"
      "global_store_dwordx4 v244, v[132:135], s[42:43] offset:1024 sc1\n\t"
      "SK7_%=:\n\t"
      "s_waitcnt vmcnt(8)\n\t"
      "s_barrier\n\t"
      "s_mul_i32 s72, s81, 172032\n\t"
      "s_add_u32 s72, s72, s37\n\t"
      "s_add_u32 s40, s6, s72\n\t"
      "s_addc_u32 s41, s7, 0\n\t"
      "s_add_u32 s81, s81, 1\n\t"
      "s_cmp_eq_u32 s81, 7\n\t"
      "s_cselect_b32 s81, 0, s81\n\t"
      "s_add_u32 m0, s39, 114688\n\t"
      "s_nop 0\n\t"
      "global_load_lds_dwordx4 v148, s[40:41]\n\t"
      "global_load_lds_dwordx4 v149, s[40:41] offset:1024\n\t"
      "global_load_lds_dwordx4 v150, s[40:41] offset:2048\n\t"
      "global_load_lds_dwordx4 v151, s[40:41] offset:3072\n\t"
      "s_bitcmp0_b32 s27, 1\n\t"
      "s_cbranch_scc1 SK8_%=\n\t"
      "global_store_dwordx4 v244, v[132:135], s[42:43] offset:2048 sc1\n\t"
      "global_store_dwordx4 v244, v[132:135], s[42:43] offset:3072 sc1\n\t"
      "SK8_%=:\n\t"
      "s_waitcnt vmcnt(8)\n\t"
      "s_barrier\n\t"
      "s_mul_i32 s72, s81, 172032\n\t"
      "s_add_u32 s72, s72, s37\n\t"
      "s_add_u32 s40, s6, s72\n\t"
      "s_addc_u32 s41, s7, 0\n\t"
      "s_add_u32 s81, s81, 1\n\t"
      "s_cmp_eq_u32 s81, 7\n\t"
      "s_cselect_b32 s81, 0, s81\n\t"
      "s_add_u32 m0, s39, 131072\n\t"
      "s_nop 0\n\t"
      "global_load_lds_dwordx4 v148, s[40:41]\n\t"
      "global_load_lds_dwordx4 v149, s[40:41] offset:1024\n\t"
      "global_load_lds_dwordx4 v150, s[40:41] offset:2048\n\t"
      "global_load_lds_dwordx4 v151, s[40:41] offset:3072\n\t"
      "s_waitcnt vmcnt(8)\n\t"
      "s_bitcmp0_b32 s27, 1\n\t"
      "s_cbranch_scc1 SK9_%=\n\t"
      "s_waitcnt vmcnt(4)\n\t"
      "SK9_%=:\n\t"
      "s_barrier\n\t"
      "s_mul_i32 s72, s81, 172032\n\t"
      "s_add_u32 s72, s72, s37\n\t"
      "s_add_u32 s40, s6, s72\n\t"
      "s_addc_u32 s41, s7, 0\n\t"
      "s_add_u32 s81, s81, 1\n\t"
      "s_cmp_eq_u32 s81, 7\n\t"
      "s_cselect_b32 s81, 0, s81\n\t"
      "s_add_u32 m0, s39, 147456\n\t"
      "s_nop 0\n\t"
      "global_load_lds_dwordx4 v148, s[40:41]\n\t"
      "global_load_lds_dwordx4 v149, s[40:41] offset:1024\n\t"
      "global_load_lds_dwordx4 v150, s[40:41] offset:2048\n\t"
      "global_load_lds_dwordx4 v151, s[40:41] offset:3072\n\t"
      "s_bitcmp0_b32 s27, 1\n\t"
      "s_cbranch_scc1 SK10_%=\n\t"
      "s_cmp_eq_u32 s24, 0\n\t"
      "s_cselect_b64 exec, 1, 0\n\t"
      "global_store_dwordx2 v130, v[128:129], s[60:61] sc1\n\t"
      "s_mov_b64 exec, -1\n\t"
      "s_mov_b32 m0, 64512\n\t"
      "s_mul_i32 s72, s75, 13\n\t"
      "v_min_u32_e32 v141, 12, v192\n\t"
      "v_add_u32_e32 v141, s72, v141\n\t"
      "v_min_u32_e32 v141, 3135, v141\n\t"
      "v_lshlrev_b32_e32 v141, 7, v141\n\t"
      "global_load_lds_dword v141, s[8:9]\n\t"
      "SK10_%=:\n\t"
      "s_waitcnt vmcnt(8)\n\t"
      "s_barrier\n\t"
      "s_mul_i32 s72, s82, 172032\n\t"
      "s_add_u32 s72, s72, s37\n\t"
      "s_add_u32 s72, s72, 1792\n\t"
      "s_add_u32 s40, s6, s72\n\t"
      "s_addc_u32 s41, s7, 0\n\t"
      "s_add_u32 s82, s82, 1\n\t"
      "s_cmp_eq_u32 s82, 7\n\t"
      "s_cselect_b32 s82, 0, s82\n\t"
      "s_add_u32 m0, s39, 65536\n\t"
      "s_nop 0\n\t"
      "global_load_lds_dwordx4 v148, s[40:41]\n\t"
      "global_load_lds_dwordx4 v152, s[40:41] offset:1024\n\t"
      "s_mul_i32 s72, s82, 172032\n\t"
      "s_add_u32 s72, s72, s37\n\t"
      "s_add_u32 s72, s72, 1792\n\t"
      "s_add_u32 s40, s6, s72\n\t"
      "s_addc_u32 s41, s7, 0\n\t"
      "s_add_u32 s82, s82, 1\n\t"
      "s_cmp_eq_u32 s82, 7\n\t"
      "s_cselect_b32 s82, 0, s82\n\t"
      "s_add_u32 m0, s39, 67584\n\t"
      "s_nop 0\n\t"
      "global_load_lds_dwordx4 v148, s[40:41]\n\t"
      "global_load_lds_dwordx4 v152, s[40:41] offset:1024\n\t"
      "s_waitcnt vmcnt(8)\n\t"
      "s_barrier\n\t"
      "s_mul_i32 s72, s82, 172032\n\t"
      "s_add_u32 s72, s72, s37\n\t"
      "s_add_u32 s72, s72, 1792\n\t"
      "s_add_u32 s40, s6, s72\n\t"
      "s_addc_u32 s41, s7, 0\n\t"
      "s_add_u32 s82, s82, 1\n\t"
      "s_cmp_eq_u32 s82, 7\n\t"
      "s_cselect_b32 s82, 0, s82\n\t"
      "s_add_u32 m0, s39, 81920\n\t"
      "s_nop 0\n\t"
      "global_load_lds_dwordx4 v148, s[40:41]\n\t"
      "global_load_lds_dwordx4 v152, s[40:41] offset:1024\n\t"
      "s_mul_i32 s72, s82, 172032\n\t"
      "s_add_u32 s72, s72, s37\n\t"
      "s_add_u32 s72, s72, 1792\n\t"
      "s_add_u32 s40, s6, s72\n\t"
      "s_addc_u32 s41, s7, 0\n\t"
      "s_add_u32 s82, s82, 1\n\t"
      "s_cmp_eq_u32 s82, 7\n\t"
      "s_cselect_b32 s82, 0, s82\n\t"
      "s_add_u32 m0, s39, 83968\n\t"
      "s_nop 0\n\t"
      "global_load_lds_dwordx4 v148, s[40:41]\n\t"
      "global_load_lds_dwordx4 v152, s[40:41] offset:1024\n\t"
      "s_waitcnt vmcnt(8)\n\t"
      "s_barrier\n\t"
      "s_cmp_lg_u32 s24, 3\n\t"
      "s_cbranch_scc1 SK11_%=\n\t"
      "global_load_dwordx2 v[138:139], v136, s[60:61] sc1\n\t"
      "SK11_%=:\n\t"
      "s_mul_i32 s72, s82, 172032\n\t"
      "s_add_u32 s72, s72, s37\n\t"
      "s_add_u32 s72, s72, 1792\n\t"
      "s_add_u32 s40, s6, s72\n\t"
      "s_addc_u32 s41, s7, 0\n\t"
      "s_add_u32 s82, s82, 1\n\t"
      "s_cmp_eq_u32 s82, 7\n\t"
      "s_cselect_b32 s82, 0, s82\n\t"
      "s_lshr_b32 s72, s39, 1\n\t"
      "s_add_u32 m0, s72, 98304\n\t"
      "s_nop 0\n\t"
      "global_load_lds_dwordx4 v148, s[40:41]\n\t"
      "global_load_lds_dwordx4 v152, s[40:41] offset:1024\n\t"
      "global_load_dword v249, v248, s[12:13]\n\t"
      "s_lshl2_add_u32 s72, s31, s24\n\t"
      "s_and_b32 s73, s72, 0x70\n\t"
      "s_add_u32 s72, s72, s73\n\t"
      "s_mov_b32 m0, 64512\n\t"
      "s_mul_i32 s72, s72, 28672\n\t"
      "s_and_b32 s73, s2, 7\n\t"
      "s_mul_i32 s73, s73, 448\n\t"
      "s_and_b32 s73, s73, 0xffffff80\n\t"
      "s_add_u32 s72, s72, s73\n\t"
      "v_bfe_u32 v141, v192, 2, 3\n\t"
      "s_cmp_ge_u32 s31, 28\n\t"
      "s_cselect_b32 s73, 32, 0\n\t"
      "v_and_b32_e32 v142, s73, v192\n\t"
      "v_lshl_or_b32 v141, v142, 2, v141\n\t"
      "v_mul_u32_u24_e32 v141, 3584, v141\n\t"
      "v_lshl_add_u32 v141, v196, 7, v141\n\t"
      "v_add_u32_e32 v141, s72, v141\n\t"
      "global_load_lds_dword v141, s[10:11]\n\t"
      "s_waitcnt vmcnt(8)\n\t"
      "s_barrier\n\t"
      "s_mul_i32 s72, s24, 14336\n\t"
      "s_add_u32 s40, s56, s72\n\t"
      "s_addc_u32 s41, s57, 0\n\t"
      "s_lshr_b32 s72, s39, 1\n\t"
      "s_add_u32 m0, s72, 106496\n\t"
      "s_nop 0\n\t"
      "global_load_lds_dwordx4 v154, s[40:41]\n\t"
      "global_load_lds_dwordx4 v155, s[40:41] offset:1024\n\t"
      "s_waitcnt vmcnt(6)\n\t"
      "s_barrier\n\t"
      "s_mul_i32 s72, s24, 28672\n\t"
      "s_add_u32 s72, s72, 57344\n\t"
      "s_add_u32 s40, s56, s72\n\t"
      "s_addc_u32 s41, s57, 0\n\t"
      "s_lshr_b32 s72, s39, 0\n\t"
      "s_add_u32 m0, s72, 114688\n\t"
      "s_nop 0\n\t"
      "global_load_lds_dwordx4 v154, s[40:41]\n\t"
      "global_load_lds_dwordx4 v155, s[40:41] offset:1024\n\t"
      "global_load_lds_dwordx4 v156, s[40:41] offset:2048\n\t"
      "global_load_lds_dwordx4 v157, s[40:41] offset:3072\n\t"
      "s_waitcnt vmcnt(8)\n\t"
      "s_cmp_lg_u32 s24, 3\n\t"
      "s_cbranch_scc1 LF1B_%=\n\t"
      "v_cmp_ne_u64_e32 vcc, s[22:23], v[138:139]\n\t"
      "s_cbranch_vccnz LF1R_%=\n\t"
      "LF1B_%=:\n\t"
      "s_barrier\n\t"
      "s_mul_i32 s72, s24, 28672\n\t"
      "s_add_u32 s72, s72, 172032\n\t"
      "s_add_u32 s40, s56, s72\n\t"
      "s_addc_u32 s41, s57, 0\n\t"
      "s_lshr_b32 s72, s39, 0\n\t"
      "s_add_u32 m0, s72, 131072\n\t"
      "s_nop 0\n\t"
      "global_load_lds_dwordx4 v154, s[40:41]\n\t"
      "global_load_lds_dwordx4 v155, s[40:41] offset:1024\n\t"
      "global_load_lds_dwordx4 v156, s[40:41] offset:2048\n\t"
      "global_load_lds_dwordx4 v157, s[40:41] offset:3072\n\t"
      "s_mul_i32 s72, s24, 28672\n\t"
      "s_add_u32 s72, s72, 286720\n\t"
      "s_add_u32 s40, s56, s72\n\t"
      "s_addc_u32 s41, s57, 0\n\t"
      "s_lshr_b32 s72, s39, 0\n\t"
      "s_add_u32 m0, s72, 147456\n\t"
      "s_nop 0\n\t"
      "global_load_lds_dwordx4 v154, s[40:41]\n\t"
      "global_load_lds_dwordx4 v155, s[40:41] offset:1024\n\t"
      "global_load_lds_dwordx4 v156, s[40:41] offset:2048\n\t"
      "global_load_lds_dwordx4 v157, s[40:41] offset:3072\n\t"
      "s_barrier\n\t"
      "s_cmp_eq_u32 s26, 7\n\t"
      "s_cbranch_scc1 SPARE_%=\n\t"
      "s_lshl_b32 s72, s24, 10\n\t"
      "s_add_u32 s72, s72, 98304\n\t"
      "v_xor_b32_e32 v203, v197, v198\n\t"
      "v_lshl_add_u32 v203, v203, 4, s72\n\t"
      "v_lshl_add_u32 v203, v197, 7, v203\n\t"
      "ds_write_b128 v203, v[204:207]\n\t"
      "s_waitcnt lgkmcnt(0)\n\t"
      "s_bitcmp0_b32 s27, 0\n\t"
      "s_cbranch_scc1 L4_%=\n\t"
      "s_add_u32 s72, s76, 0\n\t"
      "s_add_u32 s40, s52, s72\n\t"
      "s_addc_u32 s41, s53, 0\n\t"
      "s_add_u32 m0, s39, 65536\n\t"
      "s_nop 0\n\t"
      "global_load_lds_dwordx4 v144, s[40:41]\n\t"
      "global_load_lds_dwordx4 v145, s[40:41] offset:1024\n\t"
      "global_load_lds_dwordx4 v146, s[40:41] offset:2048\n\t"
      "global_load_lds_dwordx4 v147, s[40:41] offset:3072\n\t"
      "s_add_u32 s72, s76, 229376\n\t"
      "s_add_u32 s40, s52, s72\n\t"
      "s_addc_u32 s41, s53, 0\n\t"
      "s_add_u32 m0, s39, 81920\n\t"
      "s_nop 0\n\t"
      "global_load_lds_dwordx4 v144, s[40:41]\n\t"
      "global_load_lds_dwordx4 v145, s[40:41] offset:1024\n\t"
      "global_load_lds_dwordx4 v146, s[40:41] offset:2048\n\t"
      "global_load_lds_dwordx4 v147, s[40:41] offset:3072\n\t"
      "s_waitcnt vmcnt(8)\n\t"
      "s_barrier\n\t"
      "s_barrier\n\t"
      "s_add_u32 s72, s76, 3670016\n\t"
      "s_add_u32 s40, s52, s72\n\t"
      "s_addc_u32 s41, s53, 0\n\t"
      "s_add_u32 m0, s39, 16384\n\t"
      "s_nop 0\n\t"
      "global_load_lds_dwordx4 v144, s[40:41]\n\t"
      "global_load_lds_dwordx4 v145, s[40:41] offset:1024\n\t"
      "global_load_lds_dwordx4 v146, s[40:41] offset:2048\n\t"
      "global_load_lds_dwordx4 v147, s[40:41] offset:3072\n\t"
      "s_add_u32 s72, s76, 3899392\n\t"
      "s_add_u32 s40, s52, s72\n\t"
      "s_addc_u32 s41, s53, 0\n\t"
      "s_add_u32 m0, s39, 49152\n\t"
      "s_nop 0\n\t"
      "global_load_lds_dwordx4 v144, s[40:41]\n\t"
      "global_load_lds_dwordx4 v145, s[40:41] offset:1024\n\t"
      "global_load_lds_dwordx4 v146, s[40:41] offset:2048\n\t"
      "global_load_lds_dwordx4 v147, s[40:41] offset:3072\n\t"
      "s_waitcnt vmcnt(12)\n\t"
      "ds_read_b64_tr_b16 v[0:1], v172\n\t"
      "ds_read_b64_tr_b16 v[2:3], v172 offset:512\n\t"
      "ds_read_b64_tr_b16 v[4:5], v173\n\t"
      "ds_read_b64_tr_b16 v[6:7], v173 offset:512\n\t"
      "ds_read_b64_tr_b16 v[8:9], v174\n\t"
      "ds_read_b64_tr_b16 v[10:11], v174 offset:512\n\t"
      "ds_read_b64_tr_b16 v[12:13], v175\n\t"
      "ds_read_b64_tr_b16 v[14:15], v175 offset:512\n\t"
      "s_waitcnt lgkmcnt(0)\n\t"
      "s_add_u32 s72, s77, 0\n\t"
      "s_add_u32 s40, s52, s72\n\t"
      "s_addc_u32 s41, s53, 0\n\t"
      "s_add_u32 m0, s39, 65536\n\t"
      "s_nop 0\n\t"
      "global_load_lds_dwordx4 v144, s[40:41]\n\t"
      "global_load_lds_dwordx4 v145, s[40:41] offset:1024\n\t"
      "global_load_lds_dwordx4 v146, s[40:41] offset:2048\n\t"
      "global_load_lds_dwordx4 v147, s[40:41] offset:3072\n\t"
      "s_waitcnt vmcnt(12)\n\t"
      "ds_read_b64_tr_b16 v[16:17], v172 offset:16384\n\t"
      "ds_read_b64_tr_b16 v[18:19], v172 offset:16896\n\t"
      "ds_read_b64_tr_b16 v[20:21], v173 offset:16384\n\t"
      "ds_read_b64_tr_b16 v[22:23], v173 offset:16896\n\t"
      "ds_read_b64_tr_b16 v[24:25], v174 offset:16384\n\t"
      "ds_read_b64_tr_b16 v[26:27], v174 offset:16896\n\t"
      "ds_read_b64_tr_b16 v[28:29], v175 offset:16384\n\t"
      "ds_read_b64_tr_b16 v[30:31], v175 offset:16896\n\t"
      "s_waitcnt lgkmcnt(0)\n\t"
      "s_add_u32 s72, s77, 229376\n\t"
      "s_add_u32 s40, s52, s72\n\t"
      "s_addc_u32 s41, s53, 0\n\t"
      "s_add_u32 m0, s39, 81920\n\t"
      "s_nop 0\n\t"
      "global_load_lds_dwordx4 v144, s[40:41]\n\t"
      "global_load_lds_dwordx4 v145, s[40:41] offset:1024\n\t"
      "global_load_lds_dwordx4 v146, s[40:41] offset:2048\n\t"
      "global_load_lds_dwordx4 v147, s[40:41] offset:3072\n\t"
      "s_add_u32 s72, s77, 3670016\n\t"
      "s_add_u32 s40, s52, s72\n\t"
      "s_addc_u32 s41, s53, 0\n\t"
      "s_add_u32 m0, s39, 0\n\t"
      "s_nop 0\n\t"
      "global_load_lds_dwordx4 v144, s[40:41]\n\t"
      "global_load_lds_dwordx4 v145, s[40:41] offset:1024\n\t"
      "global_load_lds_dwordx4 v146, s[40:41] offset:2048\n\t"
      "global_load_lds_dwordx4 v147, s[40:41] offset:3072\n\t"
      "s_add_u32 s72, s77, 3899392\n\t"
      "s_add_u32 s40, s52, s72\n\t"
      "s_addc_u32 s41, s53, 0\n\t"
      "s_add_u32 m0, s39, 32768\n\t"
      "s_nop 0\n\t"
      "global_load_lds_dwordx4 v144, s[40:41]\n\t"
      "global_load_lds_dwordx4 v145, s[40:41] offset:1024\n\t"
      "global_load_lds_dwordx4 v146, s[40:41] offset:2048\n\t"
      "global_load_lds_dwordx4 v147, s[40:41] offset:3072\n\t"
      "s_waitcnt vmcnt(12)\n\t"
      "ds_read_b64_tr_b16 v[32:33], v172\n\t"
      "ds_read_b64_tr_b16 v[34:35], v172 offset:512\n\t"
      "ds_read_b64_tr_b16 v[36:37], v173\n\t"
      "ds_read_b64_tr_b16 v[38:39], v173 offset:512\n\t"
      "ds_read_b64_tr_b16 v[40:41], v174\n\t"
      "ds_read_b64_tr_b16 v[42:43], v174 offset:512\n\t"
      "ds_read_b64_tr_b16 v[44:45], v175\n\t"
      "ds_read_b64_tr_b16 v[46:47], v175 offset:512\n\t"
      "s_waitcnt lgkmcnt(0)\n\t"
      "s_add_u32 s72, s78, 0\n\t"
      "s_add_u32 s40, s52, s72\n\t"
      "s_addc_u32 s41, s53, 0\n\t"
      "s_add_u32 m0, s39, 65536\n\t"
      "s_nop 0\n\t"
      "global_load_lds_dwordx4 v144, s[40:41]\n\t"
      "global_load_lds_dwordx4 v145, s[40:41] offset:1024\n\t"
      "global_load_lds_dwordx4 v146, s[40:41] offset:2048\n\t"
      "global_load_lds_dwordx4 v147, s[40:41] offset:3072\n\t"
      "s_waitcnt vmcnt(12)\n\t"
      "ds_read_b64_tr_b16 v[48:49], v172 offset:16384\n\t"
      "ds_read_b64_tr_b16 v[50:51], v172 offset:16896\n\t"
      "ds_read_b64_tr_b16 v[52:53], v173 offset:16384\n\t"
      "ds_read_b64_tr_b16 v[54:55], v173 offset:16896\n\t"
      "ds_read_b64_tr_b16 v[56:57], v174 offset:16384\n\t"
      "ds_read_b64_tr_b16 v[58:59], v174 offset:16896\n\t"
      "ds_read_b64_tr_b16 v[60:61], v175 offset:16384\n\t"
      "ds_read_b64_tr_b16 v[62:63], v175 offset:16896\n\t"
      "s_waitcnt lgkmcnt(0)\n\t"
      "s_add_u32 s72, s78, 229376\n\t"
      "s_add_u32 s40, s52, s72\n\t"
      "s_addc_u32 s41, s53, 0\n\t"
      "s_add_u32 m0, s39, 81920\n\t"
      "s_nop 0\n\t"
      "global_load_lds_dwordx4 v144, s[40:41]\n\t"
      "global_load_lds_dwordx4 v145, s[40:41] offset:1024\n\t"
      "global_load_lds_dwordx4 v146, s[40:41] offset:2048\n\t"
      "global_load_lds_dwordx4 v147, s[40:41] offset:3072\n\t"
      "s_waitcnt vmcnt(8)\n\t"
      "s_barrier\n\t"
      "s_cmp_lg_u32 s24, 3\n\t"
      "s_cbranch_scc1 SK13_%=\n\t"
      "global_load_dwordx2 v[138:139], v137, s[60:61] sc1\n\t"
      "SK13_%=:\n\t"
      "s_add_u32 s72, s79, 0\n\t"
      "s_add_u32 s40, s52, s72\n\t"
      "s_addc_u32 s41, s53, 0\n\t"
      "s_add_u32 m0, s39, 98304\n\t"
      "s_nop 0\n\t"
      "global_load_lds_dwordx4 v144, s[40:41]\n\t"
      "global_load_lds_dwordx4 v145, s[40:41] offset:1024\n\t"
      "global_load_lds_dwordx4 v146, s[40:41] offset:2048\n\t"
      "global_load_lds_dwordx4 v147, s[40:41] offset:3072\n\t"
      "s_add_u32 s72, s79, 229376\n\t"
      "s_add_u32 s40, s52, s72\n\t"
      "s_addc_u32 s41, s53, 0\n\t"
      "s_add_u32 m0, s39, 114688\n\t"
      "s_nop 0\n\t"
      "global_load_lds_dwordx4 v144, s[40:41]\n\t"
      "global_load_lds_dwordx4 v145, s[40:41] offset:1024\n\t"
      "global_load_lds_dwordx4 v146, s[40:41] offset:2048\n\t"
      "global_load_lds_dwordx4 v147, s[40:41] offset:3072\n\t"
      "s_cmp_lg_u32 s24, 3\n\t"
      "s_cbranch_scc1 LF2B_%=\n\t"
      "s_waitcnt vmcnt(8)\n\t"
      "v_cmp_ne_u64_e32 vcc, s[22:23], v[138:139]\n\t"
      "s_cbranch_vccnz LF2R_%=\n\t"
      "LF2B_%=:\n\t"
      "s_barrier\n\t"
      "s_add_u32 s72, s78, 3670016\n\t"
      "s_add_u32 s40, s52, s72\n\t"
      "s_addc_u32 s41, s53, 0\n\t"
      "s_add_u32 m0, s39, 16384\n\t"
      "s_nop 0\n\t"
      "global_load_lds_dwordx4 v144, s[40:41]\n\t"
      "global_load_lds_dwordx4 v145, s[40:41] offset:1024\n\t"
      "global_load_lds_dwordx4 v146, s[40:41] offset:2048\n\t"
      "global_load_lds_dwordx4 v147, s[40:41] offset:3072\n\t"
      "s_add_u32 s72, s78, 3899392\n\t"
      "s_add_u32 s40, s52, s72\n\t"
      "s_addc_u32 s41, s53, 0\n\t"
      "s_add_u32 m0, s39, 49152\n\t"
      "s_nop 0\n\t"
      "global_load_lds_dwordx4 v144, s[40:41]\n\t"
      "global_load_lds_dwordx4 v145, s[40:41] offset:1024\n\t"
      "global_load_lds_dwordx4 v146, s[40:41] offset:2048\n\t"
      "global_load_lds_dwordx4 v147, s[40:41] offset:3072\n\t"
      "s_add_u32 s72, s79, 3670016\n\t"
      "s_add_u32 s40, s52, s72\n\t"
      "s_addc_u32 s41, s53, 0\n\t"
      "s_add_u32 m0, s39, 0\n\t"
      "s_nop 0\n\t"
      "global_load_lds_dwordx4 v144, s[40:41]\n\t"
      "global_load_lds_dwordx4 v145, s[40:41] offset:1024\n\t"
      "global_load_lds_dwordx4 v146, s[40:41] offset:2048\n\t"
      "global_load_lds_dwordx4 v147, s[40:41] offset:3072\n\t"
      "s_add_u32 s72, s79, 3899392\n\t"
      "s_add_u32 s40, s52, s72\n\t"
      "s_addc_u32 s41, s53, 0\n\t"
      "s_add_u32 m0, s39, 32768\n\t"
      "s_nop 0\n\t"
      "global_load_lds_dwordx4 v144, s[40:41]\n\t"
      "global_load_lds_dwordx4 v145, s[40:41] offset:1024\n\t"
      "global_load_lds_dwordx4 v146, s[40:41] offset:2048\n\t"
      "global_load_lds_dwordx4 v147, s[40:41] offset:3072\n\t"
      "s_cmp_lg_u32 s24, 3\n\t"
      "s_cbranch_scc1 SK14_%=\n\t"
      "global_load_dwordx2 v[138:139], v143, s[60:61] sc1\n\t"
      "SK14_%=:\n\t"
      "s_waitcnt vmcnt(28)\n\t"
      "ds_read_b64_tr_b16 v[64:65], v172\n\t"
      "ds_read_b64_tr_b16 v[66:67], v172 offset:512\n\t"
      "ds_read_b64_tr_b16 v[68:69], v173\n\t"
      "ds_read_b64_tr_b16 v[70:71], v173 offset:512\n\t"
      "ds_read_b64_tr_b16 v[72:73], v174\n\t"
      "ds_read_b64_tr_b16 v[74:75], v174 offset:512\n\t"
      "ds_read_b64_tr_b16 v[76:77], v175\n\t"
      "ds_read_b64_tr_b16 v[78:79], v175 offset:512\n\t"
      "s_waitcnt vmcnt(24)\n\t"
      "ds_read_b64_tr_b16 v[80:81], v172 offset:16384\n\t"
      "ds_read_b64_tr_b16 v[82:83], v172 offset:16896\n\t"
      "ds_read_b64_tr_b16 v[84:85], v173 offset:16384\n\t"
      "ds_read_b64_tr_b16 v[86:87], v173 offset:16896\n\t"
      "ds_read_b64_tr_b16 v[88:89], v174 offset:16384\n\t"
      "ds_read_b64_tr_b16 v[90:91], v174 offset:16896\n\t"
      "ds_read_b64_tr_b16 v[92:93], v175 offset:16384\n\t"
      "ds_read_b64_tr_b16 v[94:95], v175 offset:16896\n\t"
      "s_waitcnt vmcnt(20)\n\t"
      "ds_read_b64_tr_b16 v[96:97], v172 offset:32768\n\t"
      "ds_read_b64_tr_b16 v[98:99], v172 offset:33280\n\t"
      "ds_read_b64_tr_b16 v[100:101], v173 offset:32768\n\t"
      "ds_read_b64_tr_b16 v[102:103], v173 offset:33280\n\t"
      "ds_read_b64_tr_b16 v[104:105], v174 offset:32768\n\t"
      "ds_read_b64_tr_b16 v[106:107], v174 offset:33280\n\t"
      "ds_read_b64_tr_b16 v[108:109], v175 offset:32768\n\t"
      "ds_read_b64_tr_b16 v[110:111], v175 offset:33280\n\t"
      "s_waitcnt vmcnt(16)\n\t"
      "ds_read_b64_tr_b16 v[112:113], v172 offset:49152\n\t"
      "ds_read_b64_tr_b16 v[114:115], v172 offset:49664\n\t"
      "ds_read_b64_tr_b16 v[116:117], v173 offset:49152\n\t"
      "ds_read_b64_tr_b16 v[118:119], v173 offset:49664\n\t"
      "ds_read_b64_tr_b16 v[120:121], v174 offset:49152\n\t"
      "ds_read_b64_tr_b16 v[122:123], v174 offset:49664\n\t"
      "ds_read_b64_tr_b16 v[124:125], v175 offset:49152\n\t"
      "ds_read_b64_tr_b16 v[126:127], v175 offset:49664\n\t"
      "s_waitcnt vmcnt(0)\n\t"
      "s_cmp_lg_u32 s24, 3\n\t"
      "s_cbranch_scc1 LF3B_%=\n\t"
      "s_waitcnt vmcnt(0)\n\t"
      "v_cmp_ne_u64_e32 vcc, s[22:23], v[138:139]\n\t"
      "s_cbranch_vccnz LF3R_%=\n\t"
      "LF3B_%=:\n\t"
      "s_waitcnt lgkmcnt(0)\n\t"
      "s_barrier\n\t"
      "s_mul_i32 s34, s32, 196608\n\t"
      "s_mul_i32 s35, s24, 8192\n\t"
      "s_add_u32 s34, s34, s35\n\t"
      "s_add_u32 s34, s34, 1048576\n\t"
      "s_add_u32 s64, s48, s34\n\t"
      "s_addc_u32 s65, s49, 0\n\t"
      "v_add_u32_e32 v250, 4096, v244\n\t"
      "global_load_dwordx4 v[128:131], v250, s[64:65] offset:-4096 sc1\n\t"
      "global_load_dwordx4 v[132:135], v250, s[64:65] offset:-3072 sc1\n\t"
      "global_load_dwordx4 v[136:139], v250, s[64:65] offset:-2048 sc1\n\t"
      "global_load_dwordx4 v[140:143], v250, s[64:65] offset:-1024 sc1\n\t"
      "global_load_dwordx4 v[144:147], v250, s[64:65] sc1\n\t"
      "global_load_dwordx4 v[148:151], v250, s[64:65] offset:1024 sc1\n\t"
      "global_load_dwordx4 v[152:155], v250, s[64:65] offset:2048 sc1\n\t"
      "global_load_dwordx4 v[156:159], v250, s[64:65] offset:3072 sc1\n\t"
      "s_waitcnt vmcnt(0) lgkmcnt(0)\n\t"
      "s_branch GST_%=\n\t"
      "G4C_%=:\n\t"
      "global_store_dwordx4 v241, v[208:211], s[20:21] offset:-2048 sc1\n\t"
      "global_store_dwordx4 v241, v[212:215], s[20:21] offset:2048 sc1\n\t"
      "s_setprio 0\n\t"
      "G4_%=:\n\t"
      "s_mul_i32 s34, s32, 401408\n\t"
      "s_mul_i32 s35, s24, 7168\n\t"
      "s_add_u32 s34, s34, s35\n\t"
      "s_add_u32 s34, s34, 1835008\n\t"
      "s_add_u32 s64, s48, s34\n\t"
      "s_addc_u32 s65, s49, 0\n\t"
      "s_and_b32 s34, s30, 3\n\t"
      "s_lshl_b32 s34, s34, 11\n\t"
      "s_add_u32 s40, s34, 0\n\t"
      "s_sub_u32 s35, s40, 7168\n\t"
      "s_min_u32 s40, s40, s35\n\t"
      "s_add_u32 s41, s34, 1024\n\t"
      "s_sub_u32 s35, s41, 7168\n\t"
      "s_min_u32 s41, s41, s35\n\t"
      "s_add_u32 s42, s34, 2048\n\t"
      "s_sub_u32 s35, s42, 7168\n\t"
      "s_min_u32 s42, s42, s35\n\t"
      "s_add_u32 s43, s34, 3072\n\t"
      "s_sub_u32 s35, s43, 7168\n\t"
      "s_min_u32 s43, s43, s35\n\t"
      "s_add_u32 s44, s34, 4096\n\t"
      "s_sub_u32 s35, s44, 7168\n\t"
      "s_min_u32 s44, s44, s35\n\t"
      "s_add_u32 s45, s34, 5120\n\t"
      "s_sub_u32 s35, s45, 7168\n\t"
      "s_min_u32 s45, s45, s35\n\t"
      "s_add_u32 s46, s34, 6144\n\t"
      "s_sub_u32 s35, s46, 7168\n\t"
      "s_min_u32 s46, s46, s35\n\t"
      "s_add_u32 s66, s64, 57344\n\t"
      "s_addc_u32 s67, s65, 0\n\t"
      "s_add_u32 s68, s66, 57344\n\t"
      "s_addc_u32 s69, s67, 0\n\t"
      "s_add_u32 s70, s68, 57344\n\t"
      "s_addc_u32 s71, s69, 0\n\t"
      "s_add_u32 s72, s70, 57344\n\t"
      "s_addc_u32 s73, s71, 0\n\t"
      "s_add_u32 s74, s72, 57344\n\t"
      "s_addc_u32 s75, s73, 0\n\t"
      "s_add_u32 s76, s74, 57344\n\t"
      "s_addc_u32 s77, s75, 0\n\t"
      "v_lshrrev_b32_e32 v240, 7, v244\n\t"
      "v_and_b32_e32 v241, 0x70, v244\n\t"
      "v_and_b32_e32 v242, 6, v240\n\t"
      "v_lshlrev_b32_e32 v242, 4, v242\n\t"
      "v_xor_b32_e32 v242, v242, v241\n\t"
      "v_lshl_or_b32 v228, v240, 12, v242\n\t"
      "s_mul_i32 s34, s24, 917504\n\t"
      "s_lshl_b32 s35, s29, 7\n\t"
      "s_add_u32 s34, s34, s35\n\t"
      "s_lshl_b32 s39, s24, 12\n\t"
      "s_add_u32 s54, s14, s34\n\t"
      "s_addc_u32 s55, s15, 0\n\t"
      "v_add_u32_e32 v229, 31744, v228\n\t"
      "v_add_u32_e32 v230, 63488, v228\n\t"
      "v_add_u32_e32 v231, 95232, v228\n\t"
      "s_lshl_b32 s37, s40, 7\n\t"
      "s_add_u32 s58, s54, s37\n\t"
      "s_addc_u32 s59, s55, 0\n\t"
      "s_add_u32 m0, s39, 0\n\t"
      "s_nop 0\n\t"
      "global_load_lds_dwordx4 v228, s[58:59]\n\t"
      "global_load_lds_dwordx4 v229, s[58:59] offset:1024\n\t"
      "global_load_lds_dwordx4 v230, s[58:59] offset:2048\n\t"
      "global_load_lds_dwordx4 v231, s[58:59] offset:3072\n\t"
      "s_lshl_b32 s37, s41, 7\n\t"
      "s_add_u32 s58, s54, s37\n\t"
      "s_addc_u32 s59, s55, 0\n\t"
      "s_add_u32 m0, s39, 32768\n\t"
      "s_nop 0\n\t"
      "global_load_lds_dwordx4 v228, s[58:59]\n\t"
      "global_load_lds_dwordx4 v229, s[58:59] offset:1024\n\t"
      "global_load_lds_dwordx4 v230, s[58:59] offset:2048\n\t"
      "global_load_lds_dwordx4 v231, s[58:59] offset:3072\n\t"
      "s_lshl_b32 s37, s42, 7\n\t"
      "s_add_u32 s58, s54, s37\n\t"
      "s_addc_u32 s59, s55, 0\n\t"
      "s_add_u32 m0, s39, 65536\n\t"
      "s_nop 0\n\t"
      "global_load_lds_dwordx4 v228, s[58:59]\n\t"
      "global_load_lds_dwordx4 v229, s[58:59] offset:1024\n\t"
      "global_load_lds_dwordx4 v230, s[58:59] offset:2048\n\t"
      "global_load_lds_dwordx4 v231, s[58:59] offset:3072\n\t"
      "s_lshl_b32 s37, s43, 7\n\t"
      "s_add_u32 s58, s54, s37\n\t"
      "s_addc_u32 s59, s55, 0\n\t"
      "s_add_u32 m0, s39, 98304\n\t"
      "s_nop 0\n\t"
      "global_load_lds_dwordx4 v228, s[58:59]\n\t"
      "global_load_lds_dwordx4 v229, s[58:59] offset:1024\n\t"
      "global_load_lds_dwordx4 v230, s[58:59] offset:2048\n\t"
      "global_load_lds_dwordx4 v231, s[58:59] offset:3072\n\t"
      "s_lshr_b32 s34, s24, 1\n\t"
      "s_lshl2_add_u32 s35, s29, s34\n\t"
      "s_and_b32 s36, s24, 1\n\t"
      "s_lshl4_add_u32 s35, s35, s36\n\t"
      "v_lshl_add_u32 v242, v240, 1, s35\n\t"
      "v_lshlrev_b32_e32 v248, 1, v242\n\t"
      "global_load_ushort v249, v248, s[16:17]\n\t"
      "s_movk_i32 s35, 896\n\t"
      "v_and_b32_e32 v247, 64, v244\n\t"
      "v_lshrrev_b32_e32 v247, 2, v247\n\t"
      "v_mad_u32_u24 v247, v242, s35, v247\n\t"
      "v_and_b32_e32 v242, 0x30, v244\n\t"
      "v_lshl_add_u32 v247, v242, 1, v247\n\t"
      "s_mov_b32 s82, 0x77777777\n\t"
      "s_mov_b32 s83, 0x77777777\n\t"
      "s_lshl_b32 s35, s32, 7\n\t"
      "s_add_u32 s62, s18, s35\n\t"
      "s_addc_u32 s63, s19, 0\n\t"
      "s_mul_i32 s35, s32, 96\n\t"
      "s_add_u32 s35, s35, 512\n\t"
      "s_add_u32 s18, s18, s35\n\t"
      "s_addc_u32 s19, s19, 0\n\t"
      "s_mul_i32 s35, s29, 28\n\t"
      "s_lshl2_add_u32 s35, s32, s35\n\t"
      "s_lshl1_add_u32 s35, s35, s36\n\t"
      "s_lshl2_add_u32 s35, s35, s34\n\t"
      "s_lshl_b32 s35, s35, 8\n\t"
      "s_add_u32 s35, s35, 6295552\n\t"
      "s_add_u32 s50, s20, s35\n\t"
      "s_addc_u32 s51, s21, 0\n\t"
      "v_lshrrev_b32_e32 v248, 2, v244\n\t"
      "v_bfe_u32 v242, v244, 8, 1\n\t"
      "v_lshlrev_b32_e32 v242, 4, v242\n\t"
      "v_xor_b32_e32 v245, v244, v242\n\t"
      "v_bfe_u32 v242, v244, 9, 1\n\t"
      "v_lshlrev_b32_e32 v242, 7, v242\n\t"
      "v_xor_b32_e32 v245, v245, v242\n\t"
      "s_add_u32 s35, s39, 98304\n\t"
      "v_add_u32_e32 v245, s35, v245\n\t"
      "v_lshl_or_b32 v246, v240, 1, s36\n\t"
      "v_bfe_u32 v242, v244, 5, 1\n\t"
      "v_xor_b32_e32 v246, v246, v242\n\t"
      "v_bfe_u32 v242, v244, 6, 1\n\t"
      "v_lshlrev_b32_e32 v242, 3, v242\n\t"
      "v_xor_b32_e32 v246, v246, v242\n\t"
      "v_lshlrev_b32_e32 v246, 4, v246\n\t"
      "v_and_b32_e32 v242, 0x60, v244\n\t"
      "v_lshl_or_b32 v246, v242, 3, v246\n\t"
      "v_bfe_u32 v242, v244, 4, 1\n\t"
      "v_lshl_or_b32 v246, v242, 3, v246\n\t"
      "s_lshl_b32 s34, s34, 10\n\t"
      "s_add_u32 s34, s34, 98304\n\t"
      "v_add_u32_e32 v246, s34, v246\n\t"
      "v_and_b32_e32 v240, 0x3c0, v244\n\t"
      "v_lshlrev_b32_e32 v240, 1, v240\n\t"
      "v_and_b32_e32 v241, 0x180, v244\n\t"
      "v_lshrrev_b32_e32 v241, 2, v241\n\t"
      "v_and_b32_e32 v242, 0x30, v244\n\t"
      "v_lshrrev_b32_e32 v242, 1, v242\n\t"
      "v_or3_b32 v232, v240, v241, v242\n\t"
      "v_add_u32_e32 v232, s39, v232\n\t"
      "v_min_u32_e32 v252, 432, v244\n\t"
      "v_lshlrev_b32_e32 v252, 4, v252\n\t"
      "v_xor_b32_e32 v233, 32, v232\n\t"
      "v_xor_b32_e32 v234, 64, v232\n\t"
      "v_xor_b32_e32 v235, 96, v232\n\t"
      "v_xor_b32_e32 v236, 65536, v232\n\t"
      "v_xor_b32_e32 v237, 65568, v232\n\t"
      "v_xor_b32_e32 v238, 65600, v232\n\t"
      "v_xor_b32_e32 v239, 65632, v232\n\t"
      "s_waitcnt vmcnt(13)\n\t"
      "ds_read_b64_tr_b16 v[0:1], v232\n\t"
      "ds_read_b64_tr_b16 v[2:3], v232 offset:2048\n\t"
      "ds_read_b64_tr_b16 v[4:5], v233\n\t"
      "ds_read_b64_tr_b16 v[6:7], v233 offset:2048\n\t"
      "ds_read_b64_tr_b16 v[8:9], v234\n\t"
      "ds_read_b64_tr_b16 v[10:11], v234 offset:2048\n\t"
      "ds_read_b64_tr_b16 v[12:13], v235\n\t"
      "ds_read_b64_tr_b16 v[14:15], v235 offset:2048\n\t"
      "s_waitcnt lgkmcnt(0)\n\t"
      "s_lshl_b32 s37, s44, 7\n\t"
      "s_add_u32 s58, s54, s37\n\t"
      "s_addc_u32 s59, s55, 0\n\t"
      "s_add_u32 m0, s39, 0\n\t"
      "s_nop 0\n\t"
      "global_load_lds_dwordx4 v228, s[58:59]\n\t"
      "global_load_lds_dwordx4 v229, s[58:59] offset:1024\n\t"
      "global_load_lds_dwordx4 v230, s[58:59] offset:2048\n\t"
      "global_load_lds_dwordx4 v231, s[58:59] offset:3072\n\t"
      "s_waitcnt vmcnt(13)\n\t"
      "ds_read_b64_tr_b16 v[16:17], v232 offset:32768\n\t"
      "ds_read_b64_tr_b16 v[18:19], v232 offset:34816\n\t"
      "ds_read_b64_tr_b16 v[20:21], v233 offset:32768\n\t"
      "ds_read_b64_tr_b16 v[22:23], v233 offset:34816\n\t"
      "ds_read_b64_tr_b16 v[24:25], v234 offset:32768\n\t"
      "ds_read_b64_tr_b16 v[26:27], v234 offset:34816\n\t"
      "ds_read_b64_tr_b16 v[28:29], v235 offset:32768\n\t"
      "ds_read_b64_tr_b16 v[30:31], v235 offset:34816\n\t"
      "s_waitcnt lgkmcnt(0)\n\t"
      "s_lshl_b32 s37, s45, 7\n\t"
      "s_add_u32 s58, s54, s37\n\t"
      "s_addc_u32 s59, s55, 0\n\t"
      "s_add_u32 m0, s39, 32768\n\t"
      "s_nop 0\n\t"
      "global_load_lds_dwordx4 v228, s[58:59]\n\t"
      "global_load_lds_dwordx4 v229, s[58:59] offset:1024\n\t"
      "global_load_lds_dwordx4 v230, s[58:59] offset:2048\n\t"
      "global_load_lds_dwordx4 v231, s[58:59] offset:3072\n\t"
      "s_waitcnt vmcnt(13)\n\t"
      "ds_read_b64_tr_b16 v[32:33], v236\n\t"
      "ds_read_b64_tr_b16 v[34:35], v236 offset:2048\n\t"
      "ds_read_b64_tr_b16 v[36:37], v237\n\t"
      "ds_read_b64_tr_b16 v[38:39], v237 offset:2048\n\t"
      "ds_read_b64_tr_b16 v[40:41], v238\n\t"
      "ds_read_b64_tr_b16 v[42:43], v238 offset:2048\n\t"
      "ds_read_b64_tr_b16 v[44:45], v239\n\t"
      "ds_read_b64_tr_b16 v[46:47], v239 offset:2048\n\t"
      "s_waitcnt lgkmcnt(0)\n\t"
      "s_lshl_b32 s37, s46, 7\n\t"
      "s_add_u32 s58, s54, s37\n\t"
      "s_addc_u32 s59, s55, 0\n\t"
      "s_add_u32 m0, s39, 65536\n\t"
      "s_nop 0\n\t"
      "global_load_lds_dwordx4 v228, s[58:59]\n\t"
      "global_load_lds_dwordx4 v229, s[58:59] offset:1024\n\t"
      "global_load_lds_dwordx4 v230, s[58:59] offset:2048\n\t"
      "global_load_lds_dwordx4 v231, s[58:59] offset:3072\n\t"
      "s_waitcnt vmcnt(13)\n\t"
      "ds_read_b64_tr_b16 v[48:49], v236 offset:32768\n\t"
      "ds_read_b64_tr_b16 v[50:51], v236 offset:34816\n\t"
      "ds_read_b64_tr_b16 v[52:53], v237 offset:32768\n\t"
      "ds_read_b64_tr_b16 v[54:55], v237 offset:34816\n\t"
      "ds_read_b64_tr_b16 v[56:57], v238 offset:32768\n\t"
      "ds_read_b64_tr_b16 v[58:59], v238 offset:34816\n\t"
      "ds_read_b64_tr_b16 v[60:61], v239 offset:32768\n\t"
      "ds_read_b64_tr_b16 v[62:63], v239 offset:34816\n\t"
      "s_waitcnt vmcnt(8)\n\t"
      "ds_read_b64_tr_b16 v[64:65], v232\n\t"
      "ds_read_b64_tr_b16 v[66:67], v232 offset:2048\n\t"
      "ds_read_b64_tr_b16 v[68:69], v233\n\t"
      "ds_read_b64_tr_b16 v[70:71], v233 offset:2048\n\t"
      "ds_read_b64_tr_b16 v[72:73], v234\n\t"
      "ds_read_b64_tr_b16 v[74:75], v234 offset:2048\n\t"
      "ds_read_b64_tr_b16 v[76:77], v235\n\t"
      "ds_read_b64_tr_b16 v[78:79], v235 offset:2048\n\t"
      "s_waitcnt vmcnt(4)\n\t"
      "ds_read_b64_tr_b16 v[80:81], v232 offset:32768\n\t"
      "ds_read_b64_tr_b16 v[82:83], v232 offset:34816\n\t"
      "ds_read_b64_tr_b16 v[84:85], v233 offset:32768\n\t"
      "ds_read_b64_tr_b16 v[86:87], v233 offset:34816\n\t"
      "ds_read_b64_tr_b16 v[88:89], v234 offset:32768\n\t"
      "ds_read_b64_tr_b16 v[90:91], v234 offset:34816\n\t"
      "ds_read_b64_tr_b16 v[92:93], v235 offset:32768\n\t"
      "ds_read_b64_tr_b16 v[94:95], v235 offset:34816\n\t"
      "s_waitcnt vmcnt(0)\n\t"
      "ds_read_b64_tr_b16 v[96:97], v236\n\t"
      "ds_read_b64_tr_b16 v[98:99], v236 offset:2048\n\t"
      "ds_read_b64_tr_b16 v[100:101], v237\n\t"
      "ds_read_b64_tr_b16 v[102:103], v237 offset:2048\n\t"
      "ds_read_b64_tr_b16 v[104:105], v238\n\t"
      "ds_read_b64_tr_b16 v[106:107], v238 offset:2048\n\t"
      "ds_read_b64_tr_b16 v[108:109], v239\n\t"
      "ds_read_b64_tr_b16 v[110:111], v239 offset:2048\n\t"
      "v_lshlrev_b32_e32 v249, 16, v249\n\t"
      "s_waitcnt lgkmcnt(0)\n\t"
      "v_min_u32_e32 v240, 48, v244\n\t"
      "v_mul_u32_u24_e32 v240, 7, v240\n\t"
      "s_lshl_b32 s34, s29, 4\n\t"
      "v_add_u32_e32 v240, s34, v240\n\t"
      "v_subrev_u32_e32 v241, 448, v240\n\t"
      "v_min_u32_e32 v240, v240, v241\n\t"
      "v_subrev_u32_e32 v241, 448, v240\n\t"
      "v_min_u32_e32 v240, v240, v241\n\t"
      "s_and_b32 s34, s29, 7\n\t"
      "s_lshl_b32 s34, s34, 8\n\t"
      "v_lshl_add_u32 v253, v240, 7, s34\n\t"
      "v_add_u32_e32 v143, 32768, v143\n\t"
      "s_cmp_lg_u32 s24, 3\n\t"
      "s_cbranch_scc1 SK18_%=\n\t"
      "global_load_dwordx2 v[138:139], v143, s[60:61] sc1\n\t"
      "SK18_%=:\n\t"
      "s_cmp_lg_u32 s24, 3\n\t"
      "s_cbranch_scc1 LF5B_%=\n\t"
      "s_waitcnt vmcnt(0)\n\t"
      "v_cmp_ne_u64_e32 vcc, s[22:23], v[138:139]\n\t"
      "s_cbranch_vccnz LF5R_%=\n\t"
      "LF5B_%=:\n\t"
      "s_barrier\n\t"
      "s_cmp_lg_u32 s24, 3\n\t"
      "s_cbranch_scc1 HF4_%=\n\t"
      "s_lshl3_add_u32 s34, s25, s26\n\t"
      "s_lshl_b32 s34, s34, 7\n\t"
      "s_add_u32 s34, s34, 98304\n\t"
      "v_mov_b32_e32 v238, s34\n\t"
      "v_mov_b32_e32 v236, s22\n\t"
      "v_mov_b32_e32 v237, s23\n\t"
      "s_mov_b64 exec, 1\n\t"
      "global_store_dwordx2 v238, v[236:237], s[60:61] sc1\n\t"
      "s_mov_b64 exec, -1\n\t"
      "HF4_%=:\n\t"
      "s_and_b32 s34, s29, 30\n\t"
      "s_lshl_b32 s34, s34, 10\n\t"
      "s_add_u32 s34, s34, 98304\n\t"
      "v_bfe_u32 v143, v244, 5, 2\n\t"
      "v_add_u32_e32 v143, 3, v143\n\t"
      "v_writelane_b32 v143, s32, 0\n\t"
      "v_writelane_b32 v143, s32, 1\n\t"
      "v_lshl_add_u32 v143, v143, 7, s34\n\t"
      "v_and_b32_e32 v142, 16, v244\n\t"
      "v_lshl_add_u32 v143, v142, 6, v143\n\t"
      "s_cmp_eq_u32 s24, 0\n\t"
      "s_cbranch_scc1 PG0R_%=\n\t"
      "PG0B_%=:\n\t"
      "s_sleep 6\n\t"
      "s_barrier\n\t"
      "s_cmp_lg_u32 s24, 3\n\t"
      "s_cbranch_scc1 SK19_%=\n\t"
      "global_load_dwordx2 v[240:241], v143, s[60:61] sc1\n\t"
      "SK19_%=:\n\t"
      "v_mov_b32_e32 v242, v143\n\t"
      "s_add_u32 s58, s64, s40\n\t"
      "s_addc_u32 s59, s65, 0\n\t"
      "global_load_dwordx4 v[128:131], v244, s[58:59] sc1\n\t"
      "s_add_u32 s58, s64, s41\n\t"
      "s_addc_u32 s59, s65, 0\n\t"
      "global_load_dwordx4 v[132:135], v244, s[58:59] sc1\n\t"
      "s_add_u32 s58, s64, s42\n\t"
      "s_addc_u32 s59, s65, 0\n\t"
      "global_load_dwordx4 v[136:139], v244, s[58:59] sc1\n\t"
      "s_add_u32 s58, s64, s43\n\t"
      "s_addc_u32 s59, s65, 0\n\t"
      "global_load_dwordx4 v[140:143], v244, s[58:59] sc1\n\t"
      "s_add_u32 s58, s64, s44\n\t"
      "s_addc_u32 s59, s65, 0\n\t"
      "global_load_dwordx4 v[144:147], v244, s[58:59] sc1\n\t"
      "s_add_u32 s58, s64, s45\n\t"
      "s_addc_u32 s59, s65, 0\n\t"
      "global_load_dwordx4 v[148:151], v244, s[58:59] sc1\n\t"
      "s_add_u32 s58, s64, s46\n\t"
      "s_addc_u32 s59, s65, 0\n\t"
      "global_load_dwordx4 v[152:155], v244, s[58:59] sc1\n\t"
      "v_mov_b32_e32 v254, v249\n\t"
      "s_waitcnt vmcnt(6)\n\t"
      "v_mfma_f32_16x16x32_bf16 v[196:199], v[128:131], v[0:3], 0\n\t"
      "v_mfma_f32_16x16x32_bf16 v[200:203], v[128:131], v[4:7], 0\n\t"
      "v_mfma_f32_16x16x32_bf16 v[204:207], v[128:131], v[8:11], 0\n\t"
      "v_mfma_f32_16x16x32_bf16 v[208:211], v[128:131], v[12:15], 0\n\t"
      "s_waitcnt vmcnt(5)\n\t"
      "v_mfma_f32_16x16x32_bf16 v[196:199], v[132:135], v[16:19], v[196:199]\n\t"
      "v_mfma_f32_16x16x32_bf16 v[200:203], v[132:135], v[20:23], v[200:203]\n\t"
      "v_mfma_f32_16x16x32_bf16 v[204:207], v[132:135], v[24:27], v[204:207]\n\t"
      "s_cmp_lg_u32 s24, 3\n\t"
      "s_cbranch_scc1 LF8B_%=\n\t"
      "v_cmp_ne_u64_e32 vcc, s[22:23], v[240:241]\n\t"
      "s_cbranch_vccnz LF8R_%=\n\t"
      "LF8B_%=:\n\t"
      "s_barrier\n\t"
      "v_mfma_f32_16x16x32_bf16 v[208:211], v[132:135], v[28:31], v[208:211]\n\t"
      "global_load_dword v115, v248, s[50:51] offset:-4096 sc1\n\t"
      "global_load_dword v116, v248, s[50:51] offset:-2048 sc1\n\t"
      "s_waitcnt vmcnt(6)\n\t"
      "v_mfma_f32_16x16x32_bf16 v[196:199], v[136:139], v[32:35], v[196:199]\n\t"
      "global_load_dword v117, v248, s[50:51] sc1\n\t"
      "global_load_dword v118, v248, s[50:51] offset:2048 sc1\n\t"
      "s_add_u32 s58, s66, s40\n\t"
      "s_addc_u32 s59, s67, 0\n\t"
      "global_load_dwordx4 v[156:159], v244, s[58:59] sc1\n\t"
      "v_mfma_f32_16x16x32_bf16 v[200:203], v[136:139], v[36:39], v[200:203]\n\t"
      "s_sub_u32 s34, 16, s32\n\t"
      "s_lshl_b32 s34, s34, 11\n\t"
      "s_add_u32 s50, s50, s34\n\t"
      "s_addc_u32 s51, s51, 0\n\t"
      "global_load_dword v112, v248, s[50:51] offset:-4096 sc1\n\t"
      "global_load_dword v113, v248, s[50:51] offset:-2048 sc1\n\t"
      "v_mfma_f32_16x16x32_bf16 v[204:207], v[136:139], v[40:43], v[204:207]\n\t"
      "global_load_dword v114, v248, s[50:51] sc1\n\t"
      "v_mfma_f32_16x16x32_bf16 v[208:211], v[136:139], v[44:47], v[208:211]\n\t"
      "s_waitcnt vmcnt(11)\n\t"
      "v_mfma_f32_16x16x32_bf16 v[196:199], v[140:143], v[48:51], v[196:199]\n\t"
      "s_add_u32 s58, s66, s41\n\t"
      "s_addc_u32 s59, s67, 0\n\t"
      "global_load_dwordx4 v[160:163], v244, s[58:59] sc1\n\t"
      "v_mfma_f32_16x16x32_bf16 v[200:203], v[140:143], v[52:55], v[200:203]\n\t"
      "v_mfma_f32_16x16x32_bf16 v[204:207], v[140:143], v[56:59], v[204:207]\n\t"
      "v_mfma_f32_16x16x32_bf16 v[208:211], v[140:143], v[60:63], v[208:211]\n\t"
      "s_waitcnt vmcnt(11)\n\t"
      "v_mfma_f32_16x16x32_bf16 v[196:199], v[144:147], v[64:67], v[196:199]\n\t"
      "s_add_u32 s58, s66, s42\n\t"
      "s_addc_u32 s59, s67, 0\n\t"
      "global_load_dwordx4 v[164:167], v244, s[58:59] sc1\n\t"
      "v_mfma_f32_16x16x32_bf16 v[200:203], v[144:147], v[68:71], v[200:203]\n\t"
      "v_mfma_f32_16x16x32_bf16 v[204:207], v[144:147], v[72:75], v[204:207]\n\t"
      "v_mfma_f32_16x16x32_bf16 v[208:211], v[144:147], v[76:79], v[208:211]\n\t"
      "s_waitcnt vmcnt(11)\n\t"
      "v_mfma_f32_16x16x32_bf16 v[196:199], v[148:151], v[80:83], v[196:199]\n\t"
      "s_add_u32 s58, s66, s43\n\t"
      "s_addc_u32 s59, s67, 0\n\t"
      "global_load_dwordx4 v[168:171], v244, s[58:59] sc1\n\t"
      "v_mfma_f32_16x16x32_bf16 v[200:203], v[148:151], v[84:87], v[200:203]\n\t"
      "v_mfma_f32_16x16x32_bf16 v[204:207], v[148:151], v[88:91], v[204:207]\n\t"
      "v_mfma_f32_16x16x32_bf16 v[208:211], v[148:151], v[92:95], v[208:211]\n\t"
      "s_waitcnt vmcnt(11)\n\t"
      "v_mfma_f32_16x16x32_bf16 v[196:199], v[152:155], v[96:99], v[196:199]\n\t"
      "s_add_u32 s58, s66, s44\n\t"
      "s_addc_u32 s59, s67, 0\n\t"
      "global_load_dwordx4 v[172:175], v244, s[58:59] sc1\n\t"
      "v_mfma_f32_16x16x32_bf16 v[200:203], v[152:155], v[100:103], v[200:203]\n\t"
      "v_mfma_f32_16x16x32_bf16 v[204:207], v[152:155], v[104:107], v[204:207]\n\t"
      "v_mfma_f32_16x16x32_bf16 v[208:211], v[152:155], v[108:111], v[208:211]\n\t"
      "s_nop 1\n\t"
      "v_add_f32_e32 v251, v196, v197\n\t"
      "v_add_f32_e32 v251, v251, v198\n\t"
      "v_cmp_u_f32_e32 vcc, v251, v199\n\t"
      "s_cbranch_vccnz LA40R_%=\n\t"
      "LA40B_%=:\n\t"
      "ds_write_b128 v245, v[196:199]\n\t"
      "s_waitcnt vmcnt(7)\n\t"
      "v_mfma_f32_16x16x32_bf16 v[212:215], v[156:159], v[0:3], 0\n\t"
      "ds_write_b128 v245, v[200:203] offset:1024\n\t"
      "s_add_u32 s58, s66, s45\n\t"
      "s_addc_u32 s59, s67, 0\n\t"
      "global_load_dwordx4 v[176:179], v244, s[58:59] sc1\n\t"
      "v_mfma_f32_16x16x32_bf16 v[216:219], v[156:159], v[4:7], 0\n\t"
      "ds_write_b128 v245, v[204:207] offset:2048\n\t"
      "v_mfma_f32_16x16x32_bf16 v[220:223], v[156:159], v[8:11], 0\n\t"
      "ds_write_b128 v245, v[208:211] offset:3072\n\t"
      "v_mfma_f32_16x16x32_bf16 v[224:227], v[156:159], v[12:15], 0\n\t"
      "s_waitcnt vmcnt(4)\n\t"
      "v_mfma_f32_16x16x32_bf16 v[212:215], v[160:163], v[16:19], v[212:215]\n\t"
      "s_add_u32 s58, s66, s46\n\t"
      "s_addc_u32 s59, s67, 0\n\t"
      "global_load_dwordx4 v[180:183], v244, s[58:59] sc1\n\t"
      "v_mfma_f32_16x16x32_bf16 v[216:219], v[160:163], v[20:23], v[216:219]\n\t"
      "v_mfma_f32_16x16x32_bf16 v[220:223], v[160:163], v[24:27], v[220:223]\n\t"
      "s_waitcnt lgkmcnt(0)\n\t"
      "s_barrier\n\t"
      "ds_read_b64 v[228:229], v246\n\t"
      "ds_read_b64 v[230:231], v246 offset:4096\n\t"
      "s_add_u32 s58, s68, s40\n\t"
      "s_addc_u32 s59, s69, 0\n\t"
      "global_load_dwordx4 v[184:187], v244, s[58:59] sc1\n\t"
      "v_mfma_f32_16x16x32_bf16 v[224:227], v[160:163], v[28:31], v[224:227]\n\t"
      "ds_read_b64 v[232:233], v246 offset:8192\n\t"
      "ds_read_b64 v[234:235], v246 offset:12288\n\t"
      "s_waitcnt vmcnt(5)\n\t"
      "v_mfma_f32_16x16x32_bf16 v[212:215], v[164:167], v[32:35], v[212:215]\n\t"
      "ds_read_b64 v[236:237], v246 offset:16384\n\t"
      "ds_read_b64 v[238:239], v246 offset:20480\n\t"
      "v_mfma_f32_16x16x32_bf16 v[216:219], v[164:167], v[36:39], v[216:219]\n\t"
      "ds_read_b64 v[240:241], v246 offset:24576\n\t"
      "ds_read_b64 v[242:243], v246 offset:28672\n\t"
      "v_lshlrev_b32_e32 v248, 16, v112\n\t"
      "v_mfma_f32_16x16x32_bf16 v[220:223], v[164:167], v[40:43], v[220:223]\n\t"
      "v_and_b32_e32 v249, 0xffff0000, v112\n\t"
      "s_add_u32 s58, s68, s41\n\t"
      "s_addc_u32 s59, s69, 0\n\t"
      "global_load_dwordx4 v[188:191], v244, s[58:59] sc1\n\t"
      "v_mfma_f32_16x16x32_bf16 v[224:227], v[164:167], v[44:47], v[224:227]\n\t"
      "v_add_f32_e32 v248, v254, v248\n\t"
      "v_add_f32_e32 v249, v254, v249\n\t"
      "s_waitcnt vmcnt(5)\n\t"
      "v_mfma_f32_16x16x32_bf16 v[212:215], v[168:171], v[48:51], v[212:215]\n\t"
      "s_waitcnt lgkmcnt(4)\n\t"
      "v_mfma_f32_16x16x32_bf16 v[216:219], v[168:171], v[52:55], v[216:219]\n\t"
      "v_add_f32_e32 v228, v228, v230\n\t"
      "v_add_f32_e32 v232, v232, v234\n\t"
      "v_mfma_f32_16x16x32_bf16 v[220:223], v[168:171], v[56:59], v[220:223]\n\t"
      "v_add_f32_e32 v229, v229, v231\n\t"
      "s_add_u32 s58, s68, s42\n\t"
      "s_addc_u32 s59, s69, 0\n\t"
      "global_load_dwordx4 v[192:195], v244, s[58:59] sc1\n\t"
      "v_mfma_f32_16x16x32_bf16 v[224:227], v[168:171], v[60:63], v[224:227]\n\t"
      "v_add_f32_e32 v233, v233, v235\n\t"
      "s_waitcnt lgkmcnt(0)\n\t"
      "s_waitcnt vmcnt(5)\n\t"
      "v_mfma_f32_16x16x32_bf16 v[212:215], v[172:175], v[64:67], v[212:215]\n\t"
      "v_add_f32_e32 v236, v236, v238\n\t"
      "v_mfma_f32_16x16x32_bf16 v[216:219], v[172:175], v[68:71], v[216:219]\n\t"
      "v_add_f32_e32 v240, v240, v242\n\t"
      "v_add_f32_e32 v237, v237, v239\n\t"
      "v_mfma_f32_16x16x32_bf16 v[220:223], v[172:175], v[72:75], v[220:223]\n\t"
      "v_add_f32_e32 v241, v241, v243\n\t"
      "s_add_u32 s58, s68, s43\n\t"
      "s_addc_u32 s59, s69, 0\n\t"
      "global_load_dwordx4 v[128:131], v244, s[58:59] sc1\n\t"
      "v_mfma_f32_16x16x32_bf16 v[224:227], v[172:175], v[76:79], v[224:227]\n\t"
      "v_add_f32_e32 v228, v228, v232\n\t"
      "v_add_f32_e32 v236, v236, v240\n\t"
      "s_waitcnt vmcnt(5)\n\t"
      "v_mfma_f32_16x16x32_bf16 v[212:215], v[176:179], v[80:83], v[212:215]\n\t"
      "v_add_f32_e32 v229, v229, v233\n\t"
      "v_mfma_f32_16x16x32_bf16 v[216:219], v[176:179], v[84:87], v[216:219]\n\t"
      "v_add_f32_e32 v237, v237, v241\n\t"
      "v_add_f32_e32 v228, v228, v248\n\t"
      "v_mfma_f32_16x16x32_bf16 v[220:223], v[176:179], v[88:91], v[220:223]\n\t"
      "v_add_f32_e32 v229, v229, v249\n\t"
      "s_add_u32 s58, s68, s44\n\t"
      "s_addc_u32 s59, s69, 0\n\t"
      "global_load_dwordx4 v[132:135], v244, s[58:59] sc1\n\t"
      "v_mfma_f32_16x16x32_bf16 v[224:227], v[176:179], v[92:95], v[224:227]\n\t"
      "v_add_f32_e32 v228, v228, v236\n\t"
      "v_add_f32_e32 v229, v229, v237\n\t"
      "s_waitcnt vmcnt(5)\n\t"
      "v_mfma_f32_16x16x32_bf16 v[212:215], v[180:183], v[96:99], v[212:215]\n\t"
      "v_cvt_pk_bf16_f32 v232, v228, v229\n\t"
      "v_mfma_f32_16x16x32_bf16 v[216:219], v[180:183], v[100:103], v[216:219]\n\t"
      "s_mov_b32 vcc_lo, 0xeeeeeeee\n\t"
      "s_mov_b32 vcc_hi, 0xeeeeeeee\n\t"
      "v_cndmask_b32_dpp v120, v232, v120, vcc quad_perm:[0,0,0,0] row_mask:0xf bank_mask:0xf\n\t"
      "v_cndmask_b32_dpp v121, v232, v121, vcc quad_perm:[1,1,1,1] row_mask:0xf bank_mask:0xf\n\t"
      "v_cndmask_b32_dpp v122, v232, v122, vcc quad_perm:[2,2,2,2] row_mask:0xf bank_mask:0xf\n\t"
      "v_cndmask_b32_dpp v123, v232, v123, vcc quad_perm:[3,3,3,3] row_mask:0xf bank_mask:0xf\n\t"
      "v_mfma_f32_16x16x32_bf16 v[220:223], v[180:183], v[104:107], v[220:223]\n\t"
      "s_add_u32 s58, s68, s45\n\t"
      "s_addc_u32 s59, s69, 0\n\t"
      "global_load_dwordx4 v[136:139], v244, s[58:59] sc1\n\t"
      "v_mfma_f32_16x16x32_bf16 v[224:227], v[180:183], v[108:111], v[224:227]\n\t"
      "v_add_f32_e32 v251, v212, v213\n\t"
      "v_add_f32_e32 v251, v251, v214\n\t"
      "v_cmp_u_f32_e32 vcc, v251, v215\n\t"
      "s_cbranch_vccnz LA41R_%=\n\t"
      "LA41B_%=:\n\t"
      "ds_write_b128 v245, v[212:215] offset:32768\n\t"
      "s_waitcnt vmcnt(5)\n\t"
      "v_mfma_f32_16x16x32_bf16 v[196:199], v[184:187], v[0:3], 0\n\t"
      "ds_write_b128 v245, v[216:219] offset:33792\n\t"
      "v_mfma_f32_16x16x32_bf16 v[200:203], v[184:187], v[4:7], 0\n\t"
      "ds_write_b128 v245, v[220:223] offset:34816\n\t"
      "v_mfma_f32_16x16x32_bf16 v[204:207], v[184:187], v[8:11], 0\n\t"
      "ds_write_b128 v245, v[224:227] offset:35840\n\t"
      "s_add_u32 s58, s68, s46\n\t"
      "s_addc_u32 s59, s69, 0\n\t"
      "global_load_dwordx4 v[140:143], v244, s[58:59] sc1\n\t"
      "v_mfma_f32_16x16x32_bf16 v[208:211], v[184:187], v[12:15], 0\n\t"
      "s_waitcnt vmcnt(5)\n\t"
      "v_mfma_f32_16x16x32_bf16 v[196:199], v[188:191], v[16:19], v[196:199]\n\t"
      "v_mfma_f32_16x16x32_bf16 v[200:203], v[188:191], v[20:23], v[200:203]\n\t"
      "v_mfma_f32_16x16x32_bf16 v[204:207], v[188:191], v[24:27], v[204:207]\n\t"
      "s_waitcnt lgkmcnt(0)\n\t"
      "s_barrier\n\t"
      "ds_read_b64 v[228:229], v246 offset:32768\n\t"
      "ds_read_b64 v[230:231], v246 offset:36864\n\t"
      "s_add_u32 s58, s70, s40\n\t"
      "s_addc_u32 s59, s71, 0\n\t"
      "global_load_dwordx4 v[144:147], v244, s[58:59] sc1\n\t"
      "v_mfma_f32_16x16x32_bf16 v[208:211], v[188:191], v[28:31], v[208:211]\n\t"
      "ds_read_b64 v[232:233], v246 offset:40960\n\t"
      "ds_read_b64 v[234:235], v246 offset:45056\n\t"
      "s_waitcnt vmcnt(5)\n\t"
      "v_mfma_f32_16x16x32_bf16 v[196:199], v[192:195], v[32:35], v[196:199]\n\t"
      "ds_read_b64 v[236:237], v246 offset:49152\n\t"
      "ds_read_b64 v[238:239], v246 offset:53248\n\t"
      "v_mfma_f32_16x16x32_bf16 v[200:203], v[192:195], v[36:39], v[200:203]\n\t"
      "ds_read_b64 v[240:241], v246 offset:57344\n\t"
      "ds_read_b64 v[242:243], v246 offset:61440\n\t"
      "v_lshlrev_b32_e32 v248, 16, v113\n\t"
      "v_mfma_f32_16x16x32_bf16 v[204:207], v[192:195], v[40:43], v[204:207]\n\t"
      "v_and_b32_e32 v249, 0xffff0000, v113\n\t"
      "s_add_u32 s58, s70, s41\n\t"
      "s_addc_u32 s59, s71, 0\n\t"
      "global_load_dwordx4 v[148:151], v244, s[58:59] sc1\n\t"
      "v_mfma_f32_16x16x32_bf16 v[208:211], v[192:195], v[44:47], v[208:211]\n\t"
      "v_add_f32_e32 v248, v254, v248\n\t"
      "v_add_f32_e32 v249, v254, v249\n\t"
      "s_waitcnt vmcnt(5)\n\t"
      "v_mfma_f32_16x16x32_bf16 v[196:199], v[128:131], v[48:51], v[196:199]\n\t"
      "s_waitcnt lgkmcnt(4)\n\t"
      "v_mfma_f32_16x16x32_bf16 v[200:203], v[128:131], v[52:55], v[200:203]\n\t"
      "v_add_f32_e32 v228, v228, v230\n\t"
      "v_add_f32_e32 v232, v232, v234\n\t"
      "v_mfma_f32_16x16x32_bf16 v[204:207], v[128:131], v[56:59], v[204:207]\n\t"
      "v_add_f32_e32 v229, v229, v231\n\t"
      "s_add_u32 s58, s70, s42\n\t"
      "s_addc_u32 s59, s71, 0\n\t"
      "global_load_dwordx4 v[152:155], v244, s[58:59] sc1\n\t"
      "v_mfma_f32_16x16x32_bf16 v[208:211], v[128:131], v[60:63], v[208:211]\n\t"
      "v_add_f32_e32 v233, v233, v235\n\t"
      "s_waitcnt lgkmcnt(0)\n\t"
      "s_waitcnt vmcnt(5)\n\t"
      "v_mfma_f32_16x16x32_bf16 v[196:199], v[132:135], v[64:67], v[196:199]\n\t"
      "v_add_f32_e32 v236, v236, v238\n\t"
      "v_mfma_f32_16x16x32_bf16 v[200:203], v[132:135], v[68:71], v[200:203]\n\t"
      "v_add_f32_e32 v240, v240, v242\n\t"
      "v_add_f32_e32 v237, v237, v239\n\t"
      "v_mfma_f32_16x16x32_bf16 v[204:207], v[132:135], v[72:75], v[204:207]\n\t"
      "v_add_f32_e32 v241, v241, v243\n\t"
      "s_add_u32 s58, s70, s43\n\t"
      "s_addc_u32 s59, s71, 0\n\t"
      "global_load_dwordx4 v[156:159], v244, s[58:59] sc1\n\t"
      "v_mfma_f32_16x16x32_bf16 v[208:211], v[132:135], v[76:79], v[208:211]\n\t"
      "v_add_f32_e32 v228, v228, v232\n\t"
      "v_add_f32_e32 v236, v236, v240\n\t"
      "s_waitcnt vmcnt(5)\n\t"
      "v_mfma_f32_16x16x32_bf16 v[196:199], v[136:139], v[80:83], v[196:199]\n\t"
      "v_add_f32_e32 v229, v229, v233\n\t"
      "v_mfma_f32_16x16x32_bf16 v[200:203], v[136:139], v[84:87], v[200:203]\n\t"
      "v_add_f32_e32 v237, v237, v241\n\t"
      "v_add_f32_e32 v228, v228, v248\n\t"
      "v_mfma_f32_16x16x32_bf16 v[204:207], v[136:139], v[88:91], v[204:207]\n\t"
      "v_add_f32_e32 v229, v229, v249\n\t"
      "s_add_u32 s58, s70, s44\n\t"
      "s_addc_u32 s59, s71, 0\n\t"
      "global_load_dwordx4 v[160:163], v244, s[58:59] sc1\n\t"
      "v_mfma_f32_16x16x32_bf16 v[208:211], v[136:139], v[92:95], v[208:211]\n\t"
      "v_add_f32_e32 v228, v228, v236\n\t"
      "v_add_f32_e32 v229, v229, v237\n\t"
      "s_waitcnt vmcnt(5)\n\t"
      "v_mfma_f32_16x16x32_bf16 v[196:199], v[140:143], v[96:99], v[196:199]\n\t"
      "v_cvt_pk_bf16_f32 v232, v228, v229\n\t"
      "v_mfma_f32_16x16x32_bf16 v[200:203], v[140:143], v[100:103], v[200:203]\n\t"
      "s_mov_b32 vcc_lo, 0xdddddddd\n\t"
      "s_mov_b32 vcc_hi, 0xdddddddd\n\t"
      "v_cndmask_b32_dpp v120, v232, v120, vcc quad_perm:[0,0,0,0] row_mask:0xf bank_mask:0xf\n\t"
      "v_cndmask_b32_dpp v121, v232, v121, vcc quad_perm:[1,1,1,1] row_mask:0xf bank_mask:0xf\n\t"
      "v_cndmask_b32_dpp v122, v232, v122, vcc quad_perm:[2,2,2,2] row_mask:0xf bank_mask:0xf\n\t"
      "v_cndmask_b32_dpp v123, v232, v123, vcc quad_perm:[3,3,3,3] row_mask:0xf bank_mask:0xf\n\t"
      "v_mfma_f32_16x16x32_bf16 v[204:207], v[140:143], v[104:107], v[204:207]\n\t"
      "s_add_u32 s58, s70, s45\n\t"
      "s_addc_u32 s59, s71, 0\n\t"
      "global_load_dwordx4 v[164:167], v244, s[58:59] sc1\n\t"
      "v_mfma_f32_16x16x32_bf16 v[208:211], v[140:143], v[108:111], v[208:211]\n\t"
      "v_add_f32_e32 v251, v196, v197\n\t"
      "v_add_f32_e32 v251, v251, v198\n\t"
      "v_cmp_u_f32_e32 vcc, v251, v199\n\t"
      "s_cbranch_vccnz LA42R_%=\n\t"
      "LA42B_%=:\n\t"
      "ds_write_b128 v245, v[196:199]\n\t"
      "s_waitcnt vmcnt(5)\n\t"
      "v_mfma_f32_16x16x32_bf16 v[212:215], v[144:147], v[0:3], 0\n\t"
      "ds_write_b128 v245, v[200:203] offset:1024\n\t"
      "v_mfma_f32_16x16x32_bf16 v[216:219], v[144:147], v[4:7], 0\n\t"
      "ds_write_b128 v245, v[204:207] offset:2048\n\t"
      "v_mfma_f32_16x16x32_bf16 v[220:223], v[144:147], v[8:11], 0\n\t"
      "ds_write_b128 v245, v[208:211] offset:3072\n\t"
      "s_add_u32 s58, s70, s46\n\t"
      "s_addc_u32 s59, s71, 0\n\t"
      "global_load_dwordx4 v[168:171], v244, s[58:59] sc1\n\t"
      "v_mfma_f32_16x16x32_bf16 v[224:227], v[144:147], v[12:15], 0\n\t"
      "s_waitcnt vmcnt(5)\n\t"
      "v_mfma_f32_16x16x32_bf16 v[212:215], v[148:151], v[16:19], v[212:215]\n\t"
      "v_mfma_f32_16x16x32_bf16 v[216:219], v[148:151], v[20:23], v[216:219]\n\t"
      "v_mfma_f32_16x16x32_bf16 v[220:223], v[148:151], v[24:27], v[220:223]\n\t"
      "s_waitcnt lgkmcnt(0)\n\t"
      "s_barrier\n\t"
      "ds_read_b64 v[228:229], v246\n\t"
      "ds_read_b64 v[230:231], v246 offset:4096\n\t"
      "s_add_u32 s58, s72, s40\n\t"
      "s_addc_u32 s59, s73, 0\n\t"
      "global_load_dwordx4 v[172:175], v244, s[58:59] sc1\n\t"
      "v_mfma_f32_16x16x32_bf16 v[224:227], v[148:151], v[28:31], v[224:227]\n\t"
      "ds_read_b64 v[232:233], v246 offset:8192\n\t"
      "ds_read_b64 v[234:235], v246 offset:12288\n\t"
      "s_waitcnt vmcnt(5)\n\t"
      "v_mfma_f32_16x16x32_bf16 v[212:215], v[152:155], v[32:35], v[212:215]\n\t"
      "ds_read_b64 v[236:237], v246 offset:16384\n\t"
      "ds_read_b64 v[238:239], v246 offset:20480\n\t"
      "v_mfma_f32_16x16x32_bf16 v[216:219], v[152:155], v[36:39], v[216:219]\n\t"
      "ds_read_b64 v[240:241], v246 offset:24576\n\t"
      "ds_read_b64 v[242:243], v246 offset:28672\n\t"
      "v_lshlrev_b32_e32 v248, 16, v114\n\t"
      "v_mfma_f32_16x16x32_bf16 v[220:223], v[152:155], v[40:43], v[220:223]\n\t"
      "v_and_b32_e32 v249, 0xffff0000, v114\n\t"
      "s_add_u32 s58, s72, s41\n\t"
      "s_addc_u32 s59, s73, 0\n\t"
      "global_load_dwordx4 v[176:179], v244, s[58:59] sc1\n\t"
      "v_mfma_f32_16x16x32_bf16 v[224:227], v[152:155], v[44:47], v[224:227]\n\t"
      "v_add_f32_e32 v248, v254, v248\n\t"
      "v_add_f32_e32 v249, v254, v249\n\t"
      "s_waitcnt vmcnt(5)\n\t"
      "v_mfma_f32_16x16x32_bf16 v[212:215], v[156:159], v[48:51], v[212:215]\n\t"
      "s_waitcnt lgkmcnt(4)\n\t"
      "v_mfma_f32_16x16x32_bf16 v[216:219], v[156:159], v[52:55], v[216:219]\n\t"
      "v_add_f32_e32 v228, v228, v230\n\t"
      "v_add_f32_e32 v232, v232, v234\n\t"
      "v_mfma_f32_16x16x32_bf16 v[220:223], v[156:159], v[56:59], v[220:223]\n\t"
      "v_add_f32_e32 v229, v229, v231\n\t"
      "s_add_u32 s58, s72, s42\n\t"
      "s_addc_u32 s59, s73, 0\n\t"
      "global_load_dwordx4 v[180:183], v244, s[58:59] sc1\n\t"
      "v_mfma_f32_16x16x32_bf16 v[224:227], v[156:159], v[60:63], v[224:227]\n\t"
      "v_add_f32_e32 v233, v233, v235\n\t"
      "s_waitcnt lgkmcnt(0)\n\t"
      "s_waitcnt vmcnt(5)\n\t"
      "v_mfma_f32_16x16x32_bf16 v[212:215], v[160:163], v[64:67], v[212:215]\n\t"
      "v_add_f32_e32 v236, v236, v238\n\t"
      "v_mfma_f32_16x16x32_bf16 v[216:219], v[160:163], v[68:71], v[216:219]\n\t"
      "v_add_f32_e32 v240, v240, v242\n\t"
      "v_add_f32_e32 v237, v237, v239\n\t"
      "v_mfma_f32_16x16x32_bf16 v[220:223], v[160:163], v[72:75], v[220:223]\n\t"
      "v_add_f32_e32 v241, v241, v243\n\t"
      "s_add_u32 s58, s72, s43\n\t"
      "s_addc_u32 s59, s73, 0\n\t"
      "global_load_dwordx4 v[184:187], v244, s[58:59] sc1\n\t"
      "v_mfma_f32_16x16x32_bf16 v[224:227], v[160:163], v[76:79], v[224:227]\n\t"
      "v_add_f32_e32 v228, v228, v232\n\t"
      "v_add_f32_e32 v236, v236, v240\n\t"
      "s_waitcnt vmcnt(5)\n\t"
      "v_mfma_f32_16x16x32_bf16 v[212:215], v[164:167], v[80:83], v[212:215]\n\t"
      "v_add_f32_e32 v229, v229, v233\n\t"
      "v_mfma_f32_16x16x32_bf16 v[216:219], v[164:167], v[84:87], v[216:219]\n\t"
      "v_add_f32_e32 v237, v237, v241\n\t"
      "v_add_f32_e32 v228, v228, v248\n\t"
      "v_mfma_f32_16x16x32_bf16 v[220:223], v[164:167], v[88:91], v[220:223]\n\t"
      "v_add_f32_e32 v229, v229, v249\n\t"
      "s_add_u32 s58, s72, s44\n\t"
      "s_addc_u32 s59, s73, 0\n\t"
      "global_load_dwordx4 v[188:191], v244, s[58:59] sc1\n\t"
      "v_mfma_f32_16x16x32_bf16 v[224:227], v[164:167], v[92:95], v[224:227]\n\t"
      "v_add_f32_e32 v228, v228, v236\n\t"
      "v_add_f32_e32 v229, v229, v237\n\t"
      "s_waitcnt vmcnt(5)\n\t"
      "v_mfma_f32_16x16x32_bf16 v[212:215], v[168:171], v[96:99], v[212:215]\n\t"
      "v_cvt_pk_bf16_f32 v232, v228, v229\n\t"
      "v_mfma_f32_16x16x32_bf16 v[216:219], v[168:171], v[100:103], v[216:219]\n\t"
      "s_mov_b32 vcc_lo, 0xbbbbbbbb\n\t"
      "s_mov_b32 vcc_hi, 0xbbbbbbbb\n\t"
      "v_cndmask_b32_dpp v120, v232, v120, vcc quad_perm:[0,0,0,0] row_mask:0xf bank_mask:0xf\n\t"
      "v_cndmask_b32_dpp v121, v232, v121, vcc quad_perm:[1,1,1,1] row_mask:0xf bank_mask:0xf\n\t"
      "v_cndmask_b32_dpp v122, v232, v122, vcc quad_perm:[2,2,2,2] row_mask:0xf bank_mask:0xf\n\t"
      "v_cndmask_b32_dpp v123, v232, v123, vcc quad_perm:[3,3,3,3] row_mask:0xf bank_mask:0xf\n\t"
      "v_mfma_f32_16x16x32_bf16 v[220:223], v[168:171], v[104:107], v[220:223]\n\t"
      "s_add_u32 s58, s72, s45\n\t"
      "s_addc_u32 s59, s73, 0\n\t"
      "global_load_dwordx4 v[192:195], v244, s[58:59] sc1\n\t"
      "v_mfma_f32_16x16x32_bf16 v[224:227], v[168:171], v[108:111], v[224:227]\n\t"
      "v_add_f32_e32 v251, v212, v213\n\t"
      "v_add_f32_e32 v251, v251, v214\n\t"
      "v_cmp_u_f32_e32 vcc, v251, v215\n\t"
      "s_cbranch_vccnz LA43R_%=\n\t"
      "LA43B_%=:\n\t"
      "ds_write_b128 v245, v[212:215] offset:32768\n\t"
      "s_waitcnt vmcnt(5)\n\t"
      "v_mfma_f32_16x16x32_bf16 v[196:199], v[172:175], v[0:3], 0\n\t"
      "ds_write_b128 v245, v[216:219] offset:33792\n\t"
      "v_mfma_f32_16x16x32_bf16 v[200:203], v[172:175], v[4:7], 0\n\t"
      "ds_write_b128 v245, v[220:223] offset:34816\n\t"
      "v_mfma_f32_16x16x32_bf16 v[204:207], v[172:175], v[8:11], 0\n\t"
      "ds_write_b128 v245, v[224:227] offset:35840\n\t"
      "s_add_u32 s58, s72, s46\n\t"
      "s_addc_u32 s59, s73, 0\n\t"
      "global_load_dwordx4 v[128:131], v244, s[58:59] sc1\n\t"
      "v_mfma_f32_16x16x32_bf16 v[208:211], v[172:175], v[12:15], 0\n\t"
      "s_waitcnt vmcnt(5)\n\t"
      "v_mfma_f32_16x16x32_bf16 v[196:199], v[176:179], v[16:19], v[196:199]\n\t"
      "v_mfma_f32_16x16x32_bf16 v[200:203], v[176:179], v[20:23], v[200:203]\n\t"
      "v_mfma_f32_16x16x32_bf16 v[204:207], v[176:179], v[24:27], v[204:207]\n\t"
      "s_waitcnt lgkmcnt(0)\n\t"
      "s_barrier\n\t"
      "ds_read_b64 v[228:229], v246 offset:32768\n\t"
      "ds_read_b64 v[230:231], v246 offset:36864\n\t"
      "s_add_u32 s58, s74, s40\n\t"
      "s_addc_u32 s59, s75, 0\n\t"
      "global_load_dwordx4 v[132:135], v244, s[58:59] sc1\n\t"
      "v_mfma_f32_16x16x32_bf16 v[208:211], v[176:179], v[28:31], v[208:211]\n\t"
      "ds_read_b64 v[232:233], v246 offset:40960\n\t"
      "ds_read_b64 v[234:235], v246 offset:45056\n\t"
      "s_waitcnt vmcnt(5)\n\t"
      "v_mfma_f32_16x16x32_bf16 v[196:199], v[180:183], v[32:35], v[196:199]\n\t"
      "ds_read_b64 v[236:237], v246 offset:49152\n\t"
      "ds_read_b64 v[238:239], v246 offset:53248\n\t"
      "v_mfma_f32_16x16x32_bf16 v[200:203], v[180:183], v[36:39], v[200:203]\n\t"
      "ds_read_b64 v[240:241], v246 offset:57344\n\t"
      "ds_read_b64 v[242:243], v246 offset:61440\n\t"
      "v_lshlrev_b32_e32 v248, 16, v115\n\t"
      "v_mfma_f32_16x16x32_bf16 v[204:207], v[180:183], v[40:43], v[204:207]\n\t"
      "v_and_b32_e32 v249, 0xffff0000, v115\n\t"
      "s_add_u32 s58, s74, s41\n\t"
      "s_addc_u32 s59, s75, 0\n\t"
      "global_load_dwordx4 v[136:139], v244, s[58:59] sc1\n\t"
      "v_mfma_f32_16x16x32_bf16 v[208:211], v[180:183], v[44:47], v[208:211]\n\t"
      "v_add_f32_e32 v248, v254, v248\n\t"
      "v_add_f32_e32 v249, v254, v249\n\t"
      "s_waitcnt vmcnt(5)\n\t"
      "v_mfma_f32_16x16x32_bf16 v[196:199], v[184:187], v[48:51], v[196:199]\n\t"
      "s_waitcnt lgkmcnt(4)\n\t"
      "v_mfma_f32_16x16x32_bf16 v[200:203], v[184:187], v[52:55], v[200:203]\n\t"
      "v_add_f32_e32 v228, v228, v230\n\t"
      "v_add_f32_e32 v232, v232, v234\n\t"
      "v_mfma_f32_16x16x32_bf16 v[204:207], v[184:187], v[56:59], v[204:207]\n\t"
      "v_add_f32_e32 v229, v229, v231\n\t"
      "s_add_u32 s58, s74, s42\n\t"
      "s_addc_u32 s59, s75, 0\n\t"
      "global_load_dwordx4 v[140:143], v244, s[58:59] sc1\n\t"
      "v_mfma_f32_16x16x32_bf16 v[208:211], v[184:187], v[60:63], v[208:211]\n\t"
      "v_add_f32_e32 v233, v233, v235\n\t"
      "s_waitcnt lgkmcnt(0)\n\t"
      "s_waitcnt vmcnt(5)\n\t"
      "v_mfma_f32_16x16x32_bf16 v[196:199], v[188:191], v[64:67], v[196:199]\n\t"
      "v_add_f32_e32 v236, v236, v238\n\t"
      "v_mfma_f32_16x16x32_bf16 v[200:203], v[188:191], v[68:71], v[200:203]\n\t"
      "v_add_f32_e32 v240, v240, v242\n\t"
      "v_add_f32_e32 v237, v237, v239\n\t"
      "v_mfma_f32_16x16x32_bf16 v[204:207], v[188:191], v[72:75], v[204:207]\n\t"
      "v_add_f32_e32 v241, v241, v243\n\t"
      "s_add_u32 s58, s74, s43\n\t"
      "s_addc_u32 s59, s75, 0\n\t"
      "global_load_dwordx4 v[144:147], v244, s[58:59] sc1\n\t"
      "v_mfma_f32_16x16x32_bf16 v[208:211], v[188:191], v[76:79], v[208:211]\n\t"
      "v_add_f32_e32 v228, v228, v232\n\t"
      "v_add_f32_e32 v236, v236, v240\n\t"
      "s_waitcnt vmcnt(5)\n\t"
      "v_mfma_f32_16x16x32_bf16 v[196:199], v[192:195], v[80:83], v[196:199]\n\t"
      "v_add_f32_e32 v229, v229, v233\n\t"
      "v_mfma_f32_16x16x32_bf16 v[200:203], v[192:195], v[84:87], v[200:203]\n\t"
      "v_add_f32_e32 v237, v237, v241\n\t"
      "v_add_f32_e32 v228, v228, v248\n\t"
      "v_mfma_f32_16x16x32_bf16 v[204:207], v[192:195], v[88:91], v[204:207]\n\t"
      "v_add_f32_e32 v229, v229, v249\n\t"
      "s_add_u32 s58, s74, s44\n\t"
      "s_addc_u32 s59, s75, 0\n\t"
      "global_load_dwordx4 v[148:151], v244, s[58:59] sc1\n\t"
      "v_mfma_f32_16x16x32_bf16 v[208:211], v[192:195], v[92:95], v[208:211]\n\t"
      "v_add_f32_e32 v228, v228, v236\n\t"
      "v_add_f32_e32 v229, v229, v237\n\t"
      "s_waitcnt vmcnt(5)\n\t"
      "v_mfma_f32_16x16x32_bf16 v[196:199], v[128:131], v[96:99], v[196:199]\n\t"
      "v_cvt_pk_bf16_f32 v232, v228, v229\n\t"
      "v_mfma_f32_16x16x32_bf16 v[200:203], v[128:131], v[100:103], v[200:203]\n\t"
      "s_mov_b32 vcc_lo, 0xeeeeeeee\n\t"
      "s_mov_b32 vcc_hi, 0xeeeeeeee\n\t"
      "v_cndmask_b32_dpp v124, v232, v124, vcc quad_perm:[0,0,0,0] row_mask:0xf bank_mask:0xf\n\t"
      "v_cndmask_b32_dpp v125, v232, v125, vcc quad_perm:[1,1,1,1] row_mask:0xf bank_mask:0xf\n\t"
      "v_cndmask_b32_dpp v126, v232, v126, vcc quad_perm:[2,2,2,2] row_mask:0xf bank_mask:0xf\n\t"
      "v_cndmask_b32_dpp v127, v232, v127, vcc quad_perm:[3,3,3,3] row_mask:0xf bank_mask:0xf\n\t"
      "v_mfma_f32_16x16x32_bf16 v[204:207], v[128:131], v[104:107], v[204:207]\n\t"
      "s_add_u32 s58, s74, s45\n\t"
      "s_addc_u32 s59, s75, 0\n\t"
      "global_load_dwordx4 v[152:155], v244, s[58:59] sc1\n\t"
      "v_mfma_f32_16x16x32_bf16 v[208:211], v[128:131], v[108:111], v[208:211]\n\t"
      "v_add_f32_e32 v251, v196, v197\n\t"
      "v_add_f32_e32 v251, v251, v198\n\t"
      "v_cmp_u_f32_e32 vcc, v251, v199\n\t"
      "s_cbranch_vccnz LA44R_%=\n\t"
      "LA44B_%=:\n\t"
      "ds_write_b128 v245, v[196:199]\n\t"
      "s_waitcnt vmcnt(5)\n\t"
      "v_mfma_f32_16x16x32_bf16 v[212:215], v[132:135], v[0:3], 0\n\t"
      "ds_write_b128 v245, v[200:203] offset:1024\n\t"
      "v_mfma_f32_16x16x32_bf16 v[216:219], v[132:135], v[4:7], 0\n\t"
      "ds_write_b128 v245, v[204:207] offset:2048\n\t"
      "v_mfma_f32_16x16x32_bf16 v[220:223], v[132:135], v[8:11], 0\n\t"
      "ds_write_b128 v245, v[208:211] offset:3072\n\t"
      "s_add_u32 s58, s74, s46\n\t"
      "s_addc_u32 s59, s75, 0\n\t"
      "global_load_dwordx4 v[156:159], v244, s[58:59] sc1\n\t"
      "v_mfma_f32_16x16x32_bf16 v[224:227], v[132:135], v[12:15], 0\n\t"
      "s_waitcnt vmcnt(5)\n\t"
      "v_mfma_f32_16x16x32_bf16 v[212:215], v[136:139], v[16:19], v[212:215]\n\t"
      "v_mfma_f32_16x16x32_bf16 v[216:219], v[136:139], v[20:23], v[216:219]\n\t"
      "v_mfma_f32_16x16x32_bf16 v[220:223], v[136:139], v[24:27], v[220:223]\n\t"
      "s_waitcnt lgkmcnt(0)\n\t"
      "s_barrier\n\t"
      "ds_read_b64 v[228:229], v246\n\t"
      "ds_read_b64 v[230:231], v246 offset:4096\n\t"
      "s_add_u32 s58, s76, s40\n\t"
      "s_addc_u32 s59, s77, 0\n\t"
      "global_load_dwordx4 v[160:163], v244, s[58:59] sc1\n\t"
      "v_mfma_f32_16x16x32_bf16 v[224:227], v[136:139], v[28:31], v[224:227]\n\t"
      "ds_read_b64 v[232:233], v246 offset:8192\n\t"
      "ds_read_b64 v[234:235], v246 offset:12288\n\t"
      "s_waitcnt vmcnt(5)\n\t"
      "v_mfma_f32_16x16x32_bf16 v[212:215], v[140:143], v[32:35], v[212:215]\n\t"
      "ds_read_b64 v[236:237], v246 offset:16384\n\t"
      "ds_read_b64 v[238:239], v246 offset:20480\n\t"
      "v_mfma_f32_16x16x32_bf16 v[216:219], v[140:143], v[36:39], v[216:219]\n\t"
      "ds_read_b64 v[240:241], v246 offset:24576\n\t"
      "ds_read_b64 v[242:243], v246 offset:28672\n\t"
      "v_lshlrev_b32_e32 v248, 16, v116\n\t"
      "v_mfma_f32_16x16x32_bf16 v[220:223], v[140:143], v[40:43], v[220:223]\n\t"
      "v_and_b32_e32 v249, 0xffff0000, v116\n\t"
      "s_add_u32 s58, s76, s41\n\t"
      "s_addc_u32 s59, s77, 0\n\t"
      "global_load_dwordx4 v[164:167], v244, s[58:59] sc1\n\t"
      "v_mfma_f32_16x16x32_bf16 v[224:227], v[140:143], v[44:47], v[224:227]\n\t"
      "v_add_f32_e32 v248, v254, v248\n\t"
      "v_add_f32_e32 v249, v254, v249\n\t"
      "s_waitcnt vmcnt(5)\n\t"
      "v_mfma_f32_16x16x32_bf16 v[212:215], v[144:147], v[48:51], v[212:215]\n\t"
      "s_waitcnt lgkmcnt(4)\n\t"
      "v_mfma_f32_16x16x32_bf16 v[216:219], v[144:147], v[52:55], v[216:219]\n\t"
      "v_add_f32_e32 v228, v228, v230\n\t"
      "v_add_f32_e32 v232, v232, v234\n\t"
      "v_mfma_f32_16x16x32_bf16 v[220:223], v[144:147], v[56:59], v[220:223]\n\t"
      "v_add_f32_e32 v229, v229, v231\n\t"
      "s_add_u32 s58, s76, s42\n\t"
      "s_addc_u32 s59, s77, 0\n\t"
      "global_load_dwordx4 v[168:171], v244, s[58:59] sc1\n\t"
      "v_mfma_f32_16x16x32_bf16 v[224:227], v[144:147], v[60:63], v[224:227]\n\t"
      "v_add_f32_e32 v233, v233, v235\n\t"
      "s_waitcnt lgkmcnt(0)\n\t"
      "s_waitcnt vmcnt(5)\n\t"
      "v_mfma_f32_16x16x32_bf16 v[212:215], v[148:151], v[64:67], v[212:215]\n\t"
      "v_add_f32_e32 v236, v236, v238\n\t"
      "v_mfma_f32_16x16x32_bf16 v[216:219], v[148:151], v[68:71], v[216:219]\n\t"
      "v_add_f32_e32 v240, v240, v242\n\t"
      "v_add_f32_e32 v237, v237, v239\n\t"
      "v_mfma_f32_16x16x32_bf16 v[220:223], v[148:151], v[72:75], v[220:223]\n\t"
      "v_add_f32_e32 v241, v241, v243\n\t"
      "s_add_u32 s58, s76, s43\n\t"
      "s_addc_u32 s59, s77, 0\n\t"
      "global_load_dwordx4 v[172:175], v244, s[58:59] sc1\n\t"
      "v_mfma_f32_16x16x32_bf16 v[224:227], v[148:151], v[76:79], v[224:227]\n\t"
      "v_add_f32_e32 v228, v228, v232\n\t"
      "v_add_f32_e32 v236, v236, v240\n\t"
      "s_waitcnt vmcnt(5)\n\t"
      "v_mfma_f32_16x16x32_bf16 v[212:215], v[152:155], v[80:83], v[212:215]\n\t"
      "v_add_f32_e32 v229, v229, v233\n\t"
      "v_mfma_f32_16x16x32_bf16 v[216:219], v[152:155], v[84:87], v[216:219]\n\t"
      "v_add_f32_e32 v237, v237, v241\n\t"
      "v_add_f32_e32 v228, v228, v248\n\t"
      "v_mfma_f32_16x16x32_bf16 v[220:223], v[152:155], v[88:91], v[220:223]\n\t"
      "v_add_f32_e32 v229, v229, v249\n\t"
      "s_add_u32 s58, s76, s44\n\t"
      "s_addc_u32 s59, s77, 0\n\t"
      "global_load_dwordx4 v[176:179], v244, s[58:59] sc1\n\t"
      "v_mfma_f32_16x16x32_bf16 v[224:227], v[152:155], v[92:95], v[224:227]\n\t"
      "v_add_f32_e32 v228, v228, v236\n\t"
      "v_add_f32_e32 v229, v229, v237\n\t"
      "s_waitcnt vmcnt(5)\n\t"
      "v_mfma_f32_16x16x32_bf16 v[212:215], v[156:159], v[96:99], v[212:215]\n\t"
      "v_cvt_pk_bf16_f32 v232, v228, v229\n\t"
      "v_mfma_f32_16x16x32_bf16 v[216:219], v[156:159], v[100:103], v[216:219]\n\t"
      "s_mov_b32 vcc_lo, 0xdddddddd\n\t"
      "s_mov_b32 vcc_hi, 0xdddddddd\n\t"
      "v_cndmask_b32_dpp v124, v232, v124, vcc quad_perm:[0,0,0,0] row_mask:0xf bank_mask:0xf\n\t"
      "v_cndmask_b32_dpp v125, v232, v125, vcc quad_perm:[1,1,1,1] row_mask:0xf bank_mask:0xf\n\t"
      "v_cndmask_b32_dpp v126, v232, v126, vcc quad_perm:[2,2,2,2] row_mask:0xf bank_mask:0xf\n\t"
      "v_cndmask_b32_dpp v127, v232, v127, vcc quad_perm:[3,3,3,3] row_mask:0xf bank_mask:0xf\n\t"
      "v_mfma_f32_16x16x32_bf16 v[220:223], v[156:159], v[104:107], v[220:223]\n\t"
      "s_add_u32 s58, s76, s45\n\t"
      "s_addc_u32 s59, s77, 0\n\t"
      "global_load_dwordx4 v[180:183], v244, s[58:59] sc1\n\t"
      "v_mfma_f32_16x16x32_bf16 v[224:227], v[156:159], v[108:111], v[224:227]\n\t"
      "v_add_f32_e32 v251, v212, v213\n\t"
      "v_add_f32_e32 v251, v251, v214\n\t"
      "v_cmp_u_f32_e32 vcc, v251, v215\n\t"
      "s_cbranch_vccnz LA45R_%=\n\t"
      "LA45B_%=:\n\t"
      "ds_write_b128 v245, v[212:215] offset:32768\n\t"
      "s_waitcnt vmcnt(5)\n\t"
      "v_mfma_f32_16x16x32_bf16 v[196:199], v[160:163], v[0:3], 0\n\t"
      "ds_write_b128 v245, v[216:219] offset:33792\n\t"
      "v_mfma_f32_16x16x32_bf16 v[200:203], v[160:163], v[4:7], 0\n\t"
      "ds_write_b128 v245, v[220:223] offset:34816\n\t"
      "v_mfma_f32_16x16x32_bf16 v[204:207], v[160:163], v[8:11], 0\n\t"
      "ds_write_b128 v245, v[224:227] offset:35840\n\t"
      "s_add_u32 s58, s76, s46\n\t"
      "s_addc_u32 s59, s77, 0\n\t"
      "global_load_dwordx4 v[184:187], v244, s[58:59] sc1\n\t"
      "v_mfma_f32_16x16x32_bf16 v[208:211], v[160:163], v[12:15], 0\n\t"
      "s_waitcnt vmcnt(5)\n\t"
      "v_mfma_f32_16x16x32_bf16 v[196:199], v[164:167], v[16:19], v[196:199]\n\t"
      "v_mfma_f32_16x16x32_bf16 v[200:203], v[164:167], v[20:23], v[200:203]\n\t"
      "v_mfma_f32_16x16x32_bf16 v[204:207], v[164:167], v[24:27], v[204:207]\n\t"
      "s_waitcnt lgkmcnt(0)\n\t"
      "s_barrier\n\t"
      "ds_read_b64 v[228:229], v246 offset:32768\n\t"
      "ds_read_b64 v[230:231], v246 offset:36864\n\t"
      "v_mfma_f32_16x16x32_bf16 v[208:211], v[164:167], v[28:31], v[208:211]\n\t"
      "ds_read_b64 v[232:233], v246 offset:40960\n\t"
      "ds_read_b64 v[234:235], v246 offset:45056\n\t"
      "s_waitcnt vmcnt(4)\n\t"
      "v_mfma_f32_16x16x32_bf16 v[196:199], v[168:171], v[32:35], v[196:199]\n\t"
      "ds_read_b64 v[236:237], v246 offset:49152\n\t"
      "ds_read_b64 v[238:239], v246 offset:53248\n\t"
      "v_mfma_f32_16x16x32_bf16 v[200:203], v[168:171], v[36:39], v[200:203]\n\t"
      "ds_read_b64 v[240:241], v246 offset:57344\n\t"
      "ds_read_b64 v[242:243], v246 offset:61440\n\t"
      "v_lshlrev_b32_e32 v248, 16, v117\n\t"
      "v_and_b32_e32 v249, 0xffff0000, v117\n\t"
      "v_mfma_f32_16x16x32_bf16 v[204:207], v[168:171], v[40:43], v[204:207]\n\t"
      "v_add_f32_e32 v248, v254, v248\n\t"
      "v_add_f32_e32 v249, v254, v249\n\t"
      "v_mfma_f32_16x16x32_bf16 v[208:211], v[168:171], v[44:47], v[208:211]\n\t"
      "s_waitcnt lgkmcnt(4)\n\t"
      "v_add_f32_e32 v228, v228, v230\n\t"
      "v_add_f32_e32 v232, v232, v234\n\t"
      "s_waitcnt vmcnt(3)\n\t"
      "v_mfma_f32_16x16x32_bf16 v[196:199], v[172:175], v[48:51], v[196:199]\n\t"
      "v_add_f32_e32 v229, v229, v231\n\t"
      "v_add_f32_e32 v233, v233, v235\n\t"
      "v_mfma_f32_16x16x32_bf16 v[200:203], v[172:175], v[52:55], v[200:203]\n\t"
      "s_waitcnt lgkmcnt(0)\n\t"
      "v_add_f32_e32 v236, v236, v238\n\t"
      "v_add_f32_e32 v240, v240, v242\n\t"
      "v_mfma_f32_16x16x32_bf16 v[204:207], v[172:175], v[56:59], v[204:207]\n\t"
      "v_add_f32_e32 v237, v237, v239\n\t"
      "v_add_f32_e32 v241, v241, v243\n\t"
      "v_mfma_f32_16x16x32_bf16 v[208:211], v[172:175], v[60:63], v[208:211]\n\t"
      "v_add_f32_e32 v228, v228, v232\n\t"
      "v_add_f32_e32 v236, v236, v240\n\t"
      "v_add_f32_e32 v229, v229, v233\n\t"
      "s_waitcnt vmcnt(2)\n\t"
      "v_mfma_f32_16x16x32_bf16 v[196:199], v[176:179], v[64:67], v[196:199]\n\t"
      "v_add_f32_e32 v237, v237, v241\n\t"
      "v_add_f32_e32 v228, v228, v248\n\t"
      "v_mfma_f32_16x16x32_bf16 v[200:203], v[176:179], v[68:71], v[200:203]\n\t"
      "v_add_f32_e32 v229, v229, v249\n\t"
      "v_add_f32_e32 v228, v228, v236\n\t"
      "v_add_f32_e32 v229, v229, v237\n\t"
      "v_mfma_f32_16x16x32_bf16 v[204:207], v[176:179], v[72:75], v[204:207]\n\t"
      "v_cvt_pk_bf16_f32 v232, v228, v229\n\t"
      "s_mov_b32 vcc_lo, 0xbbbbbbbb\n\t"
      "s_mov_b32 vcc_hi, 0xbbbbbbbb\n\t"
      "v_cndmask_b32_dpp v124, v232, v124, vcc quad_perm:[0,0,0,0] row_mask:0xf bank_mask:0xf\n\t"
      "v_cndmask_b32_dpp v125, v232, v125, vcc quad_perm:[1,1,1,1] row_mask:0xf bank_mask:0xf\n\t"
      "v_cndmask_b32_dpp v126, v232, v126, vcc quad_perm:[2,2,2,2] row_mask:0xf bank_mask:0xf\n\t"
      "v_cndmask_b32_dpp v127, v232, v127, vcc quad_perm:[3,3,3,3] row_mask:0xf bank_mask:0xf\n\t"
      "v_mfma_f32_16x16x32_bf16 v[208:211], v[176:179], v[76:79], v[208:211]\n\t"
      "s_waitcnt vmcnt(1)\n\t"
      "v_mfma_f32_16x16x32_bf16 v[196:199], v[180:183], v[80:83], v[196:199]\n\t"
      "v_mfma_f32_16x16x32_bf16 v[200:203], v[180:183], v[84:87], v[200:203]\n\t"
      "v_mfma_f32_16x16x32_bf16 v[204:207], v[180:183], v[88:91], v[204:207]\n\t"
      "v_mfma_f32_16x16x32_bf16 v[208:211], v[180:183], v[92:95], v[208:211]\n\t"
      "s_waitcnt vmcnt(0)\n\t"
      "v_mfma_f32_16x16x32_bf16 v[196:199], v[184:187], v[96:99], v[196:199]\n\t"
      "v_mfma_f32_16x16x32_bf16 v[200:203], v[184:187], v[100:103], v[200:203]\n\t"
      "v_mfma_f32_16x16x32_bf16 v[204:207], v[184:187], v[104:107], v[204:207]\n\t"
      "v_mfma_f32_16x16x32_bf16 v[208:211], v[184:187], v[108:111], v[208:211]\n\t"
      "s_nop 4\n\t"
      "v_add_f32_e32 v251, v196, v197\n\t"
      "v_add_f32_e32 v251, v251, v198\n\t"
      "v_cmp_u_f32_e32 vcc, v251, v199\n\t"
      "s_cbranch_vccnz LA46R_%=\n\t"
      "LA46B_%=:\n\t"
      "ds_write_b128 v245, v[196:199]\n\t"
      "s_mov_b64 exec, s[82:83]\n\t"
      "global_store_dwordx4 v247, v[120:123], s[18:19] sc1\n\t"
      "s_mov_b64 exec, -1\n\t"
      "ds_write_b128 v245, v[200:203] offset:1024\n\t"
      "ds_write_b128 v245, v[204:207] offset:2048\n\t"
      "ds_write_b128 v245, v[208:211] offset:3072\n\t"
      "s_waitcnt lgkmcnt(0)\n\t"
      "s_barrier\n\t"
      "ds_read_b64 v[228:229], v246\n\t"
      "ds_read_b64 v[230:231], v246 offset:4096\n\t"
      "ds_read_b64 v[232:233], v246 offset:8192\n\t"
      "ds_read_b64 v[234:235], v246 offset:12288\n\t"
      "ds_read_b64 v[236:237], v246 offset:16384\n\t"
      "ds_read_b64 v[238:239], v246 offset:20480\n\t"
      "ds_read_b64 v[240:241], v246 offset:24576\n\t"
      "ds_read_b64 v[242:243], v246 offset:28672\n\t"
      "v_lshlrev_b32_e32 v248, 16, v118\n\t"
      "v_and_b32_e32 v249, 0xffff0000, v118\n\t"
      "v_add_f32_e32 v248, v254, v248\n\t"
      "v_add_f32_e32 v249, v254, v249\n\t"
      "s_waitcnt lgkmcnt(4)\n\t"
      "v_add_f32_e32 v228, v228, v230\n\t"
      "v_add_f32_e32 v232, v232, v234\n\t"
      "v_add_f32_e32 v229, v229, v231\n\t"
      "v_add_f32_e32 v233, v233, v235\n\t"
      "s_waitcnt lgkmcnt(0)\n\t"
      "v_add_f32_e32 v236, v236, v238\n\t"
      "v_add_f32_e32 v240, v240, v242\n\t"
      "v_add_f32_e32 v237, v237, v239\n\t"
      "v_add_f32_e32 v241, v241, v243\n\t"
      "v_add_f32_e32 v228, v228, v232\n\t"
      "v_add_f32_e32 v236, v236, v240\n\t"
      "v_add_f32_e32 v229, v229, v233\n\t"
      "v_add_f32_e32 v237, v237, v241\n\t"
      "v_add_f32_e32 v228, v228, v248\n\t"
      "v_add_f32_e32 v229, v229, v249\n\t"
      "v_add_f32_e32 v228, v228, v236\n\t"
      "v_add_f32_e32 v229, v229, v237\n\t"
      "v_cvt_pk_bf16_f32 v232, v228, v229\n\t"
      "s_mov_b32 vcc_lo, 0x77777777\n\t"
      "s_mov_b32 vcc_hi, 0x77777777\n\t"
      "v_cndmask_b32_dpp v124, v232, v124, vcc quad_perm:[0,0,0,0] row_mask:0xf bank_mask:0xf\n\t"
      "v_cndmask_b32_dpp v125, v232, v125, vcc quad_perm:[1,1,1,1] row_mask:0xf bank_mask:0xf\n\t"
      "v_cndmask_b32_dpp v126, v232, v126, vcc quad_perm:[2,2,2,2] row_mask:0xf bank_mask:0xf\n\t"
      "v_cndmask_b32_dpp v127, v232, v127, vcc quad_perm:[3,3,3,3] row_mask:0xf bank_mask:0xf\n\t"
      "global_store_dwordx4 v247, v[124:127], s[62:63] sc1\n\t"
      "s_endpgm\n\t"
      "LO0R_%=:\n\t"
      "s_mov_b32 s80, 0\n\t"
      "LO0L_%=:\n\t"
      "s_add_u32 s80, s80, 1\n\t"
      "s_cmp_lt_u32 s80, 262144\n\t"
      "s_cbranch_scc0 LO0X_%=\n\t"
      "s_waitcnt vmcnt(0)\n\t"
      "s_sleep 1\n\t"
      "global_load_dwordx4 v[128:131], v244, s[42:43] offset:-4096 sc1\n\t"
      "global_load_dwordx4 v[132:135], v244, s[42:43] offset:-3072 sc1\n\t"
      "global_load_dwordx4 v[136:139], v244, s[42:43] offset:-2048 sc1\n\t"
      "global_load_dwordx4 v[140:143], v244, s[42:43] offset:-1024 sc1\n\t"
      "global_load_dwordx4 v[144:147], v244, s[42:43] sc1\n\t"
      "global_load_dwordx4 v[148:151], v244, s[42:43] offset:1024 sc1\n\t"
      "global_load_dwordx4 v[152:155], v244, s[42:43] offset:2048 sc1\n\t"
      "global_load_dwordx4 v[156:159], v244, s[42:43] offset:3072 sc1\n\t"
      "global_load_dwordx4 v[160:163], v244, s[44:45] offset:-4096 sc1\n\t"
      "global_load_dwordx4 v[164:167], v244, s[44:45] offset:-3072 sc1\n\t"
      "global_load_dwordx4 v[168:171], v244, s[44:45] offset:-2048 sc1\n\t"
      "global_load_dwordx4 v[172:175], v244, s[44:45] offset:-1024 sc1\n\t"
      "global_load_dwordx4 v[176:179], v244, s[44:45] sc1\n\t"
      "global_load_dwordx4 v[180:183], v244, s[44:45] offset:1024 sc1\n\t"
      "global_load_dwordx4 v[184:187], v244, s[44:45] offset:2048 sc1\n\t"
      "global_load_dwordx4 v[188:191], v244, s[44:45] offset:3072 sc1\n\t"
      "s_waitcnt vmcnt(0)\n\t"
      "v_max3_u32 v232, v128, v129, v130\n\t"
      "v_max3_u32 v232, v232, v131, v132\n\t"
      "v_max3_u32 v232, v232, v133, v134\n\t"
      "v_max_u32_e32 v232, v232, v135\n\t"
      "v_cmp_eq_u32_e32 vcc, -1, v232\n\t"
      "s_cbranch_vccnz LO0L_%=\n\t"
      "LO0X_%=:\n\t"
      "s_branch LO0B_%=\n\t"
      "LO2R_%=:\n\t"
      "s_mov_b32 s80, 0\n\t"
      "LO2L_%=:\n\t"
      "s_add_u32 s80, s80, 1\n\t"
      "s_cmp_lt_u32 s80, 262144\n\t"
      "s_cbranch_scc0 LO2X_%=\n\t"
      "s_waitcnt vmcnt(0)\n\t"
      "s_sleep 1\n\t"
      "global_load_dwordx4 v[136:139], v244, s[42:43] offset:-2048 sc1\n\t"
      "global_load_dwordx4 v[140:143], v244, s[42:43] offset:-1024 sc1\n\t"
      "global_load_dwordx4 v[144:147], v244, s[42:43] sc1\n\t"
      "global_load_dwordx4 v[148:151], v244, s[42:43] offset:1024 sc1\n\t"
      "global_load_dwordx4 v[152:155], v244, s[42:43] offset:2048 sc1\n\t"
      "global_load_dwordx4 v[156:159], v244, s[42:43] offset:3072 sc1\n\t"
      "global_load_dwordx4 v[160:163], v244, s[44:45] offset:-4096 sc1\n\t"
      "global_load_dwordx4 v[164:167], v244, s[44:45] offset:-3072 sc1\n\t"
      "global_load_dwordx4 v[168:171], v244, s[44:45] offset:-2048 sc1\n\t"
      "global_load_dwordx4 v[172:175], v244, s[44:45] offset:-1024 sc1\n\t"
      "global_load_dwordx4 v[176:179], v244, s[44:45] sc1\n\t"
      "global_load_dwordx4 v[180:183], v244, s[44:45] offset:1024 sc1\n\t"
      "global_load_dwordx4 v[184:187], v244, s[44:45] offset:2048 sc1\n\t"
      "global_load_dwordx4 v[188:191], v244, s[44:45] offset:3072 sc1\n\t"
      "s_waitcnt vmcnt(0)\n\t"
      "v_max3_u32 v232, v136, v137, v138\n\t"
      "v_max3_u32 v232, v232, v139, v140\n\t"
      "v_max3_u32 v232, v232, v141, v142\n\t"
      "v_max_u32_e32 v232, v232, v143\n\t"
      "v_cmp_eq_u32_e32 vcc, -1, v232\n\t"
      "s_cbranch_vccnz LO2L_%=\n\t"
      "LO2X_%=:\n\t"
      "s_branch LO2B_%=\n\t"
      "LO4R_%=:\n\t"
      "s_mov_b32 s80, 0\n\t"
      "LO4L_%=:\n\t"
      "s_add_u32 s80, s80, 1\n\t"
      "s_cmp_lt_u32 s80, 262144\n\t"
      "s_cbranch_scc0 LO4X_%=\n\t"
      "s_waitcnt vmcnt(0)\n\t"
      "s_sleep 1\n\t"
      "global_load_dwordx4 v[144:147], v244, s[42:43] sc1\n\t"
      "global_load_dwordx4 v[148:151], v244, s[42:43] offset:1024 sc1\n\t"
      "global_load_dwordx4 v[152:155], v244, s[42:43] offset:2048 sc1\n\t"
      "global_load_dwordx4 v[156:159], v244, s[42:43] offset:3072 sc1\n\t"
      "global_load_dwordx4 v[160:163], v244, s[44:45] offset:-4096 sc1\n\t"
      "global_load_dwordx4 v[164:167], v244, s[44:45] offset:-3072 sc1\n\t"
      "global_load_dwordx4 v[168:171], v244, s[44:45] offset:-2048 sc1\n\t"
      "global_load_dwordx4 v[172:175], v244, s[44:45] offset:-1024 sc1\n\t"
      "global_load_dwordx4 v[176:179], v244, s[44:45] sc1\n\t"
      "global_load_dwordx4 v[180:183], v244, s[44:45] offset:1024 sc1\n\t"
      "global_load_dwordx4 v[184:187], v244, s[44:45] offset:2048 sc1\n\t"
      "global_load_dwordx4 v[188:191], v244, s[44:45] offset:3072 sc1\n\t"
      "s_waitcnt vmcnt(0)\n\t"
      "v_max3_u32 v232, v144, v145, v146\n\t"
      "v_max3_u32 v232, v232, v147, v148\n\t"
      "v_max3_u32 v232, v232, v149, v150\n\t"
      "v_max_u32_e32 v232, v232, v151\n\t"
      "v_cmp_eq_u32_e32 vcc, -1, v232\n\t"
      "s_cbranch_vccnz LO4L_%=\n\t"
      "LO4X_%=:\n\t"
      "s_branch LO4B_%=\n\t"
      "LO6R_%=:\n\t"
      "s_mov_b32 s80, 0\n\t"
      "LO6L_%=:\n\t"
      "s_add_u32 s80, s80, 1\n\t"
      "s_cmp_lt_u32 s80, 262144\n\t"
      "s_cbranch_scc0 LO6X_%=\n\t"
      "s_waitcnt vmcnt(0)\n\t"
      "s_sleep 1\n\t"
      "global_load_dwordx4 v[152:155], v244, s[42:43] offset:2048 sc1\n\t"
      "global_load_dwordx4 v[156:159], v244, s[42:43] offset:3072 sc1\n\t"
      "global_load_dwordx4 v[160:163], v244, s[44:45] offset:-4096 sc1\n\t"
      "global_load_dwordx4 v[164:167], v244, s[44:45] offset:-3072 sc1\n\t"
      "global_load_dwordx4 v[168:171], v244, s[44:45] offset:-2048 sc1\n\t"
      "global_load_dwordx4 v[172:175], v244, s[44:45] offset:-1024 sc1\n\t"
      "global_load_dwordx4 v[176:179], v244, s[44:45] sc1\n\t"
      "global_load_dwordx4 v[180:183], v244, s[44:45] offset:1024 sc1\n\t"
      "global_load_dwordx4 v[184:187], v244, s[44:45] offset:2048 sc1\n\t"
      "global_load_dwordx4 v[188:191], v244, s[44:45] offset:3072 sc1\n\t"
      "s_waitcnt vmcnt(0)\n\t"
      "v_max3_u32 v232, v152, v153, v154\n\t"
      "v_max3_u32 v232, v232, v155, v156\n\t"
      "v_max3_u32 v232, v232, v157, v158\n\t"
      "v_max_u32_e32 v232, v232, v159\n\t"
      "v_cmp_eq_u32_e32 vcc, -1, v232\n\t"
      "s_cbranch_vccnz LO6L_%=\n\t"
      "LO6X_%=:\n\t"
      "s_branch LO6B_%=\n\t"
      "LO8R_%=:\n\t"
      "s_mov_b32 s80, 0\n\t"
      "LO8L_%=:\n\t"
      "s_add_u32 s80, s80, 1\n\t"
      "s_cmp_lt_u32 s80, 262144\n\t"
      "s_cbranch_scc0 LO8X_%=\n\t"
      "s_waitcnt vmcnt(0)\n\t"
      "s_sleep 1\n\t"
      "global_load_dwordx4 v[160:163], v244, s[44:45] offset:-4096 sc1\n\t"
      "global_load_dwordx4 v[164:167], v244, s[44:45] offset:-3072 sc1\n\t"
      "global_load_dwordx4 v[168:171], v244, s[44:45] offset:-2048 sc1\n\t"
      "global_load_dwordx4 v[172:175], v244, s[44:45] offset:-1024 sc1\n\t"
      "global_load_dwordx4 v[176:179], v244, s[44:45] sc1\n\t"
      "global_load_dwordx4 v[180:183], v244, s[44:45] offset:1024 sc1\n\t"
      "global_load_dwordx4 v[184:187], v244, s[44:45] offset:2048 sc1\n\t"
      "global_load_dwordx4 v[188:191], v244, s[44:45] offset:3072 sc1\n\t"
      "s_waitcnt vmcnt(0)\n\t"
      "v_max3_u32 v232, v160, v161, v162\n\t"
      "v_max3_u32 v232, v232, v163, v164\n\t"
      "v_max3_u32 v232, v232, v165, v166\n\t"
      "v_max_u32_e32 v232, v232, v167\n\t"
      "v_cmp_eq_u32_e32 vcc, -1, v232\n\t"
      "s_cbranch_vccnz LO8L_%=\n\t"
      "LO8X_%=:\n\t"
      "s_branch LO8B_%=\n\t"
      "LO10R_%=:\n\t"
      "s_mov_b32 s80, 0\n\t"
      "LO10L_%=:\n\t"
      "s_add_u32 s80, s80, 1\n\t"
      "s_cmp_lt_u32 s80, 262144\n\t"
      "s_cbranch_scc0 LO10X_%=\n\t"
      "s_waitcnt vmcnt(0)\n\t"
      "s_sleep 1\n\t"
      "global_load_dwordx4 v[168:171], v244, s[44:45] offset:-2048 sc1\n\t"
      "global_load_dwordx4 v[172:175], v244, s[44:45] offset:-1024 sc1\n\t"
      "global_load_dwordx4 v[176:179], v244, s[44:45] sc1\n\t"
      "global_load_dwordx4 v[180:183], v244, s[44:45] offset:1024 sc1\n\t"
      "global_load_dwordx4 v[184:187], v244, s[44:45] offset:2048 sc1\n\t"
      "global_load_dwordx4 v[188:191], v244, s[44:45] offset:3072 sc1\n\t"
      "s_waitcnt vmcnt(0)\n\t"
      "v_max3_u32 v232, v168, v169, v170\n\t"
      "v_max3_u32 v232, v232, v171, v172\n\t"
      "v_max3_u32 v232, v232, v173, v174\n\t"
      "v_max_u32_e32 v232, v232, v175\n\t"
      "v_cmp_eq_u32_e32 vcc, -1, v232\n\t"
      "s_cbranch_vccnz LO10L_%=\n\t"
      "LO10X_%=:\n\t"
      "s_branch LO10B_%=\n\t"
      "LO12R_%=:\n\t"
      "s_mov_b32 s80, 0\n\t"
      "LO12L_%=:\n\t"
      "s_add_u32 s80, s80, 1\n\t"
      "s_cmp_lt_u32 s80, 262144\n\t"
      "s_cbranch_scc0 LO12X_%=\n\t"
      "s_waitcnt vmcnt(0)\n\t"
      "s_sleep 1\n\t"
      "global_load_dwordx4 v[176:179], v244, s[44:45] sc1\n\t"
      "global_load_dwordx4 v[180:183], v244, s[44:45] offset:1024 sc1\n\t"
      "global_load_dwordx4 v[184:187], v244, s[44:45] offset:2048 sc1\n\t"
      "global_load_dwordx4 v[188:191], v244, s[44:45] offset:3072 sc1\n\t"
      "s_waitcnt vmcnt(0)\n\t"
      "v_max3_u32 v232, v176, v177, v178\n\t"
      "v_max3_u32 v232, v232, v179, v180\n\t"
      "v_max3_u32 v232, v232, v181, v182\n\t"
      "v_max_u32_e32 v232, v232, v183\n\t"
      "v_cmp_eq_u32_e32 vcc, -1, v232\n\t"
      "s_cbranch_vccnz LO12L_%=\n\t"
      "LO12X_%=:\n\t"
      "s_branch LO12B_%=\n\t"
      "LO14R_%=:\n\t"
      "s_mov_b32 s80, 0\n\t"
      "LO14L_%=:\n\t"
      "s_add_u32 s80, s80, 1\n\t"
      "s_cmp_lt_u32 s80, 262144\n\t"
      "s_cbranch_scc0 LO14X_%=\n\t"
      "s_waitcnt vmcnt(0)\n\t"
      "s_sleep 1\n\t"
      "global_load_dwordx4 v[184:187], v244, s[44:45] offset:2048 sc1\n\t"
      "global_load_dwordx4 v[188:191], v244, s[44:45] offset:3072 sc1\n\t"
      "s_waitcnt vmcnt(0)\n\t"
      "v_max3_u32 v232, v184, v185, v186\n\t"
      "v_max3_u32 v232, v232, v187, v188\n\t"
      "v_max3_u32 v232, v232, v189, v190\n\t"
      "v_max_u32_e32 v232, v232, v191\n\t"
      "v_cmp_eq_u32_e32 vcc, -1, v232\n\t"
      "s_cbranch_vccnz LO14L_%=\n\t"
      "LO14X_%=:\n\t"
      "s_branch LO14B_%=\n\t"
      "LGX_%=:\n\t"
      "v_max3_u32 v232, v128, v129, v130\n\t"
      "v_max3_u32 v232, v232, v131, v132\n\t"
      "v_max3_u32 v232, v232, v133, v134\n\t"
      "v_max3_u32 v232, v232, v135, v136\n\t"
      "v_max3_u32 v232, v232, v137, v138\n\t"
      "v_max3_u32 v232, v232, v139, v140\n\t"
      "v_max3_u32 v232, v232, v141, v142\n\t"
      "v_max3_u32 v232, v232, v143, v144\n\t"
      "v_max3_u32 v232, v232, v145, v146\n\t"
      "v_max3_u32 v232, v232, v147, v148\n\t"
      "v_max3_u32 v232, v232, v149, v150\n\t"
      "v_max3_u32 v232, v232, v151, v152\n\t"
      "v_max3_u32 v232, v232, v153, v154\n\t"
      "v_max3_u32 v232, v232, v155, v156\n\t"
      "v_max3_u32 v232, v232, v157, v158\n\t"
      "v_max3_u32 v232, v232, v159, v160\n\t"
      "v_max3_u32 v232, v232, v161, v162\n\t"
      "v_max3_u32 v232, v232, v163, v164\n\t"
      "v_max3_u32 v232, v232, v165, v166\n\t"
      "v_max3_u32 v232, v232, v167, v168\n\t"
      "v_max3_u32 v232, v232, v169, v170\n\t"
      "v_max3_u32 v232, v232, v171, v172\n\t"
      "v_max3_u32 v232, v232, v173, v174\n\t"
      "v_max3_u32 v232, v232, v175, v176\n\t"
      "v_max3_u32 v232, v232, v177, v178\n\t"
      "v_max3_u32 v232, v232, v179, v180\n\t"
      "v_max3_u32 v232, v232, v181, v182\n\t"
      "v_max3_u32 v232, v232, v183, v184\n\t"
      "v_max3_u32 v232, v232, v185, v186\n\t"
      "v_max3_u32 v232, v232, v187, v188\n\t"
      "v_max3_u32 v232, v232, v189, v190\n\t"
      "v_max_u32_e32 v232, v232, v191\n\t"
      "v_cmp_eq_u32_e32 vcc, -1, v232\n\t"
      "s_cbranch_vccz LGB_%=\n\t"
      "LGYR_%=:\n\t"
      "s_mov_b32 s80, 0\n\t"
      "LGYL_%=:\n\t"
      "s_add_u32 s80, s80, 1\n\t"
      "s_cmp_lt_u32 s80, 262144\n\t"
      "s_cbranch_scc0 LGYX_%=\n\t"
      "s_sleep 1\n\t"
      "global_load_dwordx4 v[128:131], v244, s[42:43] offset:-4096 sc1\n\t"
      "global_load_dwordx4 v[132:135], v244, s[42:43] offset:-3072 sc1\n\t"
      "global_load_dwordx4 v[136:139], v244, s[42:43] offset:-2048 sc1\n\t"
      "global_load_dwordx4 v[140:143], v244, s[42:43] offset:-1024 sc1\n\t"
      "global_load_dwordx4 v[144:147], v244, s[42:43] sc1\n\t"
      "global_load_dwordx4 v[148:151], v244, s[42:43] offset:1024 sc1\n\t"
      "global_load_dwordx4 v[152:155], v244, s[42:43] offset:2048 sc1\n\t"
      "global_load_dwordx4 v[156:159], v244, s[42:43] offset:3072 sc1\n\t"
      "global_load_dwordx4 v[160:163], v244, s[44:45] offset:-4096 sc1\n\t"
      "global_load_dwordx4 v[164:167], v244, s[44:45] offset:-3072 sc1\n\t"
      "global_load_dwordx4 v[168:171], v244, s[44:45] offset:-2048 sc1\n\t"
      "global_load_dwordx4 v[172:175], v244, s[44:45] offset:-1024 sc1\n\t"
      "global_load_dwordx4 v[176:179], v244, s[44:45] sc1\n\t"
      "global_load_dwordx4 v[180:183], v244, s[44:45] offset:1024 sc1\n\t"
      "global_load_dwordx4 v[184:187], v244, s[44:45] offset:2048 sc1\n\t"
      "global_load_dwordx4 v[188:191], v244, s[44:45] offset:3072 sc1\n\t"
      "s_waitcnt vmcnt(0)\n\t"
      "v_max3_u32 v232, v128, v129, v130\n\t"
      "v_max3_u32 v232, v232, v131, v132\n\t"
      "v_max3_u32 v232, v232, v133, v134\n\t"
      "v_max3_u32 v232, v232, v135, v136\n\t"
      "v_max3_u32 v232, v232, v137, v138\n\t"
      "v_max3_u32 v232, v232, v139, v140\n\t"
      "v_max3_u32 v232, v232, v141, v142\n\t"
      "v_max3_u32 v232, v232, v143, v144\n\t"
      "v_max3_u32 v232, v232, v145, v146\n\t"
      "v_max3_u32 v232, v232, v147, v148\n\t"
      "v_max3_u32 v232, v232, v149, v150\n\t"
      "v_max3_u32 v232, v232, v151, v152\n\t"
      "v_max3_u32 v232, v232, v153, v154\n\t"
      "v_max3_u32 v232, v232, v155, v156\n\t"
      "v_max3_u32 v232, v232, v157, v158\n\t"
      "v_max3_u32 v232, v232, v159, v160\n\t"
      "v_max3_u32 v232, v232, v161, v162\n\t"
      "v_max3_u32 v232, v232, v163, v164\n\t"
      "v_max3_u32 v232, v232, v165, v166\n\t"
      "v_max3_u32 v232, v232, v167, v168\n\t"
      "v_max3_u32 v232, v232, v169, v170\n\t"
      "v_max3_u32 v232, v232, v171, v172\n\t"
      "v_max3_u32 v232, v232, v173, v174\n\t"
      "v_max3_u32 v232, v232, v175, v176\n\t"
      "v_max3_u32 v232, v232, v177, v178\n\t"
      "v_max3_u32 v232, v232, v179, v180\n\t"
      "v_max3_u32 v232, v232, v181, v182\n\t"
      "v_max3_u32 v232, v232, v183, v184\n\t"
      "v_max3_u32 v232, v232, v185, v186\n\t"
      "v_max3_u32 v232, v232, v187, v188\n\t"
      "v_max3_u32 v232, v232, v189, v190\n\t"
      "v_max_u32_e32 v232, v232, v191\n\t"
      "v_cmp_eq_u32_e32 vcc, -1, v232\n\t"
      "s_cbranch_vccnz LGYL_%=\n\t"
      "LGYX_%=:\n\t"
      "s_cbranch_vccnz LGB_%=\n\t"
      "ds_read_b64_tr_b16 v[0:1], v216\n\t"
      "ds_read_b64_tr_b16 v[2:3], v220\n\t"
      "ds_read_b64_tr_b16 v[4:5], v217\n\t"
      "ds_read_b64_tr_b16 v[6:7], v221\n\t"
      "ds_read_b64_tr_b16 v[8:9], v218\n\t"
      "ds_read_b64_tr_b16 v[10:11], v222\n\t"
      "ds_read_b64_tr_b16 v[12:13], v219\n\t"
      "ds_read_b64_tr_b16 v[14:15], v223\n\t"
      "ds_read_b64_tr_b16 v[16:17], v218 offset:3584\n\t"
      "ds_read_b64_tr_b16 v[18:19], v222 offset:3584\n\t"
      "ds_read_b64_tr_b16 v[20:21], v219 offset:3584\n\t"
      "ds_read_b64_tr_b16 v[22:23], v223 offset:3584\n\t"
      "ds_read_b64_tr_b16 v[24:25], v216 offset:3584\n\t"
      "ds_read_b64_tr_b16 v[26:27], v220 offset:3584\n\t"
      "ds_read_b64_tr_b16 v[28:29], v217 offset:3584\n\t"
      "ds_read_b64_tr_b16 v[30:31], v221 offset:3584\n\t"
      "ds_read_b64_tr_b16 v[32:33], v216 offset:7168\n\t"
      "ds_read_b64_tr_b16 v[34:35], v220 offset:7168\n\t"
      "ds_read_b64_tr_b16 v[36:37], v217 offset:7168\n\t"
      "ds_read_b64_tr_b16 v[38:39], v221 offset:7168\n\t"
      "ds_read_b64_tr_b16 v[40:41], v218 offset:7168\n\t"
      "ds_read_b64_tr_b16 v[42:43], v222 offset:7168\n\t"
      "ds_read_b64_tr_b16 v[44:45], v219 offset:7168\n\t"
      "ds_read_b64_tr_b16 v[46:47], v223 offset:7168\n\t"
      "ds_read_b64_tr_b16 v[48:49], v218 offset:10752\n\t"
      "ds_read_b64_tr_b16 v[50:51], v222 offset:10752\n\t"
      "ds_read_b64_tr_b16 v[52:53], v219 offset:10752\n\t"
      "ds_read_b64_tr_b16 v[54:55], v223 offset:10752\n\t"
      "ds_read_b64_tr_b16 v[56:57], v216 offset:10752\n\t"
      "ds_read_b64_tr_b16 v[58:59], v220 offset:10752\n\t"
      "ds_read_b64_tr_b16 v[60:61], v217 offset:10752\n\t"
      "ds_read_b64_tr_b16 v[62:63], v221 offset:10752\n\t"
      "ds_read_b64_tr_b16 v[64:65], v216 offset:14336\n\t"
      "ds_read_b64_tr_b16 v[66:67], v220 offset:14336\n\t"
      "ds_read_b64_tr_b16 v[68:69], v217 offset:14336\n\t"
      "ds_read_b64_tr_b16 v[70:71], v221 offset:14336\n\t"
      "ds_read_b64_tr_b16 v[72:73], v218 offset:14336\n\t"
      "ds_read_b64_tr_b16 v[74:75], v222 offset:14336\n\t"
      "ds_read_b64_tr_b16 v[76:77], v219 offset:14336\n\t"
      "ds_read_b64_tr_b16 v[78:79], v223 offset:14336\n\t"
      "ds_read_b64_tr_b16 v[80:81], v218 offset:17920\n\t"
      "ds_read_b64_tr_b16 v[82:83], v222 offset:17920\n\t"
      "ds_read_b64_tr_b16 v[84:85], v219 offset:17920\n\t"
      "ds_read_b64_tr_b16 v[86:87], v223 offset:17920\n\t"
      "ds_read_b64_tr_b16 v[88:89], v216 offset:17920\n\t"
      "ds_read_b64_tr_b16 v[90:91], v220 offset:17920\n\t"
      "ds_read_b64_tr_b16 v[92:93], v217 offset:17920\n\t"
      "ds_read_b64_tr_b16 v[94:95], v221 offset:17920\n\t"
      "ds_read_b64_tr_b16 v[96:97], v216 offset:21504\n\t"
      "ds_read_b64_tr_b16 v[98:99], v220 offset:21504\n\t"
      "ds_read_b64_tr_b16 v[100:101], v217 offset:21504\n\t"
      "ds_read_b64_tr_b16 v[102:103], v221 offset:21504\n\t"
      "ds_read_b64_tr_b16 v[104:105], v218 offset:21504\n\t"
      "ds_read_b64_tr_b16 v[106:107], v222 offset:21504\n\t"
      "ds_read_b64_tr_b16 v[108:109], v219 offset:21504\n\t"
      "ds_read_b64_tr_b16 v[110:111], v223 offset:21504\n\t"
      "ds_read_b64_tr_b16 v[112:113], v218 offset:25088\n\t"
      "ds_read_b64_tr_b16 v[114:115], v222 offset:25088\n\t"
      "ds_read_b64_tr_b16 v[116:117], v219 offset:25088\n\t"
      "ds_read_b64_tr_b16 v[118:119], v223 offset:25088\n\t"
      "ds_read_b64_tr_b16 v[120:121], v216 offset:25088\n\t"
      "ds_read_b64_tr_b16 v[122:123], v220 offset:25088\n\t"
      "ds_read_b64_tr_b16 v[124:125], v217 offset:25088\n\t"
      "ds_read_b64_tr_b16 v[126:127], v221 offset:25088\n\t"
      "v_lshlrev_b32_e32 v192, 16, v208\n\t"
      "v_and_b32_e32 v193, 0xffff0000, v208\n\t"
      "v_lshlrev_b32_e32 v194, 16, v209\n\t"
      "v_and_b32_e32 v195, 0xffff0000, v209\n\t"
      "v_lshlrev_b32_e32 v196, 16, v210\n\t"
      "v_and_b32_e32 v197, 0xffff0000, v210\n\t"
      "v_lshlrev_b32_e32 v198, 16, v211\n\t"
      "v_and_b32_e32 v199, 0xffff0000, v211\n\t"
      "v_lshlrev_b32_e32 v200, 16, v212\n\t"
      "v_and_b32_e32 v201, 0xffff0000, v212\n\t"
      "v_lshlrev_b32_e32 v202, 16, v213\n\t"
      "v_and_b32_e32 v203, 0xffff0000, v213\n\t"
      "v_lshlrev_b32_e32 v204, 16, v214\n\t"
      "v_and_b32_e32 v205, 0xffff0000, v214\n\t"
      "v_lshlrev_b32_e32 v206, 16, v215\n\t"
      "v_and_b32_e32 v207, 0xffff0000, v215\n\t"
      "s_waitcnt lgkmcnt(0)\n\t"
      "s_branch LGM_%=\n\t"
      "LSR_%=:\n\t"
      "s_mov_b32 s80, 0\n\t"
      "LSL_%=:\n\t"
      "s_add_u32 s80, s80, 1\n\t"
      "s_cmp_lt_u32 s80, 262144\n\t"
      "s_cbranch_scc0 LSX_%=\n\t"
      "s_sleep 1\n\t"
      "global_load_dwordx2 v[148:149], v231, s[46:47] sc1\n\t"
      "global_load_dwordx2 v[150:151], v231, s[46:47] offset:384 sc1\n\t"
      "s_waitcnt vmcnt(0)\n\t"
      "v_max3_u32 v232, v148, v149, v150\n\t"
      "v_max_u32_e32 v232, v232, v151\n\t"
      "v_cmp_eq_u32_e32 vcc, -1, v232\n\t"
      "s_cbranch_vccnz LSL_%=\n\t"
      "LSX_%=:\n\t"
      "s_branch LSB_%=\n\t"
      "LF1R_%=:\n\t"
      "s_mov_b32 s80, 0\n\t"
      "LF1L_%=:\n\t"
      "s_add_u32 s80, s80, 1\n\t"
      "s_cmp_lt_u32 s80, 262144\n\t"
      "s_cbranch_scc0 LF1X_%=\n\t"
      "s_sleep 2\n\t"
      "global_load_dwordx2 v[138:139], v136, s[60:61] sc1\n\t"
      "s_waitcnt vmcnt(0)\n\t"
      "v_cmp_ne_u64_e32 vcc, s[22:23], v[138:139]\n\t"
      "s_cbranch_vccnz LF1L_%=\n\t"
      "LF1X_%=:\n\t"
      "s_branch LF1B_%=\n\t"
      "LF4R_%=:\n\t"
      "s_mov_b32 s80, 0\n\t"
      "LF4L_%=:\n\t"
      "s_add_u32 s80, s80, 1\n\t"
      "s_cmp_lt_u32 s80, 262144\n\t"
      "s_cbranch_scc0 LF4X_%=\n\t"
      "s_sleep 2\n\t"
      "global_load_dwordx2 v[138:139], v137, s[60:61] sc1\n\t"
      "s_waitcnt vmcnt(0)\n\t"
      "v_cmp_ne_u64_e32 vcc, s[22:23], v[138:139]\n\t"
      "s_cbranch_vccnz LF4L_%=\n\t"
      "LF4X_%=:\n\t"
      "s_branch LF4B_%=\n\t"
      "L4_%=:\n\t"
      "s_waitcnt vmcnt(0)\n\t"
      "s_barrier\n\t"
      "s_barrier\n\t"
      "s_lshl2_add_u32 s72, s31, s24\n\t"
      "s_sub_u32 s72, s72, 56\n\t"
      "s_mov_b32 m0, 64512\n\t"
      "s_cmp_ge_u32 s72, 64\n\t"
      "s_cselect_b32 s73, 8, 0\n\t"
      "s_sub_u32 s73, s72, s73\n\t"
      "s_and_b32 s73, s73, 0x38\n\t"
      "s_add_u32 s72, s72, s73\n\t"
      "s_add_u32 s72, s72, 8\n\t"
      "s_mul_i32 s72, s72, 57344\n\t"
      "s_and_b32 s73, s2, 7\n\t"
      "s_mul_i32 s73, s73, 448\n\t"
      "s_and_b32 s73, s73, 0xffffff80\n\t"
      "s_add_u32 s72, s72, s73\n\t"
      "v_bfe_u32 v141, v192, 2, 4\n\t"
      "v_mul_u32_u24_e32 v141, 3584, v141\n\t"
      "v_lshl_add_u32 v141, v196, 7, v141\n\t"
      "v_add_u32_e32 v141, s72, v141\n\t"
      "global_load_lds_dword v141, s[10:11]\n\t"
      "s_barrier\n\t"
      "s_cmp_lg_u32 s24, 3\n\t"
      "s_cbranch_scc1 SK12_%=\n\t"
      "global_load_dwordx2 v[138:139], v137, s[60:61] sc1\n\t"
      "SK12_%=:\n\t"
      "s_cmp_lg_u32 s24, 3\n\t"
      "s_cbranch_scc1 LF4B_%=\n\t"
      "s_waitcnt vmcnt(0)\n\t"
      "v_cmp_ne_u64_e32 vcc, s[22:23], v[138:139]\n\t"
      "s_cbranch_vccnz LF4R_%=\n\t"
      "LF4B_%=:\n\t"
      "s_barrier\n\t"
      "s_waitcnt vmcnt(0)\n\t"
      "s_barrier\n\t"
      "s_branch G4_%=\n\t"
      "LF2R_%=:\n\t"
      "s_mov_b32 s80, 0\n\t"
      "LF2L_%=:\n\t"
      "s_add_u32 s80, s80, 1\n\t"
      "s_cmp_lt_u32 s80, 262144\n\t"
      "s_cbranch_scc0 LF2X_%=\n\t"
      "s_sleep 2\n\t"
      "global_load_dwordx2 v[138:139], v137, s[60:61] sc1\n\t"
      "s_waitcnt vmcnt(0)\n\t"
      "v_cmp_ne_u64_e32 vcc, s[22:23], v[138:139]\n\t"
      "s_cbranch_vccnz LF2L_%=\n\t"
      "LF2X_%=:\n\t"
      "s_branch LF2B_%=\n\t"
      "LF3R_%=:\n\t"
      "s_mov_b32 s80, 0\n\t"
      "LF3L_%=:\n\t"
      "s_add_u32 s80, s80, 1\n\t"
      "s_cmp_lt_u32 s80, 262144\n\t"
      "s_cbranch_scc0 LF3X_%=\n\t"
      "s_sleep 2\n\t"
      "global_load_dwordx2 v[138:139], v143, s[60:61] sc1\n\t"
      "s_waitcnt vmcnt(0)\n\t"
      "v_cmp_ne_u64_e32 vcc, s[22:23], v[138:139]\n\t"
      "s_cbranch_vccnz LF3L_%=\n\t"
      "LF3X_%=:\n\t"
      "s_branch LF3B_%=\n\t"
      "LF70R_%=:\n\t"
      "s_mov_b32 s80, 0\n\t"
      "LF70L_%=:\n\t"
      "s_add_u32 s80, s80, 1\n\t"
      "s_cmp_lt_u32 s80, 262144\n\t"
      "s_cbranch_scc0 LF70X_%=\n\t"
      "s_sleep 4\n\t"
      "global_load_dwordx2 v[138:139], v136, s[60:61] sc1\n\t"
      "s_waitcnt vmcnt(0)\n\t"
      "v_cmp_ne_u64_e32 vcc, s[22:23], v[138:139]\n\t"
      "s_cbranch_vccnz LF70L_%=\n\t"
      "LF70X_%=:\n\t"
      "s_branch LF70B_%=\n\t"
      "LF71R_%=:\n\t"
      "s_mov_b32 s80, 0\n\t"
      "LF71L_%=:\n\t"
      "s_add_u32 s80, s80, 1\n\t"
      "s_cmp_lt_u32 s80, 262144\n\t"
      "s_cbranch_scc0 LF71X_%=\n\t"
      "s_sleep 4\n\t"
      "global_load_dwordx2 v[138:139], v136, s[60:61] sc1\n\t"
      "s_waitcnt vmcnt(0)\n\t"
      "v_cmp_ne_u64_e32 vcc, s[22:23], v[138:139]\n\t"
      "s_cbranch_vccnz LF71L_%=\n\t"
      "LF71X_%=:\n\t"
      "s_branch LF71B_%=\n\t"
      "SPARE_%=:\n\t"
      "v_mov_b32_e32 v132, -1\n\t"
      "v_mov_b32_e32 v133, -1\n\t"
      "v_mov_b32_e32 v134, -1\n\t"
      "v_mov_b32_e32 v135, -1\n\t"
      "s_and_b32 s34, s25, 3\n\t"
      "s_lshl_b32 s34, s34, 15\n\t"
      "s_mul_i32 s35, s24, 917504\n\t"
      "s_add_u32 s34, s34, s35\n\t"
      "s_and_b32 s35, s2, 7\n\t"
      "s_lshl_b32 s35, s35, 9\n\t"
      "s_add_u32 s34, s34, s35\n\t"
      "v_and_b32_e32 v141, 0x1c0, v244\n\t"
      "v_lshlrev_b32_e32 v141, 6, v141\n\t"
      "v_and_b32_e32 v142, 0x30, v244\n\t"
      "v_lshl_add_u32 v141, v142, 3, v141\n\t"
      "v_add_u32_e32 v141, s34, v141\n\t"
      "s_mov_b32 m0, 64512\n\t"
      "s_lshr_b32 s34, s24, 1\n\t"
      "s_lshl_b32 s34, s34, 5\n\t"
      "s_add_u32 s34, s34, s25\n\t"
      "s_and_b32 s35, s24, 1\n\t"
      "s_lshl1_add_u32 s34, s34, s35\n\t"
      "s_lshl_b32 s34, s34, 10\n\t"
      "s_add_u32 s34, s34, 8388608\n\t"
      "s_add_u32 s40, s20, s34\n\t"
      "s_addc_u32 s41, s21, 0\n\t"
      "global_store_dwordx4 v244, v[132:135], s[40:41] sc1\n\t"
      "s_add_u32 s40, s40, 262144\n\t"
      "s_addc_u32 s41, s41, 0\n\t"
      "global_store_dwordx4 v244, v[132:135], s[40:41] sc1\n\t"
      "s_add_u32 s40, s40, 262144\n\t"
      "s_addc_u32 s41, s41, 0\n\t"
      "global_store_dwordx4 v244, v[132:135], s[40:41] sc1\n\t"
      "s_add_u32 s40, s40, 262144\n\t"
      "s_addc_u32 s41, s41, 0\n\t"
      "global_store_dwordx4 v244, v[132:135], s[40:41] sc1\n\t"
      "s_add_u32 s40, s40, 262144\n\t"
      "s_addc_u32 s41, s41, 0\n\t"
      "global_store_dwordx4 v244, v[132:135], s[40:41] sc1\n\t"
      "s_add_u32 s40, s40, 262144\n\t"
      "s_addc_u32 s41, s41, 0\n\t"
      "global_store_dwordx4 v244, v[132:135], s[40:41] sc1\n\t"
      "s_add_u32 s40, s40, 262144\n\t"
      "s_addc_u32 s41, s41, 0\n\t"
      "global_store_dwordx4 v244, v[132:135], s[40:41] sc1\n\t"
      "s_add_u32 s40, s40, 262144\n\t"
      "s_addc_u32 s41, s41, 0\n\t"
      "s_lshl_b32 s34, s25, 7\n\t"
      "s_add_u32 s34, s34, 16384\n\t"
      "v_mov_b32_e32 v131, s34\n\t"
      "s_waitcnt vmcnt(0)\n\t"
      "s_barrier\n\t"
      "s_cmp_lg_u32 s24, 0\n\t"
      "s_cbranch_scc1 SP0_%=\n\t"
      "s_mov_b64 exec, 1\n\t"
      "global_store_dwordx2 v131, v[128:129], s[60:61] sc1\n\t"
      "s_mov_b64 exec, -1\n\t"
      "SP0_%=:\n\t"
      "s_and_b32 s34, s25, 3\n\t"
      "s_lshl3_add_u32 s34, s34, s24\n\t"
      "s_cmp_lg_u32 s34, 19\n\t"
      "s_cbranch_scc1 SQ0_%=\n\t"
      "v_and_b32_e32 v136, 0x1f0, v244\n\t"
      "v_lshlrev_b32_e32 v131, 6, v136\n\t"
      "v_lshlrev_b32_e32 v136, 3, v136\n\t"
      "v_add_u32_e32 v136, 16384, v136\n\t"
      "s_and_b32 s34, s2, 7\n\t"
      "s_lshl_b32 s34, s34, 7\n\t"
      "s_add_u32 s34, s34, 32768\n\t"
      "v_add_u32_e32 v131, s34, v131\n\t"
      "s_cmp_lg_u32 s24, 3\n\t"
      "s_cbranch_scc1 SK15_%=\n\t"
      "global_load_dwordx2 v[138:139], v136, s[60:61] sc1\n\t"
      "SK15_%=:\n\t"
      "s_cmp_lg_u32 s24, 3\n\t"
      "s_cbranch_scc1 LF70B_%=\n\t"
      "s_waitcnt vmcnt(0)\n\t"
      "v_cmp_ne_u64_e32 vcc, s[22:23], v[138:139]\n\t"
      "s_cbranch_vccnz LF70R_%=\n\t"
      "LF70B_%=:\n\t"
      "global_store_dwordx2 v131, v[128:129], s[60:61] sc1\n\t"
      "SQ0_%=:\n\t"
      "s_barrier\n\t"
      "s_lshl3_add_u32 s34, s25, s24\n\t"
      "s_mul_i32 s34, s34, 7168\n\t"
      "s_add_u32 s34, s34, 10227712\n\t"
      "s_add_u32 s40, s20, s34\n\t"
      "s_addc_u32 s41, s21, 0\n\t"
      "global_store_dwordx4 v244, v[132:135], s[40:41] offset:-4096 sc1\n\t"
      "global_store_dwordx4 v244, v[132:135], s[40:41] offset:-3072 sc1\n\t"
      "global_store_dwordx4 v244, v[132:135], s[40:41] offset:-2048 sc1\n\t"
      "global_store_dwordx4 v244, v[132:135], s[40:41] offset:-1024 sc1\n\t"
      "global_store_dwordx4 v244, v[132:135], s[40:41] sc1\n\t"
      "global_store_dwordx4 v244, v[132:135], s[40:41] offset:1024 sc1\n\t"
      "global_store_dwordx4 v244, v[132:135], s[40:41] offset:2048 sc1\n\t"
      "s_lshl_b32 s34, s25, 7\n\t"
      "s_add_u32 s34, s34, 24576\n\t"
      "v_mov_b32_e32 v131, s34\n\t"
      "s_waitcnt vmcnt(0)\n\t"
      "s_barrier\n\t"
      "s_cmp_lg_u32 s24, 0\n\t"
      "s_cbranch_scc1 SP1_%=\n\t"
      "s_mov_b64 exec, 1\n\t"
      "global_store_dwordx2 v131, v[128:129], s[60:61] sc1\n\t"
      "s_mov_b64 exec, -1\n\t"
      "SP1_%=:\n\t"
      "s_and_b32 s34, s25, 3\n\t"
      "s_lshl3_add_u32 s34, s34, s24\n\t"
      "s_cmp_lg_u32 s34, 27\n\t"
      "s_cbranch_scc1 SQ1_%=\n\t"
      "v_and_b32_e32 v136, 0x1f0, v244\n\t"
      "v_lshlrev_b32_e32 v131, 6, v136\n\t"
      "v_lshlrev_b32_e32 v136, 3, v136\n\t"
      "v_add_u32_e32 v136, 24576, v136\n\t"
      "s_and_b32 s34, s2, 7\n\t"
      "s_lshl_b32 s34, s34, 7\n\t"
      "s_add_u32 s34, s34, 65536\n\t"
      "v_add_u32_e32 v131, s34, v131\n\t"
      "s_cmp_lg_u32 s24, 3\n\t"
      "s_cbranch_scc1 SK16_%=\n\t"
      "global_load_dwordx2 v[138:139], v136, s[60:61] sc1\n\t"
      "SK16_%=:\n\t"
      "s_cmp_lg_u32 s24, 3\n\t"
      "s_cbranch_scc1 LF71B_%=\n\t"
      "s_waitcnt vmcnt(0)\n\t"
      "v_cmp_ne_u64_e32 vcc, s[22:23], v[138:139]\n\t"
      "s_cbranch_vccnz LF71R_%=\n\t"
      "LF71B_%=:\n\t"
      "global_store_dwordx2 v131, v[128:129], s[60:61] sc1\n\t"
      "SQ1_%=:\n\t"
      "s_barrier\n\t"
      "s_and_b32 s35, s25, 3\n\t"
      "s_lshl2_add_u32 s31, s26, s35\n\t"
      "s_and_b32 s35, s2, 7\n\t"
      "s_mul_i32 s34, s35, 14\n\t"
      "s_lshl4_add_u32 s35, s35, -14\n\t"
      "s_cmp_lt_u32 s31, 14\n\t"
      "s_cselect_b32 s34, s34, s35\n\t"
      "s_cselect_b32 s36, 1, 0\n\t"
      "s_or_b32 s27, s27, s36\n\t"
      "s_add_u32 s34, s34, s31\n\t"
      "s_lshr_b32 s29, s34, 2\n\t"
      "s_and_b32 s32, s34, 3\n\t"
      "s_mul_i32 s30, s32, 7\n\t"
      "v_and_b32_e32 v199, 60, v192\n\t"
      "v_and_b32_e32 v200, 24, v192\n\t"
      "v_lshlrev_b32_e32 v202, 3, v196\n\t"
      "v_lshl_or_b32 v202, v200, 2, v202\n\t"
      "v_lshl_or_b32 v202, v195, 7, v202\n\t"
      "v_lshl_or_b32 v202, v193, 10, v202\n\t"
      "v_add_u32_e32 v224, s39, v202\n\t"
      "v_xor_b32_e32 v225, 32, v224\n\t"
      "v_xor_b32_e32 v226, 64, v224\n\t"
      "v_xor_b32_e32 v227, 96, v224\n\t"
      "s_lshl_b32 s34, s24, 12\n\t"
      "s_add_u32 s34, s34, 98304\n\t"
      "v_add_u32_e32 v245, s34, v244\n\t"
      "v_lshlrev_b32_e32 v199, 2, v199\n\t"
      "s_lshl_b32 s34, s24, 8\n\t"
      "v_lshl_add_u32 v247, v192, 2, s34\n\t"
      "v_and_b32_e32 v200, 2, v192\n\t"
      "v_mul_u32_u24_e32 v202, 576, v200\n\t"
      "v_xor_b32_e32 v199, v202, v199\n\t"
      "v_and_b32_e32 v202, 1, v192\n\t"
      "v_lshl_or_b32 v199, v202, 3, v199\n\t"
      "s_and_b32 s35, s34, 0x400\n\t"
      "s_add_u32 s34, s34, s35\n\t"
      "s_add_u32 s34, s34, 98304\n\t"
      "v_add_u32_e32 v246, s34, v199\n\t"
      "v_lshlrev_b32_e32 v199, 2, v202\n\t"
      "v_lshl_or_b32 v199, v200, 4, v199\n\t"
      "s_min_u32 s34, s29, 27\n\t"
      "s_lshl_b32 s34, s34, 7\n\t"
      "s_lshr_b32 s35, s24, 2\n\t"
      "s_lshl_b32 s35, s35, 6\n\t"
      "s_add_u32 s34, s34, s35\n\t"
      "s_lshr_b32 s35, s39, 9\n\t"
      "s_add_u32 s34, s34, s35\n\t"
      "v_add_u32_e32 v248, s34, v199\n\t"
      "s_add_u32 s48, s20, 8388608\n\t"
      "s_addc_u32 s49, s21, 0\n\t"
      "s_cmp_ge_u32 s31, 30\n\t"
      "s_cbranch_scc1 BYE_%=\n\t"
      "s_setprio 0\n\t"
      "s_waitcnt vmcnt(0)\n\t"
      "s_barrier\n\t"
      "s_branch G4_%=\n\t"
      "BYE_%=:\n\t"
      "s_endpgm\n\t"
      "LA30R_%=:\n\t"
      "s_mov_b32 s80, 0\n\t"
      "LA30L_%=:\n\t"
      "s_add_u32 s80, s80, 1\n\t"
      "s_cmp_lt_u32 s80, 262144\n\t"
      "s_cbranch_scc0 LA30X_%=\n\t"
      "s_waitcnt vmcnt(0)\n\t"
      "s_sleep 1\n\t"
      "global_load_dwordx4 v[128:131], v250, s[64:65] offset:-4096 sc1\n\t"
      "global_load_dwordx4 v[132:135], v250, s[64:65] offset:-3072 sc1\n\t"
      "global_load_dwordx4 v[136:139], v250, s[64:65] offset:-2048 sc1\n\t"
      "global_load_dwordx4 v[140:143], v250, s[64:65] offset:-1024 sc1\n\t"
      "global_load_dwordx4 v[144:147], v250, s[64:65] sc1\n\t"
      "global_load_dwordx4 v[148:151], v250, s[64:65] offset:1024 sc1\n\t"
      "global_load_dwordx4 v[152:155], v250, s[64:65] offset:2048 sc1\n\t"
      "global_load_dwordx4 v[156:159], v250, s[64:65] offset:3072 sc1\n\t"
      "s_waitcnt vmcnt(0)\n\t"
      "v_max3_u32 v251, v128, v129, v130\n\t"
      "v_max3_u32 v251, v251, v131, v132\n\t"
      "v_max3_u32 v251, v251, v133, v134\n\t"
      "v_max3_u32 v251, v251, v135, v136\n\t"
      "v_max3_u32 v251, v251, v137, v138\n\t"
      "v_max3_u32 v251, v251, v139, v140\n\t"
      "v_max3_u32 v251, v251, v141, v142\n\t"
      "v_max3_u32 v251, v251, v143, v144\n\t"
      "v_max3_u32 v251, v251, v145, v146\n\t"
      "v_max3_u32 v251, v251, v147, v148\n\t"
      "v_max3_u32 v251, v251, v149, v150\n\t"
      "v_max3_u32 v251, v251, v151, v152\n\t"
      "v_max3_u32 v251, v251, v153, v154\n\t"
      "v_max3_u32 v251, v251, v155, v156\n\t"
      "v_max3_u32 v251, v251, v157, v158\n\t"
      "v_max_u32_e32 v251, v251, v159\n\t"
      "v_cmp_eq_u32_e32 vcc, -1, v251\n\t"
      "s_cbranch_vccnz LA30L_%=\n\t"
      "LA30X_%=:\n\t"
      "s_branch LA30B_%=\n\t"
      "LF6R_%=:\n\t"
      "s_mov_b32 s80, 0\n\t"
      "LF6L_%=:\n\t"
      "s_add_u32 s80, s80, 1\n\t"
      "s_cmp_lt_u32 s80, 262144\n\t"
      "s_cbranch_scc0 LF6X_%=\n\t"
      "s_sleep 2\n\t"
      "global_load_dwordx2 v[228:229], v230, s[60:61] sc1\n\t"
      "s_waitcnt vmcnt(0)\n\t"
      "v_cmp_ne_u64_e32 vcc, s[22:23], v[228:229]\n\t"
      "s_cbranch_vccnz LF6L_%=\n\t"
      "LF6X_%=:\n\t"
      "s_branch LF6B_%=\n\t"
      "LA31R_%=:\n\t"
      "s_mov_b32 s80, 0\n\t"
      "LA31L_%=:\n\t"
      "s_add_u32 s80, s80, 1\n\t"
      "s_cmp_lt_u32 s80, 262144\n\t"
      "s_cbranch_scc0 LA31X_%=\n\t"
      "s_waitcnt vmcnt(0)\n\t"
      "s_sleep 1\n\t"
      "global_load_dwordx4 v[160:163], v250, s[66:67] offset:-4096 sc1\n\t"
      "global_load_dwordx4 v[164:167], v250, s[66:67] offset:-3072 sc1\n\t"
      "global_load_dwordx4 v[168:171], v250, s[66:67] offset:-2048 sc1\n\t"
      "global_load_dwordx4 v[172:175], v250, s[66:67] offset:-1024 sc1\n\t"
      "global_load_dwordx4 v[176:179], v250, s[66:67] sc1\n\t"
      "global_load_dwordx4 v[180:183], v250, s[66:67] offset:1024 sc1\n\t"
      "global_load_dwordx4 v[184:187], v250, s[66:67] offset:2048 sc1\n\t"
      "global_load_dwordx4 v[188:191], v250, s[66:67] offset:3072 sc1\n\t"
      "s_waitcnt vmcnt(0)\n\t"
      "v_max3_u32 v251, v160, v161, v162\n\t"
      "v_max3_u32 v251, v251, v163, v164\n\t"
      "v_max3_u32 v251, v251, v165, v166\n\t"
      "v_max3_u32 v251, v251, v167, v168\n\t"
      "v_max3_u32 v251, v251, v169, v170\n\t"
      "v_max3_u32 v251, v251, v171, v172\n\t"
      "v_max3_u32 v251, v251, v173, v174\n\t"
      "v_max3_u32 v251, v251, v175, v176\n\t"
      "v_max3_u32 v251, v251, v177, v178\n\t"
      "v_max3_u32 v251, v251, v179, v180\n\t"
      "v_max3_u32 v251, v251, v181, v182\n\t"
      "v_max3_u32 v251, v251, v183, v184\n\t"
      "v_max3_u32 v251, v251, v185, v186\n\t"
      "v_max3_u32 v251, v251, v187, v188\n\t"
      "v_max3_u32 v251, v251, v189, v190\n\t"
      "v_max_u32_e32 v251, v251, v191\n\t"
      "v_cmp_eq_u32_e32 vcc, -1, v251\n\t"
      "s_cbranch_vccnz LA31L_%=\n\t"
      "LA31X_%=:\n\t"
      "v_mfma_f32_16x16x32_bf16 v[224:227], v[0:3], v[160:163], 0\n\t"
      "v_mfma_f32_16x16x32_bf16 v[224:227], v[16:19], v[164:167], v[224:227]\n\t"
      "v_mfma_f32_16x16x32_bf16 v[224:227], v[32:35], v[168:171], v[224:227]\n\t"
      "v_mfma_f32_16x16x32_bf16 v[224:227], v[48:51], v[172:175], v[224:227]\n\t"
      "v_mfma_f32_16x16x32_bf16 v[224:227], v[64:67], v[176:179], v[224:227]\n\t"
      "v_mfma_f32_16x16x32_bf16 v[224:227], v[80:83], v[180:183], v[224:227]\n\t"
      "v_mfma_f32_16x16x32_bf16 v[224:227], v[96:99], v[184:187], v[224:227]\n\t"
      "v_mfma_f32_16x16x32_bf16 v[224:227], v[112:115], v[188:191], v[224:227]\n\t"
      "v_mfma_f32_16x16x32_bf16 v[208:211], v[4:7], v[160:163], 0\n\t"
      "v_mfma_f32_16x16x32_bf16 v[208:211], v[20:23], v[164:167], v[208:211]\n\t"
      "v_mfma_f32_16x16x32_bf16 v[208:211], v[36:39], v[168:171], v[208:211]\n\t"
      "s_nop 7\n\t"
      "s_branch LA31B_%=\n\t"
      "LA32R_%=:\n\t"
      "s_mov_b32 s80, 0\n\t"
      "LA32L_%=:\n\t"
      "s_add_u32 s80, s80, 1\n\t"
      "s_cmp_lt_u32 s80, 262144\n\t"
      "s_cbranch_scc0 LA32X_%=\n\t"
      "s_waitcnt vmcnt(0)\n\t"
      "s_sleep 1\n\t"
      "global_load_dwordx4 v[192:195], v250, s[68:69] offset:-4096 sc1\n\t"
      "global_load_dwordx4 v[196:199], v250, s[68:69] offset:-3072 sc1\n\t"
      "global_load_dwordx4 v[200:203], v250, s[68:69] offset:-2048 sc1\n\t"
      "global_load_dwordx4 v[204:207], v250, s[68:69] offset:-1024 sc1\n\t"
      "global_load_dwordx4 v[128:131], v250, s[68:69] sc1\n\t"
      "global_load_dwordx4 v[132:135], v250, s[68:69] offset:1024 sc1\n\t"
      "global_load_dwordx4 v[136:139], v250, s[68:69] offset:2048 sc1\n\t"
      "global_load_dwordx4 v[140:143], v250, s[68:69] offset:3072 sc1\n\t"
      "s_waitcnt vmcnt(0)\n\t"
      "v_max3_u32 v251, v192, v193, v194\n\t"
      "v_max3_u32 v251, v251, v195, v196\n\t"
      "v_max3_u32 v251, v251, v197, v198\n\t"
      "v_max3_u32 v251, v251, v199, v200\n\t"
      "v_max3_u32 v251, v251, v201, v202\n\t"
      "v_max3_u32 v251, v251, v203, v204\n\t"
      "v_max3_u32 v251, v251, v205, v206\n\t"
      "v_max3_u32 v251, v251, v207, v128\n\t"
      "v_max3_u32 v251, v251, v129, v130\n\t"
      "v_max3_u32 v251, v251, v131, v132\n\t"
      "v_max3_u32 v251, v251, v133, v134\n\t"
      "v_max3_u32 v251, v251, v135, v136\n\t"
      "v_max3_u32 v251, v251, v137, v138\n\t"
      "v_max3_u32 v251, v251, v139, v140\n\t"
      "v_max3_u32 v251, v251, v141, v142\n\t"
      "v_max_u32_e32 v251, v251, v143\n\t"
      "v_cmp_eq_u32_e32 vcc, -1, v251\n\t"
      "s_cbranch_vccnz LA32L_%=\n\t"
      "LA32X_%=:\n\t"
      "v_mfma_f32_16x16x32_bf16 v[220:223], v[0:3], v[192:195], 0\n\t"
      "v_mfma_f32_16x16x32_bf16 v[220:223], v[16:19], v[196:199], v[220:223]\n\t"
      "v_mfma_f32_16x16x32_bf16 v[220:223], v[32:35], v[200:203], v[220:223]\n\t"
      "v_mfma_f32_16x16x32_bf16 v[220:223], v[48:51], v[204:207], v[220:223]\n\t"
      "v_mfma_f32_16x16x32_bf16 v[220:223], v[64:67], v[128:131], v[220:223]\n\t"
      "v_mfma_f32_16x16x32_bf16 v[220:223], v[80:83], v[132:135], v[220:223]\n\t"
      "v_mfma_f32_16x16x32_bf16 v[220:223], v[96:99], v[136:139], v[220:223]\n\t"
      "v_mfma_f32_16x16x32_bf16 v[220:223], v[112:115], v[140:143], v[220:223]\n\t"
      "v_mfma_f32_16x16x32_bf16 v[224:227], v[4:7], v[192:195], 0\n\t"
      "v_mfma_f32_16x16x32_bf16 v[224:227], v[20:23], v[196:199], v[224:227]\n\t"
      "v_mfma_f32_16x16x32_bf16 v[224:227], v[36:39], v[200:203], v[224:227]\n\t"
      "s_nop 7\n\t"
      "s_branch LA32B_%=\n\t"
      "LA33R_%=:\n\t"
      "s_mov_b32 s80, 0\n\t"
      "LA33L_%=:\n\t"
      "s_add_u32 s80, s80, 1\n\t"
      "s_cmp_lt_u32 s80, 262144\n\t"
      "s_cbranch_scc0 LA33X_%=\n\t"
      "s_waitcnt vmcnt(0)\n\t"
      "s_sleep 1\n\t"
      "global_load_dwordx4 v[144:147], v250, s[70:71] offset:-4096 sc1\n\t"
      "global_load_dwordx4 v[148:151], v250, s[70:71] offset:-3072 sc1\n\t"
      "global_load_dwordx4 v[152:155], v250, s[70:71] offset:-2048 sc1\n\t"
      "global_load_dwordx4 v[156:159], v250, s[70:71] offset:-1024 sc1\n\t"
      "global_load_dwordx4 v[160:163], v250, s[70:71] sc1\n\t"
      "global_load_dwordx4 v[164:167], v250, s[70:71] offset:1024 sc1\n\t"
      "global_load_dwordx4 v[168:171], v250, s[70:71] offset:2048 sc1\n\t"
      "global_load_dwordx4 v[172:175], v250, s[70:71] offset:3072 sc1\n\t"
      "s_waitcnt vmcnt(0)\n\t"
      "v_max3_u32 v251, v144, v145, v146\n\t"
      "v_max3_u32 v251, v251, v147, v148\n\t"
      "v_max3_u32 v251, v251, v149, v150\n\t"
      "v_max3_u32 v251, v251, v151, v152\n\t"
      "v_max3_u32 v251, v251, v153, v154\n\t"
      "v_max3_u32 v251, v251, v155, v156\n\t"
      "v_max3_u32 v251, v251, v157, v158\n\t"
      "v_max3_u32 v251, v251, v159, v160\n\t"
      "v_max3_u32 v251, v251, v161, v162\n\t"
      "v_max3_u32 v251, v251, v163, v164\n\t"
      "v_max3_u32 v251, v251, v165, v166\n\t"
      "v_max3_u32 v251, v251, v167, v168\n\t"
      "v_max3_u32 v251, v251, v169, v170\n\t"
      "v_max3_u32 v251, v251, v171, v172\n\t"
      "v_max3_u32 v251, v251, v173, v174\n\t"
      "v_max_u32_e32 v251, v251, v175\n\t"
      "v_cmp_eq_u32_e32 vcc, -1, v251\n\t"
      "s_cbranch_vccnz LA33L_%=\n\t"
      "LA33X_%=:\n\t"
      "v_mfma_f32_16x16x32_bf16 v[216:219], v[0:3], v[144:147], 0\n\t"
      "v_mfma_f32_16x16x32_bf16 v[216:219], v[16:19], v[148:151], v[216:219]\n\t"
      "v_mfma_f32_16x16x32_bf16 v[216:219], v[32:35], v[152:155], v[216:219]\n\t"
      "v_mfma_f32_16x16x32_bf16 v[216:219], v[48:51], v[156:159], v[216:219]\n\t"
      "v_mfma_f32_16x16x32_bf16 v[216:219], v[64:67], v[160:163], v[216:219]\n\t"
      "v_mfma_f32_16x16x32_bf16 v[216:219], v[80:83], v[164:167], v[216:219]\n\t"
      "v_mfma_f32_16x16x32_bf16 v[216:219], v[96:99], v[168:171], v[216:219]\n\t"
      "v_mfma_f32_16x16x32_bf16 v[216:219], v[112:115], v[172:175], v[216:219]\n\t"
      "v_mfma_f32_16x16x32_bf16 v[220:223], v[4:7], v[144:147], 0\n\t"
      "v_mfma_f32_16x16x32_bf16 v[220:223], v[20:23], v[148:151], v[220:223]\n\t"
      "v_mfma_f32_16x16x32_bf16 v[220:223], v[36:39], v[152:155], v[220:223]\n\t"
      "s_nop 7\n\t"
      "s_branch LA33B_%=\n\t"
      "LA34R_%=:\n\t"
      "s_mov_b32 s80, 0\n\t"
      "LA34L_%=:\n\t"
      "s_add_u32 s80, s80, 1\n\t"
      "s_cmp_lt_u32 s80, 262144\n\t"
      "s_cbranch_scc0 LA34X_%=\n\t"
      "s_waitcnt vmcnt(0)\n\t"
      "s_sleep 1\n\t"
      "global_load_dwordx4 v[176:179], v250, s[72:73] offset:-4096 sc1\n\t"
      "global_load_dwordx4 v[180:183], v250, s[72:73] offset:-3072 sc1\n\t"
      "global_load_dwordx4 v[184:187], v250, s[72:73] offset:-2048 sc1\n\t"
      "global_load_dwordx4 v[188:191], v250, s[72:73] offset:-1024 sc1\n\t"
      "global_load_dwordx4 v[192:195], v250, s[72:73] sc1\n\t"
      "global_load_dwordx4 v[196:199], v250, s[72:73] offset:1024 sc1\n\t"
      "global_load_dwordx4 v[200:203], v250, s[72:73] offset:2048 sc1\n\t"
      "global_load_dwordx4 v[204:207], v250, s[72:73] offset:3072 sc1\n\t"
      "s_waitcnt vmcnt(0)\n\t"
      "v_max3_u32 v251, v176, v177, v178\n\t"
      "v_max3_u32 v251, v251, v179, v180\n\t"
      "v_max3_u32 v251, v251, v181, v182\n\t"
      "v_max3_u32 v251, v251, v183, v184\n\t"
      "v_max3_u32 v251, v251, v185, v186\n\t"
      "v_max3_u32 v251, v251, v187, v188\n\t"
      "v_max3_u32 v251, v251, v189, v190\n\t"
      "v_max3_u32 v251, v251, v191, v192\n\t"
      "v_max3_u32 v251, v251, v193, v194\n\t"
      "v_max3_u32 v251, v251, v195, v196\n\t"
      "v_max3_u32 v251, v251, v197, v198\n\t"
      "v_max3_u32 v251, v251, v199, v200\n\t"
      "v_max3_u32 v251, v251, v201, v202\n\t"
      "v_max3_u32 v251, v251, v203, v204\n\t"
      "v_max3_u32 v251, v251, v205, v206\n\t"
      "v_max_u32_e32 v251, v251, v207\n\t"
      "v_cmp_eq_u32_e32 vcc, -1, v251\n\t"
      "s_cbranch_vccnz LA34L_%=\n\t"
      "LA34X_%=:\n\t"
      "v_mfma_f32_16x16x32_bf16 v[212:215], v[0:3], v[176:179], 0\n\t"
      "v_mfma_f32_16x16x32_bf16 v[212:215], v[16:19], v[180:183], v[212:215]\n\t"
      "v_mfma_f32_16x16x32_bf16 v[212:215], v[32:35], v[184:187], v[212:215]\n\t"
      "v_mfma_f32_16x16x32_bf16 v[212:215], v[48:51], v[188:191], v[212:215]\n\t"
      "v_mfma_f32_16x16x32_bf16 v[212:215], v[64:67], v[192:195], v[212:215]\n\t"
      "v_mfma_f32_16x16x32_bf16 v[212:215], v[80:83], v[196:199], v[212:215]\n\t"
      "v_mfma_f32_16x16x32_bf16 v[212:215], v[96:99], v[200:203], v[212:215]\n\t"
      "v_mfma_f32_16x16x32_bf16 v[212:215], v[112:115], v[204:207], v[212:215]\n\t"
      "v_mfma_f32_16x16x32_bf16 v[216:219], v[4:7], v[176:179], 0\n\t"
      "v_mfma_f32_16x16x32_bf16 v[216:219], v[20:23], v[180:183], v[216:219]\n\t"
      "v_mfma_f32_16x16x32_bf16 v[216:219], v[36:39], v[184:187], v[216:219]\n\t"
      "s_nop 7\n\t"
      "s_branch LA34B_%=\n\t"
      "LA35R_%=:\n\t"
      "s_mov_b32 s80, 0\n\t"
      "LA35L_%=:\n\t"
      "s_add_u32 s80, s80, 1\n\t"
      "s_cmp_lt_u32 s80, 262144\n\t"
      "s_cbranch_scc0 LA35X_%=\n\t"
      "s_waitcnt vmcnt(0)\n\t"
      "s_sleep 1\n\t"
      "global_load_dwordx4 v[128:131], v250, s[74:75] offset:-4096 sc1\n\t"
      "global_load_dwordx4 v[132:135], v250, s[74:75] offset:-3072 sc1\n\t"
      "global_load_dwordx4 v[136:139], v250, s[74:75] offset:-2048 sc1\n\t"
      "global_load_dwordx4 v[140:143], v250, s[74:75] offset:-1024 sc1\n\t"
      "global_load_dwordx4 v[144:147], v250, s[74:75] sc1\n\t"
      "global_load_dwordx4 v[148:151], v250, s[74:75] offset:1024 sc1\n\t"
      "global_load_dwordx4 v[152:155], v250, s[74:75] offset:2048 sc1\n\t"
      "global_load_dwordx4 v[156:159], v250, s[74:75] offset:3072 sc1\n\t"
      "s_waitcnt vmcnt(0)\n\t"
      "v_max3_u32 v251, v128, v129, v130\n\t"
      "v_max3_u32 v251, v251, v131, v132\n\t"
      "v_max3_u32 v251, v251, v133, v134\n\t"
      "v_max3_u32 v251, v251, v135, v136\n\t"
      "v_max3_u32 v251, v251, v137, v138\n\t"
      "v_max3_u32 v251, v251, v139, v140\n\t"
      "v_max3_u32 v251, v251, v141, v142\n\t"
      "v_max3_u32 v251, v251, v143, v144\n\t"
      "v_max3_u32 v251, v251, v145, v146\n\t"
      "v_max3_u32 v251, v251, v147, v148\n\t"
      "v_max3_u32 v251, v251, v149, v150\n\t"
      "v_max3_u32 v251, v251, v151, v152\n\t"
      "v_max3_u32 v251, v251, v153, v154\n\t"
      "v_max3_u32 v251, v251, v155, v156\n\t"
      "v_max3_u32 v251, v251, v157, v158\n\t"
      "v_max_u32_e32 v251, v251, v159\n\t"
      "v_cmp_eq_u32_e32 vcc, -1, v251\n\t"
      "s_cbranch_vccnz LA35L_%=\n\t"
      "LA35X_%=:\n\t"
      "v_mfma_f32_16x16x32_bf16 v[208:211], v[0:3], v[128:131], 0\n\t"
      "v_mfma_f32_16x16x32_bf16 v[208:211], v[16:19], v[132:135], v[208:211]\n\t"
      "v_mfma_f32_16x16x32_bf16 v[208:211], v[32:35], v[136:139], v[208:211]\n\t"
      "v_mfma_f32_16x16x32_bf16 v[208:211], v[48:51], v[140:143], v[208:211]\n\t"
      "v_mfma_f32_16x16x32_bf16 v[208:211], v[64:67], v[144:147], v[208:211]\n\t"
      "v_mfma_f32_16x16x32_bf16 v[208:211], v[80:83], v[148:151], v[208:211]\n\t"
      "v_mfma_f32_16x16x32_bf16 v[208:211], v[96:99], v[152:155], v[208:211]\n\t"
      "v_mfma_f32_16x16x32_bf16 v[208:211], v[112:115], v[156:159], v[208:211]\n\t"
      "v_mfma_f32_16x16x32_bf16 v[212:215], v[4:7], v[128:131], 0\n\t"
      "v_mfma_f32_16x16x32_bf16 v[212:215], v[20:23], v[132:135], v[212:215]\n\t"
      "v_mfma_f32_16x16x32_bf16 v[212:215], v[36:39], v[136:139], v[212:215]\n\t"
      "s_nop 7\n\t"
      "s_branch LA35B_%=\n\t"
      "LA36R_%=:\n\t"
      "s_mov_b32 s80, 0\n\t"
      "LA36L_%=:\n\t"
      "s_add_u32 s80, s80, 1\n\t"
      "s_cmp_lt_u32 s80, 262144\n\t"
      "s_cbranch_scc0 LA36X_%=\n\t"
      "s_waitcnt vmcnt(0)\n\t"
      "s_sleep 1\n\t"
      "global_load_dwordx4 v[160:163], v250, s[76:77] offset:-4096 sc1\n\t"
      "global_load_dwordx4 v[164:167], v250, s[76:77] offset:-3072 sc1\n\t"
      "global_load_dwordx4 v[168:171], v250, s[76:77] offset:-2048 sc1\n\t"
      "global_load_dwordx4 v[172:175], v250, s[76:77] offset:-1024 sc1\n\t"
      "global_load_dwordx4 v[176:179], v250, s[76:77] sc1\n\t"
      "global_load_dwordx4 v[180:183], v250, s[76:77] offset:1024 sc1\n\t"
      "global_load_dwordx4 v[184:187], v250, s[76:77] offset:2048 sc1\n\t"
      "global_load_dwordx4 v[188:191], v250, s[76:77] offset:3072 sc1\n\t"
      "s_waitcnt vmcnt(0)\n\t"
      "v_max3_u32 v251, v160, v161, v162\n\t"
      "v_max3_u32 v251, v251, v163, v164\n\t"
      "v_max3_u32 v251, v251, v165, v166\n\t"
      "v_max3_u32 v251, v251, v167, v168\n\t"
      "v_max3_u32 v251, v251, v169, v170\n\t"
      "v_max3_u32 v251, v251, v171, v172\n\t"
      "v_max3_u32 v251, v251, v173, v174\n\t"
      "v_max3_u32 v251, v251, v175, v176\n\t"
      "v_max3_u32 v251, v251, v177, v178\n\t"
      "v_max3_u32 v251, v251, v179, v180\n\t"
      "v_max3_u32 v251, v251, v181, v182\n\t"
      "v_max3_u32 v251, v251, v183, v184\n\t"
      "v_max3_u32 v251, v251, v185, v186\n\t"
      "v_max3_u32 v251, v251, v187, v188\n\t"
      "v_max3_u32 v251, v251, v189, v190\n\t"
      "v_max_u32_e32 v251, v251, v191\n\t"
      "v_cmp_eq_u32_e32 vcc, -1, v251\n\t"
      "s_cbranch_vccnz LA36L_%=\n\t"
      "LA36X_%=:\n\t"
      "v_mfma_f32_16x16x32_bf16 v[224:227], v[0:3], v[160:163], 0\n\t"
      "v_mfma_f32_16x16x32_bf16 v[224:227], v[16:19], v[164:167], v[224:227]\n\t"
      "v_mfma_f32_16x16x32_bf16 v[224:227], v[32:35], v[168:171], v[224:227]\n\t"
      "v_mfma_f32_16x16x32_bf16 v[224:227], v[48:51], v[172:175], v[224:227]\n\t"
      "v_mfma_f32_16x16x32_bf16 v[224:227], v[64:67], v[176:179], v[224:227]\n\t"
      "v_mfma_f32_16x16x32_bf16 v[224:227], v[80:83], v[180:183], v[224:227]\n\t"
      "v_mfma_f32_16x16x32_bf16 v[224:227], v[96:99], v[184:187], v[224:227]\n\t"
      "v_mfma_f32_16x16x32_bf16 v[224:227], v[112:115], v[188:191], v[224:227]\n\t"
      "v_mfma_f32_16x16x32_bf16 v[208:211], v[4:7], v[160:163], 0\n\t"
      "v_mfma_f32_16x16x32_bf16 v[208:211], v[20:23], v[164:167], v[208:211]\n\t"
      "v_mfma_f32_16x16x32_bf16 v[208:211], v[36:39], v[168:171], v[208:211]\n\t"
      "s_nop 7\n\t"
      "s_branch LA36B_%=\n\t"
      "LF5R_%=:\n\t"
      "s_mov_b32 s80, 0\n\t"
      "LF5L_%=:\n\t"
      "s_add_u32 s80, s80, 1\n\t"
      "s_cmp_lt_u32 s80, 262144\n\t"
      "s_cbranch_scc0 LF5X_%=\n\t"
      "s_sleep 2\n\t"
      "global_load_dwordx2 v[138:139], v143, s[60:61] sc1\n\t"
      "s_waitcnt vmcnt(0)\n\t"
      "v_cmp_ne_u64_e32 vcc, s[22:23], v[138:139]\n\t"
      "s_cbranch_vccnz LF5L_%=\n\t"
      "LF5X_%=:\n\t"
      "s_branch LF5B_%=\n\t"
      "PG0R_%=:\n\t"
      "s_mov_b32 s80, 0\n\t"
      "PG0L_%=:\n\t"
      "s_add_u32 s80, s80, 1\n\t"
      "s_cmp_lt_u32 s80, 262144\n\t"
      "s_cbranch_scc0 PG0X_%=\n\t"
      "s_sleep 2\n\t"
      "global_load_dword v251, v253, s[64:65] offset:252 sc1\n\t"
      "s_waitcnt vmcnt(0)\n\t"
      "v_cmp_eq_u32_e32 vcc, -1, v251\n\t"
      "s_cbranch_vccnz PG0L_%=\n\t"
      "PG0X_%=:\n\t"
      "s_branch PG0B_%=\n\t"
      "LF8R_%=:\n\t"
      "s_mov_b32 s80, 0\n\t"
      "LF8L_%=:\n\t"
      "s_add_u32 s80, s80, 1\n\t"
      "s_cmp_lt_u32 s80, 262144\n\t"
      "s_cbranch_scc0 LF8X_%=\n\t"
      "s_sleep 2\n\t"
      "global_load_dwordx2 v[240:241], v242, s[60:61] sc1\n\t"
      "s_waitcnt vmcnt(0)\n\t"
      "v_cmp_ne_u64_e32 vcc, s[22:23], v[240:241]\n\t"
      "s_cbranch_vccnz LF8L_%=\n\t"
      "LF8X_%=:\n\t"
      "s_branch LF8B_%=\n\t"
      "LA40R_%=:\n\t"
      "s_mov_b32 s80, 0\n\t"
      "LA40L_%=:\n\t"
      "s_add_u32 s80, s80, 1\n\t"
      "s_cmp_lt_u32 s80, 262144\n\t"
      "s_cbranch_scc0 LA40X_%=\n\t"
      "s_waitcnt vmcnt(0)\n\t"
      "PQ0R_%=:\n\t"
      "PQ0L_%=:\n\t"
      "s_add_u32 s80, s80, 1\n\t"
      "s_cmp_lt_u32 s80, 262144\n\t"
      "s_cbranch_scc0 PQ0X_%=\n\t"
      "s_sleep 1\n\t"
      "global_load_dword v251, v252, s[64:65] offset:252 sc1\n\t"
      "s_waitcnt vmcnt(0)\n\t"
      "v_cmp_eq_u32_e32 vcc, -1, v251\n\t"
      "s_cbranch_vccnz PQ0L_%=\n\t"
      "PQ0X_%=:\n\t"
      "s_add_u32 s58, s64, s40\n\t"
      "s_addc_u32 s59, s65, 0\n\t"
      "global_load_dwordx4 v[128:131], v244, s[58:59] sc1\n\t"
      "s_add_u32 s58, s64, s41\n\t"
      "s_addc_u32 s59, s65, 0\n\t"
      "global_load_dwordx4 v[132:135], v244, s[58:59] sc1\n\t"
      "s_add_u32 s58, s64, s42\n\t"
      "s_addc_u32 s59, s65, 0\n\t"
      "global_load_dwordx4 v[136:139], v244, s[58:59] sc1\n\t"
      "s_add_u32 s58, s64, s43\n\t"
      "s_addc_u32 s59, s65, 0\n\t"
      "global_load_dwordx4 v[140:143], v244, s[58:59] sc1\n\t"
      "s_add_u32 s58, s64, s44\n\t"
      "s_addc_u32 s59, s65, 0\n\t"
      "global_load_dwordx4 v[144:147], v244, s[58:59] sc1\n\t"
      "s_add_u32 s58, s64, s45\n\t"
      "s_addc_u32 s59, s65, 0\n\t"
      "global_load_dwordx4 v[148:151], v244, s[58:59] sc1\n\t"
      "s_add_u32 s58, s64, s46\n\t"
      "s_addc_u32 s59, s65, 0\n\t"
      "global_load_dwordx4 v[152:155], v244, s[58:59] sc1\n\t"
      "s_waitcnt vmcnt(0)\n\t"
      "v_max3_u32 v251, v128, v129, v130\n\t"
      "v_max3_u32 v251, v251, v131, v132\n\t"
      "v_max3_u32 v251, v251, v133, v134\n\t"
      "v_max3_u32 v251, v251, v135, v136\n\t"
      "v_max3_u32 v251, v251, v137, v138\n\t"
      "v_max3_u32 v251, v251, v139, v140\n\t"
      "v_max3_u32 v251, v251, v141, v142\n\t"
      "v_max3_u32 v251, v251, v143, v144\n\t"
      "v_max3_u32 v251, v251, v145, v146\n\t"
      "v_max3_u32 v251, v251, v147, v148\n\t"
      "v_max3_u32 v251, v251, v149, v150\n\t"
      "v_max3_u32 v251, v251, v151, v152\n\t"
      "v_max3_u32 v251, v251, v153, v154\n\t"
      "v_max_u32_e32 v251, v251, v155\n\t"
      "v_cmp_eq_u32_e32 vcc, -1, v251\n\t"
      "s_cbranch_vccnz LA40L_%=\n\t"
      "LA40X_%=:\n\t"
      "v_mfma_f32_16x16x32_bf16 v[196:199], v[128:131], v[0:3], 0\n\t"
      "v_mfma_f32_16x16x32_bf16 v[200:203], v[128:131], v[4:7], 0\n\t"
      "v_mfma_f32_16x16x32_bf16 v[204:207], v[128:131], v[8:11], 0\n\t"
      "v_mfma_f32_16x16x32_bf16 v[208:211], v[128:131], v[12:15], 0\n\t"
      "v_mfma_f32_16x16x32_bf16 v[196:199], v[132:135], v[16:19], v[196:199]\n\t"
      "v_mfma_f32_16x16x32_bf16 v[200:203], v[132:135], v[20:23], v[200:203]\n\t"
      "v_mfma_f32_16x16x32_bf16 v[204:207], v[132:135], v[24:27], v[204:207]\n\t"
      "v_mfma_f32_16x16x32_bf16 v[208:211], v[132:135], v[28:31], v[208:211]\n\t"
      "v_mfma_f32_16x16x32_bf16 v[196:199], v[136:139], v[32:35], v[196:199]\n\t"
      "v_mfma_f32_16x16x32_bf16 v[200:203], v[136:139], v[36:39], v[200:203]\n\t"
      "v_mfma_f32_16x16x32_bf16 v[204:207], v[136:139], v[40:43], v[204:207]\n\t"
      "v_mfma_f32_16x16x32_bf16 v[208:211], v[136:139], v[44:47], v[208:211]\n\t"
      "v_mfma_f32_16x16x32_bf16 v[196:199], v[140:143], v[48:51], v[196:199]\n\t"
      "v_mfma_f32_16x16x32_bf16 v[200:203], v[140:143], v[52:55], v[200:203]\n\t"
      "v_mfma_f32_16x16x32_bf16 v[204:207], v[140:143], v[56:59], v[204:207]\n\t"
      "v_mfma_f32_16x16x32_bf16 v[208:211], v[140:143], v[60:63], v[208:211]\n\t"
      "v_mfma_f32_16x16x32_bf16 v[196:199], v[144:147], v[64:67], v[196:199]\n\t"
      "v_mfma_f32_16x16x32_bf16 v[200:203], v[144:147], v[68:71], v[200:203]\n\t"
      "v_mfma_f32_16x16x32_bf16 v[204:207], v[144:147], v[72:75], v[204:207]\n\t"
      "v_mfma_f32_16x16x32_bf16 v[208:211], v[144:147], v[76:79], v[208:211]\n\t"
      "v_mfma_f32_16x16x32_bf16 v[196:199], v[148:151], v[80:83], v[196:199]\n\t"
      "v_mfma_f32_16x16x32_bf16 v[200:203], v[148:151], v[84:87], v[200:203]\n\t"
      "v_mfma_f32_16x16x32_bf16 v[204:207], v[148:151], v[88:91], v[204:207]\n\t"
      "v_mfma_f32_16x16x32_bf16 v[208:211], v[148:151], v[92:95], v[208:211]\n\t"
      "v_mfma_f32_16x16x32_bf16 v[196:199], v[152:155], v[96:99], v[196:199]\n\t"
      "v_mfma_f32_16x16x32_bf16 v[200:203], v[152:155], v[100:103], v[200:203]\n\t"
      "v_mfma_f32_16x16x32_bf16 v[204:207], v[152:155], v[104:107], v[204:207]\n\t"
      "v_mfma_f32_16x16x32_bf16 v[208:211], v[152:155], v[108:111], v[208:211]\n\t"
      "s_nop 7\n\t"
      "s_branch LA40B_%=\n\t"
      "LA41R_%=:\n\t"
      "s_mov_b32 s80, 0\n\t"
      "LA41L_%=:\n\t"
      "s_add_u32 s80, s80, 1\n\t"
      "s_cmp_lt_u32 s80, 262144\n\t"
      "s_cbranch_scc0 LA41X_%=\n\t"
      "s_waitcnt vmcnt(0)\n\t"
      "PQ1R_%=:\n\t"
      "PQ1L_%=:\n\t"
      "s_add_u32 s80, s80, 1\n\t"
      "s_cmp_lt_u32 s80, 262144\n\t"
      "s_cbranch_scc0 PQ1X_%=\n\t"
      "s_sleep 1\n\t"
      "global_load_dword v251, v252, s[66:67] offset:252 sc1\n\t"
      "s_waitcnt vmcnt(0)\n\t"
      "v_cmp_eq_u32_e32 vcc, -1, v251\n\t"
      "s_cbranch_vccnz PQ1L_%=\n\t"
      "PQ1X_%=:\n\t"
      "s_add_u32 s58, s66, s40\n\t"
      "s_addc_u32 s59, s67, 0\n\t"
      "global_load_dwordx4 v[156:159], v244, s[58:59] sc1\n\t"
      "s_add_u32 s58, s66, s41\n\t"
      "s_addc_u32 s59, s67, 0\n\t"
      "global_load_dwordx4 v[160:163], v244, s[58:59] sc1\n\t"
      "s_add_u32 s58, s66, s42\n\t"
      "s_addc_u32 s59, s67, 0\n\t"
      "global_load_dwordx4 v[164:167], v244, s[58:59] sc1\n\t"
      "s_add_u32 s58, s66, s43\n\t"
      "s_addc_u32 s59, s67, 0\n\t"
      "global_load_dwordx4 v[168:171], v244, s[58:59] sc1\n\t"
      "s_add_u32 s58, s66, s44\n\t"
      "s_addc_u32 s59, s67, 0\n\t"
      "global_load_dwordx4 v[172:175], v244, s[58:59] sc1\n\t"
      "s_add_u32 s58, s66, s45\n\t"
      "s_addc_u32 s59, s67, 0\n\t"
      "global_load_dwordx4 v[176:179], v244, s[58:59] sc1\n\t"
      "s_add_u32 s58, s66, s46\n\t"
      "s_addc_u32 s59, s67, 0\n\t"
      "global_load_dwordx4 v[180:183], v244, s[58:59] sc1\n\t"
      "s_waitcnt vmcnt(0)\n\t"
      "v_max3_u32 v251, v156, v157, v158\n\t"
      "v_max3_u32 v251, v251, v159, v160\n\t"
      "v_max3_u32 v251, v251, v161, v162\n\t"
      "v_max3_u32 v251, v251, v163, v164\n\t"
      "v_max3_u32 v251, v251, v165, v166\n\t"
      "v_max3_u32 v251, v251, v167, v168\n\t"
      "v_max3_u32 v251, v251, v169, v170\n\t"
      "v_max3_u32 v251, v251, v171, v172\n\t"
      "v_max3_u32 v251, v251, v173, v174\n\t"
      "v_max3_u32 v251, v251, v175, v176\n\t"
      "v_max3_u32 v251, v251, v177, v178\n\t"
      "v_max3_u32 v251, v251, v179, v180\n\t"
      "v_max3_u32 v251, v251, v181, v182\n\t"
      "v_max_u32_e32 v251, v251, v183\n\t"
      "v_cmp_eq_u32_e32 vcc, -1, v251\n\t"
      "s_cbranch_vccnz LA41L_%=\n\t"
      "LA41X_%=:\n\t"
      "v_mfma_f32_16x16x32_bf16 v[212:215], v[156:159], v[0:3], 0\n\t"
      "v_mfma_f32_16x16x32_bf16 v[216:219], v[156:159], v[4:7], 0\n\t"
      "v_mfma_f32_16x16x32_bf16 v[220:223], v[156:159], v[8:11], 0\n\t"
      "v_mfma_f32_16x16x32_bf16 v[224:227], v[156:159], v[12:15], 0\n\t"
      "v_mfma_f32_16x16x32_bf16 v[212:215], v[160:163], v[16:19], v[212:215]\n\t"
      "v_mfma_f32_16x16x32_bf16 v[216:219], v[160:163], v[20:23], v[216:219]\n\t"
      "v_mfma_f32_16x16x32_bf16 v[220:223], v[160:163], v[24:27], v[220:223]\n\t"
      "v_mfma_f32_16x16x32_bf16 v[224:227], v[160:163], v[28:31], v[224:227]\n\t"
      "v_mfma_f32_16x16x32_bf16 v[212:215], v[164:167], v[32:35], v[212:215]\n\t"
      "v_mfma_f32_16x16x32_bf16 v[216:219], v[164:167], v[36:39], v[216:219]\n\t"
      "v_mfma_f32_16x16x32_bf16 v[220:223], v[164:167], v[40:43], v[220:223]\n\t"
      "v_mfma_f32_16x16x32_bf16 v[224:227], v[164:167], v[44:47], v[224:227]\n\t"
      "v_mfma_f32_16x16x32_bf16 v[212:215], v[168:171], v[48:51], v[212:215]\n\t"
      "v_mfma_f32_16x16x32_bf16 v[216:219], v[168:171], v[52:55], v[216:219]\n\t"
      "v_mfma_f32_16x16x32_bf16 v[220:223], v[168:171], v[56:59], v[220:223]\n\t"
      "v_mfma_f32_16x16x32_bf16 v[224:227], v[168:171], v[60:63], v[224:227]\n\t"
      "v_mfma_f32_16x16x32_bf16 v[212:215], v[172:175], v[64:67], v[212:215]\n\t"
      "v_mfma_f32_16x16x32_bf16 v[216:219], v[172:175], v[68:71], v[216:219]\n\t"
      "v_mfma_f32_16x16x32_bf16 v[220:223], v[172:175], v[72:75], v[220:223]\n\t"
      "v_mfma_f32_16x16x32_bf16 v[224:227], v[172:175], v[76:79], v[224:227]\n\t"
      "v_mfma_f32_16x16x32_bf16 v[212:215], v[176:179], v[80:83], v[212:215]\n\t"
      "v_mfma_f32_16x16x32_bf16 v[216:219], v[176:179], v[84:87], v[216:219]\n\t"
      "v_mfma_f32_16x16x32_bf16 v[220:223], v[176:179], v[88:91], v[220:223]\n\t"
      "v_mfma_f32_16x16x32_bf16 v[224:227], v[176:179], v[92:95], v[224:227]\n\t"
      "v_mfma_f32_16x16x32_bf16 v[212:215], v[180:183], v[96:99], v[212:215]\n\t"
      "v_mfma_f32_16x16x32_bf16 v[216:219], v[180:183], v[100:103], v[216:219]\n\t"
      "v_mfma_f32_16x16x32_bf16 v[220:223], v[180:183], v[104:107], v[220:223]\n\t"
      "v_mfma_f32_16x16x32_bf16 v[224:227], v[180:183], v[108:111], v[224:227]\n\t"
      "s_nop 7\n\t"
      "s_branch LA41B_%=\n\t"
      "LA42R_%=:\n\t"
      "s_mov_b32 s80, 0\n\t"
      "LA42L_%=:\n\t"
      "s_add_u32 s80, s80, 1\n\t"
      "s_cmp_lt_u32 s80, 262144\n\t"
      "s_cbranch_scc0 LA42X_%=\n\t"
      "s_waitcnt vmcnt(0)\n\t"
      "PQ2R_%=:\n\t"
      "PQ2L_%=:\n\t"
      "s_add_u32 s80, s80, 1\n\t"
      "s_cmp_lt_u32 s80, 262144\n\t"
      "s_cbranch_scc0 PQ2X_%=\n\t"
      "s_sleep 1\n\t"
      "global_load_dword v251, v252, s[68:69] offset:252 sc1\n\t"
      "s_waitcnt vmcnt(0)\n\t"
      "v_cmp_eq_u32_e32 vcc, -1, v251\n\t"
      "s_cbranch_vccnz PQ2L_%=\n\t"
      "PQ2X_%=:\n\t"
      "s_add_u32 s58, s68, s40\n\t"
      "s_addc_u32 s59, s69, 0\n\t"
      "global_load_dwordx4 v[184:187], v244, s[58:59] sc1\n\t"
      "s_add_u32 s58, s68, s41\n\t"
      "s_addc_u32 s59, s69, 0\n\t"
      "global_load_dwordx4 v[188:191], v244, s[58:59] sc1\n\t"
      "s_add_u32 s58, s68, s42\n\t"
      "s_addc_u32 s59, s69, 0\n\t"
      "global_load_dwordx4 v[192:195], v244, s[58:59] sc1\n\t"
      "s_add_u32 s58, s68, s43\n\t"
      "s_addc_u32 s59, s69, 0\n\t"
      "global_load_dwordx4 v[128:131], v244, s[58:59] sc1\n\t"
      "s_add_u32 s58, s68, s44\n\t"
      "s_addc_u32 s59, s69, 0\n\t"
      "global_load_dwordx4 v[132:135], v244, s[58:59] sc1\n\t"
      "s_add_u32 s58, s68, s45\n\t"
      "s_addc_u32 s59, s69, 0\n\t"
      "global_load_dwordx4 v[136:139], v244, s[58:59] sc1\n\t"
      "s_add_u32 s58, s68, s46\n\t"
      "s_addc_u32 s59, s69, 0\n\t"
      "global_load_dwordx4 v[140:143], v244, s[58:59] sc1\n\t"
      "s_waitcnt vmcnt(0)\n\t"
      "v_max3_u32 v251, v184, v185, v186\n\t"
      "v_max3_u32 v251, v251, v187, v188\n\t"
      "v_max3_u32 v251, v251, v189, v190\n\t"
      "v_max3_u32 v251, v251, v191, v192\n\t"
      "v_max3_u32 v251, v251, v193, v194\n\t"
      "v_max3_u32 v251, v251, v195, v128\n\t"
      "v_max3_u32 v251, v251, v129, v130\n\t"
      "v_max3_u32 v251, v251, v131, v132\n\t"
      "v_max3_u32 v251, v251, v133, v134\n\t"
      "v_max3_u32 v251, v251, v135, v136\n\t"
      "v_max3_u32 v251, v251, v137, v138\n\t"
      "v_max3_u32 v251, v251, v139, v140\n\t"
      "v_max3_u32 v251, v251, v141, v142\n\t"
      "v_max_u32_e32 v251, v251, v143\n\t"
      "v_cmp_eq_u32_e32 vcc, -1, v251\n\t"
      "s_cbranch_vccnz LA42L_%=\n\t"
      "LA42X_%=:\n\t"
      "v_mfma_f32_16x16x32_bf16 v[196:199], v[184:187], v[0:3], 0\n\t"
      "v_mfma_f32_16x16x32_bf16 v[200:203], v[184:187], v[4:7], 0\n\t"
      "v_mfma_f32_16x16x32_bf16 v[204:207], v[184:187], v[8:11], 0\n\t"
      "v_mfma_f32_16x16x32_bf16 v[208:211], v[184:187], v[12:15], 0\n\t"
      "v_mfma_f32_16x16x32_bf16 v[196:199], v[188:191], v[16:19], v[196:199]\n\t"
      "v_mfma_f32_16x16x32_bf16 v[200:203], v[188:191], v[20:23], v[200:203]\n\t"
      "v_mfma_f32_16x16x32_bf16 v[204:207], v[188:191], v[24:27], v[204:207]\n\t"
      "v_mfma_f32_16x16x32_bf16 v[208:211], v[188:191], v[28:31], v[208:211]\n\t"
      "v_mfma_f32_16x16x32_bf16 v[196:199], v[192:195], v[32:35], v[196:199]\n\t"
      "v_mfma_f32_16x16x32_bf16 v[200:203], v[192:195], v[36:39], v[200:203]\n\t"
      "v_mfma_f32_16x16x32_bf16 v[204:207], v[192:195], v[40:43], v[204:207]\n\t"
      "v_mfma_f32_16x16x32_bf16 v[208:211], v[192:195], v[44:47], v[208:211]\n\t"
      "v_mfma_f32_16x16x32_bf16 v[196:199], v[128:131], v[48:51], v[196:199]\n\t"
      "v_mfma_f32_16x16x32_bf16 v[200:203], v[128:131], v[52:55], v[200:203]\n\t"
      "v_mfma_f32_16x16x32_bf16 v[204:207], v[128:131], v[56:59], v[204:207]\n\t"
      "v_mfma_f32_16x16x32_bf16 v[208:211], v[128:131], v[60:63], v[208:211]\n\t"
      "v_mfma_f32_16x16x32_bf16 v[196:199], v[132:135], v[64:67], v[196:199]\n\t"
      "v_mfma_f32_16x16x32_bf16 v[200:203], v[132:135], v[68:71], v[200:203]\n\t"
      "v_mfma_f32_16x16x32_bf16 v[204:207], v[132:135], v[72:75], v[204:207]\n\t"
      "v_mfma_f32_16x16x32_bf16 v[208:211], v[132:135], v[76:79], v[208:211]\n\t"
      "v_mfma_f32_16x16x32_bf16 v[196:199], v[136:139], v[80:83], v[196:199]\n\t"
      "v_mfma_f32_16x16x32_bf16 v[200:203], v[136:139], v[84:87], v[200:203]\n\t"
      "v_mfma_f32_16x16x32_bf16 v[204:207], v[136:139], v[88:91], v[204:207]\n\t"
      "v_mfma_f32_16x16x32_bf16 v[208:211], v[136:139], v[92:95], v[208:211]\n\t"
      "v_mfma_f32_16x16x32_bf16 v[196:199], v[140:143], v[96:99], v[196:199]\n\t"
      "v_mfma_f32_16x16x32_bf16 v[200:203], v[140:143], v[100:103], v[200:203]\n\t"
      "v_mfma_f32_16x16x32_bf16 v[204:207], v[140:143], v[104:107], v[204:207]\n\t"
      "v_mfma_f32_16x16x32_bf16 v[208:211], v[140:143], v[108:111], v[208:211]\n\t"
      "s_nop 7\n\t"
      "s_branch LA42B_%=\n\t"
      "LA43R_%=:\n\t"
      "s_mov_b32 s80, 0\n\t"
      "LA43L_%=:\n\t"
      "s_add_u32 s80, s80, 1\n\t"
      "s_cmp_lt_u32 s80, 262144\n\t"
      "s_cbranch_scc0 LA43X_%=\n\t"
      "s_waitcnt vmcnt(0)\n\t"
      "PQ3R_%=:\n\t"
      "PQ3L_%=:\n\t"
      "s_add_u32 s80, s80, 1\n\t"
      "s_cmp_lt_u32 s80, 262144\n\t"
      "s_cbranch_scc0 PQ3X_%=\n\t"
      "s_sleep 1\n\t"
      "global_load_dword v251, v252, s[70:71] offset:252 sc1\n\t"
      "s_waitcnt vmcnt(0)\n\t"
      "v_cmp_eq_u32_e32 vcc, -1, v251\n\t"
      "s_cbranch_vccnz PQ3L_%=\n\t"
      "PQ3X_%=:\n\t"
      "s_add_u32 s58, s70, s40\n\t"
      "s_addc_u32 s59, s71, 0\n\t"
      "global_load_dwordx4 v[144:147], v244, s[58:59] sc1\n\t"
      "s_add_u32 s58, s70, s41\n\t"
      "s_addc_u32 s59, s71, 0\n\t"
      "global_load_dwordx4 v[148:151], v244, s[58:59] sc1\n\t"
      "s_add_u32 s58, s70, s42\n\t"
      "s_addc_u32 s59, s71, 0\n\t"
      "global_load_dwordx4 v[152:155], v244, s[58:59] sc1\n\t"
      "s_add_u32 s58, s70, s43\n\t"
      "s_addc_u32 s59, s71, 0\n\t"
      "global_load_dwordx4 v[156:159], v244, s[58:59] sc1\n\t"
      "s_add_u32 s58, s70, s44\n\t"
      "s_addc_u32 s59, s71, 0\n\t"
      "global_load_dwordx4 v[160:163], v244, s[58:59] sc1\n\t"
      "s_add_u32 s58, s70, s45\n\t"
      "s_addc_u32 s59, s71, 0\n\t"
      "global_load_dwordx4 v[164:167], v244, s[58:59] sc1\n\t"
      "s_add_u32 s58, s70, s46\n\t"
      "s_addc_u32 s59, s71, 0\n\t"
      "global_load_dwordx4 v[168:171], v244, s[58:59] sc1\n\t"
      "s_waitcnt vmcnt(0)\n\t"
      "v_max3_u32 v251, v144, v145, v146\n\t"
      "v_max3_u32 v251, v251, v147, v148\n\t"
      "v_max3_u32 v251, v251, v149, v150\n\t"
      "v_max3_u32 v251, v251, v151, v152\n\t"
      "v_max3_u32 v251, v251, v153, v154\n\t"
      "v_max3_u32 v251, v251, v155, v156\n\t"
      "v_max3_u32 v251, v251, v157, v158\n\t"
      "v_max3_u32 v251, v251, v159, v160\n\t"
      "v_max3_u32 v251, v251, v161, v162\n\t"
      "v_max3_u32 v251, v251, v163, v164\n\t"
      "v_max3_u32 v251, v251, v165, v166\n\t"
      "v_max3_u32 v251, v251, v167, v168\n\t"
      "v_max3_u32 v251, v251, v169, v170\n\t"
      "v_max_u32_e32 v251, v251, v171\n\t"
      "v_cmp_eq_u32_e32 vcc, -1, v251\n\t"
      "s_cbranch_vccnz LA43L_%=\n\t"
      "LA43X_%=:\n\t"
      "v_mfma_f32_16x16x32_bf16 v[212:215], v[144:147], v[0:3], 0\n\t"
      "v_mfma_f32_16x16x32_bf16 v[216:219], v[144:147], v[4:7], 0\n\t"
      "v_mfma_f32_16x16x32_bf16 v[220:223], v[144:147], v[8:11], 0\n\t"
      "v_mfma_f32_16x16x32_bf16 v[224:227], v[144:147], v[12:15], 0\n\t"
      "v_mfma_f32_16x16x32_bf16 v[212:215], v[148:151], v[16:19], v[212:215]\n\t"
      "v_mfma_f32_16x16x32_bf16 v[216:219], v[148:151], v[20:23], v[216:219]\n\t"
      "v_mfma_f32_16x16x32_bf16 v[220:223], v[148:151], v[24:27], v[220:223]\n\t"
      "v_mfma_f32_16x16x32_bf16 v[224:227], v[148:151], v[28:31], v[224:227]\n\t"
      "v_mfma_f32_16x16x32_bf16 v[212:215], v[152:155], v[32:35], v[212:215]\n\t"
      "v_mfma_f32_16x16x32_bf16 v[216:219], v[152:155], v[36:39], v[216:219]\n\t"
      "v_mfma_f32_16x16x32_bf16 v[220:223], v[152:155], v[40:43], v[220:223]\n\t"
      "v_mfma_f32_16x16x32_bf16 v[224:227], v[152:155], v[44:47], v[224:227]\n\t"
      "v_mfma_f32_16x16x32_bf16 v[212:215], v[156:159], v[48:51], v[212:215]\n\t"
      "v_mfma_f32_16x16x32_bf16 v[216:219], v[156:159], v[52:55], v[216:219]\n\t"
      "v_mfma_f32_16x16x32_bf16 v[220:223], v[156:159], v[56:59], v[220:223]\n\t"
      "v_mfma_f32_16x16x32_bf16 v[224:227], v[156:159], v[60:63], v[224:227]\n\t"
      "v_mfma_f32_16x16x32_bf16 v[212:215], v[160:163], v[64:67], v[212:215]\n\t"
      "v_mfma_f32_16x16x32_bf16 v[216:219], v[160:163], v[68:71], v[216:219]\n\t"
      "v_mfma_f32_16x16x32_bf16 v[220:223], v[160:163], v[72:75], v[220:223]\n\t"
      "v_mfma_f32_16x16x32_bf16 v[224:227], v[160:163], v[76:79], v[224:227]\n\t"
      "v_mfma_f32_16x16x32_bf16 v[212:215], v[164:167], v[80:83], v[212:215]\n\t"
      "v_mfma_f32_16x16x32_bf16 v[216:219], v[164:167], v[84:87], v[216:219]\n\t"
      "v_mfma_f32_16x16x32_bf16 v[220:223], v[164:167], v[88:91], v[220:223]\n\t"
      "v_mfma_f32_16x16x32_bf16 v[224:227], v[164:167], v[92:95], v[224:227]\n\t"
      "v_mfma_f32_16x16x32_bf16 v[212:215], v[168:171], v[96:99], v[212:215]\n\t"
      "v_mfma_f32_16x16x32_bf16 v[216:219], v[168:171], v[100:103], v[216:219]\n\t"
      "v_mfma_f32_16x16x32_bf16 v[220:223], v[168:171], v[104:107], v[220:223]\n\t"
      "v_mfma_f32_16x16x32_bf16 v[224:227], v[168:171], v[108:111], v[224:227]\n\t"
      "s_nop 7\n\t"
      "s_branch LA43B_%=\n\t"
      "LA44R_%=:\n\t"
      "s_mov_b32 s80, 0\n\t"
      "LA44L_%=:\n\t"
      "s_add_u32 s80, s80, 1\n\t"
      "s_cmp_lt_u32 s80, 262144\n\t"
      "s_cbranch_scc0 LA44X_%=\n\t"
      "s_waitcnt vmcnt(0)\n\t"
      "PQ4R_%=:\n\t"
      "PQ4L_%=:\n\t"
      "s_add_u32 s80, s80, 1\n\t"
      "s_cmp_lt_u32 s80, 262144\n\t"
      "s_cbranch_scc0 PQ4X_%=\n\t"
      "s_sleep 1\n\t"
      "global_load_dword v251, v252, s[72:73] offset:252 sc1\n\t"
      "s_waitcnt vmcnt(0)\n\t"
      "v_cmp_eq_u32_e32 vcc, -1, v251\n\t"
      "s_cbranch_vccnz PQ4L_%=\n\t"
      "PQ4X_%=:\n\t"
      "s_add_u32 s58, s72, s40\n\t"
      "s_addc_u32 s59, s73, 0\n\t"
      "global_load_dwordx4 v[172:175], v244, s[58:59] sc1\n\t"
      "s_add_u32 s58, s72, s41\n\t"
      "s_addc_u32 s59, s73, 0\n\t"
      "global_load_dwordx4 v[176:179], v244, s[58:59] sc1\n\t"
      "s_add_u32 s58, s72, s42\n\t"
      "s_addc_u32 s59, s73, 0\n\t"
      "global_load_dwordx4 v[180:183], v244, s[58:59] sc1\n\t"
      "s_add_u32 s58, s72, s43\n\t"
      "s_addc_u32 s59, s73, 0\n\t"
      "global_load_dwordx4 v[184:187], v244, s[58:59] sc1\n\t"
      "s_add_u32 s58, s72, s44\n\t"
      "s_addc_u32 s59, s73, 0\n\t"
      "global_load_dwordx4 v[188:191], v244, s[58:59] sc1\n\t"
      "s_add_u32 s58, s72, s45\n\t"
      "s_addc_u32 s59, s73, 0\n\t"
      "global_load_dwordx4 v[192:195], v244, s[58:59] sc1\n\t"
      "s_add_u32 s58, s72, s46\n\t"
      "s_addc_u32 s59, s73, 0\n\t"
      "global_load_dwordx4 v[128:131], v244, s[58:59] sc1\n\t"
      "s_waitcnt vmcnt(0)\n\t"
      "v_max3_u32 v251, v172, v173, v174\n\t"
      "v_max3_u32 v251, v251, v175, v176\n\t"
      "v_max3_u32 v251, v251, v177, v178\n\t"
      "v_max3_u32 v251, v251, v179, v180\n\t"
      "v_max3_u32 v251, v251, v181, v182\n\t"
      "v_max3_u32 v251, v251, v183, v184\n\t"
      "v_max3_u32 v251, v251, v185, v186\n\t"
      "v_max3_u32 v251, v251, v187, v188\n\t"
      "v_max3_u32 v251, v251, v189, v190\n\t"
      "v_max3_u32 v251, v251, v191, v192\n\t"
      "v_max3_u32 v251, v251, v193, v194\n\t"
      "v_max3_u32 v251, v251, v195, v128\n\t"
      "v_max3_u32 v251, v251, v129, v130\n\t"
      "v_max_u32_e32 v251, v251, v131\n\t"
      "v_cmp_eq_u32_e32 vcc, -1, v251\n\t"
      "s_cbranch_vccnz LA44L_%=\n\t"
      "LA44X_%=:\n\t"
      "v_mfma_f32_16x16x32_bf16 v[196:199], v[172:175], v[0:3], 0\n\t"
      "v_mfma_f32_16x16x32_bf16 v[200:203], v[172:175], v[4:7], 0\n\t"
      "v_mfma_f32_16x16x32_bf16 v[204:207], v[172:175], v[8:11], 0\n\t"
      "v_mfma_f32_16x16x32_bf16 v[208:211], v[172:175], v[12:15], 0\n\t"
      "v_mfma_f32_16x16x32_bf16 v[196:199], v[176:179], v[16:19], v[196:199]\n\t"
      "v_mfma_f32_16x16x32_bf16 v[200:203], v[176:179], v[20:23], v[200:203]\n\t"
      "v_mfma_f32_16x16x32_bf16 v[204:207], v[176:179], v[24:27], v[204:207]\n\t"
      "v_mfma_f32_16x16x32_bf16 v[208:211], v[176:179], v[28:31], v[208:211]\n\t"
      "v_mfma_f32_16x16x32_bf16 v[196:199], v[180:183], v[32:35], v[196:199]\n\t"
      "v_mfma_f32_16x16x32_bf16 v[200:203], v[180:183], v[36:39], v[200:203]\n\t"
      "v_mfma_f32_16x16x32_bf16 v[204:207], v[180:183], v[40:43], v[204:207]\n\t"
      "v_mfma_f32_16x16x32_bf16 v[208:211], v[180:183], v[44:47], v[208:211]\n\t"
      "v_mfma_f32_16x16x32_bf16 v[196:199], v[184:187], v[48:51], v[196:199]\n\t"
      "v_mfma_f32_16x16x32_bf16 v[200:203], v[184:187], v[52:55], v[200:203]\n\t"
      "v_mfma_f32_16x16x32_bf16 v[204:207], v[184:187], v[56:59], v[204:207]\n\t"
      "v_mfma_f32_16x16x32_bf16 v[208:211], v[184:187], v[60:63], v[208:211]\n\t"
      "v_mfma_f32_16x16x32_bf16 v[196:199], v[188:191], v[64:67], v[196:199]\n\t"
      "v_mfma_f32_16x16x32_bf16 v[200:203], v[188:191], v[68:71], v[200:203]\n\t"
      "v_mfma_f32_16x16x32_bf16 v[204:207], v[188:191], v[72:75], v[204:207]\n\t"
      "v_mfma_f32_16x16x32_bf16 v[208:211], v[188:191], v[76:79], v[208:211]\n\t"
      "v_mfma_f32_16x16x32_bf16 v[196:199], v[192:195], v[80:83], v[196:199]\n\t"
      "v_mfma_f32_16x16x32_bf16 v[200:203], v[192:195], v[84:87], v[200:203]\n\t"
      "v_mfma_f32_16x16x32_bf16 v[204:207], v[192:195], v[88:91], v[204:207]\n\t"
      "v_mfma_f32_16x16x32_bf16 v[208:211], v[192:195], v[92:95], v[208:211]\n\t"
      "v_mfma_f32_16x16x32_bf16 v[196:199], v[128:131], v[96:99], v[196:199]\n\t"
      "v_mfma_f32_16x16x32_bf16 v[200:203], v[128:131], v[100:103], v[200:203]\n\t"
      "v_mfma_f32_16x16x32_bf16 v[204:207], v[128:131], v[104:107], v[204:207]\n\t"
      "v_mfma_f32_16x16x32_bf16 v[208:211], v[128:131], v[108:111], v[208:211]\n\t"
      "s_nop 7\n\t"
      "s_branch LA44B_%=\n\t"
      "LA45R_%=:\n\t"
      "s_mov_b32 s80, 0\n\t"
      "LA45L_%=:\n\t"
      "s_add_u32 s80, s80, 1\n\t"
      "s_cmp_lt_u32 s80, 262144\n\t"
      "s_cbranch_scc0 LA45X_%=\n\t"
      "s_waitcnt vmcnt(0)\n\t"
      "PQ5R_%=:\n\t"
      "PQ5L_%=:\n\t"
      "s_add_u32 s80, s80, 1\n\t"
      "s_cmp_lt_u32 s80, 262144\n\t"
      "s_cbranch_scc0 PQ5X_%=\n\t"
      "s_sleep 1\n\t"
      "global_load_dword v251, v252, s[74:75] offset:252 sc1\n\t"
      "s_waitcnt vmcnt(0)\n\t"
      "v_cmp_eq_u32_e32 vcc, -1, v251\n\t"
      "s_cbranch_vccnz PQ5L_%=\n\t"
      "PQ5X_%=:\n\t"
      "s_add_u32 s58, s74, s40\n\t"
      "s_addc_u32 s59, s75, 0\n\t"
      "global_load_dwordx4 v[132:135], v244, s[58:59] sc1\n\t"
      "s_add_u32 s58, s74, s41\n\t"
      "s_addc_u32 s59, s75, 0\n\t"
      "global_load_dwordx4 v[136:139], v244, s[58:59] sc1\n\t"
      "s_add_u32 s58, s74, s42\n\t"
      "s_addc_u32 s59, s75, 0\n\t"
      "global_load_dwordx4 v[140:143], v244, s[58:59] sc1\n\t"
      "s_add_u32 s58, s74, s43\n\t"
      "s_addc_u32 s59, s75, 0\n\t"
      "global_load_dwordx4 v[144:147], v244, s[58:59] sc1\n\t"
      "s_add_u32 s58, s74, s44\n\t"
      "s_addc_u32 s59, s75, 0\n\t"
      "global_load_dwordx4 v[148:151], v244, s[58:59] sc1\n\t"
      "s_add_u32 s58, s74, s45\n\t"
      "s_addc_u32 s59, s75, 0\n\t"
      "global_load_dwordx4 v[152:155], v244, s[58:59] sc1\n\t"
      "s_add_u32 s58, s74, s46\n\t"
      "s_addc_u32 s59, s75, 0\n\t"
      "global_load_dwordx4 v[156:159], v244, s[58:59] sc1\n\t"
      "s_waitcnt vmcnt(0)\n\t"
      "v_max3_u32 v251, v132, v133, v134\n\t"
      "v_max3_u32 v251, v251, v135, v136\n\t"
      "v_max3_u32 v251, v251, v137, v138\n\t"
      "v_max3_u32 v251, v251, v139, v140\n\t"
      "v_max3_u32 v251, v251, v141, v142\n\t"
      "v_max3_u32 v251, v251, v143, v144\n\t"
      "v_max3_u32 v251, v251, v145, v146\n\t"
      "v_max3_u32 v251, v251, v147, v148\n\t"
      "v_max3_u32 v251, v251, v149, v150\n\t"
      "v_max3_u32 v251, v251, v151, v152\n\t"
      "v_max3_u32 v251, v251, v153, v154\n\t"
      "v_max3_u32 v251, v251, v155, v156\n\t"
      "v_max3_u32 v251, v251, v157, v158\n\t"
      "v_max_u32_e32 v251, v251, v159\n\t"
      "v_cmp_eq_u32_e32 vcc, -1, v251\n\t"
      "s_cbranch_vccnz LA45L_%=\n\t"
      "LA45X_%=:\n\t"
      "v_mfma_f32_16x16x32_bf16 v[212:215], v[132:135], v[0:3], 0\n\t"
      "v_mfma_f32_16x16x32_bf16 v[216:219], v[132:135], v[4:7], 0\n\t"
      "v_mfma_f32_16x16x32_bf16 v[220:223], v[132:135], v[8:11], 0\n\t"
      "v_mfma_f32_16x16x32_bf16 v[224:227], v[132:135], v[12:15], 0\n\t"
      "v_mfma_f32_16x16x32_bf16 v[212:215], v[136:139], v[16:19], v[212:215]\n\t"
      "v_mfma_f32_16x16x32_bf16 v[216:219], v[136:139], v[20:23], v[216:219]\n\t"
      "v_mfma_f32_16x16x32_bf16 v[220:223], v[136:139], v[24:27], v[220:223]\n\t"
      "v_mfma_f32_16x16x32_bf16 v[224:227], v[136:139], v[28:31], v[224:227]\n\t"
      "v_mfma_f32_16x16x32_bf16 v[212:215], v[140:143], v[32:35], v[212:215]\n\t"
      "v_mfma_f32_16x16x32_bf16 v[216:219], v[140:143], v[36:39], v[216:219]\n\t"
      "v_mfma_f32_16x16x32_bf16 v[220:223], v[140:143], v[40:43], v[220:223]\n\t"
      "v_mfma_f32_16x16x32_bf16 v[224:227], v[140:143], v[44:47], v[224:227]\n\t"
      "v_mfma_f32_16x16x32_bf16 v[212:215], v[144:147], v[48:51], v[212:215]\n\t"
      "v_mfma_f32_16x16x32_bf16 v[216:219], v[144:147], v[52:55], v[216:219]\n\t"
      "v_mfma_f32_16x16x32_bf16 v[220:223], v[144:147], v[56:59], v[220:223]\n\t"
      "v_mfma_f32_16x16x32_bf16 v[224:227], v[144:147], v[60:63], v[224:227]\n\t"
      "v_mfma_f32_16x16x32_bf16 v[212:215], v[148:151], v[64:67], v[212:215]\n\t"
      "v_mfma_f32_16x16x32_bf16 v[216:219], v[148:151], v[68:71], v[216:219]\n\t"
      "v_mfma_f32_16x16x32_bf16 v[220:223], v[148:151], v[72:75], v[220:223]\n\t"
      "v_mfma_f32_16x16x32_bf16 v[224:227], v[148:151], v[76:79], v[224:227]\n\t"
      "v_mfma_f32_16x16x32_bf16 v[212:215], v[152:155], v[80:83], v[212:215]\n\t"
      "v_mfma_f32_16x16x32_bf16 v[216:219], v[152:155], v[84:87], v[216:219]\n\t"
      "v_mfma_f32_16x16x32_bf16 v[220:223], v[152:155], v[88:91], v[220:223]\n\t"
      "v_mfma_f32_16x16x32_bf16 v[224:227], v[152:155], v[92:95], v[224:227]\n\t"
      "v_mfma_f32_16x16x32_bf16 v[212:215], v[156:159], v[96:99], v[212:215]\n\t"
      "v_mfma_f32_16x16x32_bf16 v[216:219], v[156:159], v[100:103], v[216:219]\n\t"
      "v_mfma_f32_16x16x32_bf16 v[220:223], v[156:159], v[104:107], v[220:223]\n\t"
      "v_mfma_f32_16x16x32_bf16 v[224:227], v[156:159], v[108:111], v[224:227]\n\t"
      "s_nop 7\n\t"
      "s_branch LA45B_%=\n\t"
      "LA46R_%=:\n\t"
      "s_mov_b32 s80, 0\n\t"
      "LA46L_%=:\n\t"
      "s_add_u32 s80, s80, 1\n\t"
      "s_cmp_lt_u32 s80, 262144\n\t"
      "s_cbranch_scc0 LA46X_%=\n\t"
      "s_waitcnt vmcnt(0)\n\t"
      "PQ6R_%=:\n\t"
      "PQ6L_%=:\n\t"
      "s_add_u32 s80, s80, 1\n\t"
      "s_cmp_lt_u32 s80, 262144\n\t"
      "s_cbranch_scc0 PQ6X_%=\n\t"
      "s_sleep 1\n\t"
      "global_load_dword v251, v252, s[76:77] offset:252 sc1\n\t"
      "s_waitcnt vmcnt(0)\n\t"
      "v_cmp_eq_u32_e32 vcc, -1, v251\n\t"
      "s_cbranch_vccnz PQ6L_%=\n\t"
      "PQ6X_%=:\n\t"
      "s_add_u32 s58, s76, s40\n\t"
      "s_addc_u32 s59, s77, 0\n\t"
      "global_load_dwordx4 v[160:163], v244, s[58:59] sc1\n\t"
      "s_add_u32 s58, s76, s41\n\t"
      "s_addc_u32 s59, s77, 0\n\t"
      "global_load_dwordx4 v[164:167], v244, s[58:59] sc1\n\t"
      "s_add_u32 s58, s76, s42\n\t"
      "s_addc_u32 s59, s77, 0\n\t"
      "global_load_dwordx4 v[168:171], v244, s[58:59] sc1\n\t"
      "s_add_u32 s58, s76, s43\n\t"
      "s_addc_u32 s59, s77, 0\n\t"
      "global_load_dwordx4 v[172:175], v244, s[58:59] sc1\n\t"
      "s_add_u32 s58, s76, s44\n\t"
      "s_addc_u32 s59, s77, 0\n\t"
      "global_load_dwordx4 v[176:179], v244, s[58:59] sc1\n\t"
      "s_add_u32 s58, s76, s45\n\t"
      "s_addc_u32 s59, s77, 0\n\t"
      "global_load_dwordx4 v[180:183], v244, s[58:59] sc1\n\t"
      "s_add_u32 s58, s76, s46\n\t"
      "s_addc_u32 s59, s77, 0\n\t"
      "global_load_dwordx4 v[184:187], v244, s[58:59] sc1\n\t"
      "s_waitcnt vmcnt(0)\n\t"
      "v_max3_u32 v251, v160, v161, v162\n\t"
      "v_max3_u32 v251, v251, v163, v164\n\t"
      "v_max3_u32 v251, v251, v165, v166\n\t"
      "v_max3_u32 v251, v251, v167, v168\n\t"
      "v_max3_u32 v251, v251, v169, v170\n\t"
      "v_max3_u32 v251, v251, v171, v172\n\t"
      "v_max3_u32 v251, v251, v173, v174\n\t"
      "v_max3_u32 v251, v251, v175, v176\n\t"
      "v_max3_u32 v251, v251, v177, v178\n\t"
      "v_max3_u32 v251, v251, v179, v180\n\t"
      "v_max3_u32 v251, v251, v181, v182\n\t"
      "v_max3_u32 v251, v251, v183, v184\n\t"
      "v_max3_u32 v251, v251, v185, v186\n\t"
      "v_max_u32_e32 v251, v251, v187\n\t"
      "v_cmp_eq_u32_e32 vcc, -1, v251\n\t"
      "s_cbranch_vccnz LA46L_%=\n\t"
      "LA46X_%=:\n\t"
      "v_mfma_f32_16x16x32_bf16 v[196:199], v[160:163], v[0:3], 0\n\t"
      "v_mfma_f32_16x16x32_bf16 v[200:203], v[160:163], v[4:7], 0\n\t"
      "v_mfma_f32_16x16x32_bf16 v[204:207], v[160:163], v[8:11], 0\n\t"
      "v_mfma_f32_16x16x32_bf16 v[208:211], v[160:163], v[12:15], 0\n\t"
      "v_mfma_f32_16x16x32_bf16 v[196:199], v[164:167], v[16:19], v[196:199]\n\t"
      "v_mfma_f32_16x16x32_bf16 v[200:203], v[164:167], v[20:23], v[200:203]\n\t"
      "v_mfma_f32_16x16x32_bf16 v[204:207], v[164:167], v[24:27], v[204:207]\n\t"
      "v_mfma_f32_16x16x32_bf16 v[208:211], v[164:167], v[28:31], v[208:211]\n\t"
      "v_mfma_f32_16x16x32_bf16 v[196:199], v[168:171], v[32:35], v[196:199]\n\t"
      "v_mfma_f32_16x16x32_bf16 v[200:203], v[168:171], v[36:39], v[200:203]\n\t"
      "v_mfma_f32_16x16x32_bf16 v[204:207], v[168:171], v[40:43], v[204:207]\n\t"
      "v_mfma_f32_16x16x32_bf16 v[208:211], v[168:171], v[44:47], v[208:211]\n\t"
      "v_mfma_f32_16x16x32_bf16 v[196:199], v[172:175], v[48:51], v[196:199]\n\t"
      "v_mfma_f32_16x16x32_bf16 v[200:203], v[172:175], v[52:55], v[200:203]\n\t"
      "v_mfma_f32_16x16x32_bf16 v[204:207], v[172:175], v[56:59], v[204:207]\n\t"
      "v_mfma_f32_16x16x32_bf16 v[208:211], v[172:175], v[60:63], v[208:211]\n\t"
      "v_mfma_f32_16x16x32_bf16 v[196:199], v[176:179], v[64:67], v[196:199]\n\t"
      "v_mfma_f32_16x16x32_bf16 v[200:203], v[176:179], v[68:71], v[200:203]\n\t"
      "v_mfma_f32_16x16x32_bf16 v[204:207], v[176:179], v[72:75], v[204:207]\n\t"
      "v_mfma_f32_16x16x32_bf16 v[208:211], v[176:179], v[76:79], v[208:211]\n\t"
      "v_mfma_f32_16x16x32_bf16 v[196:199], v[180:183], v[80:83], v[196:199]\n\t"
      "v_mfma_f32_16x16x32_bf16 v[200:203], v[180:183], v[84:87], v[200:203]\n\t"
      "v_mfma_f32_16x16x32_bf16 v[204:207], v[180:183], v[88:91], v[204:207]\n\t"
      "v_mfma_f32_16x16x32_bf16 v[208:211], v[180:183], v[92:95], v[208:211]\n\t"
      "v_mfma_f32_16x16x32_bf16 v[196:199], v[184:187], v[96:99], v[196:199]\n\t"
      "v_mfma_f32_16x16x32_bf16 v[200:203], v[184:187], v[100:103], v[200:203]\n\t"
      "v_mfma_f32_16x16x32_bf16 v[204:207], v[184:187], v[104:107], v[204:207]\n\t"
      "v_mfma_f32_16x16x32_bf16 v[208:211], v[184:187], v[108:111], v[208:211]\n\t"
      "s_nop 7\n\t"
      "s_branch LA46B_%=\n\t"
      : "+{s[0:1]}"(ka), "+{v0}"(tid)
      : "{s2}"(blockIdx.x)
      : "memory", "vcc", "scc", "m0",
        "v1", "v2", "v3", "v4", "v5", "v6", "v7", "v8", "v9", "v10", "v11", "v12", "v13", "v14", "v15", "v16", "v17",
        "v18", "v19", "v20", "v21", "v22", "v23", "v24", "v25", "v26", "v27", "v28", "v29", "v30", "v31", "v32", "v33",
        "v34", "v35", "v36", "v37", "v38", "v39", "v40", "v41", "v42", "v43", "v44", "v45", "v46", "v47", "v48", "v49",
        "v50", "v51", "v52", "v53", "v54", "v55", "v56", "v57", "v58", "v59", "v60", "v61", "v62", "v63", "v64", "v65",
        "v66", "v67", "v68", "v69", "v70", "v71", "v72", "v73", "v74", "v75", "v76", "v77", "v78", "v79", "v80", "v81",
        "v82", "v83", "v84", "v85", "v86", "v87", "v88", "v89", "v90", "v91", "v92", "v93", "v94", "v95", "v96", "v97",
        "v98", "v99", "v100", "v101", "v102", "v103", "v104", "v105", "v106", "v107", "v108", "v109", "v110", "v111",
        "v112", "v113", "v114", "v115", "v116", "v117", "v118", "v119", "v120", "v121", "v122", "v123", "v124", "v125",
        "v126", "v127", "v128", "v129", "v130", "v131", "v132", "v133", "v134", "v135", "v136", "v137", "v138", "v139",
        "v140", "v141", "v142", "v143", "v144", "v145", "v146", "v147", "v148", "v149", "v150", "v151", "v152", "v153",
        "v154", "v155", "v156", "v157", "v158", "v159", "v160", "v161", "v162", "v163", "v164", "v165", "v166", "v167",
        "v168", "v169", "v170", "v171", "v172", "v173", "v174", "v175", "v176", "v177", "v178", "v179", "v180", "v181",
        "v182", "v183", "v184", "v185", "v186", "v187", "v188", "v189", "v190", "v191", "v192", "v193", "v194", "v195",
        "v196", "v197", "v198", "v199", "v200", "v201", "v202", "v203", "v204", "v205", "v206", "v207", "v208", "v209",
        "v210", "v211", "v212", "v213", "v214", "v215", "v216", "v217", "v218", "v219", "v220", "v221", "v222", "v223",
        "v224", "v225", "v226", "v227", "v228", "v229", "v230", "v231", "v232", "v233", "v234", "v235", "v236", "v237",
        "v238", "v239", "v240", "v241", "v242", "v243", "v244", "v245", "v246", "v247", "v248", "v249", "v250", "v251",
        "v252", "v253", "v254", "v255", "s3", "s4", "s5", "s6", "s7", "s8", "s9", "s10", "s11", "s12", "s13", "s14",
        "s15", "s16", "s17", "s18", "s19", "s20", "s21", "s22", "s23", "s24", "s25", "s26", "s27", "s28", "s29", "s30",
        "s31", "s32", "s33", "s34", "s35", "s36", "s37", "s38", "s39", "s40", "s41", "s42", "s43", "s44", "s45", "s46",
        "s47", "s48", "s49", "s50", "s51", "s52", "s53", "s54", "s55", "s56", "s57", "s58", "s59", "s60", "s61", "s62",
        "s63", "s64", "s65", "s66", "s67", "s68", "s69", "s70", "s71", "s72", "s73", "s74", "s75", "s76", "s77", "s78",
        "s79", "s80", "s81", "s82", "s83"
  );
#endif
}

extern "C" void kernel_launch(void* const* d_in, const int* in_sizes, int n_in, void* d_out, int out_size,
                              void* d_ws, size_t ws_size, hipStream_t stream) {
  (void)hipFuncSetAttribute((const void*)fused_block, hipFuncAttributeMaxDynamicSharedMemorySize, 163840);
  fused_block<<<256, 512, 163840, stream>>>((const u16*)d_in[0], (const u16*)d_in[1], (const u16*)d_in[2],
                                            (const u16*)d_in[4], (const u16*)d_in[5], (const u16*)d_in[6],
                                            (const u16*)d_in[7], (u16*)d_out, (char*)d_ws);
}
```

```cpp
#include <hip/hip_runtime.h>
#include <stdint.h>
typedef unsigned short u16;
__global__ __launch_bounds__(512, 2) void fused_block(const u16* x, const u16* wqkv, const u16* wo, const u16* w1, const u16* b1,
                                                   const u16* w2, const u16* b2, u16* out, char* ws) {
#if defined(__HIP_DEVICE_COMPILE__)
  unsigned long long ka = (unsigned long long)__builtin_amdgcn_kernarg_segment_ptr();
  unsigned tid = threadIdx.x;
  asm volatile(
      "s_load_dwordx16 s[4:19], s[0:1], 0x0\n\t"
      "s_load_dwordx2 s[20:21], s[0:1], 0x40\n\t"
      "v_lshrrev_b32_e32 v199, 6, v0\n\t"
      "v_and_b32_e32 v192, 63, v0\n\t"
      "v_readfirstlane_b32 s24, v199\n\t"
      "s_and_b32 s34, s2, 7\n\t"
      "s_lshr_b32 s36, s2, 3\n\t"
      "s_mul_i32 s35, s36, 43\n\t"
      "s_lshr_b32 s35, s35, 8\n\t"
      "s_mul_i32 s26, s35, 6\n\t"
      "s_sub_u32 s26, s36, s26\n\t"
      "s_add_u32 s26, s26, 1\n\t"
      "s_bfe_u32 s25, s2, 131076\n\t"
      "s_and_b32 s37, s36, 1\n\t"
      "s_mul_i32 s37, s37, 7\n\t"
      "s_cmp_ge_u32 s36, 24\n\t"
      "s_cselect_b32 s26, s37, s26\n\t"
      "s_cselect_b32 s35, s25, s35\n\t"
      "s_lshl2_add_u32 s25, s34, s35\n\t"
      "s_and_b32 s34, s25, 30\n\t"
      "s_lshl_b32 s34, s34, 6\n\t"
      "s_and_b32 s35, s25, 1\n\t"
      "s_add_u32 s38, s34, s35\n\t"
      "v_lshrrev_b32_e32 v197, 3, v192\n\t"
      "v_and_b32_e32 v198, 7, v192\n\t"
      "s_min_u32 s28, s26, 6\n\t"
      "s_lshl4_add_u32 s34, s24, s38\n\t"
      "s_mul_i32 s34, s34, 896\n\t"
      "s_movk_i32 s35, 1792\n\t"
      "v_lshl_add_u32 v199, v198, 4, s34\n\t"
      "v_mad_u32_u24 v199, v197, s35, v199\n\t"
      "s_lshl_b32 s34, s28, 7\n\t"
      "s_cmp_lg_u32 s24, 0\n\t"
      "s_cbranch_scc1 NTC_%=\n\t"
      "s_getpc_b64 s[58:59]\n\t"
      "s_lshr_b32 s37, s2, 3\n\t"
      "v_min_u32_e32 v200, 9, v192\n\t"
      "v_lshl_add_u32 v200, v200, 5, s37\n\t"
      "s_mov_b32 m0, 64512\n\t"
      "v_min_u32_e32 v200, 288, v200\n\t"
      "v_lshlrev_b32_e32 v200, 7, v200\n\t"
      "global_load_lds_dword v200, s[58:59]\n\t"
      "NTC_%=:\n\t"
      "v_add_u32_e32 v120, s34, v199\n\t"
      "s_add_u32 s35, s34, 128\n\t"
      "s_sub_u32 s36, s35, 896\n\t"
      "s_min_u32 s35, s35, s36\n\t"
      "v_add_u32_e32 v121, s35, v199\n\t"
      "s_waitcnt lgkmcnt(0)\n\t"
      "global_load_dwordx4 v[204:207], v120, s[4:5]\n\t"
      "s_add_u32 s35, s34, 256\n\t"
      "s_sub_u32 s36, s35, 896\n\t"
      "s_min_u32 s35, s35, s36\n\t"
      "v_add_u32_e32 v122, s35, v199\n\t"
      "global_load_dwordx4 v[4:7], v121, s[4:5]\n\t"
      "s_add_u32 s35, s34, 384\n\t"
      "s_sub_u32 s36, s35, 896\n\t"
      "s_min_u32 s35, s35, s36\n\t"
      "v_add_u32_e32 v123, s35, v199\n\t"
      "global_load_dwordx4 v[8:11], v122, s[4:5]\n\t"
      "s_add_u32 s35, s34, 512\n\t"
      "s_sub_u32 s36, s35, 896\n\t"
      "s_min_u32 s35, s35, s36\n\t"
      "v_add_u32_e32 v124, s35, v199\n\t"
      "global_load_dwordx4 v[12:15], v123, s[4:5]\n\t"
      "s_add_u32 s35, s34, 640\n\t"
      "s_sub_u32 s36, s35, 896\n\t"
      "s_min_u32 s35, s35, s36\n\t"
      "v_add_u32_e32 v125, s35, v199\n\t"
      "global_load_dwordx4 v[16:19], v124, s[4:5]\n\t"
      "s_add_u32 s35, s34, 768\n\t"
      "s_sub_u32 s36, s35, 896\n\t"
      "s_min_u32 s35, s35, s36\n\t"
      "v_add_u32_e32 v126, s35, v199\n\t"
      "global_load_dwordx4 v[20:23], v125, s[4:5]\n\t"
      "s_cmp_lt_u32 s25, 2\n\t"
      "s_cselect_b32 s34, 0, 16\n\t"
      "s_sub_u32 s34, s38, s34\n\t"
      "s_lshl1_add_u32 s34, s24, s34\n\t"
      "s_mul_i32 s34, s34, 896\n\t"
      "s_and_b32 s33, s25, 3\n\t"
      "s_lshl_b32 s33, s33, 1\n\t"
      "s_and_b32 s35, s26, 1\n\t"
      "s_add_u32 s33, s33, s35\n\t"
      "s_cmp_eq_u32 s33, 7\n\t"
      "s_cselect_b32 s33, 0, s33\n\t"
      "v_min_u32_e32 v200, 55, v192\n\t"
      "s_lshl_b32 s35, s33, 3\n\t"
      "v_add_u32_e32 v201, s35, v200\n\t"
      "v_subrev_u32_e32 v202, 56, v201\n\t"
      "v_min_u32_e32 v201, v201, v202\n\t"
      "v_lshl_add_u32 v127, v201, 4, s34\n\t"
      "s_and_b32 s39, s24, 3\n\t"
      "s_lshl_b32 s39, s39, 12\n\t"
      "s_cmp_lt_u32 s24, 4\n\t"
      "s_cselect_b32 s27, 4, 0\n\t"
      "v_lshrrev_b32_e32 v193, 4, v192\n\t"
      "v_and_b32_e32 v194, 15, v192\n\t"
      "v_bfe_u32 v195, v192, 2, 2\n\t"
      "v_and_b32_e32 v196, 3, v192\n\t"
      "v_lshlrev_b32_e32 v244, 4, v192\n\t"
      "s_lshr_b32 s34, s24, 1\n\t"
      "s_mul_i32 s34, s34, 14336\n\t"
      "s_and_b32 s35, s24, 1\n\t"
      "s_lshl_b32 s35, s35, 7\n\t"
      "s_add_u32 s34, s34, s35\n\t"
      "v_xor_b32_e32 v199, v197, v198\n\t"
      "v_lshl_add_u32 v199, v199, 4, s34\n\t"
      "v_lshl_add_u32 v199, v198, 8, v199\n\t"
      "s_sub_u32 s35, s28, s33\n\t"
      "s_cselect_b32 s36, 7, 0\n\t"
      "s_add_u32 s35, s35, s36\n\t"
      "s_lshl_b32 s35, s35, 11\n\t"
      "s_add_u32 s34, s35, 0\n\t"
      "s_sub_u32 s36, s34, 14336\n\t"
      "s_min_u32 s34, s34, s36\n\t"
      "v_add_u32_e32 v112, s34, v199\n\t"
      "s_add_u32 s34, s35, 2048\n\t"
      "s_sub_u32 s36, s34, 14336\n\t"
      "s_min_u32 s34, s34, s36\n\t"
      "v_add_u32_e32 v113, s34, v199\n\t"
      "s_add_u32 s34, s35, 4096\n\t"
      "s_sub_u32 s36, s34, 14336\n\t"
      "s_min_u32 s34, s34, s36\n\t"
      "v_add_u32_e32 v114, s34, v199\n\t"
      "s_add_u32 s34, s35, 6144\n\t"
      "s_sub_u32 s36, s34, 14336\n\t"
      "s_min_u32 s34, s34, s36\n\t"
      "v_add_u32_e32 v115, s34, v199\n\t"
      "s_add_u32 s34, s35, 8192\n\t"
      "s_sub_u32 s36, s34, 14336\n\t"
      "s_min_u32 s34, s34, s36\n\t"
      "v_add_u32_e32 v116, s34, v199\n\t"
      "s_add_u32 s34, s35, 10240\n\t"
      "s_sub_u32 s36, s34, 14336\n\t"
      "s_min_u32 s34, s34, s36\n\t"
      "v_add_u32_e32 v117, s34, v199\n\t"
      "s_add_u32 s34, s35, 12288\n\t"
      "s_sub_u32 s36, s34, 14336\n\t"
      "s_min_u32 s34, s34, s36\n\t"
      "v_add_u32_e32 v118, s34, v199\n\t"
      "v_min_u32_e32 v200, 55, v192\n\t"
      "v_lshrrev_b32_e32 v201, 2, v200\n\t"
      "v_lshlrev_b32_e32 v119, 10, v201\n\t"
      "v_cmp_lt_u32_e32 vcc, 6, v201\n\t"
      "v_mov_b32_e32 v202, 57344\n\t"
      "v_mov_b32_e32 v201, 50688\n\t"
      "v_cndmask_b32_e32 v202, v202, v201, vcc\n\t"
      "v_and_b32_e32 v201, 3, v200\n\t"
      "v_lshl_add_u32 v202, v201, 7, v202\n\t"
      "v_and_b32_e32 v200, 7, v200\n\t"
      "v_xor_b32_e32 v200, s24, v200\n\t"
      "v_lshl_add_u32 v202, v200, 4, v202\n\t"
      "v_add_u32_e32 v119, v119, v202\n\t"
      "s_bitcmp1_b32 s27, 2\n\t"
      "s_cbranch_scc1 LSET_%=\n\t"
      "v_xor_b32_e32 v199, v194, v193\n\t"
      "v_lshlrev_b32_e32 v199, 4, v199\n\t"
      "v_lshl_or_b32 v200, v193, 8, v199\n\t"
      "s_and_b32 s34, s24, 1\n\t"
      "s_mul_i32 s35, s34, 28672\n\t"
      "v_add_u32_e32 v160, s35, v200\n\t"
      "v_xor_b32_e32 v161, 64, v160\n\t"
      "v_and_b32_e32 v199, 0x70, v199\n\t"
      "v_lshl_or_b32 v199, v193, 7, v199\n\t"
      "v_add_u32_e32 v199, 57344, v199\n\t"
      "v_add_u32_e32 v200, 14336, v200\n\t"
      "s_cmp_eq_u32 s34, 1\n\t"
      "s_cselect_b64 vcc, -1, 0\n\t"
      "s_cselect_b32 s35, 0, 6656\n\t"
      "v_cndmask_b32_e32 v162, v199, v200, vcc\n\t"
      "v_xor_b32_e32 v163, 64, v162\n\t"
      "v_subrev_u32_e32 v164, s35, v162\n\t"
      "v_subrev_u32_e32 v165, s35, v163\n\t"
      "s_bfe_u32 s35, s24, 65537\n\t"
      "s_mul_i32 s35, s35, 7\n\t"
      "v_and_b32_e32 v201, 24, v192\n\t"
      "v_lshlrev_b32_e32 v201, 2, v201\n\t"
      "v_lshlrev_b32_e32 v202, 7, v195\n\t"
      "v_lshl_or_b32 v202, v193, 12, v202\n\t"
      "v_add_u32_e32 v202, 65536, v202\n\t"
      "v_add_lshl_u32 v199, v196, s35, 3\n\t"
      "v_xor_b32_e32 v199, v199, v201\n\t"
      "v_add_u32_e32 v166, v199, v202\n\t"
      "v_min_u32_e32 v199, 2, v196\n\t"
      "s_add_u32 s35, s35, 4\n\t"
      "v_add_lshl_u32 v199, v199, s35, 3\n\t"
      "v_xor_b32_e32 v199, v199, v201\n\t"
      "v_add_u32_e32 v167, v199, v202\n\t"
      "v_add_u32_e32 v168, 0x10000, v166\n\t"
      "v_add_u32_e32 v169, 0x10000, v167\n\t"
      "v_lshlrev_b32_e32 v199, 11, v193\n\t"
      "v_sub_u32_e32 v170, v166, v199\n\t"
      "v_sub_u32_e32 v171, v167, v199\n\t"
      "s_branch NORM_%=\n\t"
      "LSET_%=:\n\t"
      "s_add_u32 s60, s20, 16777216\n\t"
      "s_addc_u32 s61, s21, 0\n\t"
      "s_load_dwordx2 s[22:23], s[60:61], 0x0\n\t"
      "s_lshr_b32 s34, s39, 9\n\t"
      "v_add_u32_e32 v199, s34, v197\n\t"
      "s_and_b32 s34, s24, 1\n\t"
      "s_lshl_b32 s34, s34, 2\n\t"
      "v_and_b32_e32 v200, 2, v197\n\t"
      "v_or_b32_e32 v200, s34, v200\n\t"
      "v_xor_b32_e32 v200, v200, v198\n\t"
      "v_min_u32_e32 v200, 6, v200\n\t"
      "v_lshlrev_b32_e32 v200, 4, v200\n\t"
      "s_movk_i32 s34, 2688\n\t"
      "v_mad_u32_u24 v149, v199, s34, v200\n\t"
      "v_add_u32_e32 v148, 128, v149\n\t"
      "v_add_u32_e32 v150, 84096, v149\n\t"
      "v_add_u32_e32 v151, 83968, v149\n\t"
      "v_add_u32_e32 v152, 85120, v149\n\t"
      "s_sub_u32 s6, s6, 128\n\t"
      "s_subb_u32 s7, s7, 0\n\t"
      "s_mul_i32 s37, s26, 112\n\t"
      "s_mov_b32 s81, s33\n\t"
      "s_mov_b32 s82, s33\n\t"
      "s_and_b32 s35, s25, 3\n\t"
      "s_lshl2_add_u32 s31, s26, s35\n\t"
      "s_and_b32 s35, s2, 7\n\t"
      "s_mul_i32 s34, s35, 14\n\t"
      "s_lshl4_add_u32 s35, s35, -14\n\t"
      "s_cmp_lt_u32 s31, 14\n\t"
      "s_cselect_b32 s34, s34, s35\n\t"
      "s_cselect_b32 s36, 1, 0\n\t"
      "s_or_b32 s27, s27, s36\n\t"
      "s_add_u32 s34, s34, s31\n\t"
      "s_lshr_b32 s29, s34, 2\n\t"
      "s_and_b32 s32, s34, 3\n\t"
      "s_mul_i32 s30, s32, 7\n\t"
      "v_and_b32_e32 v199, 60, v192\n\t"
      "v_and_b32_e32 v200, 24, v192\n\t"
      "v_lshlrev_b32_e32 v202, 3, v196\n\t"
      "v_lshl_or_b32 v202, v200, 2, v202\n\t"
      "v_lshl_or_b32 v202, v195, 7, v202\n\t"
      "v_lshl_or_b32 v202, v193, 10, v202\n\t"
      "v_add_u32_e32 v224, s39, v202\n\t"
      "v_xor_b32_e32 v225, 32, v224\n\t"
      "v_xor_b32_e32 v226, 64, v224\n\t"
      "v_xor_b32_e32 v227, 96, v224\n\t"
      "s_lshl_b32 s34, s24, 12\n\t"
      "s_add_u32 s34, s34, 98304\n\t"
      "v_add_u32_e32 v245, s34, v244\n\t"
      "v_lshlrev_b32_e32 v199, 2, v199\n\t"
      "s_lshl_b32 s34, s24, 8\n\t"
      "v_lshl_add_u32 v247, v192, 2, s34\n\t"
      "v_and_b32_e32 v200, 2, v192\n\t"
      "v_mul_u32_u24_e32 v202, 576, v200\n\t"
      "v_xor_b32_e32 v199, v202, v199\n\t"
      "v_and_b32_e32 v202, 1, v192\n\t"
      "v_lshl_or_b32 v199, v202, 3, v199\n\t"
      "s_and_b32 s35, s34, 0x400\n\t"
      "s_add_u32 s34, s34, s35\n\t"
      "s_add_u32 s34, s34, 98304\n\t"
      "v_add_u32_e32 v246, s34, v199\n\t"
      "v_lshlrev_b32_e32 v199, 2, v202\n\t"
      "v_lshl_or_b32 v199, v200, 4, v199\n\t"
      "s_min_u32 s34, s29, 27\n\t"
      "s_lshl_b32 s34, s34, 7\n\t"
      "s_lshr_b32 s35, s24, 2\n\t"
      "s_lshl_b32 s35, s35, 6\n\t"
      "s_add_u32 s34, s34, s35\n\t"
      "s_lshr_b32 s35, s39, 9\n\t"
      "s_add_u32 s34, s34, s35\n\t"
      "v_add_u32_e32 v248, s34, v199\n\t"
      "s_add_u32 s48, s20, 8388608\n\t"
      "s_addc_u32 s49, s21, 0\n\t"
      "v_and_b32_e32 v199, 2, v197\n\t"
      "v_xor_b32_e32 v199, v199, v198\n\t"
      "v_lshlrev_b32_e32 v199, 4, v199\n\t"
      "s_movk_i32 s34, 7168\n\t"
      "v_mad_u32_u24 v144, v197, s34, v199\n\t"
      "v_xor_b32_e32 v199, 64, v144\n\t"
      "v_add_u32_e32 v145, 56320, v199\n\t"
      "v_add_u32_e32 v146, 112640, v144\n\t"
      "v_add_u32_e32 v147, 168960, v199\n\t"
      "v_and_b32_e32 v199, 6, v197\n\t"
      "v_xor_b32_e32 v199, v199, v198\n\t"
      "v_lshlrev_b32_e32 v199, 4, v199\n\t"
      "s_movk_i32 s35, 896\n\t"
      "v_mad_u32_u24 v154, v197, s35, v199\n\t"
      "v_add_u32_e32 v155, 6144, v154\n\t"
      "v_add_u32_e32 v156, 12288, v154\n\t"
      "v_add_u32_e32 v157, 18432, v154\n\t"
      "s_mul_i32 s34, s24, 917504\n\t"
      "s_lshl_b32 s35, s29, 7\n\t"
      "s_add_u32 s34, s34, s35\n\t"
      "s_add_u32 s52, s10, s34\n\t"
      "s_addc_u32 s53, s11, 0\n\t"
      "s_lshl_b32 s34, s28, 7\n\t"
      "s_add_u32 s56, s8, s34\n\t"
      "s_addc_u32 s57, s9, 0\n\t"
      "s_mov_b32 s34, 0\n\t"
      "s_and_b32 s35, s34, 1\n\t"
      "s_and_b32 s34, s34, 2\n\t"
      "s_lshl_b32 s34, s34, 6\n\t"
      "s_add_u32 s34, s34, s35\n\t"
      "s_mul_i32 s76, s34, 3584\n\t"
      "s_mov_b32 s34, 1\n\t"
      "s_and_b32 s35, s34, 1\n\t"
      "s_and_b32 s34, s34, 2\n\t"
      "s_lshl_b32 s34, s34, 6\n\t"
      "s_add_u32 s34, s34, s35\n\t"
      "s_mul_i32 s77, s34, 3584\n\t"
      "s_mov_b32 s34, 2\n\t"
      "s_and_b32 s35, s34, 1\n\t"
      "s_and_b32 s34, s34, 2\n\t"
      "s_lshl_b32 s34, s34, 6\n\t"
      "s_add_u32 s34, s34, s35\n\t"
      "s_mul_i32 s78, s34, 3584\n\t"
      "s_mov_b32 s34, 3\n\t"
      "s_and_b32 s35, s34, 1\n\t"
      "s_and_b32 s34, s34, 2\n\t"
      "s_lshl_b32 s34, s34, 6\n\t"
      "s_add_u32 s34, s34, s35\n\t"
      "s_mul_i32 s79, s34, 3584\n\t"
      "NORM_%=:\n\t"
      "global_load_dwordx4 v[24:27], v126, s[4:5]\n\t"
      "global_load_dwordx4 v[28:31], v127, s[4:5]\n\t"
      "v_mov_b32_e32 v199, 0x3f803f80\n\t"
      "s_waitcnt vmcnt(7)\n\t"
      "v_dot2_f32_bf16 v88, v204, v199, 0\n\t"
      "v_dot2_f32_bf16 v92, v204, v204, 0\n\t"
      "v_lshlrev_b32_e32 v32, 16, v204\n\t"
      "v_and_b32_e32 v33, 0xffff0000, v204\n\t"
      "v_dot2_f32_bf16 v89, v205, v199, 0\n\t"
      "v_dot2_f32_bf16 v93, v205, v205, 0\n\t"
      "v_lshlrev_b32_e32 v34, 16, v205\n\t"
      "v_and_b32_e32 v35, 0xffff0000, v205\n\t"
      "v_dot2_f32_bf16 v90, v206, v199, 0\n\t"
      "v_dot2_f32_bf16 v94, v206, v206, 0\n\t"
      "v_lshlrev_b32_e32 v36, 16, v206\n\t"
      "v_and_b32_e32 v37, 0xffff0000, v206\n\t"
      "v_dot2_f32_bf16 v91, v207, v199, 0\n\t"
      "v_dot2_f32_bf16 v95, v207, v207, 0\n\t"
      "v_lshlrev_b32_e32 v38, 16, v207\n\t"
      "v_and_b32_e32 v39, 0xffff0000, v207\n\t"
      "s_waitcnt vmcnt(6)\n\t"
      "v_dot2_f32_bf16 v88, v4, v199, v88\n\t"
      "v_dot2_f32_bf16 v92, v4, v4, v92\n\t"
      "v_lshlrev_b32_e32 v40, 16, v4\n\t"
      "v_and_b32_e32 v41, 0xffff0000, v4\n\t"
      "v_dot2_f32_bf16 v89, v5, v199, v89\n\t"
      "v_dot2_f32_bf16 v93, v5, v5, v93\n\t"
      "v_lshlrev_b32_e32 v42, 16, v5\n\t"
      "v_and_b32_e32 v43, 0xffff0000, v5\n\t"
      "v_dot2_f32_bf16 v90, v6, v199, v90\n\t"
      "v_dot2_f32_bf16 v94, v6, v6, v94\n\t"
      "v_lshlrev_b32_e32 v44, 16, v6\n\t"
      "v_and_b32_e32 v45, 0xffff0000, v6\n\t"
      "v_dot2_f32_bf16 v91, v7, v199, v91\n\t"
      "v_dot2_f32_bf16 v95, v7, v7, v95\n\t"
      "v_lshlrev_b32_e32 v46, 16, v7\n\t"
      "v_and_b32_e32 v47, 0xffff0000, v7\n\t"
      "s_waitcnt vmcnt(5)\n\t"
      "v_dot2_f32_bf16 v88, v8, v199, v88\n\t"
      "v_dot2_f32_bf16 v92, v8, v8, v92\n\t"
      "v_lshlrev_b32_e32 v48, 16, v8\n\t"
      "v_and_b32_e32 v49, 0xffff0000, v8\n\t"
      "v_dot2_f32_bf16 v89, v9, v199, v89\n\t"
      "v_dot2_f32_bf16 v93, v9, v9, v93\n\t"
      "v_lshlrev_b32_e32 v50, 16, v9\n\t"
      "v_and_b32_e32 v51, 0xffff0000, v9\n\t"
      "v_dot2_f32_bf16 v90, v10, v199, v90\n\t"
      "v_dot2_f32_bf16 v94, v10, v10, v94\n\t"
      "v_lshlrev_b32_e32 v52, 16, v10\n\t"
      "v_and_b32_e32 v53, 0xffff0000, v10\n\t"
      "v_dot2_f32_bf16 v91, v11, v199, v91\n\t"
      "v_dot2_f32_bf16 v95, v11, v11, v95\n\t"
      "v_lshlrev_b32_e32 v54, 16, v11\n\t"
      "v_and_b32_e32 v55, 0xffff0000, v11\n\t"
      "s_waitcnt vmcnt(4)\n\t"
      "v_dot2_f32_bf16 v88, v12, v199, v88\n\t"
      "v_dot2_f32_bf16 v92, v12, v12, v92\n\t"
      "v_lshlrev_b32_e32 v56, 16, v12\n\t"
      "v_and_b32_e32 v57, 0xffff0000, v12\n\t"
      "v_dot2_f32_bf16 v89, v13, v199, v89\n\t"
      "v_dot2_f32_bf16 v93, v13, v13, v93\n\t"
      "v_lshlrev_b32_e32 v58, 16, v13\n\t"
      "v_and_b32_e32 v59, 0xffff0000, v13\n\t"
      "v_dot2_f32_bf16 v90, v14, v199, v90\n\t"
      "v_dot2_f32_bf16 v94, v14, v14, v94\n\t"
      "v_lshlrev_b32_e32 v60, 16, v14\n\t"
      "v_and_b32_e32 v61, 0xffff0000, v14\n\t"
      "v_dot2_f32_bf16 v91, v15, v199, v91\n\t"
      "v_dot2_f32_bf16 v95, v15, v15, v95\n\t"
      "v_lshlrev_b32_e32 v62, 16, v15\n\t"
      "v_and_b32_e32 v63, 0xffff0000, v15\n\t"
      "s_waitcnt vmcnt(3)\n\t"
      "v_dot2_f32_bf16 v88, v16, v199, v88\n\t"
      "v_dot2_f32_bf16 v92, v16, v16, v92\n\t"
      "v_lshlrev_b32_e32 v64, 16, v16\n\t"
      "v_and_b32_e32 v65, 0xffff0000, v16\n\t"
      "v_dot2_f32_bf16 v89, v17, v199, v89\n\t"
      "v_dot2_f32_bf16 v93, v17, v17, v93\n\t"
      "v_lshlrev_b32_e32 v66, 16, v17\n\t"
      "v_and_b32_e32 v67, 0xffff0000, v17\n\t"
      "v_dot2_f32_bf16 v90, v18, v199, v90\n\t"
      "v_dot2_f32_bf16 v94, v18, v18, v94\n\t"
      "v_lshlrev_b32_e32 v68, 16, v18\n\t"
      "v_and_b32_e32 v69, 0xffff0000, v18\n\t"
      "v_dot2_f32_bf16 v91, v19, v199, v91\n\t"
      "v_dot2_f32_bf16 v95, v19, v19, v95\n\t"
      "v_lshlrev_b32_e32 v70, 16, v19\n\t"
      "v_and_b32_e32 v71, 0xffff0000, v19\n\t"
      "s_waitcnt vmcnt(2)\n\t"
      "v_dot2_f32_bf16 v88, v20, v199, v88\n\t"
      "v_dot2_f32_bf16 v92, v20, v20, v92\n\t"
      "v_lshlrev_b32_e32 v72, 16, v20\n\t"
      "v_and_b32_e32 v73, 0xffff0000, v20\n\t"
      "v_dot2_f32_bf16 v89, v21, v199, v89\n\t"
      "v_dot2_f32_bf16 v93, v21, v21, v93\n\t"
      "v_lshlrev_b32_e32 v74, 16, v21\n\t"
      "v_and_b32_e32 v75, 0xffff0000, v21\n\t"
      "v_dot2_f32_bf16 v90, v22, v199, v90\n\t"
      "v_dot2_f32_bf16 v94, v22, v22, v94\n\t"
      "v_lshlrev_b32_e32 v76, 16, v22\n\t"
      "v_and_b32_e32 v77, 0xffff0000, v22\n\t"
      "v_dot2_f32_bf16 v91, v23, v199, v91\n\t"
      "v_dot2_f32_bf16 v95, v23, v23, v95\n\t"
      "v_lshlrev_b32_e32 v78, 16, v23\n\t"
      "v_and_b32_e32 v79, 0xffff0000, v23\n\t"
      "s_waitcnt vmcnt(1)\n\t"
      "v_dot2_f32_bf16 v88, v24, v199, v88\n\t"
      "v_dot2_f32_bf16 v92, v24, v24, v92\n\t"
      "v_lshlrev_b32_e32 v80, 16, v24\n\t"
      "v_and_b32_e32 v81, 0xffff0000, v24\n\t"
      "v_dot2_f32_bf16 v89, v25, v199, v89\n\t"
      "v_dot2_f32_bf16 v93, v25, v25, v93\n\t"
      "v_lshlrev_b32_e32 v82, 16, v25\n\t"
      "v_and_b32_e32 v83, 0xffff0000, v25\n\t"
      "v_dot2_f32_bf16 v90, v26, v199, v90\n\t"
      "v_dot2_f32_bf16 v94, v26, v26, v94\n\t"
      "v_lshlrev_b32_e32 v84, 16, v26\n\t"
      "v_and_b32_e32 v85, 0xffff0000, v26\n\t"
      "v_dot2_f32_bf16 v91, v27, v199, v91\n\t"
      "v_dot2_f32_bf16 v95, v27, v27, v95\n\t"
      "v_lshlrev_b32_e32 v86, 16, v27\n\t"
      "v_and_b32_e32 v87, 0xffff0000, v27\n\t"
      "s_waitcnt vmcnt(0)\n\t"
      "s_bitcmp0_b32 s27, 2\n\t"
      "s_cbranch_scc1 SK1_%=\n\t"
      "s_mul_i32 s72, s81, 172032\n\t"
      "s_add_u32 s72, s72, s37\n\t"
      "s_add_u32 s40, s6, s72\n\t"
      "s_addc_u32 s41, s7, 0\n\t"
      "s_add_u32 s81, s81, 1\n\t"
      "s_cmp_eq_u32 s81, 7\n\t"
      "s_cselect_b32 s81, 0, s81\n\t"
      "s_add_u32 m0, s39, 131072\n\t"
      "s_nop 0\n\t"
      "global_load_lds_dwordx4 v148, s[40:41]\n\t"
      "global_load_lds_dwordx4 v149, s[40:41] offset:1024\n\t"
      "global_load_lds_dwordx4 v150, s[40:41] offset:2048\n\t"
      "global_load_lds_dwordx4 v151, s[40:41] offset:3072\n\t"
      "SK1_%=:\n\t"
      "v_dot2_f32_bf16 v96, v28, v199, 0\n\t"
      "v_dot2_f32_bf16 v97, v28, v28, 0\n\t"
      "v_dot2_f32_bf16 v96, v29, v199, v96\n\t"
      "v_dot2_f32_bf16 v97, v29, v29, v97\n\t"
      "v_dot2_f32_bf16 v96, v30, v199, v96\n\t"
      "v_dot2_f32_bf16 v97, v30, v30, v97\n\t"
      "v_dot2_f32_bf16 v96, v31, v199, v96\n\t"
      "v_dot2_f32_bf16 v97, v31, v31, v97\n\t"
      "v_cmp_gt_u32_e32 vcc, 56, v192\n\t"
      "v_pk_add_f32 v[88:89], v[88:89], v[90:91]\n\t"
      "v_pk_add_f32 v[92:93], v[92:93], v[94:95]\n\t"
      "v_cndmask_b32_e32 v96, 0, v96, vcc\n\t"
      "v_cndmask_b32_e32 v97, 0, v97, vcc\n\t"
      "v_add_f32_e32 v88, v88, v89\n\t"
      "v_add_f32_e32 v92, v92, v93\n\t"
      "v_add_f32_dpp v96, v96, v96 quad_perm:[1,0,3,2] row_mask:0xf bank_mask:0xf\n\t"
      "v_add_f32_dpp v97, v97, v97 quad_perm:[1,0,3,2] row_mask:0xf bank_mask:0xf\n\t"
      "v_add_f32_dpp v88, v88, v88 quad_perm:[1,0,3,2] row_mask:0xf bank_mask:0xf\n\t"
      "v_add_f32_dpp v92, v92, v92 quad_perm:[1,0,3,2] row_mask:0xf bank_mask:0xf\n\t"
      "v_add_f32_dpp v96, v96, v96 quad_perm:[2,3,0,1] row_mask:0xf bank_mask:0xf\n\t"
      "v_add_f32_dpp v97, v97, v97 quad_perm:[2,3,0,1] row_mask:0xf bank_mask:0xf\n\t"
      "v_add_f32_dpp v88, v88, v88 quad_perm:[2,3,0,1] row_mask:0xf bank_mask:0xf\n\t"
      "v_add_f32_dpp v92, v92, v92 quad_perm:[2,3,0,1] row_mask:0xf bank_mask:0xf\n\t"
      "v_add_f32_dpp v96, v96, v96 row_half_mirror row_mask:0xf bank_mask:0xf\n\t"
      "v_add_f32_dpp v97, v97, v97 row_half_mirror row_mask:0xf bank_mask:0xf\n\t"
      "v_add_f32_dpp v88, v88, v88 row_half_mirror row_mask:0xf bank_mask:0xf\n\t"
      "v_add_f32_dpp v92, v92, v92 row_half_mirror row_mask:0xf bank_mask:0xf\n\t"
      "v_mul_f32_e32 v88, 0xbb124925, v88\n\t"
      "v_add_f32_dpp v96, v96, v96 row_mirror row_mask:0xf bank_mask:0xf\n\t"
      "v_mul_f32_e32 v92, 0x3b124925, v92\n\t"
      "s_bitcmp0_b32 s27, 2\n\t"
      "s_cbranch_scc1 SK2_%=\n\t"
      "s_mul_i32 s72, s81, 172032\n\t"
      "s_add_u32 s72, s72, s37\n\t"
      "s_add_u32 s40, s6, s72\n\t"
      "s_addc_u32 s41, s7, 0\n\t"
      "s_add_u32 s81, s81, 1\n\t"
      "s_cmp_eq_u32 s81, 7\n\t"
      "s_cselect_b32 s81, 0, s81\n\t"
      "s_add_u32 m0, s39, 147456\n\t"
      "s_nop 0\n\t"
      "global_load_lds_dwordx4 v148, s[40:41]\n\t"
      "global_load_lds_dwordx4 v149, s[40:41] offset:1024\n\t"
      "SK2_%=:\n\t"
      "v_add_f32_dpp v97, v97, v97 row_mirror row_mask:0xf bank_mask:0xf\n\t"
      "v_fma_f32 v92, -v88, v88, v92\n\t"
      "s_nop 0\n\t"
      "v_permlane16_swap_b32_e32 v96, v97\n\t"
      "v_max_f32_e32 v92, 0, v92\n\t"
      "v_add_f32_e32 v96, v96, v97\n\t"
      "v_add_f32_e32 v92, 0x3727c5ac, v92\n\t"
      "v_mov_b32_e32 v97, v96\n\t"
      "v_rsq_f32_e32 v92, v92\n\t"
      "s_nop 0\n\t"
      "v_permlane32_swap_b32_e32 v96, v97\n\t"
      "v_add_f32_e32 v96, v96, v97\n\t"
      "v_mul_f32_e32 v88, v88, v92\n\t"
      "v_mov_b32_e32 v97, v96\n\t"
      "v_pk_fma_f32 v[32:33], v[32:33], v[92:93], v[88:89] op_sel_hi:[1,0,0]\n\t"
      "v_pk_fma_f32 v[34:35], v[34:35], v[92:93], v[88:89] op_sel_hi:[1,0,0]\n\t"
      "v_permlane16_swap_b32_e32 v96, v97\n\t"
      "v_pk_fma_f32 v[36:37], v[36:37], v[92:93], v[88:89] op_sel_hi:[1,0,0]\n\t"
      "v_pk_fma_f32 v[38:39], v[38:39], v[92:93], v[88:89] op_sel_hi:[1,0,0]\n\t"
      "v_mul_f32_e32 v96, 0xbb124925, v96\n\t"
      "v_cvt_pk_bf16_f32 v0, v32, v33\n\t"
      "v_cvt_pk_bf16_f32 v1, v34, v35\n\t"
      "v_cvt_pk_bf16_f32 v2, v36, v37\n\t"
      "v_cvt_pk_bf16_f32 v3, v38, v39\n\t"
      "ds_write_b128 v112, v[0:3]\n\t"
      "v_pk_fma_f32 v[40:41], v[40:41], v[92:93], v[88:89] op_sel_hi:[1,0,0]\n\t"
      "v_pk_fma_f32 v[42:43], v[42:43], v[92:93], v[88:89] op_sel_hi:[1,0,0]\n\t"
      "v_mul_f32_e32 v97, 0x3b124925, v97\n\t"
      "v_pk_fma_f32 v[44:45], v[44:45], v[92:93], v[88:89] op_sel_hi:[1,0,0]\n\t"
      "v_pk_fma_f32 v[46:47], v[46:47], v[92:93], v[88:89] op_sel_hi:[1,0,0]\n\t"
      "v_fma_f32 v97, -v96, v96, v97\n\t"
      "v_cvt_pk_bf16_f32 v4, v40, v41\n\t"
      "s_bitcmp0_b32 s27, 2\n\t"
      "s_cbranch_scc1 SK3_%=\n\t"
      "global_load_lds_dwordx4 v150, s[40:41] offset:2048\n\t"
      "global_load_lds_dwordx4 v151, s[40:41] offset:3072\n\t"
      "SK3_%=:\n\t"
      "v_cvt_pk_bf16_f32 v5, v42, v43\n\t"
      "v_cvt_pk_bf16_f32 v6, v44, v45\n\t"
      "v_cvt_pk_bf16_f32 v7, v46, v47\n\t"
      "ds_write_b128 v113, v[4:7]\n\t"
      "v_pk_fma_f32 v[48:49], v[48:49], v[92:93], v[88:89] op_sel_hi:[1,0,0]\n\t"
      "v_pk_fma_f32 v[50:51], v[50:51], v[92:93], v[88:89] op_sel_hi:[1,0,0]\n\t"
      "v_max_f32_e32 v97, 0, v97\n\t"
      "v_pk_fma_f32 v[52:53], v[52:53], v[92:93], v[88:89] op_sel_hi:[1,0,0]\n\t"
      "v_pk_fma_f32 v[54:55], v[54:55], v[92:93], v[88:89] op_sel_hi:[1,0,0]\n\t"
      "v_add_f32_e32 v97, 0x3727c5ac, v97\n\t"
      "v_cvt_pk_bf16_f32 v8, v48, v49\n\t"
      "v_cvt_pk_bf16_f32 v9, v50, v51\n\t"
      "v_cvt_pk_bf16_f32 v10, v52, v53\n\t"
      "v_cvt_pk_bf16_f32 v11, v54, v55\n\t"
      "ds_write_b128 v114, v[8:11]\n\t"
      "v_pk_fma_f32 v[56:57], v[56:57], v[92:93], v[88:89] op_sel_hi:[1,0,0]\n\t"
      "v_pk_fma_f32 v[58:59], v[58:59], v[92:93], v[88:89] op_sel_hi:[1,0,0]\n\t"
      "v_rsq_f32_e32 v97, v97\n\t"
      "v_pk_fma_f32 v[60:61], v[60:61], v[92:93], v[88:89] op_sel_hi:[1,0,0]\n\t"
      "v_pk_fma_f32 v[62:63], v[62:63], v[92:93], v[88:89] op_sel_hi:[1,0,0]\n\t"
      "v_cvt_pk_bf16_f32 v12, v56, v57\n\t"
      "v_cvt_pk_bf16_f32 v13, v58, v59\n\t"
      "v_cvt_pk_bf16_f32 v14, v60, v61\n\t"
      "v_cvt_pk_bf16_f32 v15, v62, v63\n\t"
      "ds_write_b128 v115, v[12:15]\n\t"
      "v_pk_fma_f32 v[64:65], v[64:65], v[92:93], v[88:89] op_sel_hi:[1,0,0]\n\t"
      "v_pk_fma_f32 v[66:67], v[66:67], v[92:93], v[88:89] op_sel_hi:[1,0,0]\n\t"
      "v_mul_f32_e32 v96, v96, v97\n\t"
      "v_pk_fma_f32 v[68:69], v[68:69], v[92:93], v[88:89] op_sel_hi:[1,0,0]\n\t"
      "v_pk_fma_f32 v[70:71], v[70:71], v[92:93], v[88:89] op_sel_hi:[1,0,0]\n\t"
      "s_bitcmp0_b32 s27, 2\n\t"
      "s_cbranch_scc1 SK4_%=\n\t"
      "s_mul_i32 s72, s82, 172032\n\t"
      "s_add_u32 s72, s72, s37\n\t"
      "s_add_u32 s72, s72, 1792\n\t"
      "s_add_u32 s40, s6, s72\n\t"
      "s_addc_u32 s41, s7, 0\n\t"
      "s_add_u32 s82, s82, 1\n\t"
      "s_cmp_eq_u32 s82, 7\n\t"
      "s_cselect_b32 s82, 0, s82\n\t"
      "s_add_u32 m0, s39, 65536\n\t"
      "s_nop 0\n\t"
      "global_load_lds_dwordx4 v148, s[40:41]\n\t"
      "global_load_lds_dwordx4 v152, s[40:41] offset:1024\n\t"
      "SK4_%=:\n\t"
      "v_cvt_pk_bf16_f32 v16, v64, v65\n\t"
      "v_cvt_pk_bf16_f32 v17, v66, v67\n\t"
      "v_cvt_pk_bf16_f32 v18, v68, v69\n\t"
      "v_cvt_pk_bf16_f32 v19, v70, v71\n\t"
      "ds_write_b128 v116, v[16:19]\n\t"
      "v_pk_fma_f32 v[72:73], v[72:73], v[92:93], v[88:89] op_sel_hi:[1,0,0]\n\t"
      "v_pk_fma_f32 v[74:75], v[74:75], v[92:93], v[88:89] op_sel_hi:[1,0,0]\n\t"
      "v_pk_fma_f32 v[76:77], v[76:77], v[92:93], v[88:89] op_sel_hi:[1,0,0]\n\t"
      "v_pk_fma_f32 v[78:79], v[78:79], v[92:93], v[88:89] op_sel_hi:[1,0,0]\n\t"
      "v_cvt_pk_bf16_f32 v20, v72, v73\n\t"
      "v_cvt_pk_bf16_f32 v21, v74, v75\n\t"
      "v_cvt_pk_bf16_f32 v22, v76, v77\n\t"
      "v_cvt_pk_bf16_f32 v23, v78, v79\n\t"
      "ds_write_b128 v117, v[20:23]\n\t"
      "v_pk_fma_f32 v[80:81], v[80:81], v[92:93], v[88:89] op_sel_hi:[1,0,0]\n\t"
      "v_pk_fma_f32 v[82:83], v[82:83], v[92:93], v[88:89] op_sel_hi:[1,0,0]\n\t"
      "v_pk_fma_f32 v[84:85], v[84:85], v[92:93], v[88:89] op_sel_hi:[1,0,0]\n\t"
      "v_pk_fma_f32 v[86:87], v[86:87], v[92:93], v[88:89] op_sel_hi:[1,0,0]\n\t"
      "v_cvt_pk_bf16_f32 v24, v80, v81\n\t"
      "v_cvt_pk_bf16_f32 v25, v82, v83\n\t"
      "v_cvt_pk_bf16_f32 v26, v84, v85\n\t"
      "v_cvt_pk_bf16_f32 v27, v86, v87\n\t"
      "ds_write_b128 v118, v[24:27]\n\t"
      "v_lshlrev_b32_e32 v32, 16, v28\n\t"
      "v_and_b32_e32 v33, 0xffff0000, v28\n\t"
      "v_lshlrev_b32_e32 v34, 16, v29\n\t"
      "v_and_b32_e32 v35, 0xffff0000, v29\n\t"
      "v_lshlrev_b32_e32 v36, 16, v30\n\t"
      "v_and_b32_e32 v37, 0xffff0000, v30\n\t"
      "v_lshlrev_b32_e32 v38, 16, v31\n\t"
      "s_bitcmp0_b32 s27, 2\n\t"
      "s_cbranch_scc1 SK5_%=\n\t"
      "s_mul_i32 s72, s82, 172032\n\t"
      "s_add_u32 s72, s72, s37\n\t"
      "s_add_u32 s72, s72, 1792\n\t"
      "s_add_u32 s40, s6, s72\n\t"
      "s_addc_u32 s41, s7, 0\n\t"
      "s_add_u32 s82, s82, 1\n\t"
      "s_cmp_eq_u32 s82, 7\n\t"
      "s_cselect_b32 s82, 0, s82\n\t"
      "s_add_u32 m0, s39, 67584\n\t"
      "s_nop 0\n\t"
      "global_load_lds_dwordx4 v148, s[40:41]\n\t"
      "global_load_lds_dwordx4 v152, s[40:41] offset:1024\n\t"
      "SK5_%=:\n\t"
      "v_and_b32_e32 v39, 0xffff0000, v31\n\t"
      "v_pk_fma_f32 v[32:33], v[32:33], v[96:97], v[96:97] op_sel:[0,1,0] op_sel_hi:[1,1,0]\n\t"
      "v_pk_fma_f32 v[34:35], v[34:35], v[96:97], v[96:97] op_sel:[0,1,0] op_sel_hi:[1,1,0]\n\t"
      "v_pk_fma_f32 v[36:37], v[36:37], v[96:97], v[96:97] op_sel:[0,1,0] op_sel_hi:[1,1,0]\n\t"
      "v_pk_fma_f32 v[38:39], v[38:39], v[96:97], v[96:97] op_sel:[0,1,0] op_sel_hi:[1,1,0]\n\t"
      "v_cvt_pk_bf16_f32 v28, v32, v33\n\t"
      "v_cvt_pk_bf16_f32 v29, v34, v35\n\t"
      "v_cvt_pk_bf16_f32 v30, v36, v37\n\t"
      "v_cvt_pk_bf16_f32 v31, v38, v39\n\t"
      "ds_write_b128 v119, v[28:31]\n\t"
      "s_waitcnt lgkmcnt(0)\n\t"
      "s_bitcmp1_b32 s27, 2\n\t"
      "s_cbranch_scc1 LPATH_%=\n\t"
      "s_setprio 2\n\t"
      "s_barrier\n\t"
      "ds_read_b64_tr_b16 v[76:77], v168\n\t"
      "ds_read_b64_tr_b16 v[78:79], v168 offset:512\n\t"
      "ds_read_b128 v[68:71], v160\n\t"
      "ds_read_b128 v[72:75], v160 offset:14336\n\t"
      "ds_read_b64_tr_b16 v[80:81], v169\n\t"
      "ds_read_b64_tr_b16 v[82:83], v169 offset:512\n\t"
      "ds_read_b64_tr_b16 v[84:85], v168 offset:1024\n\t"
      "ds_read_b64_tr_b16 v[86:87], v168 offset:1536\n\t"
      "ds_read_b128 v[64:67], v162\n\t"
      "ds_read_b64_tr_b16 v[88:89], v169 offset:1024\n\t"
      "ds_read_b64_tr_b16 v[90:91], v169 offset:1536\n\t"
      "ds_read_b64_tr_b16 v[104:105], v168 offset:2048\n\t"
      "ds_read_b64_tr_b16 v[106:107], v168 offset:2560\n\t"
      "ds_read_b128 v[96:99], v161 offset:1024\n\t"
      "ds_read_b128 v[100:103], v161 offset:15360\n\t"
      "ds_read_b64_tr_b16 v[108:109], v169 offset:2048\n\t"
      "ds_read_b64_tr_b16 v[110:111], v169 offset:2560\n\t"
      "ds_read_b64_tr_b16 v[112:113], v168 offset:3072\n\t"
      "ds_read_b64_tr_b16 v[114:115], v168 offset:3584\n\t"
      "ds_read_b128 v[92:95], v163 offset:1024\n\t"
      "ds_read_b64_tr_b16 v[116:117], v169 offset:3072\n\t"
      "ds_read_b64_tr_b16 v[118:119], v169 offset:3584\n\t"
      "s_waitcnt vmcnt(0)\n\t"
      "s_barrier\n\t"
      "s_waitcnt lgkmcnt(14)\n\t"
      "v_mfma_f32_16x16x32_bf16 v[0:3], v[76:79], v[68:71], 0\n\t"
      "ds_read_b64_tr_b16 v[132:133], v168 offset:16384\n\t"
      "ds_read_b64_tr_b16 v[134:135], v168 offset:16896\n\t"
      "v_mfma_f32_16x16x32_bf16 v[8:11], v[76:79], v[72:75], 0\n\t"
      "ds_read_b128 v[124:127], v160 offset:2048\n\t"
      "v_mfma_f32_16x16x32_bf16 v[4:7], v[80:83], v[68:71], 0\n\t"
      "ds_read_b128 v[128:131], v160 offset:16384\n\t"
      "v_mfma_f32_16x16x32_bf16 v[12:15], v[80:83], v[72:75], 0\n\t"
      "ds_read_b64_tr_b16 v[136:137], v169 offset:16384\n\t"
      "v_mfma_f32_16x16x32_bf16 v[24:27], v[84:87], v[68:71], 0\n\t"
      "ds_read_b64_tr_b16 v[138:139], v169 offset:16896\n\t"
      "v_mfma_f32_16x16x32_bf16 v[32:35], v[84:87], v[72:75], 0\n\t"
      "ds_read_b64_tr_b16 v[140:141], v168 offset:17408\n\t"
      "s_waitcnt lgkmcnt(14)\n\t"
      "v_mfma_f32_16x16x32_bf16 v[16:19], v[84:87], v[64:67], 0\n\t"
      "ds_read_b64_tr_b16 v[142:143], v168 offset:17920\n\t"
      "v_mfma_f32_16x16x32_bf16 v[28:31], v[88:91], v[68:71], 0\n\t"
      "ds_read_b128 v[120:123], v162 offset:2048\n\t"
      "v_mfma_f32_16x16x32_bf16 v[36:39], v[88:91], v[72:75], 0\n\t"
      "ds_read_b64_tr_b16 v[144:145], v169 offset:17408\n\t"
      "v_mfma_f32_16x16x32_bf16 v[20:23], v[88:91], v[64:67], 0\n\t"
      "ds_read_b64_tr_b16 v[146:147], v169 offset:17920\n\t"
      "s_waitcnt lgkmcnt(11)\n\t"
      "v_mfma_f32_16x16x32_bf16 v[0:3], v[104:107], v[96:99], v[0:3]\n\t"
      "ds_read_b64_tr_b16 v[76:77], v168 offset:18432\n\t"
      "ds_read_b64_tr_b16 v[78:79], v168 offset:18944\n\t"
      "v_mfma_f32_16x16x32_bf16 v[8:11], v[104:107], v[100:103], v[8:11]\n\t"
      "ds_read_b128 v[68:71], v161 offset:3072\n\t"
      "v_mfma_f32_16x16x32_bf16 v[4:7], v[108:111], v[96:99], v[4:7]\n\t"
      "ds_read_b128 v[72:75], v161 offset:17408\n\t"
      "v_mfma_f32_16x16x32_bf16 v[12:15], v[108:111], v[100:103], v[12:15]\n\t"
      "ds_read_b64_tr_b16 v[80:81], v169 offset:18432\n\t"
      "v_mfma_f32_16x16x32_bf16 v[24:27], v[112:115], v[96:99], v[24:27]\n\t"
      "ds_read_b64_tr_b16 v[82:83], v169 offset:18944\n\t"
      "v_mfma_f32_16x16x32_bf16 v[32:35], v[112:115], v[100:103], v[32:35]\n\t"
      "ds_read_b64_tr_b16 v[84:85], v168 offset:19456\n\t"
      "v_mfma_f32_16x16x32_bf16 v[16:19], v[112:115], v[92:95], v[16:19]\n\t"
      "ds_read_b64_tr_b16 v[86:87], v168 offset:19968\n\t"
      "v_mfma_f32_16x16x32_bf16 v[28:31], v[116:119], v[96:99], v[28:31]\n\t"
      "ds_read_b128 v[64:67], v163 offset:3072\n\t"
      "v_mfma_f32_16x16x32_bf16 v[36:39], v[116:119], v[100:103], v[36:39]\n\t"
      "ds_read_b64_tr_b16 v[88:89], v169 offset:19456\n\t"
      "v_mfma_f32_16x16x32_bf16 v[20:23], v[116:119], v[92:95], v[20:23]\n\t"
      "ds_read_b64_tr_b16 v[90:91], v169 offset:19968\n\t"
      "s_barrier\n\t"
      "s_waitcnt lgkmcnt(11)\n\t"
      "v_mfma_f32_16x16x32_bf16 v[0:3], v[132:135], v[124:127], v[0:3]\n\t"
      "ds_read_b64_tr_b16 v[104:105], v166\n\t"
      "v_lshlrev_b32_e32 v199, 2, v193\n\t"
      "v_mfma_f32_16x16x32_bf16 v[8:11], v[132:135], v[128:131], v[8:11]\n\t"
      "ds_read_b64_tr_b16 v[106:107], v166 offset:512\n\t"
      "v_sub_u32_e32 v199, v194, v199\n\t"
      "v_mfma_f32_16x16x32_bf16 v[4:7], v[136:139], v[124:127], v[4:7]\n\t"
      "ds_read_b128 v[96:99], v160\n\t"
      "v_mov_b32_e32 v200, 0xff800000\n\t"
      "v_mfma_f32_16x16x32_bf16 v[12:15], v[136:139], v[128:131], v[12:15]\n\t"
      "ds_read_b128 v[100:103], v160 offset:14336\n\t"
      "s_cmp_lt_u32 s25, 2\n\t"
      "v_mfma_f32_16x16x32_bf16 v[24:27], v[140:143], v[124:127], v[24:27]\n\t"
      "ds_read_b128 v[92:95], v162\n\t"
      "s_cselect_b32 s35, 0xff800000, 0\n\t"
      "v_mfma_f32_16x16x32_bf16 v[32:35], v[140:143], v[128:131], v[32:35]\n\t"
      "ds_read_b64_tr_b16 v[108:109], v167\n\t"
      "s_bitcmp0_b32 s24, 0\n\t"
      "v_mfma_f32_16x16x32_bf16 v[16:19], v[140:143], v[120:123], v[16:19]\n\t"
      "ds_read_b64_tr_b16 v[110:111], v167 offset:512\n\t"
      "s_cselect_b32 s35, s35, 0\n\t"
      "v_mfma_f32_16x16x32_bf16 v[28:31], v[144:147], v[124:127], v[28:31]\n\t"
      "v_cmp_ge_i32_e32 vcc, -9, v199\n\t"
      "v_mfma_f32_16x16x32_bf16 v[36:39], v[144:147], v[128:131], v[36:39]\n\t"
      "v_subrev_u32_e32 v201, 0, v199\n\t"
      "v_mfma_f32_16x16x32_bf16 v[20:23], v[144:147], v[120:123], v[20:23]\n\t"
      "v_cmp_gt_u32_e64 s[64:65], 8, v201\n\t"
      "s_waitcnt lgkmcnt(7)\n\t"
      "v_mfma_f32_16x16x32_bf16 v[0:3], v[76:79], v[68:71], v[0:3]\n\t"
      "ds_read_b64_tr_b16 v[132:133], v166 offset:1024\n\t"
      "v_cndmask_b32_e64 v172, v200, 0, vcc\n\t"
      "v_mfma_f32_16x16x32_bf16 v[8:11], v[76:79], v[72:75], v[8:11]\n\t"
      "ds_read_b64_tr_b16 v[134:135], v166 offset:1536\n\t"
      "v_cndmask_b32_e64 v176, v200, 0, s[64:65]\n\t"
      "v_mfma_f32_16x16x32_bf16 v[4:7], v[80:83], v[68:71], v[4:7]\n\t"
      "ds_read_b128 v[124:127], v161 offset:1024\n\t"
      "v_or_b32_e32 v180, s35, v172\n\t"
      "v_mfma_f32_16x16x32_bf16 v[12:15], v[80:83], v[72:75], v[12:15]\n\t"
      "ds_read_b128 v[128:131], v161 offset:15360\n\t"
      "v_cmp_ge_i32_e32 vcc, -8, v199\n\t"
      "v_mfma_f32_16x16x32_bf16 v[24:27], v[84:87], v[68:71], v[24:27]\n\t"
      "ds_read_b128 v[120:123], v163 offset:1024\n\t"
      "v_subrev_u32_e32 v201, 1, v199\n\t"
      "v_mfma_f32_16x16x32_bf16 v[32:35], v[84:87], v[72:75], v[32:35]\n\t"
      "ds_read_b64_tr_b16 v[136:137], v167 offset:1024\n\t"
      "v_cmp_gt_u32_e64 s[64:65], 8, v201\n\t"
      "v_mfma_f32_16x16x32_bf16 v[16:19], v[84:87], v[64:67], v[16:19]\n\t"
      "ds_read_b64_tr_b16 v[138:139], v167 offset:1536\n\t"
      "v_cndmask_b32_e64 v173, v200, 0, vcc\n\t"
      "v_mfma_f32_16x16x32_bf16 v[28:31], v[88:91], v[68:71], v[28:31]\n\t"
      "v_cndmask_b32_e64 v177, v200, 0, s[64:65]\n\t"
      "v_mfma_f32_16x16x32_bf16 v[36:39], v[88:91], v[72:75], v[36:39]\n\t"
      "v_or_b32_e32 v181, s35, v173\n\t"
      "v_mfma_f32_16x16x32_bf16 v[20:23], v[88:91], v[64:67], v[20:23]\n\t"
      "v_cmp_ge_i32_e32 vcc, -7, v199\n\t"
      "s_waitcnt lgkmcnt(7)\n\t"
      "v_mfma_f32_16x16x32_bf16 v[48:51], v[96:99], v[104:107], 0\n\t"
      "ds_read_b64_tr_b16 v[76:77], v166 offset:2048\n\t"
      "ds_read_b64_tr_b16 v[78:79], v166 offset:2560\n\t"
      "v_subrev_u32_e32 v201, 2, v199\n\t"
      "v_mfma_f32_16x16x32_bf16 v[56:59], v[100:103], v[104:107], 0\n\t"
      "ds_read_b128 v[68:71], v160 offset:2048\n\t"
      "v_cmp_gt_u32_e64 s[64:65], 8, v201\n\t"
      "v_mfma_f32_16x16x32_bf16 v[40:43], v[92:95], v[104:107], 0\n\t"
      "ds_read_b128 v[72:75], v160 offset:16384\n\t"
      "v_cndmask_b32_e64 v174, v200, 0, vcc\n\t"
      "v_mfma_f32_16x16x32_bf16 v[52:55], v[96:99], v[108:111], 0\n\t"
      "ds_read_b128 v[64:67], v162 offset:2048\n\t"
      "v_cndmask_b32_e64 v178, v200, 0, s[64:65]\n\t"
      "v_mfma_f32_16x16x32_bf16 v[60:63], v[100:103], v[108:111], 0\n\t"
      "ds_read_b64_tr_b16 v[80:81], v167 offset:2048\n\t"
      "v_or_b32_e32 v182, s35, v174\n\t"
      "v_mfma_f32_16x16x32_bf16 v[44:47], v[92:95], v[108:111], 0\n\t"
      "ds_read_b64_tr_b16 v[82:83], v167 offset:2560\n\t"
      "v_cmp_ge_i32_e32 vcc, -6, v199\n\t"
      "s_waitcnt lgkmcnt(7)\n\t"
      "v_mfma_f32_16x16x32_bf16 v[48:51], v[124:127], v[132:135], v[48:51]\n\t"
      "ds_read_b64_tr_b16 v[104:105], v166 offset:3072\n\t"
      "ds_read_b64_tr_b16 v[106:107], v166 offset:3584\n\t"
      "v_subrev_u32_e32 v201, 3, v199\n\t"
      "v_mfma_f32_16x16x32_bf16 v[56:59], v[128:131], v[132:135], v[56:59]\n\t"
      "ds_read_b128 v[96:99], v161 offset:3072\n\t"
      "v_cmp_gt_u32_e64 s[64:65], 8, v201\n\t"
      "v_mfma_f32_16x16x32_bf16 v[40:43], v[120:123], v[132:135], v[40:43]\n\t"
      "ds_read_b128 v[100:103], v161 offset:17408\n\t"
      "v_cndmask_b32_e64 v175, v200, 0, vcc\n\t"
      "v_mfma_f32_16x16x32_bf16 v[52:55], v[124:127], v[136:139], v[52:55]\n\t"
      "ds_read_b128 v[92:95], v163 offset:3072\n\t"
      "v_cndmask_b32_e64 v179, v200, 0, s[64:65]\n\t"
      "v_mfma_f32_16x16x32_bf16 v[60:63], v[128:131], v[136:139], v[60:63]\n\t"
      "ds_read_b64_tr_b16 v[108:109], v167 offset:3072\n\t"
      "v_or_b32_e32 v183, s35, v175\n\t"
      "v_mfma_f32_16x16x32_bf16 v[44:47], v[120:123], v[136:139], v[44:47]\n\t"
      "ds_read_b64_tr_b16 v[110:111], v167 offset:3584\n\t"
      "v_lshrrev_b32_e32 v201, 31, v172\n\t"
      "s_barrier\n\t"
      "s_waitcnt lgkmcnt(7)\n\t"
      "v_mfma_f32_16x16x32_bf16 v[48:51], v[68:71], v[76:79], v[48:51]\n\t"
      "ds_read_b64_tr_b16 v[132:133], v166 offset:16384\n\t"
      "ds_read_b64_tr_b16 v[134:135], v166 offset:16896\n\t"
      "ds_read_b128 v[124:127], v160 offset:4096\n\t"
      "ds_read_b128 v[128:131], v160 offset:18432\n\t"
      "ds_read_b64_tr_b16 v[136:137], v167 offset:16384\n\t"
      "ds_read_b64_tr_b16 v[138:139], v167 offset:16896\n\t"
      "v_lshrrev_b32_e32 v202, 31, v173\n\t"
      "v_mfma_f32_16x16x32_bf16 v[56:59], v[72:75], v[76:79], v[56:59]\n\t"
      "ds_read_b64_tr_b16 v[140:141], v166 offset:17408\n\t"
      "v_lshl_or_b32 v201, v202, 16, v201\n\t"
      "v_mfma_f32_16x16x32_bf16 v[40:43], v[64:67], v[76:79], v[40:43]\n\t"
      "ds_read_b64_tr_b16 v[142:143], v166 offset:17920\n\t"
      "v_mul_u32_u24_e32 v184, 0xffff, v201\n\t"
      "v_mfma_f32_16x16x32_bf16 v[52:55], v[68:71], v[80:83], v[52:55]\n\t"
      "ds_read_b128 v[120:123], v162 offset:4096\n\t"
      "v_lshrrev_b32_e32 v201, 31, v174\n\t"
      "v_mfma_f32_16x16x32_bf16 v[60:63], v[72:75], v[80:83], v[60:63]\n\t"
      "ds_read_b64_tr_b16 v[144:145], v167 offset:17408\n\t"
      "v_lshrrev_b32_e32 v202, 31, v175\n\t"
      "v_mfma_f32_16x16x32_bf16 v[44:47], v[64:67], v[80:83], v[44:47]\n\t"
      "ds_read_b64_tr_b16 v[146:147], v167 offset:17920\n\t"
      "v_lshl_or_b32 v201, v202, 16, v201\n\t"
      "s_waitcnt lgkmcnt(11)\n\t"
      "v_mfma_f32_16x16x32_bf16 v[48:51], v[96:99], v[104:107], v[48:51]\n\t"
      "ds_read_b64_tr_b16 v[76:77], v166 offset:18432\n\t"
      "ds_read_b64_tr_b16 v[78:79], v166 offset:18944\n\t"
      "ds_read_b128 v[68:71], v161 offset:5120\n\t"
      "ds_read_b128 v[72:75], v161 offset:19456\n\t"
      "ds_read_b64_tr_b16 v[80:81], v167 offset:18432\n\t"
      "ds_read_b64_tr_b16 v[82:83], v167 offset:18944\n\t"
      "v_mul_u32_u24_e32 v185, 0xffff, v201\n\t"
      "v_mfma_f32_16x16x32_bf16 v[56:59], v[100:103], v[104:107], v[56:59]\n\t"
      "ds_read_b64_tr_b16 v[84:85], v166 offset:19456\n\t"
      "s_mov_b32 s68, 0x0\n\t"
      "v_mfma_f32_16x16x32_bf16 v[40:43], v[92:95], v[104:107], v[40:43]\n\t"
      "ds_read_b64_tr_b16 v[86:87], v166 offset:19968\n\t"
      "s_mov_b32 s69, 0xffff0000\n\t"
      "v_mfma_f32_16x16x32_bf16 v[52:55], v[96:99], v[108:111], v[52:55]\n\t"
      "ds_read_b128 v[64:67], v163 offset:5120\n\t"
      "s_mov_b32 s70, 0xffff\n\t"
      "v_mfma_f32_16x16x32_bf16 v[60:63], v[100:103], v[108:111], v[60:63]\n\t"
      "ds_read_b64_tr_b16 v[88:89], v167 offset:19456\n\t"
      "s_mov_b32 s71, 0x0\n\t"
      "v_mfma_f32_16x16x32_bf16 v[44:47], v[92:95], v[108:111], v[44:47]\n\t"
      "ds_read_b64_tr_b16 v[90:91], v167 offset:19968\n\t"
      "v_lshlrev_b32_e32 v231, 3, v192\n\t"
      "s_barrier\n\t"
      "s_waitcnt lgkmcnt(11)\n\t"
      "v_mfma_f32_16x16x32_bf16 v[0:3], v[132:135], v[124:127], v[0:3]\n\t"
      "ds_read_b64_tr_b16 v[104:105], v166 offset:32768\n\t"
      "ds_read_b64_tr_b16 v[106:107], v166 offset:33280\n\t"
      "s_lshl_b32 s34, s25, 12\n\t"
      "v_mfma_f32_16x16x32_bf16 v[8:11], v[132:135], v[128:131], v[8:11]\n\t"
      "ds_read_b128 v[96:99], v160 offset:6144\n\t"
      "s_lshr_b32 s35, s39, 2\n\t"
      "v_mfma_f32_16x16x32_bf16 v[4:7], v[136:139], v[124:127], v[4:7]\n\t"
      "ds_read_b128 v[100:103], v160 offset:20480\n\t"
      "s_add_u32 s34, s34, s35\n\t"
      "v_mfma_f32_16x16x32_bf16 v[12:15], v[136:139], v[128:131], v[12:15]\n\t"
      "ds_read_b64_tr_b16 v[108:109], v167 offset:32768\n\t"
      "s_add_u32 s34, s34, 12582912\n\t"
      "v_mfma_f32_16x16x32_bf16 v[24:27], v[140:143], v[124:127], v[24:27]\n\t"
      "ds_read_b64_tr_b16 v[110:111], v167 offset:33280\n\t"
      "s_add_u32 s46, s20, s34\n\t"
      "v_mfma_f32_16x16x32_bf16 v[32:35], v[140:143], v[128:131], v[32:35]\n\t"
      "ds_read_b64_tr_b16 v[112:113], v166 offset:33792\n\t"
      "s_addc_u32 s47, s21, 0\n\t"
      "v_mfma_f32_16x16x32_bf16 v[16:19], v[140:143], v[120:123], v[16:19]\n\t"
      "ds_read_b64_tr_b16 v[114:115], v166 offset:34304\n\t"
      "s_lshl_b32 s34, s28, 7\n\t"
      "v_mfma_f32_16x16x32_bf16 v[28:31], v[144:147], v[124:127], v[28:31]\n\t"
      "ds_read_b128 v[92:95], v162 offset:6144\n\t"
      "s_add_u32 s58, s46, s34\n\t"
      "v_mfma_f32_16x16x32_bf16 v[36:39], v[144:147], v[128:131], v[36:39]\n\t"
      "ds_read_b64_tr_b16 v[116:117], v167 offset:33792\n\t"
      "s_addc_u32 s59, s47, 0\n\t"
      "v_mfma_f32_16x16x32_bf16 v[20:23], v[144:147], v[120:123], v[20:23]\n\t"
      "ds_read_b64_tr_b16 v[118:119], v167 offset:34304\n\t"
      "s_and_b32 s34, s24, 1\n\t"
      "s_waitcnt lgkmcnt(11)\n\t"
      "v_mfma_f32_16x16x32_bf16 v[0:3], v[76:79], v[68:71], v[0:3]\n\t"
      "ds_read_b64_tr_b16 v[132:133], v166 offset:34816\n\t"
      "ds_read_b64_tr_b16 v[134:135], v166 offset:35328\n\t"
      "s_lshl1_add_u32 s34, s25, s34\n\t"
      "v_mfma_f32_16x16x32_bf16 v[8:11], v[76:79], v[72:75], v[8:11]\n\t"
      "ds_read_b128 v[124:127], v161 offset:7168\n\t"
      "s_lshl_b32 s34, s34, 5\n\t"
      "v_mfma_f32_16x16x32_bf16 v[4:7], v[80:83], v[68:71], v[4:7]\n\t"
      "ds_read_b128 v[128:131], v161 offset:21504\n\t"
      "s_lshl1_add_u32 s34, s26, s34\n\t"
      "v_mfma_f32_16x16x32_bf16 v[12:15], v[80:83], v[72:75], v[12:15]\n\t"
      "ds_read_b64_tr_b16 v[136:137], v167 offset:34816\n\t"
      "s_bfe_u32 s35, s24, 65537\n\t"
      "v_mfma_f32_16x16x32_bf16 v[24:27], v[84:87], v[68:71], v[24:27]\n\t"
      "ds_read_b64_tr_b16 v[138:139], v167 offset:35328\n\t"
      "s_add_u32 s34, s34, s35\n\t"
      "v_mfma_f32_16x16x32_bf16 v[32:35], v[84:87], v[72:75], v[32:35]\n\t"
      "ds_read_b64_tr_b16 v[140:141], v166 offset:35840\n\t"
      "s_lshl_b32 s34, s34, 10\n\t"
      "v_mfma_f32_16x16x32_bf16 v[16:19], v[84:87], v[64:67], v[16:19]\n\t"
      "ds_read_b64_tr_b16 v[142:143], v166 offset:36352\n\t"
      "s_add_u32 s42, s20, s34\n\t"
      "v_mfma_f32_16x16x32_bf16 v[28:31], v[88:91], v[68:71], v[28:31]\n\t"
      "ds_read_b128 v[120:123], v165 offset:7168\n\t"
      "s_addc_u32 s43, s21, 0\n\t"
      "v_mfma_f32_16x16x32_bf16 v[36:39], v[88:91], v[72:75], v[36:39]\n\t"
      "ds_read_b64_tr_b16 v[144:145], v167 offset:35840\n\t"
      "s_add_u32 s44, s42, 16384\n\t"
      "v_mfma_f32_16x16x32_bf16 v[20:23], v[88:91], v[64:67], v[20:23]\n\t"
      "ds_read_b64_tr_b16 v[146:147], v167 offset:36352\n\t"
      "s_addc_u32 s45, s43, 0\n\t"
      "s_barrier\n\t"
      "s_waitcnt lgkmcnt(11)\n\t"
      "v_mfma_f32_16x16x32_bf16 v[0:3], v[104:107], v[96:99], v[0:3]\n\t"
      "ds_read_b64_tr_b16 v[76:77], v166 offset:49152\n\t"
      "ds_read_b64_tr_b16 v[78:79], v166 offset:49664\n\t"
      "v_and_b32_e32 v199, 60, v192\n\t"
      "v_mfma_f32_16x16x32_bf16 v[8:11], v[104:107], v[100:103], v[8:11]\n\t"
      "ds_read_b128 v[68:71], v160 offset:8192\n\t"
      "v_and_b32_e32 v200, 24, v192\n\t"
      "v_mfma_f32_16x16x32_bf16 v[4:7], v[108:111], v[96:99], v[4:7]\n\t"
      "ds_read_b128 v[72:75], v160 offset:22528\n\t"
      "v_lshlrev_b32_e32 v202, 3, v196\n\t"
      "v_mfma_f32_16x16x32_bf16 v[12:15], v[108:111], v[100:103], v[12:15]\n\t"
      "ds_read_b64_tr_b16 v[80:81], v167 offset:49152\n\t"
      "v_lshl_or_b32 v202, v200, 2, v202\n\t"
      "v_mfma_f32_16x16x32_bf16 v[24:27], v[112:115], v[96:99], v[24:27]\n\t"
      "ds_read_b64_tr_b16 v[82:83], v167 offset:49664\n\t"
      "v_lshl_or_b32 v201, v199, 5, v202\n\t"
      "v_mfma_f32_16x16x32_bf16 v[32:35], v[112:115], v[100:103], v[32:35]\n\t"
      "ds_read_b64_tr_b16 v[84:85], v166 offset:50176\n\t"
      "v_add_u32_e32 v216, 106496, v201\n\t"
      "v_mfma_f32_16x16x32_bf16 v[16:19], v[112:115], v[92:95], v[16:19]\n\t"
      "ds_read_b64_tr_b16 v[86:87], v166 offset:50688\n\t"
      "v_mov_b32_e32 v199, 2048\n\t"
      "v_mfma_f32_16x16x32_bf16 v[28:31], v[116:119], v[96:99], v[28:31]\n\t"
      "ds_read_b128 v[64:67], v164 offset:8192\n\t"
      "v_cndmask_b32_e64 v199, v199, 0, s[68:69]\n\t"
      "v_mfma_f32_16x16x32_bf16 v[36:39], v[116:119], v[100:103], v[36:39]\n\t"
      "ds_read_b64_tr_b16 v[88:89], v167 offset:50176\n\t"
      "v_add_u32_e32 v220, v216, v199\n\t"
      "v_mfma_f32_16x16x32_bf16 v[20:23], v[116:119], v[92:95], v[20:23]\n\t"
      "ds_read_b64_tr_b16 v[90:91], v167 offset:50688\n\t"
      "v_xor_b32_e32 v217, 32, v216\n\t"
      "s_waitcnt lgkmcnt(11)\n\t"
      "v_mfma_f32_16x16x32_bf16 v[0:3], v[132:135], v[124:127], v[0:3]\n\t"
      "ds_read_b64_tr_b16 v[104:105], v166 offset:51200\n\t"
      "ds_read_b64_tr_b16 v[106:107], v166 offset:51712\n\t"
      "v_xor_b32_e32 v218, 64, v216\n\t"
      "v_mfma_f32_16x16x32_bf16 v[8:11], v[132:135], v[128:131], v[8:11]\n\t"
      "ds_read_b128 v[96:99], v161 offset:9216\n\t"
      "v_xor_b32_e32 v219, 96, v216\n\t"
      "v_mfma_f32_16x16x32_bf16 v[4:7], v[136:139], v[124:127], v[4:7]\n\t"
      "ds_read_b128 v[100:103], v161 offset:23552\n\t"
      "v_xor_b32_e32 v221, 32, v220\n\t"
      "v_mfma_f32_16x16x32_bf16 v[12:15], v[136:139], v[128:131], v[12:15]\n\t"
      "ds_read_b64_tr_b16 v[108:109], v167 offset:51200\n\t"
      "v_xor_b32_e32 v222, 64, v220\n\t"
      "v_mfma_f32_16x16x32_bf16 v[24:27], v[140:143], v[124:127], v[24:27]\n\t"
      "ds_read_b64_tr_b16 v[110:111], v167 offset:51712\n\t"
      "v_xor_b32_e32 v223, 96, v220\n\t"
      "v_mfma_f32_16x16x32_bf16 v[32:35], v[140:143], v[128:131], v[32:35]\n\t"
      "ds_read_b64_tr_b16 v[112:113], v166 offset:52224\n\t"
      "s_lshr_b32 s34, s39, 1\n\t"
      "v_mfma_f32_16x16x32_bf16 v[16:19], v[140:143], v[120:123], v[16:19]\n\t"
      "ds_read_b64_tr_b16 v[114:115], v166 offset:52736\n\t"
      "s_add_u32 s34, s34, 98304\n\t"
      "v_mfma_f32_16x16x32_bf16 v[28:31], v[144:147], v[124:127], v[28:31]\n\t"
      "ds_read_b128 v[92:95], v165 offset:9216\n\t"
      "v_lshrrev_b32_e32 v199, 1, v193\n\t"
      "v_mfma_f32_16x16x32_bf16 v[36:39], v[144:147], v[128:131], v[36:39]\n\t"
      "ds_read_b64_tr_b16 v[116:117], v167 offset:52224\n\t"
      "v_xor_b32_e32 v199, v199, v198\n\t"
      "v_mfma_f32_16x16x32_bf16 v[20:23], v[144:147], v[120:123], v[20:23]\n\t"
      "ds_read_b64_tr_b16 v[118:119], v167 offset:52736\n\t"
      "v_lshlrev_b32_e32 v199, 4, v199\n\t"
      "s_barrier\n\t"
      "s_waitcnt lgkmcnt(11)\n\t"
      "v_mfma_f32_16x16x32_bf16 v[0:3], v[76:79], v[68:71], v[0:3]\n\t"
      "ds_read_b64_tr_b16 v[132:133], v168\n\t"
      "ds_read_b64_tr_b16 v[134:135], v168 offset:512\n\t"
      "v_lshl_or_b32 v199, v194, 7, v199\n\t"
      "v_mfma_f32_16x16x32_bf16 v[8:11], v[76:79], v[72:75], v[8:11]\n\t"
      "ds_read_b128 v[124:127], v160 offset:10240\n\t"
      "v_and_b32_e32 v200, 1, v193\n\t"
      "v_mfma_f32_16x16x32_bf16 v[4:7], v[80:83], v[68:71], v[4:7]\n\t"
      "ds_read_b128 v[128:131], v160 offset:24576\n\t"
      "v_lshl_or_b32 v199, v200, 3, v199\n\t"
      "v_mfma_f32_16x16x32_bf16 v[12:15], v[80:83], v[72:75], v[12:15]\n\t"
      "ds_read_b64_tr_b16 v[136:137], v169\n\t"
      "v_add_u32_e32 v228, s34, v199\n\t"
      "v_mfma_f32_16x16x32_bf16 v[24:27], v[84:87], v[68:71], v[24:27]\n\t"
      "ds_read_b64_tr_b16 v[138:139], v169 offset:512\n\t"
      "v_mfma_f32_16x16x32_bf16 v[32:35], v[84:87], v[72:75], v[32:35]\n\t"
      "ds_read_b64_tr_b16 v[140:141], v168 offset:1024\n\t"
      "v_mfma_f32_16x16x32_bf16 v[16:19], v[84:87], v[64:67], v[16:19]\n\t"
      "ds_read_b64_tr_b16 v[142:143], v168 offset:1536\n\t"
      "v_mfma_f32_16x16x32_bf16 v[28:31], v[88:91], v[68:71], v[28:31]\n\t"
      "ds_read_b128 v[120:123], v164 offset:10240\n\t"
      "v_mfma_f32_16x16x32_bf16 v[36:39], v[88:91], v[72:75], v[36:39]\n\t"
      "ds_read_b64_tr_b16 v[144:145], v169 offset:1024\n\t"
      "v_mfma_f32_16x16x32_bf16 v[20:23], v[88:91], v[64:67], v[20:23]\n\t"
      "ds_read_b64_tr_b16 v[146:147], v169 offset:1536\n\t"
      "s_waitcnt lgkmcnt(11)\n\t"
      "v_mfma_f32_16x16x32_bf16 v[0:3], v[104:107], v[96:99], v[0:3]\n\t"
      "ds_read_b64_tr_b16 v[76:77], v168 offset:2048\n\t"
      "ds_read_b64_tr_b16 v[78:79], v168 offset:2560\n\t"
      "v_mfma_f32_16x16x32_bf16 v[8:11], v[104:107], v[100:103], v[8:11]\n\t"
      "ds_read_b128 v[68:71], v161 offset:11264\n\t"
      "v_mfma_f32_16x16x32_bf16 v[4:7], v[108:111], v[96:99], v[4:7]\n\t"
      "ds_read_b128 v[72:75], v161 offset:25600\n\t"
      "v_mfma_f32_16x16x32_bf16 v[12:15], v[108:111], v[100:103], v[12:15]\n\t"
      "ds_read_b64_tr_b16 v[80:81], v169 offset:2048\n\t"
      "v_mfma_f32_16x16x32_bf16 v[24:27], v[112:115], v[96:99], v[24:27]\n\t"
      "ds_read_b64_tr_b16 v[82:83], v169 offset:2560\n\t"
      "v_mfma_f32_16x16x32_bf16 v[32:35], v[112:115], v[100:103], v[32:35]\n\t"
      "ds_read_b64_tr_b16 v[84:85], v168 offset:3072\n\t"
      "v_mfma_f32_16x16x32_bf16 v[16:19], v[112:115], v[92:95], v[16:19]\n\t"
      "ds_read_b64_tr_b16 v[86:87], v168 offset:3584\n\t"
      "v_mfma_f32_16x16x32_bf16 v[28:31], v[116:119], v[96:99], v[28:31]\n\t"
      "ds_read_b128 v[64:67], v165 offset:11264\n\t"
      "v_mfma_f32_16x16x32_bf16 v[36:39], v[116:119], v[100:103], v[36:39]\n\t"
      "ds_read_b64_tr_b16 v[88:89], v169 offset:3072\n\t"
      "v_mfma_f32_16x16x32_bf16 v[20:23], v[116:119], v[92:95], v[20:23]\n\t"
      "ds_read_b64_tr_b16 v[90:91], v169 offset:3584\n\t"
      "s_barrier\n\t"
      "s_waitcnt lgkmcnt(11)\n\t"
      "v_mfma_f32_16x16x32_bf16 v[0:3], v[132:135], v[124:127], v[0:3]\n\t"
      "ds_read_b64_tr_b16 v[104:105], v168 offset:16384\n\t"
      "ds_read_b64_tr_b16 v[106:107], v168 offset:16896\n\t"
      "v_mfma_f32_16x16x32_bf16 v[8:11], v[132:135], v[128:131], v[8:11]\n\t"
      "ds_read_b128 v[96:99], v160 offset:12288\n\t"
      "v_mfma_f32_16x16x32_bf16 v[4:7], v[136:139], v[124:127], v[4:7]\n\t"
      "ds_read_b128 v[100:103], v160 offset:26624\n\t"
      "v_mfma_f32_16x16x32_bf16 v[12:15], v[136:139], v[128:131], v[12:15]\n\t"
      "ds_read_b64_tr_b16 v[108:109], v169 offset:16384\n\t"
      "v_mfma_f32_16x16x32_bf16 v[24:27], v[140:143], v[124:127], v[24:27]\n\t"
      "ds_read_b64_tr_b16 v[110:111], v169 offset:16896\n\t"
      "v_mfma_f32_16x16x32_bf16 v[32:35], v[140:143], v[128:131], v[32:35]\n\t"
      "ds_read_b64_tr_b16 v[112:113], v168 offset:17408\n\t"
      "v_mfma_f32_16x16x32_bf16 v[16:19], v[140:143], v[120:123], v[16:19]\n\t"
      "ds_read_b64_tr_b16 v[114:115], v168 offset:17920\n\t"
      "v_mfma_f32_16x16x32_bf16 v[28:31], v[144:147], v[124:127], v[28:31]\n\t"
      "ds_read_b128 v[92:95], v164 offset:12288\n\t"
      "v_mfma_f32_16x16x32_bf16 v[36:39], v[144:147], v[128:131], v[36:39]\n\t"
      "ds_read_b64_tr_b16 v[116:117], v169 offset:17408\n\t"
      "v_mfma_f32_16x16x32_bf16 v[20:23], v[144:147], v[120:123], v[20:23]\n\t"
      "ds_read_b64_tr_b16 v[118:119], v169 offset:17920\n\t"
      "s_waitcnt lgkmcnt(11)\n\t"
      "v_mfma_f32_16x16x32_bf16 v[0:3], v[76:79], v[68:71], v[0:3]\n\t"
      "ds_read_b64_tr_b16 v[132:133], v168 offset:18432\n\t"
      "ds_read_b64_tr_b16 v[134:135], v168 offset:18944\n\t"
      "v_mfma_f32_16x16x32_bf16 v[8:11], v[76:79], v[72:75], v[8:11]\n\t"
      "ds_read_b128 v[124:127], v161 offset:13312\n\t"
      "v_mfma_f32_16x16x32_bf16 v[4:7], v[80:83], v[68:71], v[4:7]\n\t"
      "ds_read_b128 v[128:131], v161 offset:27648\n\t"
      "v_mfma_f32_16x16x32_bf16 v[12:15], v[80:83], v[72:75], v[12:15]\n\t"
      "ds_read_b64_tr_b16 v[136:137], v169 offset:18432\n\t"
      "v_mfma_f32_16x16x32_bf16 v[24:27], v[84:87], v[68:71], v[24:27]\n\t"
      "ds_read_b64_tr_b16 v[138:139], v169 offset:18944\n\t"
      "v_mfma_f32_16x16x32_bf16 v[32:35], v[84:87], v[72:75], v[32:35]\n\t"
      "ds_read_b64_tr_b16 v[140:141], v168 offset:19456\n\t"
      "v_mfma_f32_16x16x32_bf16 v[16:19], v[84:87], v[64:67], v[16:19]\n\t"
      "ds_read_b64_tr_b16 v[142:143], v168 offset:19968\n\t"
      "v_mfma_f32_16x16x32_bf16 v[28:31], v[88:91], v[68:71], v[28:31]\n\t"
      "ds_read_b128 v[120:123], v165 offset:13312\n\t"
      "v_mfma_f32_16x16x32_bf16 v[36:39], v[88:91], v[72:75], v[36:39]\n\t"
      "ds_read_b64_tr_b16 v[144:145], v169 offset:19456\n\t"
      "v_mfma_f32_16x16x32_bf16 v[20:23], v[88:91], v[64:67], v[20:23]\n\t"
      "ds_read_b64_tr_b16 v[146:147], v169 offset:19968\n\t"
      "s_barrier\n\t"
      "s_waitcnt lgkmcnt(11)\n\t"
      "v_mfma_f32_16x16x32_bf16 v[0:3], v[104:107], v[96:99], v[0:3]\n\t"
      "ds_read_b64_tr_b16 v[76:77], v166\n\t"
      "v_mfma_f32_16x16x32_bf16 v[8:11], v[104:107], v[100:103], v[8:11]\n\t"
      "ds_read_b64_tr_b16 v[78:79], v166 offset:512\n\t"
      "v_mfma_f32_16x16x32_bf16 v[4:7], v[108:111], v[96:99], v[4:7]\n\t"
      "ds_read_b128 v[68:71], v160 offset:4096\n\t"
      "v_mfma_f32_16x16x32_bf16 v[12:15], v[108:111], v[100:103], v[12:15]\n\t"
      "ds_read_b128 v[72:75], v160 offset:18432\n\t"
      "v_mfma_f32_16x16x32_bf16 v[24:27], v[112:115], v[96:99], v[24:27]\n\t"
      "ds_read_b128 v[64:67], v162 offset:4096\n\t"
      "v_mfma_f32_16x16x32_bf16 v[32:35], v[112:115], v[100:103], v[32:35]\n\t"
      "ds_read_b64_tr_b16 v[80:81], v167\n\t"
      "v_mfma_f32_16x16x32_bf16 v[16:19], v[112:115], v[92:95], v[16:19]\n\t"
      "ds_read_b64_tr_b16 v[82:83], v167 offset:512\n\t"
      "v_mfma_f32_16x16x32_bf16 v[28:31], v[116:119], v[96:99], v[28:31]\n\t"
      "v_mfma_f32_16x16x32_bf16 v[36:39], v[116:119], v[100:103], v[36:39]\n\t"
      "v_mfma_f32_16x16x32_bf16 v[20:23], v[116:119], v[92:95], v[20:23]\n\t"
      "s_waitcnt lgkmcnt(7)\n\t"
      "v_mfma_f32_16x16x32_bf16 v[0:3], v[132:135], v[124:127], v[0:3]\n\t"
      "ds_read_b64_tr_b16 v[104:105], v166 offset:1024\n\t"
      "v_mfma_f32_16x16x32_bf16 v[8:11], v[132:135], v[128:131], v[8:11]\n\t"
      "ds_read_b64_tr_b16 v[106:107], v166 offset:1536\n\t"
      "v_mfma_f32_16x16x32_bf16 v[4:7], v[136:139], v[124:127], v[4:7]\n\t"
      "ds_read_b128 v[96:99], v161 offset:5120\n\t"
      "v_mfma_f32_16x16x32_bf16 v[12:15], v[136:139], v[128:131], v[12:15]\n\t"
      "ds_read_b128 v[100:103], v161 offset:19456\n\t"
      "v_mfma_f32_16x16x32_bf16 v[24:27], v[140:143], v[124:127], v[24:27]\n\t"
      "ds_read_b128 v[92:95], v163 offset:5120\n\t"
      "v_mfma_f32_16x16x32_bf16 v[32:35], v[140:143], v[128:131], v[32:35]\n\t"
      "ds_read_b64_tr_b16 v[108:109], v167 offset:1024\n\t"
      "v_mfma_f32_16x16x32_bf16 v[16:19], v[140:143], v[120:123], v[16:19]\n\t"
      "ds_read_b64_tr_b16 v[110:111], v167 offset:1536\n\t"
      "v_mfma_f32_16x16x32_bf16 v[28:31], v[144:147], v[124:127], v[28:31]\n\t"
      "v_mfma_f32_16x16x32_bf16 v[36:39], v[144:147], v[128:131], v[36:39]\n\t"
      "v_mfma_f32_16x16x32_bf16 v[20:23], v[144:147], v[120:123], v[20:23]\n\t"
      "s_waitcnt lgkmcnt(7)\n\t"
      "v_mfma_f32_16x16x32_bf16 v[48:51], v[68:71], v[76:79], v[48:51]\n\t"
      "ds_read_b64_tr_b16 v[132:133], v166 offset:2048\n\t"
      "ds_read_b64_tr_b16 v[134:135], v166 offset:2560\n\t"
      "v_mfma_f32_16x16x32_bf16 v[56:59], v[72:75], v[76:79], v[56:59]\n\t"
      "ds_read_b128 v[124:127], v160 offset:6144\n\t"
      "v_mfma_f32_16x16x32_bf16 v[40:43], v[64:67], v[76:79], v[40:43]\n\t"
      "ds_read_b128 v[128:131], v160 offset:20480\n\t"
      "v_cvt_pk_bf16_f32 v84, v0, v1\n\t"
      "v_cvt_pk_bf16_f32 v85, v2, v3\n\t"
      "v_cvt_pk_bf16_f32 v86, v4, v5\n\t"
      "v_mfma_f32_16x16x32_bf16 v[52:55], v[68:71], v[80:83], v[52:55]\n\t"
      "ds_read_b128 v[120:123], v162 offset:6144\n\t"
      "v_cvt_pk_bf16_f32 v87, v6, v7\n\t"
      "v_cvt_pk_bf16_f32 v88, v8, v9\n\t"
      "v_cvt_pk_bf16_f32 v89, v10, v11\n\t"
      "v_mfma_f32_16x16x32_bf16 v[60:63], v[72:75], v[80:83], v[60:63]\n\t"
      "ds_read_b64_tr_b16 v[136:137], v167 offset:2048\n\t"
      "v_cvt_pk_bf16_f32 v90, v12, v13\n\t"
      "v_cvt_pk_bf16_f32 v91, v14, v15\n\t"
      "v_cvt_pk_bf16_f32 v152, v24, v25\n\t"
      "v_mfma_f32_16x16x32_bf16 v[44:47], v[64:67], v[80:83], v[44:47]\n\t"
      "ds_read_b64_tr_b16 v[138:139], v167 offset:2560\n\t"
      "v_cvt_pk_bf16_f32 v153, v26, v27\n\t"
      "v_cvt_pk_bf16_f32 v154, v28, v29\n\t"
      "v_cvt_pk_bf16_f32 v155, v30, v31\n\t"
      "s_waitcnt lgkmcnt(7)\n\t"
      "v_mfma_f32_16x16x32_bf16 v[48:51], v[96:99], v[104:107], v[48:51]\n\t"
      "ds_read_b64_tr_b16 v[76:77], v166 offset:3072\n\t"
      "ds_read_b64_tr_b16 v[78:79], v166 offset:3584\n\t"
      "v_cndmask_b32_e64 v86, v86, 0, s[68:69]\n\t"
      "v_cndmask_b32_e64 v87, v87, 0, s[68:69]\n\t"
      "v_cvt_pk_bf16_f32 v156, v32, v33\n\t"
      "v_mfma_f32_16x16x32_bf16 v[56:59], v[100:103], v[104:107], v[56:59]\n\t"
      "ds_read_b128 v[68:71], v161 offset:7168\n\t"
      "v_cvt_pk_bf16_f32 v157, v34, v35\n\t"
      "v_cvt_pk_bf16_f32 v158, v36, v37\n\t"
      "v_cvt_pk_bf16_f32 v159, v38, v39\n\t"
      "v_mfma_f32_16x16x32_bf16 v[40:43], v[92:95], v[104:107], v[40:43]\n\t"
      "ds_read_b128 v[72:75], v161 offset:21504\n\t"
      "v_cndmask_b32_e64 v90, v90, 0, s[68:69]\n\t"
      "v_cndmask_b32_e64 v91, v91, 0, s[68:69]\n\t"
      "v_cvt_pk_bf16_f32 v148, v16, v17\n\t"
      "v_mfma_f32_16x16x32_bf16 v[52:55], v[96:99], v[108:111], v[52:55]\n\t"
      "ds_read_b128 v[64:67], v165 offset:7168\n\t"
      "v_cvt_pk_bf16_f32 v149, v18, v19\n\t"
      "v_cvt_pk_bf16_f32 v150, v20, v21\n\t"
      "v_cvt_pk_bf16_f32 v151, v22, v23\n\t"
      "v_mfma_f32_16x16x32_bf16 v[60:63], v[100:103], v[108:111], v[60:63]\n\t"
      "ds_read_b64_tr_b16 v[80:81], v167 offset:3072\n\t"
      "v_mov_b32_e32 v34, 0x3e8b97f6\n\t"
      "v_mfma_f32_16x16x32_bf16 v[28:31], v[156:159], v[88:91], v[176:179]\n\t"
      "v_mfma_f32_16x16x32_bf16 v[24:27], v[152:155], v[88:91], v[172:175]\n\t"
      "v_mfma_f32_16x16x32_bf16 v[44:47], v[92:95], v[108:111], v[44:47]\n\t"
      "ds_read_b64_tr_b16 v[82:83], v167 offset:3584\n\t"
      "v_mfma_f32_16x16x32_bf16 v[20:23], v[152:155], v[84:87], v[176:179]\n\t"
      "v_mfma_f32_16x16x32_bf16 v[16:19], v[148:151], v[84:87], v[180:183]\n\t"
      "s_barrier\n\t"
      "s_waitcnt lgkmcnt(7)\n\t"
      "v_mfma_f32_16x16x32_bf16 v[48:51], v[124:127], v[132:135], v[48:51]\n\t"
      "ds_read_b64_tr_b16 v[104:105], v166 offset:16384\n\t"
      "ds_read_b64_tr_b16 v[106:107], v166 offset:16896\n\t"
      "v_mfma_f32_16x16x32_bf16 v[56:59], v[128:131], v[132:135], v[56:59]\n\t"
      "ds_read_b128 v[96:99], v160 offset:8192\n\t"
      "v_max_f32_e32 v24, v24, v28\n\t"
      "v_mfma_f32_16x16x32_bf16 v[40:43], v[120:123], v[132:135], v[40:43]\n\t"
      "ds_read_b128 v[100:103], v160 offset:22528\n\t"
      "v_max_f32_e32 v25, v25, v29\n\t"
      "v_max_f32_e32 v26, v26, v30\n\t"
      "v_max_f32_e32 v27, v27, v31\n\t"
      "v_mfma_f32_16x16x32_bf16 v[52:55], v[124:127], v[136:139], v[52:55]\n\t"
      "ds_read_b128 v[92:95], v164 offset:8192\n\t"
      "v_max_f32_e32 v16, v16, v20\n\t"
      "v_max_f32_e32 v17, v17, v21\n\t"
      "v_max_f32_e32 v18, v18, v22\n\t"
      "v_mfma_f32_16x16x32_bf16 v[60:63], v[128:131], v[136:139], v[60:63]\n\t"
      "ds_read_b64_tr_b16 v[108:109], v167 offset:16384\n\t"
      "v_max_f32_e32 v19, v19, v23\n\t"
      "v_max3_f32 v33, v24, v25, v26\n\t"
      "v_max3_f32 v32, v16, v17, v18\n\t"
      "v_mfma_f32_16x16x32_bf16 v[44:47], v[120:123], v[136:139], v[44:47]\n\t"
      "ds_read_b64_tr_b16 v[110:111], v167 offset:16896\n\t"
      "v_max_f32_e32 v33, v33, v27\n\t"
      "v_max_f32_e32 v32, v32, v19\n\t"
      "s_waitcnt lgkmcnt(7)\n\t"
      "v_mfma_f32_16x16x32_bf16 v[48:51], v[68:71], v[76:79], v[48:51]\n\t"
      "ds_read_b64_tr_b16 v[132:133], v166 offset:17408\n\t"
      "ds_read_b64_tr_b16 v[134:135], v166 offset:17920\n\t"
      "v_permlane16_swap_b32_e32 v32, v33\n\t"
      "v_max_f32_e32 v32, v32, v33\n\t"
      "v_mov_b32_e32 v33, v32\n\t"
      "v_mfma_f32_16x16x32_bf16 v[56:59], v[72:75], v[76:79], v[56:59]\n\t"
      "ds_read_b128 v[124:127], v161 offset:9216\n\t"
      "v_permlane32_swap_b32_e32 v32, v33\n\t"
      "v_max_f32_e32 v32, v32, v33\n\t"
      "v_mfma_f32_16x16x32_bf16 v[40:43], v[64:67], v[76:79], v[40:43]\n\t"
      "ds_read_b128 v[128:131], v161 offset:23552\n\t"
      "v_mov_b32_e32 v33, v32\n\t"
      "s_nop 1\n\t"
      "v_permlane16_swap_b32_e32 v32, v33\n\t"
      "v_mfma_f32_16x16x32_bf16 v[52:55], v[68:71], v[80:83], v[52:55]\n\t"
      "ds_read_b128 v[120:123], v165 offset:9216\n\t"
      "v_mul_f32_e64 v33, -v33, v34\n\t"
      "v_mul_f32_e64 v32, -v32, v34\n\t"
      "v_fma_f32 v24, v24, v34, v33\n\t"
      "v_mfma_f32_16x16x32_bf16 v[60:63], v[72:75], v[80:83], v[60:63]\n\t"
      "ds_read_b64_tr_b16 v[136:137], v167 offset:17408\n\t"
      "v_fma_f32 v25, v25, v34, v33\n\t"
      "v_fma_f32 v26, v26, v34, v33\n\t"
      "v_fma_f32 v27, v27, v34, v33\n\t"
      "v_mfma_f32_16x16x32_bf16 v[44:47], v[64:67], v[80:83], v[44:47]\n\t"
      "ds_read_b64_tr_b16 v[138:139], v167 offset:17920\n\t"
      "v_fma_f32 v16, v16, v34, v32\n\t"
      "v_fma_f32 v17, v17, v34, v32\n\t"
      "v_fma_f32 v18, v18, v34, v32\n\t"
      "s_waitcnt lgkmcnt(7)\n\t"
      "v_mfma_f32_16x16x32_bf16 v[48:51], v[96:99], v[104:107], v[48:51]\n\t"
      "ds_read_b64_tr_b16 v[76:77], v166 offset:18432\n\t"
      "ds_read_b64_tr_b16 v[78:79], v166 offset:18944\n\t"
      "v_fma_f32 v19, v19, v34, v32\n\t"
      "v_exp_f32_e32 v24, v24\n\t"
      "v_mfma_f32_16x16x32_bf16 v[56:59], v[100:103], v[104:107], v[56:59]\n\t"
      "ds_read_b128 v[68:71], v160 offset:10240\n\t"
      "v_exp_f32_e32 v25, v25\n\t"
      "v_exp_f32_e32 v26, v26\n\t"
      "v_mfma_f32_16x16x32_bf16 v[40:43], v[92:95], v[104:107], v[40:43]\n\t"
      "ds_read_b128 v[72:75], v160 offset:24576\n\t"
      "v_exp_f32_e32 v27, v27\n\t"
      "v_mfma_f32_16x16x32_bf16 v[52:55], v[96:99], v[108:111], v[52:55]\n\t"
      "ds_read_b128 v[64:67], v164 offset:10240\n\t"
      "v_exp_f32_e32 v16, v16\n\t"
      "v_exp_f32_e32 v17, v17\n\t"
      "v_mfma_f32_16x16x32_bf16 v[60:63], v[100:103], v[108:111], v[60:63]\n\t"
      "ds_read_b64_tr_b16 v[80:81], v167 offset:18432\n\t"
      "v_exp_f32_e32 v18, v18\n\t"
      "v_mfma_f32_16x16x32_bf16 v[44:47], v[92:95], v[108:111], v[44:47]\n\t"
      "ds_read_b64_tr_b16 v[82:83], v167 offset:18944\n\t"
      "v_exp_f32_e32 v19, v19\n\t"
      "v_add_f32_e32 v33, v24, v25\n\t"
      "s_waitcnt lgkmcnt(7)\n\t"
      "v_mfma_f32_16x16x32_bf16 v[48:51], v[124:127], v[132:135], v[48:51]\n\t"
      "ds_read_b64_tr_b16 v[104:105], v166 offset:19456\n\t"
      "ds_read_b64_tr_b16 v[106:107], v166 offset:19968\n\t"
      "v_add_f32_e32 v28, v26, v27\n\t"
      "v_add_f32_e32 v32, v16, v17\n\t"
      "v_add_f32_e32 v20, v18, v19\n\t"
      "v_mfma_f32_16x16x32_bf16 v[56:59], v[128:131], v[132:135], v[56:59]\n\t"
      "ds_read_b128 v[96:99], v161 offset:11264\n\t"
      "v_add_f32_e32 v33, v33, v28\n\t"
      "v_add_f32_e32 v32, v32, v20\n\t"
      "v_mfma_f32_16x16x32_bf16 v[40:43], v[120:123], v[132:135], v[40:43]\n\t"
      "ds_read_b128 v[100:103], v161 offset:25600\n\t"
      "v_permlane16_swap_b32_e32 v32, v33\n\t"
      "v_add_f32_e32 v32, v32, v33\n\t"
      "v_mov_b32_e32 v33, v32\n\t"
      "v_mfma_f32_16x16x32_bf16 v[52:55], v[124:127], v[136:139], v[52:55]\n\t"
      "ds_read_b128 v[92:95], v165 offset:11264\n\t"
      "v_permlane32_swap_b32_e32 v32, v33\n\t"
      "v_add_f32_e32 v32, v32, v33\n\t"
      "v_mfma_f32_16x16x32_bf16 v[60:63], v[128:131], v[136:139], v[60:63]\n\t"
      "ds_read_b64_tr_b16 v[108:109], v167 offset:19456\n\t"
      "v_mov_b32_e32 v33, v32\n\t"
      "s_nop 1\n\t"
      "v_permlane16_swap_b32_e32 v32, v33\n\t"
      "v_mfma_f32_16x16x32_bf16 v[44:47], v[120:123], v[136:139], v[44:47]\n\t"
      "ds_read_b64_tr_b16 v[110:111], v167 offset:19968\n\t"
      "v_rcp_f32_e32 v33, v33\n\t"
      "v_rcp_f32_e32 v32, v32\n\t"
      "s_barrier\n\t"
      "s_waitcnt lgkmcnt(7)\n\t"
      "v_mfma_f32_16x16x32_bf16 v[48:51], v[68:71], v[76:79], v[48:51]\n\t"
      "ds_read_b64_tr_b16 v[132:133], v170 offset:32768\n\t"
      "ds_read_b64_tr_b16 v[134:135], v170 offset:33280\n\t"
      "v_mul_f32_e32 v24, v24, v33\n\t"
      "v_mul_f32_e32 v25, v25, v33\n\t"
      "v_mfma_f32_16x16x32_bf16 v[56:59], v[72:75], v[76:79], v[56:59]\n\t"
      "ds_read_b128 v[124:127], v160 offset:12288\n\t"
      "v_mul_f32_e32 v26, v26, v33\n\t"
      "v_mul_f32_e32 v27, v27, v33\n\t"
      "v_mul_f32_e32 v16, v16, v32\n\t"
      "v_mfma_f32_16x16x32_bf16 v[40:43], v[64:67], v[76:79], v[40:43]\n\t"
      "ds_read_b128 v[128:131], v160 offset:26624\n\t"
      "v_mul_f32_e32 v17, v17, v32\n\t"
      "v_mul_f32_e32 v18, v18, v32\n\t"
      "v_mul_f32_e32 v19, v19, v32\n\t"
      "v_mfma_f32_16x16x32_bf16 v[52:55], v[68:71], v[80:83], v[52:55]\n\t"
      "ds_read_b128 v[120:123], v164 offset:12288\n\t"
      "v_cvt_pk_bf16_f32 v4, v24, v25\n\t"
      "v_cvt_pk_bf16_f32 v5, v26, v27\n\t"
      "v_cvt_pk_bf16_f32 v0, v16, v17\n\t"
      "v_mfma_f32_16x16x32_bf16 v[60:63], v[72:75], v[80:83], v[60:63]\n\t"
      "ds_read_b64_tr_b16 v[136:137], v171 offset:32768\n\t"
      "v_cvt_pk_bf16_f32 v1, v18, v19\n\t"
      "v_and_b32_e32 v6, v184, v4\n\t"
      "v_and_b32_e32 v7, v185, v5\n\t"
      "v_mfma_f32_16x16x32_bf16 v[44:47], v[64:67], v[80:83], v[44:47]\n\t"
      "ds_read_b64_tr_b16 v[138:139], v171 offset:33280\n\t"
      "v_and_b32_e32 v2, v184, v0\n\t"
      "v_and_b32_e32 v3, v185, v1\n\t"
      "v_xor_b32_e32 v4, v4, v6\n\t"
      "s_waitcnt lgkmcnt(7)\n\t"
      "v_mfma_f32_16x16x32_bf16 v[48:51], v[96:99], v[104:107], v[48:51]\n\t"
      "ds_read_b64_tr_b16 v[76:77], v170 offset:33792\n\t"
      "ds_read_b64_tr_b16 v[78:79], v170 offset:34304\n\t"
      "v_xor_b32_e32 v5, v5, v7\n\t"
      "v_xor_b32_e32 v0, v0, v2\n\t"
      "v_xor_b32_e32 v1, v1, v3\n\t"
      "v_mfma_f32_16x16x32_bf16 v[56:59], v[100:103], v[104:107], v[56:59]\n\t"
      "ds_read_b128 v[68:71], v161 offset:13312\n\t"
      "v_mfma_f32_16x16x32_bf16 v[40:43], v[92:95], v[104:107], v[40:43]\n\t"
      "ds_read_b128 v[72:75], v161 offset:27648\n\t"
      "v_mfma_f32_16x16x32_bf16 v[52:55], v[96:99], v[108:111], v[52:55]\n\t"
      "ds_read_b128 v[64:67], v165 offset:13312\n\t"
      "v_mfma_f32_16x16x32_bf16 v[60:63], v[100:103], v[108:111], v[60:63]\n\t"
      "ds_read_b64_tr_b16 v[80:81], v171 offset:33792\n\t"
      "v_mfma_f32_16x16x32_bf16 v[44:47], v[92:95], v[108:111], v[44:47]\n\t"
      "ds_read_b64_tr_b16 v[82:83], v171 offset:34304\n\t"
      "s_waitcnt lgkmcnt(7)\n\t"
      "v_mfma_f32_16x16x32_bf16 v[48:51], v[124:127], v[132:135], v[48:51]\n\t"
      "v_mfma_f32_16x16x32_bf16 v[56:59], v[128:131], v[132:135], v[56:59]\n\t"
      "v_mfma_f32_16x16x32_bf16 v[40:43], v[120:123], v[132:135], v[40:43]\n\t"
      "v_mfma_f32_16x16x32_bf16 v[52:55], v[124:127], v[136:139], v[52:55]\n\t"
      "v_mfma_f32_16x16x32_bf16 v[60:63], v[128:131], v[136:139], v[60:63]\n\t"
      "v_mfma_f32_16x16x32_bf16 v[44:47], v[120:123], v[136:139], v[44:47]\n\t"
      "s_waitcnt lgkmcnt(0)\n\t"
      "v_mfma_f32_16x16x32_bf16 v[48:51], v[68:71], v[76:79], v[48:51]\n\t"
      "v_mfma_f32_16x16x32_bf16 v[56:59], v[72:75], v[76:79], v[56:59]\n\t"
      "v_mfma_f32_16x16x32_bf16 v[40:43], v[64:67], v[76:79], v[40:43]\n\t"
      "v_mfma_f32_16x16x32_bf16 v[52:55], v[68:71], v[80:83], v[52:55]\n\t"
      "v_mfma_f32_16x16x32_bf16 v[60:63], v[72:75], v[80:83], v[60:63]\n\t"
      "v_mfma_f32_16x16x32_bf16 v[44:47], v[64:67], v[80:83], v[44:47]\n\t"
      "s_nop 2\n\t"
      "v_cvt_pk_bf16_f32 v66, v48, v49\n\t"
      "v_cvt_pk_bf16_f32 v67, v50, v51\n\t"
      "v_cvt_pk_bf16_f32 v68, v56, v57\n\t"
      "v_cvt_pk_bf16_f32 v69, v58, v59\n\t"
      "v_cvt_pk_bf16_f32 v64, v40, v41\n\t"
      "v_cvt_pk_bf16_f32 v65, v42, v43\n\t"
      "v_mfma_f32_16x16x32_bf16 v[88:91], v[66:69], v[4:7], 0\n\t"
      "s_nop 0\n\t"
      "v_mfma_f32_16x16x32_bf16 v[80:83], v[64:67], v[0:3], 0\n\t"
      "v_cvt_pk_bf16_f32 v72, v52, v53\n\t"
      "v_cvt_pk_bf16_f32 v73, v54, v55\n\t"
      "v_cvt_pk_bf16_f32 v74, v60, v61\n\t"
      "v_cvt_pk_bf16_f32 v75, v62, v63\n\t"
      "v_cvt_pk_bf16_f32 v70, v44, v45\n\t"
      "v_cvt_pk_bf16_f32 v71, v46, v47\n\t"
      "v_mfma_f32_16x16x32_bf16 v[92:95], v[72:75], v[4:7], 0\n\t"
      "s_nop 0\n\t"
      "v_mfma_f32_16x16x32_bf16 v[84:87], v[70:73], v[0:3], 0\n\t"
      "v_cvt_pk_bf16_f32 v152, v88, v89\n\t"
      "v_cvt_pk_bf16_f32 v153, v90, v91\n\t"
      "v_min_u32_e32 v152, -2, v152\n\t"
      "v_min_u32_e32 v153, -2, v153\n\t"
      "v_cvt_pk_bf16_f32 v148, v80, v81\n\t"
      "v_cvt_pk_bf16_f32 v149, v82, v83\n\t"
      "v_min_u32_e32 v148, -2, v148\n\t"
      "v_min_u32_e32 v149, -2, v149\n\t"
      "v_cvt_pk_bf16_f32 v154, v92, v93\n\t"
      "v_cvt_pk_bf16_f32 v155, v94, v95\n\t"
      "v_cndmask_b32_e64 v154, v154, 0, s[68:69]\n\t"
      "v_min_u32_e32 v154, -2, v154\n\t"
      "v_cndmask_b32_e64 v155, v155, 0, s[68:69]\n\t"
      "v_min_u32_e32 v155, -2, v155\n\t"
      "global_store_dwordx4 v244, v[152:155], s[44:45] sc1\n\t"
      "v_cvt_pk_bf16_f32 v150, v84, v85\n\t"
      "v_cvt_pk_bf16_f32 v151, v86, v87\n\t"
      "v_cndmask_b32_e64 v150, v150, 0, s[68:69]\n\t"
      "v_min_u32_e32 v150, -2, v150\n\t"
      "v_cndmask_b32_e64 v151, v151, 0, s[68:69]\n\t"
      "v_min_u32_e32 v151, -2, v151\n\t"
      "global_store_dwordx4 v244, v[148:151], s[42:43] sc1\n\t"
      "s_barrier\n\t"
      "s_cmp_eq_u32 s26, 7\n\t"
      "s_cbranch_scc1 SPARE_%=\n\t"
      "s_lshl_b32 s34, s25, 16\n\t"
      "s_lshl2_add_u32 s34, s39, s34\n\t"
      "s_add_u32 s34, s34, 4096\n\t"
      "s_add_u32 s42, s20, s34\n\t"
      "s_addc_u32 s43, s21, 0\n\t"
      "s_add_u32 s44, s42, 8192\n\t"
      "s_addc_u32 s45, s43, 0\n\t"
      "s_lshl_b32 s72, s24, 10\n\t"
      "s_add_u32 s72, s72, 98304\n\t"
      "v_xor_b32_e32 v203, v197, v198\n\t"
      "v_lshl_add_u32 v203, v203, 4, s72\n\t"
      "v_lshl_add_u32 v203, v197, 7, v203\n\t"
      "ds_write_b128 v203, v[204:207]\n\t"
      "s_and_b32 s35, s25, 3\n\t"
      "s_lshl2_add_u32 s31, s26, s35\n\t"
      "s_and_b32 s35, s2, 7\n\t"
      "s_mul_i32 s34, s35, 14\n\t"
      "s_lshl4_add_u32 s35, s35, -14\n\t"
      "s_cmp_lt_u32 s31, 14\n\t"
      "s_cselect_b32 s34, s34, s35\n\t"
      "s_cselect_b32 s36, 1, 0\n\t"
      "s_or_b32 s27, s27, s36\n\t"
      "s_add_u32 s34, s34, s31\n\t"
      "s_lshr_b32 s29, s34, 2\n\t"
      "s_and_b32 s32, s34, 3\n\t"
      "s_mul_i32 s30, s32, 7\n\t"
      "v_and_b32_e32 v199, 60, v192\n\t"
      "v_and_b32_e32 v200, 24, v192\n\t"
      "v_lshlrev_b32_e32 v202, 3, v196\n\t"
      "v_lshl_or_b32 v202, v200, 2, v202\n\t"
      "v_lshl_or_b32 v202, v195, 7, v202\n\t"
      "v_lshl_or_b32 v202, v193, 10, v202\n\t"
      "v_add_u32_e32 v224, s39, v202\n\t"
      "v_xor_b32_e32 v225, 32, v224\n\t"
      "v_xor_b32_e32 v226, 64, v224\n\t"
      "v_xor_b32_e32 v227, 96, v224\n\t"
      "s_lshl_b32 s34, s24, 12\n\t"
      "s_add_u32 s34, s34, 98304\n\t"
      "v_add_u32_e32 v245, s34, v244\n\t"
      "v_lshlrev_b32_e32 v199, 2, v199\n\t"
      "s_lshl_b32 s34, s24, 8\n\t"
      "v_lshl_add_u32 v247, v192, 2, s34\n\t"
      "v_and_b32_e32 v200, 2, v192\n\t"
      "v_mul_u32_u24_e32 v202, 576, v200\n\t"
      "v_xor_b32_e32 v199, v202, v199\n\t"
      "v_and_b32_e32 v202, 1, v192\n\t"
      "v_lshl_or_b32 v199, v202, 3, v199\n\t"
      "s_and_b32 s35, s34, 0x400\n\t"
      "s_add_u32 s34, s34, s35\n\t"
      "s_add_u32 s34, s34, 98304\n\t"
      "v_add_u32_e32 v246, s34, v199\n\t"
      "v_lshlrev_b32_e32 v199, 2, v202\n\t"
      "v_lshl_or_b32 v199, v200, 4, v199\n\t"
      "s_waitcnt lgkmcnt(0)\n\t"
      "s_barrier\n\t"
      "v_xor_b32_e32 v210, 32, v228\n\t"
      "v_xor_b32_e32 v212, 64, v228\n\t"
      "v_xor_b32_e32 v214, 96, v228\n\t"
      "ds_read_b64 v[208:209], v228\n\t"
      "ds_read_b64 v[210:211], v210\n\t"
      "ds_read_b64 v[212:213], v212\n\t"
      "ds_read_b64 v[214:215], v214\n\t"
      "ds_read_b64_tr_b16 v[0:1], v216 offset:7168\n\t"
      "ds_read_b64_tr_b16 v[2:3], v220 offset:7168\n\t"
      "ds_read_b64_tr_b16 v[4:5], v217 offset:7168\n\t"
      "ds_read_b64_tr_b16 v[6:7], v221 offset:7168\n\t"
      "ds_read_b64_tr_b16 v[8:9], v218 offset:7168\n\t"
      "ds_read_b64_tr_b16 v[10:11], v222 offset:7168\n\t"
      "ds_read_b64_tr_b16 v[12:13], v219 offset:7168\n\t"
      "ds_read_b64_tr_b16 v[14:15], v223 offset:7168\n\t"
      "ds_read_b64_tr_b16 v[16:17], v218 offset:10752\n\t"
      "ds_read_b64_tr_b16 v[18:19], v222 offset:10752\n\t"
      "ds_read_b64_tr_b16 v[20:21], v219 offset:10752\n\t"
      "ds_read_b64_tr_b16 v[22:23], v223 offset:10752\n\t"
      "ds_read_b64_tr_b16 v[24:25], v216 offset:10752\n\t"
      "ds_read_b64_tr_b16 v[26:27], v220 offset:10752\n\t"
      "ds_read_b64_tr_b16 v[28:29], v217 offset:10752\n\t"
      "ds_read_b64_tr_b16 v[30:31], v221 offset:10752\n\t"
      "ds_read_b64_tr_b16 v[32:33], v216 offset:14336\n\t"
      "ds_read_b64_tr_b16 v[34:35], v220 offset:14336\n\t"
      "ds_read_b64_tr_b16 v[36:37], v217 offset:14336\n\t"
      "ds_read_b64_tr_b16 v[38:39], v221 offset:14336\n\t"
      "ds_read_b64_tr_b16 v[40:41], v218 offset:14336\n\t"
      "ds_read_b64_tr_b16 v[42:43], v222 offset:14336\n\t"
      "ds_read_b64_tr_b16 v[44:45], v219 offset:14336\n\t"
      "ds_read_b64_tr_b16 v[46:47], v223 offset:14336\n\t"
      "ds_read_b64_tr_b16 v[48:49], v218 offset:17920\n\t"
      "ds_read_b64_tr_b16 v[50:51], v222 offset:17920\n\t"
      "ds_read_b64_tr_b16 v[52:53], v219 offset:17920\n\t"
      "ds_read_b64_tr_b16 v[54:55], v223 offset:17920\n\t"
      "ds_read_b64_tr_b16 v[56:57], v216 offset:17920\n\t"
      "ds_read_b64_tr_b16 v[58:59], v220 offset:17920\n\t"
      "ds_read_b64_tr_b16 v[60:61], v217 offset:17920\n\t"
      "ds_read_b64_tr_b16 v[62:63], v221 offset:17920\n\t"
      "ds_read_b64_tr_b16 v[64:65], v216 offset:21504\n\t"
      "ds_read_b64_tr_b16 v[66:67], v220 offset:21504\n\t"
      "ds_read_b64_tr_b16 v[68:69], v217 offset:21504\n\t"
      "ds_read_b64_tr_b16 v[70:71], v221 offset:21504\n\t"
      "ds_read_b64_tr_b16 v[72:73], v218 offset:21504\n\t"
      "ds_read_b64_tr_b16 v[74:75], v222 offset:21504\n\t"
      "ds_read_b64_tr_b16 v[76:77], v219 offset:21504\n\t"
      "ds_read_b64_tr_b16 v[78:79], v223 offset:21504\n\t"
      "ds_read_b64_tr_b16 v[80:81], v218 offset:25088\n\t"
      "ds_read_b64_tr_b16 v[82:83], v222 offset:25088\n\t"
      "ds_read_b64_tr_b16 v[84:85], v219 offset:25088\n\t"
      "ds_read_b64_tr_b16 v[86:87], v223 offset:25088\n\t"
      "ds_read_b64_tr_b16 v[88:89], v216 offset:25088\n\t"
      "ds_read_b64_tr_b16 v[90:91], v220 offset:25088\n\t"
      "ds_read_b64_tr_b16 v[92:93], v217 offset:25088\n\t"
      "ds_read_b64_tr_b16 v[94:95], v221 offset:25088\n\t"
      "ds_read_b64_tr_b16 v[96:97], v216 offset:28672\n\t"
      "ds_read_b64_tr_b16 v[98:99], v220 offset:28672\n\t"
      "ds_read_b64_tr_b16 v[100:101], v217 offset:28672\n\t"
      "ds_read_b64_tr_b16 v[102:103], v221 offset:28672\n\t"
      "ds_read_b64_tr_b16 v[104:105], v218 offset:28672\n\t"
      "ds_read_b64_tr_b16 v[106:107], v222 offset:28672\n\t"
      "ds_read_b64_tr_b16 v[108:109], v219 offset:28672\n\t"
      "ds_read_b64_tr_b16 v[110:111], v223 offset:28672\n\t"
      "ds_read_b64_tr_b16 v[112:113], v218 offset:32256\n\t"
      "ds_read_b64_tr_b16 v[114:115], v222 offset:32256\n\t"
      "ds_read_b64_tr_b16 v[116:117], v219 offset:32256\n\t"
      "ds_read_b64_tr_b16 v[118:119], v223 offset:32256\n\t"
      "ds_read_b64_tr_b16 v[120:121], v216 offset:32256\n\t"
      "ds_read_b64_tr_b16 v[122:123], v220 offset:32256\n\t"
      "ds_read_b64_tr_b16 v[124:125], v217 offset:32256\n\t"
      "ds_read_b64_tr_b16 v[126:127], v221 offset:32256\n\t"
      "s_sleep 2\n\t"
      "global_load_dwordx4 v[128:131], v244, s[42:43] offset:-2048 sc1\n\t"
      "global_load_dwordx4 v[132:135], v244, s[42:43] offset:-1024 sc1\n\t"
      "global_load_dwordx4 v[136:139], v244, s[42:43] sc1\n\t"
      "global_load_dwordx4 v[140:143], v244, s[42:43] offset:1024 sc1\n\t"
      "global_load_dwordx4 v[144:147], v244, s[42:43] offset:2048 sc1\n\t"
      "global_load_dwordx4 v[148:151], v244, s[42:43] offset:3072 sc1\n\t"
      "global_load_dwordx4 v[152:155], v244, s[44:45] offset:-4096 sc1\n\t"
      "global_load_dwordx4 v[156:159], v244, s[44:45] offset:-3072 sc1\n\t"
      "global_load_dwordx4 v[160:163], v244, s[44:45] offset:-2048 sc1\n\t"
      "global_load_dwordx4 v[164:167], v244, s[44:45] offset:-1024 sc1\n\t"
      "s_min_u32 s34, s29, 27\n\t"
      "s_lshl_b32 s34, s34, 7\n\t"
      "s_lshr_b32 s35, s24, 2\n\t"
      "s_lshl_b32 s35, s35, 6\n\t"
      "s_add_u32 s34, s34, s35\n\t"
      "s_lshr_b32 s35, s39, 9\n\t"
      "s_add_u32 s34, s34, s35\n\t"
      "v_add_u32_e32 v248, s34, v199\n\t"
      "s_add_u32 s48, s20, 8388608\n\t"
      "s_addc_u32 s49, s21, 0\n\t"
      "v_and_b32_e32 v199, 16, v192\n\t"
      "v_lshlrev_b32_e32 v199, 1, v199\n\t"
      "v_and_b32_e32 v200, 32, v192\n\t"
      "s_lshl_b32 s34, s28, 18\n\t"
      "s_lshl_b32 s35, s25, 11\n\t"
      "s_add_u32 s34, s34, s35\n\t"
      "s_lshr_b32 s35, s39, 3\n\t"
      "s_add_u32 s34, s34, s35\n\t"
      "v_and_b32_e32 v201, 0x1f0, v244\n\t"
      "v_lshl_or_b32 v201, v200, 11, v201\n\t"
      "v_add_u32_e32 v229, s34, v201\n\t"
      "v_add_u32_e32 v230, 131072, v229\n\t"
      "s_and_b32 s34, s25, 30\n\t"
      "s_lshr_b32 s35, s39, 13\n\t"
      "s_add_u32 s34, s34, s35\n\t"
      "s_mul_i32 s34, s34, 7\n\t"
      "s_add_u32 s34, s34, s28\n\t"
      "s_lshl_b32 s34, s34, 3\n\t"
      "s_and_b32 s35, s25, 1\n\t"
      "s_add_u32 s34, s34, s35\n\t"
      "s_lshl_b32 s34, s34, 10\n\t"
      "s_and_b32 s35, s24, 1\n\t"
      "s_lshl_b32 s35, s35, 9\n\t"
      "s_add_u32 s34, s34, s35\n\t"
      "s_add_u32 s34, s34, 6293504\n\t"
      "v_lshrrev_b32_e32 v201, 1, v200\n\t"
      "v_lshl_or_b32 v201, v199, 6, v201\n\t"
      "v_lshl_or_b32 v201, v194, 5, v201\n\t"
      "v_add_u32_e32 v241, s34, v201\n\t"
      "s_lshl_b32 s34, s24, 12\n\t"
      "s_add_u32 s34, s34, 131072\n\t"
      "v_xor_b32_e32 v201, v193, v194\n\t"
      "v_lshlrev_b32_e32 v201, 3, v201\n\t"
      "v_lshl_or_b32 v201, v194, 7, v201\n\t"
      "v_add_u32_e32 v233, s34, v201\n\t"
      "v_lshrrev_b32_e32 v199, 2, v199\n\t"
      "v_or_b32_e32 v199, v199, v195\n\t"
      "v_lshrrev_b32_e32 v200, 3, v200\n\t"
      "v_or_b32_e32 v200, v196, v200\n\t"
      "v_xor_b32_e32 v200, v200, v199\n\t"
      "v_lshlrev_b32_e32 v200, 3, v200\n\t"
      "v_lshl_or_b32 v200, v199, 7, v200\n\t"
      "v_add_u32_e32 v237, s34, v200\n\t"
      "v_xor_b32_e32 v234, 32, v233\n\t"
      "v_xor_b32_e32 v238, 32, v237\n\t"
      "v_xor_b32_e32 v235, 64, v233\n\t"
      "v_xor_b32_e32 v239, 64, v237\n\t"
      "v_xor_b32_e32 v236, 96, v233\n\t"
      "v_xor_b32_e32 v240, 96, v237\n\t"
      "s_waitcnt lgkmcnt(14)\n\t"
      "v_lshlrev_b32_e32 v192, 16, v208\n\t"
      "v_and_b32_e32 v193, 0xffff0000, v208\n\t"
      "v_lshlrev_b32_e32 v194, 16, v209\n\t"
      "v_and_b32_e32 v195, 0xffff0000, v209\n\t"
      "v_lshlrev_b32_e32 v196, 16, v210\n\t"
      "v_and_b32_e32 v197, 0xffff0000, v210\n\t"
      "v_lshlrev_b32_e32 v198, 16, v211\n\t"
      "v_and_b32_e32 v199, 0xffff0000, v211\n\t"
      "v_lshlrev_b32_e32 v200, 16, v212\n\t"
      "v_and_b32_e32 v201, 0xffff0000, v212\n\t"
      "v_lshlrev_b32_e32 v202, 16, v213\n\t"
      "v_and_b32_e32 v203, 0xffff0000, v213\n\t"
      "v_lshlrev_b32_e32 v204, 16, v214\n\t"
      "v_and_b32_e32 v205, 0xffff0000, v214\n\t"
      "v_lshlrev_b32_e32 v206, 16, v215\n\t"
      "v_and_b32_e32 v207, 0xffff0000, v215\n\t"
      "global_load_dwordx4 v[168:171], v244, s[44:45] sc1\n\t"
      "global_load_dwordx4 v[172:175], v244, s[44:45] offset:1024 sc1\n\t"
      "global_load_dwordx4 v[176:179], v244, s[44:45] offset:2048 sc1\n\t"
      "global_load_dwordx4 v[180:183], v244, s[44:45] offset:3072 sc1\n\t"
      "global_load_dwordx4 v[184:187], v244, s[42:43] offset:-4096 sc1\n\t"
      "global_load_dwordx4 v[188:191], v244, s[42:43] offset:-3072 sc1\n\t"
      "global_load_dword v249, v248, s[12:13]\n\t"
      "s_barrier\n\t"
      "s_waitcnt lgkmcnt(0)\n\t"
      "LGM_%=:\n\t"
      "s_waitcnt vmcnt(15)\n\t"
      "v_max_u32_e32 v232, v128, v132\n\t"
      "v_cmp_eq_u32_e32 vcc, -1, v232\n\t"
      "s_cbranch_vccnz LO0R_%=\n\t"
      "LO0B_%=:\n\t"
      "v_mfma_f32_16x16x32_bf16 v[192:195], v[0:3], v[128:131], v[192:195]\n\t"
      "ds_read_b64_tr_b16 v[0:1], v216 offset:35840\n\t"
      "ds_read_b64_tr_b16 v[2:3], v220 offset:35840\n\t"
      "v_mfma_f32_16x16x32_bf16 v[196:199], v[4:7], v[128:131], v[196:199]\n\t"
      "ds_read_b64_tr_b16 v[4:5], v217 offset:35840\n\t"
      "v_mfma_f32_16x16x32_bf16 v[200:203], v[8:11], v[128:131], v[200:203]\n\t"
      "ds_read_b64_tr_b16 v[6:7], v221 offset:35840\n\t"
      "v_mfma_f32_16x16x32_bf16 v[204:207], v[12:15], v[128:131], v[204:207]\n\t"
      "ds_read_b64_tr_b16 v[8:9], v218 offset:35840\n\t"
      "ds_read_b64_tr_b16 v[10:11], v222 offset:35840\n\t"
      "v_mfma_f32_16x16x32_bf16 v[192:195], v[16:19], v[132:135], v[192:195]\n\t"
      "ds_read_b64_tr_b16 v[12:13], v219 offset:35840\n\t"
      "v_mfma_f32_16x16x32_bf16 v[196:199], v[20:23], v[132:135], v[196:199]\n\t"
      "ds_read_b64_tr_b16 v[14:15], v223 offset:35840\n\t"
      "v_mfma_f32_16x16x32_bf16 v[200:203], v[24:27], v[132:135], v[200:203]\n\t"
      "ds_read_b64_tr_b16 v[16:17], v218 offset:39424\n\t"
      "ds_read_b64_tr_b16 v[18:19], v222 offset:39424\n\t"
      "v_mfma_f32_16x16x32_bf16 v[204:207], v[28:31], v[132:135], v[204:207]\n\t"
      "ds_read_b64_tr_b16 v[20:21], v219 offset:39424\n\t"
      "s_waitcnt vmcnt(13)\n\t"
      "v_max_u32_e32 v232, v136, v140\n\t"
      "v_cmp_eq_u32_e32 vcc, -1, v232\n\t"
      "s_cbranch_vccnz LO2R_%=\n\t"
      "LO2B_%=:\n\t"
      "v_mfma_f32_16x16x32_bf16 v[192:195], v[32:35], v[136:139], v[192:195]\n\t"
      "ds_read_b64_tr_b16 v[22:23], v223 offset:39424\n\t"
      "v_mfma_f32_16x16x32_bf16 v[196:199], v[36:39], v[136:139], v[196:199]\n\t"
      "ds_read_b64_tr_b16 v[24:25], v216 offset:39424\n\t"
      "ds_read_b64_tr_b16 v[26:27], v220 offset:39424\n\t"
      "v_mfma_f32_16x16x32_bf16 v[200:203], v[40:43], v[136:139], v[200:203]\n\t"
      "ds_read_b64_tr_b16 v[28:29], v217 offset:39424\n\t"
      "v_mfma_f32_16x16x32_bf16 v[204:207], v[44:47], v[136:139], v[204:207]\n\t"
      "ds_read_b64_tr_b16 v[30:31], v221 offset:39424\n\t"
      "v_mfma_f32_16x16x32_bf16 v[192:195], v[48:51], v[140:143], v[192:195]\n\t"
      "ds_read_b64_tr_b16 v[32:33], v216 offset:43008\n\t"
      "ds_read_b64_tr_b16 v[34:35], v220 offset:43008\n\t"
      "v_mfma_f32_16x16x32_bf16 v[196:199], v[52:55], v[140:143], v[196:199]\n\t"
      "ds_read_b64_tr_b16 v[36:37], v217 offset:43008\n\t"
      "v_mfma_f32_16x16x32_bf16 v[200:203], v[56:59], v[140:143], v[200:203]\n\t"
      "ds_read_b64_tr_b16 v[38:39], v221 offset:43008\n\t"
      "v_mfma_f32_16x16x32_bf16 v[204:207], v[60:63], v[140:143], v[204:207]\n\t"
      "ds_read_b64_tr_b16 v[40:41], v218 offset:43008\n\t"
      "ds_read_b64_tr_b16 v[42:43], v222 offset:43008\n\t"
      "s_waitcnt vmcnt(11)\n\t"
      "v_max_u32_e32 v232, v144, v148\n\t"
      "v_cmp_eq_u32_e32 vcc, -1, v232\n\t"
      "s_cbranch_vccnz LO4R_%=\n\t"
      "LO4B_%=:\n\t"
      "v_mfma_f32_16x16x32_bf16 v[192:195], v[64:67], v[144:147], v[192:195]\n\t"
      "ds_read_b64_tr_b16 v[44:45], v219 offset:43008\n\t"
      "v_mfma_f32_16x16x32_bf16 v[196:199], v[68:71], v[144:147], v[196:199]\n\t"
      "ds_read_b64_tr_b16 v[46:47], v223 offset:43008\n\t"
      "v_mfma_f32_16x16x32_bf16 v[200:203], v[72:75], v[144:147], v[200:203]\n\t"
      "ds_read_b64_tr_b16 v[48:49], v218 offset:46592\n\t"
      "ds_read_b64_tr_b16 v[50:51], v222 offset:46592\n\t"
      "v_mfma_f32_16x16x32_bf16 v[204:207], v[76:79], v[144:147], v[204:207]\n\t"
      "ds_read_b64_tr_b16 v[52:53], v219 offset:46592\n\t"
      "v_mfma_f32_16x16x32_bf16 v[192:195], v[80:83], v[148:151], v[192:195]\n\t"
      "ds_read_b64_tr_b16 v[54:55], v223 offset:46592\n\t"
      "v_mfma_f32_16x16x32_bf16 v[196:199], v[84:87], v[148:151], v[196:199]\n\t"
      "ds_read_b64_tr_b16 v[56:57], v216 offset:46592\n\t"
      "ds_read_b64_tr_b16 v[58:59], v220 offset:46592\n\t"
      "v_mfma_f32_16x16x32_bf16 v[200:203], v[88:91], v[148:151], v[200:203]\n\t"
      "ds_read_b64_tr_b16 v[60:61], v217 offset:46592\n\t"
      "v_mfma_f32_16x16x32_bf16 v[204:207], v[92:95], v[148:151], v[204:207]\n\t"
      "ds_read_b64_tr_b16 v[62:63], v221 offset:46592\n\t"
      "s_waitcnt vmcnt(9)\n\t"
      "v_max_u32_e32 v232, v152, v156\n\t"
      "v_cmp_eq_u32_e32 vcc, -1, v232\n\t"
      "s_cbranch_vccnz LO6R_%=\n\t"
      "LO6B_%=:\n\t"
      "v_mfma_f32_16x16x32_bf16 v[192:195], v[96:99], v[152:155], v[192:195]\n\t"
      "ds_read_b64_tr_b16 v[64:65], v216 offset:50176\n\t"
      "ds_read_b64_tr_b16 v[66:67], v220 offset:50176\n\t"
      "v_mfma_f32_16x16x32_bf16 v[196:199], v[100:103], v[152:155], v[196:199]\n\t"
      "ds_read_b64_tr_b16 v[68:69], v217 offset:50176\n\t"
      "v_mfma_f32_16x16x32_bf16 v[200:203], v[104:107], v[152:155], v[200:203]\n\t"
      "ds_read_b64_tr_b16 v[70:71], v221 offset:50176\n\t"
      "v_mfma_f32_16x16x32_bf16 v[204:207], v[108:111], v[152:155], v[204:207]\n\t"
      "ds_read_b64_tr_b16 v[72:73], v218 offset:50176\n\t"
      "ds_read_b64_tr_b16 v[74:75], v222 offset:50176\n\t"
      "v_mfma_f32_16x16x32_bf16 v[192:195], v[112:115], v[156:159], v[192:195]\n\t"
      "ds_read_b64_tr_b16 v[76:77], v219 offset:50176\n\t"
      "v_mfma_f32_16x16x32_bf16 v[196:199], v[116:119], v[156:159], v[196:199]\n\t"
      "ds_read_b64_tr_b16 v[78:79], v223 offset:50176\n\t"
      "v_mfma_f32_16x16x32_bf16 v[200:203], v[120:123], v[156:159], v[200:203]\n\t"
      "ds_read_b64_tr_b16 v[80:81], v218 offset:53760\n\t"
      "ds_read_b64_tr_b16 v[82:83], v222 offset:53760\n\t"
      "v_mfma_f32_16x16x32_bf16 v[204:207], v[124:127], v[156:159], v[204:207]\n\t"
      "ds_read_b64_tr_b16 v[84:85], v219 offset:53760\n\t"
      "s_waitcnt vmcnt(7)\n\t"
      "v_max_u32_e32 v232, v160, v164\n\t"
      "v_cmp_eq_u32_e32 vcc, -1, v232\n\t"
      "s_cbranch_vccnz LO8R_%=\n\t"
      "LO8B_%=:\n\t"
      "s_waitcnt lgkmcnt(14)\n\t"
      "v_mfma_f32_16x16x32_bf16 v[192:195], v[0:3], v[160:163], v[192:195]\n\t"
      "ds_read_b64_tr_b16 v[86:87], v223 offset:53760\n\t"
      "v_mfma_f32_16x16x32_bf16 v[196:199], v[4:7], v[160:163], v[196:199]\n\t"
      "ds_read_b64_tr_b16 v[88:89], v216 offset:53760\n\t"
      "ds_read_b64_tr_b16 v[90:91], v220 offset:53760\n\t"
      "v_mfma_f32_16x16x32_bf16 v[200:203], v[8:11], v[160:163], v[200:203]\n\t"
      "ds_read_b64_tr_b16 v[92:93], v217 offset:53760\n\t"
      "v_mfma_f32_16x16x32_bf16 v[204:207], v[12:15], v[160:163], v[204:207]\n\t"
      "ds_read_b64_tr_b16 v[94:95], v221 offset:53760\n\t"
      "v_mfma_f32_16x16x32_bf16 v[192:195], v[16:19], v[164:167], v[192:195]\n\t"
      "ds_read_b64_tr_b16 v[96:97], v216\n\t"
      "ds_read_b64_tr_b16 v[98:99], v220\n\t"
      "v_mfma_f32_16x16x32_bf16 v[196:199], v[20:23], v[164:167], v[196:199]\n\t"
      "ds_read_b64_tr_b16 v[100:101], v217\n\t"
      "v_mfma_f32_16x16x32_bf16 v[200:203], v[24:27], v[164:167], v[200:203]\n\t"
      "ds_read_b64_tr_b16 v[102:103], v221\n\t"
      "v_mfma_f32_16x16x32_bf16 v[204:207], v[28:31], v[164:167], v[204:207]\n\t"
      "ds_read_b64_tr_b16 v[104:105], v218\n\t"
      "ds_read_b64_tr_b16 v[106:107], v222\n\t"
      "s_waitcnt vmcnt(5)\n\t"
      "v_max_u32_e32 v232, v168, v172\n\t"
      "v_cmp_eq_u32_e32 vcc, -1, v232\n\t"
      "s_cbranch_vccnz LO10R_%=\n\t"
      "LO10B_%=:\n\t"
      "v_mfma_f32_16x16x32_bf16 v[192:195], v[32:35], v[168:171], v[192:195]\n\t"
      "ds_read_b64_tr_b16 v[108:109], v219\n\t"
      "v_mfma_f32_16x16x32_bf16 v[196:199], v[36:39], v[168:171], v[196:199]\n\t"
      "ds_read_b64_tr_b16 v[110:111], v223\n\t"
      "v_mfma_f32_16x16x32_bf16 v[200:203], v[40:43], v[168:171], v[200:203]\n\t"
      "ds_read_b64_tr_b16 v[112:113], v218 offset:3584\n\t"
      "ds_read_b64_tr_b16 v[114:115], v222 offset:3584\n\t"
      "v_mfma_f32_16x16x32_bf16 v[204:207], v[44:47], v[168:171], v[204:207]\n\t"
      "ds_read_b64_tr_b16 v[116:117], v219 offset:3584\n\t"
      "s_waitcnt lgkmcnt(14)\n\t"
      "v_mfma_f32_16x16x32_bf16 v[192:195], v[48:51], v[172:175], v[192:195]\n\t"
      "ds_read_b64_tr_b16 v[118:119], v223 offset:3584\n\t"
      "v_mfma_f32_16x16x32_bf16 v[196:199], v[52:55], v[172:175], v[196:199]\n\t"
      "ds_read_b64_tr_b16 v[120:121], v216 offset:3584\n\t"
      "ds_read_b64_tr_b16 v[122:123], v220 offset:3584\n\t"
      "v_mfma_f32_16x16x32_bf16 v[200:203], v[56:59], v[172:175], v[200:203]\n\t"
      "ds_read_b64_tr_b16 v[124:125], v217 offset:3584\n\t"
      "v_mfma_f32_16x16x32_bf16 v[204:207], v[60:63], v[172:175], v[204:207]\n\t"
      "ds_read_b64_tr_b16 v[126:127], v221 offset:3584\n\t"
      "s_waitcnt vmcnt(3)\n\t"
      "v_max_u32_e32 v232, v176, v180\n\t"
      "v_cmp_eq_u32_e32 vcc, -1, v232\n\t"
      "s_cbranch_vccnz LO12R_%=\n\t"
      "LO12B_%=:\n\t"
      "v_mfma_f32_16x16x32_bf16 v[192:195], v[64:67], v[176:179], v[192:195]\n\t"
      "v_mfma_f32_16x16x32_bf16 v[196:199], v[68:71], v[176:179], v[196:199]\n\t"
      "v_mfma_f32_16x16x32_bf16 v[200:203], v[72:75], v[176:179], v[200:203]\n\t"
      "v_mfma_f32_16x16x32_bf16 v[204:207], v[76:79], v[176:179], v[204:207]\n\t"
      "s_waitcnt lgkmcnt(14)\n\t"
      "v_mfma_f32_16x16x32_bf16 v[192:195], v[80:83], v[180:183], v[192:195]\n\t"
      "v_mfma_f32_16x16x32_bf16 v[196:199], v[84:87], v[180:183], v[196:199]\n\t"
      "v_mfma_f32_16x16x32_bf16 v[200:203], v[88:91], v[180:183], v[200:203]\n\t"
      "v_mfma_f32_16x16x32_bf16 v[204:207], v[92:95], v[180:183], v[204:207]\n\t"
      "s_waitcnt vmcnt(1)\n\t"
      "v_max_u32_e32 v232, v184, v188\n\t"
      "v_cmp_eq_u32_e32 vcc, -1, v232\n\t"
      "s_cbranch_vccnz LO14R_%=\n\t"
      "LO14B_%=:\n\t"
      "s_waitcnt lgkmcnt(8)\n\t"
      "v_mfma_f32_16x16x32_bf16 v[192:195], v[96:99], v[184:187], v[192:195]\n\t"
      "v_mfma_f32_16x16x32_bf16 v[196:199], v[100:103], v[184:187], v[196:199]\n\t"
      "v_mfma_f32_16x16x32_bf16 v[200:203], v[104:107], v[184:187], v[200:203]\n\t"
      "v_mfma_f32_16x16x32_bf16 v[204:207], v[108:111], v[184:187], v[204:207]\n\t"
      "s_waitcnt lgkmcnt(0)\n\t"
      "v_mfma_f32_16x16x32_bf16 v[192:195], v[112:115], v[188:191], v[192:195]\n\t"
      "v_mfma_f32_16x16x32_bf16 v[196:199], v[116:119], v[188:191], v[196:199]\n\t"
      "v_mfma_f32_16x16x32_bf16 v[200:203], v[120:123], v[188:191], v[200:203]\n\t"
      "v_mfma_f32_16x16x32_bf16 v[204:207], v[124:127], v[188:191], v[204:207]\n\t"
      "s_nop 4\n\t"
      "v_cmp_u_f32_e32 vcc, v192, v192\n\t"
      "s_cbranch_vccnz LGX_%=\n\t"
      "LGB_%=:\n\t"
      "v_cvt_pk_bf16_f32 v208, v192, v193\n\t"
      "v_cvt_pk_bf16_f32 v209, v194, v195\n\t"
      "v_cvt_pk_bf16_f32 v210, v196, v197\n\t"
      "v_cvt_pk_bf16_f32 v211, v198, v199\n\t"
      "v_cvt_pk_bf16_f32 v212, v200, v201\n\t"
      "v_cvt_pk_bf16_f32 v213, v202, v203\n\t"
      "v_cvt_pk_bf16_f32 v214, v204, v205\n\t"
      "v_cvt_pk_bf16_f32 v215, v206, v207\n\t"
      "v_mov_b32_e32 v148, 0x3f803f80\n\t"
      "v_dot2_f32_bf16 v144, v208, v148, 0\n\t"
      "v_dot2_f32_bf16 v145, v208, v208, 0\n\t"
      "v_dot2_f32_bf16 v146, v209, v148, 0\n\t"
      "v_dot2_f32_bf16 v147, v209, v209, 0\n\t"
      "v_dot2_f32_bf16 v144, v210, v148, v144\n\t"
      "v_dot2_f32_bf16 v145, v210, v210, v145\n\t"
      "v_dot2_f32_bf16 v146, v211, v148, v146\n\t"
      "v_dot2_f32_bf16 v147, v211, v211, v147\n\t"
      "v_dot2_f32_bf16 v144, v212, v148, v144\n\t"
      "v_dot2_f32_bf16 v145, v212, v212, v145\n\t"
      "v_dot2_f32_bf16 v146, v213, v148, v146\n\t"
      "v_dot2_f32_bf16 v147, v213, v213, v147\n\t"
      "v_dot2_f32_bf16 v144, v214, v148, v144\n\t"
      "v_dot2_f32_bf16 v145, v214, v214, v145\n\t"
      "v_dot2_f32_bf16 v146, v215, v148, v146\n\t"
      "v_dot2_f32_bf16 v147, v215, v215, v147\n\t"
      "v_lshlrev_b32_e32 v128, 16, v208\n\t"
      "v_and_b32_e32 v129, 0xffff0000, v208\n\t"
      "v_lshlrev_b32_e32 v130, 16, v209\n\t"
      "v_pk_add_f32 v[144:145], v[144:145], v[146:147]\n\t"
      "v_and_b32_e32 v131, 0xffff0000, v209\n\t"
      "v_lshlrev_b32_e32 v132, 16, v210\n\t"
      "v_permlane16_swap_b32_e32 v144, v145\n\t"
      "v_add_f32_e32 v144, v144, v145\n\t"
      "v_mov_b32_e32 v145, v144\n\t"
      "v_and_b32_e32 v133, 0xffff0000, v210\n\t"
      "v_lshlrev_b32_e32 v134, 16, v211\n\t"
      "v_permlane32_swap_b32_e32 v144, v145\n\t"
      "v_add_f32_e32 v144, v144, v145\n\t"
      "v_mov_b32_e32 v145, v144\n\t"
      "v_and_b32_e32 v135, 0xffff0000, v211\n\t"
      "v_lshlrev_b32_e32 v136, 16, v212\n\t"
      "v_permlane16_swap_b32_e32 v144, v145\n\t"
      "s_mov_b64 exec, s[70:71]\n\t"
      "v_min_u32_e32 v144, -2, v144\n\t"
      "v_min_u32_e32 v145, -2, v145\n\t"
      "global_store_dwordx2 v231, v[144:145], s[58:59] sc1\n\t"
      "s_mov_b64 exec, -1\n\t"
      "s_barrier\n\t"
      "v_and_b32_e32 v137, 0xffff0000, v212\n\t"
      "v_lshlrev_b32_e32 v138, 16, v213\n\t"
      "v_and_b32_e32 v139, 0xffff0000, v213\n\t"
      "v_lshlrev_b32_e32 v140, 16, v214\n\t"
      "v_and_b32_e32 v141, 0xffff0000, v214\n\t"
      "v_lshlrev_b32_e32 v142, 16, v215\n\t"
      "v_and_b32_e32 v143, 0xffff0000, v215\n\t"
      "v_permlane16_swap_b32_e32 v208, v210\n\t"
      "v_permlane16_swap_b32_e32 v209, v211\n\t"
      "v_permlane16_swap_b32_e32 v212, v214\n\t"
      "v_permlane16_swap_b32_e32 v213, v215\n\t"
      "s_bitcmp0_b32 s27, 0\n\t"
      "s_cbranch_scc1 NTK0_%=\n\t"
      "ds_read_b64_tr_b16 v[0:1], v224 offset:16384\n\t"
      "ds_read_b64_tr_b16 v[2:3], v224 offset:16896\n\t"
      "ds_read_b64_tr_b16 v[4:5], v225 offset:16384\n\t"
      "ds_read_b64_tr_b16 v[6:7], v225 offset:16896\n\t"
      "ds_read_b64_tr_b16 v[8:9], v226 offset:16384\n\t"
      "ds_read_b64_tr_b16 v[10:11], v226 offset:16896\n\t"
      "ds_read_b64_tr_b16 v[12:13], v227 offset:16384\n\t"
      "ds_read_b64_tr_b16 v[14:15], v227 offset:16896\n\t"
      "ds_read_b64_tr_b16 v[16:17], v224 offset:49152\n\t"
      "ds_read_b64_tr_b16 v[18:19], v224 offset:49664\n\t"
      "ds_read_b64_tr_b16 v[20:21], v225 offset:49152\n\t"
      "ds_read_b64_tr_b16 v[22:23], v225 offset:49664\n\t"
      "ds_read_b64_tr_b16 v[24:25], v226 offset:49152\n\t"
      "ds_read_b64_tr_b16 v[26:27], v226 offset:49664\n\t"
      "ds_read_b64_tr_b16 v[28:29], v227 offset:49152\n\t"
      "ds_read_b64_tr_b16 v[30:31], v227 offset:49664\n\t"
      "ds_read_b64_tr_b16 v[32:33], v224\n\t"
      "ds_read_b64_tr_b16 v[34:35], v224 offset:512\n\t"
      "ds_read_b64_tr_b16 v[36:37], v225\n\t"
      "ds_read_b64_tr_b16 v[38:39], v225 offset:512\n\t"
      "ds_read_b64_tr_b16 v[40:41], v226\n\t"
      "ds_read_b64_tr_b16 v[42:43], v226 offset:512\n\t"
      "ds_read_b64_tr_b16 v[44:45], v227\n\t"
      "ds_read_b64_tr_b16 v[46:47], v227 offset:512\n\t"
      "ds_read_b64_tr_b16 v[48:49], v224 offset:32768\n\t"
      "ds_read_b64_tr_b16 v[50:51], v224 offset:33280\n\t"
      "ds_read_b64_tr_b16 v[52:53], v225 offset:32768\n\t"
      "ds_read_b64_tr_b16 v[54:55], v225 offset:33280\n\t"
      "ds_read_b64_tr_b16 v[56:57], v226 offset:32768\n\t"
      "ds_read_b64_tr_b16 v[58:59], v226 offset:33280\n\t"
      "ds_read_b64_tr_b16 v[60:61], v227 offset:32768\n\t"
      "ds_read_b64_tr_b16 v[62:63], v227 offset:33280\n\t"
      "NTK0_%=:\n\t"
      "s_waitcnt lgkmcnt(0)\n\t"
      "s_barrier\n\t"
      "global_load_dwordx2 v[148:149], v231, s[46:47] sc1\n\t"
      "global_load_dwordx2 v[150:151], v231, s[46:47] offset:384 sc1\n\t"
      "s_waitcnt vmcnt(0)\n\t"
      "v_max3_u32 v232, v148, v149, v150\n\t"
      "v_max_u32_e32 v232, v232, v151\n\t"
      "v_cmp_eq_u32_e32 vcc, -1, v232\n\t"
      "s_cbranch_vccnz LSR_%=\n\t"
      "LSB_%=:\n\t"
      "v_cndmask_b32_e64 v150, v150, 0, s[70:71]\n\t"
      "v_cndmask_b32_e64 v151, v151, 0, s[70:71]\n\t"
      "v_pk_add_f32 v[144:145], v[148:149], v[150:151]\n\t"
      "s_nop 1\n\t"
      "v_permlane16_swap_b32_e32 v144, v145\n\t"
      "v_add_f32_e32 v144, v144, v145\n\t"
      "v_mov_b32_e32 v145, v144\n\t"
      "s_nop 1\n\t"
      "v_permlane32_swap_b32_e32 v144, v145\n\t"
      "v_add_f32_e32 v144, v144, v145\n\t"
      "v_mov_b32_e32 v145, v144\n\t"
      "s_nop 1\n\t"
      "v_permlane16_swap_b32_e32 v144, v145\n\t"
      "v_mul_f32_e32 v144, 0xbb124925, v144\n\t"
      "v_mul_f32_e32 v145, 0x3b124925, v145\n\t"
      "v_fma_f32 v145, -v144, v144, v145\n\t"
      "v_max_f32_e32 v145, 0, v145\n\t"
      "v_add_f32_e32 v146, 0x3727c5ac, v145\n\t"
      "v_fmac_f32_e32 v145, 0x3727c5ac, v146\n\t"
      "v_rsq_f32_e32 v145, v145\n\t"
      "s_nop 0\n\t"
      "v_mul_f32_e32 v144, v144, v145\n\t"
      "v_pk_fma_f32 v[128:129], v[128:129], v[144:145], v[144:145] op_sel:[0,1,0] op_sel_hi:[1,1,0]\n\t"
      "v_pk_fma_f32 v[130:131], v[130:131], v[144:145], v[144:145] op_sel:[0,1,0] op_sel_hi:[1,1,0]\n\t"
      "v_cvt_pk_bf16_f32 v152, v128, v129\n\t"
      "v_cvt_pk_bf16_f32 v153, v130, v131\n\t"
      "ds_write_b64 v233, v[152:153]\n\t"
      "v_pk_fma_f32 v[132:133], v[132:133], v[144:145], v[144:145] op_sel:[0,1,0] op_sel_hi:[1,1,0]\n\t"
      "v_pk_fma_f32 v[134:135], v[134:135], v[144:145], v[144:145] op_sel:[0,1,0] op_sel_hi:[1,1,0]\n\t"
      "v_cvt_pk_bf16_f32 v154, v132, v133\n\t"
      "v_cvt_pk_bf16_f32 v155, v134, v135\n\t"
      "ds_write_b64 v234, v[154:155]\n\t"
      "ds_read_b64_tr_b16 v[160:161], v237\n\t"
      "ds_read_b64_tr_b16 v[162:163], v238 offset:512\n\t"
      "v_pk_fma_f32 v[136:137], v[136:137], v[144:145], v[144:145] op_sel:[0,1,0] op_sel_hi:[1,1,0]\n\t"
      "v_pk_fma_f32 v[138:139], v[138:139], v[144:145], v[144:145] op_sel:[0,1,0] op_sel_hi:[1,1,0]\n\t"
      "v_cvt_pk_bf16_f32 v156, v136, v137\n\t"
      "v_cvt_pk_bf16_f32 v157, v138, v139\n\t"
      "ds_write_b64 v235, v[156:157]\n\t"
      "v_pk_fma_f32 v[140:141], v[140:141], v[144:145], v[144:145] op_sel:[0,1,0] op_sel_hi:[1,1,0]\n\t"
      "v_pk_fma_f32 v[142:143], v[142:143], v[144:145], v[144:145] op_sel:[0,1,0] op_sel_hi:[1,1,0]\n\t"
      "v_cvt_pk_bf16_f32 v158, v140, v141\n\t"
      "v_cvt_pk_bf16_f32 v159, v142, v143\n\t"
      "ds_write_b64 v236, v[158:159]\n\t"
      "ds_read_b64_tr_b16 v[164:165], v239\n\t"
      "ds_read_b64_tr_b16 v[166:167], v240 offset:512\n\t"
      "s_waitcnt lgkmcnt(4)\n\t"
      "v_min_u32_e32 v160, -2, v160\n\t"
      "v_min_u32_e32 v161, -2, v161\n\t"
      "v_min_u32_e32 v162, -2, v162\n\t"
      "v_min_u32_e32 v163, -2, v163\n\t"
      "global_store_dwordx4 v229, v[160:163], s[48:49] sc1\n\t"
      "s_waitcnt lgkmcnt(0)\n\t"
      "v_min_u32_e32 v164, -2, v164\n\t"
      "v_min_u32_e32 v165, -2, v165\n\t"
      "v_min_u32_e32 v166, -2, v166\n\t"
      "v_min_u32_e32 v167, -2, v167\n\t"
      "global_store_dwordx4 v230, v[164:167], s[48:49] sc1\n\t"
      "s_barrier\n\t"
      "s_bitcmp0_b32 s27, 0\n\t"
      "s_cbranch_scc1 G4C_%=\n\t"
      "s_mul_i32 s34, s32, 196608\n\t"
      "s_mul_i32 s35, s24, 8192\n\t"
      "s_add_u32 s34, s34, s35\n\t"
      "s_add_u32 s34, s34, 1048576\n\t"
      "s_add_u32 s64, s48, s34\n\t"
      "s_addc_u32 s65, s49, 0\n\t"
      "v_add_u32_e32 v250, 4096, v244\n\t"
      "global_load_dwordx4 v[128:131], v250, s[64:65] offset:-4096 sc1\n\t"
      "global_load_dwordx4 v[132:135], v250, s[64:65] offset:-3072 sc1\n\t"
      "global_load_dwordx4 v[136:139], v250, s[64:65] offset:-2048 sc1\n\t"
      "global_load_dwordx4 v[140:143], v250, s[64:65] offset:-1024 sc1\n\t"
      "global_load_dwordx4 v[144:147], v250, s[64:65] sc1\n\t"
      "global_load_dwordx4 v[148:151], v250, s[64:65] offset:1024 sc1\n\t"
      "global_load_dwordx4 v[152:155], v250, s[64:65] offset:2048 sc1\n\t"
      "global_load_dwordx4 v[156:159], v250, s[64:65] offset:3072 sc1\n\t"
      "global_store_dwordx4 v241, v[208:211], s[20:21] offset:-2048 sc1\n\t"
      "global_store_dwordx4 v241, v[212:215], s[20:21] offset:2048 sc1\n\t"
      "s_setprio 0\n\t"
      "ds_read_b64_tr_b16 v[64:65], v224 offset:16384\n\t"
      "ds_read_b64_tr_b16 v[66:67], v224 offset:16896\n\t"
      "ds_read_b64_tr_b16 v[68:69], v225 offset:16384\n\t"
      "ds_read_b64_tr_b16 v[70:71], v225 offset:16896\n\t"
      "ds_read_b64_tr_b16 v[72:73], v226 offset:16384\n\t"
      "ds_read_b64_tr_b16 v[74:75], v226 offset:16896\n\t"
      "ds_read_b64_tr_b16 v[76:77], v227 offset:16384\n\t"
      "ds_read_b64_tr_b16 v[78:79], v227 offset:16896\n\t"
      "ds_read_b64_tr_b16 v[80:81], v224 offset:49152\n\t"
      "ds_read_b64_tr_b16 v[82:83], v224 offset:49664\n\t"
      "ds_read_b64_tr_b16 v[84:85], v225 offset:49152\n\t"
      "ds_read_b64_tr_b16 v[86:87], v225 offset:49664\n\t"
      "ds_read_b64_tr_b16 v[88:89], v226 offset:49152\n\t"
      "ds_read_b64_tr_b16 v[90:91], v226 offset:49664\n\t"
      "ds_read_b64_tr_b16 v[92:93], v227 offset:49152\n\t"
      "ds_read_b64_tr_b16 v[94:95], v227 offset:49664\n\t"
      "ds_read_b64_tr_b16 v[96:97], v224\n\t"
      "ds_read_b64_tr_b16 v[98:99], v224 offset:512\n\t"
      "ds_read_b64_tr_b16 v[100:101], v225\n\t"
      "ds_read_b64_tr_b16 v[102:103], v225 offset:512\n\t"
      "ds_read_b64_tr_b16 v[104:105], v226\n\t"
      "ds_read_b64_tr_b16 v[106:107], v226 offset:512\n\t"
      "ds_read_b64_tr_b16 v[108:109], v227\n\t"
      "ds_read_b64_tr_b16 v[110:111], v227 offset:512\n\t"
      "ds_read_b64_tr_b16 v[112:113], v224 offset:32768\n\t"
      "ds_read_b64_tr_b16 v[114:115], v224 offset:33280\n\t"
      "ds_read_b64_tr_b16 v[116:117], v225 offset:32768\n\t"
      "ds_read_b64_tr_b16 v[118:119], v225 offset:33280\n\t"
      "ds_read_b64_tr_b16 v[120:121], v226 offset:32768\n\t"
      "ds_read_b64_tr_b16 v[122:123], v226 offset:33280\n\t"
      "ds_read_b64_tr_b16 v[124:125], v227 offset:32768\n\t"
      "ds_read_b64_tr_b16 v[126:127], v227 offset:33280\n\t"
      "s_waitcnt vmcnt(2) lgkmcnt(0)\n\t"
      "GST_%=:\n\t"
      "s_add_u32 s66, s64, 65536\n\t"
      "s_addc_u32 s67, s65, 0\n\t"
      "s_add_u32 s68, s66, 65536\n\t"
      "s_addc_u32 s69, s67, 0\n\t"
      "s_lshl_b32 s34, s32, 18\n\t"
      "s_lshl_b32 s35, s24, 13\n\t"
      "s_add_u32 s34, s34, s35\n\t"
      "s_add_u32 s70, s48, s34\n\t"
      "s_addc_u32 s71, s49, 0\n\t"
      "s_add_u32 s72, s70, 65536\n\t"
      "s_addc_u32 s73, s71, 0\n\t"
      "s_add_u32 s74, s72, 65536\n\t"
      "s_addc_u32 s75, s73, 0\n\t"
      "s_add_u32 s76, s74, 65536\n\t"
      "s_addc_u32 s77, s75, 0\n\t"
      "s_mul_i32 s34, s30, 28\n\t"
      "s_add_u32 s34, s34, s29\n\t"
      "s_lshl_b32 s34, s34, 11\n\t"
      "s_add_u32 s34, s34, 10223616\n\t"
      "s_add_u32 s62, s20, s34\n\t"
      "s_addc_u32 s63, s21, 0\n\t"
      "v_lshlrev_b32_e32 v248, 16, v249\n\t"
      "v_and_b32_e32 v249, 0xffff0000, v249\n\t"
      "v_mov_b32_e32 v252, 0xbdd2d3e8\n\t"
      "v_xor_b32_e32 v253, 128, v245\n\t"
      "v_max3_u32 v251, v128, v129, v130\n\t"
      "v_max3_u32 v251, v251, v131, v132\n\t"
      "v_max3_u32 v251, v251, v133, v134\n\t"
      "v_max3_u32 v251, v251, v135, v136\n\t"
      "v_max3_u32 v251, v251, v137, v138\n\t"
      "v_max3_u32 v251, v251, v139, v140\n\t"
      "v_max3_u32 v251, v251, v141, v142\n\t"
      "v_max3_u32 v251, v251, v143, v144\n\t"
      "v_max3_u32 v251, v251, v145, v146\n\t"
      "v_max3_u32 v251, v251, v147, v148\n\t"
      "v_max3_u32 v251, v251, v149, v150\n\t"
      "v_max3_u32 v251, v251, v151, v152\n\t"
      "v_max3_u32 v251, v251, v153, v154\n\t"
      "v_max3_u32 v251, v251, v155, v156\n\t"
      "v_max3_u32 v251, v251, v157, v158\n\t"
      "v_max_u32_e32 v251, v251, v159\n\t"
      "v_cmp_eq_u32_e32 vcc, -1, v251\n\t"
      "s_cbranch_vccnz LA30R_%=\n\t"
      "LA30B_%=:\n\t"
      "v_mfma_f32_16x16x32_bf16 v[208:211], v[0:3], v[128:131], 0\n\t"
      "global_load_dwordx4 v[160:163], v250, s[66:67] offset:-4096 sc1\n\t"
      "v_mfma_f32_16x16x32_bf16 v[208:211], v[16:19], v[132:135], v[208:211]\n\t"
      "v_mfma_f32_16x16x32_bf16 v[208:211], v[32:35], v[136:139], v[208:211]\n\t"
      "global_load_dwordx4 v[164:167], v250, s[66:67] offset:-3072 sc1\n\t"
      "v_mfma_f32_16x16x32_bf16 v[208:211], v[48:51], v[140:143], v[208:211]\n\t"
      "v_mfma_f32_16x16x32_bf16 v[208:211], v[64:67], v[144:147], v[208:211]\n\t"
      "global_load_dwordx4 v[168:171], v250, s[66:67] offset:-2048 sc1\n\t"
      "v_mfma_f32_16x16x32_bf16 v[208:211], v[80:83], v[148:151], v[208:211]\n\t"
      "v_mfma_f32_16x16x32_bf16 v[208:211], v[96:99], v[152:155], v[208:211]\n\t"
      "s_lshl_b32 s34, s2, 7\n\t"
      "s_add_u32 s34, s34, 65536\n\t"
      "v_mov_b32_e32 v230, s34\n\t"
      "s_cmp_lg_u32 s24, 3\n\t"
      "s_cbranch_scc1 SK17_%=\n\t"
      "global_load_dwordx2 v[228:229], v230, s[60:61] sc1\n\t"
      "SK17_%=:\n\t"
      "global_load_dwordx4 v[172:175], v250, s[66:67] offset:-1024 sc1\n\t"
      "v_mfma_f32_16x16x32_bf16 v[208:211], v[112:115], v[156:159], v[208:211]\n\t"
      "v_mfma_f32_16x16x32_bf16 v[212:215], v[4:7], v[128:131], 0\n\t"
      "global_load_dwordx4 v[176:179], v250, s[66:67] sc1\n\t"
      "v_mfma_f32_16x16x32_bf16 v[212:215], v[20:23], v[132:135], v[212:215]\n\t"
      "v_mfma_f32_16x16x32_bf16 v[212:215], v[36:39], v[136:139], v[212:215]\n\t"
      "global_load_dwordx4 v[180:183], v250, s[66:67] offset:1024 sc1\n\t"
      "v_mfma_f32_16x16x32_bf16 v[212:215], v[52:55], v[140:143], v[212:215]\n\t"
      "v_mfma_f32_16x16x32_bf16 v[212:215], v[68:71], v[144:147], v[212:215]\n\t"
      "global_load_dwordx4 v[184:187], v250, s[66:67] offset:2048 sc1\n\t"
      "v_mfma_f32_16x16x32_bf16 v[212:215], v[84:87], v[148:151], v[212:215]\n\t"
      "v_mfma_f32_16x16x32_bf16 v[212:215], v[100:103], v[152:155], v[212:215]\n\t"
      "ds_write_b128 v245, v[208:211]\n\t"
      "global_load_dwordx4 v[188:191], v250, s[66:67] offset:3072 sc1\n\t"
      "v_mfma_f32_16x16x32_bf16 v[212:215], v[116:119], v[156:159], v[212:215]\n\t"
      "v_mfma_f32_16x16x32_bf16 v[216:219], v[8:11], v[128:131], 0\n\t"
      "global_load_dwordx4 v[192:195], v250, s[68:69] offset:-4096 sc1\n\t"
      "v_mfma_f32_16x16x32_bf16 v[216:219], v[24:27], v[132:135], v[216:219]\n\t"
      "v_mfma_f32_16x16x32_bf16 v[216:219], v[40:43], v[136:139], v[216:219]\n\t"
      "global_load_dwordx4 v[196:199], v250, s[68:69] offset:-3072 sc1\n\t"
      "v_mfma_f32_16x16x32_bf16 v[216:219], v[56:59], v[140:143], v[216:219]\n\t"
      "v_mfma_f32_16x16x32_bf16 v[216:219], v[72:75], v[144:147], v[216:219]\n\t"
      "global_load_dwordx4 v[200:203], v250, s[68:69] offset:-2048 sc1\n\t"
      "v_mfma_f32_16x16x32_bf16 v[216:219], v[88:91], v[148:151], v[216:219]\n\t"
      "v_mfma_f32_16x16x32_bf16 v[216:219], v[104:107], v[152:155], v[216:219]\n\t"
      "ds_write_b128 v253, v[212:215] offset:1024\n\t"
      "global_load_dwordx4 v[204:207], v250, s[68:69] offset:-1024 sc1\n\t"
      "v_mfma_f32_16x16x32_bf16 v[216:219], v[120:123], v[156:159], v[216:219]\n\t"
      "v_mfma_f32_16x16x32_bf16 v[220:223], v[12:15], v[128:131], 0\n\t"
      "global_load_dwordx4 v[128:131], v250, s[68:69] sc1\n\t"
      "v_mfma_f32_16x16x32_bf16 v[220:223], v[28:31], v[132:135], v[220:223]\n\t"
      "v_mfma_f32_16x16x32_bf16 v[220:223], v[44:47], v[136:139], v[220:223]\n\t"
      "v_mfma_f32_16x16x32_bf16 v[220:223], v[60:63], v[140:143], v[220:223]\n\t"
      "v_mfma_f32_16x16x32_bf16 v[220:223], v[76:79], v[144:147], v[220:223]\n\t"
      "global_load_dwordx4 v[132:135], v250, s[68:69] offset:1024 sc1\n\t"
      "v_mfma_f32_16x16x32_bf16 v[220:223], v[92:95], v[148:151], v[220:223]\n\t"
      "v_mfma_f32_16x16x32_bf16 v[220:223], v[108:111], v[152:155], v[220:223]\n\t"
      "ds_write_b128 v245, v[216:219] offset:2048\n\t"
      "v_mfma_f32_16x16x32_bf16 v[220:223], v[124:127], v[156:159], v[220:223]\n\t"
      "s_waitcnt vmcnt(10)\n\t"
      "v_mfma_f32_16x16x32_bf16 v[224:227], v[0:3], v[160:163], 0\n\t"
      "global_load_dwordx4 v[136:139], v250, s[68:69] offset:2048 sc1\n\t"
      "v_mfma_f32_16x16x32_bf16 v[224:227], v[16:19], v[164:167], v[224:227]\n\t"
      "s_nop 3\n\t"
      "ds_write_b128 v253, v[220:223] offset:3072\n\t"
      "v_mfma_f32_16x16x32_bf16 v[224:227], v[32:35], v[168:171], v[224:227]\n\t"
      "v_mfma_f32_16x16x32_bf16 v[224:227], v[48:51], v[172:175], v[224:227]\n\t"
      "s_waitcnt vmcnt(7)\n\t"
      "v_mfma_f32_16x16x32_bf16 v[224:227], v[64:67], v[176:179], v[224:227]\n\t"
      "global_load_dwordx4 v[140:143], v250, s[68:69] offset:3072 sc1\n\t"
      "v_mfma_f32_16x16x32_bf16 v[224:227], v[80:83], v[180:183], v[224:227]\n\t"
      "s_cmp_lg_u32 s24, 3\n\t"
      "s_cbranch_scc1 LF6B_%=\n\t"
      "v_cmp_ne_u64_e32 vcc, s[22:23], v[228:229]\n\t"
      "s_cbranch_vccnz LF6R_%=\n\t"
      "LF6B_%=:\n\t"
      "s_waitcnt lgkmcnt(0)\n\t"
      "s_barrier\n\t"
      "s_cmp_lg_u32 s24, 3\n\t"
      "s_cbranch_scc1 HF3_%=\n\t"
      "s_lshl3_add_u32 s34, s25, s26\n\t"
      "s_lshl_b32 s34, s34, 7\n\t"
      "s_add_u32 s34, s34, 98304\n\t"
      "v_mov_b32_e32 v238, s34\n\t"
      "v_mov_b32_e32 v236, s22\n\t"
      "v_mov_b32_e32 v237, s23\n\t"
      "s_mov_b64 exec, 1\n\t"
      "global_store_dwordx2 v238, v[236:237], s[60:61] sc1\n\t"
      "s_mov_b64 exec, -1\n\t"
      "HF3_%=:\n\t"
      "ds_read_b64 v[228:229], v246\n\t"
      "ds_read_b64 v[230:231], v246 offset:4096\n\t"
      "v_mfma_f32_16x16x32_bf16 v[224:227], v[96:99], v[184:187], v[224:227]\n\t"
      "ds_read_b64 v[232:233], v246 offset:8192\n\t"
      "ds_read_b64 v[234:235], v246 offset:12288\n\t"
      "v_mfma_f32_16x16x32_bf16 v[224:227], v[112:115], v[188:191], v[224:227]\n\t"
      "ds_read_b64 v[236:237], v246 offset:16384\n\t"
      "ds_read_b64 v[238:239], v246 offset:20480\n\t"
      "v_mfma_f32_16x16x32_bf16 v[208:211], v[4:7], v[160:163], 0\n\t"
      "ds_read_b64 v[240:241], v246 offset:24576\n\t"
      "ds_read_b64 v[242:243], v246 offset:28672\n\t"
      "global_load_dwordx4 v[144:147], v250, s[70:71] offset:-4096 sc1\n\t"
      "v_mfma_f32_16x16x32_bf16 v[208:211], v[20:23], v[164:167], v[208:211]\n\t"
      "v_mfma_f32_16x16x32_bf16 v[208:211], v[36:39], v[168:171], v[208:211]\n\t"
      "s_waitcnt lgkmcnt(4)\n\t"
      "v_add_f32_e32 v228, v228, v230\n\t"
      "v_add_f32_e32 v232, v232, v234\n\t"
      "v_cmp_u_f32_e32 vcc, v224, v224\n\t"
      "s_cbranch_vccnz LA31R_%=\n\t"
      "LA31B_%=:\n\t"
      "v_mfma_f32_16x16x32_bf16 v[208:211], v[52:55], v[172:175], v[208:211]\n\t"
      "v_add_f32_e32 v229, v229, v231\n\t"
      "v_add_f32_e32 v233, v233, v235\n\t"
      "v_mfma_f32_16x16x32_bf16 v[208:211], v[68:71], v[176:179], v[208:211]\n\t"
      "s_waitcnt lgkmcnt(0)\n\t"
      "v_add_f32_e32 v236, v236, v238\n\t"
      "v_add_f32_e32 v240, v240, v242\n\t"
      "global_load_dwordx4 v[148:151], v250, s[70:71] offset:-3072 sc1\n\t"
      "v_mfma_f32_16x16x32_bf16 v[208:211], v[84:87], v[180:183], v[208:211]\n\t"
      "v_add_f32_e32 v237, v237, v239\n\t"
      "v_add_f32_e32 v241, v241, v243\n\t"
      "v_mfma_f32_16x16x32_bf16 v[208:211], v[100:103], v[184:187], v[208:211]\n\t"
      "v_add_f32_e32 v228, v228, v232\n\t"
      "v_add_f32_e32 v236, v236, v240\n\t"
      "v_add_f32_e32 v229, v229, v233\n\t"
      "ds_write_b128 v245, v[224:227] offset:32768\n\t"
      "v_mfma_f32_16x16x32_bf16 v[208:211], v[116:119], v[188:191], v[208:211]\n\t"
      "v_add_f32_e32 v237, v237, v241\n\t"
      "v_add_f32_e32 v228, v228, v248\n\t"
      "v_mfma_f32_16x16x32_bf16 v[212:215], v[8:11], v[160:163], 0\n\t"
      "v_add_f32_e32 v229, v229, v249\n\t"
      "v_add_f32_e32 v228, v228, v236\n\t"
      "v_add_f32_e32 v229, v229, v237\n\t"
      "global_load_dwordx4 v[152:155], v250, s[70:71] offset:-2048 sc1\n\t"
      "v_mfma_f32_16x16x32_bf16 v[212:215], v[24:27], v[164:167], v[212:215]\n\t"
      "v_mul_f32_e32 v230, v228, v228\n\t"
      "v_mul_f32_e32 v231, v229, v229\n\t"
      "v_mfma_f32_16x16x32_bf16 v[212:215], v[40:43], v[168:171], v[212:215]\n\t"
      "v_fmaak_f32 v230, v252, v230, 0xc0135761\n\t"
      "v_fmaak_f32 v231, v252, v231, 0xc0135761\n\t"
      "v_mul_f32_e32 v230, v228, v230\n\t"
      "v_mfma_f32_16x16x32_bf16 v[212:215], v[56:59], v[172:175], v[212:215]\n\t"
      "v_mul_f32_e32 v231, v229, v231\n\t"
      "v_exp_f32_e32 v230, v230\n\t"
      "v_mfma_f32_16x16x32_bf16 v[212:215], v[72:75], v[176:179], v[212:215]\n\t"
      "v_exp_f32_e32 v231, v231\n\t"
      "v_add_f32_e32 v230, 1.0, v230\n\t"
      "v_add_f32_e32 v231, 1.0, v231\n\t"
      "global_load_dwordx4 v[156:159], v250, s[70:71] offset:-1024 sc1\n\t"
      "v_mfma_f32_16x16x32_bf16 v[212:215], v[88:91], v[180:183], v[212:215]\n\t"
      "v_rcp_f32_e32 v230, v230\n\t"
      "v_rcp_f32_e32 v231, v231\n\t"
      "v_mfma_f32_16x16x32_bf16 v[212:215], v[104:107], v[184:187], v[212:215]\n\t"
      "v_mul_f32_e32 v228, v228, v230\n\t"
      "v_mul_f32_e32 v229, v229, v231\n\t"
      "v_cvt_pk_bf16_f32 v232, v228, v229\n\t"
      "ds_write_b128 v253, v[208:211] offset:33792\n\t"
      "v_mfma_f32_16x16x32_bf16 v[212:215], v[120:123], v[188:191], v[212:215]\n\t"
      "v_min_u32_e32 v254, -2, v232\n\t"
      "global_store_dword v247, v254, s[62:63] sc1\n\t"
      "s_add_u32 s62, s62, 57344\n\t"
      "s_addc_u32 s63, s63, 0\n\t"
      "v_mfma_f32_16x16x32_bf16 v[216:219], v[12:15], v[160:163], 0\n\t"
      "global_load_dwordx4 v[160:163], v250, s[70:71] sc1\n\t"
      "v_mfma_f32_16x16x32_bf16 v[216:219], v[28:31], v[164:167], v[216:219]\n\t"
      "v_mfma_f32_16x16x32_bf16 v[216:219], v[44:47], v[168:171], v[216:219]\n\t"
      "v_mfma_f32_16x16x32_bf16 v[216:219], v[60:63], v[172:175], v[216:219]\n\t"
      "v_mfma_f32_16x16x32_bf16 v[216:219], v[76:79], v[176:179], v[216:219]\n\t"
      "global_load_dwordx4 v[164:167], v250, s[70:71] offset:1024 sc1\n\t"
      "v_mfma_f32_16x16x32_bf16 v[216:219], v[92:95], v[180:183], v[216:219]\n\t"
      "v_mfma_f32_16x16x32_bf16 v[216:219], v[108:111], v[184:187], v[216:219]\n\t"
      "ds_write_b128 v245, v[212:215] offset:34816\n\t"
      "v_mfma_f32_16x16x32_bf16 v[216:219], v[124:127], v[188:191], v[216:219]\n\t"
      "s_waitcnt vmcnt(11)\n\t"
      "v_mfma_f32_16x16x32_bf16 v[220:223], v[0:3], v[192:195], 0\n\t"
      "global_load_dwordx4 v[168:171], v250, s[70:71] offset:2048 sc1\n\t"
      "v_mfma_f32_16x16x32_bf16 v[220:223], v[16:19], v[196:199], v[220:223]\n\t"
      "s_nop 3\n\t"
      "ds_write_b128 v253, v[216:219] offset:35840\n\t"
      "v_mfma_f32_16x16x32_bf16 v[220:223], v[32:35], v[200:203], v[220:223]\n\t"
      "v_mfma_f32_16x16x32_bf16 v[220:223], v[48:51], v[204:207], v[220:223]\n\t"
      "s_waitcnt vmcnt(8)\n\t"
      "v_mfma_f32_16x16x32_bf16 v[220:223], v[64:67], v[128:131], v[220:223]\n\t"
      "global_load_dwordx4 v[172:175], v250, s[70:71] offset:3072 sc1\n\t"
      "v_mfma_f32_16x16x32_bf16 v[220:223], v[80:83], v[132:135], v[220:223]\n\t"
      "s_waitcnt lgkmcnt(0)\n\t"
      "s_barrier\n\t"
      "ds_read_b64 v[228:229], v246 offset:32768\n\t"
      "ds_read_b64 v[230:231], v246 offset:36864\n\t"
      "v_mfma_f32_16x16x32_bf16 v[220:223], v[96:99], v[136:139], v[220:223]\n\t"
      "ds_read_b64 v[232:233], v246 offset:40960\n\t"
      "ds_read_b64 v[234:235], v246 offset:45056\n\t"
      "v_mfma_f32_16x16x32_bf16 v[220:223], v[112:115], v[140:143], v[220:223]\n\t"
      "ds_read_b64 v[236:237], v246 offset:49152\n\t"
      "ds_read_b64 v[238:239], v246 offset:53248\n\t"
      "v_mfma_f32_16x16x32_bf16 v[224:227], v[4:7], v[192:195], 0\n\t"
      "ds_read_b64 v[240:241], v246 offset:57344\n\t"
      "ds_read_b64 v[242:243], v246 offset:61440\n\t"
      "global_load_dwordx4 v[176:179], v250, s[72:73] offset:-4096 sc1\n\t"
      "v_mfma_f32_16x16x32_bf16 v[224:227], v[20:23], v[196:199], v[224:227]\n\t"
      "v_mfma_f32_16x16x32_bf16 v[224:227], v[36:39], v[200:203], v[224:227]\n\t"
      "s_waitcnt lgkmcnt(4)\n\t"
      "v_add_f32_e32 v228, v228, v230\n\t"
      "v_add_f32_e32 v232, v232, v234\n\t"
      "v_cmp_u_f32_e32 vcc, v220, v220\n\t"
      "s_cbranch_vccnz LA32R_%=\n\t"
      "LA32B_%=:\n\t"
      "v_mfma_f32_16x16x32_bf16 v[224:227], v[52:55], v[204:207], v[224:227]\n\t"
      "v_add_f32_e32 v229, v229, v231\n\t"
      "v_add_f32_e32 v233, v233, v235\n\t"
      "v_mfma_f32_16x16x32_bf16 v[224:227], v[68:71], v[128:131], v[224:227]\n\t"
      "s_waitcnt lgkmcnt(0)\n\t"
      "v_add_f32_e32 v236, v236, v238\n\t"
      "v_add_f32_e32 v240, v240, v242\n\t"
      "global_load_dwordx4 v[180:183], v250, s[72:73] offset:-3072 sc1\n\t"
      "v_mfma_f32_16x16x32_bf16 v[224:227], v[84:87], v[132:135], v[224:227]\n\t"
      "v_add_f32_e32 v237, v237, v239\n\t"
      "v_add_f32_e32 v241, v241, v243\n\t"
      "v_mfma_f32_16x16x32_bf16 v[224:227], v[100:103], v[136:139], v[224:227]\n\t"
      "v_add_f32_e32 v228, v228, v232\n\t"
      "v_add_f32_e32 v236, v236, v240\n\t"
      "v_add_f32_e32 v229, v229, v233\n\t"
      "ds_write_b128 v245, v[220:223]\n\t"
      "v_mfma_f32_16x16x32_bf16 v[224:227], v[116:119], v[140:143], v[224:227]\n\t"
      "v_add_f32_e32 v237, v237, v241\n\t"
      "v_add_f32_e32 v228, v228, v248\n\t"
      "v_mfma_f32_16x16x32_bf16 v[208:211], v[8:11], v[192:195], 0\n\t"
      "v_add_f32_e32 v229, v229, v249\n\t"
      "v_add_f32_e32 v228, v228, v236\n\t"
      "v_add_f32_e32 v229, v229, v237\n\t"
      "global_load_dwordx4 v[184:187], v250, s[72:73] offset:-2048 sc1\n\t"
      "v_mfma_f32_16x16x32_bf16 v[208:211], v[24:27], v[196:199], v[208:211]\n\t"
      "v_mul_f32_e32 v230, v228, v228\n\t"
      "v_mul_f32_e32 v231, v229, v229\n\t"
      "v_mfma_f32_16x16x32_bf16 v[208:211], v[40:43], v[200:203], v[208:211]\n\t"
      "v_fmaak_f32 v230, v252, v230, 0xc0135761\n\t"
      "v_fmaak_f32 v231, v252, v231, 0xc0135761\n\t"
      "v_mul_f32_e32 v230, v228, v230\n\t"
      "v_mfma_f32_16x16x32_bf16 v[208:211], v[56:59], v[204:207], v[208:211]\n\t"
      "v_mul_f32_e32 v231, v229, v231\n\t"
      "v_exp_f32_e32 v230, v230\n\t"
      "v_mfma_f32_16x16x32_bf16 v[208:211], v[72:75], v[128:131], v[208:211]\n\t"
      "v_exp_f32_e32 v231, v231\n\t"
      "v_add_f32_e32 v230, 1.0, v230\n\t"
      "v_add_f32_e32 v231, 1.0, v231\n\t"
      "global_load_dwordx4 v[188:191], v250, s[72:73] offset:-1024 sc1\n\t"
      "v_mfma_f32_16x16x32_bf16 v[208:211], v[88:91], v[132:135], v[208:211]\n\t"
      "v_rcp_f32_e32 v230, v230\n\t"
      "v_rcp_f32_e32 v231, v231\n\t"
      "v_mfma_f32_16x16x32_bf16 v[208:211], v[104:107], v[136:139], v[208:211]\n\t"
      "v_mul_f32_e32 v228, v228, v230\n\t"
      "v_mul_f32_e32 v229, v229, v231\n\t"
      "v_cvt_pk_bf16_f32 v232, v228, v229\n\t"
      "ds_write_b128 v253, v[224:227] offset:1024\n\t"
      "v_mfma_f32_16x16x32_bf16 v[208:211], v[120:123], v[140:143], v[208:211]\n\t"
      "v_min_u32_e32 v254, -2, v232\n\t"
      "global_store_dword v247, v254, s[62:63] sc1\n\t"
      "s_add_u32 s62, s62, 57344\n\t"
      "s_addc_u32 s63, s63, 0\n\t"
      "v_mfma_f32_16x16x32_bf16 v[212:215], v[12:15], v[192:195], 0\n\t"
      "global_load_dwordx4 v[192:195], v250, s[72:73] sc1\n\t"
      "v_mfma_f32_16x16x32_bf16 v[212:215], v[28:31], v[196:199], v[212:215]\n\t"
      "v_mfma_f32_16x16x32_bf16 v[212:215], v[44:47], v[200:203], v[212:215]\n\t"
      "v_mfma_f32_16x16x32_bf16 v[212:215], v[60:63], v[204:207], v[212:215]\n\t"
      "v_mfma_f32_16x16x32_bf16 v[212:215], v[76:79], v[128:131], v[212:215]\n\t"
      "global_load_dwordx4 v[196:199], v250, s[72:73] offset:1024 sc1\n\t"
      "v_mfma_f32_16x16x32_bf16 v[212:215], v[92:95], v[132:135], v[212:215]\n\t"
      "v_mfma_f32_16x16x32_bf16 v[212:215], v[108:111], v[136:139], v[212:215]\n\t"
      "ds_write_b128 v245, v[208:211] offset:2048\n\t"
      "v_mfma_f32_16x16x32_bf16 v[212:215], v[124:127], v[140:143], v[212:215]\n\t"
      "s_waitcnt vmcnt(12)\n\t"
      "v_mfma_f32_16x16x32_bf16 v[216:219], v[0:3], v[144:147], 0\n\t"
      "global_load_dwordx4 v[200:203], v250, s[72:73] offset:2048 sc1\n\t"
      "v_mfma_f32_16x16x32_bf16 v[216:219], v[16:19], v[148:151], v[216:219]\n\t"
      "s_nop 3\n\t"
      "ds_write_b128 v253, v[212:215] offset:3072\n\t"
      "v_mfma_f32_16x16x32_bf16 v[216:219], v[32:35], v[152:155], v[216:219]\n\t"
      "v_mfma_f32_16x16x32_bf16 v[216:219], v[48:51], v[156:159], v[216:219]\n\t"
      "s_waitcnt vmcnt(8)\n\t"
      "v_mfma_f32_16x16x32_bf16 v[216:219], v[64:67], v[160:163], v[216:219]\n\t"
      "global_load_dwordx4 v[204:207], v250, s[72:73] offset:3072 sc1\n\t"
      "v_mfma_f32_16x16x32_bf16 v[216:219], v[80:83], v[164:167], v[216:219]\n\t"
      "s_waitcnt lgkmcnt(0)\n\t"
      "s_barrier\n\t"
      "ds_read_b64 v[228:229], v246\n\t"
      "ds_read_b64 v[230:231], v246 offset:4096\n\t"
      "v_mfma_f32_16x16x32_bf16 v[216:219], v[96:99], v[168:171], v[216:219]\n\t"
      "ds_read_b64 v[232:233], v246 offset:8192\n\t"
      "ds_read_b64 v[234:235], v246 offset:12288\n\t"
      "v_mfma_f32_16x16x32_bf16 v[216:219], v[112:115], v[172:175], v[216:219]\n\t"
      "ds_read_b64 v[236:237], v246 offset:16384\n\t"
      "ds_read_b64 v[238:239], v246 offset:20480\n\t"
      "v_mfma_f32_16x16x32_bf16 v[220:223], v[4:7], v[144:147], 0\n\t"
      "ds_read_b64 v[240:241], v246 offset:24576\n\t"
      "ds_read_b64 v[242:243], v246 offset:28672\n\t"
      "global_load_dwordx4 v[128:131], v250, s[74:75] offset:-4096 sc1\n\t"
      "v_mfma_f32_16x16x32_bf16 v[220:223], v[20:23], v[148:151], v[220:223]\n\t"
      "v_mfma_f32_16x16x32_bf16 v[220:223], v[36:39], v[152:155], v[220:223]\n\t"
      "s_waitcnt lgkmcnt(4)\n\t"
      "v_add_f32_e32 v228, v228, v230\n\t"
      "v_add_f32_e32 v232, v232, v234\n\t"
      "v_cmp_u_f32_e32 vcc, v216, v216\n\t"
      "s_cbranch_vccnz LA33R_%=\n\t"
      "LA33B_%=:\n\t"
      "v_mfma_f32_16x16x32_bf16 v[220:223], v[52:55], v[156:159], v[220:223]\n\t"
      "v_add_f32_e32 v229, v229, v231\n\t"
      "v_add_f32_e32 v233, v233, v235\n\t"
      "v_mfma_f32_16x16x32_bf16 v[220:223], v[68:71], v[160:163], v[220:223]\n\t"
      "s_waitcnt lgkmcnt(0)\n\t"
      "v_add_f32_e32 v236, v236, v238\n\t"
      "v_add_f32_e32 v240, v240, v242\n\t"
      "global_load_dwordx4 v[132:135], v250, s[74:75] offset:-3072 sc1\n\t"
      "v_mfma_f32_16x16x32_bf16 v[220:223], v[84:87], v[164:167], v[220:223]\n\t"
      "v_add_f32_e32 v237, v237, v239\n\t"
      "v_add_f32_e32 v241, v241, v243\n\t"
      "v_mfma_f32_16x16x32_bf16 v[220:223], v[100:103], v[168:171], v[220:223]\n\t"
      "v_add_f32_e32 v228, v228, v232\n\t"
      "v_add_f32_e32 v236, v236, v240\n\t"
      "v_add_f32_e32 v229, v229, v233\n\t"
      "ds_write_b128 v245, v[216:219] offset:32768\n\t"
      "v_mfma_f32_16x16x32_bf16 v[220:223], v[116:119], v[172:175], v[220:223]\n\t"
      "v_add_f32_e32 v237, v237, v241\n\t"
      "v_add_f32_e32 v228, v228, v248\n\t"
      "v_mfma_f32_16x16x32_bf16 v[224:227], v[8:11], v[144:147], 0\n\t"
      "v_add_f32_e32 v229, v229, v249\n\t"
      "v_add_f32_e32 v228, v228, v236\n\t"
      "v_add_f32_e32 v229, v229, v237\n\t"
      "global_load_dwordx4 v[136:139], v250, s[74:75] offset:-2048 sc1\n\t"
      "v_mfma_f32_16x16x32_bf16 v[224:227], v[24:27], v[148:151], v[224:227]\n\t"
      "v_mul_f32_e32 v230, v228, v228\n\t"
      "v_mul_f32_e32 v231, v229, v229\n\t"
      "v_mfma_f32_16x16x32_bf16 v[224:227], v[40:43], v[152:155], v[224:227]\n\t"
      "v_fmaak_f32 v230, v252, v230, 0xc0135761\n\t"
      "v_fmaak_f32 v231, v252, v231, 0xc0135761\n\t"
      "v_mul_f32_e32 v230, v228, v230\n\t"
      "v_mfma_f32_16x16x32_bf16 v[224:227], v[56:59], v[156:159], v[224:227]\n\t"
      "v_mul_f32_e32 v231, v229, v231\n\t"
      "v_exp_f32_e32 v230, v230\n\t"
      "v_mfma_f32_16x16x32_bf16 v[224:227], v[72:75], v[160:163], v[224:227]\n\t"
      "v_exp_f32_e32 v231, v231\n\t"
      "v_add_f32_e32 v230, 1.0, v230\n\t"
      "v_add_f32_e32 v231, 1.0, v231\n\t"
      "global_load_dwordx4 v[140:143], v250, s[74:75] offset:-1024 sc1\n\t"
      "v_mfma_f32_16x16x32_bf16 v[224:227], v[88:91], v[164:167], v[224:227]\n\t"
      "v_rcp_f32_e32 v230, v230\n\t"
      "v_rcp_f32_e32 v231, v231\n\t"
      "v_mfma_f32_16x16x32_bf16 v[224:227], v[104:107], v[168:171], v[224:227]\n\t"
      "v_mul_f32_e32 v228, v228, v230\n\t"
      "v_mul_f32_e32 v229, v229, v231\n\t"
      "v_cvt_pk_bf16_f32 v232, v228, v229\n\t"
      "ds_write_b128 v253, v[220:223] offset:33792\n\t"
      "v_mfma_f32_16x16x32_bf16 v[224:227], v[120:123], v[172:175], v[224:227]\n\t"
      "v_min_u32_e32 v254, -2, v232\n\t"
      "global_store_dword v247, v254, s[62:63] sc1\n\t"
      "s_add_u32 s62, s62, 57344\n\t"
      "s_addc_u32 s63, s63, 0\n\t"
      "v_mfma_f32_16x16x32_bf16 v[208:211], v[12:15], v[144:147], 0\n\t"
      "global_load_dwordx4 v[144:147], v250, s[74:75] sc1\n\t"
      "v_mfma_f32_16x16x32_bf16 v[208:211], v[28:31], v[148:151], v[208:211]\n\t"
      "v_mfma_f32_16x16x32_bf16 v[208:211], v[44:47], v[152:155], v[208:211]\n\t"
      "v_mfma_f32_16x16x32_bf16 v[208:211], v[60:63], v[156:159], v[208:211]\n\t"
      "v_mfma_f32_16x16x32_bf16 v[208:211], v[76:79], v[160:163], v[208:211]\n\t"
      "global_load_dwordx4 v[148:151], v250, s[74:75] offset:1024 sc1\n\t"
      "v_mfma_f32_16x16x32_bf16 v[208:211], v[92:95], v[164:167], v[208:211]\n\t"
      "v_mfma_f32_16x16x32_bf16 v[208:211], v[108:111], v[168:171], v[208:211]\n\t"
      "ds_write_b128 v245, v[224:227] offset:34816\n\t"
      "v_mfma_f32_16x16x32_bf16 v[208:211], v[124:127], v[172:175], v[208:211]\n\t"
      "s_waitcnt vmcnt(12)\n\t"
      "v_mfma_f32_16x16x32_bf16 v[212:215], v[0:3], v[176:179], 0\n\t"
      "global_load_dwordx4 v[152:155], v250, s[74:75] offset:2048 sc1\n\t"
      "v_mfma_f32_16x16x32_bf16 v[212:215], v[16:19], v[180:183], v[212:215]\n\t"
      "s_nop 3\n\t"
      "ds_write_b128 v253, v[208:211] offset:35840\n\t"
      "v_mfma_f32_16x16x32_bf16 v[212:215], v[32:35], v[184:187], v[212:215]\n\t"
      "v_mfma_f32_16x16x32_bf16 v[212:215], v[48:51], v[188:191], v[212:215]\n\t"
      "s_waitcnt vmcnt(8)\n\t"
      "v_mfma_f32_16x16x32_bf16 v[212:215], v[64:67], v[192:195], v[212:215]\n\t"
      "global_load_dwordx4 v[156:159], v250, s[74:75] offset:3072 sc1\n\t"
      "v_mfma_f32_16x16x32_bf16 v[212:215], v[80:83], v[196:199], v[212:215]\n\t"
      "s_waitcnt lgkmcnt(0)\n\t"
      "s_barrier\n\t"
      "ds_read_b64 v[228:229], v246 offset:32768\n\t"
      "ds_read_b64 v[230:231], v246 offset:36864\n\t"
      "v_mfma_f32_16x16x32_bf16 v[212:215], v[96:99], v[200:203], v[212:215]\n\t"
      "ds_read_b64 v[232:233], v246 offset:40960\n\t"
      "ds_read_b64 v[234:235], v246 offset:45056\n\t"
      "v_mfma_f32_16x16x32_bf16 v[212:215], v[112:115], v[204:207], v[212:215]\n\t"
      "ds_read_b64 v[236:237], v246 offset:49152\n\t"
      "ds_read_b64 v[238:239], v246 offset:53248\n\t"
      "v_mfma_f32_16x16x32_bf16 v[216:219], v[4:7], v[176:179], 0\n\t"
      "ds_read_b64 v[240:241], v246 offset:57344\n\t"
      "ds_read_b64 v[242:243], v246 offset:61440\n\t"
      "global_load_dwordx4 v[160:163], v250, s[76:77] offset:-4096 sc1\n\t"
      "v_mfma_f32_16x16x32_bf16 v[216:219], v[20:23], v[180:183], v[216:219]\n\t"
      "v_mfma_f32_16x16x32_bf16 v[216:219], v[36:39], v[184:187], v[216:219]\n\t"
      "s_waitcnt lgkmcnt(4)\n\t"
      "v_add_f32_e32 v228, v228, v230\n\t"
      "v_add_f32_e32 v232, v232, v234\n\t"
      "v_cmp_u_f32_e32 vcc, v212, v212\n\t"
      "s_cbranch_vccnz LA34R_%=\n\t"
      "LA34B_%=:\n\t"
      "v_mfma_f32_16x16x32_bf16 v[216:219], v[52:55], v[188:191], v[216:219]\n\t"
      "v_add_f32_e32 v229, v229, v231\n\t"
      "v_add_f32_e32 v233, v233, v235\n\t"
      "v_mfma_f32_16x16x32_bf16 v[216:219], v[68:71], v[192:195], v[216:219]\n\t"
      "s_waitcnt lgkmcnt(0)\n\t"
      "v_add_f32_e32 v236, v236, v238\n\t"
      "v_add_f32_e32 v240, v240, v242\n\t"
      "global_load_dwordx4 v[164:167], v250, s[76:77] offset:-3072 sc1\n\t"
      "v_mfma_f32_16x16x32_bf16 v[216:219], v[84:87], v[196:199], v[216:219]\n\t"
      "v_add_f32_e32 v237, v237, v239\n\t"
      "v_add_f32_e32 v241, v241, v243\n\t"
      "v_mfma_f32_16x16x32_bf16 v[216:219], v[100:103], v[200:203], v[216:219]\n\t"
      "v_add_f32_e32 v228, v228, v232\n\t"
      "v_add_f32_e32 v236, v236, v240\n\t"
      "v_add_f32_e32 v229, v229, v233\n\t"
      "ds_write_b128 v245, v[212:215]\n\t"
      "v_mfma_f32_16x16x32_bf16 v[216:219], v[116:119], v[204:207], v[216:219]\n\t"
      "v_add_f32_e32 v237, v237, v241\n\t"
      "v_add_f32_e32 v228, v228, v248\n\t"
      "v_mfma_f32_16x16x32_bf16 v[220:223], v[8:11], v[176:179], 0\n\t"
      "v_add_f32_e32 v229, v229, v249\n\t"
      "v_add_f32_e32 v228, v228, v236\n\t"
      "v_add_f32_e32 v229, v229, v237\n\t"
      "global_load_dwordx4 v[168:171], v250, s[76:77] offset:-2048 sc1\n\t"
      "v_mfma_f32_16x16x32_bf16 v[220:223], v[24:27], v[180:183], v[220:223]\n\t"
      "v_mul_f32_e32 v230, v228, v228\n\t"
      "v_mul_f32_e32 v231, v229, v229\n\t"
      "v_mfma_f32_16x16x32_bf16 v[220:223], v[40:43], v[184:187], v[220:223]\n\t"
      "v_fmaak_f32 v230, v252, v230, 0xc0135761\n\t"
      "v_fmaak_f32 v231, v252, v231, 0xc0135761\n\t"
      "v_mul_f32_e32 v230, v228, v230\n\t"
      "v_mfma_f32_16x16x32_bf16 v[220:223], v[56:59], v[188:191], v[220:223]\n\t"
      "v_mul_f32_e32 v231, v229, v231\n\t"
      "v_exp_f32_e32 v230, v230\n\t"
      "v_mfma_f32_16x16x32_bf16 v[220:223], v[72:75], v[192:195], v[220:223]\n\t"
      "v_exp_f32_e32 v231, v231\n\t"
      "v_add_f32_e32 v230, 1.0, v230\n\t"
      "v_add_f32_e32 v231, 1.0, v231\n\t"
      "global_load_dwordx4 v[172:175], v250, s[76:77] offset:-1024 sc1\n\t"
      "v_mfma_f32_16x16x32_bf16 v[220:223], v[88:91], v[196:199], v[220:223]\n\t"
      "v_rcp_f32_e32 v230, v230\n\t"
      "v_rcp_f32_e32 v231, v231\n\t"
      "v_mfma_f32_16x16x32_bf16 v[220:223], v[104:107], v[200:203], v[220:223]\n\t"
      "v_mul_f32_e32 v228, v228, v230\n\t"
      "v_mul_f32_e32 v229, v229, v231\n\t"
      "v_cvt_pk_bf16_f32 v232, v228, v229\n\t"
      "ds_write_b128 v253, v[216:219] offset:1024\n\t"
      "v_mfma_f32_16x16x32_bf16 v[220:223], v[120:123], v[204:207], v[220:223]\n\t"
      "v_min_u32_e32 v254, -2, v232\n\t"
      "global_store_dword v247, v254, s[62:63] sc1\n\t"
      "s_add_u32 s62, s62, 57344\n\t"
      "s_addc_u32 s63, s63, 0\n\t"
      "v_mfma_f32_16x16x32_bf16 v[224:227], v[12:15], v[176:179], 0\n\t"
      "global_load_dwordx4 v[176:179], v250, s[76:77] sc1\n\t"
      "v_mfma_f32_16x16x32_bf16 v[224:227], v[28:31], v[180:183], v[224:227]\n\t"
      "v_mfma_f32_16x16x32_bf16 v[224:227], v[44:47], v[184:187], v[224:227]\n\t"
      "v_mfma_f32_16x16x32_bf16 v[224:227], v[60:63], v[188:191], v[224:227]\n\t"
      "v_mfma_f32_16x16x32_bf16 v[224:227], v[76:79], v[192:195], v[224:227]\n\t"
      "global_load_dwordx4 v[180:183], v250, s[76:77] offset:1024 sc1\n\t"
      "v_mfma_f32_16x16x32_bf16 v[224:227], v[92:95], v[196:199], v[224:227]\n\t"
      "v_mfma_f32_16x16x32_bf16 v[224:227], v[108:111], v[200:203], v[224:227]\n\t"
      "ds_write_b128 v245, v[220:223] offset:2048\n\t"
      "v_mfma_f32_16x16x32_bf16 v[224:227], v[124:127], v[204:207], v[224:227]\n\t"
      "s_waitcnt vmcnt(12)\n\t"
      "v_mfma_f32_16x16x32_bf16 v[208:211], v[0:3], v[128:131], 0\n\t"
      "global_load_dwordx4 v[184:187], v250, s[76:77] offset:2048 sc1\n\t"
      "v_mfma_f32_16x16x32_bf16 v[208:211], v[16:19], v[132:135], v[208:211]\n\t"
      "s_nop 3\n\t"
      "ds_write_b128 v253, v[224:227] offset:3072\n\t"
      "v_mfma_f32_16x16x32_bf16 v[208:211], v[32:35], v[136:139], v[208:211]\n\t"
      "v_mfma_f32_16x16x32_bf16 v[208:211], v[48:51], v[140:143], v[208:211]\n\t"
      "s_waitcnt vmcnt(8)\n\t"
      "v_mfma_f32_16x16x32_bf16 v[208:211], v[64:67], v[144:147], v[208:211]\n\t"
      "global_load_dwordx4 v[188:191], v250, s[76:77] offset:3072 sc1\n\t"
      "v_mfma_f32_16x16x32_bf16 v[208:211], v[80:83], v[148:151], v[208:211]\n\t"
      "s_waitcnt lgkmcnt(0)\n\t"
      "s_barrier\n\t"
      "ds_read_b64 v[228:229], v246\n\t"
      "ds_read_b64 v[230:231], v246 offset:4096\n\t"
      "v_mfma_f32_16x16x32_bf16 v[208:211], v[96:99], v[152:155], v[208:211]\n\t"
      "ds_read_b64 v[232:233], v246 offset:8192\n\t"
      "ds_read_b64 v[234:235], v246 offset:12288\n\t"
      "v_mfma_f32_16x16x32_bf16 v[208:211], v[112:115], v[156:159], v[208:211]\n\t"
      "ds_read_b64 v[236:237], v246 offset:16384\n\t"
      "ds_read_b64 v[238:239], v246 offset:20480\n\t"
      "v_mfma_f32_16x16x32_bf16 v[212:215], v[4:7], v[128:131], 0\n\t"
      "ds_read_b64 v[240:241], v246 offset:24576\n\t"
      "ds_read_b64 v[242:243], v246 offset:28672\n\t"
      "v_mfma_f32_16x16x32_bf16 v[212:215], v[20:23], v[132:135], v[212:215]\n\t"
      "v_mfma_f32_16x16x32_bf16 v[212:215], v[36:39], v[136:139], v[212:215]\n\t"
      "s_waitcnt lgkmcnt(4)\n\t"
      "v_add_f32_e32 v228, v228, v230\n\t"
      "v_add_f32_e32 v232, v232, v234\n\t"
      "v_cmp_u_f32_e32 vcc, v208, v208\n\t"
      "s_cbranch_vccnz LA35R_%=\n\t"
      "LA35B_%=:\n\t"
      "v_mfma_f32_16x16x32_bf16 v[212:215], v[52:55], v[140:143], v[212:215]\n\t"
      "v_add_f32_e32 v229, v229, v231\n\t"
      "v_add_f32_e32 v233, v233, v235\n\t"
      "v_mfma_f32_16x16x32_bf16 v[212:215], v[68:71], v[144:147], v[212:215]\n\t"
      "s_waitcnt lgkmcnt(0)\n\t"
      "v_add_f32_e32 v236, v236, v238\n\t"
      "v_add_f32_e32 v240, v240, v242\n\t"
      "v_mfma_f32_16x16x32_bf16 v[212:215], v[84:87], v[148:151], v[212:215]\n\t"
      "v_add_f32_e32 v237, v237, v239\n\t"
      "v_add_f32_e32 v241, v241, v243\n\t"
      "v_mfma_f32_16x16x32_bf16 v[212:215], v[100:103], v[152:155], v[212:215]\n\t"
      "v_add_f32_e32 v228, v228, v232\n\t"
      "v_add_f32_e32 v236, v236, v240\n\t"
      "v_add_f32_e32 v229, v229, v233\n\t"
      "ds_write_b128 v245, v[208:211] offset:32768\n\t"
      "v_mfma_f32_16x16x32_bf16 v[212:215], v[116:119], v[156:159], v[212:215]\n\t"
      "v_add_f32_e32 v237, v237, v241\n\t"
      "v_add_f32_e32 v228, v228, v248\n\t"
      "v_mfma_f32_16x16x32_bf16 v[216:219], v[8:11], v[128:131], 0\n\t"
      "v_add_f32_e32 v229, v229, v249\n\t"
      "v_add_f32_e32 v228, v228, v236\n\t"
      "v_add_f32_e32 v229, v229, v237\n\t"
      "v_mfma_f32_16x16x32_bf16 v[216:219], v[24:27], v[132:135], v[216:219]\n\t"
      "v_mul_f32_e32 v230, v228, v228\n\t"
      "v_mul_f32_e32 v231, v229, v229\n\t"
      "v_mfma_f32_16x16x32_bf16 v[216:219], v[40:43], v[136:139], v[216:219]\n\t"
      "v_fmaak_f32 v230, v252, v230, 0xc0135761\n\t"
      "v_fmaak_f32 v231, v252, v231, 0xc0135761\n\t"
      "v_mul_f32_e32 v230, v228, v230\n\t"
      "v_mfma_f32_16x16x32_bf16 v[216:219], v[56:59], v[140:143], v[216:219]\n\t"
      "v_mul_f32_e32 v231, v229, v231\n\t"
      "v_exp_f32_e32 v230, v230\n\t"
      "v_mfma_f32_16x16x32_bf16 v[216:219], v[72:75], v[144:147], v[216:219]\n\t"
      "v_exp_f32_e32 v231, v231\n\t"
      "v_add_f32_e32 v230, 1.0, v230\n\t"
      "v_add_f32_e32 v231, 1.0, v231\n\t"
      "v_mfma_f32_16x16x32_bf16 v[216:219], v[88:91], v[148:151], v[216:219]\n\t"
      "v_rcp_f32_e32 v230, v230\n\t"
      "v_rcp_f32_e32 v231, v231\n\t"
      "v_mfma_f32_16x16x32_bf16 v[216:219], v[104:107], v[152:155], v[216:219]\n\t"
      "v_mul_f32_e32 v228, v228, v230\n\t"
      "v_mul_f32_e32 v229, v229, v231\n\t"
      "v_cvt_pk_bf16_f32 v232, v228, v229\n\t"
      "ds_write_b128 v253, v[212:215] offset:33792\n\t"
      "v_mfma_f32_16x16x32_bf16 v[216:219], v[120:123], v[156:159], v[216:219]\n\t"
      "v_min_u32_e32 v254, -2, v232\n\t"
      "global_store_dword v247, v254, s[62:63] sc1\n\t"
      "s_add_u32 s62, s62, 57344\n\t"
      "s_addc_u32 s63, s63, 0\n\t"
      "v_mfma_f32_16x16x32_bf16 v[220:223], v[12:15], v[128:131], 0\n\t"
      "v_mfma_f32_16x16x32_bf16 v[220:223], v[28:31], v[132:135], v[220:223]\n\t"
      "v_mfma_f32_16x16x32_bf16 v[220:223], v[44:47], v[136:139], v[220:223]\n\t"
      "v_mfma_f32_16x16x32_bf16 v[220:223], v[60:63], v[140:143], v[220:223]\n\t"
      "v_mfma_f32_16x16x32_bf16 v[220:223], v[76:79], v[144:147], v[220:223]\n\t"
      "v_mfma_f32_16x16x32_bf16 v[220:223], v[92:95], v[148:151], v[220:223]\n\t"
      "v_mfma_f32_16x16x32_bf16 v[220:223], v[108:111], v[152:155], v[220:223]\n\t"
      "ds_write_b128 v245, v[216:219] offset:34816\n\t"
      "v_mfma_f32_16x16x32_bf16 v[220:223], v[124:127], v[156:159], v[220:223]\n\t"
      "s_waitcnt vmcnt(6)\n\t"
      "v_mfma_f32_16x16x32_bf16 v[224:227], v[0:3], v[160:163], 0\n\t"
      "v_mfma_f32_16x16x32_bf16 v[224:227], v[16:19], v[164:167], v[224:227]\n\t"
      "s_nop 4\n\t"
      "ds_write_b128 v253, v[220:223] offset:35840\n\t"
      "v_mfma_f32_16x16x32_bf16 v[224:227], v[32:35], v[168:171], v[224:227]\n\t"
      "v_mfma_f32_16x16x32_bf16 v[224:227], v[48:51], v[172:175], v[224:227]\n\t"
      "s_waitcnt vmcnt(1)\n\t"
      "v_mfma_f32_16x16x32_bf16 v[224:227], v[64:67], v[176:179], v[224:227]\n\t"
      "v_mfma_f32_16x16x32_bf16 v[224:227], v[80:83], v[180:183], v[224:227]\n\t"
      "s_waitcnt lgkmcnt(0)\n\t"
      "s_barrier\n\t"
      "ds_read_b64 v[228:229], v246 offset:32768\n\t"
      "ds_read_b64 v[230:231], v246 offset:36864\n\t"
      "v_mfma_f32_16x16x32_bf16 v[224:227], v[96:99], v[184:187], v[224:227]\n\t"
      "ds_read_b64 v[232:233], v246 offset:40960\n\t"
      "ds_read_b64 v[234:235], v246 offset:45056\n\t"
      "v_mfma_f32_16x16x32_bf16 v[224:227], v[112:115], v[188:191], v[224:227]\n\t"
      "ds_read_b64 v[236:237], v246 offset:49152\n\t"
      "ds_read_b64 v[238:239], v246 offset:53248\n\t"
      "v_mfma_f32_16x16x32_bf16 v[208:211], v[4:7], v[160:163], 0\n\t"
      "ds_read_b64 v[240:241], v246 offset:57344\n\t"
      "ds_read_b64 v[242:243], v246 offset:61440\n\t"
      "v_mfma_f32_16x16x32_bf16 v[208:211], v[20:23], v[164:167], v[208:211]\n\t"
      "v_mfma_f32_16x16x32_bf16 v[208:211], v[36:39], v[168:171], v[208:211]\n\t"
      "s_waitcnt lgkmcnt(4)\n\t"
      "v_add_f32_e32 v228, v228, v230\n\t"
      "v_add_f32_e32 v232, v232, v234\n\t"
      "v_cmp_u_f32_e32 vcc, v224, v224\n\t"
      "s_cbranch_vccnz LA36R_%=\n\t"
      "LA36B_%=:\n\t"
      "v_mfma_f32_16x16x32_bf16 v[208:211], v[52:55], v[172:175], v[208:211]\n\t"
      "v_add_f32_e32 v229, v229, v231\n\t"
      "v_add_f32_e32 v233, v233, v235\n\t"
      "v_mfma_f32_16x16x32_bf16 v[208:211], v[68:71], v[176:179], v[208:211]\n\t"
      "s_waitcnt lgkmcnt(0)\n\t"
      "v_add_f32_e32 v236, v236, v238\n\t"
      "v_add_f32_e32 v240, v240, v242\n\t"
      "v_mfma_f32_16x16x32_bf16 v[208:211], v[84:87], v[180:183], v[208:211]\n\t"
      "v_add_f32_e32 v237, v237, v239\n\t"
      "v_add_f32_e32 v241, v241, v243\n\t"
      "v_mfma_f32_16x16x32_bf16 v[208:211], v[100:103], v[184:187], v[208:211]\n\t"
      "v_add_f32_e32 v228, v228, v232\n\t"
      "v_add_f32_e32 v236, v236, v240\n\t"
      "v_add_f32_e32 v229, v229, v233\n\t"
      "ds_write_b128 v245, v[224:227]\n\t"
      "v_mfma_f32_16x16x32_bf16 v[208:211], v[116:119], v[188:191], v[208:211]\n\t"
      "v_add_f32_e32 v237, v237, v241\n\t"
      "v_add_f32_e32 v228, v228, v248\n\t"
      "v_mfma_f32_16x16x32_bf16 v[212:215], v[8:11], v[160:163], 0\n\t"
      "v_add_f32_e32 v229, v229, v249\n\t"
      "v_add_f32_e32 v228, v228, v236\n\t"
      "v_add_f32_e32 v229, v229, v237\n\t"
      "v_mfma_f32_16x16x32_bf16 v[212:215], v[24:27], v[164:167], v[212:215]\n\t"
      "v_mul_f32_e32 v230, v228, v228\n\t"
      "v_mul_f32_e32 v231, v229, v229\n\t"
      "v_mfma_f32_16x16x32_bf16 v[212:215], v[40:43], v[168:171], v[212:215]\n\t"
      "v_fmaak_f32 v230, v252, v230, 0xc0135761\n\t"
      "v_fmaak_f32 v231, v252, v231, 0xc0135761\n\t"
      "v_mul_f32_e32 v230, v228, v230\n\t"
      "v_mfma_f32_16x16x32_bf16 v[212:215], v[56:59], v[172:175], v[212:215]\n\t"
      "v_mul_f32_e32 v231, v229, v231\n\t"
      "v_exp_f32_e32 v230, v230\n\t"
      "v_mfma_f32_16x16x32_bf16 v[212:215], v[72:75], v[176:179], v[212:215]\n\t"
      "v_exp_f32_e32 v231, v231\n\t"
      "v_add_f32_e32 v230, 1.0, v230\n\t"
      "v_add_f32_e32 v231, 1.0, v231\n\t"
      "v_mfma_f32_16x16x32_bf16 v[212:215], v[88:91], v[180:183], v[212:215]\n\t"
      "v_rcp_f32_e32 v230, v230\n\t"
      "v_rcp_f32_e32 v231, v231\n\t"
      "v_mfma_f32_16x16x32_bf16 v[212:215], v[104:107], v[184:187], v[212:215]\n\t"
      "v_mul_f32_e32 v228, v228, v230\n\t"
      "v_mul_f32_e32 v229, v229, v231\n\t"
      "v_cvt_pk_bf16_f32 v232, v228, v229\n\t"
      "ds_write_b128 v253, v[208:211] offset:1024\n\t"
      "v_mfma_f32_16x16x32_bf16 v[212:215], v[120:123], v[188:191], v[212:215]\n\t"
      "v_min_u32_e32 v254, -2, v232\n\t"
      "global_store_dword v247, v254, s[62:63] sc1\n\t"
      "s_add_u32 s62, s62, 57344\n\t"
      "s_addc_u32 s63, s63, 0\n\t"
      "v_mfma_f32_16x16x32_bf16 v[216:219], v[12:15], v[160:163], 0\n\t"
      "v_mfma_f32_16x16x32_bf16 v[216:219], v[28:31], v[164:167], v[216:219]\n\t"
      "v_mfma_f32_16x16x32_bf16 v[216:219], v[44:47], v[168:171], v[216:219]\n\t"
      "v_mfma_f32_16x16x32_bf16 v[216:219], v[60:63], v[172:175], v[216:219]\n\t"
      "v_mfma_f32_16x16x32_bf16 v[216:219], v[76:79], v[176:179], v[216:219]\n\t"
      "v_mfma_f32_16x16x32_bf16 v[216:219], v[92:95], v[180:183], v[216:219]\n\t"
      "v_mfma_f32_16x16x32_bf16 v[216:219], v[108:111], v[184:187], v[216:219]\n\t"
      "ds_write_b128 v245, v[212:215] offset:2048\n\t"
      "v_mfma_f32_16x16x32_bf16 v[216:219], v[124:127], v[188:191], v[216:219]\n\t"
      "s_nop 7\n\t"
      "ds_write_b128 v253, v[216:219] offset:3072\n\t"
      "s_waitcnt lgkmcnt(0)\n\t"
      "s_barrier\n\t"
      "ds_read_b64 v[228:229], v246\n\t"
      "ds_read_b64 v[230:231], v246 offset:4096\n\t"
      "ds_read_b64 v[232:233], v246 offset:8192\n\t"
      "ds_read_b64 v[234:235], v246 offset:12288\n\t"
      "ds_read_b64 v[236:237], v246 offset:16384\n\t"
      "ds_read_b64 v[238:239], v246 offset:20480\n\t"
      "ds_read_b64 v[240:241], v246 offset:24576\n\t"
      "ds_read_b64 v[242:243], v246 offset:28672\n\t"
      "s_waitcnt lgkmcnt(4)\n\t"
      "v_add_f32_e32 v228, v228, v230\n\t"
      "v_add_f32_e32 v232, v232, v234\n\t"
      "v_add_f32_e32 v229, v229, v231\n\t"
      "v_add_f32_e32 v233, v233, v235\n\t"
      "s_waitcnt lgkmcnt(0)\n\t"
      "v_add_f32_e32 v236, v236, v238\n\t"
      "v_add_f32_e32 v240, v240, v242\n\t"
      "v_add_f32_e32 v237, v237, v239\n\t"
      "v_add_f32_e32 v241, v241, v243\n\t"
      "v_add_f32_e32 v228, v228, v232\n\t"
      "v_add_f32_e32 v236, v236, v240\n\t"
      "v_add_f32_e32 v229, v229, v233\n\t"
      "v_add_f32_e32 v237, v237, v241\n\t"
      "v_add_f32_e32 v228, v228, v248\n\t"
      "v_add_f32_e32 v229, v229, v249\n\t"
      "v_add_f32_e32 v228, v228, v236\n\t"
      "v_add_f32_e32 v229, v229, v237\n\t"
      "v_mul_f32_e32 v230, v228, v228\n\t"
      "v_mul_f32_e32 v231, v229, v229\n\t"
      "v_fmaak_f32 v230, v252, v230, 0xc0135761\n\t"
      "v_fmaak_f32 v231, v252, v231, 0xc0135761\n\t"
      "v_mul_f32_e32 v230, v228, v230\n\t"
      "v_mul_f32_e32 v231, v229, v231\n\t"
      "v_exp_f32_e32 v230, v230\n\t"
      "v_exp_f32_e32 v231, v231\n\t"
      "v_add_f32_e32 v230, 1.0, v230\n\t"
      "v_add_f32_e32 v231, 1.0, v231\n\t"
      "v_rcp_f32_e32 v230, v230\n\t"
      "v_rcp_f32_e32 v231, v231\n\t"
      "v_mul_f32_e32 v228, v228, v230\n\t"
      "v_mul_f32_e32 v229, v229, v231\n\t"
      "v_cvt_pk_bf16_f32 v232, v228, v229\n\t"
      "v_min_u32_e32 v254, -2, v232\n\t"
      "global_store_dword v247, v254, s[62:63] sc1\n\t"
      "s_add_u32 s62, s62, 57344\n\t"
      "s_addc_u32 s63, s63, 0\n\t"
      "s_or_b32 s34, s2, s24\n\t"
      "s_cbranch_scc1 EPB_%=\n\t"
      "v_mov_b32_e32 v228, s22\n\t"
      "v_mov_b32_e32 v229, s23\n\t"
      "v_mov_b32_e32 v230, 0\n\t"
      "s_mov_b64 exec, 1\n\t"
      "global_store_dwordx2 v230, v[228:229], s[60:61] sc1\n\t"
      "s_mov_b64 exec, -1\n\t"
      "EPB_%=:\n\t"
      "s_endpgm\n\t"
      "LPATH_%=:\n\t"
      "s_add_u32 s22, s22, 1\n\t"
      "s_addc_u32 s23, s23, 0\n\t"
      "s_add_u32 s72, s26, 1\n\t"
      "s_and_b32 s72, s72, 6\n\t"
      "s_cselect_b32 s72, 0, 2\n\t"
      "s_or_b32 s27, s27, s72\n\t"
      "s_mul_i32 s72, s81, 172032\n\t"
      "s_add_u32 s72, s72, s37\n\t"
      "s_add_u32 s40, s6, s72\n\t"
      "s_addc_u32 s41, s7, 0\n\t"
      "s_add_u32 s81, s81, 1\n\t"
      "s_cmp_eq_u32 s81, 7\n\t"
      "s_cselect_b32 s81, 0, s81\n\t"
      "s_add_u32 m0, s39, 81920\n\t"
      "s_nop 0\n\t"
      "global_load_lds_dwordx4 v148, s[40:41]\n\t"
      "global_load_lds_dwordx4 v149, s[40:41] offset:1024\n\t"
      "global_load_lds_dwordx4 v150, s[40:41] offset:2048\n\t"
      "global_load_lds_dwordx4 v151, s[40:41] offset:3072\n\t"
      "s_mul_i32 s72, s81, 172032\n\t"
      "s_add_u32 s72, s72, s37\n\t"
      "s_add_u32 s40, s6, s72\n\t"
      "s_addc_u32 s41, s7, 0\n\t"
      "s_add_u32 s81, s81, 1\n\t"
      "s_cmp_eq_u32 s81, 7\n\t"
      "s_cselect_b32 s81, 0, s81\n\t"
      "s_add_u32 m0, s39, 98304\n\t"
      "s_nop 0\n\t"
      "global_load_lds_dwordx4 v148, s[40:41]\n\t"
      "global_load_lds_dwordx4 v149, s[40:41] offset:1024\n\t"
      "s_waitcnt vmcnt(14)\n\t"
      "s_barrier\n\t"
      "v_add_u32_e32 v172, 0x10000, v224\n\t"
      "v_add_u32_e32 v173, 0x10000, v225\n\t"
      "v_add_u32_e32 v174, 0x10000, v226\n\t"
      "v_add_u32_e32 v175, 0x10000, v227\n\t"
      "s_lshl_b32 s34, s25, 8\n\t"
      "s_add_u32 s34, s34, 4096\n\t"
      "v_and_b32_e32 v136, 1, v192\n\t"
      "v_lshl_add_u32 v136, v136, 7, s34\n\t"
      "s_lshl_b32 s34, s25, 7\n\t"
      "s_add_u32 s34, s34, 16384\n\t"
      "v_mov_b32_e32 v137, s34\n\t"
      "s_lshl_b32 s34, s2, 7\n\t"
      "s_add_u32 s34, s34, 32768\n\t"
      "v_mov_b32_e32 v143, s34\n\t"
      "s_bitcmp0_b32 s27, 1\n\t"
      "s_cbranch_scc1 SK6_%=\n\t"
      "s_lshr_b32 s73, s26, 2\n\t"
      "s_lshl1_add_u32 s73, s25, s73\n\t"
      "s_lshl_b32 s72, s73, 7\n\t"
      "s_add_u32 s72, s72, 4096\n\t"
      "v_mov_b32_e32 v130, s72\n\t"
      "s_lshl2_add_u32 s75, s73, s24\n\t"
      "v_mov_b32_e32 v128, s22\n\t"
      "v_mov_b32_e32 v129, s23\n\t"
      "v_lshrrev_b32_e32 v140, 1, v244\n\t"
      "s_lshl_b32 s72, s75, 9\n\t"
      "s_add_u32 s72, s72, 12582912\n\t"
      "v_mov_b32_e32 v132, -1\n\t"
      "v_mov_b32_e32 v133, -1\n\t"
      "v_mov_b32_e32 v134, -1\n\t"
      "v_mov_b32_e32 v135, -1\n\t"
      "s_add_u32 s42, s20, s72\n\t"
      "s_addc_u32 s43, s21, 0\n\t"
      "global_store_dwordx2 v140, v[132:133], s[42:43] sc1\n\t"
      "s_lshl_b32 s72, s75, 13\n\t"
      "s_add_u32 s72, s72, 4096\n\t"
      "s_add_u32 s42, s20, s72\n\t"
      "s_addc_u32 s43, s21, 0\n\t"
      "global_store_dwordx4 v244, v[132:135], s[42:43] offset:-4096 sc1\n\t"
      "global_store_dwordx4 v244, v[132:135], s[42:43] offset:-3072 sc1\n\t"
      "global_store_dwordx4 v244, v[132:135], s[42:43] offset:-2048 sc1\n\t"
      "SK6_%=:\n\t"
      "s_waitcnt vmcnt(10)\n\t"
      "s_barrier\n\t"
      "global_load_lds_dwordx4 v150, s[40:41] offset:2048\n\t"
      "global_load_lds_dwordx4 v151, s[40:41] offset:3072\n\t"
      "s_bitcmp0_b32 s27, 1\n\t"
      "s_cbranch_scc1 SK7_%=\n\t"
      "global_store_dwordx4 v244, v[132:135], s[42:43] offset:-1024 sc1\n\t"
      "global_store_dwordx4 v244, v[132:135], s[42:43] sc1\n\t"
      "global_store_dwordx4 v244, v[132:135], s[42:43] offset:1024 sc1\n\t"
      "SK7_%=:\n\t"
      "s_waitcnt vmcnt(8)\n\t"
      "s_barrier\n\t"
      "s_mul_i32 s72, s81, 172032\n\t"
      "s_add_u32 s72, s72, s37\n\t"
      "s_add_u32 s40, s6, s72\n\t"
      "s_addc_u32 s41, s7, 0\n\t"
      "s_add_u32 s81, s81, 1\n\t"
      "s_cmp_eq_u32 s81, 7\n\t"
      "s_cselect_b32 s81, 0, s81\n\t"
      "s_add_u32 m0, s39, 114688\n\t"
      "s_nop 0\n\t"
      "global_load_lds_dwordx4 v148, s[40:41]\n\t"
      "global_load_lds_dwordx4 v149, s[40:41] offset:1024\n\t"
      "global_load_lds_dwordx4 v150, s[40:41] offset:2048\n\t"
      "global_load_lds_dwordx4 v151, s[40:41] offset:3072\n\t"
      "s_bitcmp0_b32 s27, 1\n\t"
      "s_cbranch_scc1 SK8_%=\n\t"
      "global_store_dwordx4 v244, v[132:135], s[42:43] offset:2048 sc1\n\t"
      "global_store_dwordx4 v244, v[132:135], s[42:43] offset:3072 sc1\n\t"
      "SK8_%=:\n\t"
      "s_waitcnt vmcnt(8)\n\t"
      "s_barrier\n\t"
      "s_mul_i32 s72, s81, 172032\n\t"
      "s_add_u32 s72, s72, s37\n\t"
      "s_add_u32 s40, s6, s72\n\t"
      "s_addc_u32 s41, s7, 0\n\t"
      "s_add_u32 s81, s81, 1\n\t"
      "s_cmp_eq_u32 s81, 7\n\t"
      "s_cselect_b32 s81, 0, s81\n\t"
      "s_add_u32 m0, s39, 131072\n\t"
      "s_nop 0\n\t"
      "global_load_lds_dwordx4 v148, s[40:41]\n\t"
      "global_load_lds_dwordx4 v149, s[40:41] offset:1024\n\t"
      "global_load_lds_dwordx4 v150, s[40:41] offset:2048\n\t"
      "global_load_lds_dwordx4 v151, s[40:41] offset:3072\n\t"
      "s_waitcnt vmcnt(8)\n\t"
      "s_bitcmp0_b32 s27, 1\n\t"
      "s_cbranch_scc1 SK9_%=\n\t"
      "s_waitcnt vmcnt(4)\n\t"
      "SK9_%=:\n\t"
      "s_barrier\n\t"
      "s_mul_i32 s72, s81, 172032\n\t"
      "s_add_u32 s72, s72, s37\n\t"
      "s_add_u32 s40, s6, s72\n\t"
      "s_addc_u32 s41, s7, 0\n\t"
      "s_add_u32 s81, s81, 1\n\t"
      "s_cmp_eq_u32 s81, 7\n\t"
      "s_cselect_b32 s81, 0, s81\n\t"
      "s_add_u32 m0, s39, 147456\n\t"
      "s_nop 0\n\t"
      "global_load_lds_dwordx4 v148, s[40:41]\n\t"
      "global_load_lds_dwordx4 v149, s[40:41] offset:1024\n\t"
      "global_load_lds_dwordx4 v150, s[40:41] offset:2048\n\t"
      "global_load_lds_dwordx4 v151, s[40:41] offset:3072\n\t"
      "s_bitcmp0_b32 s27, 1\n\t"
      "s_cbranch_scc1 SK10_%=\n\t"
      "s_cmp_eq_u32 s24, 0\n\t"
      "s_cselect_b64 exec, 1, 0\n\t"
      "global_store_dwordx2 v130, v[128:129], s[60:61] sc1\n\t"
      "s_mov_b64 exec, -1\n\t"
      "s_mov_b32 m0, 64512\n\t"
      "s_mul_i32 s72, s75, 13\n\t"
      "v_min_u32_e32 v141, 12, v192\n\t"
      "v_add_u32_e32 v141, s72, v141\n\t"
      "v_min_u32_e32 v141, 3135, v141\n\t"
      "v_lshlrev_b32_e32 v141, 7, v141\n\t"
      "global_load_lds_dword v141, s[8:9]\n\t"
      "SK10_%=:\n\t"
      "s_waitcnt vmcnt(8)\n\t"
      "s_barrier\n\t"
      "s_mul_i32 s72, s82, 172032\n\t"
      "s_add_u32 s72, s72, s37\n\t"
      "s_add_u32 s72, s72, 1792\n\t"
      "s_add_u32 s40, s6, s72\n\t"
      "s_addc_u32 s41, s7, 0\n\t"
      "s_add_u32 s82, s82, 1\n\t"
      "s_cmp_eq_u32 s82, 7\n\t"
      "s_cselect_b32 s82, 0, s82\n\t"
      "s_add_u32 m0, s39, 65536\n\t"
      "s_nop 0\n\t"
      "global_load_lds_dwordx4 v148, s[40:41]\n\t"
      "global_load_lds_dwordx4 v152, s[40:41] offset:1024\n\t"
      "s_mul_i32 s72, s82, 172032\n\t"
      "s_add_u32 s72, s72, s37\n\t"
      "s_add_u32 s72, s72, 1792\n\t"
      "s_add_u32 s40, s6, s72\n\t"
      "s_addc_u32 s41, s7, 0\n\t"
      "s_add_u32 s82, s82, 1\n\t"
      "s_cmp_eq_u32 s82, 7\n\t"
      "s_cselect_b32 s82, 0, s82\n\t"
      "s_add_u32 m0, s39, 67584\n\t"
      "s_nop 0\n\t"
      "global_load_lds_dwordx4 v148, s[40:41]\n\t"
      "global_load_lds_dwordx4 v152, s[40:41] offset:1024\n\t"
      "s_waitcnt vmcnt(8)\n\t"
      "s_barrier\n\t"
      "s_mul_i32 s72, s82, 172032\n\t"
      "s_add_u32 s72, s72, s37\n\t"
      "s_add_u32 s72, s72, 1792\n\t"
      "s_add_u32 s40, s6, s72\n\t"
      "s_addc_u32 s41, s7, 0\n\t"
      "s_add_u32 s82, s82, 1\n\t"
      "s_cmp_eq_u32 s82, 7\n\t"
      "s_cselect_b32 s82, 0, s82\n\t"
      "s_add_u32 m0, s39, 81920\n\t"
      "s_nop 0\n\t"
      "global_load_lds_dwordx4 v148, s[40:41]\n\t"
      "global_load_lds_dwordx4 v152, s[40:41] offset:1024\n\t"
      "s_mul_i32 s72, s82, 172032\n\t"
      "s_add_u32 s72, s72, s37\n\t"
      "s_add_u32 s72, s72, 1792\n\t"
      "s_add_u32 s40, s6, s72\n\t"
      "s_addc_u32 s41, s7, 0\n\t"
      "s_add_u32 s82, s82, 1\n\t"
      "s_cmp_eq_u32 s82, 7\n\t"
      "s_cselect_b32 s82, 0, s82\n\t"
      "s_add_u32 m0, s39, 83968\n\t"
      "s_nop 0\n\t"
      "global_load_lds_dwordx4 v148, s[40:41]\n\t"
      "global_load_lds_dwordx4 v152, s[40:41] offset:1024\n\t"
      "s_waitcnt vmcnt(8)\n\t"
      "s_barrier\n\t"
      "s_cmp_lg_u32 s24, 3\n\t"
      "s_cbranch_scc1 SK11_%=\n\t"
      "global_load_dwordx2 v[138:139], v136, s[60:61] sc1\n\t"
      "SK11_%=:\n\t"
      "s_mul_i32 s72, s82, 172032\n\t"
      "s_add_u32 s72, s72, s37\n\t"
      "s_add_u32 s72, s72, 1792\n\t"
      "s_add_u32 s40, s6, s72\n\t"
      "s_addc_u32 s41, s7, 0\n\t"
      "s_add_u32 s82, s82, 1\n\t"
      "s_cmp_eq_u32 s82, 7\n\t"
      "s_cselect_b32 s82, 0, s82\n\t"
      "s_lshr_b32 s72, s39, 1\n\t"
      "s_add_u32 m0, s72, 98304\n\t"
      "s_nop 0\n\t"
      "global_load_lds_dwordx4 v148, s[40:41]\n\t"
      "global_load_lds_dwordx4 v152, s[40:41] offset:1024\n\t"
      "global_load_dword v249, v248, s[12:13]\n\t"
      "s_lshl2_add_u32 s72, s31, s24\n\t"
      "s_and_b32 s73, s72, 0x70\n\t"
      "s_add_u32 s72, s72, s73\n\t"
      "s_mov_b32 m0, 64512\n\t"
      "s_mul_i32 s72, s72, 28672\n\t"
      "s_and_b32 s73, s2, 7\n\t"
      "s_mul_i32 s73, s73, 448\n\t"
      "s_and_b32 s73, s73, 0xffffff80\n\t"
      "s_add_u32 s72, s72, s73\n\t"
      "v_bfe_u32 v141, v192, 2, 3\n\t"
      "s_cmp_ge_u32 s31, 28\n\t"
      "s_cselect_b32 s73, 32, 0\n\t"
      "v_and_b32_e32 v142, s73, v192\n\t"
      "v_lshl_or_b32 v141, v142, 2, v141\n\t"
      "v_mul_u32_u24_e32 v141, 3584, v141\n\t"
      "v_lshl_add_u32 v141, v196, 7, v141\n\t"
      "v_add_u32_e32 v141, s72, v141\n\t"
      "global_load_lds_dword v141, s[10:11]\n\t"
      "s_waitcnt vmcnt(8)\n\t"
      "s_barrier\n\t"
      "s_mul_i32 s72, s24, 14336\n\t"
      "s_add_u32 s40, s56, s72\n\t"
      "s_addc_u32 s41, s57, 0\n\t"
      "s_lshr_b32 s72, s39, 1\n\t"
      "s_add_u32 m0, s72, 106496\n\t"
      "s_nop 0\n\t"
      "global_load_lds_dwordx4 v154, s[40:41]\n\t"
      "global_load_lds_dwordx4 v155, s[40:41] offset:1024\n\t"
      "s_waitcnt vmcnt(6)\n\t"
      "s_barrier\n\t"
      "s_mul_i32 s72, s24, 28672\n\t"
      "s_add_u32 s72, s72, 57344\n\t"
      "s_add_u32 s40, s56, s72\n\t"
      "s_addc_u32 s41, s57, 0\n\t"
      "s_lshr_b32 s72, s39, 0\n\t"
      "s_add_u32 m0, s72, 114688\n\t"
      "s_nop 0\n\t"
      "global_load_lds_dwordx4 v154, s[40:41]\n\t"
      "global_load_lds_dwordx4 v155, s[40:41] offset:1024\n\t"
      "global_load_lds_dwordx4 v156, s[40:41] offset:2048\n\t"
      "global_load_lds_dwordx4 v157, s[40:41] offset:3072\n\t"
      "s_waitcnt vmcnt(8)\n\t"
      "s_cmp_lg_u32 s24, 3\n\t"
      "s_cbranch_scc1 LF1B_%=\n\t"
      "v_cmp_ne_u64_e32 vcc, s[22:23], v[138:139]\n\t"
      "s_cbranch_vccnz LF1R_%=\n\t"
      "LF1B_%=:\n\t"
      "s_barrier\n\t"
      "s_mul_i32 s72, s24, 28672\n\t"
      "s_add_u32 s72, s72, 172032\n\t"
      "s_add_u32 s40, s56, s72\n\t"
      "s_addc_u32 s41, s57, 0\n\t"
      "s_lshr_b32 s72, s39, 0\n\t"
      "s_add_u32 m0, s72, 131072\n\t"
      "s_nop 0\n\t"
      "global_load_lds_dwordx4 v154, s[40:41]\n\t"
      "global_load_lds_dwordx4 v155, s[40:41] offset:1024\n\t"
      "global_load_lds_dwordx4 v156, s[40:41] offset:2048\n\t"
      "global_load_lds_dwordx4 v157, s[40:41] offset:3072\n\t"
      "s_mul_i32 s72, s24, 28672\n\t"
      "s_add_u32 s72, s72, 286720\n\t"
      "s_add_u32 s40, s56, s72\n\t"
      "s_addc_u32 s41, s57, 0\n\t"
      "s_lshr_b32 s72, s39, 0\n\t"
      "s_add_u32 m0, s72, 147456\n\t"
      "s_nop 0\n\t"
      "global_load_lds_dwordx4 v154, s[40:41]\n\t"
      "global_load_lds_dwordx4 v155, s[40:41] offset:1024\n\t"
      "global_load_lds_dwordx4 v156, s[40:41] offset:2048\n\t"
      "global_load_lds_dwordx4 v157, s[40:41] offset:3072\n\t"
      "s_barrier\n\t"
      "s_cmp_eq_u32 s26, 7\n\t"
      "s_cbranch_scc1 SPARE_%=\n\t"
      "s_lshl_b32 s72, s24, 10\n\t"
      "s_add_u32 s72, s72, 98304\n\t"
      "v_xor_b32_e32 v203, v197, v198\n\t"
      "v_lshl_add_u32 v203, v203, 4, s72\n\t"
      "v_lshl_add_u32 v203, v197, 7, v203\n\t"
      "ds_write_b128 v203, v[204:207]\n\t"
      "s_waitcnt lgkmcnt(0)\n\t"
      "s_bitcmp0_b32 s27, 0\n\t"
      "s_cbranch_scc1 L4_%=\n\t"
      "s_add_u32 s72, s76, 0\n\t"
      "s_add_u32 s40, s52, s72\n\t"
      "s_addc_u32 s41, s53, 0\n\t"
      "s_add_u32 m0, s39, 65536\n\t"
      "s_nop 0\n\t"
      "global_load_lds_dwordx4 v144, s[40:41]\n\t"
      "global_load_lds_dwordx4 v145, s[40:41] offset:1024\n\t"
      "global_load_lds_dwordx4 v146, s[40:41] offset:2048\n\t"
      "global_load_lds_dwordx4 v147, s[40:41] offset:3072\n\t"
      "s_add_u32 s72, s76, 229376\n\t"
      "s_add_u32 s40, s52, s72\n\t"
      "s_addc_u32 s41, s53, 0\n\t"
      "s_add_u32 m0, s39, 81920\n\t"
      "s_nop 0\n\t"
      "global_load_lds_dwordx4 v144, s[40:41]\n\t"
      "global_load_lds_dwordx4 v145, s[40:41] offset:1024\n\t"
      "global_load_lds_dwordx4 v146, s[40:41] offset:2048\n\t"
      "global_load_lds_dwordx4 v147, s[40:41] offset:3072\n\t"
      "s_waitcnt vmcnt(8)\n\t"
      "s_barrier\n\t"
      "s_barrier\n\t"
      "s_add_u32 s72, s76, 3670016\n\t"
      "s_add_u32 s40, s52, s72\n\t"
      "s_addc_u32 s41, s53, 0\n\t"
      "s_add_u32 m0, s39, 16384\n\t"
      "s_nop 0\n\t"
      "global_load_lds_dwordx4 v144, s[40:41]\n\t"
      "global_load_lds_dwordx4 v145, s[40:41] offset:1024\n\t"
      "global_load_lds_dwordx4 v146, s[40:41] offset:2048\n\t"
      "global_load_lds_dwordx4 v147, s[40:41] offset:3072\n\t"
      "s_add_u32 s72, s76, 3899392\n\t"
      "s_add_u32 s40, s52, s72\n\t"
      "s_addc_u32 s41, s53, 0\n\t"
      "s_add_u32 m0, s39, 49152\n\t"
      "s_nop 0\n\t"
      "global_load_lds_dwordx4 v144, s[40:41]\n\t"
      "global_load_lds_dwordx4 v145, s[40:41] offset:1024\n\t"
      "global_load_lds_dwordx4 v146, s[40:41] offset:2048\n\t"
      "global_load_lds_dwordx4 v147, s[40:41] offset:3072\n\t"
      "s_waitcnt vmcnt(12)\n\t"
      "ds_read_b64_tr_b16 v[0:1], v172\n\t"
      "ds_read_b64_tr_b16 v[2:3], v172 offset:512\n\t"
      "ds_read_b64_tr_b16 v[4:5], v173\n\t"
      "ds_read_b64_tr_b16 v[6:7], v173 offset:512\n\t"
      "ds_read_b64_tr_b16 v[8:9], v174\n\t"
      "ds_read_b64_tr_b16 v[10:11], v174 offset:512\n\t"
      "ds_read_b64_tr_b16 v[12:13], v175\n\t"
      "ds_read_b64_tr_b16 v[14:15], v175 offset:512\n\t"
      "s_waitcnt lgkmcnt(0)\n\t"
      "s_add_u32 s72, s77, 0\n\t"
      "s_add_u32 s40, s52, s72\n\t"
      "s_addc_u32 s41, s53, 0\n\t"
      "s_add_u32 m0, s39, 65536\n\t"
      "s_nop 0\n\t"
      "global_load_lds_dwordx4 v144, s[40:41]\n\t"
      "global_load_lds_dwordx4 v145, s[40:41] offset:1024\n\t"
      "global_load_lds_dwordx4 v146, s[40:41] offset:2048\n\t"
      "global_load_lds_dwordx4 v147, s[40:41] offset:3072\n\t"
      "s_waitcnt vmcnt(12)\n\t"
      "ds_read_b64_tr_b16 v[16:17], v172 offset:16384\n\t"
      "ds_read_b64_tr_b16 v[18:19], v172 offset:16896\n\t"
      "ds_read_b64_tr_b16 v[20:21], v173 offset:16384\n\t"
      "ds_read_b64_tr_b16 v[22:23], v173 offset:16896\n\t"
      "ds_read_b64_tr_b16 v[24:25], v174 offset:16384\n\t"
      "ds_read_b64_tr_b16 v[26:27], v174 offset:16896\n\t"
      "ds_read_b64_tr_b16 v[28:29], v175 offset:16384\n\t"
      "ds_read_b64_tr_b16 v[30:31], v175 offset:16896\n\t"
      "s_waitcnt lgkmcnt(0)\n\t"
      "s_add_u32 s72, s77, 229376\n\t"
      "s_add_u32 s40, s52, s72\n\t"
      "s_addc_u32 s41, s53, 0\n\t"
      "s_add_u32 m0, s39, 81920\n\t"
      "s_nop 0\n\t"
      "global_load_lds_dwordx4 v144, s[40:41]\n\t"
      "global_load_lds_dwordx4 v145, s[40:41] offset:1024\n\t"
      "global_load_lds_dwordx4 v146, s[40:41] offset:2048\n\t"
      "global_load_lds_dwordx4 v147, s[40:41] offset:3072\n\t"
      "s_add_u32 s72, s77, 3670016\n\t"
      "s_add_u32 s40, s52, s72\n\t"
      "s_addc_u32 s41, s53, 0\n\t"
      "s_add_u32 m0, s39, 0\n\t"
      "s_nop 0\n\t"
      "global_load_lds_dwordx4 v144, s[40:41]\n\t"
      "global_load_lds_dwordx4 v145, s[40:41] offset:1024\n\t"
      "global_load_lds_dwordx4 v146, s[40:41] offset:2048\n\t"
      "global_load_lds_dwordx4 v147, s[40:41] offset:3072\n\t"
      "s_add_u32 s72, s77, 3899392\n\t"
      "s_add_u32 s40, s52, s72\n\t"
      "s_addc_u32 s41, s53, 0\n\t"
      "s_add_u32 m0, s39, 32768\n\t"
      "s_nop 0\n\t"
      "global_load_lds_dwordx4 v144, s[40:41]\n\t"
      "global_load_lds_dwordx4 v145, s[40:41] offset:1024\n\t"
      "global_load_lds_dwordx4 v146, s[40:41] offset:2048\n\t"
      "global_load_lds_dwordx4 v147, s[40:41] offset:3072\n\t"
      "s_waitcnt vmcnt(12)\n\t"
      "ds_read_b64_tr_b16 v[32:33], v172\n\t"
      "ds_read_b64_tr_b16 v[34:35], v172 offset:512\n\t"
      "ds_read_b64_tr_b16 v[36:37], v173\n\t"
      "ds_read_b64_tr_b16 v[38:39], v173 offset:512\n\t"
      "ds_read_b64_tr_b16 v[40:41], v174\n\t"
      "ds_read_b64_tr_b16 v[42:43], v174 offset:512\n\t"
      "ds_read_b64_tr_b16 v[44:45], v175\n\t"
      "ds_read_b64_tr_b16 v[46:47], v175 offset:512\n\t"
      "s_waitcnt lgkmcnt(0)\n\t"
      "s_add_u32 s72, s78, 0\n\t"
      "s_add_u32 s40, s52, s72\n\t"
      "s_addc_u32 s41, s53, 0\n\t"
      "s_add_u32 m0, s39, 65536\n\t"
      "s_nop 0\n\t"
      "global_load_lds_dwordx4 v144, s[40:41]\n\t"
      "global_load_lds_dwordx4 v145, s[40:41] offset:1024\n\t"
      "global_load_lds_dwordx4 v146, s[40:41] offset:2048\n\t"
      "global_load_lds_dwordx4 v147, s[40:41] offset:3072\n\t"
      "s_waitcnt vmcnt(12)\n\t"
      "ds_read_b64_tr_b16 v[48:49], v172 offset:16384\n\t"
      "ds_read_b64_tr_b16 v[50:51], v172 offset:16896\n\t"
      "ds_read_b64_tr_b16 v[52:53], v173 offset:16384\n\t"
      "ds_read_b64_tr_b16 v[54:55], v173 offset:16896\n\t"
      "ds_read_b64_tr_b16 v[56:57], v174 offset:16384\n\t"
      "ds_read_b64_tr_b16 v[58:59], v174 offset:16896\n\t"
      "ds_read_b64_tr_b16 v[60:61], v175 offset:16384\n\t"
      "ds_read_b64_tr_b16 v[62:63], v175 offset:16896\n\t"
      "s_waitcnt lgkmcnt(0)\n\t"
      "s_add_u32 s72, s78, 229376\n\t"
      "s_add_u32 s40, s52, s72\n\t"
      "s_addc_u32 s41, s53, 0\n\t"
      "s_add_u32 m0, s39, 81920\n\t"
      "s_nop 0\n\t"
      "global_load_lds_dwordx4 v144, s[40:41]\n\t"
      "global_load_lds_dwordx4 v145, s[40:41] offset:1024\n\t"
      "global_load_lds_dwordx4 v146, s[40:41] offset:2048\n\t"
      "global_load_lds_dwordx4 v147, s[40:41] offset:3072\n\t"
      "s_waitcnt vmcnt(8)\n\t"
      "s_barrier\n\t"
      "s_cmp_lg_u32 s24, 3\n\t"
      "s_cbranch_scc1 SK13_%=\n\t"
      "global_load_dwordx2 v[138:139], v137, s[60:61] sc1\n\t"
      "SK13_%=:\n\t"
      "s_add_u32 s72, s79, 0\n\t"
      "s_add_u32 s40, s52, s72\n\t"
      "s_addc_u32 s41, s53, 0\n\t"
      "s_add_u32 m0, s39, 98304\n\t"
      "s_nop 0\n\t"
      "global_load_lds_dwordx4 v144, s[40:41]\n\t"
      "global_load_lds_dwordx4 v145, s[40:41] offset:1024\n\t"
      "global_load_lds_dwordx4 v146, s[40:41] offset:2048\n\t"
      "global_load_lds_dwordx4 v147, s[40:41] offset:3072\n\t"
      "s_add_u32 s72, s79, 229376\n\t"
      "s_add_u32 s40, s52, s72\n\t"
      "s_addc_u32 s41, s53, 0\n\t"
      "s_add_u32 m0, s39, 114688\n\t"
      "s_nop 0\n\t"
      "global_load_lds_dwordx4 v144, s[40:41]\n\t"
      "global_load_lds_dwordx4 v145, s[40:41] offset:1024\n\t"
      "global_load_lds_dwordx4 v146, s[40:41] offset:2048\n\t"
      "global_load_lds_dwordx4 v147, s[40:41] offset:3072\n\t"
      "s_cmp_lg_u32 s24, 3\n\t"
      "s_cbranch_scc1 LF2B_%=\n\t"
      "s_waitcnt vmcnt(8)\n\t"
      "v_cmp_ne_u64_e32 vcc, s[22:23], v[138:139]\n\t"
      "s_cbranch_vccnz LF2R_%=\n\t"
      "LF2B_%=:\n\t"
      "s_barrier\n\t"
      "s_add_u32 s72, s78, 3670016\n\t"
      "s_add_u32 s40, s52, s72\n\t"
      "s_addc_u32 s41, s53, 0\n\t"
      "s_add_u32 m0, s39, 16384\n\t"
      "s_nop 0\n\t"
      "global_load_lds_dwordx4 v144, s[40:41]\n\t"
      "global_load_lds_dwordx4 v145, s[40:41] offset:1024\n\t"
      "global_load_lds_dwordx4 v146, s[40:41] offset:2048\n\t"
      "global_load_lds_dwordx4 v147, s[40:41] offset:3072\n\t"
      "s_add_u32 s72, s78, 3899392\n\t"
      "s_add_u32 s40, s52, s72\n\t"
      "s_addc_u32 s41, s53, 0\n\t"
      "s_add_u32 m0, s39, 49152\n\t"
      "s_nop 0\n\t"
      "global_load_lds_dwordx4 v144, s[40:41]\n\t"
      "global_load_lds_dwordx4 v145, s[40:41] offset:1024\n\t"
      "global_load_lds_dwordx4 v146, s[40:41] offset:2048\n\t"
      "global_load_lds_dwordx4 v147, s[40:41] offset:3072\n\t"
      "s_add_u32 s72, s79, 3670016\n\t"
      "s_add_u32 s40, s52, s72\n\t"
      "s_addc_u32 s41, s53, 0\n\t"
      "s_add_u32 m0, s39, 0\n\t"
      "s_nop 0\n\t"
      "global_load_lds_dwordx4 v144, s[40:41]\n\t"
      "global_load_lds_dwordx4 v145, s[40:41] offset:1024\n\t"
      "global_load_lds_dwordx4 v146, s[40:41] offset:2048\n\t"
      "global_load_lds_dwordx4 v147, s[40:41] offset:3072\n\t"
      "s_add_u32 s72, s79, 3899392\n\t"
      "s_add_u32 s40, s52, s72\n\t"
      "s_addc_u32 s41, s53, 0\n\t"
      "s_add_u32 m0, s39, 32768\n\t"
      "s_nop 0\n\t"
      "global_load_lds_dwordx4 v144, s[40:41]\n\t"
      "global_load_lds_dwordx4 v145, s[40:41] offset:1024\n\t"
      "global_load_lds_dwordx4 v146, s[40:41] offset:2048\n\t"
      "global_load_lds_dwordx4 v147, s[40:41] offset:3072\n\t"
      "s_cmp_lg_u32 s24, 3\n\t"
      "s_cbranch_scc1 SK14_%=\n\t"
      "global_load_dwordx2 v[138:139], v143, s[60:61] sc1\n\t"
      "SK14_%=:\n\t"
      "s_waitcnt vmcnt(28)\n\t"
      "ds_read_b64_tr_b16 v[64:65], v172\n\t"
      "ds_read_b64_tr_b16 v[66:67], v172 offset:512\n\t"
      "ds_read_b64_tr_b16 v[68:69], v173\n\t"
      "ds_read_b64_tr_b16 v[70:71], v173 offset:512\n\t"
      "ds_read_b64_tr_b16 v[72:73], v174\n\t"
      "ds_read_b64_tr_b16 v[74:75], v174 offset:512\n\t"
      "ds_read_b64_tr_b16 v[76:77], v175\n\t"
      "ds_read_b64_tr_b16 v[78:79], v175 offset:512\n\t"
      "s_waitcnt vmcnt(24)\n\t"
      "ds_read_b64_tr_b16 v[80:81], v172 offset:16384\n\t"
      "ds_read_b64_tr_b16 v[82:83], v172 offset:16896\n\t"
      "ds_read_b64_tr_b16 v[84:85], v173 offset:16384\n\t"
      "ds_read_b64_tr_b16 v[86:87], v173 offset:16896\n\t"
      "ds_read_b64_tr_b16 v[88:89], v174 offset:16384\n\t"
      "ds_read_b64_tr_b16 v[90:91], v174 offset:16896\n\t"
      "ds_read_b64_tr_b16 v[92:93], v175 offset:16384\n\t"
      "ds_read_b64_tr_b16 v[94:95], v175 offset:16896\n\t"
      "s_waitcnt vmcnt(20)\n\t"
      "ds_read_b64_tr_b16 v[96:97], v172 offset:32768\n\t"
      "ds_read_b64_tr_b16 v[98:99], v172 offset:33280\n\t"
      "ds_read_b64_tr_b16 v[100:101], v173 offset:32768\n\t"
      "ds_read_b64_tr_b16 v[102:103], v173 offset:33280\n\t"
      "ds_read_b64_tr_b16 v[104:105], v174 offset:32768\n\t"
      "ds_read_b64_tr_b16 v[106:107], v174 offset:33280\n\t"
      "ds_read_b64_tr_b16 v[108:109], v175 offset:32768\n\t"
      "ds_read_b64_tr_b16 v[110:111], v175 offset:33280\n\t"
      "s_waitcnt vmcnt(16)\n\t"
      "ds_read_b64_tr_b16 v[112:113], v172 offset:49152\n\t"
      "ds_read_b64_tr_b16 v[114:115], v172 offset:49664\n\t"
      "ds_read_b64_tr_b16 v[116:117], v173 offset:49152\n\t"
      "ds_read_b64_tr_b16 v[118:119], v173 offset:49664\n\t"
      "ds_read_b64_tr_b16 v[120:121], v174 offset:49152\n\t"
      "ds_read_b64_tr_b16 v[122:123], v174 offset:49664\n\t"
      "ds_read_b64_tr_b16 v[124:125], v175 offset:49152\n\t"
      "ds_read_b64_tr_b16 v[126:127], v175 offset:49664\n\t"
      "s_waitcnt vmcnt(0)\n\t"
      "s_cmp_lg_u32 s24, 3\n\t"
      "s_cbranch_scc1 LF3B_%=\n\t"
      "s_waitcnt vmcnt(0)\n\t"
      "v_cmp_ne_u64_e32 vcc, s[22:23], v[138:139]\n\t"
      "s_cbranch_vccnz LF3R_%=\n\t"
      "LF3B_%=:\n\t"
      "s_waitcnt lgkmcnt(0)\n\t"
      "s_barrier\n\t"
      "s_mul_i32 s34, s32, 196608\n\t"
      "s_mul_i32 s35, s24, 8192\n\t"
      "s_add_u32 s34, s34, s35\n\t"
      "s_add_u32 s34, s34, 1048576\n\t"
      "s_add_u32 s64, s48, s34\n\t"
      "s_addc_u32 s65, s49, 0\n\t"
      "v_add_u32_e32 v250, 4096, v244\n\t"
      "global_load_dwordx4 v[128:131], v250, s[64:65] offset:-4096 sc1\n\t"
      "global_load_dwordx4 v[132:135], v250, s[64:65] offset:-3072 sc1\n\t"
      "global_load_dwordx4 v[136:139], v250, s[64:65] offset:-2048 sc1\n\t"
      "global_load_dwordx4 v[140:143], v250, s[64:65] offset:-1024 sc1\n\t"
      "global_load_dwordx4 v[144:147], v250, s[64:65] sc1\n\t"
      "global_load_dwordx4 v[148:151], v250, s[64:65] offset:1024 sc1\n\t"
      "global_load_dwordx4 v[152:155], v250, s[64:65] offset:2048 sc1\n\t"
      "global_load_dwordx4 v[156:159], v250, s[64:65] offset:3072 sc1\n\t"
      "s_waitcnt vmcnt(0) lgkmcnt(0)\n\t"
      "s_branch GST_%=\n\t"
      "G4C_%=:\n\t"
      "global_store_dwordx4 v241, v[208:211], s[20:21] offset:-2048 sc1\n\t"
      "global_store_dwordx4 v241, v[212:215], s[20:21] offset:2048 sc1\n\t"
      "s_setprio 0\n\t"
      "G4_%=:\n\t"
      "s_mul_i32 s34, s32, 401408\n\t"
      "s_mul_i32 s35, s24, 7168\n\t"
      "s_add_u32 s34, s34, s35\n\t"
      "s_add_u32 s34, s34, 1835008\n\t"
      "s_add_u32 s64, s48, s34\n\t"
      "s_addc_u32 s65, s49, 0\n\t"
      "s_and_b32 s34, s30, 3\n\t"
      "s_lshl_b32 s34, s34, 11\n\t"
      "s_add_u32 s40, s34, 0\n\t"
      "s_sub_u32 s35, s40, 7168\n\t"
      "s_min_u32 s40, s40, s35\n\t"
      "s_add_u32 s41, s34, 1024\n\t"
      "s_sub_u32 s35, s41, 7168\n\t"
      "s_min_u32 s41, s41, s35\n\t"
      "s_add_u32 s42, s34, 2048\n\t"
      "s_sub_u32 s35, s42, 7168\n\t"
      "s_min_u32 s42, s42, s35\n\t"
      "s_add_u32 s43, s34, 3072\n\t"
      "s_sub_u32 s35, s43, 7168\n\t"
      "s_min_u32 s43, s43, s35\n\t"
      "s_add_u32 s44, s34, 4096\n\t"
      "s_sub_u32 s35, s44, 7168\n\t"
      "s_min_u32 s44, s44, s35\n\t"
      "s_add_u32 s45, s34, 5120\n\t"
      "s_sub_u32 s35, s45, 7168\n\t"
      "s_min_u32 s45, s45, s35\n\t"
      "s_add_u32 s46, s34, 6144\n\t"
      "s_sub_u32 s35, s46, 7168\n\t"
      "s_min_u32 s46, s46, s35\n\t"
      "s_add_u32 s66, s64, 57344\n\t"
      "s_addc_u32 s67, s65, 0\n\t"
      "s_add_u32 s68, s66, 57344\n\t"
      "s_addc_u32 s69, s67, 0\n\t"
      "s_add_u32 s70, s68, 57344\n\t"
      "s_addc_u32 s71, s69, 0\n\t"
      "s_add_u32 s72, s70, 57344\n\t"
      "s_addc_u32 s73, s71, 0\n\t"
      "s_add_u32 s74, s72, 57344\n\t"
      "s_addc_u32 s75, s73, 0\n\t"
      "s_add_u32 s76, s74, 57344\n\t"
      "s_addc_u32 s77, s75, 0\n\t"
      "v_lshrrev_b32_e32 v240, 7, v244\n\t"
      "v_and_b32_e32 v241, 0x70, v244\n\t"
      "v_and_b32_e32 v242, 6, v240\n\t"
      "v_lshlrev_b32_e32 v242, 4, v242\n\t"
      "v_xor_b32_e32 v242, v242, v241\n\t"
      "v_lshl_or_b32 v228, v240, 12, v242\n\t"
      "s_mul_i32 s34, s24, 917504\n\t"
      "s_lshl_b32 s35, s29, 7\n\t"
      "s_add_u32 s34, s34, s35\n\t"
      "s_lshl_b32 s39, s24, 12\n\t"
      "s_add_u32 s54, s14, s34\n\t"
      "s_addc_u32 s55, s15, 0\n\t"
      "v_add_u32_e32 v229, 31744, v228\n\t"
      "v_add_u32_e32 v230, 63488, v228\n\t"
      "v_add_u32_e32 v231, 95232, v228\n\t"
      "s_lshl_b32 s37, s40, 7\n\t"
      "s_add_u32 s58, s54, s37\n\t"
      "s_addc_u32 s59, s55, 0\n\t"
      "s_add_u32 m0, s39, 0\n\t"
      "s_nop 0\n\t"
      "global_load_lds_dwordx4 v228, s[58:59]\n\t"
      "global_load_lds_dwordx4 v229, s[58:59] offset:1024\n\t"
      "global_load_lds_dwordx4 v230, s[58:59] offset:2048\n\t"
      "global_load_lds_dwordx4 v231, s[58:59] offset:3072\n\t"
      "s_lshl_b32 s37, s41, 7\n\t"
      "s_add_u32 s58, s54, s37\n\t"
      "s_addc_u32 s59, s55, 0\n\t"
      "s_add_u32 m0, s39, 32768\n\t"
      "s_nop 0\n\t"
      "global_load_lds_dwordx4 v228, s[58:59]\n\t"
      "global_load_lds_dwordx4 v229, s[58:59] offset:1024\n\t"
      "global_load_lds_dwordx4 v230, s[58:59] offset:2048\n\t"
      "global_load_lds_dwordx4 v231, s[58:59] offset:3072\n\t"
      "s_lshl_b32 s37, s42, 7\n\t"
      "s_add_u32 s58, s54, s37\n\t"
      "s_addc_u32 s59, s55, 0\n\t"
      "s_add_u32 m0, s39, 65536\n\t"
      "s_nop 0\n\t"
      "global_load_lds_dwordx4 v228, s[58:59]\n\t"
      "global_load_lds_dwordx4 v229, s[58:59] offset:1024\n\t"
      "global_load_lds_dwordx4 v230, s[58:59] offset:2048\n\t"
      "global_load_lds_dwordx4 v231, s[58:59] offset:3072\n\t"
      "s_lshl_b32 s37, s43, 7\n\t"
      "s_add_u32 s58, s54, s37\n\t"
      "s_addc_u32 s59, s55, 0\n\t"
      "s_add_u32 m0, s39, 98304\n\t"
      "s_nop 0\n\t"
      "global_load_lds_dwordx4 v228, s[58:59]\n\t"
      "global_load_lds_dwordx4 v229, s[58:59] offset:1024\n\t"
      "global_load_lds_dwordx4 v230, s[58:59] offset:2048\n\t"
      "global_load_lds_dwordx4 v231, s[58:59] offset:3072\n\t"
      "s_lshr_b32 s34, s24, 1\n\t"
      "s_lshl2_add_u32 s35, s29, s34\n\t"
      "s_and_b32 s36, s24, 1\n\t"
      "s_lshl4_add_u32 s35, s35, s36\n\t"
      "v_lshl_add_u32 v242, v240, 1, s35\n\t"
      "v_lshlrev_b32_e32 v248, 1, v242\n\t"
      "global_load_ushort v249, v248, s[16:17]\n\t"
      "s_movk_i32 s35, 896\n\t"
      "v_and_b32_e32 v247, 64, v244\n\t"
      "v_lshrrev_b32_e32 v247, 2, v247\n\t"
      "v_mad_u32_u24 v247, v242, s35, v247\n\t"
      "v_and_b32_e32 v242, 0x30, v244\n\t"
      "v_lshl_add_u32 v247, v242, 1, v247\n\t"
      "s_mov_b32 s82, 0x77777777\n\t"
      "s_mov_b32 s83, 0x77777777\n\t"
      "s_lshl_b32 s35, s32, 7\n\t"
      "s_add_u32 s62, s18, s35\n\t"
      "s_addc_u32 s63, s19, 0\n\t"
      "s_mul_i32 s35, s32, 96\n\t"
      "s_add_u32 s35, s35, 512\n\t"
      "s_add_u32 s18, s18, s35\n\t"
      "s_addc_u32 s19, s19, 0\n\t"
      "s_mul_i32 s35, s29, 28\n\t"
      "s_lshl2_add_u32 s35, s32, s35\n\t"
      "s_lshl1_add_u32 s35, s35, s36\n\t"
      "s_lshl2_add_u32 s35, s35, s34\n\t"
      "s_lshl_b32 s35, s35, 8\n\t"
      "s_add_u32 s35, s35, 6295552\n\t"
      "s_add_u32 s50, s20, s35\n\t"
      "s_addc_u32 s51, s21, 0\n\t"
      "v_lshrrev_b32_e32 v248, 2, v244\n\t"
      "v_bfe_u32 v242, v244, 8, 1\n\t"
      "v_lshlrev_b32_e32 v242, 4, v242\n\t"
      "v_xor_b32_e32 v245, v244, v242\n\t"
      "v_bfe_u32 v242, v244, 9, 1\n\t"
      "v_lshlrev_b32_e32 v242, 7, v242\n\t"
      "v_xor_b32_e32 v245, v245, v242\n\t"
      "s_add_u32 s35, s39, 98304\n\t"
      "v_add_u32_e32 v245, s35, v245\n\t"
      "v_lshl_or_b32 v246, v240, 1, s36\n\t"
      "v_bfe_u32 v242, v244, 5, 1\n\t"
      "v_xor_b32_e32 v246, v246, v242\n\t"
      "v_bfe_u32 v242, v244, 6, 1\n\t"
      "v_lshlrev_b32_e32 v242, 3, v242\n\t"
      "v_xor_b32_e32 v246, v246, v242\n\t"
      "v_lshlrev_b32_e32 v246, 4, v246\n\t"
      "v_and_b32_e32 v242, 0x60, v244\n\t"
      "v_lshl_or_b32 v246, v242, 3, v246\n\t"
      "v_bfe_u32 v242, v244, 4, 1\n\t"
      "v_lshl_or_b32 v246, v242, 3, v246\n\t"
      "s_lshl_b32 s34, s34, 10\n\t"
      "s_add_u32 s34, s34, 98304\n\t"
      "v_add_u32_e32 v246, s34, v246\n\t"
      "v_and_b32_e32 v240, 0x3c0, v244\n\t"
      "v_lshlrev_b32_e32 v240, 1, v240\n\t"
      "v_and_b32_e32 v241, 0x180, v244\n\t"
      "v_lshrrev_b32_e32 v241, 2, v241\n\t"
      "v_and_b32_e32 v242, 0x30, v244\n\t"
      "v_lshrrev_b32_e32 v242, 1, v242\n\t"
      "v_or3_b32 v232, v240, v241, v242\n\t"
      "v_add_u32_e32 v232, s39, v232\n\t"
      "v_min_u32_e32 v252, 432, v244\n\t"
      "v_lshlrev_b32_e32 v252, 4, v252\n\t"
      "v_xor_b32_e32 v233, 32, v232\n\t"
      "v_xor_b32_e32 v234, 64, v232\n\t"
      "v_xor_b32_e32 v235, 96, v232\n\t"
      "v_xor_b32_e32 v236, 65536, v232\n\t"
      "v_xor_b32_e32 v237, 65568, v232\n\t"
      "v_xor_b32_e32 v238, 65600, v232\n\t"
      "v_xor_b32_e32 v239, 65632, v232\n\t"
      "s_waitcnt vmcnt(13)\n\t"
      "ds_read_b64_tr_b16 v[0:1], v232\n\t"
      "ds_read_b64_tr_b16 v[2:3], v232 offset:2048\n\t"
      "ds_read_b64_tr_b16 v[4:5], v233\n\t"
      "ds_read_b64_tr_b16 v[6:7], v233 offset:2048\n\t"
      "ds_read_b64_tr_b16 v[8:9], v234\n\t"
      "ds_read_b64_tr_b16 v[10:11], v234 offset:2048\n\t"
      "ds_read_b64_tr_b16 v[12:13], v235\n\t"
      "ds_read_b64_tr_b16 v[14:15], v235 offset:2048\n\t"
      "s_waitcnt lgkmcnt(0)\n\t"
      "s_lshl_b32 s37, s44, 7\n\t"
      "s_add_u32 s58, s54, s37\n\t"
      "s_addc_u32 s59, s55, 0\n\t"
      "s_add_u32 m0, s39, 0\n\t"
      "s_nop 0\n\t"
      "global_load_lds_dwordx4 v228, s[58:59]\n\t"
      "global_load_lds_dwordx4 v229, s[58:59] offset:1024\n\t"
      "global_load_lds_dwordx4 v230, s[58:59] offset:2048\n\t"
      "global_load_lds_dwordx4 v231, s[58:59] offset:3072\n\t"
      "s_waitcnt vmcnt(13)\n\t"
      "ds_read_b64_tr_b16 v[16:17], v232 offset:32768\n\t"
      "ds_read_b64_tr_b16 v[18:19], v232 offset:34816\n\t"
      "ds_read_b64_tr_b16 v[20:21], v233 offset:32768\n\t"
      "ds_read_b64_tr_b16 v[22:23], v233 offset:34816\n\t"
      "ds_read_b64_tr_b16 v[24:25], v234 offset:32768\n\t"
      "ds_read_b64_tr_b16 v[26:27], v234 offset:34816\n\t"
      "ds_read_b64_tr_b16 v[28:29], v235 offset:32768\n\t"
      "ds_read_b64_tr_b16 v[30:31], v235 offset:34816\n\t"
      "s_waitcnt lgkmcnt(0)\n\t"
      "s_lshl_b32 s37, s45, 7\n\t"
      "s_add_u32 s58, s54, s37\n\t"
      "s_addc_u32 s59, s55, 0\n\t"
      "s_add_u32 m0, s39, 32768\n\t"
      "s_nop 0\n\t"
      "global_load_lds_dwordx4 v228, s[58:59]\n\t"
      "global_load_lds_dwordx4 v229, s[58:59] offset:1024\n\t"
      "global_load_lds_dwordx4 v230, s[58:59] offset:2048\n\t"
      "global_load_lds_dwordx4 v231, s[58:59] offset:3072\n\t"
      "s_waitcnt vmcnt(13)\n\t"
      "ds_read_b64_tr_b16 v[32:33], v236\n\t"
      "ds_read_b64_tr_b16 v[34:35], v236 offset:2048\n\t"
      "ds_read_b64_tr_b16 v[36:37], v237\n\t"
      "ds_read_b64_tr_b16 v[38:39], v237 offset:2048\n\t"
      "ds_read_b64_tr_b16 v[40:41], v238\n\t"
      "ds_read_b64_tr_b16 v[42:43], v238 offset:2048\n\t"
      "ds_read_b64_tr_b16 v[44:45], v239\n\t"
      "ds_read_b64_tr_b16 v[46:47], v239 offset:2048\n\t"
      "s_waitcnt lgkmcnt(0)\n\t"
      "s_lshl_b32 s37, s46, 7\n\t"
      "s_add_u32 s58, s54, s37\n\t"
      "s_addc_u32 s59, s55, 0\n\t"
      "s_add_u32 m0, s39, 65536\n\t"
      "s_nop 0\n\t"
      "global_load_lds_dwordx4 v228, s[58:59]\n\t"
      "global_load_lds_dwordx4 v229, s[58:59] offset:1024\n\t"
      "global_load_lds_dwordx4 v230, s[58:59] offset:2048\n\t"
      "global_load_lds_dwordx4 v231, s[58:59] offset:3072\n\t"
      "s_waitcnt vmcnt(13)\n\t"
      "ds_read_b64_tr_b16 v[48:49], v236 offset:32768\n\t"
      "ds_read_b64_tr_b16 v[50:51], v236 offset:34816\n\t"
      "ds_read_b64_tr_b16 v[52:53], v237 offset:32768\n\t"
      "ds_read_b64_tr_b16 v[54:55], v237 offset:34816\n\t"
      "ds_read_b64_tr_b16 v[56:57], v238 offset:32768\n\t"
      "ds_read_b64_tr_b16 v[58:59], v238 offset:34816\n\t"
      "ds_read_b64_tr_b16 v[60:61], v239 offset:32768\n\t"
      "ds_read_b64_tr_b16 v[62:63], v239 offset:34816\n\t"
      "s_waitcnt vmcnt(8)\n\t"
      "ds_read_b64_tr_b16 v[64:65], v232\n\t"
      "ds_read_b64_tr_b16 v[66:67], v232 offset:2048\n\t"
      "ds_read_b64_tr_b16 v[68:69], v233\n\t"
      "ds_read_b64_tr_b16 v[70:71], v233 offset:2048\n\t"
      "ds_read_b64_tr_b16 v[72:73], v234\n\t"
      "ds_read_b64_tr_b16 v[74:75], v234 offset:2048\n\t"
      "ds_read_b64_tr_b16 v[76:77], v235\n\t"
      "ds_read_b64_tr_b16 v[78:79], v235 offset:2048\n\t"
      "s_waitcnt vmcnt(4)\n\t"
      "ds_read_b64_tr_b16 v[80:81], v232 offset:32768\n\t"
      "ds_read_b64_tr_b16 v[82:83], v232 offset:34816\n\t"
      "ds_read_b64_tr_b16 v[84:85], v233 offset:32768\n\t"
      "ds_read_b64_tr_b16 v[86:87], v233 offset:34816\n\t"
      "ds_read_b64_tr_b16 v[88:89], v234 offset:32768\n\t"
      "ds_read_b64_tr_b16 v[90:91], v234 offset:34816\n\t"
      "ds_read_b64_tr_b16 v[92:93], v235 offset:32768\n\t"
      "ds_read_b64_tr_b16 v[94:95], v235 offset:34816\n\t"
      "s_waitcnt vmcnt(0)\n\t"
      "ds_read_b64_tr_b16 v[96:97], v236\n\t"
      "ds_read_b64_tr_b16 v[98:99], v236 offset:2048\n\t"
      "ds_read_b64_tr_b16 v[100:101], v237\n\t"
      "ds_read_b64_tr_b16 v[102:103], v237 offset:2048\n\t"
      "ds_read_b64_tr_b16 v[104:105], v238\n\t"
      "ds_read_b64_tr_b16 v[106:107], v238 offset:2048\n\t"
      "ds_read_b64_tr_b16 v[108:109], v239\n\t"
      "ds_read_b64_tr_b16 v[110:111], v239 offset:2048\n\t"
      "v_lshlrev_b32_e32 v249, 16, v249\n\t"
      "s_waitcnt lgkmcnt(0)\n\t"
      "v_min_u32_e32 v240, 48, v244\n\t"
      "v_mul_u32_u24_e32 v240, 7, v240\n\t"
      "s_lshl_b32 s34, s29, 4\n\t"
      "v_add_u32_e32 v240, s34, v240\n\t"
      "v_subrev_u32_e32 v241, 448, v240\n\t"
      "v_min_u32_e32 v240, v240, v241\n\t"
      "v_subrev_u32_e32 v241, 448, v240\n\t"
      "v_min_u32_e32 v240, v240, v241\n\t"
      "s_and_b32 s34, s29, 7\n\t"
      "s_lshl_b32 s34, s34, 8\n\t"
      "v_lshl_add_u32 v253, v240, 7, s34\n\t"
      "v_add_u32_e32 v143, 32768, v143\n\t"
      "s_cmp_lg_u32 s24, 3\n\t"
      "s_cbranch_scc1 SK18_%=\n\t"
      "global_load_dwordx2 v[138:139], v143, s[60:61] sc1\n\t"
      "SK18_%=:\n\t"
      "s_cmp_lg_u32 s24, 3\n\t"
      "s_cbranch_scc1 LF5B_%=\n\t"
      "s_waitcnt vmcnt(0)\n\t"
      "v_cmp_ne_u64_e32 vcc, s[22:23], v[138:139]\n\t"
      "s_cbranch_vccnz LF5R_%=\n\t"
      "LF5B_%=:\n\t"
      "s_barrier\n\t"
      "s_cmp_lg_u32 s24, 3\n\t"
      "s_cbranch_scc1 HF4_%=\n\t"
      "s_lshl3_add_u32 s34, s25, s26\n\t"
      "s_lshl_b32 s34, s34, 7\n\t"
      "s_add_u32 s34, s34, 98304\n\t"
      "v_mov_b32_e32 v238, s34\n\t"
      "v_mov_b32_e32 v236, s22\n\t"
      "v_mov_b32_e32 v237, s23\n\t"
      "s_mov_b64 exec, 1\n\t"
      "global_store_dwordx2 v238, v[236:237], s[60:61] sc1\n\t"
      "s_mov_b64 exec, -1\n\t"
      "HF4_%=:\n\t"
      "s_and_b32 s34, s29, 30\n\t"
      "s_lshl_b32 s34, s34, 10\n\t"
      "s_add_u32 s34, s34, 98304\n\t"
      "v_bfe_u32 v143, v244, 5, 2\n\t"
      "v_add_u32_e32 v143, 3, v143\n\t"
      "v_writelane_b32 v143, s32, 0\n\t"
      "v_writelane_b32 v143, s32, 1\n\t"
      "v_lshl_add_u32 v143, v143, 7, s34\n\t"
      "v_and_b32_e32 v142, 16, v244\n\t"
      "v_lshl_add_u32 v143, v142, 6, v143\n\t"
      "s_cmp_eq_u32 s24, 0\n\t"
      "s_cbranch_scc1 PG0R_%=\n\t"
      "PG0B_%=:\n\t"
      "s_sleep 6\n\t"
      "s_barrier\n\t"
      "s_cmp_lg_u32 s24, 3\n\t"
      "s_cbranch_scc1 SK19_%=\n\t"
      "global_load_dwordx2 v[240:241], v143, s[60:61] sc1\n\t"
      "SK19_%=:\n\t"
      "v_mov_b32_e32 v242, v143\n\t"
      "s_add_u32 s58, s64, s40\n\t"
      "s_addc_u32 s59, s65, 0\n\t"
      "global_load_dwordx4 v[128:131], v244, s[58:59] sc1\n\t"
      "s_add_u32 s58, s64, s41\n\t"
      "s_addc_u32 s59, s65, 0\n\t"
      "global_load_dwordx4 v[132:135], v244, s[58:59] sc1\n\t"
      "s_add_u32 s58, s64, s42\n\t"
      "s_addc_u32 s59, s65, 0\n\t"
      "global_load_dwordx4 v[136:139], v244, s[58:59] sc1\n\t"
      "s_add_u32 s58, s64, s43\n\t"
      "s_addc_u32 s59, s65, 0\n\t"
      "global_load_dwordx4 v[140:143], v244, s[58:59] sc1\n\t"
      "s_add_u32 s58, s64, s44\n\t"
      "s_addc_u32 s59, s65, 0\n\t"
      "global_load_dwordx4 v[144:147], v244, s[58:59] sc1\n\t"
      "s_add_u32 s58, s64, s45\n\t"
      "s_addc_u32 s59, s65, 0\n\t"
      "global_load_dwordx4 v[148:151], v244, s[58:59] sc1\n\t"
      "s_add_u32 s58, s64, s46\n\t"
      "s_addc_u32 s59, s65, 0\n\t"
      "global_load_dwordx4 v[152:155], v244, s[58:59] sc1\n\t"
      "v_mov_b32_e32 v254, v249\n\t"
      "s_waitcnt vmcnt(6)\n\t"
      "v_mfma_f32_16x16x32_bf16 v[196:199], v[128:131], v[0:3], 0\n\t"
      "v_mfma_f32_16x16x32_bf16 v[200:203], v[128:131], v[4:7], 0\n\t"
      "v_mfma_f32_16x16x32_bf16 v[204:207], v[128:131], v[8:11], 0\n\t"
      "v_mfma_f32_16x16x32_bf16 v[208:211], v[128:131], v[12:15], 0\n\t"
      "s_waitcnt vmcnt(5)\n\t"
      "v_mfma_f32_16x16x32_bf16 v[196:199], v[132:135], v[16:19], v[196:199]\n\t"
      "v_mfma_f32_16x16x32_bf16 v[200:203], v[132:135], v[20:23], v[200:203]\n\t"
      "v_mfma_f32_16x16x32_bf16 v[204:207], v[132:135], v[24:27], v[204:207]\n\t"
      "s_cmp_lg_u32 s24, 3\n\t"
      "s_cbranch_scc1 LF8B_%=\n\t"
      "v_cmp_ne_u64_e32 vcc, s[22:23], v[240:241]\n\t"
      "s_cbranch_vccnz LF8R_%=\n\t"
      "LF8B_%=:\n\t"
      "s_barrier\n\t"
      "v_mfma_f32_16x16x32_bf16 v[208:211], v[132:135], v[28:31], v[208:211]\n\t"
      "global_load_dword v115, v248, s[50:51] offset:-4096 sc1\n\t"
      "global_load_dword v116, v248, s[50:51] offset:-2048 sc1\n\t"
      "s_waitcnt vmcnt(6)\n\t"
      "v_mfma_f32_16x16x32_bf16 v[196:199], v[136:139], v[32:35], v[196:199]\n\t"
      "global_load_dword v117, v248, s[50:51] sc1\n\t"
      "global_load_dword v118, v248, s[50:51] offset:2048 sc1\n\t"
      "s_add_u32 s58, s66, s40\n\t"
      "s_addc_u32 s59, s67, 0\n\t"
      "global_load_dwordx4 v[156:159], v244, s[58:59] sc1\n\t"
      "v_mfma_f32_16x16x32_bf16 v[200:203], v[136:139], v[36:39], v[200:203]\n\t"
      "s_sub_u32 s34, 16, s32\n\t"
      "s_lshl_b32 s34, s34, 11\n\t"
      "s_add_u32 s50, s50, s34\n\t"
      "s_addc_u32 s51, s51, 0\n\t"
      "global_load_dword v112, v248, s[50:51] offset:-4096 sc1\n\t"
      "global_load_dword v113, v248, s[50:51] offset:-2048 sc1\n\t"
      "v_mfma_f32_16x16x32_bf16 v[204:207], v[136:139], v[40:43], v[204:207]\n\t"
      "global_load_dword v114, v248, s[50:51] sc1\n\t"
      "v_mfma_f32_16x16x32_bf16 v[208:211], v[136:139], v[44:47], v[208:211]\n\t"
      "s_waitcnt vmcnt(11)\n\t"
      "v_mfma_f32_16x16x32_bf16 v[196:199], v[140:143], v[48:51], v[196:199]\n\t"
      "s_add_u32 s58, s66, s41\n\t"
      "s_addc_u32 s59, s67, 0\n\t"
      "global_load_dwordx4 v[160:163], v244, s[58:59] sc1\n\t"
      "v_mfma_f32_16x16x32_bf16 v[200:203], v[140:143], v[52:55], v[200:203]\n\t"
      "v_mfma_f32_16x16x32_bf16 v[204:207], v[140:143], v[56:59], v[204:207]\n\t"
      "v_mfma_f32_16x16x32_bf16 v[208:211], v[140:143], v[60:63], v[208:211]\n\t"
      "s_waitcnt vmcnt(11)\n\t"
      "v_mfma_f32_16x16x32_bf16 v[196:199], v[144:147], v[64:67], v[196:199]\n\t"
      "s_add_u32 s58, s66, s42\n\t"
      "s_addc_u32 s59, s67, 0\n\t"
      "global_load_dwordx4 v[164:167], v244, s[58:59] sc1\n\t"
      "v_mfma_f32_16x16x32_bf16 v[200:203], v[144:147], v[68:71], v[200:203]\n\t"
      "v_mfma_f32_16x16x32_bf16 v[204:207], v[144:147], v[72:75], v[204:207]\n\t"
      "v_mfma_f32_16x16x32_bf16 v[208:211], v[144:147], v[76:79], v[208:211]\n\t"
      "s_waitcnt vmcnt(11)\n\t"
      "v_mfma_f32_16x16x32_bf16 v[196:199], v[148:151], v[80:83], v[196:199]\n\t"
      "s_add_u32 s58, s66, s43\n\t"
      "s_addc_u32 s59, s67, 0\n\t"
      "global_load_dwordx4 v[168:171], v244, s[58:59] sc1\n\t"
      "v_mfma_f32_16x16x32_bf16 v[200:203], v[148:151], v[84:87], v[200:203]\n\t"
      "v_mfma_f32_16x16x32_bf16 v[204:207], v[148:151], v[88:91], v[204:207]\n\t"
      "v_mfma_f32_16x16x32_bf16 v[208:211], v[148:151], v[92:95], v[208:211]\n\t"
      "s_waitcnt vmcnt(11)\n\t"
      "v_mfma_f32_16x16x32_bf16 v[196:199], v[152:155], v[96:99], v[196:199]\n\t"
      "s_add_u32 s58, s66, s44\n\t"
      "s_addc_u32 s59, s67, 0\n\t"
      "global_load_dwordx4 v[172:175], v244, s[58:59] sc1\n\t"
      "v_mfma_f32_16x16x32_bf16 v[200:203], v[152:155], v[100:103], v[200:203]\n\t"
      "v_mfma_f32_16x16x32_bf16 v[204:207], v[152:155], v[104:107], v[204:207]\n\t"
      "v_mfma_f32_16x16x32_bf16 v[208:211], v[152:155], v[108:111], v[208:211]\n\t"
      "s_nop 1\n\t"
      "v_add_f32_e32 v251, v196, v197\n\t"
      "v_add_f32_e32 v251, v251, v198\n\t"
      "v_cmp_u_f32_e32 vcc, v251, v199\n\t"
      "s_cbranch_vccnz LA40R_%=\n\t"
      "LA40B_%=:\n\t"
      "ds_write_b128 v245, v[196:199]\n\t"
      "s_waitcnt vmcnt(7)\n\t"
      "v_mfma_f32_16x16x32_bf16 v[212:215], v[156:159], v[0:3], 0\n\t"
      "ds_write_b128 v245, v[200:203] offset:1024\n\t"
      "s_add_u32 s58, s66, s45\n\t"
      "s_addc_u32 s59, s67, 0\n\t"
      "global_load_dwordx4 v[176:179], v244, s[58:59] sc1\n\t"
      "v_mfma_f32_16x16x32_bf16 v[216:219], v[156:159], v[4:7], 0\n\t"
      "ds_write_b128 v245, v[204:207] offset:2048\n\t"
      "v_mfma_f32_16x16x32_bf16 v[220:223], v[156:159], v[8:11], 0\n\t"
      "ds_write_b128 v245, v[208:211] offset:3072\n\t"
      "v_mfma_f32_16x16x32_bf16 v[224:227], v[156:159], v[12:15], 0\n\t"
      "s_waitcnt vmcnt(4)\n\t"
      "v_mfma_f32_16x16x32_bf16 v[212:215], v[160:163], v[16:19], v[212:215]\n\t"
      "s_add_u32 s58, s66, s46\n\t"
      "s_addc_u32 s59, s67, 0\n\t"
      "global_load_dwordx4 v[180:183], v244, s[58:59] sc1\n\t"
      "v_mfma_f32_16x16x32_bf16 v[216:219], v[160:163], v[20:23], v[216:219]\n\t"
      "v_mfma_f32_16x16x32_bf16 v[220:223], v[160:163], v[24:27], v[220:223]\n\t"
      "s_waitcnt lgkmcnt(0)\n\t"
      "s_barrier\n\t"
      "ds_read_b64 v[228:229], v246\n\t"
      "ds_read_b64 v[230:231], v246 offset:4096\n\t"
      "s_add_u32 s58, s68, s40\n\t"
      "s_addc_u32 s59, s69, 0\n\t"
      "global_load_dwordx4 v[184:187], v244, s[58:59] sc1\n\t"
      "v_mfma_f32_16x16x32_bf16 v[224:227], v[160:163], v[28:31], v[224:227]\n\t"
      "ds_read_b64 v[232:233], v246 offset:8192\n\t"
      "ds_read_b64 v[234:235], v246 offset:12288\n\t"
      "s_waitcnt vmcnt(5)\n\t"
      "v_mfma_f32_16x16x32_bf16 v[212:215], v[164:167], v[32:35], v[212:215]\n\t"
      "ds_read_b64 v[236:237], v246 offset:16384\n\t"
      "ds_read_b64 v[238:239], v246 offset:20480\n\t"
      "v_mfma_f32_16x16x32_bf16 v[216:219], v[164:167], v[36:39], v[216:219]\n\t"
      "ds_read_b64 v[240:241], v246 offset:24576\n\t"
      "ds_read_b64 v[242:243], v246 offset:28672\n\t"
      "v_lshlrev_b32_e32 v248, 16, v112\n\t"
      "v_mfma_f32_16x16x32_bf16 v[220:223], v[164:167], v[40:43], v[220:223]\n\t"
      "v_and_b32_e32 v249, 0xffff0000, v112\n\t"
      "s_add_u32 s58, s68, s41\n\t"
      "s_addc_u32 s59, s69, 0\n\t"
      "global_load_dwordx4 v[188:191], v244, s[58:59] sc1\n\t"
      "v_mfma_f32_16x16x32_bf16 v[224:227], v[164:167], v[44:47], v[224:227]\n\t"
      "v_add_f32_e32 v248, v254, v248\n\t"
      "v_add_f32_e32 v249, v254, v249\n\t"
      "s_waitcnt vmcnt(5)\n\t"
      "v_mfma_f32_16x16x32_bf16 v[212:215], v[168:171], v[48:51], v[212:215]\n\t"
      "s_waitcnt lgkmcnt(4)\n\t"
      "v_mfma_f32_16x16x32_bf16 v[216:219], v[168:171], v[52:55], v[216:219]\n\t"
      "v_add_f32_e32 v228, v228, v230\n\t"
      "v_add_f32_e32 v232, v232, v234\n\t"
      "v_mfma_f32_16x16x32_bf16 v[220:223], v[168:171], v[56:59], v[220:223]\n\t"
      "v_add_f32_e32 v229, v229, v231\n\t"
      "s_add_u32 s58, s68, s42\n\t"
      "s_addc_u32 s59, s69, 0\n\t"
      "global_load_dwordx4 v[192:195], v244, s[58:59] sc1\n\t"
      "v_mfma_f32_16x16x32_bf16 v[224:227], v[168:171], v[60:63], v[224:227]\n\t"
      "v_add_f32_e32 v233, v233, v235\n\t"
      "s_waitcnt lgkmcnt(0)\n\t"
      "s_waitcnt vmcnt(5)\n\t"
      "v_mfma_f32_16x16x32_bf16 v[212:215], v[172:175], v[64:67], v[212:215]\n\t"
      "v_add_f32_e32 v236, v236, v238\n\t"
      "v_mfma_f32_16x16x32_bf16 v[216:219], v[172:175], v[68:71], v[216:219]\n\t"
      "v_add_f32_e32 v240, v240, v242\n\t"
      "v_add_f32_e32 v237, v237, v239\n\t"
      "v_mfma_f32_16x16x32_bf16 v[220:223], v[172:175], v[72:75], v[220:223]\n\t"
      "v_add_f32_e32 v241, v241, v243\n\t"
      "s_add_u32 s58, s68, s43\n\t"
      "s_addc_u32 s59, s69, 0\n\t"
      "global_load_dwordx4 v[128:131], v244, s[58:59] sc1\n\t"
      "v_mfma_f32_16x16x32_bf16 v[224:227], v[172:175], v[76:79], v[224:227]\n\t"
      "v_add_f32_e32 v228, v228, v232\n\t"
      "v_add_f32_e32 v236, v236, v240\n\t"
      "s_waitcnt vmcnt(5)\n\t"
      "v_mfma_f32_16x16x32_bf16 v[212:215], v[176:179], v[80:83], v[212:215]\n\t"
      "v_add_f32_e32 v229, v229, v233\n\t"
      "v_mfma_f32_16x16x32_bf16 v[216:219], v[176:179], v[84:87], v[216:219]\n\t"
      "v_add_f32_e32 v237, v237, v241\n\t"
      "v_add_f32_e32 v228, v228, v248\n\t"
      "v_mfma_f32_16x16x32_bf16 v[220:223], v[176:179], v[88:91], v[220:223]\n\t"
      "v_add_f32_e32 v229, v229, v249\n\t"
      "s_add_u32 s58, s68, s44\n\t"
      "s_addc_u32 s59, s69, 0\n\t"
      "global_load_dwordx4 v[132:135], v244, s[58:59] sc1\n\t"
      "v_mfma_f32_16x16x32_bf16 v[224:227], v[176:179], v[92:95], v[224:227]\n\t"
      "v_add_f32_e32 v228, v228, v236\n\t"
      "v_add_f32_e32 v229, v229, v237\n\t"
      "s_waitcnt vmcnt(5)\n\t"
      "v_mfma_f32_16x16x32_bf16 v[212:215], v[180:183], v[96:99], v[212:215]\n\t"
      "v_cvt_pk_bf16_f32 v232, v228, v229\n\t"
      "v_mfma_f32_16x16x32_bf16 v[216:219], v[180:183], v[100:103], v[216:219]\n\t"
      "s_mov_b32 vcc_lo, 0xeeeeeeee\n\t"
      "s_mov_b32 vcc_hi, 0xeeeeeeee\n\t"
      "v_cndmask_b32_dpp v120, v232, v120, vcc quad_perm:[0,0,0,0] row_mask:0xf bank_mask:0xf\n\t"
      "v_cndmask_b32_dpp v121, v232, v121, vcc quad_perm:[1,1,1,1] row_mask:0xf bank_mask:0xf\n\t"
      "v_cndmask_b32_dpp v122, v232, v122, vcc quad_perm:[2,2,2,2] row_mask:0xf bank_mask:0xf\n\t"
      "v_cndmask_b32_dpp v123, v232, v123, vcc quad_perm:[3,3,3,3] row_mask:0xf bank_mask:0xf\n\t"
      "v_mfma_f32_16x16x32_bf16 v[220:223], v[180:183], v[104:107], v[220:223]\n\t"
      "s_add_u32 s58, s68, s45\n\t"
      "s_addc_u32 s59, s69, 0\n\t"
      "global_load_dwordx4 v[136:139], v244, s[58:59] sc1\n\t"
      "v_mfma_f32_16x16x32_bf16 v[224:227], v[180:183], v[108:111], v[224:227]\n\t"
      "v_add_f32_e32 v251, v212, v213\n\t"
      "v_add_f32_e32 v251, v251, v214\n\t"
      "v_cmp_u_f32_e32 vcc, v251, v215\n\t"
      "s_cbranch_vccnz LA41R_%=\n\t"
      "LA41B_%=:\n\t"
      "ds_write_b128 v245, v[212:215] offset:32768\n\t"
      "s_waitcnt vmcnt(5)\n\t"
      "v_mfma_f32_16x16x32_bf16 v[196:199], v[184:187], v[0:3], 0\n\t"
      "ds_write_b128 v245, v[216:219] offset:33792\n\t"
      "v_mfma_f32_16x16x32_bf16 v[200:203], v[184:187], v[4:7], 0\n\t"
      "ds_write_b128 v245, v[220:223] offset:34816\n\t"
      "v_mfma_f32_16x16x32_bf16 v[204:207], v[184:187], v[8:11], 0\n\t"
      "ds_write_b128 v245, v[224:227] offset:35840\n\t"
      "s_add_u32 s58, s68, s46\n\t"
      "s_addc_u32 s59, s69, 0\n\t"
      "global_load_dwordx4 v[140:143], v244, s[58:59] sc1\n\t"
      "v_mfma_f32_16x16x32_bf16 v[208:211], v[184:187], v[12:15], 0\n\t"
      "s_waitcnt vmcnt(5)\n\t"
      "v_mfma_f32_16x16x32_bf16 v[196:199], v[188:191], v[16:19], v[196:199]\n\t"
      "v_mfma_f32_16x16x32_bf16 v[200:203], v[188:191], v[20:23], v[200:203]\n\t"
      "v_mfma_f32_16x16x32_bf16 v[204:207], v[188:191], v[24:27], v[204:207]\n\t"
      "s_waitcnt lgkmcnt(0)\n\t"
      "s_barrier\n\t"
      "ds_read_b64 v[228:229], v246 offset:32768\n\t"
      "ds_read_b64 v[230:231], v246 offset:36864\n\t"
      "s_add_u32 s58, s70, s40\n\t"
      "s_addc_u32 s59, s71, 0\n\t"
      "global_load_dwordx4 v[144:147], v244, s[58:59] sc1\n\t"
      "v_mfma_f32_16x16x32_bf16 v[208:211], v[188:191], v[28:31], v[208:211]\n\t"
      "ds_read_b64 v[232:233], v246 offset:40960\n\t"
      "ds_read_b64 v[234:235], v246 offset:45056\n\t"
      "s_waitcnt vmcnt(5)\n\t"
      "v_mfma_f32_16x16x32_bf16 v[196:199], v[192:195], v[32:35], v[196:199]\n\t"
      "ds_read_b64 v[236:237], v246 offset:49152\n\t"
      "ds_read_b64 v[238:239], v246 offset:53248\n\t"
      "v_mfma_f32_16x16x32_bf16 v[200:203], v[192:195], v[36:39], v[200:203]\n\t"
      "ds_read_b64 v[240:241], v246 offset:57344\n\t"
      "ds_read_b64 v[242:243], v246 offset:61440\n\t"
      "v_lshlrev_b32_e32 v248, 16, v113\n\t"
      "v_mfma_f32_16x16x32_bf16 v[204:207], v[192:195], v[40:43], v[204:207]\n\t"
      "v_and_b32_e32 v249, 0xffff0000, v113\n\t"
      "s_add_u32 s58, s70, s41\n\t"
      "s_addc_u32 s59, s71, 0\n\t"
      "global_load_dwordx4 v[148:151], v244, s[58:59] sc1\n\t"
      "v_mfma_f32_16x16x32_bf16 v[208:211], v[192:195], v[44:47], v[208:211]\n\t"
      "v_add_f32_e32 v248, v254, v248\n\t"
      "v_add_f32_e32 v249, v254, v249\n\t"
      "s_waitcnt vmcnt(5)\n\t"
      "v_mfma_f32_16x16x32_bf16 v[196:199], v[128:131], v[48:51], v[196:199]\n\t"
      "s_waitcnt lgkmcnt(4)\n\t"
      "v_mfma_f32_16x16x32_bf16 v[200:203], v[128:131], v[52:55], v[200:203]\n\t"
      "v_add_f32_e32 v228, v228, v230\n\t"
      "v_add_f32_e32 v232, v232, v234\n\t"
      "v_mfma_f32_16x16x32_bf16 v[204:207], v[128:131], v[56:59], v[204:207]\n\t"
      "v_add_f32_e32 v229, v229, v231\n\t"
      "s_add_u32 s58, s70, s42\n\t"
      "s_addc_u32 s59, s71, 0\n\t"
      "global_load_dwordx4 v[152:155], v244, s[58:59] sc1\n\t"
      "v_mfma_f32_16x16x32_bf16 v[208:211], v[128:131], v[60:63], v[208:211]\n\t"
      "v_add_f32_e32 v233, v233, v235\n\t"
      "s_waitcnt lgkmcnt(0)\n\t"
      "s_waitcnt vmcnt(5)\n\t"
      "v_mfma_f32_16x16x32_bf16 v[196:199], v[132:135], v[64:67], v[196:199]\n\t"
      "v_add_f32_e32 v236, v236, v238\n\t"
      "v_mfma_f32_16x16x32_bf16 v[200:203], v[132:135], v[68:71], v[200:203]\n\t"
      "v_add_f32_e32 v240, v240, v242\n\t"
      "v_add_f32_e32 v237, v237, v239\n\t"
      "v_mfma_f32_16x16x32_bf16 v[204:207], v[132:135], v[72:75], v[204:207]\n\t"
      "v_add_f32_e32 v241, v241, v243\n\t"
      "s_add_u32 s58, s70, s43\n\t"
      "s_addc_u32 s59, s71, 0\n\t"
      "global_load_dwordx4 v[156:159], v244, s[58:59] sc1\n\t"
      "v_mfma_f32_16x16x32_bf16 v[208:211], v[132:135], v[76:79], v[208:211]\n\t"
      "v_add_f32_e32 v228, v228, v232\n\t"
      "v_add_f32_e32 v236, v236, v240\n\t"
      "s_waitcnt vmcnt(5)\n\t"
      "v_mfma_f32_16x16x32_bf16 v[196:199], v[136:139], v[80:83], v[196:199]\n\t"
      "v_add_f32_e32 v229, v229, v233\n\t"
      "v_mfma_f32_16x16x32_bf16 v[200:203], v[136:139], v[84:87], v[200:203]\n\t"
      "v_add_f32_e32 v237, v237, v241\n\t"
      "v_add_f32_e32 v228, v228, v248\n\t"
      "v_mfma_f32_16x16x32_bf16 v[204:207], v[136:139], v[88:91], v[204:207]\n\t"
      "v_add_f32_e32 v229, v229, v249\n\t"
      "s_add_u32 s58, s70, s44\n\t"
      "s_addc_u32 s59, s71, 0\n\t"
      "global_load_dwordx4 v[160:163], v244, s[58:59] sc1\n\t"
      "v_mfma_f32_16x16x32_bf16 v[208:211], v[136:139], v[92:95], v[208:211]\n\t"
      "v_add_f32_e32 v228, v228, v236\n\t"
      "v_add_f32_e32 v229, v229, v237\n\t"
      "s_waitcnt vmcnt(5)\n\t"
      "v_mfma_f32_16x16x32_bf16 v[196:199], v[140:143], v[96:99], v[196:199]\n\t"
      "v_cvt_pk_bf16_f32 v232, v228, v229\n\t"
      "v_mfma_f32_16x16x32_bf16 v[200:203], v[140:143], v[100:103], v[200:203]\n\t"
      "s_mov_b32 vcc_lo, 0xdddddddd\n\t"
      "s_mov_b32 vcc_hi, 0xdddddddd\n\t"
      "v_cndmask_b32_dpp v120, v232, v120, vcc quad_perm:[0,0,0,0] row_mask:0xf bank_mask:0xf\n\t"
      "v_cndmask_b32_dpp v121, v232, v121, vcc quad_perm:[1,1,1,1] row_mask:0xf bank_mask:0xf\n\t"
      "v_cndmask_b32_dpp v122, v232, v122, vcc quad_perm:[2,2,2,2] row_mask:0xf bank_mask:0xf\n\t"
      "v_cndmask_b32_dpp v123, v232, v123, vcc quad_perm:[3,3,3,3] row_mask:0xf bank_mask:0xf\n\t"
      "v_mfma_f32_16x16x32_bf16 v[204:207], v[140:143], v[104:107], v[204:207]\n\t"
      "s_add_u32 s58, s70, s45\n\t"
      "s_addc_u32 s59, s71, 0\n\t"
      "global_load_dwordx4 v[164:167], v244, s[58:59] sc1\n\t"
      "v_mfma_f32_16x16x32_bf16 v[208:211], v[140:143], v[108:111], v[208:211]\n\t"
      "v_add_f32_e32 v251, v196, v197\n\t"
      "v_add_f32_e32 v251, v251, v198\n\t"
      "v_cmp_u_f32_e32 vcc, v251, v199\n\t"
      "s_cbranch_vccnz LA42R_%=\n\t"
      "LA42B_%=:\n\t"
      "ds_write_b128 v245, v[196:199]\n\t"
      "s_waitcnt vmcnt(5)\n\t"
      "v_mfma_f32_16x16x32_bf16 v[212:215], v[144:147], v[0:3], 0\n\t"
      "ds_write_b128 v245, v[200:203] offset:1024\n\t"
      "v_mfma_f32_16x16x32_bf16 v[216:219], v[144:147], v[4:7], 0\n\t"
      "ds_write_b128 v245, v[204:207] offset:2048\n\t"
      "v_mfma_f32_16x16x32_bf16 v[220:223], v[144:147], v[8:11], 0\n\t"
      "ds_write_b128 v245, v[208:211] offset:3072\n\t"
      "s_add_u32 s58, s70, s46\n\t"
      "s_addc_u32 s59, s71, 0\n\t"
      "global_load_dwordx4 v[168:171], v244, s[58:59] sc1\n\t"
      "v_mfma_f32_16x16x32_bf16 v[224:227], v[144:147], v[12:15], 0\n\t"
      "s_waitcnt vmcnt(5)\n\t"
      "v_mfma_f32_16x16x32_bf16 v[212:215], v[148:151], v[16:19], v[212:215]\n\t"
      "v_mfma_f32_16x16x32_bf16 v[216:219], v[148:151], v[20:23], v[216:219]\n\t"
      "v_mfma_f32_16x16x32_bf16 v[220:223], v[148:151], v[24:27], v[220:223]\n\t"
      "s_waitcnt lgkmcnt(0)\n\t"
      "s_barrier\n\t"
      "ds_read_b64 v[228:229], v246\n\t"
      "ds_read_b64 v[230:231], v246 offset:4096\n\t"
      "s_add_u32 s58, s72, s40\n\t"
      "s_addc_u32 s59, s73, 0\n\t"
      "global_load_dwordx4 v[172:175], v244, s[58:59] sc1\n\t"
      "v_mfma_f32_16x16x32_bf16 v[224:227], v[148:151], v[28:31], v[224:227]\n\t"
      "ds_read_b64 v[232:233], v246 offset:8192\n\t"
      "ds_read_b64 v[234:235], v246 offset:12288\n\t"
      "s_waitcnt vmcnt(5)\n\t"
      "v_mfma_f32_16x16x32_bf16 v[212:215], v[152:155], v[32:35], v[212:215]\n\t"
      "ds_read_b64 v[236:237], v246 offset:16384\n\t"
      "ds_read_b64 v[238:239], v246 offset:20480\n\t"
      "v_mfma_f32_16x16x32_bf16 v[216:219], v[152:155], v[36:39], v[216:219]\n\t"
      "ds_read_b64 v[240:241], v246 offset:24576\n\t"
      "ds_read_b64 v[242:243], v246 offset:28672\n\t"
      "v_lshlrev_b32_e32 v248, 16, v114\n\t"
      "v_mfma_f32_16x16x32_bf16 v[220:223], v[152:155], v[40:43], v[220:223]\n\t"
      "v_and_b32_e32 v249, 0xffff0000, v114\n\t"
      "s_add_u32 s58, s72, s41\n\t"
      "s_addc_u32 s59, s73, 0\n\t"
      "global_load_dwordx4 v[176:179], v244, s[58:59] sc1\n\t"
      "v_mfma_f32_16x16x32_bf16 v[224:227], v[152:155], v[44:47], v[224:227]\n\t"
      "v_add_f32_e32 v248, v254, v248\n\t"
      "v_add_f32_e32 v249, v254, v249\n\t"
      "s_waitcnt vmcnt(5)\n\t"
      "v_mfma_f32_16x16x32_bf16 v[212:215], v[156:159], v[48:51], v[212:215]\n\t"
      "s_waitcnt lgkmcnt(4)\n\t"
      "v_mfma_f32_16x16x32_bf16 v[216:219], v[156:159], v[52:55], v[216:219]\n\t"
      "v_add_f32_e32 v228, v228, v230\n\t"
      "v_add_f32_e32 v232, v232, v234\n\t"
      "v_mfma_f32_16x16x32_bf16 v[220:223], v[156:159], v[56:59], v[220:223]\n\t"
      "v_add_f32_e32 v229, v229, v231\n\t"
      "s_add_u32 s58, s72, s42\n\t"
      "s_addc_u32 s59, s73, 0\n\t"
      "global_load_dwordx4 v[180:183], v244, s[58:59] sc1\n\t"
      "v_mfma_f32_16x16x32_bf16 v[224:227], v[156:159], v[60:63], v[224:227]\n\t"
      "v_add_f32_e32 v233, v233, v235\n\t"
      "s_waitcnt lgkmcnt(0)\n\t"
      "s_waitcnt vmcnt(5)\n\t"
      "v_mfma_f32_16x16x32_bf16 v[212:215], v[160:163], v[64:67], v[212:215]\n\t"
      "v_add_f32_e32 v236, v236, v238\n\t"
      "v_mfma_f32_16x16x32_bf16 v[216:219], v[160:163], v[68:71], v[216:219]\n\t"
      "v_add_f32_e32 v240, v240, v242\n\t"
      "v_add_f32_e32 v237, v237, v239\n\t"
      "v_mfma_f32_16x16x32_bf16 v[220:223], v[160:163], v[72:75], v[220:223]\n\t"
      "v_add_f32_e32 v241, v241, v243\n\t"
      "s_add_u32 s58, s72, s43\n\t"
      "s_addc_u32 s59, s73, 0\n\t"
      "global_load_dwordx4 v[184:187], v244, s[58:59] sc1\n\t"
      "v_mfma_f32_16x16x32_bf16 v[224:227], v[160:163], v[76:79], v[224:227]\n\t"
      "v_add_f32_e32 v228, v228, v232\n\t"
      "v_add_f32_e32 v236, v236, v240\n\t"
      "s_waitcnt vmcnt(5)\n\t"
      "v_mfma_f32_16x16x32_bf16 v[212:215], v[164:167], v[80:83], v[212:215]\n\t"
      "v_add_f32_e32 v229, v229, v233\n\t"
      "v_mfma_f32_16x16x32_bf16 v[216:219], v[164:167], v[84:87], v[216:219]\n\t"
      "v_add_f32_e32 v237, v237, v241\n\t"
      "v_add_f32_e32 v228, v228, v248\n\t"
      "v_mfma_f32_16x16x32_bf16 v[220:223], v[164:167], v[88:91], v[220:223]\n\t"
      "v_add_f32_e32 v229, v229, v249\n\t"
      "s_add_u32 s58, s72, s44\n\t"
      "s_addc_u32 s59, s73, 0\n\t"
      "global_load_dwordx4 v[188:191], v244, s[58:59] sc1\n\t"
      "v_mfma_f32_16x16x32_bf16 v[224:227], v[164:167], v[92:95], v[224:227]\n\t"
      "v_add_f32_e32 v228, v228, v236\n\t"
      "v_add_f32_e32 v229, v229, v237\n\t"
      "s_waitcnt vmcnt(5)\n\t"
      "v_mfma_f32_16x16x32_bf16 v[212:215], v[168:171], v[96:99], v[212:215]\n\t"
      "v_cvt_pk_bf16_f32 v232, v228, v229\n\t"
      "v_mfma_f32_16x16x32_bf16 v[216:219], v[168:171], v[100:103], v[216:219]\n\t"
      "s_mov_b32 vcc_lo, 0xbbbbbbbb\n\t"
      "s_mov_b32 vcc_hi, 0xbbbbbbbb\n\t"
      "v_cndmask_b32_dpp v120, v232, v120, vcc quad_perm:[0,0,0,0] row_mask:0xf bank_mask:0xf\n\t"
      "v_cndmask_b32_dpp v121, v232, v121, vcc quad_perm:[1,1,1,1] row_mask:0xf bank_mask:0xf\n\t"
      "v_cndmask_b32_dpp v122, v232, v122, vcc quad_perm:[2,2,2,2] row_mask:0xf bank_mask:0xf\n\t"
      "v_cndmask_b32_dpp v123, v232, v123, vcc quad_perm:[3,3,3,3] row_mask:0xf bank_mask:0xf\n\t"
      "v_mfma_f32_16x16x32_bf16 v[220:223], v[168:171], v[104:107], v[220:223]\n\t"
      "s_add_u32 s58, s72, s45\n\t"
      "s_addc_u32 s59, s73, 0\n\t"
      "global_load_dwordx4 v[192:195], v244, s[58:59] sc1\n\t"
      "v_mfma_f32_16x16x32_bf16 v[224:227], v[168:171], v[108:111], v[224:227]\n\t"
      "v_add_f32_e32 v251, v212, v213\n\t"
      "v_add_f32_e32 v251, v251, v214\n\t"
      "v_cmp_u_f32_e32 vcc, v251, v215\n\t"
      "s_cbranch_vccnz LA43R_%=\n\t"
      "LA43B_%=:\n\t"
      "ds_write_b128 v245, v[212:215] offset:32768\n\t"
      "s_waitcnt vmcnt(5)\n\t"
      "v_mfma_f32_16x16x32_bf16 v[196:199], v[172:175], v[0:3], 0\n\t"
      "ds_write_b128 v245, v[216:219] offset:33792\n\t"
      "v_mfma_f32_16x16x32_bf16 v[200:203], v[172:175], v[4:7], 0\n\t"
      "ds_write_b128 v245, v[220:223] offset:34816\n\t"
      "v_mfma_f32_16x16x32_bf16 v[204:207], v[172:175], v[8:11], 0\n\t"
      "ds_write_b128 v245, v[224:227] offset:35840\n\t"
      "s_add_u32 s58, s72, s46\n\t"
      "s_addc_u32 s59, s73, 0\n\t"
      "global_load_dwordx4 v[128:131], v244, s[58:59] sc1\n\t"
      "v_mfma_f32_16x16x32_bf16 v[208:211], v[172:175], v[12:15], 0\n\t"
      "s_waitcnt vmcnt(5)\n\t"
      "v_mfma_f32_16x16x32_bf16 v[196:199], v[176:179], v[16:19], v[196:199]\n\t"
      "v_mfma_f32_16x16x32_bf16 v[200:203], v[176:179], v[20:23], v[200:203]\n\t"
      "v_mfma_f32_16x16x32_bf16 v[204:207], v[176:179], v[24:27], v[204:207]\n\t"
      "s_waitcnt lgkmcnt(0)\n\t"
      "s_barrier\n\t"
      "ds_read_b64 v[228:229], v246 offset:32768\n\t"
      "ds_read_b64 v[230:231], v246 offset:36864\n\t"
      "s_add_u32 s58, s74, s40\n\t"
      "s_addc_u32 s59, s75, 0\n\t"
      "global_load_dwordx4 v[132:135], v244, s[58:59] sc1\n\t"
      "v_mfma_f32_16x16x32_bf16 v[208:211], v[176:179], v[28:31], v[208:211]\n\t"
      "ds_read_b64 v[232:233], v246 offset:40960\n\t"
      "ds_read_b64 v[234:235], v246 offset:45056\n\t"
      "s_waitcnt vmcnt(5)\n\t"
      "v_mfma_f32_16x16x32_bf16 v[196:199], v[180:183], v[32:35], v[196:199]\n\t"
      "ds_read_b64 v[236:237], v246 offset:49152\n\t"
      "ds_read_b64 v[238:239], v246 offset:53248\n\t"
      "v_mfma_f32_16x16x32_bf16 v[200:203], v[180:183], v[36:39], v[200:203]\n\t"
      "ds_read_b64 v[240:241], v246 offset:57344\n\t"
      "ds_read_b64 v[242:243], v246 offset:61440\n\t"
      "v_lshlrev_b32_e32 v248, 16, v115\n\t"
      "v_mfma_f32_16x16x32_bf16 v[204:207], v[180:183], v[40:43], v[204:207]\n\t"
      "v_and_b32_e32 v249, 0xffff0000, v115\n\t"
      "s_add_u32 s58, s74, s41\n\t"
      "s_addc_u32 s59, s75, 0\n\t"
      "global_load_dwordx4 v[136:139], v244, s[58:59] sc1\n\t"
      "v_mfma_f32_16x16x32_bf16 v[208:211], v[180:183], v[44:47], v[208:211]\n\t"
      "v_add_f32_e32 v248, v254, v248\n\t"
      "v_add_f32_e32 v249, v254, v249\n\t"
      "s_waitcnt vmcnt(5)\n\t"
      "v_mfma_f32_16x16x32_bf16 v[196:199], v[184:187], v[48:51], v[196:199]\n\t"
      "s_waitcnt lgkmcnt(4)\n\t"
      "v_mfma_f32_16x16x32_bf16 v[200:203], v[184:187], v[52:55], v[200:203]\n\t"
      "v_add_f32_e32 v228, v228, v230\n\t"
      "v_add_f32_e32 v232, v232, v234\n\t"
      "v_mfma_f32_16x16x32_bf16 v[204:207], v[184:187], v[56:59], v[204:207]\n\t"
      "v_add_f32_e32 v229, v229, v231\n\t"
      "s_add_u32 s58, s74, s42\n\t"
      "s_addc_u32 s59, s75, 0\n\t"
      "global_load_dwordx4 v[140:143], v244, s[58:59] sc1\n\t"
      "v_mfma_f32_16x16x32_bf16 v[208:211], v[184:187], v[60:63], v[208:211]\n\t"
      "v_add_f32_e32 v233, v233, v235\n\t"
      "s_waitcnt lgkmcnt(0)\n\t"
      "s_waitcnt vmcnt(5)\n\t"
      "v_mfma_f32_16x16x32_bf16 v[196:199], v[188:191], v[64:67], v[196:199]\n\t"
      "v_add_f32_e32 v236, v236, v238\n\t"
      "v_mfma_f32_16x16x32_bf16 v[200:203], v[188:191], v[68:71], v[200:203]\n\t"
      "v_add_f32_e32 v240, v240, v242\n\t"
      "v_add_f32_e32 v237, v237, v239\n\t"
      "v_mfma_f32_16x16x32_bf16 v[204:207], v[188:191], v[72:75], v[204:207]\n\t"
      "v_add_f32_e32 v241, v241, v243\n\t"
      "s_add_u32 s58, s74, s43\n\t"
      "s_addc_u32 s59, s75, 0\n\t"
      "global_load_dwordx4 v[144:147], v244, s[58:59] sc1\n\t"
      "v_mfma_f32_16x16x32_bf16 v[208:211], v[188:191], v[76:79], v[208:211]\n\t"
      "v_add_f32_e32 v228, v228, v232\n\t"
      "v_add_f32_e32 v236, v236, v240\n\t"
      "s_waitcnt vmcnt(5)\n\t"
      "v_mfma_f32_16x16x32_bf16 v[196:199], v[192:195], v[80:83], v[196:199]\n\t"
      "v_add_f32_e32 v229, v229, v233\n\t"
      "v_mfma_f32_16x16x32_bf16 v[200:203], v[192:195], v[84:87], v[200:203]\n\t"
      "v_add_f32_e32 v237, v237, v241\n\t"
      "v_add_f32_e32 v228, v228, v248\n\t"
      "v_mfma_f32_16x16x32_bf16 v[204:207], v[192:195], v[88:91], v[204:207]\n\t"
      "v_add_f32_e32 v229, v229, v249\n\t"
      "s_add_u32 s58, s74, s44\n\t"
      "s_addc_u32 s59, s75, 0\n\t"
      "global_load_dwordx4 v[148:151], v244, s[58:59] sc1\n\t"
      "v_mfma_f32_16x16x32_bf16 v[208:211], v[192:195], v[92:95], v[208:211]\n\t"
      "v_add_f32_e32 v228, v228, v236\n\t"
      "v_add_f32_e32 v229, v229, v237\n\t"
      "s_waitcnt vmcnt(5)\n\t"
      "v_mfma_f32_16x16x32_bf16 v[196:199], v[128:131], v[96:99], v[196:199]\n\t"
      "v_cvt_pk_bf16_f32 v232, v228, v229\n\t"
      "v_mfma_f32_16x16x32_bf16 v[200:203], v[128:131], v[100:103], v[200:203]\n\t"
      "s_mov_b32 vcc_lo, 0xeeeeeeee\n\t"
      "s_mov_b32 vcc_hi, 0xeeeeeeee\n\t"
      "v_cndmask_b32_dpp v124, v232, v124, vcc quad_perm:[0,0,0,0] row_mask:0xf bank_mask:0xf\n\t"
      "v_cndmask_b32_dpp v125, v232, v125, vcc quad_perm:[1,1,1,1] row_mask:0xf bank_mask:0xf\n\t"
      "v_cndmask_b32_dpp v126, v232, v126, vcc quad_perm:[2,2,2,2] row_mask:0xf bank_mask:0xf\n\t"
      "v_cndmask_b32_dpp v127, v232, v127, vcc quad_perm:[3,3,3,3] row_mask:0xf bank_mask:0xf\n\t"
      "v_mfma_f32_16x16x32_bf16 v[204:207], v[128:131], v[104:107], v[204:207]\n\t"
      "s_add_u32 s58, s74, s45\n\t"
      "s_addc_u32 s59, s75, 0\n\t"
      "global_load_dwordx4 v[152:155], v244, s[58:59] sc1\n\t"
      "v_mfma_f32_16x16x32_bf16 v[208:211], v[128:131], v[108:111], v[208:211]\n\t"
      "v_add_f32_e32 v251, v196, v197\n\t"
      "v_add_f32_e32 v251, v251, v198\n\t"
      "v_cmp_u_f32_e32 vcc, v251, v199\n\t"
      "s_cbranch_vccnz LA44R_%=\n\t"
      "LA44B_%=:\n\t"
      "ds_write_b128 v245, v[196:199]\n\t"
      "s_waitcnt vmcnt(5)\n\t"
      "v_mfma_f32_16x16x32_bf16 v[212:215], v[132:135], v[0:3], 0\n\t"
      "ds_write_b128 v245, v[200:203] offset:1024\n\t"
      "v_mfma_f32_16x16x32_bf16 v[216:219], v[132:135], v[4:7], 0\n\t"
      "ds_write_b128 v245, v[204:207] offset:2048\n\t"
      "v_mfma_f32_16x16x32_bf16 v[220:223], v[132:135], v[8:11], 0\n\t"
      "ds_write_b128 v245, v[208:211] offset:3072\n\t"
      "s_add_u32 s58, s74, s46\n\t"
      "s_addc_u32 s59, s75, 0\n\t"
      "global_load_dwordx4 v[156:159], v244, s[58:59] sc1\n\t"
      "v_mfma_f32_16x16x32_bf16 v[224:227], v[132:135], v[12:15], 0\n\t"
      "s_waitcnt vmcnt(5)\n\t"
      "v_mfma_f32_16x16x32_bf16 v[212:215], v[136:139], v[16:19], v[212:215]\n\t"
      "v_mfma_f32_16x16x32_bf16 v[216:219], v[136:139], v[20:23], v[216:219]\n\t"
      "v_mfma_f32_16x16x32_bf16 v[220:223], v[136:139], v[24:27], v[220:223]\n\t"
      "s_waitcnt lgkmcnt(0)\n\t"
      "s_barrier\n\t"
      "ds_read_b64 v[228:229], v246\n\t"
      "ds_read_b64 v[230:231], v246 offset:4096\n\t"
      "s_add_u32 s58, s76, s40\n\t"
      "s_addc_u32 s59, s77, 0\n\t"
      "global_load_dwordx4 v[160:163], v244, s[58:59] sc1\n\t"
      "v_mfma_f32_16x16x32_bf16 v[224:227], v[136:139], v[28:31], v[224:227]\n\t"
      "ds_read_b64 v[232:233], v246 offset:8192\n\t"
      "ds_read_b64 v[234:235], v246 offset:12288\n\t"
      "s_waitcnt vmcnt(5)\n\t"
      "v_mfma_f32_16x16x32_bf16 v[212:215], v[140:143], v[32:35], v[212:215]\n\t"
      "ds_read_b64 v[236:237], v246 offset:16384\n\t"
      "ds_read_b64 v[238:239], v246 offset:20480\n\t"
      "v_mfma_f32_16x16x32_bf16 v[216:219], v[140:143], v[36:39], v[216:219]\n\t"
      "ds_read_b64 v[240:241], v246 offset:24576\n\t"
      "ds_read_b64 v[242:243], v246 offset:28672\n\t"
      "v_lshlrev_b32_e32 v248, 16, v116\n\t"
      "v_mfma_f32_16x16x32_bf16 v[220:223], v[140:143], v[40:43], v[220:223]\n\t"
      "v_and_b32_e32 v249, 0xffff0000, v116\n\t"
      "s_add_u32 s58, s76, s41\n\t"
      "s_addc_u32 s59, s77, 0\n\t"
      "global_load_dwordx4 v[164:167], v244, s[58:59] sc1\n\t"
      "v_mfma_f32_16x16x32_bf16 v[224:227], v[140:143], v[44:47], v[224:227]\n\t"
      "v_add_f32_e32 v248, v254, v248\n\t"
      "v_add_f32_e32 v249, v254, v249\n\t"
      "s_waitcnt vmcnt(5)\n\t"
      "v_mfma_f32_16x16x32_bf16 v[212:215], v[144:147], v[48:51], v[212:215]\n\t"
      "s_waitcnt lgkmcnt(4)\n\t"
      "v_mfma_f32_16x16x32_bf16 v[216:219], v[144:147], v[52:55], v[216:219]\n\t"
      "v_add_f32_e32 v228, v228, v230\n\t"
      "v_add_f32_e32 v232, v232, v234\n\t"
      "v_mfma_f32_16x16x32_bf16 v[220:223], v[144:147], v[56:59], v[220:223]\n\t"
      "v_add_f32_e32 v229, v229, v231\n\t"
      "s_add_u32 s58, s76, s42\n\t"
      "s_addc_u32 s59, s77, 0\n\t"
      "global_load_dwordx4 v[168:171], v244, s[58:59] sc1\n\t"
      "v_mfma_f32_16x16x32_bf16 v[224:227], v[144:147], v[60:63], v[224:227]\n\t"
      "v_add_f32_e32 v233, v233, v235\n\t"
      "s_waitcnt lgkmcnt(0)\n\t"
      "s_waitcnt vmcnt(5)\n\t"
      "v_mfma_f32_16x16x32_bf16 v[212:215], v[148:151], v[64:67], v[212:215]\n\t"
      "v_add_f32_e32 v236, v236, v238\n\t"
      "v_mfma_f32_16x16x32_bf16 v[216:219], v[148:151], v[68:71], v[216:219]\n\t"
      "v_add_f32_e32 v240, v240, v242\n\t"
      "v_add_f32_e32 v237, v237, v239\n\t"
      "v_mfma_f32_16x16x32_bf16 v[220:223], v[148:151], v[72:75], v[220:223]\n\t"
      "v_add_f32_e32 v241, v241, v243\n\t"
      "s_add_u32 s58, s76, s43\n\t"
      "s_addc_u32 s59, s77, 0\n\t"
      "global_load_dwordx4 v[172:175], v244, s[58:59] sc1\n\t"
      "v_mfma_f32_16x16x32_bf16 v[224:227], v[148:151], v[76:79], v[224:227]\n\t"
      "v_add_f32_e32 v228, v228, v232\n\t"
      "v_add_f32_e32 v236, v236, v240\n\t"
      "s_waitcnt vmcnt(5)\n\t"
      "v_mfma_f32_16x16x32_bf16 v[212:215], v[152:155], v[80:83], v[212:215]\n\t"
      "v_add_f32_e32 v229, v229, v233\n\t"
      "v_mfma_f32_16x16x32_bf16 v[216:219], v[152:155], v[84:87], v[216:219]\n\t"
      "v_add_f32_e32 v237, v237, v241\n\t"
      "v_add_f32_e32 v228, v228, v248\n\t"
      "v_mfma_f32_16x16x32_bf16 v[220:223], v[152:155], v[88:91], v[220:223]\n\t"
      "v_add_f32_e32 v229, v229, v249\n\t"
      "s_add_u32 s58, s76, s44\n\t"
      "s_addc_u32 s59, s77, 0\n\t"
      "global_load_dwordx4 v[176:179], v244, s[58:59] sc1\n\t"
      "v_mfma_f32_16x16x32_bf16 v[224:227], v[152:155], v[92:95], v[224:227]\n\t"
      "v_add_f32_e32 v228, v228, v236\n\t"
      "v_add_f32_e32 v229, v229, v237\n\t"
      "s_waitcnt vmcnt(5)\n\t"
      "v_mfma_f32_16x16x32_bf16 v[212:215], v[156:159], v[96:99], v[212:215]\n\t"
      "v_cvt_pk_bf16_f32 v232, v228, v229\n\t"
      "v_mfma_f32_16x16x32_bf16 v[216:219], v[156:159], v[100:103], v[216:219]\n\t"
      "s_mov_b32 vcc_lo, 0xdddddddd\n\t"
      "s_mov_b32 vcc_hi, 0xdddddddd\n\t"
      "v_cndmask_b32_dpp v124, v232, v124, vcc quad_perm:[0,0,0,0] row_mask:0xf bank_mask:0xf\n\t"
      "v_cndmask_b32_dpp v125, v232, v125, vcc quad_perm:[1,1,1,1] row_mask:0xf bank_mask:0xf\n\t"
      "v_cndmask_b32_dpp v126, v232, v126, vcc quad_perm:[2,2,2,2] row_mask:0xf bank_mask:0xf\n\t"
      "v_cndmask_b32_dpp v127, v232, v127, vcc quad_perm:[3,3,3,3] row_mask:0xf bank_mask:0xf\n\t"
      "v_mfma_f32_16x16x32_bf16 v[220:223], v[156:159], v[104:107], v[220:223]\n\t"
      "s_add_u32 s58, s76, s45\n\t"
      "s_addc_u32 s59, s77, 0\n\t"
      "global_load_dwordx4 v[180:183], v244, s[58:59] sc1\n\t"
      "v_mfma_f32_16x16x32_bf16 v[224:227], v[156:159], v[108:111], v[224:227]\n\t"
      "v_add_f32_e32 v251, v212, v213\n\t"
      "v_add_f32_e32 v251, v251, v214\n\t"
      "v_cmp_u_f32_e32 vcc, v251, v215\n\t"
      "s_cbranch_vccnz LA45R_%=\n\t"
      "LA45B_%=:\n\t"
      "ds_write_b128 v245, v[212:215] offset:32768\n\t"
      "s_waitcnt vmcnt(5)\n\t"
      "v_mfma_f32_16x16x32_bf16 v[196:199], v[160:163], v[0:3], 0\n\t"
      "ds_write_b128 v245, v[216:219] offset:33792\n\t"
      "v_mfma_f32_16x16x32_bf16 v[200:203], v[160:163], v[4:7], 0\n\t"
      "ds_write_b128 v245, v[220:223] offset:34816\n\t"
      "v_mfma_f32_16x16x32_bf16 v[204:207], v[160:163], v[8:11], 0\n\t"
      "ds_write_b128 v245, v[224:227] offset:35840\n\t"
      "s_add_u32 s58, s76, s46\n\t"
      "s_addc_u32 s59, s77, 0\n\t"
      "global_load_dwordx4 v[184:187], v244, s[58:59] sc1\n\t"
      "v_mfma_f32_16x16x32_bf16 v[208:211], v[160:163], v[12:15], 0\n\t"
      "s_waitcnt vmcnt(5)\n\t"
      "v_mfma_f32_16x16x32_bf16 v[196:199], v[164:167], v[16:19], v[196:199]\n\t"
      "v_mfma_f32_16x16x32_bf16 v[200:203], v[164:167], v[20:23], v[200:203]\n\t"
      "v_mfma_f32_16x16x32_bf16 v[204:207], v[164:167], v[24:27], v[204:207]\n\t"
      "s_waitcnt lgkmcnt(0)\n\t"
      "s_barrier\n\t"
      "ds_read_b64 v[228:229], v246 offset:32768\n\t"
      "ds_read_b64 v[230:231], v246 offset:36864\n\t"
      "v_mfma_f32_16x16x32_bf16 v[208:211], v[164:167], v[28:31], v[208:211]\n\t"
      "ds_read_b64 v[232:233], v246 offset:40960\n\t"
      "ds_read_b64 v[234:235], v246 offset:45056\n\t"
      "s_waitcnt vmcnt(4)\n\t"
      "v_mfma_f32_16x16x32_bf16 v[196:199], v[168:171], v[32:35], v[196:199]\n\t"
      "ds_read_b64 v[236:237], v246 offset:49152\n\t"
      "ds_read_b64 v[238:239], v246 offset:53248\n\t"
      "v_mfma_f32_16x16x32_bf16 v[200:203], v[168:171], v[36:39], v[200:203]\n\t"
      "ds_read_b64 v[240:241], v246 offset:57344\n\t"
      "ds_read_b64 v[242:243], v246 offset:61440\n\t"
      "v_lshlrev_b32_e32 v248, 16, v117\n\t"
      "v_and_b32_e32 v249, 0xffff0000, v117\n\t"
      "v_mfma_f32_16x16x32_bf16 v[204:207], v[168:171], v[40:43], v[204:207]\n\t"
      "v_add_f32_e32 v248, v254, v248\n\t"
      "v_add_f32_e32 v249, v254, v249\n\t"
      "v_mfma_f32_16x16x32_bf16 v[208:211], v[168:171], v[44:47], v[208:211]\n\t"
      "s_waitcnt lgkmcnt(4)\n\t"
      "v_add_f32_e32 v228, v228, v230\n\t"
      "v_add_f32_e32 v232, v232, v234\n\t"
      "s_waitcnt vmcnt(3)\n\t"
      "v_mfma_f32_16x16x32_bf16 v[196:199], v[172:175], v[48:51], v[196:199]\n\t"
      "v_add_f32_e32 v229, v229, v231\n\t"
      "v_add_f32_e32 v233, v233, v235\n\t"
      "v_mfma_f32_16x16x32_bf16 v[200:203], v[172:175], v[52:55], v[200:203]\n\t"
      "s_waitcnt lgkmcnt(0)\n\t"
      "v_add_f32_e32 v236, v236, v238\n\t"
      "v_add_f32_e32 v240, v240, v242\n\t"
      "v_mfma_f32_16x16x32_bf16 v[204:207], v[172:175], v[56:59], v[204:207]\n\t"
      "v_add_f32_e32 v237, v237, v239\n\t"
      "v_add_f32_e32 v241, v241, v243\n\t"
      "v_mfma_f32_16x16x32_bf16 v[208:211], v[172:175], v[60:63], v[208:211]\n\t"
      "v_add_f32_e32 v228, v228, v232\n\t"
      "v_add_f32_e32 v236, v236, v240\n\t"
      "v_add_f32_e32 v229, v229, v233\n\t"
      "s_waitcnt vmcnt(2)\n\t"
      "v_mfma_f32_16x16x32_bf16 v[196:199], v[176:179], v[64:67], v[196:199]\n\t"
      "v_add_f32_e32 v237, v237, v241\n\t"
      "v_add_f32_e32 v228, v228, v248\n\t"
      "v_mfma_f32_16x16x32_bf16 v[200:203], v[176:179], v[68:71], v[200:203]\n\t"
      "v_add_f32_e32 v229, v229, v249\n\t"
      "v_add_f32_e32 v228, v228, v236\n\t"
      "v_add_f32_e32 v229, v229, v237\n\t"
      "v_mfma_f32_16x16x32_bf16 v[204:207], v[176:179], v[72:75], v[204:207]\n\t"
      "v_cvt_pk_bf16_f32 v232, v228, v229\n\t"
      "s_mov_b32 vcc_lo, 0xbbbbbbbb\n\t"
      "s_mov_b32 vcc_hi, 0xbbbbbbbb\n\t"
      "v_cndmask_b32_dpp v124, v232, v124, vcc quad_perm:[0,0,0,0] row_mask:0xf bank_mask:0xf\n\t"
      "v_cndmask_b32_dpp v125, v232, v125, vcc quad_perm:[1,1,1,1] row_mask:0xf bank_mask:0xf\n\t"
      "v_cndmask_b32_dpp v126, v232, v126, vcc quad_perm:[2,2,2,2] row_mask:0xf bank_mask:0xf\n\t"
      "v_cndmask_b32_dpp v127, v232, v127, vcc quad_perm:[3,3,3,3] row_mask:0xf bank_mask:0xf\n\t"
      "v_mfma_f32_16x16x32_bf16 v[208:211], v[176:179], v[76:79], v[208:211]\n\t"
      "s_waitcnt vmcnt(1)\n\t"
      "v_mfma_f32_16x16x32_bf16 v[196:199], v[180:183], v[80:83], v[196:199]\n\t"
      "v_mfma_f32_16x16x32_bf16 v[200:203], v[180:183], v[84:87], v[200:203]\n\t"
      "v_mfma_f32_16x16x32_bf16 v[204:207], v[180:183], v[88:91], v[204:207]\n\t"
      "v_mfma_f32_16x16x32_bf16 v[208:211], v[180:183], v[92:95], v[208:211]\n\t"
      "s_waitcnt vmcnt(0)\n\t"
      "v_mfma_f32_16x16x32_bf16 v[196:199], v[184:187], v[96:99], v[196:199]\n\t"
      "v_mfma_f32_16x16x32_bf16 v[200:203], v[184:187], v[100:103], v[200:203]\n\t"
      "v_mfma_f32_16x16x32_bf16 v[204:207], v[184:187], v[104:107], v[204:207]\n\t"
      "v_mfma_f32_16x16x32_bf16 v[208:211], v[184:187], v[108:111], v[208:211]\n\t"
      "s_nop 4\n\t"
      "v_add_f32_e32 v251, v196, v197\n\t"
      "v_add_f32_e32 v251, v251, v198\n\t"
      "v_cmp_u_f32_e32 vcc, v251, v199\n\t"
      "s_cbranch_vccnz LA46R_%=\n\t"
      "LA46B_%=:\n\t"
      "ds_write_b128 v245, v[196:199]\n\t"
      "s_mov_b64 exec, s[82:83]\n\t"
      "global_store_dwordx4 v247, v[120:123], s[18:19] sc1\n\t"
      "s_mov_b64 exec, -1\n\t"
      "ds_write_b128 v245, v[200:203] offset:1024\n\t"
      "ds_write_b128 v245, v[204:207] offset:2048\n\t"
      "ds_write_b128 v245, v[208:211] offset:3072\n\t"
      "s_waitcnt lgkmcnt(0)\n\t"
      "s_barrier\n\t"
      "ds_read_b64 v[228:229], v246\n\t"
      "ds_read_b64 v[230:231], v246 offset:4096\n\t"
      "ds_read_b64 v[232:233], v246 offset:8192\n\t"
      "ds_read_b64 v[234:235], v246 offset:12288\n\t"
      "ds_read_b64 v[236:237], v246 offset:16384\n\t"
      "ds_read_b64 v[238:239], v246 offset:20480\n\t"
      "ds_read_b64 v[240:241], v246 offset:24576\n\t"
      "ds_read_b64 v[242:243], v246 offset:28672\n\t"
      "v_lshlrev_b32_e32 v248, 16, v118\n\t"
      "v_and_b32_e32 v249, 0xffff0000, v118\n\t"
      "v_add_f32_e32 v248, v254, v248\n\t"
      "v_add_f32_e32 v249, v254, v249\n\t"
      "s_waitcnt lgkmcnt(4)\n\t"
      "v_add_f32_e32 v228, v228, v230\n\t"
      "v_add_f32_e32 v232, v232, v234\n\t"
      "v_add_f32_e32 v229, v229, v231\n\t"
      "v_add_f32_e32 v233, v233, v235\n\t"
      "s_waitcnt lgkmcnt(0)\n\t"
      "v_add_f32_e32 v236, v236, v238\n\t"
      "v_add_f32_e32 v240, v240, v242\n\t"
      "v_add_f32_e32 v237, v237, v239\n\t"
      "v_add_f32_e32 v241, v241, v243\n\t"
      "v_add_f32_e32 v228, v228, v232\n\t"
      "v_add_f32_e32 v236, v236, v240\n\t"
      "v_add_f32_e32 v229, v229, v233\n\t"
      "v_add_f32_e32 v237, v237, v241\n\t"
      "v_add_f32_e32 v228, v228, v248\n\t"
      "v_add_f32_e32 v229, v229, v249\n\t"
      "v_add_f32_e32 v228, v228, v236\n\t"
      "v_add_f32_e32 v229, v229, v237\n\t"
      "v_cvt_pk_bf16_f32 v232, v228, v229\n\t"
      "s_mov_b32 vcc_lo, 0x77777777\n\t"
      "s_mov_b32 vcc_hi, 0x77777777\n\t"
      "v_cndmask_b32_dpp v124, v232, v124, vcc quad_perm:[0,0,0,0] row_mask:0xf bank_mask:0xf\n\t"
      "v_cndmask_b32_dpp v125, v232, v125, vcc quad_perm:[1,1,1,1] row_mask:0xf bank_mask:0xf\n\t"
      "v_cndmask_b32_dpp v126, v232, v126, vcc quad_perm:[2,2,2,2] row_mask:0xf bank_mask:0xf\n\t"
      "v_cndmask_b32_dpp v127, v232, v127, vcc quad_perm:[3,3,3,3] row_mask:0xf bank_mask:0xf\n\t"
      "global_store_dwordx4 v247, v[124:127], s[62:63] sc1\n\t"
      "s_endpgm\n\t"
      "LO0R_%=:\n\t"
      "s_mov_b32 s80, 0\n\t"
      "LO0L_%=:\n\t"
      "s_add_u32 s80, s80, 1\n\t"
      "s_cmp_lt_u32 s80, 262144\n\t"
      "s_cbranch_scc0 LO0X_%=\n\t"
      "s_waitcnt vmcnt(0)\n\t"
      "s_sleep 1\n\t"
      "global_load_dwordx4 v[128:131], v244, s[42:43] offset:-2048 sc1\n\t"
      "global_load_dwordx4 v[132:135], v244, s[42:43] offset:-1024 sc1\n\t"
      "global_load_dwordx4 v[136:139], v244, s[42:43] sc1\n\t"
      "global_load_dwordx4 v[140:143], v244, s[42:43] offset:1024 sc1\n\t"
      "global_load_dwordx4 v[144:147], v244, s[42:43] offset:2048 sc1\n\t"
      "global_load_dwordx4 v[148:151], v244, s[42:43] offset:3072 sc1\n\t"
      "global_load_dwordx4 v[152:155], v244, s[44:45] offset:-4096 sc1\n\t"
      "global_load_dwordx4 v[156:159], v244, s[44:45] offset:-3072 sc1\n\t"
      "global_load_dwordx4 v[160:163], v244, s[44:45] offset:-2048 sc1\n\t"
      "global_load_dwordx4 v[164:167], v244, s[44:45] offset:-1024 sc1\n\t"
      "global_load_dwordx4 v[168:171], v244, s[44:45] sc1\n\t"
      "global_load_dwordx4 v[172:175], v244, s[44:45] offset:1024 sc1\n\t"
      "global_load_dwordx4 v[176:179], v244, s[44:45] offset:2048 sc1\n\t"
      "global_load_dwordx4 v[180:183], v244, s[44:45] offset:3072 sc1\n\t"
      "global_load_dwordx4 v[184:187], v244, s[42:43] offset:-4096 sc1\n\t"
      "global_load_dwordx4 v[188:191], v244, s[42:43] offset:-3072 sc1\n\t"
      "s_waitcnt vmcnt(0)\n\t"
      "v_max3_u32 v232, v128, v129, v130\n\t"
      "v_max3_u32 v232, v232, v131, v132\n\t"
      "v_max3_u32 v232, v232, v133, v134\n\t"
      "v_max_u32_e32 v232, v232, v135\n\t"
      "v_cmp_eq_u32_e32 vcc, -1, v232\n\t"
      "s_cbranch_vccnz LO0L_%=\n\t"
      "LO0X_%=:\n\t"
      "s_branch LO0B_%=\n\t"
      "LO2R_%=:\n\t"
      "s_mov_b32 s80, 0\n\t"
      "LO2L_%=:\n\t"
      "s_add_u32 s80, s80, 1\n\t"
      "s_cmp_lt_u32 s80, 262144\n\t"
      "s_cbranch_scc0 LO2X_%=\n\t"
      "s_waitcnt vmcnt(0)\n\t"
      "s_sleep 1\n\t"
      "global_load_dwordx4 v[136:139], v244, s[42:43] sc1\n\t"
      "global_load_dwordx4 v[140:143], v244, s[42:43] offset:1024 sc1\n\t"
      "global_load_dwordx4 v[144:147], v244, s[42:43] offset:2048 sc1\n\t"
      "global_load_dwordx4 v[148:151], v244, s[42:43] offset:3072 sc1\n\t"
      "global_load_dwordx4 v[152:155], v244, s[44:45] offset:-4096 sc1\n\t"
      "global_load_dwordx4 v[156:159], v244, s[44:45] offset:-3072 sc1\n\t"
      "global_load_dwordx4 v[160:163], v244, s[44:45] offset:-2048 sc1\n\t"
      "global_load_dwordx4 v[164:167], v244, s[44:45] offset:-1024 sc1\n\t"
      "global_load_dwordx4 v[168:171], v244, s[44:45] sc1\n\t"
      "global_load_dwordx4 v[172:175], v244, s[44:45] offset:1024 sc1\n\t"
      "global_load_dwordx4 v[176:179], v244, s[44:45] offset:2048 sc1\n\t"
      "global_load_dwordx4 v[180:183], v244, s[44:45] offset:3072 sc1\n\t"
      "global_load_dwordx4 v[184:187], v244, s[42:43] offset:-4096 sc1\n\t"
      "global_load_dwordx4 v[188:191], v244, s[42:43] offset:-3072 sc1\n\t"
      "s_waitcnt vmcnt(0)\n\t"
      "v_max3_u32 v232, v136, v137, v138\n\t"
      "v_max3_u32 v232, v232, v139, v140\n\t"
      "v_max3_u32 v232, v232, v141, v142\n\t"
      "v_max_u32_e32 v232, v232, v143\n\t"
      "v_cmp_eq_u32_e32 vcc, -1, v232\n\t"
      "s_cbranch_vccnz LO2L_%=\n\t"
      "LO2X_%=:\n\t"
      "s_branch LO2B_%=\n\t"
      "LO4R_%=:\n\t"
      "s_mov_b32 s80, 0\n\t"
      "LO4L_%=:\n\t"
      "s_add_u32 s80, s80, 1\n\t"
      "s_cmp_lt_u32 s80, 262144\n\t"
      "s_cbranch_scc0 LO4X_%=\n\t"
      "s_waitcnt vmcnt(0)\n\t"
      "s_sleep 1\n\t"
      "global_load_dwordx4 v[144:147], v244, s[42:43] offset:2048 sc1\n\t"
      "global_load_dwordx4 v[148:151], v244, s[42:43] offset:3072 sc1\n\t"
      "global_load_dwordx4 v[152:155], v244, s[44:45] offset:-4096 sc1\n\t"
      "global_load_dwordx4 v[156:159], v244, s[44:45] offset:-3072 sc1\n\t"
      "global_load_dwordx4 v[160:163], v244, s[44:45] offset:-2048 sc1\n\t"
      "global_load_dwordx4 v[164:167], v244, s[44:45] offset:-1024 sc1\n\t"
      "global_load_dwordx4 v[168:171], v244, s[44:45] sc1\n\t"
      "global_load_dwordx4 v[172:175], v244, s[44:45] offset:1024 sc1\n\t"
      "global_load_dwordx4 v[176:179], v244, s[44:45] offset:2048 sc1\n\t"
      "global_load_dwordx4 v[180:183], v244, s[44:45] offset:3072 sc1\n\t"
      "global_load_dwordx4 v[184:187], v244, s[42:43] offset:-4096 sc1\n\t"
      "global_load_dwordx4 v[188:191], v244, s[42:43] offset:-3072 sc1\n\t"
      "s_waitcnt vmcnt(0)\n\t"
      "v_max3_u32 v232, v144, v145, v146\n\t"
      "v_max3_u32 v232, v232, v147, v148\n\t"
      "v_max3_u32 v232, v232, v149, v150\n\t"
      "v_max_u32_e32 v232, v232, v151\n\t"
      "v_cmp_eq_u32_e32 vcc, -1, v232\n\t"
      "s_cbranch_vccnz LO4L_%=\n\t"
      "LO4X_%=:\n\t"
      "s_branch LO4B_%=\n\t"
      "LO6R_%=:\n\t"
      "s_mov_b32 s80, 0\n\t"
      "LO6L_%=:\n\t"
      "s_add_u32 s80, s80, 1\n\t"
      "s_cmp_lt_u32 s80, 262144\n\t"
      "s_cbranch_scc0 LO6X_%=\n\t"
      "s_waitcnt vmcnt(0)\n\t"
      "s_sleep 1\n\t"
      "global_load_dwordx4 v[152:155], v244, s[44:45] offset:-4096 sc1\n\t"
      "global_load_dwordx4 v[156:159], v244, s[44:45] offset:-3072 sc1\n\t"
      "global_load_dwordx4 v[160:163], v244, s[44:45] offset:-2048 sc1\n\t"
      "global_load_dwordx4 v[164:167], v244, s[44:45] offset:-1024 sc1\n\t"
      "global_load_dwordx4 v[168:171], v244, s[44:45] sc1\n\t"
      "global_load_dwordx4 v[172:175], v244, s[44:45] offset:1024 sc1\n\t"
      "global_load_dwordx4 v[176:179], v244, s[44:45] offset:2048 sc1\n\t"
      "global_load_dwordx4 v[180:183], v244, s[44:45] offset:3072 sc1\n\t"
      "global_load_dwordx4 v[184:187], v244, s[42:43] offset:-4096 sc1\n\t"
      "global_load_dwordx4 v[188:191], v244, s[42:43] offset:-3072 sc1\n\t"
      "s_waitcnt vmcnt(0)\n\t"
      "v_max3_u32 v232, v152, v153, v154\n\t"
      "v_max3_u32 v232, v232, v155, v156\n\t"
      "v_max3_u32 v232, v232, v157, v158\n\t"
      "v_max_u32_e32 v232, v232, v159\n\t"
      "v_cmp_eq_u32_e32 vcc, -1, v232\n\t"
      "s_cbranch_vccnz LO6L_%=\n\t"
      "LO6X_%=:\n\t"
      "s_branch LO6B_%=\n\t"
      "LO8R_%=:\n\t"
      "s_mov_b32 s80, 0\n\t"
      "LO8L_%=:\n\t"
      "s_add_u32 s80, s80, 1\n\t"
      "s_cmp_lt_u32 s80, 262144\n\t"
      "s_cbranch_scc0 LO8X_%=\n\t"
      "s_waitcnt vmcnt(0)\n\t"
      "s_sleep 1\n\t"
      "global_load_dwordx4 v[160:163], v244, s[44:45] offset:-2048 sc1\n\t"
      "global_load_dwordx4 v[164:167], v244, s[44:45] offset:-1024 sc1\n\t"
      "global_load_dwordx4 v[168:171], v244, s[44:45] sc1\n\t"
      "global_load_dwordx4 v[172:175], v244, s[44:45] offset:1024 sc1\n\t"
      "global_load_dwordx4 v[176:179], v244, s[44:45] offset:2048 sc1\n\t"
      "global_load_dwordx4 v[180:183], v244, s[44:45] offset:3072 sc1\n\t"
      "global_load_dwordx4 v[184:187], v244, s[42:43] offset:-4096 sc1\n\t"
      "global_load_dwordx4 v[188:191], v244, s[42:43] offset:-3072 sc1\n\t"
      "s_waitcnt vmcnt(0)\n\t"
      "v_max3_u32 v232, v160, v161, v162\n\t"
      "v_max3_u32 v232, v232, v163, v164\n\t"
      "v_max3_u32 v232, v232, v165, v166\n\t"
      "v_max_u32_e32 v232, v232, v167\n\t"
      "v_cmp_eq_u32_e32 vcc, -1, v232\n\t"
      "s_cbranch_vccnz LO8L_%=\n\t"
      "LO8X_%=:\n\t"
      "s_branch LO8B_%=\n\t"
      "LO10R_%=:\n\t"
      "s_mov_b32 s80, 0\n\t"
      "LO10L_%=:\n\t"
      "s_add_u32 s80, s80, 1\n\t"
      "s_cmp_lt_u32 s80, 262144\n\t"
      "s_cbranch_scc0 LO10X_%=\n\t"
      "s_waitcnt vmcnt(0)\n\t"
      "s_sleep 1\n\t"
      "global_load_dwordx4 v[168:171], v244, s[44:45] sc1\n\t"
      "global_load_dwordx4 v[172:175], v244, s[44:45] offset:1024 sc1\n\t"
      "global_load_dwordx4 v[176:179], v244, s[44:45] offset:2048 sc1\n\t"
      "global_load_dwordx4 v[180:183], v244, s[44:45] offset:3072 sc1\n\t"
      "global_load_dwordx4 v[184:187], v244, s[42:43] offset:-4096 sc1\n\t"
      "global_load_dwordx4 v[188:191], v244, s[42:43] offset:-3072 sc1\n\t"
      "s_waitcnt vmcnt(0)\n\t"
      "v_max3_u32 v232, v168, v169, v170\n\t"
      "v_max3_u32 v232, v232, v171, v172\n\t"
      "v_max3_u32 v232, v232, v173, v174\n\t"
      "v_max_u32_e32 v232, v232, v175\n\t"
      "v_cmp_eq_u32_e32 vcc, -1, v232\n\t"
      "s_cbranch_vccnz LO10L_%=\n\t"
      "LO10X_%=:\n\t"
      "s_branch LO10B_%=\n\t"
      "LO12R_%=:\n\t"
      "s_mov_b32 s80, 0\n\t"
      "LO12L_%=:\n\t"
      "s_add_u32 s80, s80, 1\n\t"
      "s_cmp_lt_u32 s80, 262144\n\t"
      "s_cbranch_scc0 LO12X_%=\n\t"
      "s_waitcnt vmcnt(0)\n\t"
      "s_sleep 1\n\t"
      "global_load_dwordx4 v[176:179], v244, s[44:45] offset:2048 sc1\n\t"
      "global_load_dwordx4 v[180:183], v244, s[44:45] offset:3072 sc1\n\t"
      "global_load_dwordx4 v[184:187], v244, s[42:43] offset:-4096 sc1\n\t"
      "global_load_dwordx4 v[188:191], v244, s[42:43] offset:-3072 sc1\n\t"
      "s_waitcnt vmcnt(0)\n\t"
      "v_max3_u32 v232, v176, v177, v178\n\t"
      "v_max3_u32 v232, v232, v179, v180\n\t"
      "v_max3_u32 v232, v232, v181, v182\n\t"
      "v_max_u32_e32 v232, v232, v183\n\t"
      "v_cmp_eq_u32_e32 vcc, -1, v232\n\t"
      "s_cbranch_vccnz LO12L_%=\n\t"
      "LO12X_%=:\n\t"
      "s_branch LO12B_%=\n\t"
      "LO14R_%=:\n\t"
      "s_mov_b32 s80, 0\n\t"
      "LO14L_%=:\n\t"
      "s_add_u32 s80, s80, 1\n\t"
      "s_cmp_lt_u32 s80, 262144\n\t"
      "s_cbranch_scc0 LO14X_%=\n\t"
      "s_waitcnt vmcnt(0)\n\t"
      "s_sleep 1\n\t"
      "global_load_dwordx4 v[184:187], v244, s[42:43] offset:-4096 sc1\n\t"
      "global_load_dwordx4 v[188:191], v244, s[42:43] offset:-3072 sc1\n\t"
      "s_waitcnt vmcnt(0)\n\t"
      "v_max3_u32 v232, v184, v185, v186\n\t"
      "v_max3_u32 v232, v232, v187, v188\n\t"
      "v_max3_u32 v232, v232, v189, v190\n\t"
      "v_max_u32_e32 v232, v232, v191\n\t"
      "v_cmp_eq_u32_e32 vcc, -1, v232\n\t"
      "s_cbranch_vccnz LO14L_%=\n\t"
      "LO14X_%=:\n\t"
      "s_branch LO14B_%=\n\t"
      "LGX_%=:\n\t"
      "v_max3_u32 v232, v128, v129, v130\n\t"
      "v_max3_u32 v232, v232, v131, v132\n\t"
      "v_max3_u32 v232, v232, v133, v134\n\t"
      "v_max3_u32 v232, v232, v135, v136\n\t"
      "v_max3_u32 v232, v232, v137, v138\n\t"
      "v_max3_u32 v232, v232, v139, v140\n\t"
      "v_max3_u32 v232, v232, v141, v142\n\t"
      "v_max3_u32 v232, v232, v143, v144\n\t"
      "v_max3_u32 v232, v232, v145, v146\n\t"
      "v_max3_u32 v232, v232, v147, v148\n\t"
      "v_max3_u32 v232, v232, v149, v150\n\t"
      "v_max3_u32 v232, v232, v151, v152\n\t"
      "v_max3_u32 v232, v232, v153, v154\n\t"
      "v_max3_u32 v232, v232, v155, v156\n\t"
      "v_max3_u32 v232, v232, v157, v158\n\t"
      "v_max3_u32 v232, v232, v159, v160\n\t"
      "v_max3_u32 v232, v232, v161, v162\n\t"
      "v_max3_u32 v232, v232, v163, v164\n\t"
      "v_max3_u32 v232, v232, v165, v166\n\t"
      "v_max3_u32 v232, v232, v167, v168\n\t"
      "v_max3_u32 v232, v232, v169, v170\n\t"
      "v_max3_u32 v232, v232, v171, v172\n\t"
      "v_max3_u32 v232, v232, v173, v174\n\t"
      "v_max3_u32 v232, v232, v175, v176\n\t"
      "v_max3_u32 v232, v232, v177, v178\n\t"
      "v_max3_u32 v232, v232, v179, v180\n\t"
      "v_max3_u32 v232, v232, v181, v182\n\t"
      "v_max3_u32 v232, v232, v183, v184\n\t"
      "v_max3_u32 v232, v232, v185, v186\n\t"
      "v_max3_u32 v232, v232, v187, v188\n\t"
      "v_max3_u32 v232, v232, v189, v190\n\t"
      "v_max_u32_e32 v232, v232, v191\n\t"
      "v_cmp_eq_u32_e32 vcc, -1, v232\n\t"
      "s_cbranch_vccz LGB_%=\n\t"
      "LGYR_%=:\n\t"
      "s_mov_b32 s80, 0\n\t"
      "LGYL_%=:\n\t"
      "s_add_u32 s80, s80, 1\n\t"
      "s_cmp_lt_u32 s80, 262144\n\t"
      "s_cbranch_scc0 LGYX_%=\n\t"
      "s_sleep 1\n\t"
      "global_load_dwordx4 v[128:131], v244, s[42:43] offset:-2048 sc1\n\t"
      "global_load_dwordx4 v[132:135], v244, s[42:43] offset:-1024 sc1\n\t"
      "global_load_dwordx4 v[136:139], v244, s[42:43] sc1\n\t"
      "global_load_dwordx4 v[140:143], v244, s[42:43] offset:1024 sc1\n\t"
      "global_load_dwordx4 v[144:147], v244, s[42:43] offset:2048 sc1\n\t"
      "global_load_dwordx4 v[148:151], v244, s[42:43] offset:3072 sc1\n\t"
      "global_load_dwordx4 v[152:155], v244, s[44:45] offset:-4096 sc1\n\t"
      "global_load_dwordx4 v[156:159], v244, s[44:45] offset:-3072 sc1\n\t"
      "global_load_dwordx4 v[160:163], v244, s[44:45] offset:-2048 sc1\n\t"
      "global_load_dwordx4 v[164:167], v244, s[44:45] offset:-1024 sc1\n\t"
      "global_load_dwordx4 v[168:171], v244, s[44:45] sc1\n\t"
      "global_load_dwordx4 v[172:175], v244, s[44:45] offset:1024 sc1\n\t"
      "global_load_dwordx4 v[176:179], v244, s[44:45] offset:2048 sc1\n\t"
      "global_load_dwordx4 v[180:183], v244, s[44:45] offset:3072 sc1\n\t"
      "global_load_dwordx4 v[184:187], v244, s[42:43] offset:-4096 sc1\n\t"
      "global_load_dwordx4 v[188:191], v244, s[42:43] offset:-3072 sc1\n\t"
      "s_waitcnt vmcnt(0)\n\t"
      "v_max3_u32 v232, v128, v129, v130\n\t"
      "v_max3_u32 v232, v232, v131, v132\n\t"
      "v_max3_u32 v232, v232, v133, v134\n\t"
      "v_max3_u32 v232, v232, v135, v136\n\t"
      "v_max3_u32 v232, v232, v137, v138\n\t"
      "v_max3_u32 v232, v232, v139, v140\n\t"
      "v_max3_u32 v232, v232, v141, v142\n\t"
      "v_max3_u32 v232, v232, v143, v144\n\t"
      "v_max3_u32 v232, v232, v145, v146\n\t"
      "v_max3_u32 v232, v232, v147, v148\n\t"
      "v_max3_u32 v232, v232, v149, v150\n\t"
      "v_max3_u32 v232, v232, v151, v152\n\t"
      "v_max3_u32 v232, v232, v153, v154\n\t"
      "v_max3_u32 v232, v232, v155, v156\n\t"
      "v_max3_u32 v232, v232, v157, v158\n\t"
      "v_max3_u32 v232, v232, v159, v160\n\t"
      "v_max3_u32 v232, v232, v161, v162\n\t"
      "v_max3_u32 v232, v232, v163, v164\n\t"
      "v_max3_u32 v232, v232, v165, v166\n\t"
      "v_max3_u32 v232, v232, v167, v168\n\t"
      "v_max3_u32 v232, v232, v169, v170\n\t"
      "v_max3_u32 v232, v232, v171, v172\n\t"
      "v_max3_u32 v232, v232, v173, v174\n\t"
      "v_max3_u32 v232, v232, v175, v176\n\t"
      "v_max3_u32 v232, v232, v177, v178\n\t"
      "v_max3_u32 v232, v232, v179, v180\n\t"
      "v_max3_u32 v232, v232, v181, v182\n\t"
      "v_max3_u32 v232, v232, v183, v184\n\t"
      "v_max3_u32 v232, v232, v185, v186\n\t"
      "v_max3_u32 v232, v232, v187, v188\n\t"
      "v_max3_u32 v232, v232, v189, v190\n\t"
      "v_max_u32_e32 v232, v232, v191\n\t"
      "v_cmp_eq_u32_e32 vcc, -1, v232\n\t"
      "s_cbranch_vccnz LGYL_%=\n\t"
      "LGYX_%=:\n\t"
      "s_cbranch_vccnz LGB_%=\n\t"
      "ds_read_b64_tr_b16 v[0:1], v216 offset:7168\n\t"
      "ds_read_b64_tr_b16 v[2:3], v220 offset:7168\n\t"
      "ds_read_b64_tr_b16 v[4:5], v217 offset:7168\n\t"
      "ds_read_b64_tr_b16 v[6:7], v221 offset:7168\n\t"
      "ds_read_b64_tr_b16 v[8:9], v218 offset:7168\n\t"
      "ds_read_b64_tr_b16 v[10:11], v222 offset:7168\n\t"
      "ds_read_b64_tr_b16 v[12:13], v219 offset:7168\n\t"
      "ds_read_b64_tr_b16 v[14:15], v223 offset:7168\n\t"
      "ds_read_b64_tr_b16 v[16:17], v218 offset:10752\n\t"
      "ds_read_b64_tr_b16 v[18:19], v222 offset:10752\n\t"
      "ds_read_b64_tr_b16 v[20:21], v219 offset:10752\n\t"
      "ds_read_b64_tr_b16 v[22:23], v223 offset:10752\n\t"
      "ds_read_b64_tr_b16 v[24:25], v216 offset:10752\n\t"
      "ds_read_b64_tr_b16 v[26:27], v220 offset:10752\n\t"
      "ds_read_b64_tr_b16 v[28:29], v217 offset:10752\n\t"
      "ds_read_b64_tr_b16 v[30:31], v221 offset:10752\n\t"
      "ds_read_b64_tr_b16 v[32:33], v216 offset:14336\n\t"
      "ds_read_b64_tr_b16 v[34:35], v220 offset:14336\n\t"
      "ds_read_b64_tr_b16 v[36:37], v217 offset:14336\n\t"
      "ds_read_b64_tr_b16 v[38:39], v221 offset:14336\n\t"
      "ds_read_b64_tr_b16 v[40:41], v218 offset:14336\n\t"
      "ds_read_b64_tr_b16 v[42:43], v222 offset:14336\n\t"
      "ds_read_b64_tr_b16 v[44:45], v219 offset:14336\n\t"
      "ds_read_b64_tr_b16 v[46:47], v223 offset:14336\n\t"
      "ds_read_b64_tr_b16 v[48:49], v218 offset:17920\n\t"
      "ds_read_b64_tr_b16 v[50:51], v222 offset:17920\n\t"
      "ds_read_b64_tr_b16 v[52:53], v219 offset:17920\n\t"
      "ds_read_b64_tr_b16 v[54:55], v223 offset:17920\n\t"
      "ds_read_b64_tr_b16 v[56:57], v216 offset:17920\n\t"
      "ds_read_b64_tr_b16 v[58:59], v220 offset:17920\n\t"
      "ds_read_b64_tr_b16 v[60:61], v217 offset:17920\n\t"
      "ds_read_b64_tr_b16 v[62:63], v221 offset:17920\n\t"
      "ds_read_b64_tr_b16 v[64:65], v216 offset:21504\n\t"
      "ds_read_b64_tr_b16 v[66:67], v220 offset:21504\n\t"
      "ds_read_b64_tr_b16 v[68:69], v217 offset:21504\n\t"
      "ds_read_b64_tr_b16 v[70:71], v221 offset:21504\n\t"
      "ds_read_b64_tr_b16 v[72:73], v218 offset:21504\n\t"
      "ds_read_b64_tr_b16 v[74:75], v222 offset:21504\n\t"
      "ds_read_b64_tr_b16 v[76:77], v219 offset:21504\n\t"
      "ds_read_b64_tr_b16 v[78:79], v223 offset:21504\n\t"
      "ds_read_b64_tr_b16 v[80:81], v218 offset:25088\n\t"
      "ds_read_b64_tr_b16 v[82:83], v222 offset:25088\n\t"
      "ds_read_b64_tr_b16 v[84:85], v219 offset:25088\n\t"
      "ds_read_b64_tr_b16 v[86:87], v223 offset:25088\n\t"
      "ds_read_b64_tr_b16 v[88:89], v216 offset:25088\n\t"
      "ds_read_b64_tr_b16 v[90:91], v220 offset:25088\n\t"
      "ds_read_b64_tr_b16 v[92:93], v217 offset:25088\n\t"
      "ds_read_b64_tr_b16 v[94:95], v221 offset:25088\n\t"
      "ds_read_b64_tr_b16 v[96:97], v216 offset:28672\n\t"
      "ds_read_b64_tr_b16 v[98:99], v220 offset:28672\n\t"
      "ds_read_b64_tr_b16 v[100:101], v217 offset:28672\n\t"
      "ds_read_b64_tr_b16 v[102:103], v221 offset:28672\n\t"
      "ds_read_b64_tr_b16 v[104:105], v218 offset:28672\n\t"
      "ds_read_b64_tr_b16 v[106:107], v222 offset:28672\n\t"
      "ds_read_b64_tr_b16 v[108:109], v219 offset:28672\n\t"
      "ds_read_b64_tr_b16 v[110:111], v223 offset:28672\n\t"
      "ds_read_b64_tr_b16 v[112:113], v218 offset:32256\n\t"
      "ds_read_b64_tr_b16 v[114:115], v222 offset:32256\n\t"
      "ds_read_b64_tr_b16 v[116:117], v219 offset:32256\n\t"
      "ds_read_b64_tr_b16 v[118:119], v223 offset:32256\n\t"
      "ds_read_b64_tr_b16 v[120:121], v216 offset:32256\n\t"
      "ds_read_b64_tr_b16 v[122:123], v220 offset:32256\n\t"
      "ds_read_b64_tr_b16 v[124:125], v217 offset:32256\n\t"
      "ds_read_b64_tr_b16 v[126:127], v221 offset:32256\n\t"
      "v_lshlrev_b32_e32 v192, 16, v208\n\t"
      "v_and_b32_e32 v193, 0xffff0000, v208\n\t"
      "v_lshlrev_b32_e32 v194, 16, v209\n\t"
      "v_and_b32_e32 v195, 0xffff0000, v209\n\t"
      "v_lshlrev_b32_e32 v196, 16, v210\n\t"
      "v_and_b32_e32 v197, 0xffff0000, v210\n\t"
      "v_lshlrev_b32_e32 v198, 16, v211\n\t"
      "v_and_b32_e32 v199, 0xffff0000, v211\n\t"
      "v_lshlrev_b32_e32 v200, 16, v212\n\t"
      "v_and_b32_e32 v201, 0xffff0000, v212\n\t"
      "v_lshlrev_b32_e32 v202, 16, v213\n\t"
      "v_and_b32_e32 v203, 0xffff0000, v213\n\t"
      "v_lshlrev_b32_e32 v204, 16, v214\n\t"
      "v_and_b32_e32 v205, 0xffff0000, v214\n\t"
      "v_lshlrev_b32_e32 v206, 16, v215\n\t"
      "v_and_b32_e32 v207, 0xffff0000, v215\n\t"
      "s_waitcnt lgkmcnt(0)\n\t"
      "s_branch LGM_%=\n\t"
      "LSR_%=:\n\t"
      "s_mov_b32 s80, 0\n\t"
      "LSL_%=:\n\t"
      "s_add_u32 s80, s80, 1\n\t"
      "s_cmp_lt_u32 s80, 262144\n\t"
      "s_cbranch_scc0 LSX_%=\n\t"
      "s_sleep 1\n\t"
      "global_load_dwordx2 v[148:149], v231, s[46:47] sc1\n\t"
      "global_load_dwordx2 v[150:151], v231, s[46:47] offset:384 sc1\n\t"
      "s_waitcnt vmcnt(0)\n\t"
      "v_max3_u32 v232, v148, v149, v150\n\t"
      "v_max_u32_e32 v232, v232, v151\n\t"
      "v_cmp_eq_u32_e32 vcc, -1, v232\n\t"
      "s_cbranch_vccnz LSL_%=\n\t"
      "LSX_%=:\n\t"
      "s_branch LSB_%=\n\t"
      "LF1R_%=:\n\t"
      "s_mov_b32 s80, 0\n\t"
      "LF1L_%=:\n\t"
      "s_add_u32 s80, s80, 1\n\t"
      "s_cmp_lt_u32 s80, 262144\n\t"
      "s_cbranch_scc0 LF1X_%=\n\t"
      "s_sleep 2\n\t"
      "global_load_dwordx2 v[138:139], v136, s[60:61] sc1\n\t"
      "s_waitcnt vmcnt(0)\n\t"
      "v_cmp_ne_u64_e32 vcc, s[22:23], v[138:139]\n\t"
      "s_cbranch_vccnz LF1L_%=\n\t"
      "LF1X_%=:\n\t"
      "s_branch LF1B_%=\n\t"
      "LF4R_%=:\n\t"
      "s_mov_b32 s80, 0\n\t"
      "LF4L_%=:\n\t"
      "s_add_u32 s80, s80, 1\n\t"
      "s_cmp_lt_u32 s80, 262144\n\t"
      "s_cbranch_scc0 LF4X_%=\n\t"
      "s_sleep 2\n\t"
      "global_load_dwordx2 v[138:139], v137, s[60:61] sc1\n\t"
      "s_waitcnt vmcnt(0)\n\t"
      "v_cmp_ne_u64_e32 vcc, s[22:23], v[138:139]\n\t"
      "s_cbranch_vccnz LF4L_%=\n\t"
      "LF4X_%=:\n\t"
      "s_branch LF4B_%=\n\t"
      "L4_%=:\n\t"
      "s_waitcnt vmcnt(0)\n\t"
      "s_barrier\n\t"
      "s_barrier\n\t"
      "s_lshl2_add_u32 s72, s31, s24\n\t"
      "s_sub_u32 s72, s72, 56\n\t"
      "s_mov_b32 m0, 64512\n\t"
      "s_cmp_ge_u32 s72, 64\n\t"
      "s_cselect_b32 s73, 8, 0\n\t"
      "s_sub_u32 s73, s72, s73\n\t"
      "s_and_b32 s73, s73, 0x38\n\t"
      "s_add_u32 s72, s72, s73\n\t"
      "s_add_u32 s72, s72, 8\n\t"
      "s_mul_i32 s72, s72, 57344\n\t"
      "s_and_b32 s73, s2, 7\n\t"
      "s_mul_i32 s73, s73, 448\n\t"
      "s_and_b32 s73, s73, 0xffffff80\n\t"
      "s_add_u32 s72, s72, s73\n\t"
      "v_bfe_u32 v141, v192, 2, 4\n\t"
      "v_mul_u32_u24_e32 v141, 3584, v141\n\t"
      "v_lshl_add_u32 v141, v196, 7, v141\n\t"
      "v_add_u32_e32 v141, s72, v141\n\t"
      "global_load_lds_dword v141, s[10:11]\n\t"
      "s_barrier\n\t"
      "s_cmp_lg_u32 s24, 3\n\t"
      "s_cbranch_scc1 SK12_%=\n\t"
      "global_load_dwordx2 v[138:139], v137, s[60:61] sc1\n\t"
      "SK12_%=:\n\t"
      "s_cmp_lg_u32 s24, 3\n\t"
      "s_cbranch_scc1 LF4B_%=\n\t"
      "s_waitcnt vmcnt(0)\n\t"
      "v_cmp_ne_u64_e32 vcc, s[22:23], v[138:139]\n\t"
      "s_cbranch_vccnz LF4R_%=\n\t"
      "LF4B_%=:\n\t"
      "s_barrier\n\t"
      "s_waitcnt vmcnt(0)\n\t"
      "s_barrier\n\t"
      "s_branch G4_%=\n\t"
      "LF2R_%=:\n\t"
      "s_mov_b32 s80, 0\n\t"
      "LF2L_%=:\n\t"
      "s_add_u32 s80, s80, 1\n\t"
      "s_cmp_lt_u32 s80, 262144\n\t"
      "s_cbranch_scc0 LF2X_%=\n\t"
      "s_sleep 2\n\t"
      "global_load_dwordx2 v[138:139], v137, s[60:61] sc1\n\t"
      "s_waitcnt vmcnt(0)\n\t"
      "v_cmp_ne_u64_e32 vcc, s[22:23], v[138:139]\n\t"
      "s_cbranch_vccnz LF2L_%=\n\t"
      "LF2X_%=:\n\t"
      "s_branch LF2B_%=\n\t"
      "LF3R_%=:\n\t"
      "s_mov_b32 s80, 0\n\t"
      "LF3L_%=:\n\t"
      "s_add_u32 s80, s80, 1\n\t"
      "s_cmp_lt_u32 s80, 262144\n\t"
      "s_cbranch_scc0 LF3X_%=\n\t"
      "s_sleep 2\n\t"
      "global_load_dwordx2 v[138:139], v143, s[60:61] sc1\n\t"
      "s_waitcnt vmcnt(0)\n\t"
      "v_cmp_ne_u64_e32 vcc, s[22:23], v[138:139]\n\t"
      "s_cbranch_vccnz LF3L_%=\n\t"
      "LF3X_%=:\n\t"
      "s_branch LF3B_%=\n\t"
      "LF70R_%=:\n\t"
      "s_mov_b32 s80, 0\n\t"
      "LF70L_%=:\n\t"
      "s_add_u32 s80, s80, 1\n\t"
      "s_cmp_lt_u32 s80, 262144\n\t"
      "s_cbranch_scc0 LF70X_%=\n\t"
      "s_sleep 4\n\t"
      "global_load_dwordx2 v[138:139], v136, s[60:61] sc1\n\t"
      "s_waitcnt vmcnt(0)\n\t"
      "v_cmp_ne_u64_e32 vcc, s[22:23], v[138:139]\n\t"
      "s_cbranch_vccnz LF70L_%=\n\t"
      "LF70X_%=:\n\t"
      "s_branch LF70B_%=\n\t"
      "LF71R_%=:\n\t"
      "s_mov_b32 s80, 0\n\t"
      "LF71L_%=:\n\t"
      "s_add_u32 s80, s80, 1\n\t"
      "s_cmp_lt_u32 s80, 262144\n\t"
      "s_cbranch_scc0 LF71X_%=\n\t"
      "s_sleep 4\n\t"
      "global_load_dwordx2 v[138:139], v136, s[60:61] sc1\n\t"
      "s_waitcnt vmcnt(0)\n\t"
      "v_cmp_ne_u64_e32 vcc, s[22:23], v[138:139]\n\t"
      "s_cbranch_vccnz LF71L_%=\n\t"
      "LF71X_%=:\n\t"
      "s_branch LF71B_%=\n\t"
      "SPARE_%=:\n\t"
      "v_mov_b32_e32 v132, -1\n\t"
      "v_mov_b32_e32 v133, -1\n\t"
      "v_mov_b32_e32 v134, -1\n\t"
      "v_mov_b32_e32 v135, -1\n\t"
      "s_and_b32 s34, s25, 3\n\t"
      "s_lshl_b32 s34, s34, 15\n\t"
      "s_mul_i32 s35, s24, 917504\n\t"
      "s_add_u32 s34, s34, s35\n\t"
      "s_and_b32 s35, s2, 7\n\t"
      "s_lshl_b32 s35, s35, 9\n\t"
      "s_add_u32 s34, s34, s35\n\t"
      "v_and_b32_e32 v141, 0x1c0, v244\n\t"
      "v_lshlrev_b32_e32 v141, 6, v141\n\t"
      "v_and_b32_e32 v142, 0x30, v244\n\t"
      "v_lshl_add_u32 v141, v142, 3, v141\n\t"
      "v_add_u32_e32 v141, s34, v141\n\t"
      "s_mov_b32 m0, 64512\n\t"
      "s_lshr_b32 s34, s24, 1\n\t"
      "s_lshl_b32 s34, s34, 5\n\t"
      "s_add_u32 s34, s34, s25\n\t"
      "s_and_b32 s35, s24, 1\n\t"
      "s_lshl1_add_u32 s34, s34, s35\n\t"
      "s_lshl_b32 s34, s34, 10\n\t"
      "s_add_u32 s34, s34, 8388608\n\t"
      "s_add_u32 s40, s20, s34\n\t"
      "s_addc_u32 s41, s21, 0\n\t"
      "global_store_dwordx4 v244, v[132:135], s[40:41] sc1\n\t"
      "s_add_u32 s40, s40, 262144\n\t"
      "s_addc_u32 s41, s41, 0\n\t"
      "global_store_dwordx4 v244, v[132:135], s[40:41] sc1\n\t"
      "s_add_u32 s40, s40, 262144\n\t"
      "s_addc_u32 s41, s41, 0\n\t"
      "global_store_dwordx4 v244, v[132:135], s[40:41] sc1\n\t"
      "s_add_u32 s40, s40, 262144\n\t"
      "s_addc_u32 s41, s41, 0\n\t"
      "global_store_dwordx4 v244, v[132:135], s[40:41] sc1\n\t"
      "s_add_u32 s40, s40, 262144\n\t"
      "s_addc_u32 s41, s41, 0\n\t"
      "global_store_dwordx4 v244, v[132:135], s[40:41] sc1\n\t"
      "s_add_u32 s40, s40, 262144\n\t"
      "s_addc_u32 s41, s41, 0\n\t"
      "global_store_dwordx4 v244, v[132:135], s[40:41] sc1\n\t"
      "s_add_u32 s40, s40, 262144\n\t"
      "s_addc_u32 s41, s41, 0\n\t"
      "global_store_dwordx4 v244, v[132:135], s[40:41] sc1\n\t"
      "s_add_u32 s40, s40, 262144\n\t"
      "s_addc_u32 s41, s41, 0\n\t"
      "s_lshl_b32 s34, s25, 7\n\t"
      "s_add_u32 s34, s34, 16384\n\t"
      "v_mov_b32_e32 v131, s34\n\t"
      "s_waitcnt vmcnt(0)\n\t"
      "s_barrier\n\t"
      "s_cmp_lg_u32 s24, 0\n\t"
      "s_cbranch_scc1 SP0_%=\n\t"
      "s_mov_b64 exec, 1\n\t"
      "global_store_dwordx2 v131, v[128:129], s[60:61] sc1\n\t"
      "s_mov_b64 exec, -1\n\t"
      "SP0_%=:\n\t"
      "s_and_b32 s34, s25, 3\n\t"
      "s_lshl3_add_u32 s34, s34, s24\n\t"
      "s_cmp_lg_u32 s34, 19\n\t"
      "s_cbranch_scc1 SQ0_%=\n\t"
      "v_and_b32_e32 v136, 0x1f0, v244\n\t"
      "v_lshlrev_b32_e32 v131, 6, v136\n\t"
      "v_lshlrev_b32_e32 v136, 3, v136\n\t"
      "v_add_u32_e32 v136, 16384, v136\n\t"
      "s_and_b32 s34, s2, 7\n\t"
      "s_lshl_b32 s34, s34, 7\n\t"
      "s_add_u32 s34, s34, 32768\n\t"
      "v_add_u32_e32 v131, s34, v131\n\t"
      "s_cmp_lg_u32 s24, 3\n\t"
      "s_cbranch_scc1 SK15_%=\n\t"
      "global_load_dwordx2 v[138:139], v136, s[60:61] sc1\n\t"
      "SK15_%=:\n\t"
      "s_cmp_lg_u32 s24, 3\n\t"
      "s_cbranch_scc1 LF70B_%=\n\t"
      "s_waitcnt vmcnt(0)\n\t"
      "v_cmp_ne_u64_e32 vcc, s[22:23], v[138:139]\n\t"
      "s_cbranch_vccnz LF70R_%=\n\t"
      "LF70B_%=:\n\t"
      "global_store_dwordx2 v131, v[128:129], s[60:61] sc1\n\t"
      "SQ0_%=:\n\t"
      "s_barrier\n\t"
      "s_lshl3_add_u32 s34, s25, s24\n\t"
      "s_mul_i32 s34, s34, 7168\n\t"
      "s_add_u32 s34, s34, 10227712\n\t"
      "s_add_u32 s40, s20, s34\n\t"
      "s_addc_u32 s41, s21, 0\n\t"
      "global_store_dwordx4 v244, v[132:135], s[40:41] offset:-4096 sc1\n\t"
      "global_store_dwordx4 v244, v[132:135], s[40:41] offset:-3072 sc1\n\t"
      "global_store_dwordx4 v244, v[132:135], s[40:41] offset:-2048 sc1\n\t"
      "global_store_dwordx4 v244, v[132:135], s[40:41] offset:-1024 sc1\n\t"
      "global_store_dwordx4 v244, v[132:135], s[40:41] sc1\n\t"
      "global_store_dwordx4 v244, v[132:135], s[40:41] offset:1024 sc1\n\t"
      "global_store_dwordx4 v244, v[132:135], s[40:41] offset:2048 sc1\n\t"
      "s_lshl_b32 s34, s25, 7\n\t"
      "s_add_u32 s34, s34, 24576\n\t"
      "v_mov_b32_e32 v131, s34\n\t"
      "s_waitcnt vmcnt(0)\n\t"
      "s_barrier\n\t"
      "s_cmp_lg_u32 s24, 0\n\t"
      "s_cbranch_scc1 SP1_%=\n\t"
      "s_mov_b64 exec, 1\n\t"
      "global_store_dwordx2 v131, v[128:129], s[60:61] sc1\n\t"
      "s_mov_b64 exec, -1\n\t"
      "SP1_%=:\n\t"
      "s_and_b32 s34, s25, 3\n\t"
      "s_lshl3_add_u32 s34, s34, s24\n\t"
      "s_cmp_lg_u32 s34, 27\n\t"
      "s_cbranch_scc1 SQ1_%=\n\t"
      "v_and_b32_e32 v136, 0x1f0, v244\n\t"
      "v_lshlrev_b32_e32 v131, 6, v136\n\t"
      "v_lshlrev_b32_e32 v136, 3, v136\n\t"
      "v_add_u32_e32 v136, 24576, v136\n\t"
      "s_and_b32 s34, s2, 7\n\t"
      "s_lshl_b32 s34, s34, 7\n\t"
      "s_add_u32 s34, s34, 65536\n\t"
      "v_add_u32_e32 v131, s34, v131\n\t"
      "s_cmp_lg_u32 s24, 3\n\t"
      "s_cbranch_scc1 SK16_%=\n\t"
      "global_load_dwordx2 v[138:139], v136, s[60:61] sc1\n\t"
      "SK16_%=:\n\t"
      "s_cmp_lg_u32 s24, 3\n\t"
      "s_cbranch_scc1 LF71B_%=\n\t"
      "s_waitcnt vmcnt(0)\n\t"
      "v_cmp_ne_u64_e32 vcc, s[22:23], v[138:139]\n\t"
      "s_cbranch_vccnz LF71R_%=\n\t"
      "LF71B_%=:\n\t"
      "global_store_dwordx2 v131, v[128:129], s[60:61] sc1\n\t"
      "SQ1_%=:\n\t"
      "s_barrier\n\t"
      "s_and_b32 s35, s25, 3\n\t"
      "s_lshl2_add_u32 s31, s26, s35\n\t"
      "s_and_b32 s35, s2, 7\n\t"
      "s_mul_i32 s34, s35, 14\n\t"
      "s_lshl4_add_u32 s35, s35, -14\n\t"
      "s_cmp_lt_u32 s31, 14\n\t"
      "s_cselect_b32 s34, s34, s35\n\t"
      "s_cselect_b32 s36, 1, 0\n\t"
      "s_or_b32 s27, s27, s36\n\t"
      "s_add_u32 s34, s34, s31\n\t"
      "s_lshr_b32 s29, s34, 2\n\t"
      "s_and_b32 s32, s34, 3\n\t"
      "s_mul_i32 s30, s32, 7\n\t"
      "v_and_b32_e32 v199, 60, v192\n\t"
      "v_and_b32_e32 v200, 24, v192\n\t"
      "v_lshlrev_b32_e32 v202, 3, v196\n\t"
      "v_lshl_or_b32 v202, v200, 2, v202\n\t"
      "v_lshl_or_b32 v202, v195, 7, v202\n\t"
      "v_lshl_or_b32 v202, v193, 10, v202\n\t"
      "v_add_u32_e32 v224, s39, v202\n\t"
      "v_xor_b32_e32 v225, 32, v224\n\t"
      "v_xor_b32_e32 v226, 64, v224\n\t"
      "v_xor_b32_e32 v227, 96, v224\n\t"
      "s_lshl_b32 s34, s24, 12\n\t"
      "s_add_u32 s34, s34, 98304\n\t"
      "v_add_u32_e32 v245, s34, v244\n\t"
      "v_lshlrev_b32_e32 v199, 2, v199\n\t"
      "s_lshl_b32 s34, s24, 8\n\t"
      "v_lshl_add_u32 v247, v192, 2, s34\n\t"
      "v_and_b32_e32 v200, 2, v192\n\t"
      "v_mul_u32_u24_e32 v202, 576, v200\n\t"
      "v_xor_b32_e32 v199, v202, v199\n\t"
      "v_and_b32_e32 v202, 1, v192\n\t"
      "v_lshl_or_b32 v199, v202, 3, v199\n\t"
      "s_and_b32 s35, s34, 0x400\n\t"
      "s_add_u32 s34, s34, s35\n\t"
      "s_add_u32 s34, s34, 98304\n\t"
      "v_add_u32_e32 v246, s34, v199\n\t"
      "v_lshlrev_b32_e32 v199, 2, v202\n\t"
      "v_lshl_or_b32 v199, v200, 4, v199\n\t"
      "s_min_u32 s34, s29, 27\n\t"
      "s_lshl_b32 s34, s34, 7\n\t"
      "s_lshr_b32 s35, s24, 2\n\t"
      "s_lshl_b32 s35, s35, 6\n\t"
      "s_add_u32 s34, s34, s35\n\t"
      "s_lshr_b32 s35, s39, 9\n\t"
      "s_add_u32 s34, s34, s35\n\t"
      "v_add_u32_e32 v248, s34, v199\n\t"
      "s_add_u32 s48, s20, 8388608\n\t"
      "s_addc_u32 s49, s21, 0\n\t"
      "s_cmp_ge_u32 s31, 30\n\t"
      "s_cbranch_scc1 BYE_%=\n\t"
      "s_setprio 0\n\t"
      "s_waitcnt vmcnt(0)\n\t"
      "s_barrier\n\t"
      "s_branch G4_%=\n\t"
      "BYE_%=:\n\t"
      "s_endpgm\n\t"
      "LA30R_%=:\n\t"
      "s_mov_b32 s80, 0\n\t"
      "LA30L_%=:\n\t"
      "s_add_u32 s80, s80, 1\n\t"
      "s_cmp_lt_u32 s80, 262144\n\t"
      "s_cbranch_scc0 LA30X_%=\n\t"
      "s_waitcnt vmcnt(0)\n\t"
      "s_sleep 1\n\t"
      "global_load_dwordx4 v[128:131], v250, s[64:65] offset:-4096 sc1\n\t"
      "global_load_dwordx4 v[132:135], v250, s[64:65] offset:-3072 sc1\n\t"
      "global_load_dwordx4 v[136:139], v250, s[64:65] offset:-2048 sc1\n\t"
      "global_load_dwordx4 v[140:143], v250, s[64:65] offset:-1024 sc1\n\t"
      "global_load_dwordx4 v[144:147], v250, s[64:65] sc1\n\t"
      "global_load_dwordx4 v[148:151], v250, s[64:65] offset:1024 sc1\n\t"
      "global_load_dwordx4 v[152:155], v250, s[64:65] offset:2048 sc1\n\t"
      "global_load_dwordx4 v[156:159], v250, s[64:65] offset:3072 sc1\n\t"
      "s_waitcnt vmcnt(0)\n\t"
      "v_max3_u32 v251, v128, v129, v130\n\t"
      "v_max3_u32 v251, v251, v131, v132\n\t"
      "v_max3_u32 v251, v251, v133, v134\n\t"
      "v_max3_u32 v251, v251, v135, v136\n\t"
      "v_max3_u32 v251, v251, v137, v138\n\t"
      "v_max3_u32 v251, v251, v139, v140\n\t"
      "v_max3_u32 v251, v251, v141, v142\n\t"
      "v_max3_u32 v251, v251, v143, v144\n\t"
      "v_max3_u32 v251, v251, v145, v146\n\t"
      "v_max3_u32 v251, v251, v147, v148\n\t"
      "v_max3_u32 v251, v251, v149, v150\n\t"
      "v_max3_u32 v251, v251, v151, v152\n\t"
      "v_max3_u32 v251, v251, v153, v154\n\t"
      "v_max3_u32 v251, v251, v155, v156\n\t"
      "v_max3_u32 v251, v251, v157, v158\n\t"
      "v_max_u32_e32 v251, v251, v159\n\t"
      "v_cmp_eq_u32_e32 vcc, -1, v251\n\t"
      "s_cbranch_vccnz LA30L_%=\n\t"
      "LA30X_%=:\n\t"
      "s_branch LA30B_%=\n\t"
      "LF6R_%=:\n\t"
      "s_mov_b32 s80, 0\n\t"
      "LF6L_%=:\n\t"
      "s_add_u32 s80, s80, 1\n\t"
      "s_cmp_lt_u32 s80, 262144\n\t"
      "s_cbranch_scc0 LF6X_%=\n\t"
      "s_sleep 2\n\t"
      "global_load_dwordx2 v[228:229], v230, s[60:61] sc1\n\t"
      "s_waitcnt vmcnt(0)\n\t"
      "v_cmp_ne_u64_e32 vcc, s[22:23], v[228:229]\n\t"
      "s_cbranch_vccnz LF6L_%=\n\t"
      "LF6X_%=:\n\t"
      "s_branch LF6B_%=\n\t"
      "LA31R_%=:\n\t"
      "s_mov_b32 s80, 0\n\t"
      "LA31L_%=:\n\t"
      "s_add_u32 s80, s80, 1\n\t"
      "s_cmp_lt_u32 s80, 262144\n\t"
      "s_cbranch_scc0 LA31X_%=\n\t"
      "s_waitcnt vmcnt(0)\n\t"
      "s_sleep 1\n\t"
      "global_load_dwordx4 v[160:163], v250, s[66:67] offset:-4096 sc1\n\t"
      "global_load_dwordx4 v[164:167], v250, s[66:67] offset:-3072 sc1\n\t"
      "global_load_dwordx4 v[168:171], v250, s[66:67] offset:-2048 sc1\n\t"
      "global_load_dwordx4 v[172:175], v250, s[66:67] offset:-1024 sc1\n\t"
      "global_load_dwordx4 v[176:179], v250, s[66:67] sc1\n\t"
      "global_load_dwordx4 v[180:183], v250, s[66:67] offset:1024 sc1\n\t"
      "global_load_dwordx4 v[184:187], v250, s[66:67] offset:2048 sc1\n\t"
      "global_load_dwordx4 v[188:191], v250, s[66:67] offset:3072 sc1\n\t"
      "s_waitcnt vmcnt(0)\n\t"
      "v_max3_u32 v251, v160, v161, v162\n\t"
      "v_max3_u32 v251, v251, v163, v164\n\t"
      "v_max3_u32 v251, v251, v165, v166\n\t"
      "v_max3_u32 v251, v251, v167, v168\n\t"
      "v_max3_u32 v251, v251, v169, v170\n\t"
      "v_max3_u32 v251, v251, v171, v172\n\t"
      "v_max3_u32 v251, v251, v173, v174\n\t"
      "v_max3_u32 v251, v251, v175, v176\n\t"
      "v_max3_u32 v251, v251, v177, v178\n\t"
      "v_max3_u32 v251, v251, v179, v180\n\t"
      "v_max3_u32 v251, v251, v181, v182\n\t"
      "v_max3_u32 v251, v251, v183, v184\n\t"
      "v_max3_u32 v251, v251, v185, v186\n\t"
      "v_max3_u32 v251, v251, v187, v188\n\t"
      "v_max3_u32 v251, v251, v189, v190\n\t"
      "v_max_u32_e32 v251, v251, v191\n\t"
      "v_cmp_eq_u32_e32 vcc, -1, v251\n\t"
      "s_cbranch_vccnz LA31L_%=\n\t"
      "LA31X_%=:\n\t"
      "v_mfma_f32_16x16x32_bf16 v[224:227], v[0:3], v[160:163], 0\n\t"
      "v_mfma_f32_16x16x32_bf16 v[224:227], v[16:19], v[164:167], v[224:227]\n\t"
      "v_mfma_f32_16x16x32_bf16 v[224:227], v[32:35], v[168:171], v[224:227]\n\t"
      "v_mfma_f32_16x16x32_bf16 v[224:227], v[48:51], v[172:175], v[224:227]\n\t"
      "v_mfma_f32_16x16x32_bf16 v[224:227], v[64:67], v[176:179], v[224:227]\n\t"
      "v_mfma_f32_16x16x32_bf16 v[224:227], v[80:83], v[180:183], v[224:227]\n\t"
      "v_mfma_f32_16x16x32_bf16 v[224:227], v[96:99], v[184:187], v[224:227]\n\t"
      "v_mfma_f32_16x16x32_bf16 v[224:227], v[112:115], v[188:191], v[224:227]\n\t"
      "v_mfma_f32_16x16x32_bf16 v[208:211], v[4:7], v[160:163], 0\n\t"
      "v_mfma_f32_16x16x32_bf16 v[208:211], v[20:23], v[164:167], v[208:211]\n\t"
      "v_mfma_f32_16x16x32_bf16 v[208:211], v[36:39], v[168:171], v[208:211]\n\t"
      "s_nop 7\n\t"
      "s_branch LA31B_%=\n\t"
      "LA32R_%=:\n\t"
      "s_mov_b32 s80, 0\n\t"
      "LA32L_%=:\n\t"
      "s_add_u32 s80, s80, 1\n\t"
      "s_cmp_lt_u32 s80, 262144\n\t"
      "s_cbranch_scc0 LA32X_%=\n\t"
      "s_waitcnt vmcnt(0)\n\t"
      "s_sleep 1\n\t"
      "global_load_dwordx4 v[192:195], v250, s[68:69] offset:-4096 sc1\n\t"
      "global_load_dwordx4 v[196:199], v250, s[68:69] offset:-3072 sc1\n\t"
      "global_load_dwordx4 v[200:203], v250, s[68:69] offset:-2048 sc1\n\t"
      "global_load_dwordx4 v[204:207], v250, s[68:69] offset:-1024 sc1\n\t"
      "global_load_dwordx4 v[128:131], v250, s[68:69] sc1\n\t"
      "global_load_dwordx4 v[132:135], v250, s[68:69] offset:1024 sc1\n\t"
      "global_load_dwordx4 v[136:139], v250, s[68:69] offset:2048 sc1\n\t"
      "global_load_dwordx4 v[140:143], v250, s[68:69] offset:3072 sc1\n\t"
      "s_waitcnt vmcnt(0)\n\t"
      "v_max3_u32 v251, v192, v193, v194\n\t"
      "v_max3_u32 v251, v251, v195, v196\n\t"
      "v_max3_u32 v251, v251, v197, v198\n\t"
      "v_max3_u32 v251, v251, v199, v200\n\t"
      "v_max3_u32 v251, v251, v201, v202\n\t"
      "v_max3_u32 v251, v251, v203, v204\n\t"
      "v_max3_u32 v251, v251, v205, v206\n\t"
      "v_max3_u32 v251, v251, v207, v128\n\t"
      "v_max3_u32 v251, v251, v129, v130\n\t"
      "v_max3_u32 v251, v251, v131, v132\n\t"
      "v_max3_u32 v251, v251, v133, v134\n\t"
      "v_max3_u32 v251, v251, v135, v136\n\t"
      "v_max3_u32 v251, v251, v137, v138\n\t"
      "v_max3_u32 v251, v251, v139, v140\n\t"
      "v_max3_u32 v251, v251, v141, v142\n\t"
      "v_max_u32_e32 v251, v251, v143\n\t"
      "v_cmp_eq_u32_e32 vcc, -1, v251\n\t"
      "s_cbranch_vccnz LA32L_%=\n\t"
      "LA32X_%=:\n\t"
      "v_mfma_f32_16x16x32_bf16 v[220:223], v[0:3], v[192:195], 0\n\t"
      "v_mfma_f32_16x16x32_bf16 v[220:223], v[16:19], v[196:199], v[220:223]\n\t"
      "v_mfma_f32_16x16x32_bf16 v[220:223], v[32:35], v[200:203], v[220:223]\n\t"
      "v_mfma_f32_16x16x32_bf16 v[220:223], v[48:51], v[204:207], v[220:223]\n\t"
      "v_mfma_f32_16x16x32_bf16 v[220:223], v[64:67], v[128:131], v[220:223]\n\t"
      "v_mfma_f32_16x16x32_bf16 v[220:223], v[80:83], v[132:135], v[220:223]\n\t"
      "v_mfma_f32_16x16x32_bf16 v[220:223], v[96:99], v[136:139], v[220:223]\n\t"
      "v_mfma_f32_16x16x32_bf16 v[220:223], v[112:115], v[140:143], v[220:223]\n\t"
      "v_mfma_f32_16x16x32_bf16 v[224:227], v[4:7], v[192:195], 0\n\t"
      "v_mfma_f32_16x16x32_bf16 v[224:227], v[20:23], v[196:199], v[224:227]\n\t"
      "v_mfma_f32_16x16x32_bf16 v[224:227], v[36:39], v[200:203], v[224:227]\n\t"
      "s_nop 7\n\t"
      "s_branch LA32B_%=\n\t"
      "LA33R_%=:\n\t"
      "s_mov_b32 s80, 0\n\t"
      "LA33L_%=:\n\t"
      "s_add_u32 s80, s80, 1\n\t"
      "s_cmp_lt_u32 s80, 262144\n\t"
      "s_cbranch_scc0 LA33X_%=\n\t"
      "s_waitcnt vmcnt(0)\n\t"
      "s_sleep 1\n\t"
      "global_load_dwordx4 v[144:147], v250, s[70:71] offset:-4096 sc1\n\t"
      "global_load_dwordx4 v[148:151], v250, s[70:71] offset:-3072 sc1\n\t"
      "global_load_dwordx4 v[152:155], v250, s[70:71] offset:-2048 sc1\n\t"
      "global_load_dwordx4 v[156:159], v250, s[70:71] offset:-1024 sc1\n\t"
      "global_load_dwordx4 v[160:163], v250, s[70:71] sc1\n\t"
      "global_load_dwordx4 v[164:167], v250, s[70:71] offset:1024 sc1\n\t"
      "global_load_dwordx4 v[168:171], v250, s[70:71] offset:2048 sc1\n\t"
      "global_load_dwordx4 v[172:175], v250, s[70:71] offset:3072 sc1\n\t"
      "s_waitcnt vmcnt(0)\n\t"
      "v_max3_u32 v251, v144, v145, v146\n\t"
      "v_max3_u32 v251, v251, v147, v148\n\t"
      "v_max3_u32 v251, v251, v149, v150\n\t"
      "v_max3_u32 v251, v251, v151, v152\n\t"
      "v_max3_u32 v251, v251, v153, v154\n\t"
      "v_max3_u32 v251, v251, v155, v156\n\t"
      "v_max3_u32 v251, v251, v157, v158\n\t"
      "v_max3_u32 v251, v251, v159, v160\n\t"
      "v_max3_u32 v251, v251, v161, v162\n\t"
      "v_max3_u32 v251, v251, v163, v164\n\t"
      "v_max3_u32 v251, v251, v165, v166\n\t"
      "v_max3_u32 v251, v251, v167, v168\n\t"
      "v_max3_u32 v251, v251, v169, v170\n\t"
      "v_max3_u32 v251, v251, v171, v172\n\t"
      "v_max3_u32 v251, v251, v173, v174\n\t"
      "v_max_u32_e32 v251, v251, v175\n\t"
      "v_cmp_eq_u32_e32 vcc, -1, v251\n\t"
      "s_cbranch_vccnz LA33L_%=\n\t"
      "LA33X_%=:\n\t"
      "v_mfma_f32_16x16x32_bf16 v[216:219], v[0:3], v[144:147], 0\n\t"
      "v_mfma_f32_16x16x32_bf16 v[216:219], v[16:19], v[148:151], v[216:219]\n\t"
      "v_mfma_f32_16x16x32_bf16 v[216:219], v[32:35], v[152:155], v[216:219]\n\t"
      "v_mfma_f32_16x16x32_bf16 v[216:219], v[48:51], v[156:159], v[216:219]\n\t"
      "v_mfma_f32_16x16x32_bf16 v[216:219], v[64:67], v[160:163], v[216:219]\n\t"
      "v_mfma_f32_16x16x32_bf16 v[216:219], v[80:83], v[164:167], v[216:219]\n\t"
      "v_mfma_f32_16x16x32_bf16 v[216:219], v[96:99], v[168:171], v[216:219]\n\t"
      "v_mfma_f32_16x16x32_bf16 v[216:219], v[112:115], v[172:175], v[216:219]\n\t"
      "v_mfma_f32_16x16x32_bf16 v[220:223], v[4:7], v[144:147], 0\n\t"
      "v_mfma_f32_16x16x32_bf16 v[220:223], v[20:23], v[148:151], v[220:223]\n\t"
      "v_mfma_f32_16x16x32_bf16 v[220:223], v[36:39], v[152:155], v[220:223]\n\t"
      "s_nop 7\n\t"
      "s_branch LA33B_%=\n\t"
      "LA34R_%=:\n\t"
      "s_mov_b32 s80, 0\n\t"
      "LA34L_%=:\n\t"
      "s_add_u32 s80, s80, 1\n\t"
      "s_cmp_lt_u32 s80, 262144\n\t"
      "s_cbranch_scc0 LA34X_%=\n\t"
      "s_waitcnt vmcnt(0)\n\t"
      "s_sleep 1\n\t"
      "global_load_dwordx4 v[176:179], v250, s[72:73] offset:-4096 sc1\n\t"
      "global_load_dwordx4 v[180:183], v250, s[72:73] offset:-3072 sc1\n\t"
      "global_load_dwordx4 v[184:187], v250, s[72:73] offset:-2048 sc1\n\t"
      "global_load_dwordx4 v[188:191], v250, s[72:73] offset:-1024 sc1\n\t"
      "global_load_dwordx4 v[192:195], v250, s[72:73] sc1\n\t"
      "global_load_dwordx4 v[196:199], v250, s[72:73] offset:1024 sc1\n\t"
      "global_load_dwordx4 v[200:203], v250, s[72:73] offset:2048 sc1\n\t"
      "global_load_dwordx4 v[204:207], v250, s[72:73] offset:3072 sc1\n\t"
      "s_waitcnt vmcnt(0)\n\t"
      "v_max3_u32 v251, v176, v177, v178\n\t"
      "v_max3_u32 v251, v251, v179, v180\n\t"
      "v_max3_u32 v251, v251, v181, v182\n\t"
      "v_max3_u32 v251, v251, v183, v184\n\t"
      "v_max3_u32 v251, v251, v185, v186\n\t"
      "v_max3_u32 v251, v251, v187, v188\n\t"
      "v_max3_u32 v251, v251, v189, v190\n\t"
      "v_max3_u32 v251, v251, v191, v192\n\t"
      "v_max3_u32 v251, v251, v193, v194\n\t"
      "v_max3_u32 v251, v251, v195, v196\n\t"
      "v_max3_u32 v251, v251, v197, v198\n\t"
      "v_max3_u32 v251, v251, v199, v200\n\t"
      "v_max3_u32 v251, v251, v201, v202\n\t"
      "v_max3_u32 v251, v251, v203, v204\n\t"
      "v_max3_u32 v251, v251, v205, v206\n\t"
      "v_max_u32_e32 v251, v251, v207\n\t"
      "v_cmp_eq_u32_e32 vcc, -1, v251\n\t"
      "s_cbranch_vccnz LA34L_%=\n\t"
      "LA34X_%=:\n\t"
      "v_mfma_f32_16x16x32_bf16 v[212:215], v[0:3], v[176:179], 0\n\t"
      "v_mfma_f32_16x16x32_bf16 v[212:215], v[16:19], v[180:183], v[212:215]\n\t"
      "v_mfma_f32_16x16x32_bf16 v[212:215], v[32:35], v[184:187], v[212:215]\n\t"
      "v_mfma_f32_16x16x32_bf16 v[212:215], v[48:51], v[188:191], v[212:215]\n\t"
      "v_mfma_f32_16x16x32_bf16 v[212:215], v[64:67], v[192:195], v[212:215]\n\t"
      "v_mfma_f32_16x16x32_bf16 v[212:215], v[80:83], v[196:199], v[212:215]\n\t"
      "v_mfma_f32_16x16x32_bf16 v[212:215], v[96:99], v[200:203], v[212:215]\n\t"
      "v_mfma_f32_16x16x32_bf16 v[212:215], v[112:115], v[204:207], v[212:215]\n\t"
      "v_mfma_f32_16x16x32_bf16 v[216:219], v[4:7], v[176:179], 0\n\t"
      "v_mfma_f32_16x16x32_bf16 v[216:219], v[20:23], v[180:183], v[216:219]\n\t"
      "v_mfma_f32_16x16x32_bf16 v[216:219], v[36:39], v[184:187], v[216:219]\n\t"
      "s_nop 7\n\t"
      "s_branch LA34B_%=\n\t"
      "LA35R_%=:\n\t"
      "s_mov_b32 s80, 0\n\t"
      "LA35L_%=:\n\t"
      "s_add_u32 s80, s80, 1\n\t"
      "s_cmp_lt_u32 s80, 262144\n\t"
      "s_cbranch_scc0 LA35X_%=\n\t"
      "s_waitcnt vmcnt(0)\n\t"
      "s_sleep 1\n\t"
      "global_load_dwordx4 v[128:131], v250, s[74:75] offset:-4096 sc1\n\t"
      "global_load_dwordx4 v[132:135], v250, s[74:75] offset:-3072 sc1\n\t"
      "global_load_dwordx4 v[136:139], v250, s[74:75] offset:-2048 sc1\n\t"
      "global_load_dwordx4 v[140:143], v250, s[74:75] offset:-1024 sc1\n\t"
      "global_load_dwordx4 v[144:147], v250, s[74:75] sc1\n\t"
      "global_load_dwordx4 v[148:151], v250, s[74:75] offset:1024 sc1\n\t"
      "global_load_dwordx4 v[152:155], v250, s[74:75] offset:2048 sc1\n\t"
      "global_load_dwordx4 v[156:159], v250, s[74:75] offset:3072 sc1\n\t"
      "s_waitcnt vmcnt(0)\n\t"
      "v_max3_u32 v251, v128, v129, v130\n\t"
      "v_max3_u32 v251, v251, v131, v132\n\t"
      "v_max3_u32 v251, v251, v133, v134\n\t"
      "v_max3_u32 v251, v251, v135, v136\n\t"
      "v_max3_u32 v251, v251, v137, v138\n\t"
      "v_max3_u32 v251, v251, v139, v140\n\t"
      "v_max3_u32 v251, v251, v141, v142\n\t"
      "v_max3_u32 v251, v251, v143, v144\n\t"
      "v_max3_u32 v251, v251, v145, v146\n\t"
      "v_max3_u32 v251, v251, v147, v148\n\t"
      "v_max3_u32 v251, v251, v149, v150\n\t"
      "v_max3_u32 v251, v251, v151, v152\n\t"
      "v_max3_u32 v251, v251, v153, v154\n\t"
      "v_max3_u32 v251, v251, v155, v156\n\t"
      "v_max3_u32 v251, v251, v157, v158\n\t"
      "v_max_u32_e32 v251, v251, v159\n\t"
      "v_cmp_eq_u32_e32 vcc, -1, v251\n\t"
      "s_cbranch_vccnz LA35L_%=\n\t"
      "LA35X_%=:\n\t"
      "v_mfma_f32_16x16x32_bf16 v[208:211], v[0:3], v[128:131], 0\n\t"
      "v_mfma_f32_16x16x32_bf16 v[208:211], v[16:19], v[132:135], v[208:211]\n\t"
      "v_mfma_f32_16x16x32_bf16 v[208:211], v[32:35], v[136:139], v[208:211]\n\t"
      "v_mfma_f32_16x16x32_bf16 v[208:211], v[48:51], v[140:143], v[208:211]\n\t"
      "v_mfma_f32_16x16x32_bf16 v[208:211], v[64:67], v[144:147], v[208:211]\n\t"
      "v_mfma_f32_16x16x32_bf16 v[208:211], v[80:83], v[148:151], v[208:211]\n\t"
      "v_mfma_f32_16x16x32_bf16 v[208:211], v[96:99], v[152:155], v[208:211]\n\t"
      "v_mfma_f32_16x16x32_bf16 v[208:211], v[112:115], v[156:159], v[208:211]\n\t"
      "v_mfma_f32_16x16x32_bf16 v[212:215], v[4:7], v[128:131], 0\n\t"
      "v_mfma_f32_16x16x32_bf16 v[212:215], v[20:23], v[132:135], v[212:215]\n\t"
      "v_mfma_f32_16x16x32_bf16 v[212:215], v[36:39], v[136:139], v[212:215]\n\t"
      "s_nop 7\n\t"
      "s_branch LA35B_%=\n\t"
      "LA36R_%=:\n\t"
      "s_mov_b32 s80, 0\n\t"
      "LA36L_%=:\n\t"
      "s_add_u32 s80, s80, 1\n\t"
      "s_cmp_lt_u32 s80, 262144\n\t"
      "s_cbranch_scc0 LA36X_%=\n\t"
      "s_waitcnt vmcnt(0)\n\t"
      "s_sleep 1\n\t"
      "global_load_dwordx4 v[160:163], v250, s[76:77] offset:-4096 sc1\n\t"
      "global_load_dwordx4 v[164:167], v250, s[76:77] offset:-3072 sc1\n\t"
      "global_load_dwordx4 v[168:171], v250, s[76:77] offset:-2048 sc1\n\t"
      "global_load_dwordx4 v[172:175], v250, s[76:77] offset:-1024 sc1\n\t"
      "global_load_dwordx4 v[176:179], v250, s[76:77] sc1\n\t"
      "global_load_dwordx4 v[180:183], v250, s[76:77] offset:1024 sc1\n\t"
      "global_load_dwordx4 v[184:187], v250, s[76:77] offset:2048 sc1\n\t"
      "global_load_dwordx4 v[188:191], v250, s[76:77] offset:3072 sc1\n\t"
      "s_waitcnt vmcnt(0)\n\t"
      "v_max3_u32 v251, v160, v161, v162\n\t"
      "v_max3_u32 v251, v251, v163, v164\n\t"
      "v_max3_u32 v251, v251, v165, v166\n\t"
      "v_max3_u32 v251, v251, v167, v168\n\t"
      "v_max3_u32 v251, v251, v169, v170\n\t"
      "v_max3_u32 v251, v251, v171, v172\n\t"
      "v_max3_u32 v251, v251, v173, v174\n\t"
      "v_max3_u32 v251, v251, v175, v176\n\t"
      "v_max3_u32 v251, v251, v177, v178\n\t"
      "v_max3_u32 v251, v251, v179, v180\n\t"
      "v_max3_u32 v251, v251, v181, v182\n\t"
      "v_max3_u32 v251, v251, v183, v184\n\t"
      "v_max3_u32 v251, v251, v185, v186\n\t"
      "v_max3_u32 v251, v251, v187, v188\n\t"
      "v_max3_u32 v251, v251, v189, v190\n\t"
      "v_max_u32_e32 v251, v251, v191\n\t"
      "v_cmp_eq_u32_e32 vcc, -1, v251\n\t"
      "s_cbranch_vccnz LA36L_%=\n\t"
      "LA36X_%=:\n\t"
      "v_mfma_f32_16x16x32_bf16 v[224:227], v[0:3], v[160:163], 0\n\t"
      "v_mfma_f32_16x16x32_bf16 v[224:227], v[16:19], v[164:167], v[224:227]\n\t"
      "v_mfma_f32_16x16x32_bf16 v[224:227], v[32:35], v[168:171], v[224:227]\n\t"
      "v_mfma_f32_16x16x32_bf16 v[224:227], v[48:51], v[172:175], v[224:227]\n\t"
      "v_mfma_f32_16x16x32_bf16 v[224:227], v[64:67], v[176:179], v[224:227]\n\t"
      "v_mfma_f32_16x16x32_bf16 v[224:227], v[80:83], v[180:183], v[224:227]\n\t"
      "v_mfma_f32_16x16x32_bf16 v[224:227], v[96:99], v[184:187], v[224:227]\n\t"
      "v_mfma_f32_16x16x32_bf16 v[224:227], v[112:115], v[188:191], v[224:227]\n\t"
      "v_mfma_f32_16x16x32_bf16 v[208:211], v[4:7], v[160:163], 0\n\t"
      "v_mfma_f32_16x16x32_bf16 v[208:211], v[20:23], v[164:167], v[208:211]\n\t"
      "v_mfma_f32_16x16x32_bf16 v[208:211], v[36:39], v[168:171], v[208:211]\n\t"
      "s_nop 7\n\t"
      "s_branch LA36B_%=\n\t"
      "LF5R_%=:\n\t"
      "s_mov_b32 s80, 0\n\t"
      "LF5L_%=:\n\t"
      "s_add_u32 s80, s80, 1\n\t"
      "s_cmp_lt_u32 s80, 262144\n\t"
      "s_cbranch_scc0 LF5X_%=\n\t"
      "s_sleep 2\n\t"
      "global_load_dwordx2 v[138:139], v143, s[60:61] sc1\n\t"
      "s_waitcnt vmcnt(0)\n\t"
      "v_cmp_ne_u64_e32 vcc, s[22:23], v[138:139]\n\t"
      "s_cbranch_vccnz LF5L_%=\n\t"
      "LF5X_%=:\n\t"
      "s_branch LF5B_%=\n\t"
      "PG0R_%=:\n\t"
      "s_mov_b32 s80, 0\n\t"
      "PG0L_%=:\n\t"
      "s_add_u32 s80, s80, 1\n\t"
      "s_cmp_lt_u32 s80, 262144\n\t"
      "s_cbranch_scc0 PG0X_%=\n\t"
      "s_sleep 2\n\t"
      "global_load_dword v251, v253, s[64:65] offset:252 sc1\n\t"
      "s_waitcnt vmcnt(0)\n\t"
      "v_cmp_eq_u32_e32 vcc, -1, v251\n\t"
      "s_cbranch_vccnz PG0L_%=\n\t"
      "PG0X_%=:\n\t"
      "s_branch PG0B_%=\n\t"
      "LF8R_%=:\n\t"
      "s_mov_b32 s80, 0\n\t"
      "LF8L_%=:\n\t"
      "s_add_u32 s80, s80, 1\n\t"
      "s_cmp_lt_u32 s80, 262144\n\t"
      "s_cbranch_scc0 LF8X_%=\n\t"
      "s_sleep 2\n\t"
      "global_load_dwordx2 v[240:241], v242, s[60:61] sc1\n\t"
      "s_waitcnt vmcnt(0)\n\t"
      "v_cmp_ne_u64_e32 vcc, s[22:23], v[240:241]\n\t"
      "s_cbranch_vccnz LF8L_%=\n\t"
      "LF8X_%=:\n\t"
      "s_branch LF8B_%=\n\t"
      "LA40R_%=:\n\t"
      "s_mov_b32 s80, 0\n\t"
      "LA40L_%=:\n\t"
      "s_add_u32 s80, s80, 1\n\t"
      "s_cmp_lt_u32 s80, 262144\n\t"
      "s_cbranch_scc0 LA40X_%=\n\t"
      "s_waitcnt vmcnt(0)\n\t"
      "PQ0R_%=:\n\t"
      "PQ0L_%=:\n\t"
      "s_add_u32 s80, s80, 1\n\t"
      "s_cmp_lt_u32 s80, 262144\n\t"
      "s_cbranch_scc0 PQ0X_%=\n\t"
      "s_sleep 1\n\t"
      "global_load_dword v251, v252, s[64:65] offset:252 sc1\n\t"
      "s_waitcnt vmcnt(0)\n\t"
      "v_cmp_eq_u32_e32 vcc, -1, v251\n\t"
      "s_cbranch_vccnz PQ0L_%=\n\t"
      "PQ0X_%=:\n\t"
      "s_add_u32 s58, s64, s40\n\t"
      "s_addc_u32 s59, s65, 0\n\t"
      "global_load_dwordx4 v[128:131], v244, s[58:59] sc1\n\t"
      "s_add_u32 s58, s64, s41\n\t"
      "s_addc_u32 s59, s65, 0\n\t"
      "global_load_dwordx4 v[132:135], v244, s[58:59] sc1\n\t"
      "s_add_u32 s58, s64, s42\n\t"
      "s_addc_u32 s59, s65, 0\n\t"
      "global_load_dwordx4 v[136:139], v244, s[58:59] sc1\n\t"
      "s_add_u32 s58, s64, s43\n\t"
      "s_addc_u32 s59, s65, 0\n\t"
      "global_load_dwordx4 v[140:143], v244, s[58:59] sc1\n\t"
      "s_add_u32 s58, s64, s44\n\t"
      "s_addc_u32 s59, s65, 0\n\t"
      "global_load_dwordx4 v[144:147], v244, s[58:59] sc1\n\t"
      "s_add_u32 s58, s64, s45\n\t"
      "s_addc_u32 s59, s65, 0\n\t"
      "global_load_dwordx4 v[148:151], v244, s[58:59] sc1\n\t"
      "s_add_u32 s58, s64, s46\n\t"
      "s_addc_u32 s59, s65, 0\n\t"
      "global_load_dwordx4 v[152:155], v244, s[58:59] sc1\n\t"
      "s_waitcnt vmcnt(0)\n\t"
      "v_max3_u32 v251, v128, v129, v130\n\t"
      "v_max3_u32 v251, v251, v131, v132\n\t"
      "v_max3_u32 v251, v251, v133, v134\n\t"
      "v_max3_u32 v251, v251, v135, v136\n\t"
      "v_max3_u32 v251, v251, v137, v138\n\t"
      "v_max3_u32 v251, v251, v139, v140\n\t"
      "v_max3_u32 v251, v251, v141, v142\n\t"
      "v_max3_u32 v251, v251, v143, v144\n\t"
      "v_max3_u32 v251, v251, v145, v146\n\t"
      "v_max3_u32 v251, v251, v147, v148\n\t"
      "v_max3_u32 v251, v251, v149, v150\n\t"
      "v_max3_u32 v251, v251, v151, v152\n\t"
      "v_max3_u32 v251, v251, v153, v154\n\t"
      "v_max_u32_e32 v251, v251, v155\n\t"
      "v_cmp_eq_u32_e32 vcc, -1, v251\n\t"
      "s_cbranch_vccnz LA40L_%=\n\t"
      "LA40X_%=:\n\t"
      "v_mfma_f32_16x16x32_bf16 v[196:199], v[128:131], v[0:3], 0\n\t"
      "v_mfma_f32_16x16x32_bf16 v[200:203], v[128:131], v[4:7], 0\n\t"
      "v_mfma_f32_16x16x32_bf16 v[204:207], v[128:131], v[8:11], 0\n\t"
      "v_mfma_f32_16x16x32_bf16 v[208:211], v[128:131], v[12:15], 0\n\t"
      "v_mfma_f32_16x16x32_bf16 v[196:199], v[132:135], v[16:19], v[196:199]\n\t"
      "v_mfma_f32_16x16x32_bf16 v[200:203], v[132:135], v[20:23], v[200:203]\n\t"
      "v_mfma_f32_16x16x32_bf16 v[204:207], v[132:135], v[24:27], v[204:207]\n\t"
      "v_mfma_f32_16x16x32_bf16 v[208:211], v[132:135], v[28:31], v[208:211]\n\t"
      "v_mfma_f32_16x16x32_bf16 v[196:199], v[136:139], v[32:35], v[196:199]\n\t"
      "v_mfma_f32_16x16x32_bf16 v[200:203], v[136:139], v[36:39], v[200:203]\n\t"
      "v_mfma_f32_16x16x32_bf16 v[204:207], v[136:139], v[40:43], v[204:207]\n\t"
      "v_mfma_f32_16x16x32_bf16 v[208:211], v[136:139], v[44:47], v[208:211]\n\t"
      "v_mfma_f32_16x16x32_bf16 v[196:199], v[140:143], v[48:51], v[196:199]\n\t"
      "v_mfma_f32_16x16x32_bf16 v[200:203], v[140:143], v[52:55], v[200:203]\n\t"
      "v_mfma_f32_16x16x32_bf16 v[204:207], v[140:143], v[56:59], v[204:207]\n\t"
      "v_mfma_f32_16x16x32_bf16 v[208:211], v[140:143], v[60:63], v[208:211]\n\t"
      "v_mfma_f32_16x16x32_bf16 v[196:199], v[144:147], v[64:67], v[196:199]\n\t"
      "v_mfma_f32_16x16x32_bf16 v[200:203], v[144:147], v[68:71], v[200:203]\n\t"
      "v_mfma_f32_16x16x32_bf16 v[204:207], v[144:147], v[72:75], v[204:207]\n\t"
      "v_mfma_f32_16x16x32_bf16 v[208:211], v[144:147], v[76:79], v[208:211]\n\t"
      "v_mfma_f32_16x16x32_bf16 v[196:199], v[148:151], v[80:83], v[196:199]\n\t"
      "v_mfma_f32_16x16x32_bf16 v[200:203], v[148:151], v[84:87], v[200:203]\n\t"
      "v_mfma_f32_16x16x32_bf16 v[204:207], v[148:151], v[88:91], v[204:207]\n\t"
      "v_mfma_f32_16x16x32_bf16 v[208:211], v[148:151], v[92:95], v[208:211]\n\t"
      "v_mfma_f32_16x16x32_bf16 v[196:199], v[152:155], v[96:99], v[196:199]\n\t"
      "v_mfma_f32_16x16x32_bf16 v[200:203], v[152:155], v[100:103], v[200:203]\n\t"
      "v_mfma_f32_16x16x32_bf16 v[204:207], v[152:155], v[104:107], v[204:207]\n\t"
      "v_mfma_f32_16x16x32_bf16 v[208:211], v[152:155], v[108:111], v[208:211]\n\t"
      "s_nop 7\n\t"
      "s_branch LA40B_%=\n\t"
      "LA41R_%=:\n\t"
      "s_mov_b32 s80, 0\n\t"
      "LA41L_%=:\n\t"
      "s_add_u32 s80, s80, 1\n\t"
      "s_cmp_lt_u32 s80, 262144\n\t"
      "s_cbranch_scc0 LA41X_%=\n\t"
      "s_waitcnt vmcnt(0)\n\t"
      "PQ1R_%=:\n\t"
      "PQ1L_%=:\n\t"
      "s_add_u32 s80, s80, 1\n\t"
      "s_cmp_lt_u32 s80, 262144\n\t"
      "s_cbranch_scc0 PQ1X_%=\n\t"
      "s_sleep 1\n\t"
      "global_load_dword v251, v252, s[66:67] offset:252 sc1\n\t"
      "s_waitcnt vmcnt(0)\n\t"
      "v_cmp_eq_u32_e32 vcc, -1, v251\n\t"
      "s_cbranch_vccnz PQ1L_%=\n\t"
      "PQ1X_%=:\n\t"
      "s_add_u32 s58, s66, s40\n\t"
      "s_addc_u32 s59, s67, 0\n\t"
      "global_load_dwordx4 v[156:159], v244, s[58:59] sc1\n\t"
      "s_add_u32 s58, s66, s41\n\t"
      "s_addc_u32 s59, s67, 0\n\t"
      "global_load_dwordx4 v[160:163], v244, s[58:59] sc1\n\t"
      "s_add_u32 s58, s66, s42\n\t"
      "s_addc_u32 s59, s67, 0\n\t"
      "global_load_dwordx4 v[164:167], v244, s[58:59] sc1\n\t"
      "s_add_u32 s58, s66, s43\n\t"
      "s_addc_u32 s59, s67, 0\n\t"
      "global_load_dwordx4 v[168:171], v244, s[58:59] sc1\n\t"
      "s_add_u32 s58, s66, s44\n\t"
      "s_addc_u32 s59, s67, 0\n\t"
      "global_load_dwordx4 v[172:175], v244, s[58:59] sc1\n\t"
      "s_add_u32 s58, s66, s45\n\t"
      "s_addc_u32 s59, s67, 0\n\t"
      "global_load_dwordx4 v[176:179], v244, s[58:59] sc1\n\t"
      "s_add_u32 s58, s66, s46\n\t"
      "s_addc_u32 s59, s67, 0\n\t"
      "global_load_dwordx4 v[180:183], v244, s[58:59] sc1\n\t"
      "s_waitcnt vmcnt(0)\n\t"
      "v_max3_u32 v251, v156, v157, v158\n\t"
      "v_max3_u32 v251, v251, v159, v160\n\t"
      "v_max3_u32 v251, v251, v161, v162\n\t"
      "v_max3_u32 v251, v251, v163, v164\n\t"
      "v_max3_u32 v251, v251, v165, v166\n\t"
      "v_max3_u32 v251, v251, v167, v168\n\t"
      "v_max3_u32 v251, v251, v169, v170\n\t"
      "v_max3_u32 v251, v251, v171, v172\n\t"
      "v_max3_u32 v251, v251, v173, v174\n\t"
      "v_max3_u32 v251, v251, v175, v176\n\t"
      "v_max3_u32 v251, v251, v177, v178\n\t"
      "v_max3_u32 v251, v251, v179, v180\n\t"
      "v_max3_u32 v251, v251, v181, v182\n\t"
      "v_max_u32_e32 v251, v251, v183\n\t"
      "v_cmp_eq_u32_e32 vcc, -1, v251\n\t"
      "s_cbranch_vccnz LA41L_%=\n\t"
      "LA41X_%=:\n\t"
      "v_mfma_f32_16x16x32_bf16 v[212:215], v[156:159], v[0:3], 0\n\t"
      "v_mfma_f32_16x16x32_bf16 v[216:219], v[156:159], v[4:7], 0\n\t"
      "v_mfma_f32_16x16x32_bf16 v[220:223], v[156:159], v[8:11], 0\n\t"
      "v_mfma_f32_16x16x32_bf16 v[224:227], v[156:159], v[12:15], 0\n\t"
      "v_mfma_f32_16x16x32_bf16 v[212:215], v[160:163], v[16:19], v[212:215]\n\t"
      "v_mfma_f32_16x16x32_bf16 v[216:219], v[160:163], v[20:23], v[216:219]\n\t"
      "v_mfma_f32_16x16x32_bf16 v[220:223], v[160:163], v[24:27], v[220:223]\n\t"
      "v_mfma_f32_16x16x32_bf16 v[224:227], v[160:163], v[28:31], v[224:227]\n\t"
      "v_mfma_f32_16x16x32_bf16 v[212:215], v[164:167], v[32:35], v[212:215]\n\t"
      "v_mfma_f32_16x16x32_bf16 v[216:219], v[164:167], v[36:39], v[216:219]\n\t"
      "v_mfma_f32_16x16x32_bf16 v[220:223], v[164:167], v[40:43], v[220:223]\n\t"
      "v_mfma_f32_16x16x32_bf16 v[224:227], v[164:167], v[44:47], v[224:227]\n\t"
      "v_mfma_f32_16x16x32_bf16 v[212:215], v[168:171], v[48:51], v[212:215]\n\t"
      "v_mfma_f32_16x16x32_bf16 v[216:219], v[168:171], v[52:55], v[216:219]\n\t"
      "v_mfma_f32_16x16x32_bf16 v[220:223], v[168:171], v[56:59], v[220:223]\n\t"
      "v_mfma_f32_16x16x32_bf16 v[224:227], v[168:171], v[60:63], v[224:227]\n\t"
      "v_mfma_f32_16x16x32_bf16 v[212:215], v[172:175], v[64:67], v[212:215]\n\t"
      "v_mfma_f32_16x16x32_bf16 v[216:219], v[172:175], v[68:71], v[216:219]\n\t"
      "v_mfma_f32_16x16x32_bf16 v[220:223], v[172:175], v[72:75], v[220:223]\n\t"
      "v_mfma_f32_16x16x32_bf16 v[224:227], v[172:175], v[76:79], v[224:227]\n\t"
      "v_mfma_f32_16x16x32_bf16 v[212:215], v[176:179], v[80:83], v[212:215]\n\t"
      "v_mfma_f32_16x16x32_bf16 v[216:219], v[176:179], v[84:87], v[216:219]\n\t"
      "v_mfma_f32_16x16x32_bf16 v[220:223], v[176:179], v[88:91], v[220:223]\n\t"
      "v_mfma_f32_16x16x32_bf16 v[224:227], v[176:179], v[92:95], v[224:227]\n\t"
      "v_mfma_f32_16x16x32_bf16 v[212:215], v[180:183], v[96:99], v[212:215]\n\t"
      "v_mfma_f32_16x16x32_bf16 v[216:219], v[180:183], v[100:103], v[216:219]\n\t"
      "v_mfma_f32_16x16x32_bf16 v[220:223], v[180:183], v[104:107], v[220:223]\n\t"
      "v_mfma_f32_16x16x32_bf16 v[224:227], v[180:183], v[108:111], v[224:227]\n\t"
      "s_nop 7\n\t"
      "s_branch LA41B_%=\n\t"
      "LA42R_%=:\n\t"
      "s_mov_b32 s80, 0\n\t"
      "LA42L_%=:\n\t"
      "s_add_u32 s80, s80, 1\n\t"
      "s_cmp_lt_u32 s80, 262144\n\t"
      "s_cbranch_scc0 LA42X_%=\n\t"
      "s_waitcnt vmcnt(0)\n\t"
      "PQ2R_%=:\n\t"
      "PQ2L_%=:\n\t"
      "s_add_u32 s80, s80, 1\n\t"
      "s_cmp_lt_u32 s80, 262144\n\t"
      "s_cbranch_scc0 PQ2X_%=\n\t"
      "s_sleep 1\n\t"
      "global_load_dword v251, v252, s[68:69] offset:252 sc1\n\t"
      "s_waitcnt vmcnt(0)\n\t"
      "v_cmp_eq_u32_e32 vcc, -1, v251\n\t"
      "s_cbranch_vccnz PQ2L_%=\n\t"
      "PQ2X_%=:\n\t"
      "s_add_u32 s58, s68, s40\n\t"
      "s_addc_u32 s59, s69, 0\n\t"
      "global_load_dwordx4 v[184:187], v244, s[58:59] sc1\n\t"
      "s_add_u32 s58, s68, s41\n\t"
      "s_addc_u32 s59, s69, 0\n\t"
      "global_load_dwordx4 v[188:191], v244, s[58:59] sc1\n\t"
      "s_add_u32 s58, s68, s42\n\t"
      "s_addc_u32 s59, s69, 0\n\t"
      "global_load_dwordx4 v[192:195], v244, s[58:59] sc1\n\t"
      "s_add_u32 s58, s68, s43\n\t"
      "s_addc_u32 s59, s69, 0\n\t"
      "global_load_dwordx4 v[128:131], v244, s[58:59] sc1\n\t"
      "s_add_u32 s58, s68, s44\n\t"
      "s_addc_u32 s59, s69, 0\n\t"
      "global_load_dwordx4 v[132:135], v244, s[58:59] sc1\n\t"
      "s_add_u32 s58, s68, s45\n\t"
      "s_addc_u32 s59, s69, 0\n\t"
      "global_load_dwordx4 v[136:139], v244, s[58:59] sc1\n\t"
      "s_add_u32 s58, s68, s46\n\t"
      "s_addc_u32 s59, s69, 0\n\t"
      "global_load_dwordx4 v[140:143], v244, s[58:59] sc1\n\t"
      "s_waitcnt vmcnt(0)\n\t"
      "v_max3_u32 v251, v184, v185, v186\n\t"
      "v_max3_u32 v251, v251, v187, v188\n\t"
      "v_max3_u32 v251, v251, v189, v190\n\t"
      "v_max3_u32 v251, v251, v191, v192\n\t"
      "v_max3_u32 v251, v251, v193, v194\n\t"
      "v_max3_u32 v251, v251, v195, v128\n\t"
      "v_max3_u32 v251, v251, v129, v130\n\t"
      "v_max3_u32 v251, v251, v131, v132\n\t"
      "v_max3_u32 v251, v251, v133, v134\n\t"
      "v_max3_u32 v251, v251, v135, v136\n\t"
      "v_max3_u32 v251, v251, v137, v138\n\t"
      "v_max3_u32 v251, v251, v139, v140\n\t"
      "v_max3_u32 v251, v251, v141, v142\n\t"
      "v_max_u32_e32 v251, v251, v143\n\t"
      "v_cmp_eq_u32_e32 vcc, -1, v251\n\t"
      "s_cbranch_vccnz LA42L_%=\n\t"
      "LA42X_%=:\n\t"
      "v_mfma_f32_16x16x32_bf16 v[196:199], v[184:187], v[0:3], 0\n\t"
      "v_mfma_f32_16x16x32_bf16 v[200:203], v[184:187], v[4:7], 0\n\t"
      "v_mfma_f32_16x16x32_bf16 v[204:207], v[184:187], v[8:11], 0\n\t"
      "v_mfma_f32_16x16x32_bf16 v[208:211], v[184:187], v[12:15], 0\n\t"
      "v_mfma_f32_16x16x32_bf16 v[196:199], v[188:191], v[16:19], v[196:199]\n\t"
      "v_mfma_f32_16x16x32_bf16 v[200:203], v[188:191], v[20:23], v[200:203]\n\t"
      "v_mfma_f32_16x16x32_bf16 v[204:207], v[188:191], v[24:27], v[204:207]\n\t"
      "v_mfma_f32_16x16x32_bf16 v[208:211], v[188:191], v[28:31], v[208:211]\n\t"
      "v_mfma_f32_16x16x32_bf16 v[196:199], v[192:195], v[32:35], v[196:199]\n\t"
      "v_mfma_f32_16x16x32_bf16 v[200:203], v[192:195], v[36:39], v[200:203]\n\t"
      "v_mfma_f32_16x16x32_bf16 v[204:207], v[192:195], v[40:43], v[204:207]\n\t"
      "v_mfma_f32_16x16x32_bf16 v[208:211], v[192:195], v[44:47], v[208:211]\n\t"
      "v_mfma_f32_16x16x32_bf16 v[196:199], v[128:131], v[48:51], v[196:199]\n\t"
      "v_mfma_f32_16x16x32_bf16 v[200:203], v[128:131], v[52:55], v[200:203]\n\t"
      "v_mfma_f32_16x16x32_bf16 v[204:207], v[128:131], v[56:59], v[204:207]\n\t"
      "v_mfma_f32_16x16x32_bf16 v[208:211], v[128:131], v[60:63], v[208:211]\n\t"
      "v_mfma_f32_16x16x32_bf16 v[196:199], v[132:135], v[64:67], v[196:199]\n\t"
      "v_mfma_f32_16x16x32_bf16 v[200:203], v[132:135], v[68:71], v[200:203]\n\t"
      "v_mfma_f32_16x16x32_bf16 v[204:207], v[132:135], v[72:75], v[204:207]\n\t"
      "v_mfma_f32_16x16x32_bf16 v[208:211], v[132:135], v[76:79], v[208:211]\n\t"
      "v_mfma_f32_16x16x32_bf16 v[196:199], v[136:139], v[80:83], v[196:199]\n\t"
      "v_mfma_f32_16x16x32_bf16 v[200:203], v[136:139], v[84:87], v[200:203]\n\t"
      "v_mfma_f32_16x16x32_bf16 v[204:207], v[136:139], v[88:91], v[204:207]\n\t"
      "v_mfma_f32_16x16x32_bf16 v[208:211], v[136:139], v[92:95], v[208:211]\n\t"
      "v_mfma_f32_16x16x32_bf16 v[196:199], v[140:143], v[96:99], v[196:199]\n\t"
      "v_mfma_f32_16x16x32_bf16 v[200:203], v[140:143], v[100:103], v[200:203]\n\t"
      "v_mfma_f32_16x16x32_bf16 v[204:207], v[140:143], v[104:107], v[204:207]\n\t"
      "v_mfma_f32_16x16x32_bf16 v[208:211], v[140:143], v[108:111], v[208:211]\n\t"
      "s_nop 7\n\t"
      "s_branch LA42B_%=\n\t"
      "LA43R_%=:\n\t"
      "s_mov_b32 s80, 0\n\t"
      "LA43L_%=:\n\t"
      "s_add_u32 s80, s80, 1\n\t"
      "s_cmp_lt_u32 s80, 262144\n\t"
      "s_cbranch_scc0 LA43X_%=\n\t"
      "s_waitcnt vmcnt(0)\n\t"
      "PQ3R_%=:\n\t"
      "PQ3L_%=:\n\t"
      "s_add_u32 s80, s80, 1\n\t"
      "s_cmp_lt_u32 s80, 262144\n\t"
      "s_cbranch_scc0 PQ3X_%=\n\t"
      "s_sleep 1\n\t"
      "global_load_dword v251, v252, s[70:71] offset:252 sc1\n\t"
      "s_waitcnt vmcnt(0)\n\t"
      "v_cmp_eq_u32_e32 vcc, -1, v251\n\t"
      "s_cbranch_vccnz PQ3L_%=\n\t"
      "PQ3X_%=:\n\t"
      "s_add_u32 s58, s70, s40\n\t"
      "s_addc_u32 s59, s71, 0\n\t"
      "global_load_dwordx4 v[144:147], v244, s[58:59] sc1\n\t"
      "s_add_u32 s58, s70, s41\n\t"
      "s_addc_u32 s59, s71, 0\n\t"
      "global_load_dwordx4 v[148:151], v244, s[58:59] sc1\n\t"
      "s_add_u32 s58, s70, s42\n\t"
      "s_addc_u32 s59, s71, 0\n\t"
      "global_load_dwordx4 v[152:155], v244, s[58:59] sc1\n\t"
      "s_add_u32 s58, s70, s43\n\t"
      "s_addc_u32 s59, s71, 0\n\t"
      "global_load_dwordx4 v[156:159], v244, s[58:59] sc1\n\t"
      "s_add_u32 s58, s70, s44\n\t"
      "s_addc_u32 s59, s71, 0\n\t"
      "global_load_dwordx4 v[160:163], v244, s[58:59] sc1\n\t"
      "s_add_u32 s58, s70, s45\n\t"
      "s_addc_u32 s59, s71, 0\n\t"
      "global_load_dwordx4 v[164:167], v244, s[58:59] sc1\n\t"
      "s_add_u32 s58, s70, s46\n\t"
      "s_addc_u32 s59, s71, 0\n\t"
      "global_load_dwordx4 v[168:171], v244, s[58:59] sc1\n\t"
      "s_waitcnt vmcnt(0)\n\t"
      "v_max3_u32 v251, v144, v145, v146\n\t"
      "v_max3_u32 v251, v251, v147, v148\n\t"
      "v_max3_u32 v251, v251, v149, v150\n\t"
      "v_max3_u32 v251, v251, v151, v152\n\t"
      "v_max3_u32 v251, v251, v153, v154\n\t"
      "v_max3_u32 v251, v251, v155, v156\n\t"
      "v_max3_u32 v251, v251, v157, v158\n\t"
      "v_max3_u32 v251, v251, v159, v160\n\t"
      "v_max3_u32 v251, v251, v161, v162\n\t"
      "v_max3_u32 v251, v251, v163, v164\n\t"
      "v_max3_u32 v251, v251, v165, v166\n\t"
      "v_max3_u32 v251, v251, v167, v168\n\t"
      "v_max3_u32 v251, v251, v169, v170\n\t"
      "v_max_u32_e32 v251, v251, v171\n\t"
      "v_cmp_eq_u32_e32 vcc, -1, v251\n\t"
      "s_cbranch_vccnz LA43L_%=\n\t"
      "LA43X_%=:\n\t"
      "v_mfma_f32_16x16x32_bf16 v[212:215], v[144:147], v[0:3], 0\n\t"
      "v_mfma_f32_16x16x32_bf16 v[216:219], v[144:147], v[4:7], 0\n\t"
      "v_mfma_f32_16x16x32_bf16 v[220:223], v[144:147], v[8:11], 0\n\t"
      "v_mfma_f32_16x16x32_bf16 v[224:227], v[144:147], v[12:15], 0\n\t"
      "v_mfma_f32_16x16x32_bf16 v[212:215], v[148:151], v[16:19], v[212:215]\n\t"
      "v_mfma_f32_16x16x32_bf16 v[216:219], v[148:151], v[20:23], v[216:219]\n\t"
      "v_mfma_f32_16x16x32_bf16 v[220:223], v[148:151], v[24:27], v[220:223]\n\t"
      "v_mfma_f32_16x16x32_bf16 v[224:227], v[148:151], v[28:31], v[224:227]\n\t"
      "v_mfma_f32_16x16x32_bf16 v[212:215], v[152:155], v[32:35], v[212:215]\n\t"
      "v_mfma_f32_16x16x32_bf16 v[216:219], v[152:155], v[36:39], v[216:219]\n\t"
      "v_mfma_f32_16x16x32_bf16 v[220:223], v[152:155], v[40:43], v[220:223]\n\t"
      "v_mfma_f32_16x16x32_bf16 v[224:227], v[152:155], v[44:47], v[224:227]\n\t"
      "v_mfma_f32_16x16x32_bf16 v[212:215], v[156:159], v[48:51], v[212:215]\n\t"
      "v_mfma_f32_16x16x32_bf16 v[216:219], v[156:159], v[52:55], v[216:219]\n\t"
      "v_mfma_f32_16x16x32_bf16 v[220:223], v[156:159], v[56:59], v[220:223]\n\t"
      "v_mfma_f32_16x16x32_bf16 v[224:227], v[156:159], v[60:63], v[224:227]\n\t"
      "v_mfma_f32_16x16x32_bf16 v[212:215], v[160:163], v[64:67], v[212:215]\n\t"
      "v_mfma_f32_16x16x32_bf16 v[216:219], v[160:163], v[68:71], v[216:219]\n\t"
      "v_mfma_f32_16x16x32_bf16 v[220:223], v[160:163], v[72:75], v[220:223]\n\t"
      "v_mfma_f32_16x16x32_bf16 v[224:227], v[160:163], v[76:79], v[224:227]\n\t"
      "v_mfma_f32_16x16x32_bf16 v[212:215], v[164:167], v[80:83], v[212:215]\n\t"
      "v_mfma_f32_16x16x32_bf16 v[216:219], v[164:167], v[84:87], v[216:219]\n\t"
      "v_mfma_f32_16x16x32_bf16 v[220:223], v[164:167], v[88:91], v[220:223]\n\t"
      "v_mfma_f32_16x16x32_bf16 v[224:227], v[164:167], v[92:95], v[224:227]\n\t"
      "v_mfma_f32_16x16x32_bf16 v[212:215], v[168:171], v[96:99], v[212:215]\n\t"
      "v_mfma_f32_16x16x32_bf16 v[216:219], v[168:171], v[100:103], v[216:219]\n\t"
      "v_mfma_f32_16x16x32_bf16 v[220:223], v[168:171], v[104:107], v[220:223]\n\t"
      "v_mfma_f32_16x16x32_bf16 v[224:227], v[168:171], v[108:111], v[224:227]\n\t"
      "s_nop 7\n\t"
      "s_branch LA43B_%=\n\t"
      "LA44R_%=:\n\t"
      "s_mov_b32 s80, 0\n\t"
      "LA44L_%=:\n\t"
      "s_add_u32 s80, s80, 1\n\t"
      "s_cmp_lt_u32 s80, 262144\n\t"
      "s_cbranch_scc0 LA44X_%=\n\t"
      "s_waitcnt vmcnt(0)\n\t"
      "PQ4R_%=:\n\t"
      "PQ4L_%=:\n\t"
      "s_add_u32 s80, s80, 1\n\t"
      "s_cmp_lt_u32 s80, 262144\n\t"
      "s_cbranch_scc0 PQ4X_%=\n\t"
      "s_sleep 1\n\t"
      "global_load_dword v251, v252, s[72:73] offset:252 sc1\n\t"
      "s_waitcnt vmcnt(0)\n\t"
      "v_cmp_eq_u32_e32 vcc, -1, v251\n\t"
      "s_cbranch_vccnz PQ4L_%=\n\t"
      "PQ4X_%=:\n\t"
      "s_add_u32 s58, s72, s40\n\t"
      "s_addc_u32 s59, s73, 0\n\t"
      "global_load_dwordx4 v[172:175], v244, s[58:59] sc1\n\t"
      "s_add_u32 s58, s72, s41\n\t"
      "s_addc_u32 s59, s73, 0\n\t"
      "global_load_dwordx4 v[176:179], v244, s[58:59] sc1\n\t"
      "s_add_u32 s58, s72, s42\n\t"
      "s_addc_u32 s59, s73, 0\n\t"
      "global_load_dwordx4 v[180:183], v244, s[58:59] sc1\n\t"
      "s_add_u32 s58, s72, s43\n\t"
      "s_addc_u32 s59, s73, 0\n\t"
      "global_load_dwordx4 v[184:187], v244, s[58:59] sc1\n\t"
      "s_add_u32 s58, s72, s44\n\t"
      "s_addc_u32 s59, s73, 0\n\t"
      "global_load_dwordx4 v[188:191], v244, s[58:59] sc1\n\t"
      "s_add_u32 s58, s72, s45\n\t"
      "s_addc_u32 s59, s73, 0\n\t"
      "global_load_dwordx4 v[192:195], v244, s[58:59] sc1\n\t"
      "s_add_u32 s58, s72, s46\n\t"
      "s_addc_u32 s59, s73, 0\n\t"
      "global_load_dwordx4 v[128:131], v244, s[58:59] sc1\n\t"
      "s_waitcnt vmcnt(0)\n\t"
      "v_max3_u32 v251, v172, v173, v174\n\t"
      "v_max3_u32 v251, v251, v175, v176\n\t"
      "v_max3_u32 v251, v251, v177, v178\n\t"
      "v_max3_u32 v251, v251, v179, v180\n\t"
      "v_max3_u32 v251, v251, v181, v182\n\t"
      "v_max3_u32 v251, v251, v183, v184\n\t"
      "v_max3_u32 v251, v251, v185, v186\n\t"
      "v_max3_u32 v251, v251, v187, v188\n\t"
      "v_max3_u32 v251, v251, v189, v190\n\t"
      "v_max3_u32 v251, v251, v191, v192\n\t"
      "v_max3_u32 v251, v251, v193, v194\n\t"
      "v_max3_u32 v251, v251, v195, v128\n\t"
      "v_max3_u32 v251, v251, v129, v130\n\t"
      "v_max_u32_e32 v251, v251, v131\n\t"
      "v_cmp_eq_u32_e32 vcc, -1, v251\n\t"
      "s_cbranch_vccnz LA44L_%=\n\t"
      "LA44X_%=:\n\t"
      "v_mfma_f32_16x16x32_bf16 v[196:199], v[172:175], v[0:3], 0\n\t"
      "v_mfma_f32_16x16x32_bf16 v[200:203], v[172:175], v[4:7], 0\n\t"
      "v_mfma_f32_16x16x32_bf16 v[204:207], v[172:175], v[8:11], 0\n\t"
      "v_mfma_f32_16x16x32_bf16 v[208:211], v[172:175], v[12:15], 0\n\t"
      "v_mfma_f32_16x16x32_bf16 v[196:199], v[176:179], v[16:19], v[196:199]\n\t"
      "v_mfma_f32_16x16x32_bf16 v[200:203], v[176:179], v[20:23], v[200:203]\n\t"
      "v_mfma_f32_16x16x32_bf16 v[204:207], v[176:179], v[24:27], v[204:207]\n\t"
      "v_mfma_f32_16x16x32_bf16 v[208:211], v[176:179], v[28:31], v[208:211]\n\t"
      "v_mfma_f32_16x16x32_bf16 v[196:199], v[180:183], v[32:35], v[196:199]\n\t"
      "v_mfma_f32_16x16x32_bf16 v[200:203], v[180:183], v[36:39], v[200:203]\n\t"
      "v_mfma_f32_16x16x32_bf16 v[204:207], v[180:183], v[40:43], v[204:207]\n\t"
      "v_mfma_f32_16x16x32_bf16 v[208:211], v[180:183], v[44:47], v[208:211]\n\t"
      "v_mfma_f32_16x16x32_bf16 v[196:199], v[184:187], v[48:51], v[196:199]\n\t"
      "v_mfma_f32_16x16x32_bf16 v[200:203], v[184:187], v[52:55], v[200:203]\n\t"
      "v_mfma_f32_16x16x32_bf16 v[204:207], v[184:187], v[56:59], v[204:207]\n\t"
      "v_mfma_f32_16x16x32_bf16 v[208:211], v[184:187], v[60:63], v[208:211]\n\t"
      "v_mfma_f32_16x16x32_bf16 v[196:199], v[188:191], v[64:67], v[196:199]\n\t"
      "v_mfma_f32_16x16x32_bf16 v[200:203], v[188:191], v[68:71], v[200:203]\n\t"
      "v_mfma_f32_16x16x32_bf16 v[204:207], v[188:191], v[72:75], v[204:207]\n\t"
      "v_mfma_f32_16x16x32_bf16 v[208:211], v[188:191], v[76:79], v[208:211]\n\t"
      "v_mfma_f32_16x16x32_bf16 v[196:199], v[192:195], v[80:83], v[196:199]\n\t"
      "v_mfma_f32_16x16x32_bf16 v[200:203], v[192:195], v[84:87], v[200:203]\n\t"
      "v_mfma_f32_16x16x32_bf16 v[204:207], v[192:195], v[88:91], v[204:207]\n\t"
      "v_mfma_f32_16x16x32_bf16 v[208:211], v[192:195], v[92:95], v[208:211]\n\t"
      "v_mfma_f32_16x16x32_bf16 v[196:199], v[128:131], v[96:99], v[196:199]\n\t"
      "v_mfma_f32_16x16x32_bf16 v[200:203], v[128:131], v[100:103], v[200:203]\n\t"
      "v_mfma_f32_16x16x32_bf16 v[204:207], v[128:131], v[104:107], v[204:207]\n\t"
      "v_mfma_f32_16x16x32_bf16 v[208:211], v[128:131], v[108:111], v[208:211]\n\t"
      "s_nop 7\n\t"
      "s_branch LA44B_%=\n\t"
      "LA45R_%=:\n\t"
      "s_mov_b32 s80, 0\n\t"
      "LA45L_%=:\n\t"
      "s_add_u32 s80, s80, 1\n\t"
      "s_cmp_lt_u32 s80, 262144\n\t"
      "s_cbranch_scc0 LA45X_%=\n\t"
      "s_waitcnt vmcnt(0)\n\t"
      "PQ5R_%=:\n\t"
      "PQ5L_%=:\n\t"
      "s_add_u32 s80, s80, 1\n\t"
      "s_cmp_lt_u32 s80, 262144\n\t"
      "s_cbranch_scc0 PQ5X_%=\n\t"
      "s_sleep 1\n\t"
      "global_load_dword v251, v252, s[74:75] offset:252 sc1\n\t"
      "s_waitcnt vmcnt(0)\n\t"
      "v_cmp_eq_u32_e32 vcc, -1, v251\n\t"
      "s_cbranch_vccnz PQ5L_%=\n\t"
      "PQ5X_%=:\n\t"
      "s_add_u32 s58, s74, s40\n\t"
      "s_addc_u32 s59, s75, 0\n\t"
      "global_load_dwordx4 v[132:135], v244, s[58:59] sc1\n\t"
      "s_add_u32 s58, s74, s41\n\t"
      "s_addc_u32 s59, s75, 0\n\t"
      "global_load_dwordx4 v[136:139], v244, s[58:59] sc1\n\t"
      "s_add_u32 s58, s74, s42\n\t"
      "s_addc_u32 s59, s75, 0\n\t"
      "global_load_dwordx4 v[140:143], v244, s[58:59] sc1\n\t"
      "s_add_u32 s58, s74, s43\n\t"
      "s_addc_u32 s59, s75, 0\n\t"
      "global_load_dwordx4 v[144:147], v244, s[58:59] sc1\n\t"
      "s_add_u32 s58, s74, s44\n\t"
      "s_addc_u32 s59, s75, 0\n\t"
      "global_load_dwordx4 v[148:151], v244, s[58:59] sc1\n\t"
      "s_add_u32 s58, s74, s45\n\t"
      "s_addc_u32 s59, s75, 0\n\t"
      "global_load_dwordx4 v[152:155], v244, s[58:59] sc1\n\t"
      "s_add_u32 s58, s74, s46\n\t"
      "s_addc_u32 s59, s75, 0\n\t"
      "global_load_dwordx4 v[156:159], v244, s[58:59] sc1\n\t"
      "s_waitcnt vmcnt(0)\n\t"
      "v_max3_u32 v251, v132, v133, v134\n\t"
      "v_max3_u32 v251, v251, v135, v136\n\t"
      "v_max3_u32 v251, v251, v137, v138\n\t"
      "v_max3_u32 v251, v251, v139, v140\n\t"
      "v_max3_u32 v251, v251, v141, v142\n\t"
      "v_max3_u32 v251, v251, v143, v144\n\t"
      "v_max3_u32 v251, v251, v145, v146\n\t"
      "v_max3_u32 v251, v251, v147, v148\n\t"
      "v_max3_u32 v251, v251, v149, v150\n\t"
      "v_max3_u32 v251, v251, v151, v152\n\t"
      "v_max3_u32 v251, v251, v153, v154\n\t"
      "v_max3_u32 v251, v251, v155, v156\n\t"
      "v_max3_u32 v251, v251, v157, v158\n\t"
      "v_max_u32_e32 v251, v251, v159\n\t"
      "v_cmp_eq_u32_e32 vcc, -1, v251\n\t"
      "s_cbranch_vccnz LA45L_%=\n\t"
      "LA45X_%=:\n\t"
      "v_mfma_f32_16x16x32_bf16 v[212:215], v[132:135], v[0:3], 0\n\t"
      "v_mfma_f32_16x16x32_bf16 v[216:219], v[132:135], v[4:7], 0\n\t"
      "v_mfma_f32_16x16x32_bf16 v[220:223], v[132:135], v[8:11], 0\n\t"
      "v_mfma_f32_16x16x32_bf16 v[224:227], v[132:135], v[12:15], 0\n\t"
      "v_mfma_f32_16x16x32_bf16 v[212:215], v[136:139], v[16:19], v[212:215]\n\t"
      "v_mfma_f32_16x16x32_bf16 v[216:219], v[136:139], v[20:23], v[216:219]\n\t"
      "v_mfma_f32_16x16x32_bf16 v[220:223], v[136:139], v[24:27], v[220:223]\n\t"
      "v_mfma_f32_16x16x32_bf16 v[224:227], v[136:139], v[28:31], v[224:227]\n\t"
      "v_mfma_f32_16x16x32_bf16 v[212:215], v[140:143], v[32:35], v[212:215]\n\t"
      "v_mfma_f32_16x16x32_bf16 v[216:219], v[140:143], v[36:39], v[216:219]\n\t"
      "v_mfma_f32_16x16x32_bf16 v[220:223], v[140:143], v[40:43], v[220:223]\n\t"
      "v_mfma_f32_16x16x32_bf16 v[224:227], v[140:143], v[44:47], v[224:227]\n\t"
      "v_mfma_f32_16x16x32_bf16 v[212:215], v[144:147], v[48:51], v[212:215]\n\t"
      "v_mfma_f32_16x16x32_bf16 v[216:219], v[144:147], v[52:55], v[216:219]\n\t"
      "v_mfma_f32_16x16x32_bf16 v[220:223], v[144:147], v[56:59], v[220:223]\n\t"
      "v_mfma_f32_16x16x32_bf16 v[224:227], v[144:147], v[60:63], v[224:227]\n\t"
      "v_mfma_f32_16x16x32_bf16 v[212:215], v[148:151], v[64:67], v[212:215]\n\t"
      "v_mfma_f32_16x16x32_bf16 v[216:219], v[148:151], v[68:71], v[216:219]\n\t"
      "v_mfma_f32_16x16x32_bf16 v[220:223], v[148:151], v[72:75], v[220:223]\n\t"
      "v_mfma_f32_16x16x32_bf16 v[224:227], v[148:151], v[76:79], v[224:227]\n\t"
      "v_mfma_f32_16x16x32_bf16 v[212:215], v[152:155], v[80:83], v[212:215]\n\t"
      "v_mfma_f32_16x16x32_bf16 v[216:219], v[152:155], v[84:87], v[216:219]\n\t"
      "v_mfma_f32_16x16x32_bf16 v[220:223], v[152:155], v[88:91], v[220:223]\n\t"
      "v_mfma_f32_16x16x32_bf16 v[224:227], v[152:155], v[92:95], v[224:227]\n\t"
      "v_mfma_f32_16x16x32_bf16 v[212:215], v[156:159], v[96:99], v[212:215]\n\t"
      "v_mfma_f32_16x16x32_bf16 v[216:219], v[156:159], v[100:103], v[216:219]\n\t"
      "v_mfma_f32_16x16x32_bf16 v[220:223], v[156:159], v[104:107], v[220:223]\n\t"
      "v_mfma_f32_16x16x32_bf16 v[224:227], v[156:159], v[108:111], v[224:227]\n\t"
      "s_nop 7\n\t"
      "s_branch LA45B_%=\n\t"
      "LA46R_%=:\n\t"
      "s_mov_b32 s80, 0\n\t"
      "LA46L_%=:\n\t"
      "s_add_u32 s80, s80, 1\n\t"
      "s_cmp_lt_u32 s80, 262144\n\t"
      "s_cbranch_scc0 LA46X_%=\n\t"
      "s_waitcnt vmcnt(0)\n\t"
      "PQ6R_%=:\n\t"
      "PQ6L_%=:\n\t"
      "s_add_u32 s80, s80, 1\n\t"
      "s_cmp_lt_u32 s80, 262144\n\t"
      "s_cbranch_scc0 PQ6X_%=\n\t"
      "s_sleep 1\n\t"
      "global_load_dword v251, v252, s[76:77] offset:252 sc1\n\t"
      "s_waitcnt vmcnt(0)\n\t"
      "v_cmp_eq_u32_e32 vcc, -1, v251\n\t"
      "s_cbranch_vccnz PQ6L_%=\n\t"
      "PQ6X_%=:\n\t"
      "s_add_u32 s58, s76, s40\n\t"
      "s_addc_u32 s59, s77, 0\n\t"
      "global_load_dwordx4 v[160:163], v244, s[58:59] sc1\n\t"
      "s_add_u32 s58, s76, s41\n\t"
      "s_addc_u32 s59, s77, 0\n\t"
      "global_load_dwordx4 v[164:167], v244, s[58:59] sc1\n\t"
      "s_add_u32 s58, s76, s42\n\t"
      "s_addc_u32 s59, s77, 0\n\t"
      "global_load_dwordx4 v[168:171], v244, s[58:59] sc1\n\t"
      "s_add_u32 s58, s76, s43\n\t"
      "s_addc_u32 s59, s77, 0\n\t"
      "global_load_dwordx4 v[172:175], v244, s[58:59] sc1\n\t"
      "s_add_u32 s58, s76, s44\n\t"
      "s_addc_u32 s59, s77, 0\n\t"
      "global_load_dwordx4 v[176:179], v244, s[58:59] sc1\n\t"
      "s_add_u32 s58, s76, s45\n\t"
      "s_addc_u32 s59, s77, 0\n\t"
      "global_load_dwordx4 v[180:183], v244, s[58:59] sc1\n\t"
      "s_add_u32 s58, s76, s46\n\t"
      "s_addc_u32 s59, s77, 0\n\t"
      "global_load_dwordx4 v[184:187], v244, s[58:59] sc1\n\t"
      "s_waitcnt vmcnt(0)\n\t"
      "v_max3_u32 v251, v160, v161, v162\n\t"
      "v_max3_u32 v251, v251, v163, v164\n\t"
      "v_max3_u32 v251, v251, v165, v166\n\t"
      "v_max3_u32 v251, v251, v167, v168\n\t"
      "v_max3_u32 v251, v251, v169, v170\n\t"
      "v_max3_u32 v251, v251, v171, v172\n\t"
      "v_max3_u32 v251, v251, v173, v174\n\t"
      "v_max3_u32 v251, v251, v175, v176\n\t"
      "v_max3_u32 v251, v251, v177, v178\n\t"
      "v_max3_u32 v251, v251, v179, v180\n\t"
      "v_max3_u32 v251, v251, v181, v182\n\t"
      "v_max3_u32 v251, v251, v183, v184\n\t"
      "v_max3_u32 v251, v251, v185, v186\n\t"
      "v_max_u32_e32 v251, v251, v187\n\t"
      "v_cmp_eq_u32_e32 vcc, -1, v251\n\t"
      "s_cbranch_vccnz LA46L_%=\n\t"
      "LA46X_%=:\n\t"
      "v_mfma_f32_16x16x32_bf16 v[196:199], v[160:163], v[0:3], 0\n\t"
      "v_mfma_f32_16x16x32_bf16 v[200:203], v[160:163], v[4:7], 0\n\t"
      "v_mfma_f32_16x16x32_bf16 v[204:207], v[160:163], v[8:11], 0\n\t"
      "v_mfma_f32_16x16x32_bf16 v[208:211], v[160:163], v[12:15], 0\n\t"
      "v_mfma_f32_16x16x32_bf16 v[196:199], v[164:167], v[16:19], v[196:199]\n\t"
      "v_mfma_f32_16x16x32_bf16 v[200:203], v[164:167], v[20:23], v[200:203]\n\t"
      "v_mfma_f32_16x16x32_bf16 v[204:207], v[164:167], v[24:27], v[204:207]\n\t"
      "v_mfma_f32_16x16x32_bf16 v[208:211], v[164:167], v[28:31], v[208:211]\n\t"
      "v_mfma_f32_16x16x32_bf16 v[196:199], v[168:171], v[32:35], v[196:199]\n\t"
      "v_mfma_f32_16x16x32_bf16 v[200:203], v[168:171], v[36:39], v[200:203]\n\t"
      "v_mfma_f32_16x16x32_bf16 v[204:207], v[168:171], v[40:43], v[204:207]\n\t"
      "v_mfma_f32_16x16x32_bf16 v[208:211], v[168:171], v[44:47], v[208:211]\n\t"
      "v_mfma_f32_16x16x32_bf16 v[196:199], v[172:175], v[48:51], v[196:199]\n\t"
      "v_mfma_f32_16x16x32_bf16 v[200:203], v[172:175], v[52:55], v[200:203]\n\t"
      "v_mfma_f32_16x16x32_bf16 v[204:207], v[172:175], v[56:59], v[204:207]\n\t"
      "v_mfma_f32_16x16x32_bf16 v[208:211], v[172:175], v[60:63], v[208:211]\n\t"
      "v_mfma_f32_16x16x32_bf16 v[196:199], v[176:179], v[64:67], v[196:199]\n\t"
      "v_mfma_f32_16x16x32_bf16 v[200:203], v[176:179], v[68:71], v[200:203]\n\t"
      "v_mfma_f32_16x16x32_bf16 v[204:207], v[176:179], v[72:75], v[204:207]\n\t"
      "v_mfma_f32_16x16x32_bf16 v[208:211], v[176:179], v[76:79], v[208:211]\n\t"
      "v_mfma_f32_16x16x32_bf16 v[196:199], v[180:183], v[80:83], v[196:199]\n\t"
      "v_mfma_f32_16x16x32_bf16 v[200:203], v[180:183], v[84:87], v[200:203]\n\t"
      "v_mfma_f32_16x16x32_bf16 v[204:207], v[180:183], v[88:91], v[204:207]\n\t"
      "v_mfma_f32_16x16x32_bf16 v[208:211], v[180:183], v[92:95], v[208:211]\n\t"
      "v_mfma_f32_16x16x32_bf16 v[196:199], v[184:187], v[96:99], v[196:199]\n\t"
      "v_mfma_f32_16x16x32_bf16 v[200:203], v[184:187], v[100:103], v[200:203]\n\t"
      "v_mfma_f32_16x16x32_bf16 v[204:207], v[184:187], v[104:107], v[204:207]\n\t"
      "v_mfma_f32_16x16x32_bf16 v[208:211], v[184:187], v[108:111], v[208:211]\n\t"
      "s_nop 7\n\t"
      "s_branch LA46B_%=\n\t"
      : "+{s[0:1]}"(ka), "+{v0}"(tid)
      : "{s2}"(blockIdx.x)
      : "memory", "vcc", "scc", "m0",
        "v1", "v2", "v3", "v4", "v5", "v6", "v7", "v8", "v9", "v10", "v11", "v12", "v13", "v14", "v15", "v16", "v17",
        "v18", "v19", "v20", "v21", "v22", "v23", "v24", "v25", "v26", "v27", "v28", "v29", "v30", "v31", "v32", "v33",
        "v34", "v35", "v36", "v37", "v38", "v39", "v40", "v41", "v42", "v43", "v44", "v45", "v46", "v47", "v48", "v49",
        "v50", "v51", "v52", "v53", "v54", "v55", "v56", "v57", "v58", "v59", "v60", "v61", "v62", "v63", "v64", "v65",
        "v66", "v67", "v68", "v69", "v70", "v71", "v72", "v73", "v74", "v75", "v76", "v77", "v78", "v79", "v80", "v81",
        "v82", "v83", "v84", "v85", "v86", "v87", "v88", "v89", "v90", "v91", "v92", "v93", "v94", "v95", "v96", "v97",
        "v98", "v99", "v100", "v101", "v102", "v103", "v104", "v105", "v106", "v107", "v108", "v109", "v110", "v111",
        "v112", "v113", "v114", "v115", "v116", "v117", "v118", "v119", "v120", "v121", "v122", "v123", "v124", "v125",
        "v126", "v127", "v128", "v129", "v130", "v131", "v132", "v133", "v134", "v135", "v136", "v137", "v138", "v139",
        "v140", "v141", "v142", "v143", "v144", "v145", "v146", "v147", "v148", "v149", "v150", "v151", "v152", "v153",
        "v154", "v155", "v156", "v157", "v158", "v159", "v160", "v161", "v162", "v163", "v164", "v165", "v166", "v167",
        "v168", "v169", "v170", "v171", "v172", "v173", "v174", "v175", "v176", "v177", "v178", "v179", "v180", "v181",
        "v182", "v183", "v184", "v185", "v186", "v187", "v188", "v189", "v190", "v191", "v192", "v193", "v194", "v195",
        "v196", "v197", "v198", "v199", "v200", "v201", "v202", "v203", "v204", "v205", "v206", "v207", "v208", "v209",
        "v210", "v211", "v212", "v213", "v214", "v215", "v216", "v217", "v218", "v219", "v220", "v221", "v222", "v223",
        "v224", "v225", "v226", "v227", "v228", "v229", "v230", "v231", "v232", "v233", "v234", "v235", "v236", "v237",
        "v238", "v239", "v240", "v241", "v242", "v243", "v244", "v245", "v246", "v247", "v248", "v249", "v250", "v251",
        "v252", "v253", "v254", "v255", "s3", "s4", "s5", "s6", "s7", "s8", "s9", "s10", "s11", "s12", "s13", "s14",
        "s15", "s16", "s17", "s18", "s19", "s20", "s21", "s22", "s23", "s24", "s25", "s26", "s27", "s28", "s29", "s30",
        "s31", "s32", "s33", "s34", "s35", "s36", "s37", "s38", "s39", "s40", "s41", "s42", "s43", "s44", "s45", "s46",
        "s47", "s48", "s49", "s50", "s51", "s52", "s53", "s54", "s55", "s56", "s57", "s58", "s59", "s60", "s61", "s62",
        "s63", "s64", "s65", "s66", "s67", "s68", "s69", "s70", "s71", "s72", "s73", "s74", "s75", "s76", "s77", "s78",
        "s79", "s80", "s81", "s82", "s83"
  );
#endif
}

extern "C" void kernel_launch(void* const* d_in, const int* in_sizes, int n_in, void* d_out, int out_size,
                              void* d_ws, size_t ws_size, hipStream_t stream) {
  (void)hipFuncSetAttribute((const void*)fused_block, hipFuncAttributeMaxDynamicSharedMemorySize, 163840);
  fused_block<<<256, 512, 163840, stream>>>((const u16*)d_in[0], (const u16*)d_in[1], (const u16*)d_in[2],
                                            (const u16*)d_in[4], (const u16*)d_in[5], (const u16*)d_in[6],
                                            (const u16*)d_in[7], (u16*)d_out, (char*)d_ws);
}
```

```cpp
#include <hip/hip_runtime.h>
#include <stdint.h>
typedef unsigned short u16;
__global__ __launch_bounds__(512, 2) void fused_block(const u16* x, const u16* wqkv, const u16* wo, const u16* w1, const u16* b1,
                                                   const u16* w2, const u16* b2, u16* out, char* ws) {
#if defined(__HIP_DEVICE_COMPILE__)
  unsigned long long ka = (unsigned long long)__builtin_amdgcn_kernarg_segment_ptr();
  unsigned tid = threadIdx.x;
  asm volatile(
      "s_load_dwordx16 s[4:19], s[0:1], 0x0\n\t"
      "s_load_dwordx2 s[20:21], s[0:1], 0x40\n\t"
      "v_lshrrev_b32_e32 v199, 6, v0\n\t"
      "v_and_b32_e32 v192, 63, v0\n\t"
      "v_readfirstlane_b32 s24, v199\n\t"
      "s_and_b32 s34, s2, 7\n\t"
      "s_lshr_b32 s36, s2, 3\n\t"
      "s_mul_i32 s35, s36, 43\n\t"
      "s_lshr_b32 s35, s35, 8\n\t"
      "s_mul_i32 s26, s35, 6\n\t"
      "s_sub_u32 s26, s36, s26\n\t"
      "s_add_u32 s26, s26, 1\n\t"
      "s_bfe_u32 s25, s2, 131076\n\t"
      "s_and_b32 s37, s36, 1\n\t"
      "s_mul_i32 s37, s37, 7\n\t"
      "s_cmp_ge_u32 s36, 24\n\t"
      "s_cselect_b32 s26, s37, s26\n\t"
      "s_cselect_b32 s35, s25, s35\n\t"
      "s_lshl2_add_u32 s25, s34, s35\n\t"
      "s_and_b32 s34, s25, 30\n\t"
      "s_lshl_b32 s34, s34, 6\n\t"
      "s_and_b32 s35, s25, 1\n\t"
      "s_add_u32 s38, s34, s35\n\t"
      "v_lshrrev_b32_e32 v197, 3, v192\n\t"
      "v_and_b32_e32 v198, 7, v192\n\t"
      "s_min_u32 s28, s26, 6\n\t"
      "s_lshl4_add_u32 s34, s24, s38\n\t"
      "s_mul_i32 s34, s34, 896\n\t"
      "s_movk_i32 s35, 1792\n\t"
      "v_lshl_add_u32 v199, v198, 4, s34\n\t"
      "v_mad_u32_u24 v199, v197, s35, v199\n\t"
      "s_lshl_b32 s34, s28, 7\n\t"
      "s_cmp_lg_u32 s24, 0\n\t"
      "s_cbranch_scc1 NTC_%=\n\t"
      "s_getpc_b64 s[58:59]\n\t"
      "s_lshr_b32 s37, s2, 3\n\t"
      "v_min_u32_e32 v200, 9, v192\n\t"
      "v_lshl_add_u32 v200, v200, 5, s37\n\t"
      "s_mov_b32 m0, 64512\n\t"
      "v_min_u32_e32 v200, 289, v200\n\t"
      "v_lshlrev_b32_e32 v200, 7, v200\n\t"
      "global_load_lds_dword v200, s[58:59]\n\t"
      "NTC_%=:\n\t"
      "v_add_u32_e32 v120, s34, v199\n\t"
      "s_add_u32 s35, s34, 128\n\t"
      "s_sub_u32 s36, s35, 896\n\t"
      "s_min_u32 s35, s35, s36\n\t"
      "v_add_u32_e32 v121, s35, v199\n\t"
      "s_waitcnt lgkmcnt(0)\n\t"
      "global_load_dwordx4 v[204:207], v120, s[4:5]\n\t"
      "s_add_u32 s35, s34, 256\n\t"
      "s_sub_u32 s36, s35, 896\n\t"
      "s_min_u32 s35, s35, s36\n\t"
      "v_add_u32_e32 v122, s35, v199\n\t"
      "global_load_dwordx4 v[4:7], v121, s[4:5]\n\t"
      "s_add_u32 s35, s34, 384\n\t"
      "s_sub_u32 s36, s35, 896\n\t"
      "s_min_u32 s35, s35, s36\n\t"
      "v_add_u32_e32 v123, s35, v199\n\t"
      "global_load_dwordx4 v[8:11], v122, s[4:5]\n\t"
      "s_add_u32 s35, s34, 512\n\t"
      "s_sub_u32 s36, s35, 896\n\t"
      "s_min_u32 s35, s35, s36\n\t"
      "v_add_u32_e32 v124, s35, v199\n\t"
      "global_load_dwordx4 v[12:15], v123, s[4:5]\n\t"
      "s_add_u32 s35, s34, 640\n\t"
      "s_sub_u32 s36, s35, 896\n\t"
      "s_min_u32 s35, s35, s36\n\t"
      "v_add_u32_e32 v125, s35, v199\n\t"
      "global_load_dwordx4 v[16:19], v124, s[4:5]\n\t"
      "s_add_u32 s35, s34, 768\n\t"
      "s_sub_u32 s36, s35, 896\n\t"
      "s_min_u32 s35, s35, s36\n\t"
      "v_add_u32_e32 v126, s35, v199\n\t"
      "global_load_dwordx4 v[20:23], v125, s[4:5]\n\t"
      "s_cmp_lt_u32 s25, 2\n\t"
      "s_cselect_b32 s34, 0, 16\n\t"
      "s_sub_u32 s34, s38, s34\n\t"
      "s_lshl1_add_u32 s34, s24, s34\n\t"
      "s_mul_i32 s34, s34, 896\n\t"
      "s_and_b32 s33, s25, 3\n\t"
      "s_lshl_b32 s33, s33, 1\n\t"
      "s_and_b32 s35, s26, 1\n\t"
      "s_add_u32 s33, s33, s35\n\t"
      "s_cmp_eq_u32 s33, 7\n\t"
      "s_cselect_b32 s33, 0, s33\n\t"
      "v_min_u32_e32 v200, 55, v192\n\t"
      "s_lshl_b32 s35, s33, 3\n\t"
      "v_add_u32_e32 v201, s35, v200\n\t"
      "v_subrev_u32_e32 v202, 56, v201\n\t"
      "v_min_u32_e32 v201, v201, v202\n\t"
      "v_lshl_add_u32 v127, v201, 4, s34\n\t"
      "s_and_b32 s39, s24, 3\n\t"
      "s_lshl_b32 s39, s39, 12\n\t"
      "s_cmp_lt_u32 s24, 4\n\t"
      "s_cselect_b32 s27, 4, 0\n\t"
      "v_lshrrev_b32_e32 v193, 4, v192\n\t"
      "v_and_b32_e32 v194, 15, v192\n\t"
      "v_bfe_u32 v195, v192, 2, 2\n\t"
      "v_and_b32_e32 v196, 3, v192\n\t"
      "v_lshlrev_b32_e32 v244, 4, v192\n\t"
      "s_lshr_b32 s34, s24, 1\n\t"
      "s_mul_i32 s34, s34, 14336\n\t"
      "s_and_b32 s35, s24, 1\n\t"
      "s_lshl_b32 s35, s35, 7\n\t"
      "s_add_u32 s34, s34, s35\n\t"
      "v_xor_b32_e32 v199, v197, v198\n\t"
      "v_lshl_add_u32 v199, v199, 4, s34\n\t"
      "v_lshl_add_u32 v199, v198, 8, v199\n\t"
      "s_sub_u32 s35, s28, s33\n\t"
      "s_cselect_b32 s36, 7, 0\n\t"
      "s_add_u32 s35, s35, s36\n\t"
      "s_lshl_b32 s35, s35, 11\n\t"
      "s_add_u32 s34, s35, 0\n\t"
      "s_sub_u32 s36, s34, 14336\n\t"
      "s_min_u32 s34, s34, s36\n\t"
      "v_add_u32_e32 v112, s34, v199\n\t"
      "s_add_u32 s34, s35, 2048\n\t"
      "s_sub_u32 s36, s34, 14336\n\t"
      "s_min_u32 s34, s34, s36\n\t"
      "v_add_u32_e32 v113, s34, v199\n\t"
      "s_add_u32 s34, s35, 4096\n\t"
      "s_sub_u32 s36, s34, 14336\n\t"
      "s_min_u32 s34, s34, s36\n\t"
      "v_add_u32_e32 v114, s34, v199\n\t"
      "s_add_u32 s34, s35, 6144\n\t"
      "s_sub_u32 s36, s34, 14336\n\t"
      "s_min_u32 s34, s34, s36\n\t"
      "v_add_u32_e32 v115, s34, v199\n\t"
      "s_add_u32 s34, s35, 8192\n\t"
      "s_sub_u32 s36, s34, 14336\n\t"
      "s_min_u32 s34, s34, s36\n\t"
      "v_add_u32_e32 v116, s34, v199\n\t"
      "s_add_u32 s34, s35, 10240\n\t"
      "s_sub_u32 s36, s34, 14336\n\t"
      "s_min_u32 s34, s34, s36\n\t"
      "v_add_u32_e32 v117, s34, v199\n\t"
      "s_add_u32 s34, s35, 12288\n\t"
      "s_sub_u32 s36, s34, 14336\n\t"
      "s_min_u32 s34, s34, s36\n\t"
      "v_add_u32_e32 v118, s34, v199\n\t"
      "v_min_u32_e32 v200, 55, v192\n\t"
      "v_lshrrev_b32_e32 v201, 2, v200\n\t"
      "v_lshlrev_b32_e32 v119, 10, v201\n\t"
      "v_cmp_lt_u32_e32 vcc, 6, v201\n\t"
      "v_mov_b32_e32 v202, 57344\n\t"
      "v_mov_b32_e32 v201, 50688\n\t"
      "v_cndmask_b32_e32 v202, v202, v201, vcc\n\t"
      "v_and_b32_e32 v201, 3, v200\n\t"
      "v_lshl_add_u32 v202, v201, 7, v202\n\t"
      "v_and_b32_e32 v200, 7, v200\n\t"
      "v_xor_b32_e32 v200, s24, v200\n\t"
      "v_lshl_add_u32 v202, v200, 4, v202\n\t"
      "v_add_u32_e32 v119, v119, v202\n\t"
      "s_bitcmp1_b32 s27, 2\n\t"
      "s_cbranch_scc1 LSET_%=\n\t"
      "v_xor_b32_e32 v199, v194, v193\n\t"
      "v_lshlrev_b32_e32 v199, 4, v199\n\t"
      "v_lshl_or_b32 v200, v193, 8, v199\n\t"
      "s_and_b32 s34, s24, 1\n\t"
      "s_mul_i32 s35, s34, 28672\n\t"
      "v_add_u32_e32 v160, s35, v200\n\t"
      "v_xor_b32_e32 v161, 64, v160\n\t"
      "v_and_b32_e32 v199, 0x70, v199\n\t"
      "v_lshl_or_b32 v199, v193, 7, v199\n\t"
      "v_add_u32_e32 v199, 57344, v199\n\t"
      "v_add_u32_e32 v200, 14336, v200\n\t"
      "s_cmp_eq_u32 s34, 1\n\t"
      "s_cselect_b64 vcc, -1, 0\n\t"
      "s_cselect_b32 s35, 0, 6656\n\t"
      "v_cndmask_b32_e32 v162, v199, v200, vcc\n\t"
      "v_xor_b32_e32 v163, 64, v162\n\t"
      "v_subrev_u32_e32 v164, s35, v162\n\t"
      "v_subrev_u32_e32 v165, s35, v163\n\t"
      "s_bfe_u32 s35, s24, 65537\n\t"
      "s_mul_i32 s35, s35, 7\n\t"
      "v_and_b32_e32 v201, 24, v192\n\t"
      "v_lshlrev_b32_e32 v201, 2, v201\n\t"
      "v_lshlrev_b32_e32 v202, 7, v195\n\t"
      "v_lshl_or_b32 v202, v193, 12, v202\n\t"
      "v_add_u32_e32 v202, 65536, v202\n\t"
      "v_add_lshl_u32 v199, v196, s35, 3\n\t"
      "v_xor_b32_e32 v199, v199, v201\n\t"
      "v_add_u32_e32 v166, v199, v202\n\t"
      "v_min_u32_e32 v199, 2, v196\n\t"
      "s_add_u32 s35, s35, 4\n\t"
      "v_add_lshl_u32 v199, v199, s35, 3\n\t"
      "v_xor_b32_e32 v199, v199, v201\n\t"
      "v_add_u32_e32 v167, v199, v202\n\t"
      "v_add_u32_e32 v168, 0x10000, v166\n\t"
      "v_add_u32_e32 v169, 0x10000, v167\n\t"
      "v_lshlrev_b32_e32 v199, 11, v193\n\t"
      "v_sub_u32_e32 v170, v166, v199\n\t"
      "v_sub_u32_e32 v171, v167, v199\n\t"
      "s_branch NORM_%=\n\t"
      "LSET_%=:\n\t"
      "s_add_u32 s60, s20, 16777216\n\t"
      "s_addc_u32 s61, s21, 0\n\t"
      "s_load_dwordx2 s[22:23], s[60:61], 0x0\n\t"
      "s_lshr_b32 s34, s39, 9\n\t"
      "v_add_u32_e32 v199, s34, v197\n\t"
      "s_and_b32 s34, s24, 1\n\t"
      "s_lshl_b32 s34, s34, 2\n\t"
      "v_and_b32_e32 v200, 2, v197\n\t"
      "v_or_b32_e32 v200, s34, v200\n\t"
      "v_xor_b32_e32 v200, v200, v198\n\t"
      "v_min_u32_e32 v200, 6, v200\n\t"
      "v_lshlrev_b32_e32 v200, 4, v200\n\t"
      "s_movk_i32 s34, 2688\n\t"
      "v_mad_u32_u24 v149, v199, s34, v200\n\t"
      "v_add_u32_e32 v148, 128, v149\n\t"
      "v_add_u32_e32 v150, 84096, v149\n\t"
      "v_add_u32_e32 v151, 83968, v149\n\t"
      "v_add_u32_e32 v152, 85120, v149\n\t"
      "s_sub_u32 s6, s6, 128\n\t"
      "s_subb_u32 s7, s7, 0\n\t"
      "s_mul_i32 s37, s26, 112\n\t"
      "s_mov_b32 s81, s33\n\t"
      "s_mov_b32 s82, s33\n\t"
      "s_and_b32 s35, s25, 3\n\t"
      "s_lshl2_add_u32 s31, s26, s35\n\t"
      "s_and_b32 s35, s2, 7\n\t"
      "s_mul_i32 s34, s35, 14\n\t"
      "s_lshl4_add_u32 s35, s35, -14\n\t"
      "s_cmp_lt_u32 s31, 14\n\t"
      "s_cselect_b32 s34, s34, s35\n\t"
      "s_cselect_b32 s36, 1, 0\n\t"
      "s_or_b32 s27, s27, s36\n\t"
      "s_add_u32 s34, s34, s31\n\t"
      "s_lshr_b32 s29, s34, 2\n\t"
      "s_and_b32 s32, s34, 3\n\t"
      "s_mul_i32 s30, s32, 7\n\t"
      "v_and_b32_e32 v199, 60, v192\n\t"
      "v_and_b32_e32 v200, 24, v192\n\t"
      "v_lshlrev_b32_e32 v202, 3, v196\n\t"
      "v_lshl_or_b32 v202, v200, 2, v202\n\t"
      "v_lshl_or_b32 v202, v195, 7, v202\n\t"
      "v_lshl_or_b32 v202, v193, 10, v202\n\t"
      "v_add_u32_e32 v224, s39, v202\n\t"
      "v_xor_b32_e32 v225, 32, v224\n\t"
      "v_xor_b32_e32 v226, 64, v224\n\t"
      "v_xor_b32_e32 v227, 96, v224\n\t"
      "s_lshl_b32 s34, s24, 12\n\t"
      "s_add_u32 s34, s34, 98304\n\t"
      "v_add_u32_e32 v245, s34, v244\n\t"
      "v_lshlrev_b32_e32 v199, 2, v199\n\t"
      "s_lshl_b32 s34, s24, 8\n\t"
      "v_lshl_add_u32 v247, v192, 2, s34\n\t"
      "v_and_b32_e32 v200, 2, v192\n\t"
      "v_mul_u32_u24_e32 v202, 576, v200\n\t"
      "v_xor_b32_e32 v199, v202, v199\n\t"
      "v_and_b32_e32 v202, 1, v192\n\t"
      "v_lshl_or_b32 v199, v202, 3, v199\n\t"
      "s_and_b32 s35, s34, 0x400\n\t"
      "s_add_u32 s34, s34, s35\n\t"
      "s_add_u32 s34, s34, 98304\n\t"
      "v_add_u32_e32 v246, s34, v199\n\t"
      "v_lshlrev_b32_e32 v199, 2, v202\n\t"
      "v_lshl_or_b32 v199, v200, 4, v199\n\t"
      "s_min_u32 s34, s29, 27\n\t"
      "s_lshl_b32 s34, s34, 7\n\t"
      "s_lshr_b32 s35, s24, 2\n\t"
      "s_lshl_b32 s35, s35, 6\n\t"
      "s_add_u32 s34, s34, s35\n\t"
      "s_lshr_b32 s35, s39, 9\n\t"
      "s_add_u32 s34, s34, s35\n\t"
      "v_add_u32_e32 v248, s34, v199\n\t"
      "s_add_u32 s48, s20, 8388608\n\t"
      "s_addc_u32 s49, s21, 0\n\t"
      "v_and_b32_e32 v199, 2, v197\n\t"
      "v_xor_b32_e32 v199, v199, v198\n\t"
      "v_lshlrev_b32_e32 v199, 4, v199\n\t"
      "s_movk_i32 s34, 7168\n\t"
      "v_mad_u32_u24 v144, v197, s34, v199\n\t"
      "v_xor_b32_e32 v199, 64, v144\n\t"
      "v_add_u32_e32 v145, 56320, v199\n\t"
      "v_add_u32_e32 v146, 112640, v144\n\t"
      "v_add_u32_e32 v147, 168960, v199\n\t"
      "v_and_b32_e32 v199, 6, v197\n\t"
      "v_xor_b32_e32 v199, v199, v198\n\t"
      "v_lshlrev_b32_e32 v199, 4, v199\n\t"
      "s_movk_i32 s35, 896\n\t"
      "v_mad_u32_u24 v154, v197, s35, v199\n\t"
      "v_add_u32_e32 v155, 6144, v154\n\t"
      "v_add_u32_e32 v156, 12288, v154\n\t"
      "v_add_u32_e32 v157, 18432, v154\n\t"
      "s_mul_i32 s34, s24, 917504\n\t"
      "s_lshl_b32 s35, s29, 7\n\t"
      "s_add_u32 s34, s34, s35\n\t"
      "s_add_u32 s52, s10, s34\n\t"
      "s_addc_u32 s53, s11, 0\n\t"
      "s_lshl_b32 s34, s28, 7\n\t"
      "s_add_u32 s56, s8, s34\n\t"
      "s_addc_u32 s57, s9, 0\n\t"
      "s_mov_b32 s34, 0\n\t"
      "s_and_b32 s35, s34, 1\n\t"
      "s_and_b32 s34, s34, 2\n\t"
      "s_lshl_b32 s34, s34, 6\n\t"
      "s_add_u32 s34, s34, s35\n\t"
      "s_mul_i32 s76, s34, 3584\n\t"
      "s_mov_b32 s34, 1\n\t"
      "s_and_b32 s35, s34, 1\n\t"
      "s_and_b32 s34, s34, 2\n\t"
      "s_lshl_b32 s34, s34, 6\n\t"
      "s_add_u32 s34, s34, s35\n\t"
      "s_mul_i32 s77, s34, 3584\n\t"
      "s_mov_b32 s34, 2\n\t"
      "s_and_b32 s35, s34, 1\n\t"
      "s_and_b32 s34, s34, 2\n\t"
      "s_lshl_b32 s34, s34, 6\n\t"
      "s_add_u32 s34, s34, s35\n\t"
      "s_mul_i32 s78, s34, 3584\n\t"
      "s_mov_b32 s34, 3\n\t"
      "s_and_b32 s35, s34, 1\n\t"
      "s_and_b32 s34, s34, 2\n\t"
      "s_lshl_b32 s34, s34, 6\n\t"
      "s_add_u32 s34, s34, s35\n\t"
      "s_mul_i32 s79, s34, 3584\n\t"
      "NORM_%=:\n\t"
      "global_load_dwordx4 v[24:27], v126, s[4:5]\n\t"
      "global_load_dwordx4 v[28:31], v127, s[4:5]\n\t"
      "v_mov_b32_e32 v199, 0x3f803f80\n\t"
      "s_waitcnt vmcnt(7)\n\t"
      "v_dot2_f32_bf16 v88, v204, v199, 0\n\t"
      "v_dot2_f32_bf16 v92, v204, v204, 0\n\t"
      "v_lshlrev_b32_e32 v32, 16, v204\n\t"
      "v_and_b32_e32 v33, 0xffff0000, v204\n\t"
      "v_dot2_f32_bf16 v89, v205, v199, 0\n\t"
      "v_dot2_f32_bf16 v93, v205, v205, 0\n\t"
      "v_lshlrev_b32_e32 v34, 16, v205\n\t"
      "v_and_b32_e32 v35, 0xffff0000, v205\n\t"
      "v_dot2_f32_bf16 v90, v206, v199, 0\n\t"
      "v_dot2_f32_bf16 v94, v206, v206, 0\n\t"
      "v_lshlrev_b32_e32 v36, 16, v206\n\t"
      "v_and_b32_e32 v37, 0xffff0000, v206\n\t"
      "v_dot2_f32_bf16 v91, v207, v199, 0\n\t"
      "v_dot2_f32_bf16 v95, v207, v207, 0\n\t"
      "v_lshlrev_b32_e32 v38, 16, v207\n\t"
      "v_and_b32_e32 v39, 0xffff0000, v207\n\t"
      "s_waitcnt vmcnt(6)\n\t"
      "v_dot2_f32_bf16 v88, v4, v199, v88\n\t"
      "v_dot2_f32_bf16 v92, v4, v4, v92\n\t"
      "v_lshlrev_b32_e32 v40, 16, v4\n\t"
      "v_and_b32_e32 v41, 0xffff0000, v4\n\t"
      "v_dot2_f32_bf16 v89, v5, v199, v89\n\t"
      "v_dot2_f32_bf16 v93, v5, v5, v93\n\t"
      "v_lshlrev_b32_e32 v42, 16, v5\n\t"
      "v_and_b32_e32 v43, 0xffff0000, v5\n\t"
      "v_dot2_f32_bf16 v90, v6, v199, v90\n\t"
      "v_dot2_f32_bf16 v94, v6, v6, v94\n\t"
      "v_lshlrev_b32_e32 v44, 16, v6\n\t"
      "v_and_b32_e32 v45, 0xffff0000, v6\n\t"
      "v_dot2_f32_bf16 v91, v7, v199, v91\n\t"
      "v_dot2_f32_bf16 v95, v7, v7, v95\n\t"
      "v_lshlrev_b32_e32 v46, 16, v7\n\t"
      "v_and_b32_e32 v47, 0xffff0000, v7\n\t"
      "s_waitcnt vmcnt(5)\n\t"
      "v_dot2_f32_bf16 v88, v8, v199, v88\n\t"
      "v_dot2_f32_bf16 v92, v8, v8, v92\n\t"
      "v_lshlrev_b32_e32 v48, 16, v8\n\t"
      "v_and_b32_e32 v49, 0xffff0000, v8\n\t"
      "v_dot2_f32_bf16 v89, v9, v199, v89\n\t"
      "v_dot2_f32_bf16 v93, v9, v9, v93\n\t"
      "v_lshlrev_b32_e32 v50, 16, v9\n\t"
      "v_and_b32_e32 v51, 0xffff0000, v9\n\t"
      "v_dot2_f32_bf16 v90, v10, v199, v90\n\t"
      "v_dot2_f32_bf16 v94, v10, v10, v94\n\t"
      "v_lshlrev_b32_e32 v52, 16, v10\n\t"
      "v_and_b32_e32 v53, 0xffff0000, v10\n\t"
      "v_dot2_f32_bf16 v91, v11, v199, v91\n\t"
      "v_dot2_f32_bf16 v95, v11, v11, v95\n\t"
      "v_lshlrev_b32_e32 v54, 16, v11\n\t"
      "v_and_b32_e32 v55, 0xffff0000, v11\n\t"
      "s_waitcnt vmcnt(4)\n\t"
      "v_dot2_f32_bf16 v88, v12, v199, v88\n\t"
      "v_dot2_f32_bf16 v92, v12, v12, v92\n\t"
      "v_lshlrev_b32_e32 v56, 16, v12\n\t"
      "v_and_b32_e32 v57, 0xffff0000, v12\n\t"
      "v_dot2_f32_bf16 v89, v13, v199, v89\n\t"
      "v_dot2_f32_bf16 v93, v13, v13, v93\n\t"
      "v_lshlrev_b32_e32 v58, 16, v13\n\t"
      "v_and_b32_e32 v59, 0xffff0000, v13\n\t"
      "v_dot2_f32_bf16 v90, v14, v199, v90\n\t"
      "v_dot2_f32_bf16 v94, v14, v14, v94\n\t"
      "v_lshlrev_b32_e32 v60, 16, v14\n\t"
      "v_and_b32_e32 v61, 0xffff0000, v14\n\t"
      "v_dot2_f32_bf16 v91, v15, v199, v91\n\t"
      "v_dot2_f32_bf16 v95, v15, v15, v95\n\t"
      "v_lshlrev_b32_e32 v62, 16, v15\n\t"
      "v_and_b32_e32 v63, 0xffff0000, v15\n\t"
      "s_waitcnt vmcnt(3)\n\t"
      "v_dot2_f32_bf16 v88, v16, v199, v88\n\t"
      "v_dot2_f32_bf16 v92, v16, v16, v92\n\t"
      "v_lshlrev_b32_e32 v64, 16, v16\n\t"
      "v_and_b32_e32 v65, 0xffff0000, v16\n\t"
      "v_dot2_f32_bf16 v89, v17, v199, v89\n\t"
      "v_dot2_f32_bf16 v93, v17, v17, v93\n\t"
      "v_lshlrev_b32_e32 v66, 16, v17\n\t"
      "v_and_b32_e32 v67, 0xffff0000, v17\n\t"
      "v_dot2_f32_bf16 v90, v18, v199, v90\n\t"
      "v_dot2_f32_bf16 v94, v18, v18, v94\n\t"
      "v_lshlrev_b32_e32 v68, 16, v18\n\t"
      "v_and_b32_e32 v69, 0xffff0000, v18\n\t"
      "v_dot2_f32_bf16 v91, v19, v199, v91\n\t"
      "v_dot2_f32_bf16 v95, v19, v19, v95\n\t"
      "v_lshlrev_b32_e32 v70, 16, v19\n\t"
      "v_and_b32_e32 v71, 0xffff0000, v19\n\t"
      "s_waitcnt vmcnt(2)\n\t"
      "v_dot2_f32_bf16 v88, v20, v199, v88\n\t"
      "v_dot2_f32_bf16 v92, v20, v20, v92\n\t"
      "v_lshlrev_b32_e32 v72, 16, v20\n\t"
      "v_and_b32_e32 v73, 0xffff0000, v20\n\t"
      "v_dot2_f32_bf16 v89, v21, v199, v89\n\t"
      "v_dot2_f32_bf16 v93, v21, v21, v93\n\t"
      "v_lshlrev_b32_e32 v74, 16, v21\n\t"
      "v_and_b32_e32 v75, 0xffff0000, v21\n\t"
      "v_dot2_f32_bf16 v90, v22, v199, v90\n\t"
      "v_dot2_f32_bf16 v94, v22, v22, v94\n\t"
      "v_lshlrev_b32_e32 v76, 16, v22\n\t"
      "v_and_b32_e32 v77, 0xffff0000, v22\n\t"
      "v_dot2_f32_bf16 v91, v23, v199, v91\n\t"
      "v_dot2_f32_bf16 v95, v23, v23, v95\n\t"
      "v_lshlrev_b32_e32 v78, 16, v23\n\t"
      "v_and_b32_e32 v79, 0xffff0000, v23\n\t"
      "s_waitcnt vmcnt(1)\n\t"
      "v_dot2_f32_bf16 v88, v24, v199, v88\n\t"
      "v_dot2_f32_bf16 v92, v24, v24, v92\n\t"
      "v_lshlrev_b32_e32 v80, 16, v24\n\t"
      "v_and_b32_e32 v81, 0xffff0000, v24\n\t"
      "v_dot2_f32_bf16 v89, v25, v199, v89\n\t"
      "v_dot2_f32_bf16 v93, v25, v25, v93\n\t"
      "v_lshlrev_b32_e32 v82, 16, v25\n\t"
      "v_and_b32_e32 v83, 0xffff0000, v25\n\t"
      "v_dot2_f32_bf16 v90, v26, v199, v90\n\t"
      "v_dot2_f32_bf16 v94, v26, v26, v94\n\t"
      "v_lshlrev_b32_e32 v84, 16, v26\n\t"
      "v_and_b32_e32 v85, 0xffff0000, v26\n\t"
      "v_dot2_f32_bf16 v91, v27, v199, v91\n\t"
      "v_dot2_f32_bf16 v95, v27, v27, v95\n\t"
      "v_lshlrev_b32_e32 v86, 16, v27\n\t"
      "v_and_b32_e32 v87, 0xffff0000, v27\n\t"
      "s_waitcnt vmcnt(0)\n\t"
      "s_bitcmp0_b32 s27, 2\n\t"
      "s_cbranch_scc1 SK1_%=\n\t"
      "s_mul_i32 s72, s81, 172032\n\t"
      "s_add_u32 s72, s72, s37\n\t"
      "s_add_u32 s40, s6, s72\n\t"
      "s_addc_u32 s41, s7, 0\n\t"
      "s_add_u32 s81, s81, 1\n\t"
      "s_cmp_eq_u32 s81, 7\n\t"
      "s_cselect_b32 s81, 0, s81\n\t"
      "s_add_u32 m0, s39, 131072\n\t"
      "s_nop 0\n\t"
      "global_load_lds_dwordx4 v148, s[40:41]\n\t"
      "global_load_lds_dwordx4 v149, s[40:41] offset:1024\n\t"
      "global_load_lds_dwordx4 v150, s[40:41] offset:2048\n\t"
      "global_load_lds_dwordx4 v151, s[40:41] offset:3072\n\t"
      "SK1_%=:\n\t"
      "v_dot2_f32_bf16 v96, v28, v199, 0\n\t"
      "v_dot2_f32_bf16 v97, v28, v28, 0\n\t"
      "v_dot2_f32_bf16 v96, v29, v199, v96\n\t"
      "v_dot2_f32_bf16 v97, v29, v29, v97\n\t"
      "v_dot2_f32_bf16 v96, v30, v199, v96\n\t"
      "v_dot2_f32_bf16 v97, v30, v30, v97\n\t"
      "v_dot2_f32_bf16 v96, v31, v199, v96\n\t"
      "v_dot2_f32_bf16 v97, v31, v31, v97\n\t"
      "v_cmp_gt_u32_e32 vcc, 56, v192\n\t"
      "v_pk_add_f32 v[88:89], v[88:89], v[90:91]\n\t"
      "v_pk_add_f32 v[92:93], v[92:93], v[94:95]\n\t"
      "v_cndmask_b32_e32 v96, 0, v96, vcc\n\t"
      "v_cndmask_b32_e32 v97, 0, v97, vcc\n\t"
      "v_add_f32_e32 v88, v88, v89\n\t"
      "v_add_f32_e32 v92, v92, v93\n\t"
      "v_add_f32_dpp v96, v96, v96 quad_perm:[1,0,3,2] row_mask:0xf bank_mask:0xf\n\t"
      "v_add_f32_dpp v97, v97, v97 quad_perm:[1,0,3,2] row_mask:0xf bank_mask:0xf\n\t"
      "v_add_f32_dpp v88, v88, v88 quad_perm:[1,0,3,2] row_mask:0xf bank_mask:0xf\n\t"
      "v_add_f32_dpp v92, v92, v92 quad_perm:[1,0,3,2] row_mask:0xf bank_mask:0xf\n\t"
      "v_add_f32_dpp v96, v96, v96 quad_perm:[2,3,0,1] row_mask:0xf bank_mask:0xf\n\t"
      "v_add_f32_dpp v97, v97, v97 quad_perm:[2,3,0,1] row_mask:0xf bank_mask:0xf\n\t"
      "v_add_f32_dpp v88, v88, v88 quad_perm:[2,3,0,1] row_mask:0xf bank_mask:0xf\n\t"
      "v_add_f32_dpp v92, v92, v92 quad_perm:[2,3,0,1] row_mask:0xf bank_mask:0xf\n\t"
      "v_add_f32_dpp v96, v96, v96 row_half_mirror row_mask:0xf bank_mask:0xf\n\t"
      "v_add_f32_dpp v97, v97, v97 row_half_mirror row_mask:0xf bank_mask:0xf\n\t"
      "v_add_f32_dpp v88, v88, v88 row_half_mirror row_mask:0xf bank_mask:0xf\n\t"
      "v_add_f32_dpp v92, v92, v92 row_half_mirror row_mask:0xf bank_mask:0xf\n\t"
      "v_mul_f32_e32 v88, 0xbb124925, v88\n\t"
      "v_add_f32_dpp v96, v96, v96 row_mirror row_mask:0xf bank_mask:0xf\n\t"
      "v_mul_f32_e32 v92, 0x3b124925, v92\n\t"
      "s_bitcmp0_b32 s27, 2\n\t"
      "s_cbranch_scc1 SK2_%=\n\t"
      "s_mul_i32 s72, s81, 172032\n\t"
      "s_add_u32 s72, s72, s37\n\t"
      "s_add_u32 s40, s6, s72\n\t"
      "s_addc_u32 s41, s7, 0\n\t"
      "s_add_u32 s81, s81, 1\n\t"
      "s_cmp_eq_u32 s81, 7\n\t"
      "s_cselect_b32 s81, 0, s81\n\t"
      "s_add_u32 m0, s39, 147456\n\t"
      "s_nop 0\n\t"
      "global_load_lds_dwordx4 v148, s[40:41]\n\t"
      "global_load_lds_dwordx4 v149, s[40:41] offset:1024\n\t"
      "SK2_%=:\n\t"
      "v_add_f32_dpp v97, v97, v97 row_mirror row_mask:0xf bank_mask:0xf\n\t"
      "v_fma_f32 v92, -v88, v88, v92\n\t"
      "s_nop 0\n\t"
      "v_permlane16_swap_b32_e32 v96, v97\n\t"
      "v_max_f32_e32 v92, 0, v92\n\t"
      "v_add_f32_e32 v96, v96, v97\n\t"
      "v_add_f32_e32 v92, 0x3727c5ac, v92\n\t"
      "v_mov_b32_e32 v97, v96\n\t"
      "v_rsq_f32_e32 v92, v92\n\t"
      "s_nop 0\n\t"
      "v_permlane32_swap_b32_e32 v96, v97\n\t"
      "v_add_f32_e32 v96, v96, v97\n\t"
      "v_mul_f32_e32 v88, v88, v92\n\t"
      "v_mov_b32_e32 v97, v96\n\t"
      "v_pk_fma_f32 v[32:33], v[32:33], v[92:93], v[88:89] op_sel_hi:[1,0,0]\n\t"
      "v_pk_fma_f32 v[34:35], v[34:35], v[92:93], v[88:89] op_sel_hi:[1,0,0]\n\t"
      "v_permlane16_swap_b32_e32 v96, v97\n\t"
      "v_pk_fma_f32 v[36:37], v[36:37], v[92:93], v[88:89] op_sel_hi:[1,0,0]\n\t"
      "v_pk_fma_f32 v[38:39], v[38:39], v[92:93], v[88:89] op_sel_hi:[1,0,0]\n\t"
      "v_mul_f32_e32 v96, 0xbb124925, v96\n\t"
      "v_cvt_pk_bf16_f32 v0, v32, v33\n\t"
      "v_cvt_pk_bf16_f32 v1, v34, v35\n\t"
      "v_cvt_pk_bf16_f32 v2, v36, v37\n\t"
      "v_cvt_pk_bf16_f32 v3, v38, v39\n\t"
      "ds_write_b128 v112, v[0:3]\n\t"
      "v_pk_fma_f32 v[40:41], v[40:41], v[92:93], v[88:89] op_sel_hi:[1,0,0]\n\t"
      "v_pk_fma_f32 v[42:43], v[42:43], v[92:93], v[88:89] op_sel_hi:[1,0,0]\n\t"
      "v_mul_f32_e32 v97, 0x3b124925, v97\n\t"
      "v_pk_fma_f32 v[44:45], v[44:45], v[92:93], v[88:89] op_sel_hi:[1,0,0]\n\t"
      "v_pk_fma_f32 v[46:47], v[46:47], v[92:93], v[88:89] op_sel_hi:[1,0,0]\n\t"
      "v_fma_f32 v97, -v96, v96, v97\n\t"
      "v_cvt_pk_bf16_f32 v4, v40, v41\n\t"
      "s_bitcmp0_b32 s27, 2\n\t"
      "s_cbranch_scc1 SK3_%=\n\t"
      "global_load_lds_dwordx4 v150, s[40:41] offset:2048\n\t"
      "global_load_lds_dwordx4 v151, s[40:41] offset:3072\n\t"
      "SK3_%=:\n\t"
      "v_cvt_pk_bf16_f32 v5, v42, v43\n\t"
      "v_cvt_pk_bf16_f32 v6, v44, v45\n\t"
      "v_cvt_pk_bf16_f32 v7, v46, v47\n\t"
      "ds_write_b128 v113, v[4:7]\n\t"
      "v_pk_fma_f32 v[48:49], v[48:49], v[92:93], v[88:89] op_sel_hi:[1,0,0]\n\t"
      "v_pk_fma_f32 v[50:51], v[50:51], v[92:93], v[88:89] op_sel_hi:[1,0,0]\n\t"
      "v_max_f32_e32 v97, 0, v97\n\t"
      "v_pk_fma_f32 v[52:53], v[52:53], v[92:93], v[88:89] op_sel_hi:[1,0,0]\n\t"
      "v_pk_fma_f32 v[54:55], v[54:55], v[92:93], v[88:89] op_sel_hi:[1,0,0]\n\t"
      "v_add_f32_e32 v97, 0x3727c5ac, v97\n\t"
      "v_cvt_pk_bf16_f32 v8, v48, v49\n\t"
      "v_cvt_pk_bf16_f32 v9, v50, v51\n\t"
      "v_cvt_pk_bf16_f32 v10, v52, v53\n\t"
      "v_cvt_pk_bf16_f32 v11, v54, v55\n\t"
      "ds_write_b128 v114, v[8:11]\n\t"
      "v_pk_fma_f32 v[56:57], v[56:57], v[92:93], v[88:89] op_sel_hi:[1,0,0]\n\t"
      "v_pk_fma_f32 v[58:59], v[58:59], v[92:93], v[88:89] op_sel_hi:[1,0,0]\n\t"
      "v_rsq_f32_e32 v97, v97\n\t"
      "v_pk_fma_f32 v[60:61], v[60:61], v[92:93], v[88:89] op_sel_hi:[1,0,0]\n\t"
      "v_pk_fma_f32 v[62:63], v[62:63], v[92:93], v[88:89] op_sel_hi:[1,0,0]\n\t"
      "v_cvt_pk_bf16_f32 v12, v56, v57\n\t"
      "v_cvt_pk_bf16_f32 v13, v58, v59\n\t"
      "v_cvt_pk_bf16_f32 v14, v60, v61\n\t"
      "v_cvt_pk_bf16_f32 v15, v62, v63\n\t"
      "ds_write_b128 v115, v[12:15]\n\t"
      "v_pk_fma_f32 v[64:65], v[64:65], v[92:93], v[88:89] op_sel_hi:[1,0,0]\n\t"
      "v_pk_fma_f32 v[66:67], v[66:67], v[92:93], v[88:89] op_sel_hi:[1,0,0]\n\t"
      "v_mul_f32_e32 v96, v96, v97\n\t"
      "v_pk_fma_f32 v[68:69], v[68:69], v[92:93], v[88:89] op_sel_hi:[1,0,0]\n\t"
      "v_pk_fma_f32 v[70:71], v[70:71], v[92:93], v[88:89] op_sel_hi:[1,0,0]\n\t"
      "s_bitcmp0_b32 s27, 2\n\t"
      "s_cbranch_scc1 SK4_%=\n\t"
      "s_mul_i32 s72, s82, 172032\n\t"
      "s_add_u32 s72, s72, s37\n\t"
      "s_add_u32 s72, s72, 1792\n\t"
      "s_add_u32 s40, s6, s72\n\t"
      "s_addc_u32 s41, s7, 0\n\t"
      "s_add_u32 s82, s82, 1\n\t"
      "s_cmp_eq_u32 s82, 7\n\t"
      "s_cselect_b32 s82, 0, s82\n\t"
      "s_add_u32 m0, s39, 65536\n\t"
      "s_nop 0\n\t"
      "global_load_lds_dwordx4 v148, s[40:41]\n\t"
      "global_load_lds_dwordx4 v152, s[40:41] offset:1024\n\t"
      "SK4_%=:\n\t"
      "v_cvt_pk_bf16_f32 v16, v64, v65\n\t"
      "v_cvt_pk_bf16_f32 v17, v66, v67\n\t"
      "v_cvt_pk_bf16_f32 v18, v68, v69\n\t"
      "v_cvt_pk_bf16_f32 v19, v70, v71\n\t"
      "ds_write_b128 v116, v[16:19]\n\t"
      "v_pk_fma_f32 v[72:73], v[72:73], v[92:93], v[88:89] op_sel_hi:[1,0,0]\n\t"
      "v_pk_fma_f32 v[74:75], v[74:75], v[92:93], v[88:89] op_sel_hi:[1,0,0]\n\t"
      "v_pk_fma_f32 v[76:77], v[76:77], v[92:93], v[88:89] op_sel_hi:[1,0,0]\n\t"
      "v_pk_fma_f32 v[78:79], v[78:79], v[92:93], v[88:89] op_sel_hi:[1,0,0]\n\t"
      "v_cvt_pk_bf16_f32 v20, v72, v73\n\t"
      "v_cvt_pk_bf16_f32 v21, v74, v75\n\t"
      "v_cvt_pk_bf16_f32 v22, v76, v77\n\t"
      "v_cvt_pk_bf16_f32 v23, v78, v79\n\t"
      "ds_write_b128 v117, v[20:23]\n\t"
      "v_pk_fma_f32 v[80:81], v[80:81], v[92:93], v[88:89] op_sel_hi:[1,0,0]\n\t"
      "v_pk_fma_f32 v[82:83], v[82:83], v[92:93], v[88:89] op_sel_hi:[1,0,0]\n\t"
      "v_pk_fma_f32 v[84:85], v[84:85], v[92:93], v[88:89] op_sel_hi:[1,0,0]\n\t"
      "v_pk_fma_f32 v[86:87], v[86:87], v[92:93], v[88:89] op_sel_hi:[1,0,0]\n\t"
      "v_cvt_pk_bf16_f32 v24, v80, v81\n\t"
      "v_cvt_pk_bf16_f32 v25, v82, v83\n\t"
      "v_cvt_pk_bf16_f32 v26, v84, v85\n\t"
      "v_cvt_pk_bf16_f32 v27, v86, v87\n\t"
      "ds_write_b128 v118, v[24:27]\n\t"
      "v_lshlrev_b32_e32 v32, 16, v28\n\t"
      "v_and_b32_e32 v33, 0xffff0000, v28\n\t"
      "v_lshlrev_b32_e32 v34, 16, v29\n\t"
      "v_and_b32_e32 v35, 0xffff0000, v29\n\t"
      "v_lshlrev_b32_e32 v36, 16, v30\n\t"
      "v_and_b32_e32 v37, 0xffff0000, v30\n\t"
      "v_lshlrev_b32_e32 v38, 16, v31\n\t"
      "s_bitcmp0_b32 s27, 2\n\t"
      "s_cbranch_scc1 SK5_%=\n\t"
      "s_mul_i32 s72, s82, 172032\n\t"
      "s_add_u32 s72, s72, s37\n\t"
      "s_add_u32 s72, s72, 1792\n\t"
      "s_add_u32 s40, s6, s72\n\t"
      "s_addc_u32 s41, s7, 0\n\t"
      "s_add_u32 s82, s82, 1\n\t"
      "s_cmp_eq_u32 s82, 7\n\t"
      "s_cselect_b32 s82, 0, s82\n\t"
      "s_add_u32 m0, s39, 67584\n\t"
      "s_nop 0\n\t"
      "global_load_lds_dwordx4 v148, s[40:41]\n\t"
      "global_load_lds_dwordx4 v152, s[40:41] offset:1024\n\t"
      "SK5_%=:\n\t"
      "v_and_b32_e32 v39, 0xffff0000, v31\n\t"
      "v_pk_fma_f32 v[32:33], v[32:33], v[96:97], v[96:97] op_sel:[0,1,0] op_sel_hi:[1,1,0]\n\t"
      "v_pk_fma_f32 v[34:35], v[34:35], v[96:97], v[96:97] op_sel:[0,1,0] op_sel_hi:[1,1,0]\n\t"
      "v_pk_fma_f32 v[36:37], v[36:37], v[96:97], v[96:97] op_sel:[0,1,0] op_sel_hi:[1,1,0]\n\t"
      "v_pk_fma_f32 v[38:39], v[38:39], v[96:97], v[96:97] op_sel:[0,1,0] op_sel_hi:[1,1,0]\n\t"
      "v_cvt_pk_bf16_f32 v28, v32, v33\n\t"
      "v_cvt_pk_bf16_f32 v29, v34, v35\n\t"
      "v_cvt_pk_bf16_f32 v30, v36, v37\n\t"
      "v_cvt_pk_bf16_f32 v31, v38, v39\n\t"
      "ds_write_b128 v119, v[28:31]\n\t"
      "s_waitcnt lgkmcnt(0)\n\t"
      "s_bitcmp1_b32 s27, 2\n\t"
      "s_cbranch_scc1 LPATH_%=\n\t"
      "s_setprio 2\n\t"
      "s_barrier\n\t"
      "ds_read_b64_tr_b16 v[76:77], v168\n\t"
      "ds_read_b64_tr_b16 v[78:79], v168 offset:512\n\t"
      "ds_read_b128 v[68:71], v160\n\t"
      "ds_read_b128 v[72:75], v160 offset:14336\n\t"
      "ds_read_b64_tr_b16 v[80:81], v169\n\t"
      "ds_read_b64_tr_b16 v[82:83], v169 offset:512\n\t"
      "ds_read_b64_tr_b16 v[84:85], v168 offset:1024\n\t"
      "ds_read_b64_tr_b16 v[86:87], v168 offset:1536\n\t"
      "ds_read_b128 v[64:67], v162\n\t"
      "ds_read_b64_tr_b16 v[88:89], v169 offset:1024\n\t"
      "ds_read_b64_tr_b16 v[90:91], v169 offset:1536\n\t"
      "ds_read_b64_tr_b16 v[104:105], v168 offset:2048\n\t"
      "ds_read_b64_tr_b16 v[106:107], v168 offset:2560\n\t"
      "ds_read_b128 v[96:99], v161 offset:1024\n\t"
      "ds_read_b128 v[100:103], v161 offset:15360\n\t"
      "ds_read_b64_tr_b16 v[108:109], v169 offset:2048\n\t"
      "ds_read_b64_tr_b16 v[110:111], v169 offset:2560\n\t"
      "ds_read_b64_tr_b16 v[112:113], v168 offset:3072\n\t"
      "ds_read_b64_tr_b16 v[114:115], v168 offset:3584\n\t"
      "ds_read_b128 v[92:95], v163 offset:1024\n\t"
      "ds_read_b64_tr_b16 v[116:117], v169 offset:3072\n\t"
      "ds_read_b64_tr_b16 v[118:119], v169 offset:3584\n\t"
      "s_waitcnt vmcnt(0)\n\t"
      "s_barrier\n\t"
      "s_waitcnt lgkmcnt(14)\n\t"
      "v_mfma_f32_16x16x32_bf16 v[0:3], v[76:79], v[68:71], 0\n\t"
      "ds_read_b64_tr_b16 v[132:133], v168 offset:16384\n\t"
      "ds_read_b64_tr_b16 v[134:135], v168 offset:16896\n\t"
      "v_mfma_f32_16x16x32_bf16 v[8:11], v[76:79], v[72:75], 0\n\t"
      "ds_read_b128 v[124:127], v160 offset:2048\n\t"
      "v_mfma_f32_16x16x32_bf16 v[4:7], v[80:83], v[68:71], 0\n\t"
      "ds_read_b128 v[128:131], v160 offset:16384\n\t"
      "v_mfma_f32_16x16x32_bf16 v[12:15], v[80:83], v[72:75], 0\n\t"
      "ds_read_b64_tr_b16 v[136:137], v169 offset:16384\n\t"
      "v_mfma_f32_16x16x32_bf16 v[24:27], v[84:87], v[68:71], 0\n\t"
      "ds_read_b64_tr_b16 v[138:139], v169 offset:16896\n\t"
      "v_mfma_f32_16x16x32_bf16 v[32:35], v[84:87], v[72:75], 0\n\t"
      "ds_read_b64_tr_b16 v[140:141], v168 offset:17408\n\t"
      "s_waitcnt lgkmcnt(14)\n\t"
      "v_mfma_f32_16x16x32_bf16 v[16:19], v[84:87], v[64:67], 0\n\t"
      "ds_read_b64_tr_b16 v[142:143], v168 offset:17920\n\t"
      "v_mfma_f32_16x16x32_bf16 v[28:31], v[88:91], v[68:71], 0\n\t"
      "ds_read_b128 v[120:123], v162 offset:2048\n\t"
      "v_mfma_f32_16x16x32_bf16 v[36:39], v[88:91], v[72:75], 0\n\t"
      "ds_read_b64_tr_b16 v[144:145], v169 offset:17408\n\t"
      "v_mfma_f32_16x16x32_bf16 v[20:23], v[88:91], v[64:67], 0\n\t"
      "ds_read_b64_tr_b16 v[146:147], v169 offset:17920\n\t"
      "s_waitcnt lgkmcnt(11)\n\t"
      "v_mfma_f32_16x16x32_bf16 v[0:3], v[104:107], v[96:99], v[0:3]\n\t"
      "ds_read_b64_tr_b16 v[76:77], v168 offset:18432\n\t"
      "ds_read_b64_tr_b16 v[78:79], v168 offset:18944\n\t"
      "v_mfma_f32_16x16x32_bf16 v[8:11], v[104:107], v[100:103], v[8:11]\n\t"
      "ds_read_b128 v[68:71], v161 offset:3072\n\t"
      "v_mfma_f32_16x16x32_bf16 v[4:7], v[108:111], v[96:99], v[4:7]\n\t"
      "ds_read_b128 v[72:75], v161 offset:17408\n\t"
      "v_mfma_f32_16x16x32_bf16 v[12:15], v[108:111], v[100:103], v[12:15]\n\t"
      "ds_read_b64_tr_b16 v[80:81], v169 offset:18432\n\t"
      "v_mfma_f32_16x16x32_bf16 v[24:27], v[112:115], v[96:99], v[24:27]\n\t"
      "ds_read_b64_tr_b16 v[82:83], v169 offset:18944\n\t"
      "v_mfma_f32_16x16x32_bf16 v[32:35], v[112:115], v[100:103], v[32:35]\n\t"
      "ds_read_b64_tr_b16 v[84:85], v168 offset:19456\n\t"
      "v_mfma_f32_16x16x32_bf16 v[16:19], v[112:115], v[92:95], v[16:19]\n\t"
      "ds_read_b64_tr_b16 v[86:87], v168 offset:19968\n\t"
      "v_mfma_f32_16x16x32_bf16 v[28:31], v[116:119], v[96:99], v[28:31]\n\t"
      "ds_read_b128 v[64:67], v163 offset:3072\n\t"
      "v_mfma_f32_16x16x32_bf16 v[36:39], v[116:119], v[100:103], v[36:39]\n\t"
      "ds_read_b64_tr_b16 v[88:89], v169 offset:19456\n\t"
      "v_mfma_f32_16x16x32_bf16 v[20:23], v[116:119], v[92:95], v[20:23]\n\t"
      "ds_read_b64_tr_b16 v[90:91], v169 offset:19968\n\t"
      "s_barrier\n\t"
      "s_waitcnt lgkmcnt(11)\n\t"
      "v_mfma_f32_16x16x32_bf16 v[0:3], v[132:135], v[124:127], v[0:3]\n\t"
      "ds_read_b64_tr_b16 v[104:105], v166\n\t"
      "v_lshlrev_b32_e32 v199, 2, v193\n\t"
      "v_mfma_f32_16x16x32_bf16 v[8:11], v[132:135], v[128:131], v[8:11]\n\t"
      "ds_read_b64_tr_b16 v[106:107], v166 offset:512\n\t"
      "v_sub_u32_e32 v199, v194, v199\n\t"
      "v_mfma_f32_16x16x32_bf16 v[4:7], v[136:139], v[124:127], v[4:7]\n\t"
      "ds_read_b128 v[96:99], v160\n\t"
      "v_mov_b32_e32 v200, 0xff800000\n\t"
      "v_mfma_f32_16x16x32_bf16 v[12:15], v[136:139], v[128:131], v[12:15]\n\t"
      "ds_read_b128 v[100:103], v160 offset:14336\n\t"
      "s_cmp_lt_u32 s25, 2\n\t"
      "v_mfma_f32_16x16x32_bf16 v[24:27], v[140:143], v[124:127], v[24:27]\n\t"
      "ds_read_b128 v[92:95], v162\n\t"
      "s_cselect_b32 s35, 0xff800000, 0\n\t"
      "v_mfma_f32_16x16x32_bf16 v[32:35], v[140:143], v[128:131], v[32:35]\n\t"
      "ds_read_b64_tr_b16 v[108:109], v167\n\t"
      "s_bitcmp0_b32 s24, 0\n\t"
      "v_mfma_f32_16x16x32_bf16 v[16:19], v[140:143], v[120:123], v[16:19]\n\t"
      "ds_read_b64_tr_b16 v[110:111], v167 offset:512\n\t"
      "s_cselect_b32 s35, s35, 0\n\t"
      "v_mfma_f32_16x16x32_bf16 v[28:31], v[144:147], v[124:127], v[28:31]\n\t"
      "v_cmp_ge_i32_e32 vcc, -9, v199\n\t"
      "v_mfma_f32_16x16x32_bf16 v[36:39], v[144:147], v[128:131], v[36:39]\n\t"
      "v_subrev_u32_e32 v201, 0, v199\n\t"
      "v_mfma_f32_16x16x32_bf16 v[20:23], v[144:147], v[120:123], v[20:23]\n\t"
      "v_cmp_gt_u32_e64 s[64:65], 8, v201\n\t"
      "s_waitcnt lgkmcnt(7)\n\t"
      "v_mfma_f32_16x16x32_bf16 v[0:3], v[76:79], v[68:71], v[0:3]\n\t"
      "ds_read_b64_tr_b16 v[132:133], v166 offset:1024\n\t"
      "v_cndmask_b32_e64 v172, v200, 0, vcc\n\t"
      "v_mfma_f32_16x16x32_bf16 v[8:11], v[76:79], v[72:75], v[8:11]\n\t"
      "ds_read_b64_tr_b16 v[134:135], v166 offset:1536\n\t"
      "v_cndmask_b32_e64 v176, v200, 0, s[64:65]\n\t"
      "v_mfma_f32_16x16x32_bf16 v[4:7], v[80:83], v[68:71], v[4:7]\n\t"
      "ds_read_b128 v[124:127], v161 offset:1024\n\t"
      "v_or_b32_e32 v180, s35, v172\n\t"
      "v_mfma_f32_16x16x32_bf16 v[12:15], v[80:83], v[72:75], v[12:15]\n\t"
      "ds_read_b128 v[128:131], v161 offset:15360\n\t"
      "v_cmp_ge_i32_e32 vcc, -8, v199\n\t"
      "v_mfma_f32_16x16x32_bf16 v[24:27], v[84:87], v[68:71], v[24:27]\n\t"
      "ds_read_b128 v[120:123], v163 offset:1024\n\t"
      "v_subrev_u32_e32 v201, 1, v199\n\t"
      "v_mfma_f32_16x16x32_bf16 v[32:35], v[84:87], v[72:75], v[32:35]\n\t"
      "ds_read_b64_tr_b16 v[136:137], v167 offset:1024\n\t"
      "v_cmp_gt_u32_e64 s[64:65], 8, v201\n\t"
      "v_mfma_f32_16x16x32_bf16 v[16:19], v[84:87], v[64:67], v[16:19]\n\t"
      "ds_read_b64_tr_b16 v[138:139], v167 offset:1536\n\t"
      "v_cndmask_b32_e64 v173, v200, 0, vcc\n\t"
      "v_mfma_f32_16x16x32_bf16 v[28:31], v[88:91], v[68:71], v[28:31]\n\t"
      "v_cndmask_b32_e64 v177, v200, 0, s[64:65]\n\t"
      "v_mfma_f32_16x16x32_bf16 v[36:39], v[88:91], v[72:75], v[36:39]\n\t"
      "v_or_b32_e32 v181, s35, v173\n\t"
      "v_mfma_f32_16x16x32_bf16 v[20:23], v[88:91], v[64:67], v[20:23]\n\t"
      "v_cmp_ge_i32_e32 vcc, -7, v199\n\t"
      "s_waitcnt lgkmcnt(7)\n\t"
      "v_mfma_f32_16x16x32_bf16 v[48:51], v[96:99], v[104:107], 0\n\t"
      "ds_read_b64_tr_b16 v[76:77], v166 offset:2048\n\t"
      "ds_read_b64_tr_b16 v[78:79], v166 offset:2560\n\t"
      "v_subrev_u32_e32 v201, 2, v199\n\t"
      "v_mfma_f32_16x16x32_bf16 v[56:59], v[100:103], v[104:107], 0\n\t"
      "ds_read_b128 v[68:71], v160 offset:2048\n\t"
      "v_cmp_gt_u32_e64 s[64:65], 8, v201\n\t"
      "v_mfma_f32_16x16x32_bf16 v[40:43], v[92:95], v[104:107], 0\n\t"
      "ds_read_b128 v[72:75], v160 offset:16384\n\t"
      "v_cndmask_b32_e64 v174, v200, 0, vcc\n\t"
      "v_mfma_f32_16x16x32_bf16 v[52:55], v[96:99], v[108:111], 0\n\t"
      "ds_read_b128 v[64:67], v162 offset:2048\n\t"
      "v_cndmask_b32_e64 v178, v200, 0, s[64:65]\n\t"
      "v_mfma_f32_16x16x32_bf16 v[60:63], v[100:103], v[108:111], 0\n\t"
      "ds_read_b64_tr_b16 v[80:81], v167 offset:2048\n\t"
      "v_or_b32_e32 v182, s35, v174\n\t"
      "v_mfma_f32_16x16x32_bf16 v[44:47], v[92:95], v[108:111], 0\n\t"
      "ds_read_b64_tr_b16 v[82:83], v167 offset:2560\n\t"
      "v_cmp_ge_i32_e32 vcc, -6, v199\n\t"
      "s_waitcnt lgkmcnt(7)\n\t"
      "v_mfma_f32_16x16x32_bf16 v[48:51], v[124:127], v[132:135], v[48:51]\n\t"
      "ds_read_b64_tr_b16 v[104:105], v166 offset:3072\n\t"
      "ds_read_b64_tr_b16 v[106:107], v166 offset:3584\n\t"
      "v_subrev_u32_e32 v201, 3, v199\n\t"
      "v_mfma_f32_16x16x32_bf16 v[56:59], v[128:131], v[132:135], v[56:59]\n\t"
      "ds_read_b128 v[96:99], v161 offset:3072\n\t"
      "v_cmp_gt_u32_e64 s[64:65], 8, v201\n\t"
      "v_mfma_f32_16x16x32_bf16 v[40:43], v[120:123], v[132:135], v[40:43]\n\t"
      "ds_read_b128 v[100:103], v161 offset:17408\n\t"
      "v_cndmask_b32_e64 v175, v200, 0, vcc\n\t"
      "v_mfma_f32_16x16x32_bf16 v[52:55], v[124:127], v[136:139], v[52:55]\n\t"
      "ds_read_b128 v[92:95], v163 offset:3072\n\t"
      "v_cndmask_b32_e64 v179, v200, 0, s[64:65]\n\t"
      "v_mfma_f32_16x16x32_bf16 v[60:63], v[128:131], v[136:139], v[60:63]\n\t"
      "ds_read_b64_tr_b16 v[108:109], v167 offset:3072\n\t"
      "v_or_b32_e32 v183, s35, v175\n\t"
      "v_mfma_f32_16x16x32_bf16 v[44:47], v[120:123], v[136:139], v[44:47]\n\t"
      "ds_read_b64_tr_b16 v[110:111], v167 offset:3584\n\t"
      "v_lshrrev_b32_e32 v201, 31, v172\n\t"
      "s_barrier\n\t"
      "s_waitcnt lgkmcnt(7)\n\t"
      "v_mfma_f32_16x16x32_bf16 v[48:51], v[68:71], v[76:79], v[48:51]\n\t"
      "ds_read_b64_tr_b16 v[132:133], v166 offset:16384\n\t"
      "ds_read_b64_tr_b16 v[134:135], v166 offset:16896\n\t"
      "ds_read_b128 v[124:127], v160 offset:4096\n\t"
      "ds_read_b128 v[128:131], v160 offset:18432\n\t"
      "ds_read_b64_tr_b16 v[136:137], v167 offset:16384\n\t"
      "ds_read_b64_tr_b16 v[138:139], v167 offset:16896\n\t"
      "v_lshrrev_b32_e32 v202, 31, v173\n\t"
      "v_mfma_f32_16x16x32_bf16 v[56:59], v[72:75], v[76:79], v[56:59]\n\t"
      "ds_read_b64_tr_b16 v[140:141], v166 offset:17408\n\t"
      "v_lshl_or_b32 v201, v202, 16, v201\n\t"
      "v_mfma_f32_16x16x32_bf16 v[40:43], v[64:67], v[76:79], v[40:43]\n\t"
      "ds_read_b64_tr_b16 v[142:143], v166 offset:17920\n\t"
      "v_mul_u32_u24_e32 v184, 0xffff, v201\n\t"
      "v_mfma_f32_16x16x32_bf16 v[52:55], v[68:71], v[80:83], v[52:55]\n\t"
      "ds_read_b128 v[120:123], v162 offset:4096\n\t"
      "v_lshrrev_b32_e32 v201, 31, v174\n\t"
      "v_mfma_f32_16x16x32_bf16 v[60:63], v[72:75], v[80:83], v[60:63]\n\t"
      "ds_read_b64_tr_b16 v[144:145], v167 offset:17408\n\t"
      "v_lshrrev_b32_e32 v202, 31, v175\n\t"
      "v_mfma_f32_16x16x32_bf16 v[44:47], v[64:67], v[80:83], v[44:47]\n\t"
      "ds_read_b64_tr_b16 v[146:147], v167 offset:17920\n\t"
      "v_lshl_or_b32 v201, v202, 16, v201\n\t"
      "s_waitcnt lgkmcnt(11)\n\t"
      "v_mfma_f32_16x16x32_bf16 v[48:51], v[96:99], v[104:107], v[48:51]\n\t"
      "ds_read_b64_tr_b16 v[76:77], v166 offset:18432\n\t"
      "ds_read_b64_tr_b16 v[78:79], v166 offset:18944\n\t"
      "ds_read_b128 v[68:71], v161 offset:5120\n\t"
      "ds_read_b128 v[72:75], v161 offset:19456\n\t"
      "ds_read_b64_tr_b16 v[80:81], v167 offset:18432\n\t"
      "ds_read_b64_tr_b16 v[82:83], v167 offset:18944\n\t"
      "v_mul_u32_u24_e32 v185, 0xffff, v201\n\t"
      "v_mfma_f32_16x16x32_bf16 v[56:59], v[100:103], v[104:107], v[56:59]\n\t"
      "ds_read_b64_tr_b16 v[84:85], v166 offset:19456\n\t"
      "s_mov_b32 s68, 0x0\n\t"
      "v_mfma_f32_16x16x32_bf16 v[40:43], v[92:95], v[104:107], v[40:43]\n\t"
      "ds_read_b64_tr_b16 v[86:87], v166 offset:19968\n\t"
      "s_mov_b32 s69, 0xffff0000\n\t"
      "v_mfma_f32_16x16x32_bf16 v[52:55], v[96:99], v[108:111], v[52:55]\n\t"
      "ds_read_b128 v[64:67], v163 offset:5120\n\t"
      "s_mov_b32 s70, 0xffff\n\t"
      "v_mfma_f32_16x16x32_bf16 v[60:63], v[100:103], v[108:111], v[60:63]\n\t"
      "ds_read_b64_tr_b16 v[88:89], v167 offset:19456\n\t"
      "s_mov_b32 s71, 0x0\n\t"
      "v_mfma_f32_16x16x32_bf16 v[44:47], v[92:95], v[108:111], v[44:47]\n\t"
      "ds_read_b64_tr_b16 v[90:91], v167 offset:19968\n\t"
      "v_lshlrev_b32_e32 v231, 3, v192\n\t"
      "s_barrier\n\t"
      "s_waitcnt lgkmcnt(11)\n\t"
      "v_mfma_f32_16x16x32_bf16 v[0:3], v[132:135], v[124:127], v[0:3]\n\t"
      "ds_read_b64_tr_b16 v[104:105], v166 offset:32768\n\t"
      "ds_read_b64_tr_b16 v[106:107], v166 offset:33280\n\t"
      "s_lshl_b32 s34, s25, 12\n\t"
      "v_mfma_f32_16x16x32_bf16 v[8:11], v[132:135], v[128:131], v[8:11]\n\t"
      "ds_read_b128 v[96:99], v160 offset:6144\n\t"
      "s_lshr_b32 s35, s39, 2\n\t"
      "v_mfma_f32_16x16x32_bf16 v[4:7], v[136:139], v[124:127], v[4:7]\n\t"
      "ds_read_b128 v[100:103], v160 offset:20480\n\t"
      "s_add_u32 s34, s34, s35\n\t"
      "v_mfma_f32_16x16x32_bf16 v[12:15], v[136:139], v[128:131], v[12:15]\n\t"
      "ds_read_b64_tr_b16 v[108:109], v167 offset:32768\n\t"
      "s_add_u32 s34, s34, 12582912\n\t"
      "v_mfma_f32_16x16x32_bf16 v[24:27], v[140:143], v[124:127], v[24:27]\n\t"
      "ds_read_b64_tr_b16 v[110:111], v167 offset:33280\n\t"
      "s_add_u32 s46, s20, s34\n\t"
      "v_mfma_f32_16x16x32_bf16 v[32:35], v[140:143], v[128:131], v[32:35]\n\t"
      "ds_read_b64_tr_b16 v[112:113], v166 offset:33792\n\t"
      "s_addc_u32 s47, s21, 0\n\t"
      "v_mfma_f32_16x16x32_bf16 v[16:19], v[140:143], v[120:123], v[16:19]\n\t"
      "ds_read_b64_tr_b16 v[114:115], v166 offset:34304\n\t"
      "s_lshl_b32 s34, s28, 7\n\t"
      "v_mfma_f32_16x16x32_bf16 v[28:31], v[144:147], v[124:127], v[28:31]\n\t"
      "ds_read_b128 v[92:95], v162 offset:6144\n\t"
      "s_add_u32 s58, s46, s34\n\t"
      "v_mfma_f32_16x16x32_bf16 v[36:39], v[144:147], v[128:131], v[36:39]\n\t"
      "ds_read_b64_tr_b16 v[116:117], v167 offset:33792\n\t"
      "s_addc_u32 s59, s47, 0\n\t"
      "v_mfma_f32_16x16x32_bf16 v[20:23], v[144:147], v[120:123], v[20:23]\n\t"
      "ds_read_b64_tr_b16 v[118:119], v167 offset:34304\n\t"
      "s_and_b32 s34, s24, 1\n\t"
      "s_waitcnt lgkmcnt(11)\n\t"
      "v_mfma_f32_16x16x32_bf16 v[0:3], v[76:79], v[68:71], v[0:3]\n\t"
      "ds_read_b64_tr_b16 v[132:133], v166 offset:34816\n\t"
      "ds_read_b64_tr_b16 v[134:135], v166 offset:35328\n\t"
      "s_lshl1_add_u32 s34, s25, s34\n\t"
      "v_mfma_f32_16x16x32_bf16 v[8:11], v[76:79], v[72:75], v[8:11]\n\t"
      "ds_read_b128 v[124:127], v161 offset:7168\n\t"
      "s_lshl_b32 s34, s34, 5\n\t"
      "v_mfma_f32_16x16x32_bf16 v[4:7], v[80:83], v[68:71], v[4:7]\n\t"
      "ds_read_b128 v[128:131], v161 offset:21504\n\t"
      "s_lshl1_add_u32 s34, s26, s34\n\t"
      "v_mfma_f32_16x16x32_bf16 v[12:15], v[80:83], v[72:75], v[12:15]\n\t"
      "ds_read_b64_tr_b16 v[136:137], v167 offset:34816\n\t"
      "s_bfe_u32 s35, s24, 65537\n\t"
      "v_mfma_f32_16x16x32_bf16 v[24:27], v[84:87], v[68:71], v[24:27]\n\t"
      "ds_read_b64_tr_b16 v[138:139], v167 offset:35328\n\t"
      "s_add_u32 s34, s34, s35\n\t"
      "v_mfma_f32_16x16x32_bf16 v[32:35], v[84:87], v[72:75], v[32:35]\n\t"
      "ds_read_b64_tr_b16 v[140:141], v166 offset:35840\n\t"
      "s_lshl_b32 s34, s34, 10\n\t"
      "v_mfma_f32_16x16x32_bf16 v[16:19], v[84:87], v[64:67], v[16:19]\n\t"
      "ds_read_b64_tr_b16 v[142:143], v166 offset:36352\n\t"
      "s_add_u32 s42, s20, s34\n\t"
      "v_mfma_f32_16x16x32_bf16 v[28:31], v[88:91], v[68:71], v[28:31]\n\t"
      "ds_read_b128 v[120:123], v165 offset:7168\n\t"
      "s_addc_u32 s43, s21, 0\n\t"
      "v_mfma_f32_16x16x32_bf16 v[36:39], v[88:91], v[72:75], v[36:39]\n\t"
      "ds_read_b64_tr_b16 v[144:145], v167 offset:35840\n\t"
      "s_add_u32 s44, s42, 16384\n\t"
      "v_mfma_f32_16x16x32_bf16 v[20:23], v[88:91], v[64:67], v[20:23]\n\t"
      "ds_read_b64_tr_b16 v[146:147], v167 offset:36352\n\t"
      "s_addc_u32 s45, s43, 0\n\t"
      "s_barrier\n\t"
      "s_waitcnt lgkmcnt(11)\n\t"
      "v_mfma_f32_16x16x32_bf16 v[0:3], v[104:107], v[96:99], v[0:3]\n\t"
      "ds_read_b64_tr_b16 v[76:77], v166 offset:49152\n\t"
      "ds_read_b64_tr_b16 v[78:79], v166 offset:49664\n\t"
      "v_and_b32_e32 v199, 60, v192\n\t"
      "v_mfma_f32_16x16x32_bf16 v[8:11], v[104:107], v[100:103], v[8:11]\n\t"
      "ds_read_b128 v[68:71], v160 offset:8192\n\t"
      "v_and_b32_e32 v200, 24, v192\n\t"
      "v_mfma_f32_16x16x32_bf16 v[4:7], v[108:111], v[96:99], v[4:7]\n\t"
      "ds_read_b128 v[72:75], v160 offset:22528\n\t"
      "v_lshlrev_b32_e32 v202, 3, v196\n\t"
      "v_mfma_f32_16x16x32_bf16 v[12:15], v[108:111], v[100:103], v[12:15]\n\t"
      "ds_read_b64_tr_b16 v[80:81], v167 offset:49152\n\t"
      "v_lshl_or_b32 v202, v200, 2, v202\n\t"
      "v_mfma_f32_16x16x32_bf16 v[24:27], v[112:115], v[96:99], v[24:27]\n\t"
      "ds_read_b64_tr_b16 v[82:83], v167 offset:49664\n\t"
      "v_lshl_or_b32 v201, v199, 5, v202\n\t"
      "v_mfma_f32_16x16x32_bf16 v[32:35], v[112:115], v[100:103], v[32:35]\n\t"
      "ds_read_b64_tr_b16 v[84:85], v166 offset:50176\n\t"
      "v_add_u32_e32 v216, 106496, v201\n\t"
      "v_mfma_f32_16x16x32_bf16 v[16:19], v[112:115], v[92:95], v[16:19]\n\t"
      "ds_read_b64_tr_b16 v[86:87], v166 offset:50688\n\t"
      "v_mov_b32_e32 v199, 2048\n\t"
      "v_mfma_f32_16x16x32_bf16 v[28:31], v[116:119], v[96:99], v[28:31]\n\t"
      "ds_read_b128 v[64:67], v164 offset:8192\n\t"
      "v_cndmask_b32_e64 v199, v199, 0, s[68:69]\n\t"
      "v_mfma_f32_16x16x32_bf16 v[36:39], v[116:119], v[100:103], v[36:39]\n\t"
      "ds_read_b64_tr_b16 v[88:89], v167 offset:50176\n\t"
      "v_add_u32_e32 v220, v216, v199\n\t"
      "v_mfma_f32_16x16x32_bf16 v[20:23], v[116:119], v[92:95], v[20:23]\n\t"
      "ds_read_b64_tr_b16 v[90:91], v167 offset:50688\n\t"
      "v_xor_b32_e32 v217, 32, v216\n\t"
      "s_waitcnt lgkmcnt(11)\n\t"
      "v_mfma_f32_16x16x32_bf16 v[0:3], v[132:135], v[124:127], v[0:3]\n\t"
      "ds_read_b64_tr_b16 v[104:105], v166 offset:51200\n\t"
      "ds_read_b64_tr_b16 v[106:107], v166 offset:51712\n\t"
      "v_xor_b32_e32 v218, 64, v216\n\t"
      "v_mfma_f32_16x16x32_bf16 v[8:11], v[132:135], v[128:131], v[8:11]\n\t"
      "ds_read_b128 v[96:99], v161 offset:9216\n\t"
      "v_xor_b32_e32 v219, 96, v216\n\t"
      "v_mfma_f32_16x16x32_bf16 v[4:7], v[136:139], v[124:127], v[4:7]\n\t"
      "ds_read_b128 v[100:103], v161 offset:23552\n\t"
      "v_xor_b32_e32 v221, 32, v220\n\t"
      "v_mfma_f32_16x16x32_bf16 v[12:15], v[136:139], v[128:131], v[12:15]\n\t"
      "ds_read_b64_tr_b16 v[108:109], v167 offset:51200\n\t"
      "v_xor_b32_e32 v222, 64, v220\n\t"
      "v_mfma_f32_16x16x32_bf16 v[24:27], v[140:143], v[124:127], v[24:27]\n\t"
      "ds_read_b64_tr_b16 v[110:111], v167 offset:51712\n\t"
      "v_xor_b32_e32 v223, 96, v220\n\t"
      "v_mfma_f32_16x16x32_bf16 v[32:35], v[140:143], v[128:131], v[32:35]\n\t"
      "ds_read_b64_tr_b16 v[112:113], v166 offset:52224\n\t"
      "s_lshr_b32 s34, s39, 1\n\t"
      "v_mfma_f32_16x16x32_bf16 v[16:19], v[140:143], v[120:123], v[16:19]\n\t"
      "ds_read_b64_tr_b16 v[114:115], v166 offset:52736\n\t"
      "s_add_u32 s34, s34, 98304\n\t"
      "v_mfma_f32_16x16x32_bf16 v[28:31], v[144:147], v[124:127], v[28:31]\n\t"
      "ds_read_b128 v[92:95], v165 offset:9216\n\t"
      "v_lshrrev_b32_e32 v199, 1, v193\n\t"
      "v_mfma_f32_16x16x32_bf16 v[36:39], v[144:147], v[128:131], v[36:39]\n\t"
      "ds_read_b64_tr_b16 v[116:117], v167 offset:52224\n\t"
      "v_xor_b32_e32 v199, v199, v198\n\t"
      "v_mfma_f32_16x16x32_bf16 v[20:23], v[144:147], v[120:123], v[20:23]\n\t"
      "ds_read_b64_tr_b16 v[118:119], v167 offset:52736\n\t"
      "v_lshlrev_b32_e32 v199, 4, v199\n\t"
      "s_barrier\n\t"
      "s_waitcnt lgkmcnt(11)\n\t"
      "v_mfma_f32_16x16x32_bf16 v[0:3], v[76:79], v[68:71], v[0:3]\n\t"
      "ds_read_b64_tr_b16 v[132:133], v168\n\t"
      "ds_read_b64_tr_b16 v[134:135], v168 offset:512\n\t"
      "v_lshl_or_b32 v199, v194, 7, v199\n\t"
      "v_mfma_f32_16x16x32_bf16 v[8:11], v[76:79], v[72:75], v[8:11]\n\t"
      "ds_read_b128 v[124:127], v160 offset:10240\n\t"
      "v_and_b32_e32 v200, 1, v193\n\t"
      "v_mfma_f32_16x16x32_bf16 v[4:7], v[80:83], v[68:71], v[4:7]\n\t"
      "ds_read_b128 v[128:131], v160 offset:24576\n\t"
      "v_lshl_or_b32 v199, v200, 3, v199\n\t"
      "v_mfma_f32_16x16x32_bf16 v[12:15], v[80:83], v[72:75], v[12:15]\n\t"
      "ds_read_b64_tr_b16 v[136:137], v169\n\t"
      "v_add_u32_e32 v228, s34, v199\n\t"
      "v_mfma_f32_16x16x32_bf16 v[24:27], v[84:87], v[68:71], v[24:27]\n\t"
      "ds_read_b64_tr_b16 v[138:139], v169 offset:512\n\t"
      "v_mfma_f32_16x16x32_bf16 v[32:35], v[84:87], v[72:75], v[32:35]\n\t"
      "ds_read_b64_tr_b16 v[140:141], v168 offset:1024\n\t"
      "v_mfma_f32_16x16x32_bf16 v[16:19], v[84:87], v[64:67], v[16:19]\n\t"
      "ds_read_b64_tr_b16 v[142:143], v168 offset:1536\n\t"
      "v_mfma_f32_16x16x32_bf16 v[28:31], v[88:91], v[68:71], v[28:31]\n\t"
      "ds_read_b128 v[120:123], v164 offset:10240\n\t"
      "v_mfma_f32_16x16x32_bf16 v[36:39], v[88:91], v[72:75], v[36:39]\n\t"
      "ds_read_b64_tr_b16 v[144:145], v169 offset:1024\n\t"
      "v_mfma_f32_16x16x32_bf16 v[20:23], v[88:91], v[64:67], v[20:23]\n\t"
      "ds_read_b64_tr_b16 v[146:147], v169 offset:1536\n\t"
      "s_waitcnt lgkmcnt(11)\n\t"
      "v_mfma_f32_16x16x32_bf16 v[0:3], v[104:107], v[96:99], v[0:3]\n\t"
      "ds_read_b64_tr_b16 v[76:77], v168 offset:2048\n\t"
      "ds_read_b64_tr_b16 v[78:79], v168 offset:2560\n\t"
      "v_mfma_f32_16x16x32_bf16 v[8:11], v[104:107], v[100:103], v[8:11]\n\t"
      "ds_read_b128 v[68:71], v161 offset:11264\n\t"
      "v_mfma_f32_16x16x32_bf16 v[4:7], v[108:111], v[96:99], v[4:7]\n\t"
      "ds_read_b128 v[72:75], v161 offset:25600\n\t"
      "v_mfma_f32_16x16x32_bf16 v[12:15], v[108:111], v[100:103], v[12:15]\n\t"
      "ds_read_b64_tr_b16 v[80:81], v169 offset:2048\n\t"
      "v_mfma_f32_16x16x32_bf16 v[24:27], v[112:115], v[96:99], v[24:27]\n\t"
      "ds_read_b64_tr_b16 v[82:83], v169 offset:2560\n\t"
      "v_mfma_f32_16x16x32_bf16 v[32:35], v[112:115], v[100:103], v[32:35]\n\t"
      "ds_read_b64_tr_b16 v[84:85], v168 offset:3072\n\t"
      "v_mfma_f32_16x16x32_bf16 v[16:19], v[112:115], v[92:95], v[16:19]\n\t"
      "ds_read_b64_tr_b16 v[86:87], v168 offset:3584\n\t"
      "v_mfma_f32_16x16x32_bf16 v[28:31], v[116:119], v[96:99], v[28:31]\n\t"
      "ds_read_b128 v[64:67], v165 offset:11264\n\t"
      "v_mfma_f32_16x16x32_bf16 v[36:39], v[116:119], v[100:103], v[36:39]\n\t"
      "ds_read_b64_tr_b16 v[88:89], v169 offset:3072\n\t"
      "v_mfma_f32_16x16x32_bf16 v[20:23], v[116:119], v[92:95], v[20:23]\n\t"
      "ds_read_b64_tr_b16 v[90:91], v169 offset:3584\n\t"
      "s_barrier\n\t"
      "s_waitcnt lgkmcnt(11)\n\t"
      "v_mfma_f32_16x16x32_bf16 v[0:3], v[132:135], v[124:127], v[0:3]\n\t"
      "ds_read_b64_tr_b16 v[104:105], v168 offset:16384\n\t"
      "ds_read_b64_tr_b16 v[106:107], v168 offset:16896\n\t"
      "v_mfma_f32_16x16x32_bf16 v[8:11], v[132:135], v[128:131], v[8:11]\n\t"
      "ds_read_b128 v[96:99], v160 offset:12288\n\t"
      "v_mfma_f32_16x16x32_bf16 v[4:7], v[136:139], v[124:127], v[4:7]\n\t"
      "ds_read_b128 v[100:103], v160 offset:26624\n\t"
      "v_mfma_f32_16x16x32_bf16 v[12:15], v[136:139], v[128:131], v[12:15]\n\t"
      "ds_read_b64_tr_b16 v[108:109], v169 offset:16384\n\t"
      "v_mfma_f32_16x16x32_bf16 v[24:27], v[140:143], v[124:127], v[24:27]\n\t"
      "ds_read_b64_tr_b16 v[110:111], v169 offset:16896\n\t"
      "v_mfma_f32_16x16x32_bf16 v[32:35], v[140:143], v[128:131], v[32:35]\n\t"
      "ds_read_b64_tr_b16 v[112:113], v168 offset:17408\n\t"
      "v_mfma_f32_16x16x32_bf16 v[16:19], v[140:143], v[120:123], v[16:19]\n\t"
      "ds_read_b64_tr_b16 v[114:115], v168 offset:17920\n\t"
      "v_mfma_f32_16x16x32_bf16 v[28:31], v[144:147], v[124:127], v[28:31]\n\t"
      "ds_read_b128 v[92:95], v164 offset:12288\n\t"
      "v_mfma_f32_16x16x32_bf16 v[36:39], v[144:147], v[128:131], v[36:39]\n\t"
      "ds_read_b64_tr_b16 v[116:117], v169 offset:17408\n\t"
      "v_mfma_f32_16x16x32_bf16 v[20:23], v[144:147], v[120:123], v[20:23]\n\t"
      "ds_read_b64_tr_b16 v[118:119], v169 offset:17920\n\t"
      "s_waitcnt lgkmcnt(11)\n\t"
      "v_mfma_f32_16x16x32_bf16 v[0:3], v[76:79], v[68:71], v[0:3]\n\t"
      "ds_read_b64_tr_b16 v[132:133], v168 offset:18432\n\t"
      "ds_read_b64_tr_b16 v[134:135], v168 offset:18944\n\t"
      "v_mfma_f32_16x16x32_bf16 v[8:11], v[76:79], v[72:75], v[8:11]\n\t"
      "ds_read_b128 v[124:127], v161 offset:13312\n\t"
      "v_mfma_f32_16x16x32_bf16 v[4:7], v[80:83], v[68:71], v[4:7]\n\t"
      "ds_read_b128 v[128:131], v161 offset:27648\n\t"
      "v_mfma_f32_16x16x32_bf16 v[12:15], v[80:83], v[72:75], v[12:15]\n\t"
      "ds_read_b64_tr_b16 v[136:137], v169 offset:18432\n\t"
      "v_mfma_f32_16x16x32_bf16 v[24:27], v[84:87], v[68:71], v[24:27]\n\t"
      "ds_read_b64_tr_b16 v[138:139], v169 offset:18944\n\t"
      "v_mfma_f32_16x16x32_bf16 v[32:35], v[84:87], v[72:75], v[32:35]\n\t"
      "ds_read_b64_tr_b16 v[140:141], v168 offset:19456\n\t"
      "v_mfma_f32_16x16x32_bf16 v[16:19], v[84:87], v[64:67], v[16:19]\n\t"
      "ds_read_b64_tr_b16 v[142:143], v168 offset:19968\n\t"
      "v_mfma_f32_16x16x32_bf16 v[28:31], v[88:91], v[68:71], v[28:31]\n\t"
      "ds_read_b128 v[120:123], v165 offset:13312\n\t"
      "v_mfma_f32_16x16x32_bf16 v[36:39], v[88:91], v[72:75], v[36:39]\n\t"
      "ds_read_b64_tr_b16 v[144:145], v169 offset:19456\n\t"
      "v_mfma_f32_16x16x32_bf16 v[20:23], v[88:91], v[64:67], v[20:23]\n\t"
      "ds_read_b64_tr_b16 v[146:147], v169 offset:19968\n\t"
      "s_barrier\n\t"
      "s_waitcnt lgkmcnt(11)\n\t"
      "v_mfma_f32_16x16x32_bf16 v[0:3], v[104:107], v[96:99], v[0:3]\n\t"
      "ds_read_b64_tr_b16 v[76:77], v166\n\t"
      "v_mfma_f32_16x16x32_bf16 v[8:11], v[104:107], v[100:103], v[8:11]\n\t"
      "ds_read_b64_tr_b16 v[78:79], v166 offset:512\n\t"
      "v_mfma_f32_16x16x32_bf16 v[4:7], v[108:111], v[96:99], v[4:7]\n\t"
      "ds_read_b128 v[68:71], v160 offset:4096\n\t"
      "v_mfma_f32_16x16x32_bf16 v[12:15], v[108:111], v[100:103], v[12:15]\n\t"
      "ds_read_b128 v[72:75], v160 offset:18432\n\t"
      "v_mfma_f32_16x16x32_bf16 v[24:27], v[112:115], v[96:99], v[24:27]\n\t"
      "ds_read_b128 v[64:67], v162 offset:4096\n\t"
      "v_mfma_f32_16x16x32_bf16 v[32:35], v[112:115], v[100:103], v[32:35]\n\t"
      "ds_read_b64_tr_b16 v[80:81], v167\n\t"
      "v_mfma_f32_16x16x32_bf16 v[16:19], v[112:115], v[92:95], v[16:19]\n\t"
      "ds_read_b64_tr_b16 v[82:83], v167 offset:512\n\t"
      "v_mfma_f32_16x16x32_bf16 v[28:31], v[116:119], v[96:99], v[28:31]\n\t"
      "v_mfma_f32_16x16x32_bf16 v[36:39], v[116:119], v[100:103], v[36:39]\n\t"
      "v_mfma_f32_16x16x32_bf16 v[20:23], v[116:119], v[92:95], v[20:23]\n\t"
      "s_waitcnt lgkmcnt(7)\n\t"
      "v_mfma_f32_16x16x32_bf16 v[0:3], v[132:135], v[124:127], v[0:3]\n\t"
      "ds_read_b64_tr_b16 v[104:105], v166 offset:1024\n\t"
      "v_mfma_f32_16x16x32_bf16 v[8:11], v[132:135], v[128:131], v[8:11]\n\t"
      "ds_read_b64_tr_b16 v[106:107], v166 offset:1536\n\t"
      "v_mfma_f32_16x16x32_bf16 v[4:7], v[136:139], v[124:127], v[4:7]\n\t"
      "ds_read_b128 v[96:99], v161 offset:5120\n\t"
      "v_mfma_f32_16x16x32_bf16 v[12:15], v[136:139], v[128:131], v[12:15]\n\t"
      "ds_read_b128 v[100:103], v161 offset:19456\n\t"
      "v_mfma_f32_16x16x32_bf16 v[24:27], v[140:143], v[124:127], v[24:27]\n\t"
      "ds_read_b128 v[92:95], v163 offset:5120\n\t"
      "v_mfma_f32_16x16x32_bf16 v[32:35], v[140:143], v[128:131], v[32:35]\n\t"
      "ds_read_b64_tr_b16 v[108:109], v167 offset:1024\n\t"
      "v_mfma_f32_16x16x32_bf16 v[16:19], v[140:143], v[120:123], v[16:19]\n\t"
      "ds_read_b64_tr_b16 v[110:111], v167 offset:1536\n\t"
      "v_mfma_f32_16x16x32_bf16 v[28:31], v[144:147], v[124:127], v[28:31]\n\t"
      "v_mfma_f32_16x16x32_bf16 v[36:39], v[144:147], v[128:131], v[36:39]\n\t"
      "v_mfma_f32_16x16x32_bf16 v[20:23], v[144:147], v[120:123], v[20:23]\n\t"
      "s_waitcnt lgkmcnt(7)\n\t"
      "v_mfma_f32_16x16x32_bf16 v[48:51], v[68:71], v[76:79], v[48:51]\n\t"
      "ds_read_b64_tr_b16 v[132:133], v166 offset:2048\n\t"
      "ds_read_b64_tr_b16 v[134:135], v166 offset:2560\n\t"
      "v_mfma_f32_16x16x32_bf16 v[56:59], v[72:75], v[76:79], v[56:59]\n\t"
      "ds_read_b128 v[124:127], v160 offset:6144\n\t"
      "v_mfma_f32_16x16x32_bf16 v[40:43], v[64:67], v[76:79], v[40:43]\n\t"
      "ds_read_b128 v[128:131], v160 offset:20480\n\t"
      "v_cvt_pk_bf16_f32 v84, v0, v1\n\t"
      "v_cvt_pk_bf16_f32 v85, v2, v3\n\t"
      "v_cvt_pk_bf16_f32 v86, v4, v5\n\t"
      "v_mfma_f32_16x16x32_bf16 v[52:55], v[68:71], v[80:83], v[52:55]\n\t"
      "ds_read_b128 v[120:123], v162 offset:6144\n\t"
      "v_cvt_pk_bf16_f32 v87, v6, v7\n\t"
      "v_cvt_pk_bf16_f32 v88, v8, v9\n\t"
      "v_cvt_pk_bf16_f32 v89, v10, v11\n\t"
      "v_mfma_f32_16x16x32_bf16 v[60:63], v[72:75], v[80:83], v[60:63]\n\t"
      "ds_read_b64_tr_b16 v[136:137], v167 offset:2048\n\t"
      "v_cvt_pk_bf16_f32 v90, v12, v13\n\t"
      "v_cvt_pk_bf16_f32 v91, v14, v15\n\t"
      "v_cvt_pk_bf16_f32 v152, v24, v25\n\t"
      "v_mfma_f32_16x16x32_bf16 v[44:47], v[64:67], v[80:83], v[44:47]\n\t"
      "ds_read_b64_tr_b16 v[138:139], v167 offset:2560\n\t"
      "v_cvt_pk_bf16_f32 v153, v26, v27\n\t"
      "v_cvt_pk_bf16_f32 v154, v28, v29\n\t"
      "v_cvt_pk_bf16_f32 v155, v30, v31\n\t"
      "s_waitcnt lgkmcnt(7)\n\t"
      "v_mfma_f32_16x16x32_bf16 v[48:51], v[96:99], v[104:107], v[48:51]\n\t"
      "ds_read_b64_tr_b16 v[76:77], v166 offset:3072\n\t"
      "ds_read_b64_tr_b16 v[78:79], v166 offset:3584\n\t"
      "v_cndmask_b32_e64 v86, v86, 0, s[68:69]\n\t"
      "v_cndmask_b32_e64 v87, v87, 0, s[68:69]\n\t"
      "v_cvt_pk_bf16_f32 v156, v32, v33\n\t"
      "v_mfma_f32_16x16x32_bf16 v[56:59], v[100:103], v[104:107], v[56:59]\n\t"
      "ds_read_b128 v[68:71], v161 offset:7168\n\t"
      "v_cvt_pk_bf16_f32 v157, v34, v35\n\t"
      "v_cvt_pk_bf16_f32 v158, v36, v37\n\t"
      "v_cvt_pk_bf16_f32 v159, v38, v39\n\t"
      "v_mfma_f32_16x16x32_bf16 v[40:43], v[92:95], v[104:107], v[40:43]\n\t"
      "ds_read_b128 v[72:75], v161 offset:21504\n\t"
      "v_cndmask_b32_e64 v90, v90, 0, s[68:69]\n\t"
      "v_cndmask_b32_e64 v91, v91, 0, s[68:69]\n\t"
      "v_cvt_pk_bf16_f32 v148, v16, v17\n\t"
      "v_mfma_f32_16x16x32_bf16 v[52:55], v[96:99], v[108:111], v[52:55]\n\t"
      "ds_read_b128 v[64:67], v165 offset:7168\n\t"
      "v_cvt_pk_bf16_f32 v149, v18, v19\n\t"
      "v_cvt_pk_bf16_f32 v150, v20, v21\n\t"
      "v_cvt_pk_bf16_f32 v151, v22, v23\n\t"
      "v_mfma_f32_16x16x32_bf16 v[60:63], v[100:103], v[108:111], v[60:63]\n\t"
      "ds_read_b64_tr_b16 v[80:81], v167 offset:3072\n\t"
      "v_mov_b32_e32 v34, 0x3e8b97f6\n\t"
      "v_mfma_f32_16x16x32_bf16 v[28:31], v[156:159], v[88:91], v[176:179]\n\t"
      "v_mfma_f32_16x16x32_bf16 v[24:27], v[152:155], v[88:91], v[172:175]\n\t"
      "v_mfma_f32_16x16x32_bf16 v[44:47], v[92:95], v[108:111], v[44:47]\n\t"
      "ds_read_b64_tr_b16 v[82:83], v167 offset:3584\n\t"
      "v_mfma_f32_16x16x32_bf16 v[20:23], v[152:155], v[84:87], v[176:179]\n\t"
      "v_mfma_f32_16x16x32_bf16 v[16:19], v[148:151], v[84:87], v[180:183]\n\t"
      "s_barrier\n\t"
      "s_waitcnt lgkmcnt(7)\n\t"
      "v_mfma_f32_16x16x32_bf16 v[48:51], v[124:127], v[132:135], v[48:51]\n\t"
      "ds_read_b64_tr_b16 v[104:105], v166 offset:16384\n\t"
      "ds_read_b64_tr_b16 v[106:107], v166 offset:16896\n\t"
      "v_mfma_f32_16x16x32_bf16 v[56:59], v[128:131], v[132:135], v[56:59]\n\t"
      "ds_read_b128 v[96:99], v160 offset:8192\n\t"
      "v_max_f32_e32 v24, v24, v28\n\t"
      "v_mfma_f32_16x16x32_bf16 v[40:43], v[120:123], v[132:135], v[40:43]\n\t"
      "ds_read_b128 v[100:103], v160 offset:22528\n\t"
      "v_max_f32_e32 v25, v25, v29\n\t"
      "v_max_f32_e32 v26, v26, v30\n\t"
      "v_max_f32_e32 v27, v27, v31\n\t"
      "v_mfma_f32_16x16x32_bf16 v[52:55], v[124:127], v[136:139], v[52:55]\n\t"
      "ds_read_b128 v[92:95], v164 offset:8192\n\t"
      "v_max_f32_e32 v16, v16, v20\n\t"
      "v_max_f32_e32 v17, v17, v21\n\t"
      "v_max_f32_e32 v18, v18, v22\n\t"
      "v_mfma_f32_16x16x32_bf16 v[60:63], v[128:131], v[136:139], v[60:63]\n\t"
      "ds_read_b64_tr_b16 v[108:109], v167 offset:16384\n\t"
      "v_max_f32_e32 v19, v19, v23\n\t"
      "v_max3_f32 v33, v24, v25, v26\n\t"
      "v_max3_f32 v32, v16, v17, v18\n\t"
      "v_mfma_f32_16x16x32_bf16 v[44:47], v[120:123], v[136:139], v[44:47]\n\t"
      "ds_read_b64_tr_b16 v[110:111], v167 offset:16896\n\t"
      "v_max_f32_e32 v33, v33, v27\n\t"
      "v_max_f32_e32 v32, v32, v19\n\t"
      "s_waitcnt lgkmcnt(7)\n\t"
      "v_mfma_f32_16x16x32_bf16 v[48:51], v[68:71], v[76:79], v[48:51]\n\t"
      "ds_read_b64_tr_b16 v[132:133], v166 offset:17408\n\t"
      "ds_read_b64_tr_b16 v[134:135], v166 offset:17920\n\t"
      "v_permlane16_swap_b32_e32 v32, v33\n\t"
      "v_max_f32_e32 v32, v32, v33\n\t"
      "v_mov_b32_e32 v33, v32\n\t"
      "v_mfma_f32_16x16x32_bf16 v[56:59], v[72:75], v[76:79], v[56:59]\n\t"
      "ds_read_b128 v[124:127], v161 offset:9216\n\t"
      "v_permlane32_swap_b32_e32 v32, v33\n\t"
      "v_max_f32_e32 v32, v32, v33\n\t"
      "v_mfma_f32_16x16x32_bf16 v[40:43], v[64:67], v[76:79], v[40:43]\n\t"
      "ds_read_b128 v[128:131], v161 offset:23552\n\t"
      "v_mov_b32_e32 v33, v32\n\t"
      "s_nop 1\n\t"
      "v_permlane16_swap_b32_e32 v32, v33\n\t"
      "v_mfma_f32_16x16x32_bf16 v[52:55], v[68:71], v[80:83], v[52:55]\n\t"
      "ds_read_b128 v[120:123], v165 offset:9216\n\t"
      "v_mul_f32_e64 v33, -v33, v34\n\t"
      "v_mul_f32_e64 v32, -v32, v34\n\t"
      "v_fma_f32 v24, v24, v34, v33\n\t"
      "v_mfma_f32_16x16x32_bf16 v[60:63], v[72:75], v[80:83], v[60:63]\n\t"
      "ds_read_b64_tr_b16 v[136:137], v167 offset:17408\n\t"
      "v_fma_f32 v25, v25, v34, v33\n\t"
      "v_fma_f32 v26, v26, v34, v33\n\t"
      "v_fma_f32 v27, v27, v34, v33\n\t"
      "v_mfma_f32_16x16x32_bf16 v[44:47], v[64:67], v[80:83], v[44:47]\n\t"
      "ds_read_b64_tr_b16 v[138:139], v167 offset:17920\n\t"
      "v_fma_f32 v16, v16, v34, v32\n\t"
      "v_fma_f32 v17, v17, v34, v32\n\t"
      "v_fma_f32 v18, v18, v34, v32\n\t"
      "s_waitcnt lgkmcnt(7)\n\t"
      "v_mfma_f32_16x16x32_bf16 v[48:51], v[96:99], v[104:107], v[48:51]\n\t"
      "ds_read_b64_tr_b16 v[76:77], v166 offset:18432\n\t"
      "ds_read_b64_tr_b16 v[78:79], v166 offset:18944\n\t"
      "v_fma_f32 v19, v19, v34, v32\n\t"
      "v_exp_f32_e32 v24, v24\n\t"
      "v_mfma_f32_16x16x32_bf16 v[56:59], v[100:103], v[104:107], v[56:59]\n\t"
      "ds_read_b128 v[68:71], v160 offset:10240\n\t"
      "v_exp_f32_e32 v25, v25\n\t"
      "v_exp_f32_e32 v26, v26\n\t"
      "v_mfma_f32_16x16x32_bf16 v[40:43], v[92:95], v[104:107], v[40:43]\n\t"
      "ds_read_b128 v[72:75], v160 offset:24576\n\t"
      "v_exp_f32_e32 v27, v27\n\t"
      "v_mfma_f32_16x16x32_bf16 v[52:55], v[96:99], v[108:111], v[52:55]\n\t"
      "ds_read_b128 v[64:67], v164 offset:10240\n\t"
      "v_exp_f32_e32 v16, v16\n\t"
      "v_exp_f32_e32 v17, v17\n\t"
      "v_mfma_f32_16x16x32_bf16 v[60:63], v[100:103], v[108:111], v[60:63]\n\t"
      "ds_read_b64_tr_b16 v[80:81], v167 offset:18432\n\t"
      "v_exp_f32_e32 v18, v18\n\t"
      "v_mfma_f32_16x16x32_bf16 v[44:47], v[92:95], v[108:111], v[44:47]\n\t"
      "ds_read_b64_tr_b16 v[82:83], v167 offset:18944\n\t"
      "v_exp_f32_e32 v19, v19\n\t"
      "v_add_f32_e32 v33, v24, v25\n\t"
      "s_waitcnt lgkmcnt(7)\n\t"
      "v_mfma_f32_16x16x32_bf16 v[48:51], v[124:127], v[132:135], v[48:51]\n\t"
      "ds_read_b64_tr_b16 v[104:105], v166 offset:19456\n\t"
      "ds_read_b64_tr_b16 v[106:107], v166 offset:19968\n\t"
      "v_add_f32_e32 v28, v26, v27\n\t"
      "v_add_f32_e32 v32, v16, v17\n\t"
      "v_add_f32_e32 v20, v18, v19\n\t"
      "v_mfma_f32_16x16x32_bf16 v[56:59], v[128:131], v[132:135], v[56:59]\n\t"
      "ds_read_b128 v[96:99], v161 offset:11264\n\t"
      "v_add_f32_e32 v33, v33, v28\n\t"
      "v_add_f32_e32 v32, v32, v20\n\t"
      "v_mfma_f32_16x16x32_bf16 v[40:43], v[120:123], v[132:135], v[40:43]\n\t"
      "ds_read_b128 v[100:103], v161 offset:25600\n\t"
      "v_permlane16_swap_b32_e32 v32, v33\n\t"
      "v_add_f32_e32 v32, v32, v33\n\t"
      "v_mov_b32_e32 v33, v32\n\t"
      "v_mfma_f32_16x16x32_bf16 v[52:55], v[124:127], v[136:139], v[52:55]\n\t"
      "ds_read_b128 v[92:95], v165 offset:11264\n\t"
      "v_permlane32_swap_b32_e32 v32, v33\n\t"
      "v_add_f32_e32 v32, v32, v33\n\t"
      "v_mfma_f32_16x16x32_bf16 v[60:63], v[128:131], v[136:139], v[60:63]\n\t"
      "ds_read_b64_tr_b16 v[108:109], v167 offset:19456\n\t"
      "v_mov_b32_e32 v33, v32\n\t"
      "s_nop 1\n\t"
      "v_permlane16_swap_b32_e32 v32, v33\n\t"
      "v_mfma_f32_16x16x32_bf16 v[44:47], v[120:123], v[136:139], v[44:47]\n\t"
      "ds_read_b64_tr_b16 v[110:111], v167 offset:19968\n\t"
      "v_rcp_f32_e32 v33, v33\n\t"
      "v_rcp_f32_e32 v32, v32\n\t"
      "s_barrier\n\t"
      "s_waitcnt lgkmcnt(7)\n\t"
      "v_mfma_f32_16x16x32_bf16 v[48:51], v[68:71], v[76:79], v[48:51]\n\t"
      "ds_read_b64_tr_b16 v[132:133], v170 offset:32768\n\t"
      "ds_read_b64_tr_b16 v[134:135], v170 offset:33280\n\t"
      "v_mul_f32_e32 v24, v24, v33\n\t"
      "v_mul_f32_e32 v25, v25, v33\n\t"
      "v_mfma_f32_16x16x32_bf16 v[56:59], v[72:75], v[76:79], v[56:59]\n\t"
      "ds_read_b128 v[124:127], v160 offset:12288\n\t"
      "v_mul_f32_e32 v26, v26, v33\n\t"
      "v_mul_f32_e32 v27, v27, v33\n\t"
      "v_mul_f32_e32 v16, v16, v32\n\t"
      "v_mfma_f32_16x16x32_bf16 v[40:43], v[64:67], v[76:79], v[40:43]\n\t"
      "ds_read_b128 v[128:131], v160 offset:26624\n\t"
      "v_mul_f32_e32 v17, v17, v32\n\t"
      "v_mul_f32_e32 v18, v18, v32\n\t"
      "v_mul_f32_e32 v19, v19, v32\n\t"
      "v_mfma_f32_16x16x32_bf16 v[52:55], v[68:71], v[80:83], v[52:55]\n\t"
      "ds_read_b128 v[120:123], v164 offset:12288\n\t"
      "v_cvt_pk_bf16_f32 v4, v24, v25\n\t"
      "v_cvt_pk_bf16_f32 v5, v26, v27\n\t"
      "v_cvt_pk_bf16_f32 v0, v16, v17\n\t"
      "v_mfma_f32_16x16x32_bf16 v[60:63], v[72:75], v[80:83], v[60:63]\n\t"
      "ds_read_b64_tr_b16 v[136:137], v171 offset:32768\n\t"
      "v_cvt_pk_bf16_f32 v1, v18, v19\n\t"
      "v_and_b32_e32 v6, v184, v4\n\t"
      "v_and_b32_e32 v7, v185, v5\n\t"
      "v_mfma_f32_16x16x32_bf16 v[44:47], v[64:67], v[80:83], v[44:47]\n\t"
      "ds_read_b64_tr_b16 v[138:139], v171 offset:33280\n\t"
      "v_and_b32_e32 v2, v184, v0\n\t"
      "v_and_b32_e32 v3, v185, v1\n\t"
      "v_xor_b32_e32 v4, v4, v6\n\t"
      "s_waitcnt lgkmcnt(7)\n\t"
      "v_mfma_f32_16x16x32_bf16 v[48:51], v[96:99], v[104:107], v[48:51]\n\t"
      "ds_read_b64_tr_b16 v[76:77], v170 offset:33792\n\t"
      "ds_read_b64_tr_b16 v[78:79], v170 offset:34304\n\t"
      "v_xor_b32_e32 v5, v5, v7\n\t"
      "v_xor_b32_e32 v0, v0, v2\n\t"
      "v_xor_b32_e32 v1, v1, v3\n\t"
      "v_mfma_f32_16x16x32_bf16 v[56:59], v[100:103], v[104:107], v[56:59]\n\t"
      "ds_read_b128 v[68:71], v161 offset:13312\n\t"
      "v_mfma_f32_16x16x32_bf16 v[40:43], v[92:95], v[104:107], v[40:43]\n\t"
      "ds_read_b128 v[72:75], v161 offset:27648\n\t"
      "v_mfma_f32_16x16x32_bf16 v[52:55], v[96:99], v[108:111], v[52:55]\n\t"
      "ds_read_b128 v[64:67], v165 offset:13312\n\t"
      "v_mfma_f32_16x16x32_bf16 v[60:63], v[100:103], v[108:111], v[60:63]\n\t"
      "ds_read_b64_tr_b16 v[80:81], v171 offset:33792\n\t"
      "v_mfma_f32_16x16x32_bf16 v[44:47], v[92:95], v[108:111], v[44:47]\n\t"
      "ds_read_b64_tr_b16 v[82:83], v171 offset:34304\n\t"
      "s_waitcnt lgkmcnt(7)\n\t"
      "v_mfma_f32_16x16x32_bf16 v[48:51], v[124:127], v[132:135], v[48:51]\n\t"
      "v_mfma_f32_16x16x32_bf16 v[56:59], v[128:131], v[132:135], v[56:59]\n\t"
      "v_mfma_f32_16x16x32_bf16 v[40:43], v[120:123], v[132:135], v[40:43]\n\t"
      "v_mfma_f32_16x16x32_bf16 v[52:55], v[124:127], v[136:139], v[52:55]\n\t"
      "v_mfma_f32_16x16x32_bf16 v[60:63], v[128:131], v[136:139], v[60:63]\n\t"
      "v_mfma_f32_16x16x32_bf16 v[44:47], v[120:123], v[136:139], v[44:47]\n\t"
      "s_waitcnt lgkmcnt(0)\n\t"
      "v_mfma_f32_16x16x32_bf16 v[48:51], v[68:71], v[76:79], v[48:51]\n\t"
      "v_mfma_f32_16x16x32_bf16 v[56:59], v[72:75], v[76:79], v[56:59]\n\t"
      "v_mfma_f32_16x16x32_bf16 v[40:43], v[64:67], v[76:79], v[40:43]\n\t"
      "v_mfma_f32_16x16x32_bf16 v[52:55], v[68:71], v[80:83], v[52:55]\n\t"
      "v_mfma_f32_16x16x32_bf16 v[60:63], v[72:75], v[80:83], v[60:63]\n\t"
      "v_mfma_f32_16x16x32_bf16 v[44:47], v[64:67], v[80:83], v[44:47]\n\t"
      "s_nop 2\n\t"
      "v_cvt_pk_bf16_f32 v66, v48, v49\n\t"
      "v_cvt_pk_bf16_f32 v67, v50, v51\n\t"
      "v_cvt_pk_bf16_f32 v68, v56, v57\n\t"
      "v_cvt_pk_bf16_f32 v69, v58, v59\n\t"
      "v_cvt_pk_bf16_f32 v64, v40, v41\n\t"
      "v_cvt_pk_bf16_f32 v65, v42, v43\n\t"
      "v_mfma_f32_16x16x32_bf16 v[88:91], v[66:69], v[4:7], 0\n\t"
      "s_nop 0\n\t"
      "v_mfma_f32_16x16x32_bf16 v[80:83], v[64:67], v[0:3], 0\n\t"
      "v_cvt_pk_bf16_f32 v72, v52, v53\n\t"
      "v_cvt_pk_bf16_f32 v73, v54, v55\n\t"
      "v_cvt_pk_bf16_f32 v74, v60, v61\n\t"
      "v_cvt_pk_bf16_f32 v75, v62, v63\n\t"
      "v_cvt_pk_bf16_f32 v70, v44, v45\n\t"
      "v_cvt_pk_bf16_f32 v71, v46, v47\n\t"
      "v_mfma_f32_16x16x32_bf16 v[92:95], v[72:75], v[4:7], 0\n\t"
      "s_nop 0\n\t"
      "v_mfma_f32_16x16x32_bf16 v[84:87], v[70:73], v[0:3], 0\n\t"
      "v_cvt_pk_bf16_f32 v152, v88, v89\n\t"
      "v_cvt_pk_bf16_f32 v153, v90, v91\n\t"
      "v_min_u32_e32 v152, -2, v152\n\t"
      "v_min_u32_e32 v153, -2, v153\n\t"
      "v_cvt_pk_bf16_f32 v148, v80, v81\n\t"
      "v_cvt_pk_bf16_f32 v149, v82, v83\n\t"
      "v_min_u32_e32 v148, -2, v148\n\t"
      "v_min_u32_e32 v149, -2, v149\n\t"
      "v_cvt_pk_bf16_f32 v154, v92, v93\n\t"
      "v_cvt_pk_bf16_f32 v155, v94, v95\n\t"
      "v_cndmask_b32_e64 v154, v154, 0, s[68:69]\n\t"
      "v_min_u32_e32 v154, -2, v154\n\t"
      "v_cndmask_b32_e64 v155, v155, 0, s[68:69]\n\t"
      "v_min_u32_e32 v155, -2, v155\n\t"
      "global_store_dwordx4 v244, v[152:155], s[44:45] sc1\n\t"
      "v_cvt_pk_bf16_f32 v150, v84, v85\n\t"
      "v_cvt_pk_bf16_f32 v151, v86, v87\n\t"
      "v_cndmask_b32_e64 v150, v150, 0, s[68:69]\n\t"
      "v_min_u32_e32 v150, -2, v150\n\t"
      "v_cndmask_b32_e64 v151, v151, 0, s[68:69]\n\t"
      "v_min_u32_e32 v151, -2, v151\n\t"
      "global_store_dwordx4 v244, v[148:151], s[42:43] sc1\n\t"
      "s_barrier\n\t"
      "s_cmp_eq_u32 s26, 7\n\t"
      "s_cbranch_scc1 SPARE_%=\n\t"
      "s_lshl_b32 s34, s25, 16\n\t"
      "s_lshl2_add_u32 s34, s39, s34\n\t"
      "s_add_u32 s34, s34, 4096\n\t"
      "s_add_u32 s42, s20, s34\n\t"
      "s_addc_u32 s43, s21, 0\n\t"
      "s_add_u32 s44, s42, 8192\n\t"
      "s_addc_u32 s45, s43, 0\n\t"
      "s_lshl_b32 s72, s24, 10\n\t"
      "s_add_u32 s72, s72, 98304\n\t"
      "v_xor_b32_e32 v203, v197, v198\n\t"
      "v_lshl_add_u32 v203, v203, 4, s72\n\t"
      "v_lshl_add_u32 v203, v197, 7, v203\n\t"
      "ds_write_b128 v203, v[204:207]\n\t"
      "s_and_b32 s35, s25, 3\n\t"
      "s_lshl2_add_u32 s31, s26, s35\n\t"
      "s_and_b32 s35, s2, 7\n\t"
      "s_mul_i32 s34, s35, 14\n\t"
      "s_lshl4_add_u32 s35, s35, -14\n\t"
      "s_cmp_lt_u32 s31, 14\n\t"
      "s_cselect_b32 s34, s34, s35\n\t"
      "s_cselect_b32 s36, 1, 0\n\t"
      "s_or_b32 s27, s27, s36\n\t"
      "s_add_u32 s34, s34, s31\n\t"
      "s_lshr_b32 s29, s34, 2\n\t"
      "s_and_b32 s32, s34, 3\n\t"
      "s_mul_i32 s30, s32, 7\n\t"
      "v_and_b32_e32 v199, 60, v192\n\t"
      "v_and_b32_e32 v200, 24, v192\n\t"
      "v_lshlrev_b32_e32 v202, 3, v196\n\t"
      "v_lshl_or_b32 v202, v200, 2, v202\n\t"
      "v_lshl_or_b32 v202, v195, 7, v202\n\t"
      "v_lshl_or_b32 v202, v193, 10, v202\n\t"
      "v_add_u32_e32 v224, s39, v202\n\t"
      "v_xor_b32_e32 v225, 32, v224\n\t"
      "v_xor_b32_e32 v226, 64, v224\n\t"
      "v_xor_b32_e32 v227, 96, v224\n\t"
      "s_lshl_b32 s34, s24, 12\n\t"
      "s_add_u32 s34, s34, 98304\n\t"
      "v_add_u32_e32 v245, s34, v244\n\t"
      "v_lshlrev_b32_e32 v199, 2, v199\n\t"
      "s_lshl_b32 s34, s24, 8\n\t"
      "v_lshl_add_u32 v247, v192, 2, s34\n\t"
      "v_and_b32_e32 v200, 2, v192\n\t"
      "v_mul_u32_u24_e32 v202, 576, v200\n\t"
      "v_xor_b32_e32 v199, v202, v199\n\t"
      "v_and_b32_e32 v202, 1, v192\n\t"
      "v_lshl_or_b32 v199, v202, 3, v199\n\t"
      "s_and_b32 s35, s34, 0x400\n\t"
      "s_add_u32 s34, s34, s35\n\t"
      "s_add_u32 s34, s34, 98304\n\t"
      "v_add_u32_e32 v246, s34, v199\n\t"
      "v_lshlrev_b32_e32 v199, 2, v202\n\t"
      "v_lshl_or_b32 v199, v200, 4, v199\n\t"
      "s_waitcnt lgkmcnt(0)\n\t"
      "s_barrier\n\t"
      "v_xor_b32_e32 v210, 32, v228\n\t"
      "v_xor_b32_e32 v212, 64, v228\n\t"
      "v_xor_b32_e32 v214, 96, v228\n\t"
      "ds_read_b64 v[208:209], v228\n\t"
      "ds_read_b64 v[210:211], v210\n\t"
      "ds_read_b64 v[212:213], v212\n\t"
      "ds_read_b64 v[214:215], v214\n\t"
      "ds_read_b64_tr_b16 v[0:1], v216 offset:7168\n\t"
      "ds_read_b64_tr_b16 v[2:3], v220 offset:7168\n\t"
      "ds_read_b64_tr_b16 v[4:5], v217 offset:7168\n\t"
      "ds_read_b64_tr_b16 v[6:7], v221 offset:7168\n\t"
      "ds_read_b64_tr_b16 v[8:9], v218 offset:7168\n\t"
      "ds_read_b64_tr_b16 v[10:11], v222 offset:7168\n\t"
      "ds_read_b64_tr_b16 v[12:13], v219 offset:7168\n\t"
      "ds_read_b64_tr_b16 v[14:15], v223 offset:7168\n\t"
      "ds_read_b64_tr_b16 v[16:17], v218 offset:10752\n\t"
      "ds_read_b64_tr_b16 v[18:19], v222 offset:10752\n\t"
      "ds_read_b64_tr_b16 v[20:21], v219 offset:10752\n\t"
      "ds_read_b64_tr_b16 v[22:23], v223 offset:10752\n\t"
      "ds_read_b64_tr_b16 v[24:25], v216 offset:10752\n\t"
      "ds_read_b64_tr_b16 v[26:27], v220 offset:10752\n\t"
      "ds_read_b64_tr_b16 v[28:29], v217 offset:10752\n\t"
      "ds_read_b64_tr_b16 v[30:31], v221 offset:10752\n\t"
      "ds_read_b64_tr_b16 v[32:33], v216 offset:14336\n\t"
      "ds_read_b64_tr_b16 v[34:35], v220 offset:14336\n\t"
      "ds_read_b64_tr_b16 v[36:37], v217 offset:14336\n\t"
      "ds_read_b64_tr_b16 v[38:39], v221 offset:14336\n\t"
      "ds_read_b64_tr_b16 v[40:41], v218 offset:14336\n\t"
      "ds_read_b64_tr_b16 v[42:43], v222 offset:14336\n\t"
      "ds_read_b64_tr_b16 v[44:45], v219 offset:14336\n\t"
      "ds_read_b64_tr_b16 v[46:47], v223 offset:14336\n\t"
      "ds_read_b64_tr_b16 v[48:49], v218 offset:17920\n\t"
      "ds_read_b64_tr_b16 v[50:51], v222 offset:17920\n\t"
      "ds_read_b64_tr_b16 v[52:53], v219 offset:17920\n\t"
      "ds_read_b64_tr_b16 v[54:55], v223 offset:17920\n\t"
      "ds_read_b64_tr_b16 v[56:57], v216 offset:17920\n\t"
      "ds_read_b64_tr_b16 v[58:59], v220 offset:17920\n\t"
      "ds_read_b64_tr_b16 v[60:61], v217 offset:17920\n\t"
      "ds_read_b64_tr_b16 v[62:63], v221 offset:17920\n\t"
      "ds_read_b64_tr_b16 v[64:65], v216 offset:21504\n\t"
      "ds_read_b64_tr_b16 v[66:67], v220 offset:21504\n\t"
      "ds_read_b64_tr_b16 v[68:69], v217 offset:21504\n\t"
      "ds_read_b64_tr_b16 v[70:71], v221 offset:21504\n\t"
      "ds_read_b64_tr_b16 v[72:73], v218 offset:21504\n\t"
      "ds_read_b64_tr_b16 v[74:75], v222 offset:21504\n\t"
      "ds_read_b64_tr_b16 v[76:77], v219 offset:21504\n\t"
      "ds_read_b64_tr_b16 v[78:79], v223 offset:21504\n\t"
      "ds_read_b64_tr_b16 v[80:81], v218 offset:25088\n\t"
      "ds_read_b64_tr_b16 v[82:83], v222 offset:25088\n\t"
      "ds_read_b64_tr_b16 v[84:85], v219 offset:25088\n\t"
      "ds_read_b64_tr_b16 v[86:87], v223 offset:25088\n\t"
      "ds_read_b64_tr_b16 v[88:89], v216 offset:25088\n\t"
      "ds_read_b64_tr_b16 v[90:91], v220 offset:25088\n\t"
      "ds_read_b64_tr_b16 v[92:93], v217 offset:25088\n\t"
      "ds_read_b64_tr_b16 v[94:95], v221 offset:25088\n\t"
      "ds_read_b64_tr_b16 v[96:97], v216 offset:28672\n\t"
      "ds_read_b64_tr_b16 v[98:99], v220 offset:28672\n\t"
      "ds_read_b64_tr_b16 v[100:101], v217 offset:28672\n\t"
      "ds_read_b64_tr_b16 v[102:103], v221 offset:28672\n\t"
      "ds_read_b64_tr_b16 v[104:105], v218 offset:28672\n\t"
      "ds_read_b64_tr_b16 v[106:107], v222 offset:28672\n\t"
      "ds_read_b64_tr_b16 v[108:109], v219 offset:28672\n\t"
      "ds_read_b64_tr_b16 v[110:111], v223 offset:28672\n\t"
      "ds_read_b64_tr_b16 v[112:113], v218 offset:32256\n\t"
      "ds_read_b64_tr_b16 v[114:115], v222 offset:32256\n\t"
      "ds_read_b64_tr_b16 v[116:117], v219 offset:32256\n\t"
      "ds_read_b64_tr_b16 v[118:119], v223 offset:32256\n\t"
      "ds_read_b64_tr_b16 v[120:121], v216 offset:32256\n\t"
      "ds_read_b64_tr_b16 v[122:123], v220 offset:32256\n\t"
      "ds_read_b64_tr_b16 v[124:125], v217 offset:32256\n\t"
      "ds_read_b64_tr_b16 v[126:127], v221 offset:32256\n\t"
      "s_sleep 2\n\t"
      "global_load_dwordx4 v[128:131], v244, s[42:43] offset:-2048 sc1\n\t"
      "global_load_dwordx4 v[132:135], v244, s[42:43] offset:-1024 sc1\n\t"
      "global_load_dwordx4 v[136:139], v244, s[42:43] sc1\n\t"
      "global_load_dwordx4 v[140:143], v244, s[42:43] offset:1024 sc1\n\t"
      "global_load_dwordx4 v[144:147], v244, s[42:43] offset:2048 sc1\n\t"
      "global_load_dwordx4 v[148:151], v244, s[42:43] offset:3072 sc1\n\t"
      "global_load_dwordx4 v[152:155], v244, s[44:45] offset:-4096 sc1\n\t"
      "global_load_dwordx4 v[156:159], v244, s[44:45] offset:-3072 sc1\n\t"
      "global_load_dwordx4 v[160:163], v244, s[44:45] offset:-2048 sc1\n\t"
      "global_load_dwordx4 v[164:167], v244, s[44:45] offset:-1024 sc1\n\t"
      "s_min_u32 s34, s29, 27\n\t"
      "s_lshl_b32 s34, s34, 7\n\t"
      "s_lshr_b32 s35, s24, 2\n\t"
      "s_lshl_b32 s35, s35, 6\n\t"
      "s_add_u32 s34, s34, s35\n\t"
      "s_lshr_b32 s35, s39, 9\n\t"
      "s_add_u32 s34, s34, s35\n\t"
      "v_add_u32_e32 v248, s34, v199\n\t"
      "s_add_u32 s48, s20, 8388608\n\t"
      "s_addc_u32 s49, s21, 0\n\t"
      "v_and_b32_e32 v199, 16, v192\n\t"
      "v_lshlrev_b32_e32 v199, 1, v199\n\t"
      "v_and_b32_e32 v200, 32, v192\n\t"
      "s_lshl_b32 s34, s28, 18\n\t"
      "s_lshl_b32 s35, s25, 11\n\t"
      "s_add_u32 s34, s34, s35\n\t"
      "s_lshr_b32 s35, s39, 3\n\t"
      "s_add_u32 s34, s34, s35\n\t"
      "v_and_b32_e32 v201, 0x1f0, v244\n\t"
      "v_lshl_or_b32 v201, v200, 11, v201\n\t"
      "v_add_u32_e32 v229, s34, v201\n\t"
      "v_add_u32_e32 v230, 131072, v229\n\t"
      "s_and_b32 s34, s25, 30\n\t"
      "s_lshr_b32 s35, s39, 13\n\t"
      "s_add_u32 s34, s34, s35\n\t"
      "s_mul_i32 s34, s34, 7\n\t"
      "s_add_u32 s34, s34, s28\n\t"
      "s_lshl_b32 s34, s34, 3\n\t"
      "s_and_b32 s35, s25, 1\n\t"
      "s_add_u32 s34, s34, s35\n\t"
      "s_lshl_b32 s34, s34, 10\n\t"
      "s_and_b32 s35, s24, 1\n\t"
      "s_lshl_b32 s35, s35, 9\n\t"
      "s_add_u32 s34, s34, s35\n\t"
      "s_add_u32 s34, s34, 6293504\n\t"
      "v_lshrrev_b32_e32 v201, 1, v200\n\t"
      "v_lshl_or_b32 v201, v199, 6, v201\n\t"
      "v_lshl_or_b32 v201, v194, 5, v201\n\t"
      "v_add_u32_e32 v241, s34, v201\n\t"
      "s_lshl_b32 s34, s24, 12\n\t"
      "s_add_u32 s34, s34, 131072\n\t"
      "v_xor_b32_e32 v201, v193, v194\n\t"
      "v_lshlrev_b32_e32 v201, 3, v201\n\t"
      "v_lshl_or_b32 v201, v194, 7, v201\n\t"
      "v_add_u32_e32 v233, s34, v201\n\t"
      "v_lshrrev_b32_e32 v199, 2, v199\n\t"
      "v_or_b32_e32 v199, v199, v195\n\t"
      "v_lshrrev_b32_e32 v200, 3, v200\n\t"
      "v_or_b32_e32 v200, v196, v200\n\t"
      "v_xor_b32_e32 v200, v200, v199\n\t"
      "v_lshlrev_b32_e32 v200, 3, v200\n\t"
      "v_lshl_or_b32 v200, v199, 7, v200\n\t"
      "v_add_u32_e32 v237, s34, v200\n\t"
      "v_xor_b32_e32 v234, 32, v233\n\t"
      "v_xor_b32_e32 v238, 32, v237\n\t"
      "v_xor_b32_e32 v235, 64, v233\n\t"
      "v_xor_b32_e32 v239, 64, v237\n\t"
      "v_xor_b32_e32 v236, 96, v233\n\t"
      "v_xor_b32_e32 v240, 96, v237\n\t"
      "s_waitcnt lgkmcnt(14)\n\t"
      "v_lshlrev_b32_e32 v192, 16, v208\n\t"
      "v_and_b32_e32 v193, 0xffff0000, v208\n\t"
      "v_lshlrev_b32_e32 v194, 16, v209\n\t"
      "v_and_b32_e32 v195, 0xffff0000, v209\n\t"
      "v_lshlrev_b32_e32 v196, 16, v210\n\t"
      "v_and_b32_e32 v197, 0xffff0000, v210\n\t"
      "v_lshlrev_b32_e32 v198, 16, v211\n\t"
      "v_and_b32_e32 v199, 0xffff0000, v211\n\t"
      "v_lshlrev_b32_e32 v200, 16, v212\n\t"
      "v_and_b32_e32 v201, 0xffff0000, v212\n\t"
      "v_lshlrev_b32_e32 v202, 16, v213\n\t"
      "v_and_b32_e32 v203, 0xffff0000, v213\n\t"
      "v_lshlrev_b32_e32 v204, 16, v214\n\t"
      "v_and_b32_e32 v205, 0xffff0000, v214\n\t"
      "v_lshlrev_b32_e32 v206, 16, v215\n\t"
      "v_and_b32_e32 v207, 0xffff0000, v215\n\t"
      "global_load_dwordx4 v[168:171], v244, s[44:45] sc1\n\t"
      "global_load_dwordx4 v[172:175], v244, s[44:45] offset:1024 sc1\n\t"
      "global_load_dwordx4 v[176:179], v244, s[44:45] offset:2048 sc1\n\t"
      "global_load_dwordx4 v[180:183], v244, s[44:45] offset:3072 sc1\n\t"
      "global_load_dwordx4 v[184:187], v244, s[42:43] offset:-4096 sc1\n\t"
      "global_load_dwordx4 v[188:191], v244, s[42:43] offset:-3072 sc1\n\t"
      "global_load_dword v249, v248, s[12:13]\n\t"
      "s_barrier\n\t"
      "s_waitcnt lgkmcnt(0)\n\t"
      "LGM_%=:\n\t"
      "s_waitcnt vmcnt(15)\n\t"
      "v_max_u32_e32 v232, v128, v132\n\t"
      "v_cmp_eq_u32_e32 vcc, -1, v232\n\t"
      "s_cbranch_vccnz LO0R_%=\n\t"
      "LO0B_%=:\n\t"
      "v_mfma_f32_16x16x32_bf16 v[192:195], v[0:3], v[128:131], v[192:195]\n\t"
      "ds_read_b64_tr_b16 v[0:1], v216 offset:35840\n\t"
      "ds_read_b64_tr_b16 v[2:3], v220 offset:35840\n\t"
      "v_mfma_f32_16x16x32_bf16 v[196:199], v[4:7], v[128:131], v[196:199]\n\t"
      "ds_read_b64_tr_b16 v[4:5], v217 offset:35840\n\t"
      "v_mfma_f32_16x16x32_bf16 v[200:203], v[8:11], v[128:131], v[200:203]\n\t"
      "ds_read_b64_tr_b16 v[6:7], v221 offset:35840\n\t"
      "v_mfma_f32_16x16x32_bf16 v[204:207], v[12:15], v[128:131], v[204:207]\n\t"
      "ds_read_b64_tr_b16 v[8:9], v218 offset:35840\n\t"
      "ds_read_b64_tr_b16 v[10:11], v222 offset:35840\n\t"
      "v_mfma_f32_16x16x32_bf16 v[192:195], v[16:19], v[132:135], v[192:195]\n\t"
      "ds_read_b64_tr_b16 v[12:13], v219 offset:35840\n\t"
      "v_mfma_f32_16x16x32_bf16 v[196:199], v[20:23], v[132:135], v[196:199]\n\t"
      "ds_read_b64_tr_b16 v[14:15], v223 offset:35840\n\t"
      "v_mfma_f32_16x16x32_bf16 v[200:203], v[24:27], v[132:135], v[200:203]\n\t"
      "ds_read_b64_tr_b16 v[16:17], v218 offset:39424\n\t"
      "ds_read_b64_tr_b16 v[18:19], v222 offset:39424\n\t"
      "v_mfma_f32_16x16x32_bf16 v[204:207], v[28:31], v[132:135], v[204:207]\n\t"
      "ds_read_b64_tr_b16 v[20:21], v219 offset:39424\n\t"
      "s_waitcnt vmcnt(13)\n\t"
      "v_max_u32_e32 v232, v136, v140\n\t"
      "v_cmp_eq_u32_e32 vcc, -1, v232\n\t"
      "s_cbranch_vccnz LO2R_%=\n\t"
      "LO2B_%=:\n\t"
      "v_mfma_f32_16x16x32_bf16 v[192:195], v[32:35], v[136:139], v[192:195]\n\t"
      "ds_read_b64_tr_b16 v[22:23], v223 offset:39424\n\t"
      "v_mfma_f32_16x16x32_bf16 v[196:199], v[36:39], v[136:139], v[196:199]\n\t"
      "ds_read_b64_tr_b16 v[24:25], v216 offset:39424\n\t"
      "ds_read_b64_tr_b16 v[26:27], v220 offset:39424\n\t"
      "v_mfma_f32_16x16x32_bf16 v[200:203], v[40:43], v[136:139], v[200:203]\n\t"
      "ds_read_b64_tr_b16 v[28:29], v217 offset:39424\n\t"
      "v_mfma_f32_16x16x32_bf16 v[204:207], v[44:47], v[136:139], v[204:207]\n\t"
      "ds_read_b64_tr_b16 v[30:31], v221 offset:39424\n\t"
      "v_mfma_f32_16x16x32_bf16 v[192:195], v[48:51], v[140:143], v[192:195]\n\t"
      "ds_read_b64_tr_b16 v[32:33], v216 offset:43008\n\t"
      "ds_read_b64_tr_b16 v[34:35], v220 offset:43008\n\t"
      "v_mfma_f32_16x16x32_bf16 v[196:199], v[52:55], v[140:143], v[196:199]\n\t"
      "ds_read_b64_tr_b16 v[36:37], v217 offset:43008\n\t"
      "v_mfma_f32_16x16x32_bf16 v[200:203], v[56:59], v[140:143], v[200:203]\n\t"
      "ds_read_b64_tr_b16 v[38:39], v221 offset:43008\n\t"
      "v_mfma_f32_16x16x32_bf16 v[204:207], v[60:63], v[140:143], v[204:207]\n\t"
      "ds_read_b64_tr_b16 v[40:41], v218 offset:43008\n\t"
      "ds_read_b64_tr_b16 v[42:43], v222 offset:43008\n\t"
      "s_waitcnt vmcnt(11)\n\t"
      "v_max_u32_e32 v232, v144, v148\n\t"
      "v_cmp_eq_u32_e32 vcc, -1, v232\n\t"
      "s_cbranch_vccnz LO4R_%=\n\t"
      "LO4B_%=:\n\t"
      "v_mfma_f32_16x16x32_bf16 v[192:195], v[64:67], v[144:147], v[192:195]\n\t"
      "ds_read_b64_tr_b16 v[44:45], v219 offset:43008\n\t"
      "v_mfma_f32_16x16x32_bf16 v[196:199], v[68:71], v[144:147], v[196:199]\n\t"
      "ds_read_b64_tr_b16 v[46:47], v223 offset:43008\n\t"
      "v_mfma_f32_16x16x32_bf16 v[200:203], v[72:75], v[144:147], v[200:203]\n\t"
      "ds_read_b64_tr_b16 v[48:49], v218 offset:46592\n\t"
      "ds_read_b64_tr_b16 v[50:51], v222 offset:46592\n\t"
      "v_mfma_f32_16x16x32_bf16 v[204:207], v[76:79], v[144:147], v[204:207]\n\t"
      "ds_read_b64_tr_b16 v[52:53], v219 offset:46592\n\t"
      "v_mfma_f32_16x16x32_bf16 v[192:195], v[80:83], v[148:151], v[192:195]\n\t"
      "ds_read_b64_tr_b16 v[54:55], v223 offset:46592\n\t"
      "v_mfma_f32_16x16x32_bf16 v[196:199], v[84:87], v[148:151], v[196:199]\n\t"
      "ds_read_b64_tr_b16 v[56:57], v216 offset:46592\n\t"
      "ds_read_b64_tr_b16 v[58:59], v220 offset:46592\n\t"
      "v_mfma_f32_16x16x32_bf16 v[200:203], v[88:91], v[148:151], v[200:203]\n\t"
      "ds_read_b64_tr_b16 v[60:61], v217 offset:46592\n\t"
      "v_mfma_f32_16x16x32_bf16 v[204:207], v[92:95], v[148:151], v[204:207]\n\t"
      "ds_read_b64_tr_b16 v[62:63], v221 offset:46592\n\t"
      "s_waitcnt vmcnt(9)\n\t"
      "v_max_u32_e32 v232, v152, v156\n\t"
      "v_cmp_eq_u32_e32 vcc, -1, v232\n\t"
      "s_cbranch_vccnz LO6R_%=\n\t"
      "LO6B_%=:\n\t"
      "v_mfma_f32_16x16x32_bf16 v[192:195], v[96:99], v[152:155], v[192:195]\n\t"
      "ds_read_b64_tr_b16 v[64:65], v216 offset:50176\n\t"
      "ds_read_b64_tr_b16 v[66:67], v220 offset:50176\n\t"
      "v_mfma_f32_16x16x32_bf16 v[196:199], v[100:103], v[152:155], v[196:199]\n\t"
      "ds_read_b64_tr_b16 v[68:69], v217 offset:50176\n\t"
      "v_mfma_f32_16x16x32_bf16 v[200:203], v[104:107], v[152:155], v[200:203]\n\t"
      "ds_read_b64_tr_b16 v[70:71], v221 offset:50176\n\t"
      "v_mfma_f32_16x16x32_bf16 v[204:207], v[108:111], v[152:155], v[204:207]\n\t"
      "ds_read_b64_tr_b16 v[72:73], v218 offset:50176\n\t"
      "ds_read_b64_tr_b16 v[74:75], v222 offset:50176\n\t"
      "v_mfma_f32_16x16x32_bf16 v[192:195], v[112:115], v[156:159], v[192:195]\n\t"
      "ds_read_b64_tr_b16 v[76:77], v219 offset:50176\n\t"
      "v_mfma_f32_16x16x32_bf16 v[196:199], v[116:119], v[156:159], v[196:199]\n\t"
      "ds_read_b64_tr_b16 v[78:79], v223 offset:50176\n\t"
      "v_mfma_f32_16x16x32_bf16 v[200:203], v[120:123], v[156:159], v[200:203]\n\t"
      "ds_read_b64_tr_b16 v[80:81], v218 offset:53760\n\t"
      "ds_read_b64_tr_b16 v[82:83], v222 offset:53760\n\t"
      "v_mfma_f32_16x16x32_bf16 v[204:207], v[124:127], v[156:159], v[204:207]\n\t"
      "ds_read_b64_tr_b16 v[84:85], v219 offset:53760\n\t"
      "s_waitcnt vmcnt(7)\n\t"
      "v_max_u32_e32 v232, v160, v164\n\t"
      "v_cmp_eq_u32_e32 vcc, -1, v232\n\t"
      "s_cbranch_vccnz LO8R_%=\n\t"
      "LO8B_%=:\n\t"
      "s_waitcnt lgkmcnt(14)\n\t"
      "v_mfma_f32_16x16x32_bf16 v[192:195], v[0:3], v[160:163], v[192:195]\n\t"
      "ds_read_b64_tr_b16 v[86:87], v223 offset:53760\n\t"
      "v_mfma_f32_16x16x32_bf16 v[196:199], v[4:7], v[160:163], v[196:199]\n\t"
      "ds_read_b64_tr_b16 v[88:89], v216 offset:53760\n\t"
      "ds_read_b64_tr_b16 v[90:91], v220 offset:53760\n\t"
      "v_mfma_f32_16x16x32_bf16 v[200:203], v[8:11], v[160:163], v[200:203]\n\t"
      "ds_read_b64_tr_b16 v[92:93], v217 offset:53760\n\t"
      "v_mfma_f32_16x16x32_bf16 v[204:207], v[12:15], v[160:163], v[204:207]\n\t"
      "ds_read_b64_tr_b16 v[94:95], v221 offset:53760\n\t"
      "v_mfma_f32_16x16x32_bf16 v[192:195], v[16:19], v[164:167], v[192:195]\n\t"
      "ds_read_b64_tr_b16 v[96:97], v216\n\t"
      "ds_read_b64_tr_b16 v[98:99], v220\n\t"
      "v_mfma_f32_16x16x32_bf16 v[196:199], v[20:23], v[164:167], v[196:199]\n\t"
      "ds_read_b64_tr_b16 v[100:101], v217\n\t"
      "v_mfma_f32_16x16x32_bf16 v[200:203], v[24:27], v[164:167], v[200:203]\n\t"
      "ds_read_b64_tr_b16 v[102:103], v221\n\t"
      "v_mfma_f32_16x16x32_bf16 v[204:207], v[28:31], v[164:167], v[204:207]\n\t"
      "ds_read_b64_tr_b16 v[104:105], v218\n\t"
      "ds_read_b64_tr_b16 v[106:107], v222\n\t"
      "s_waitcnt vmcnt(5)\n\t"
      "v_max_u32_e32 v232, v168, v172\n\t"
      "v_cmp_eq_u32_e32 vcc, -1, v232\n\t"
      "s_cbranch_vccnz LO10R_%=\n\t"
      "LO10B_%=:\n\t"
      "v_mfma_f32_16x16x32_bf16 v[192:195], v[32:35], v[168:171], v[192:195]\n\t"
      "ds_read_b64_tr_b16 v[108:109], v219\n\t"
      "v_mfma_f32_16x16x32_bf16 v[196:199], v[36:39], v[168:171], v[196:199]\n\t"
      "ds_read_b64_tr_b16 v[110:111], v223\n\t"
      "v_mfma_f32_16x16x32_bf16 v[200:203], v[40:43], v[168:171], v[200:203]\n\t"
      "ds_read_b64_tr_b16 v[112:113], v218 offset:3584\n\t"
      "ds_read_b64_tr_b16 v[114:115], v222 offset:3584\n\t"
      "v_mfma_f32_16x16x32_bf16 v[204:207], v[44:47], v[168:171], v[204:207]\n\t"
      "ds_read_b64_tr_b16 v[116:117], v219 offset:3584\n\t"
      "s_waitcnt lgkmcnt(14)\n\t"
      "v_mfma_f32_16x16x32_bf16 v[192:195], v[48:51], v[172:175], v[192:195]\n\t"
      "ds_read_b64_tr_b16 v[118:119], v223 offset:3584\n\t"
      "v_mfma_f32_16x16x32_bf16 v[196:199], v[52:55], v[172:175], v[196:199]\n\t"
      "ds_read_b64_tr_b16 v[120:121], v216 offset:3584\n\t"
      "ds_read_b64_tr_b16 v[122:123], v220 offset:3584\n\t"
      "v_mfma_f32_16x16x32_bf16 v[200:203], v[56:59], v[172:175], v[200:203]\n\t"
      "ds_read_b64_tr_b16 v[124:125], v217 offset:3584\n\t"
      "v_mfma_f32_16x16x32_bf16 v[204:207], v[60:63], v[172:175], v[204:207]\n\t"
      "ds_read_b64_tr_b16 v[126:127], v221 offset:3584\n\t"
      "s_waitcnt vmcnt(3)\n\t"
      "v_max_u32_e32 v232, v176, v180\n\t"
      "v_cmp_eq_u32_e32 vcc, -1, v232\n\t"
      "s_cbranch_vccnz LO12R_%=\n\t"
      "LO12B_%=:\n\t"
      "v_mfma_f32_16x16x32_bf16 v[192:195], v[64:67], v[176:179], v[192:195]\n\t"
      "v_mfma_f32_16x16x32_bf16 v[196:199], v[68:71], v[176:179], v[196:199]\n\t"
      "v_mfma_f32_16x16x32_bf16 v[200:203], v[72:75], v[176:179], v[200:203]\n\t"
      "v_mfma_f32_16x16x32_bf16 v[204:207], v[76:79], v[176:179], v[204:207]\n\t"
      "s_waitcnt lgkmcnt(14)\n\t"
      "v_mfma_f32_16x16x32_bf16 v[192:195], v[80:83], v[180:183], v[192:195]\n\t"
      "v_mfma_f32_16x16x32_bf16 v[196:199], v[84:87], v[180:183], v[196:199]\n\t"
      "v_mfma_f32_16x16x32_bf16 v[200:203], v[88:91], v[180:183], v[200:203]\n\t"
      "v_mfma_f32_16x16x32_bf16 v[204:207], v[92:95], v[180:183], v[204:207]\n\t"
      "s_waitcnt vmcnt(1)\n\t"
      "v_max_u32_e32 v232, v184, v188\n\t"
      "v_cmp_eq_u32_e32 vcc, -1, v232\n\t"
      "s_cbranch_vccnz LO14R_%=\n\t"
      "LO14B_%=:\n\t"
      "s_waitcnt lgkmcnt(8)\n\t"
      "v_mfma_f32_16x16x32_bf16 v[192:195], v[96:99], v[184:187], v[192:195]\n\t"
      "v_mfma_f32_16x16x32_bf16 v[196:199], v[100:103], v[184:187], v[196:199]\n\t"
      "v_mfma_f32_16x16x32_bf16 v[200:203], v[104:107], v[184:187], v[200:203]\n\t"
      "v_mfma_f32_16x16x32_bf16 v[204:207], v[108:111], v[184:187], v[204:207]\n\t"
      "s_waitcnt lgkmcnt(0)\n\t"
      "v_mfma_f32_16x16x32_bf16 v[192:195], v[112:115], v[188:191], v[192:195]\n\t"
      "v_mfma_f32_16x16x32_bf16 v[196:199], v[116:119], v[188:191], v[196:199]\n\t"
      "v_mfma_f32_16x16x32_bf16 v[200:203], v[120:123], v[188:191], v[200:203]\n\t"
      "v_mfma_f32_16x16x32_bf16 v[204:207], v[124:127], v[188:191], v[204:207]\n\t"
      "s_nop 4\n\t"
      "v_cmp_u_f32_e32 vcc, v192, v192\n\t"
      "s_cbranch_vccnz LGX_%=\n\t"
      "LGB_%=:\n\t"
      "v_cvt_pk_bf16_f32 v208, v192, v193\n\t"
      "v_cvt_pk_bf16_f32 v209, v194, v195\n\t"
      "v_cvt_pk_bf16_f32 v210, v196, v197\n\t"
      "v_cvt_pk_bf16_f32 v211, v198, v199\n\t"
      "v_cvt_pk_bf16_f32 v212, v200, v201\n\t"
      "v_cvt_pk_bf16_f32 v213, v202, v203\n\t"
      "v_cvt_pk_bf16_f32 v214, v204, v205\n\t"
      "v_cvt_pk_bf16_f32 v215, v206, v207\n\t"
      "v_mov_b32_e32 v148, 0x3f803f80\n\t"
      "v_dot2_f32_bf16 v144, v208, v148, 0\n\t"
      "v_dot2_f32_bf16 v145, v208, v208, 0\n\t"
      "v_dot2_f32_bf16 v146, v209, v148, 0\n\t"
      "v_dot2_f32_bf16 v147, v209, v209, 0\n\t"
      "v_dot2_f32_bf16 v144, v210, v148, v144\n\t"
      "v_dot2_f32_bf16 v145, v210, v210, v145\n\t"
      "v_dot2_f32_bf16 v146, v211, v148, v146\n\t"
      "v_dot2_f32_bf16 v147, v211, v211, v147\n\t"
      "v_dot2_f32_bf16 v144, v212, v148, v144\n\t"
      "v_dot2_f32_bf16 v145, v212, v212, v145\n\t"
      "v_dot2_f32_bf16 v146, v213, v148, v146\n\t"
      "v_dot2_f32_bf16 v147, v213, v213, v147\n\t"
      "v_dot2_f32_bf16 v144, v214, v148, v144\n\t"
      "v_dot2_f32_bf16 v145, v214, v214, v145\n\t"
      "v_dot2_f32_bf16 v146, v215, v148, v146\n\t"
      "v_dot2_f32_bf16 v147, v215, v215, v147\n\t"
      "v_lshlrev_b32_e32 v128, 16, v208\n\t"
      "v_and_b32_e32 v129, 0xffff0000, v208\n\t"
      "v_lshlrev_b32_e32 v130, 16, v209\n\t"
      "v_pk_add_f32 v[144:145], v[144:145], v[146:147]\n\t"
      "v_and_b32_e32 v131, 0xffff0000, v209\n\t"
      "v_lshlrev_b32_e32 v132, 16, v210\n\t"
      "v_permlane16_swap_b32_e32 v144, v145\n\t"
      "v_add_f32_e32 v144, v144, v145\n\t"
      "v_mov_b32_e32 v145, v144\n\t"
      "v_and_b32_e32 v133, 0xffff0000, v210\n\t"
      "v_lshlrev_b32_e32 v134, 16, v211\n\t"
      "v_permlane32_swap_b32_e32 v144, v145\n\t"
      "v_add_f32_e32 v144, v144, v145\n\t"
      "v_mov_b32_e32 v145, v144\n\t"
      "v_and_b32_e32 v135, 0xffff0000, v211\n\t"
      "v_lshlrev_b32_e32 v136, 16, v212\n\t"
      "v_permlane16_swap_b32_e32 v144, v145\n\t"
      "s_mov_b64 exec, s[70:71]\n\t"
      "v_min_u32_e32 v144, -2, v144\n\t"
      "v_min_u32_e32 v145, -2, v145\n\t"
      "global_store_dwordx2 v231, v[144:145], s[58:59] sc1\n\t"
      "s_mov_b64 exec, -1\n\t"
      "s_barrier\n\t"
      "v_and_b32_e32 v137, 0xffff0000, v212\n\t"
      "v_lshlrev_b32_e32 v138, 16, v213\n\t"
      "v_and_b32_e32 v139, 0xffff0000, v213\n\t"
      "v_lshlrev_b32_e32 v140, 16, v214\n\t"
      "v_and_b32_e32 v141, 0xffff0000, v214\n\t"
      "v_lshlrev_b32_e32 v142, 16, v215\n\t"
      "v_and_b32_e32 v143, 0xffff0000, v215\n\t"
      "v_permlane16_swap_b32_e32 v208, v210\n\t"
      "v_permlane16_swap_b32_e32 v209, v211\n\t"
      "v_permlane16_swap_b32_e32 v212, v214\n\t"
      "v_permlane16_swap_b32_e32 v213, v215\n\t"
      "s_bitcmp0_b32 s27, 0\n\t"
      "s_cbranch_scc1 NTK0_%=\n\t"
      "ds_read_b64_tr_b16 v[0:1], v224 offset:16384\n\t"
      "ds_read_b64_tr_b16 v[2:3], v224 offset:16896\n\t"
      "ds_read_b64_tr_b16 v[4:5], v225 offset:16384\n\t"
      "ds_read_b64_tr_b16 v[6:7], v225 offset:16896\n\t"
      "ds_read_b64_tr_b16 v[8:9], v226 offset:16384\n\t"
      "ds_read_b64_tr_b16 v[10:11], v226 offset:16896\n\t"
      "ds_read_b64_tr_b16 v[12:13], v227 offset:16384\n\t"
      "ds_read_b64_tr_b16 v[14:15], v227 offset:16896\n\t"
      "ds_read_b64_tr_b16 v[16:17], v224 offset:49152\n\t"
      "ds_read_b64_tr_b16 v[18:19], v224 offset:49664\n\t"
      "ds_read_b64_tr_b16 v[20:21], v225 offset:49152\n\t"
      "ds_read_b64_tr_b16 v[22:23], v225 offset:49664\n\t"
      "ds_read_b64_tr_b16 v[24:25], v226 offset:49152\n\t"
      "ds_read_b64_tr_b16 v[26:27], v226 offset:49664\n\t"
      "ds_read_b64_tr_b16 v[28:29], v227 offset:49152\n\t"
      "ds_read_b64_tr_b16 v[30:31], v227 offset:49664\n\t"
      "ds_read_b64_tr_b16 v[32:33], v224\n\t"
      "ds_read_b64_tr_b16 v[34:35], v224 offset:512\n\t"
      "ds_read_b64_tr_b16 v[36:37], v225\n\t"
      "ds_read_b64_tr_b16 v[38:39], v225 offset:512\n\t"
      "ds_read_b64_tr_b16 v[40:41], v226\n\t"
      "ds_read_b64_tr_b16 v[42:43], v226 offset:512\n\t"
      "ds_read_b64_tr_b16 v[44:45], v227\n\t"
      "ds_read_b64_tr_b16 v[46:47], v227 offset:512\n\t"
      "ds_read_b64_tr_b16 v[48:49], v224 offset:32768\n\t"
      "ds_read_b64_tr_b16 v[50:51], v224 offset:33280\n\t"
      "ds_read_b64_tr_b16 v[52:53], v225 offset:32768\n\t"
      "ds_read_b64_tr_b16 v[54:55], v225 offset:33280\n\t"
      "ds_read_b64_tr_b16 v[56:57], v226 offset:32768\n\t"
      "ds_read_b64_tr_b16 v[58:59], v226 offset:33280\n\t"
      "ds_read_b64_tr_b16 v[60:61], v227 offset:32768\n\t"
      "ds_read_b64_tr_b16 v[62:63], v227 offset:33280\n\t"
      "NTK0_%=:\n\t"
      "s_waitcnt lgkmcnt(0)\n\t"
      "s_barrier\n\t"
      "global_load_dwordx2 v[148:149], v231, s[46:47] sc1\n\t"
      "global_load_dwordx2 v[150:151], v231, s[46:47] offset:384 sc1\n\t"
      "s_waitcnt vmcnt(0)\n\t"
      "v_max3_u32 v232, v148, v149, v150\n\t"
      "v_max_u32_e32 v232, v232, v151\n\t"
      "v_cmp_eq_u32_e32 vcc, -1, v232\n\t"
      "s_cbranch_vccnz LSR_%=\n\t"
      "LSB_%=:\n\t"
      "v_cndmask_b32_e64 v150, v150, 0, s[70:71]\n\t"
      "v_cndmask_b32_e64 v151, v151, 0, s[70:71]\n\t"
      "v_pk_add_f32 v[144:145], v[148:149], v[150:151]\n\t"
      "s_nop 1\n\t"
      "v_permlane16_swap_b32_e32 v144, v145\n\t"
      "v_add_f32_e32 v144, v144, v145\n\t"
      "v_mov_b32_e32 v145, v144\n\t"
      "s_nop 1\n\t"
      "v_permlane32_swap_b32_e32 v144, v145\n\t"
      "v_add_f32_e32 v144, v144, v145\n\t"
      "v_mov_b32_e32 v145, v144\n\t"
      "s_nop 1\n\t"
      "v_permlane16_swap_b32_e32 v144, v145\n\t"
      "v_mul_f32_e32 v144, 0xbb124925, v144\n\t"
      "v_mul_f32_e32 v145, 0x3b124925, v145\n\t"
      "v_fma_f32 v145, -v144, v144, v145\n\t"
      "v_max_f32_e32 v145, 0, v145\n\t"
      "v_add_f32_e32 v146, 0x3727c5ac, v145\n\t"
      "v_fmac_f32_e32 v145, 0x3727c5ac, v146\n\t"
      "v_rsq_f32_e32 v145, v145\n\t"
      "s_nop 0\n\t"
      "v_mul_f32_e32 v144, v144, v145\n\t"
      "v_pk_fma_f32 v[128:129], v[128:129], v[144:145], v[144:145] op_sel:[0,1,0] op_sel_hi:[1,1,0]\n\t"
      "v_pk_fma_f32 v[130:131], v[130:131], v[144:145], v[144:145] op_sel:[0,1,0] op_sel_hi:[1,1,0]\n\t"
      "v_cvt_pk_bf16_f32 v152, v128, v129\n\t"
      "v_cvt_pk_bf16_f32 v153, v130, v131\n\t"
      "ds_write_b64 v233, v[152:153]\n\t"
      "v_pk_fma_f32 v[132:133], v[132:133], v[144:145], v[144:145] op_sel:[0,1,0] op_sel_hi:[1,1,0]\n\t"
      "v_pk_fma_f32 v[134:135], v[134:135], v[144:145], v[144:145] op_sel:[0,1,0] op_sel_hi:[1,1,0]\n\t"
      "v_cvt_pk_bf16_f32 v154, v132, v133\n\t"
      "v_cvt_pk_bf16_f32 v155, v134, v135\n\t"
      "ds_write_b64 v234, v[154:155]\n\t"
      "ds_read_b64_tr_b16 v[160:161], v237\n\t"
      "ds_read_b64_tr_b16 v[162:163], v238 offset:512\n\t"
      "v_pk_fma_f32 v[136:137], v[136:137], v[144:145], v[144:145] op_sel:[0,1,0] op_sel_hi:[1,1,0]\n\t"
      "v_pk_fma_f32 v[138:139], v[138:139], v[144:145], v[144:145] op_sel:[0,1,0] op_sel_hi:[1,1,0]\n\t"
      "v_cvt_pk_bf16_f32 v156, v136, v137\n\t"
      "v_cvt_pk_bf16_f32 v157, v138, v139\n\t"
      "ds_write_b64 v235, v[156:157]\n\t"
      "v_pk_fma_f32 v[140:141], v[140:141], v[144:145], v[144:145] op_sel:[0,1,0] op_sel_hi:[1,1,0]\n\t"
      "v_pk_fma_f32 v[142:143], v[142:143], v[144:145], v[144:145] op_sel:[0,1,0] op_sel_hi:[1,1,0]\n\t"
      "v_cvt_pk_bf16_f32 v158, v140, v141\n\t"
      "v_cvt_pk_bf16_f32 v159, v142, v143\n\t"
      "ds_write_b64 v236, v[158:159]\n\t"
      "ds_read_b64_tr_b16 v[164:165], v239\n\t"
      "ds_read_b64_tr_b16 v[166:167], v240 offset:512\n\t"
      "s_waitcnt lgkmcnt(4)\n\t"
      "v_min_u32_e32 v160, -2, v160\n\t"
      "v_min_u32_e32 v161, -2, v161\n\t"
      "v_min_u32_e32 v162, -2, v162\n\t"
      "v_min_u32_e32 v163, -2, v163\n\t"
      "global_store_dwordx4 v229, v[160:163], s[48:49] sc1\n\t"
      "s_waitcnt lgkmcnt(0)\n\t"
      "v_min_u32_e32 v164, -2, v164\n\t"
      "v_min_u32_e32 v165, -2, v165\n\t"
      "v_min_u32_e32 v166, -2, v166\n\t"
      "v_min_u32_e32 v167, -2, v167\n\t"
      "global_store_dwordx4 v230, v[164:167], s[48:49] sc1\n\t"
      "s_barrier\n\t"
      "s_bitcmp0_b32 s27, 0\n\t"
      "s_cbranch_scc1 G4C_%=\n\t"
      "s_mul_i32 s34, s32, 196608\n\t"
      "s_mul_i32 s35, s24, 8192\n\t"
      "s_add_u32 s34, s34, s35\n\t"
      "s_add_u32 s34, s34, 1048576\n\t"
      "s_add_u32 s64, s48, s34\n\t"
      "s_addc_u32 s65, s49, 0\n\t"
      "v_add_u32_e32 v250, 4096, v244\n\t"
      "global_load_dwordx4 v[128:131], v250, s[64:65] offset:-4096 sc1\n\t"
      "global_load_dwordx4 v[132:135], v250, s[64:65] offset:-3072 sc1\n\t"
      "global_load_dwordx4 v[136:139], v250, s[64:65] offset:-2048 sc1\n\t"
      "global_load_dwordx4 v[140:143], v250, s[64:65] offset:-1024 sc1\n\t"
      "global_load_dwordx4 v[144:147], v250, s[64:65] sc1\n\t"
      "global_load_dwordx4 v[148:151], v250, s[64:65] offset:1024 sc1\n\t"
      "global_load_dwordx4 v[152:155], v250, s[64:65] offset:2048 sc1\n\t"
      "global_load_dwordx4 v[156:159], v250, s[64:65] offset:3072 sc1\n\t"
      "global_store_dwordx4 v241, v[208:211], s[20:21] offset:-2048 sc1\n\t"
      "global_store_dwordx4 v241, v[212:215], s[20:21] offset:2048 sc1\n\t"
      "s_setprio 0\n\t"
      "ds_read_b64_tr_b16 v[64:65], v224 offset:16384\n\t"
      "ds_read_b64_tr_b16 v[66:67], v224 offset:16896\n\t"
      "ds_read_b64_tr_b16 v[68:69], v225 offset:16384\n\t"
      "ds_read_b64_tr_b16 v[70:71], v225 offset:16896\n\t"
      "ds_read_b64_tr_b16 v[72:73], v226 offset:16384\n\t"
      "ds_read_b64_tr_b16 v[74:75], v226 offset:16896\n\t"
      "ds_read_b64_tr_b16 v[76:77], v227 offset:16384\n\t"
      "ds_read_b64_tr_b16 v[78:79], v227 offset:16896\n\t"
      "ds_read_b64_tr_b16 v[80:81], v224 offset:49152\n\t"
      "ds_read_b64_tr_b16 v[82:83], v224 offset:49664\n\t"
      "ds_read_b64_tr_b16 v[84:85], v225 offset:49152\n\t"
      "ds_read_b64_tr_b16 v[86:87], v225 offset:49664\n\t"
      "ds_read_b64_tr_b16 v[88:89], v226 offset:49152\n\t"
      "ds_read_b64_tr_b16 v[90:91], v226 offset:49664\n\t"
      "ds_read_b64_tr_b16 v[92:93], v227 offset:49152\n\t"
      "ds_read_b64_tr_b16 v[94:95], v227 offset:49664\n\t"
      "ds_read_b64_tr_b16 v[96:97], v224\n\t"
      "ds_read_b64_tr_b16 v[98:99], v224 offset:512\n\t"
      "ds_read_b64_tr_b16 v[100:101], v225\n\t"
      "ds_read_b64_tr_b16 v[102:103], v225 offset:512\n\t"
      "ds_read_b64_tr_b16 v[104:105], v226\n\t"
      "ds_read_b64_tr_b16 v[106:107], v226 offset:512\n\t"
      "ds_read_b64_tr_b16 v[108:109], v227\n\t"
      "ds_read_b64_tr_b16 v[110:111], v227 offset:512\n\t"
      "ds_read_b64_tr_b16 v[112:113], v224 offset:32768\n\t"
      "ds_read_b64_tr_b16 v[114:115], v224 offset:33280\n\t"
      "ds_read_b64_tr_b16 v[116:117], v225 offset:32768\n\t"
      "ds_read_b64_tr_b16 v[118:119], v225 offset:33280\n\t"
      "ds_read_b64_tr_b16 v[120:121], v226 offset:32768\n\t"
      "ds_read_b64_tr_b16 v[122:123], v226 offset:33280\n\t"
      "ds_read_b64_tr_b16 v[124:125], v227 offset:32768\n\t"
      "ds_read_b64_tr_b16 v[126:127], v227 offset:33280\n\t"
      "s_waitcnt vmcnt(2) lgkmcnt(0)\n\t"
      "GST_%=:\n\t"
      "s_add_u32 s66, s64, 65536\n\t"
      "s_addc_u32 s67, s65, 0\n\t"
      "s_add_u32 s68, s66, 65536\n\t"
      "s_addc_u32 s69, s67, 0\n\t"
      "s_lshl_b32 s34, s32, 18\n\t"
      "s_lshl_b32 s35, s24, 13\n\t"
      "s_add_u32 s34, s34, s35\n\t"
      "s_add_u32 s70, s48, s34\n\t"
      "s_addc_u32 s71, s49, 0\n\t"
      "s_add_u32 s72, s70, 65536\n\t"
      "s_addc_u32 s73, s71, 0\n\t"
      "s_add_u32 s74, s72, 65536\n\t"
      "s_addc_u32 s75, s73, 0\n\t"
      "s_add_u32 s76, s74, 65536\n\t"
      "s_addc_u32 s77, s75, 0\n\t"
      "s_mul_i32 s34, s30, 28\n\t"
      "s_add_u32 s34, s34, s29\n\t"
      "s_lshl_b32 s34, s34, 11\n\t"
      "s_add_u32 s34, s34, 10223616\n\t"
      "s_add_u32 s62, s20, s34\n\t"
      "s_addc_u32 s63, s21, 0\n\t"
      "v_lshlrev_b32_e32 v248, 16, v249\n\t"
      "v_and_b32_e32 v249, 0xffff0000, v249\n\t"
      "v_mov_b32_e32 v252, 0xbdd2d3e8\n\t"
      "v_xor_b32_e32 v253, 128, v245\n\t"
      "v_max3_u32 v251, v128, v129, v130\n\t"
      "v_max3_u32 v251, v251, v131, v132\n\t"
      "v_max3_u32 v251, v251, v133, v134\n\t"
      "v_max3_u32 v251, v251, v135, v136\n\t"
      "v_max3_u32 v251, v251, v137, v138\n\t"
      "v_max3_u32 v251, v251, v139, v140\n\t"
      "v_max3_u32 v251, v251, v141, v142\n\t"
      "v_max3_u32 v251, v251, v143, v144\n\t"
      "v_max3_u32 v251, v251, v145, v146\n\t"
      "v_max3_u32 v251, v251, v147, v148\n\t"
      "v_max3_u32 v251, v251, v149, v150\n\t"
      "v_max3_u32 v251, v251, v151, v152\n\t"
      "v_max3_u32 v251, v251, v153, v154\n\t"
      "v_max3_u32 v251, v251, v155, v156\n\t"
      "v_max3_u32 v251, v251, v157, v158\n\t"
      "v_max_u32_e32 v251, v251, v159\n\t"
      "v_cmp_eq_u32_e32 vcc, -1, v251\n\t"
      "s_cbranch_vccnz LA30R_%=\n\t"
      "LA30B_%=:\n\t"
      "v_mfma_f32_16x16x32_bf16 v[208:211], v[0:3], v[128:131], 0\n\t"
      "global_load_dwordx4 v[160:163], v250, s[66:67] offset:-4096 sc1\n\t"
      "v_mfma_f32_16x16x32_bf16 v[208:211], v[16:19], v[132:135], v[208:211]\n\t"
      "v_mfma_f32_16x16x32_bf16 v[208:211], v[32:35], v[136:139], v[208:211]\n\t"
      "global_load_dwordx4 v[164:167], v250, s[66:67] offset:-3072 sc1\n\t"
      "v_mfma_f32_16x16x32_bf16 v[208:211], v[48:51], v[140:143], v[208:211]\n\t"
      "v_mfma_f32_16x16x32_bf16 v[208:211], v[64:67], v[144:147], v[208:211]\n\t"
      "global_load_dwordx4 v[168:171], v250, s[66:67] offset:-2048 sc1\n\t"
      "v_mfma_f32_16x16x32_bf16 v[208:211], v[80:83], v[148:151], v[208:211]\n\t"
      "v_mfma_f32_16x16x32_bf16 v[208:211], v[96:99], v[152:155], v[208:211]\n\t"
      "s_lshl_b32 s34, s2, 7\n\t"
      "s_add_u32 s34, s34, 65536\n\t"
      "v_mov_b32_e32 v230, s34\n\t"
      "s_cmp_lg_u32 s24, 3\n\t"
      "s_cbranch_scc1 SK17_%=\n\t"
      "global_load_dwordx2 v[228:229], v230, s[60:61] sc1\n\t"
      "SK17_%=:\n\t"
      "global_load_dwordx4 v[172:175], v250, s[66:67] offset:-1024 sc1\n\t"
      "v_mfma_f32_16x16x32_bf16 v[208:211], v[112:115], v[156:159], v[208:211]\n\t"
      "v_mfma_f32_16x16x32_bf16 v[212:215], v[4:7], v[128:131], 0\n\t"
      "global_load_dwordx4 v[176:179], v250, s[66:67] sc1\n\t"
      "v_mfma_f32_16x16x32_bf16 v[212:215], v[20:23], v[132:135], v[212:215]\n\t"
      "v_mfma_f32_16x16x32_bf16 v[212:215], v[36:39], v[136:139], v[212:215]\n\t"
      "global_load_dwordx4 v[180:183], v250, s[66:67] offset:1024 sc1\n\t"
      "v_mfma_f32_16x16x32_bf16 v[212:215], v[52:55], v[140:143], v[212:215]\n\t"
      "v_mfma_f32_16x16x32_bf16 v[212:215], v[68:71], v[144:147], v[212:215]\n\t"
      "global_load_dwordx4 v[184:187], v250, s[66:67] offset:2048 sc1\n\t"
      "v_mfma_f32_16x16x32_bf16 v[212:215], v[84:87], v[148:151], v[212:215]\n\t"
      "v_mfma_f32_16x16x32_bf16 v[212:215], v[100:103], v[152:155], v[212:215]\n\t"
      "ds_write_b128 v245, v[208:211]\n\t"
      "global_load_dwordx4 v[188:191], v250, s[66:67] offset:3072 sc1\n\t"
      "v_mfma_f32_16x16x32_bf16 v[212:215], v[116:119], v[156:159], v[212:215]\n\t"
      "v_mfma_f32_16x16x32_bf16 v[216:219], v[8:11], v[128:131], 0\n\t"
      "global_load_dwordx4 v[192:195], v250, s[68:69] offset:-4096 sc1\n\t"
      "v_mfma_f32_16x16x32_bf16 v[216:219], v[24:27], v[132:135], v[216:219]\n\t"
      "v_mfma_f32_16x16x32_bf16 v[216:219], v[40:43], v[136:139], v[216:219]\n\t"
      "global_load_dwordx4 v[196:199], v250, s[68:69] offset:-3072 sc1\n\t"
      "v_mfma_f32_16x16x32_bf16 v[216:219], v[56:59], v[140:143], v[216:219]\n\t"
      "v_mfma_f32_16x16x32_bf16 v[216:219], v[72:75], v[144:147], v[216:219]\n\t"
      "global_load_dwordx4 v[200:203], v250, s[68:69] offset:-2048 sc1\n\t"
      "v_mfma_f32_16x16x32_bf16 v[216:219], v[88:91], v[148:151], v[216:219]\n\t"
      "v_mfma_f32_16x16x32_bf16 v[216:219], v[104:107], v[152:155], v[216:219]\n\t"
      "ds_write_b128 v253, v[212:215] offset:1024\n\t"
      "global_load_dwordx4 v[204:207], v250, s[68:69] offset:-1024 sc1\n\t"
      "v_mfma_f32_16x16x32_bf16 v[216:219], v[120:123], v[156:159], v[216:219]\n\t"
      "v_mfma_f32_16x16x32_bf16 v[220:223], v[12:15], v[128:131], 0\n\t"
      "global_load_dwordx4 v[128:131], v250, s[68:69] sc1\n\t"
      "v_mfma_f32_16x16x32_bf16 v[220:223], v[28:31], v[132:135], v[220:223]\n\t"
      "v_mfma_f32_16x16x32_bf16 v[220:223], v[44:47], v[136:139], v[220:223]\n\t"
      "v_mfma_f32_16x16x32_bf16 v[220:223], v[60:63], v[140:143], v[220:223]\n\t"
      "v_mfma_f32_16x16x32_bf16 v[220:223], v[76:79], v[144:147], v[220:223]\n\t"
      "global_load_dwordx4 v[132:135], v250, s[68:69] offset:1024 sc1\n\t"
      "v_mfma_f32_16x16x32_bf16 v[220:223], v[92:95], v[148:151], v[220:223]\n\t"
      "v_mfma_f32_16x16x32_bf16 v[220:223], v[108:111], v[152:155], v[220:223]\n\t"
      "ds_write_b128 v245, v[216:219] offset:2048\n\t"
      "v_mfma_f32_16x16x32_bf16 v[220:223], v[124:127], v[156:159], v[220:223]\n\t"
      "s_waitcnt vmcnt(10)\n\t"
      "v_mfma_f32_16x16x32_bf16 v[224:227], v[0:3], v[160:163], 0\n\t"
      "global_load_dwordx4 v[136:139], v250, s[68:69] offset:2048 sc1\n\t"
      "v_mfma_f32_16x16x32_bf16 v[224:227], v[16:19], v[164:167], v[224:227]\n\t"
      "s_nop 3\n\t"
      "ds_write_b128 v253, v[220:223] offset:3072\n\t"
      "v_mfma_f32_16x16x32_bf16 v[224:227], v[32:35], v[168:171], v[224:227]\n\t"
      "v_mfma_f32_16x16x32_bf16 v[224:227], v[48:51], v[172:175], v[224:227]\n\t"
      "s_waitcnt vmcnt(7)\n\t"
      "v_mfma_f32_16x16x32_bf16 v[224:227], v[64:67], v[176:179], v[224:227]\n\t"
      "global_load_dwordx4 v[140:143], v250, s[68:69] offset:3072 sc1\n\t"
      "v_mfma_f32_16x16x32_bf16 v[224:227], v[80:83], v[180:183], v[224:227]\n\t"
      "s_cmp_lg_u32 s24, 3\n\t"
      "s_cbranch_scc1 LF6B_%=\n\t"
      "v_cmp_ne_u64_e32 vcc, s[22:23], v[228:229]\n\t"
      "s_cbranch_vccnz LF6R_%=\n\t"
      "LF6B_%=:\n\t"
      "s_waitcnt lgkmcnt(0)\n\t"
      "s_barrier\n\t"
      "s_cmp_lg_u32 s24, 3\n\t"
      "s_cbranch_scc1 HF3_%=\n\t"
      "s_lshl3_add_u32 s34, s25, s26\n\t"
      "s_lshl_b32 s34, s34, 7\n\t"
      "s_add_u32 s34, s34, 98304\n\t"
      "v_mov_b32_e32 v238, s34\n\t"
      "v_mov_b32_e32 v236, s22\n\t"
      "v_mov_b32_e32 v237, s23\n\t"
      "s_mov_b64 exec, 1\n\t"
      "global_store_dwordx2 v238, v[236:237], s[60:61] sc1\n\t"
      "s_mov_b64 exec, -1\n\t"
      "HF3_%=:\n\t"
      "ds_read_b64 v[228:229], v246\n\t"
      "ds_read_b64 v[230:231], v246 offset:4096\n\t"
      "v_mfma_f32_16x16x32_bf16 v[224:227], v[96:99], v[184:187], v[224:227]\n\t"
      "ds_read_b64 v[232:233], v246 offset:8192\n\t"
      "ds_read_b64 v[234:235], v246 offset:12288\n\t"
      "v_mfma_f32_16x16x32_bf16 v[224:227], v[112:115], v[188:191], v[224:227]\n\t"
      "ds_read_b64 v[236:237], v246 offset:16384\n\t"
      "ds_read_b64 v[238:239], v246 offset:20480\n\t"
      "v_mfma_f32_16x16x32_bf16 v[208:211], v[4:7], v[160:163], 0\n\t"
      "ds_read_b64 v[240:241], v246 offset:24576\n\t"
      "ds_read_b64 v[242:243], v246 offset:28672\n\t"
      "global_load_dwordx4 v[144:147], v250, s[70:71] offset:-4096 sc1\n\t"
      "v_mfma_f32_16x16x32_bf16 v[208:211], v[20:23], v[164:167], v[208:211]\n\t"
      "v_mfma_f32_16x16x32_bf16 v[208:211], v[36:39], v[168:171], v[208:211]\n\t"
      "s_waitcnt lgkmcnt(4)\n\t"
      "v_add_f32_e32 v228, v228, v230\n\t"
      "v_add_f32_e32 v232, v232, v234\n\t"
      "v_cmp_u_f32_e32 vcc, v224, v224\n\t"
      "s_cbranch_vccnz LA31R_%=\n\t"
      "LA31B_%=:\n\t"
      "v_mfma_f32_16x16x32_bf16 v[208:211], v[52:55], v[172:175], v[208:211]\n\t"
      "v_add_f32_e32 v229, v229, v231\n\t"
      "v_add_f32_e32 v233, v233, v235\n\t"
      "v_mfma_f32_16x16x32_bf16 v[208:211], v[68:71], v[176:179], v[208:211]\n\t"
      "s_waitcnt lgkmcnt(0)\n\t"
      "v_add_f32_e32 v236, v236, v238\n\t"
      "v_add_f32_e32 v240, v240, v242\n\t"
      "global_load_dwordx4 v[148:151], v250, s[70:71] offset:-3072 sc1\n\t"
      "v_mfma_f32_16x16x32_bf16 v[208:211], v[84:87], v[180:183], v[208:211]\n\t"
      "v_add_f32_e32 v237, v237, v239\n\t"
      "v_add_f32_e32 v241, v241, v243\n\t"
      "v_mfma_f32_16x16x32_bf16 v[208:211], v[100:103], v[184:187], v[208:211]\n\t"
      "v_add_f32_e32 v228, v228, v232\n\t"
      "v_add_f32_e32 v236, v236, v240\n\t"
      "v_add_f32_e32 v229, v229, v233\n\t"
      "ds_write_b128 v245, v[224:227] offset:32768\n\t"
      "v_mfma_f32_16x16x32_bf16 v[208:211], v[116:119], v[188:191], v[208:211]\n\t"
      "v_add_f32_e32 v237, v237, v241\n\t"
      "v_add_f32_e32 v228, v228, v248\n\t"
      "v_mfma_f32_16x16x32_bf16 v[212:215], v[8:11], v[160:163], 0\n\t"
      "v_add_f32_e32 v229, v229, v249\n\t"
      "v_add_f32_e32 v228, v228, v236\n\t"
      "v_add_f32_e32 v229, v229, v237\n\t"
      "global_load_dwordx4 v[152:155], v250, s[70:71] offset:-2048 sc1\n\t"
      "v_mfma_f32_16x16x32_bf16 v[212:215], v[24:27], v[164:167], v[212:215]\n\t"
      "v_mul_f32_e32 v230, v228, v228\n\t"
      "v_mul_f32_e32 v231, v229, v229\n\t"
      "v_mfma_f32_16x16x32_bf16 v[212:215], v[40:43], v[168:171], v[212:215]\n\t"
      "v_fmaak_f32 v230, v252, v230, 0xc0135761\n\t"
      "v_fmaak_f32 v231, v252, v231, 0xc0135761\n\t"
      "v_mul_f32_e32 v230, v228, v230\n\t"
      "v_mfma_f32_16x16x32_bf16 v[212:215], v[56:59], v[172:175], v[212:215]\n\t"
      "v_mul_f32_e32 v231, v229, v231\n\t"
      "v_exp_f32_e32 v230, v230\n\t"
      "v_mfma_f32_16x16x32_bf16 v[212:215], v[72:75], v[176:179], v[212:215]\n\t"
      "v_exp_f32_e32 v231, v231\n\t"
      "v_add_f32_e32 v230, 1.0, v230\n\t"
      "v_add_f32_e32 v231, 1.0, v231\n\t"
      "global_load_dwordx4 v[156:159], v250, s[70:71] offset:-1024 sc1\n\t"
      "v_mfma_f32_16x16x32_bf16 v[212:215], v[88:91], v[180:183], v[212:215]\n\t"
      "v_rcp_f32_e32 v230, v230\n\t"
      "v_rcp_f32_e32 v231, v231\n\t"
      "v_mfma_f32_16x16x32_bf16 v[212:215], v[104:107], v[184:187], v[212:215]\n\t"
      "v_mul_f32_e32 v228, v228, v230\n\t"
      "v_mul_f32_e32 v229, v229, v231\n\t"
      "v_cvt_pk_bf16_f32 v232, v228, v229\n\t"
      "ds_write_b128 v253, v[208:211] offset:33792\n\t"
      "v_mfma_f32_16x16x32_bf16 v[212:215], v[120:123], v[188:191], v[212:215]\n\t"
      "v_min_u32_e32 v254, -2, v232\n\t"
      "global_store_dword v247, v254, s[62:63] sc1\n\t"
      "s_add_u32 s62, s62, 57344\n\t"
      "s_addc_u32 s63, s63, 0\n\t"
      "v_mfma_f32_16x16x32_bf16 v[216:219], v[12:15], v[160:163], 0\n\t"
      "global_load_dwordx4 v[160:163], v250, s[70:71] sc1\n\t"
      "v_mfma_f32_16x16x32_bf16 v[216:219], v[28:31], v[164:167], v[216:219]\n\t"
      "v_mfma_f32_16x16x32_bf16 v[216:219], v[44:47], v[168:171], v[216:219]\n\t"
      "v_mfma_f32_16x16x32_bf16 v[216:219], v[60:63], v[172:175], v[216:219]\n\t"
      "v_mfma_f32_16x16x32_bf16 v[216:219], v[76:79], v[176:179], v[216:219]\n\t"
      "global_load_dwordx4 v[164:167], v250, s[70:71] offset:1024 sc1\n\t"
      "v_mfma_f32_16x16x32_bf16 v[216:219], v[92:95], v[180:183], v[216:219]\n\t"
      "v_mfma_f32_16x16x32_bf16 v[216:219], v[108:111], v[184:187], v[216:219]\n\t"
      "ds_write_b128 v245, v[212:215] offset:34816\n\t"
      "v_mfma_f32_16x16x32_bf16 v[216:219], v[124:127], v[188:191], v[216:219]\n\t"
      "s_waitcnt vmcnt(11)\n\t"
      "v_mfma_f32_16x16x32_bf16 v[220:223], v[0:3], v[192:195], 0\n\t"
      "global_load_dwordx4 v[168:171], v250, s[70:71] offset:2048 sc1\n\t"
      "v_mfma_f32_16x16x32_bf16 v[220:223], v[16:19], v[196:199], v[220:223]\n\t"
      "s_nop 3\n\t"
      "ds_write_b128 v253, v[216:219] offset:35840\n\t"
      "v_mfma_f32_16x16x32_bf16 v[220:223], v[32:35], v[200:203], v[220:223]\n\t"
      "v_mfma_f32_16x16x32_bf16 v[220:223], v[48:51], v[204:207], v[220:223]\n\t"
      "s_waitcnt vmcnt(8)\n\t"
      "v_mfma_f32_16x16x32_bf16 v[220:223], v[64:67], v[128:131], v[220:223]\n\t"
      "global_load_dwordx4 v[172:175], v250, s[70:71] offset:3072 sc1\n\t"
      "v_mfma_f32_16x16x32_bf16 v[220:223], v[80:83], v[132:135], v[220:223]\n\t"
      "s_waitcnt lgkmcnt(0)\n\t"
      "s_barrier\n\t"
      "ds_read_b64 v[228:229], v246 offset:32768\n\t"
      "ds_read_b64 v[230:231], v246 offset:36864\n\t"
      "v_mfma_f32_16x16x32_bf16 v[220:223], v[96:99], v[136:139], v[220:223]\n\t"
      "ds_read_b64 v[232:233], v246 offset:40960\n\t"
      "ds_read_b64 v[234:235], v246 offset:45056\n\t"
      "v_mfma_f32_16x16x32_bf16 v[220:223], v[112:115], v[140:143], v[220:223]\n\t"
      "ds_read_b64 v[236:237], v246 offset:49152\n\t"
      "ds_read_b64 v[238:239], v246 offset:53248\n\t"
      "v_mfma_f32_16x16x32_bf16 v[224:227], v[4:7], v[192:195], 0\n\t"
      "ds_read_b64 v[240:241], v246 offset:57344\n\t"
      "ds_read_b64 v[242:243], v246 offset:61440\n\t"
      "global_load_dwordx4 v[176:179], v250, s[72:73] offset:-4096 sc1\n\t"
      "v_mfma_f32_16x16x32_bf16 v[224:227], v[20:23], v[196:199], v[224:227]\n\t"
      "v_mfma_f32_16x16x32_bf16 v[224:227], v[36:39], v[200:203], v[224:227]\n\t"
      "s_waitcnt lgkmcnt(4)\n\t"
      "v_add_f32_e32 v228, v228, v230\n\t"
      "v_add_f32_e32 v232, v232, v234\n\t"
      "v_cmp_u_f32_e32 vcc, v220, v220\n\t"
      "s_cbranch_vccnz LA32R_%=\n\t"
      "LA32B_%=:\n\t"
      "v_mfma_f32_16x16x32_bf16 v[224:227], v[52:55], v[204:207], v[224:227]\n\t"
      "v_add_f32_e32 v229, v229, v231\n\t"
      "v_add_f32_e32 v233, v233, v235\n\t"
      "v_mfma_f32_16x16x32_bf16 v[224:227], v[68:71], v[128:131], v[224:227]\n\t"
      "s_waitcnt lgkmcnt(0)\n\t"
      "v_add_f32_e32 v236, v236, v238\n\t"
      "v_add_f32_e32 v240, v240, v242\n\t"
      "global_load_dwordx4 v[180:183], v250, s[72:73] offset:-3072 sc1\n\t"
      "v_mfma_f32_16x16x32_bf16 v[224:227], v[84:87], v[132:135], v[224:227]\n\t"
      "v_add_f32_e32 v237, v237, v239\n\t"
      "v_add_f32_e32 v241, v241, v243\n\t"
      "v_mfma_f32_16x16x32_bf16 v[224:227], v[100:103], v[136:139], v[224:227]\n\t"
      "v_add_f32_e32 v228, v228, v232\n\t"
      "v_add_f32_e32 v236, v236, v240\n\t"
      "v_add_f32_e32 v229, v229, v233\n\t"
      "ds_write_b128 v245, v[220:223]\n\t"
      "v_mfma_f32_16x16x32_bf16 v[224:227], v[116:119], v[140:143], v[224:227]\n\t"
      "v_add_f32_e32 v237, v237, v241\n\t"
      "v_add_f32_e32 v228, v228, v248\n\t"
      "v_mfma_f32_16x16x32_bf16 v[208:211], v[8:11], v[192:195], 0\n\t"
      "v_add_f32_e32 v229, v229, v249\n\t"
      "v_add_f32_e32 v228, v228, v236\n\t"
      "v_add_f32_e32 v229, v229, v237\n\t"
      "global_load_dwordx4 v[184:187], v250, s[72:73] offset:-2048 sc1\n\t"
      "v_mfma_f32_16x16x32_bf16 v[208:211], v[24:27], v[196:199], v[208:211]\n\t"
      "v_mul_f32_e32 v230, v228, v228\n\t"
      "v_mul_f32_e32 v231, v229, v229\n\t"
      "v_mfma_f32_16x16x32_bf16 v[208:211], v[40:43], v[200:203], v[208:211]\n\t"
      "v_fmaak_f32 v230, v252, v230, 0xc0135761\n\t"
      "v_fmaak_f32 v231, v252, v231, 0xc0135761\n\t"
      "v_mul_f32_e32 v230, v228, v230\n\t"
      "v_mfma_f32_16x16x32_bf16 v[208:211], v[56:59], v[204:207], v[208:211]\n\t"
      "v_mul_f32_e32 v231, v229, v231\n\t"
      "v_exp_f32_e32 v230, v230\n\t"
      "v_mfma_f32_16x16x32_bf16 v[208:211], v[72:75], v[128:131], v[208:211]\n\t"
      "v_exp_f32_e32 v231, v231\n\t"
      "v_add_f32_e32 v230, 1.0, v230\n\t"
      "v_add_f32_e32 v231, 1.0, v231\n\t"
      "global_load_dwordx4 v[188:191], v250, s[72:73] offset:-1024 sc1\n\t"
      "v_mfma_f32_16x16x32_bf16 v[208:211], v[88:91], v[132:135], v[208:211]\n\t"
      "v_rcp_f32_e32 v230, v230\n\t"
      "v_rcp_f32_e32 v231, v231\n\t"
      "v_mfma_f32_16x16x32_bf16 v[208:211], v[104:107], v[136:139], v[208:211]\n\t"
      "v_mul_f32_e32 v228, v228, v230\n\t"
      "v_mul_f32_e32 v229, v229, v231\n\t"
      "v_cvt_pk_bf16_f32 v232, v228, v229\n\t"
      "ds_write_b128 v253, v[224:227] offset:1024\n\t"
      "v_mfma_f32_16x16x32_bf16 v[208:211], v[120:123], v[140:143], v[208:211]\n\t"
      "v_min_u32_e32 v254, -2, v232\n\t"
      "global_store_dword v247, v254, s[62:63] sc1\n\t"
      "s_add_u32 s62, s62, 57344\n\t"
      "s_addc_u32 s63, s63, 0\n\t"
      "v_mfma_f32_16x16x32_bf16 v[212:215], v[12:15], v[192:195], 0\n\t"
      "global_load_dwordx4 v[192:195], v250, s[72:73] sc1\n\t"
      "v_mfma_f32_16x16x32_bf16 v[212:215], v[28:31], v[196:199], v[212:215]\n\t"
      "v_mfma_f32_16x16x32_bf16 v[212:215], v[44:47], v[200:203], v[212:215]\n\t"
      "v_mfma_f32_16x16x32_bf16 v[212:215], v[60:63], v[204:207], v[212:215]\n\t"
      "v_mfma_f32_16x16x32_bf16 v[212:215], v[76:79], v[128:131], v[212:215]\n\t"
      "global_load_dwordx4 v[196:199], v250, s[72:73] offset:1024 sc1\n\t"
      "v_mfma_f32_16x16x32_bf16 v[212:215], v[92:95], v[132:135], v[212:215]\n\t"
      "v_mfma_f32_16x16x32_bf16 v[212:215], v[108:111], v[136:139], v[212:215]\n\t"
      "ds_write_b128 v245, v[208:211] offset:2048\n\t"
      "v_mfma_f32_16x16x32_bf16 v[212:215], v[124:127], v[140:143], v[212:215]\n\t"
      "s_waitcnt vmcnt(12)\n\t"
      "v_mfma_f32_16x16x32_bf16 v[216:219], v[0:3], v[144:147], 0\n\t"
      "global_load_dwordx4 v[200:203], v250, s[72:73] offset:2048 sc1\n\t"
      "v_mfma_f32_16x16x32_bf16 v[216:219], v[16:19], v[148:151], v[216:219]\n\t"
      "s_nop 3\n\t"
      "ds_write_b128 v253, v[212:215] offset:3072\n\t"
      "v_mfma_f32_16x16x32_bf16 v[216:219], v[32:35], v[152:155], v[216:219]\n\t"
      "v_mfma_f32_16x16x32_bf16 v[216:219], v[48:51], v[156:159], v[216:219]\n\t"
      "s_waitcnt vmcnt(8)\n\t"
      "v_mfma_f32_16x16x32_bf16 v[216:219], v[64:67], v[160:163], v[216:219]\n\t"
      "global_load_dwordx4 v[204:207], v250, s[72:73] offset:3072 sc1\n\t"
      "v_mfma_f32_16x16x32_bf16 v[216:219], v[80:83], v[164:167], v[216:219]\n\t"
      "s_waitcnt lgkmcnt(0)\n\t"
      "s_barrier\n\t"
      "ds_read_b64 v[228:229], v246\n\t"
      "ds_read_b64 v[230:231], v246 offset:4096\n\t"
      "v_mfma_f32_16x16x32_bf16 v[216:219], v[96:99], v[168:171], v[216:219]\n\t"
      "ds_read_b64 v[232:233], v246 offset:8192\n\t"
      "ds_read_b64 v[234:235], v246 offset:12288\n\t"
      "v_mfma_f32_16x16x32_bf16 v[216:219], v[112:115], v[172:175], v[216:219]\n\t"
      "ds_read_b64 v[236:237], v246 offset:16384\n\t"
      "ds_read_b64 v[238:239], v246 offset:20480\n\t"
      "v_mfma_f32_16x16x32_bf16 v[220:223], v[4:7], v[144:147], 0\n\t"
      "ds_read_b64 v[240:241], v246 offset:24576\n\t"
      "ds_read_b64 v[242:243], v246 offset:28672\n\t"
      "global_load_dwordx4 v[128:131], v250, s[74:75] offset:-4096 sc1\n\t"
      "v_mfma_f32_16x16x32_bf16 v[220:223], v[20:23], v[148:151], v[220:223]\n\t"
      "v_mfma_f32_16x16x32_bf16 v[220:223], v[36:39], v[152:155], v[220:223]\n\t"
      "s_waitcnt lgkmcnt(4)\n\t"
      "v_add_f32_e32 v228, v228, v230\n\t"
      "v_add_f32_e32 v232, v232, v234\n\t"
      "v_cmp_u_f32_e32 vcc, v216, v216\n\t"
      "s_cbranch_vccnz LA33R_%=\n\t"
      "LA33B_%=:\n\t"
      "v_mfma_f32_16x16x32_bf16 v[220:223], v[52:55], v[156:159], v[220:223]\n\t"
      "v_add_f32_e32 v229, v229, v231\n\t"
      "v_add_f32_e32 v233, v233, v235\n\t"
      "v_mfma_f32_16x16x32_bf16 v[220:223], v[68:71], v[160:163], v[220:223]\n\t"
      "s_waitcnt lgkmcnt(0)\n\t"
      "v_add_f32_e32 v236, v236, v238\n\t"
      "v_add_f32_e32 v240, v240, v242\n\t"
      "global_load_dwordx4 v[132:135], v250, s[74:75] offset:-3072 sc1\n\t"
      "v_mfma_f32_16x16x32_bf16 v[220:223], v[84:87], v[164:167], v[220:223]\n\t"
      "v_add_f32_e32 v237, v237, v239\n\t"
      "v_add_f32_e32 v241, v241, v243\n\t"
      "v_mfma_f32_16x16x32_bf16 v[220:223], v[100:103], v[168:171], v[220:223]\n\t"
      "v_add_f32_e32 v228, v228, v232\n\t"
      "v_add_f32_e32 v236, v236, v240\n\t"
      "v_add_f32_e32 v229, v229, v233\n\t"
      "ds_write_b128 v245, v[216:219] offset:32768\n\t"
      "v_mfma_f32_16x16x32_bf16 v[220:223], v[116:119], v[172:175], v[220:223]\n\t"
      "v_add_f32_e32 v237, v237, v241\n\t"
      "v_add_f32_e32 v228, v228, v248\n\t"
      "v_mfma_f32_16x16x32_bf16 v[224:227], v[8:11], v[144:147], 0\n\t"
      "v_add_f32_e32 v229, v229, v249\n\t"
      "v_add_f32_e32 v228, v228, v236\n\t"
      "v_add_f32_e32 v229, v229, v237\n\t"
      "global_load_dwordx4 v[136:139], v250, s[74:75] offset:-2048 sc1\n\t"
      "v_mfma_f32_16x16x32_bf16 v[224:227], v[24:27], v[148:151], v[224:227]\n\t"
      "v_mul_f32_e32 v230, v228, v228\n\t"
      "v_mul_f32_e32 v231, v229, v229\n\t"
      "v_mfma_f32_16x16x32_bf16 v[224:227], v[40:43], v[152:155], v[224:227]\n\t"
      "v_fmaak_f32 v230, v252, v230, 0xc0135761\n\t"
      "v_fmaak_f32 v231, v252, v231, 0xc0135761\n\t"
      "v_mul_f32_e32 v230, v228, v230\n\t"
      "v_mfma_f32_16x16x32_bf16 v[224:227], v[56:59], v[156:159], v[224:227]\n\t"
      "v_mul_f32_e32 v231, v229, v231\n\t"
      "v_exp_f32_e32 v230, v230\n\t"
      "v_mfma_f32_16x16x32_bf16 v[224:227], v[72:75], v[160:163], v[224:227]\n\t"
      "v_exp_f32_e32 v231, v231\n\t"
      "v_add_f32_e32 v230, 1.0, v230\n\t"
      "v_add_f32_e32 v231, 1.0, v231\n\t"
      "global_load_dwordx4 v[140:143], v250, s[74:75] offset:-1024 sc1\n\t"
      "v_mfma_f32_16x16x32_bf16 v[224:227], v[88:91], v[164:167], v[224:227]\n\t"
      "v_rcp_f32_e32 v230, v230\n\t"
      "v_rcp_f32_e32 v231, v231\n\t"
      "v_mfma_f32_16x16x32_bf16 v[224:227], v[104:107], v[168:171], v[224:227]\n\t"
      "v_mul_f32_e32 v228, v228, v230\n\t"
      "v_mul_f32_e32 v229, v229, v231\n\t"
      "v_cvt_pk_bf16_f32 v232, v228, v229\n\t"
      "ds_write_b128 v253, v[220:223] offset:33792\n\t"
      "v_mfma_f32_16x16x32_bf16 v[224:227], v[120:123], v[172:175], v[224:227]\n\t"
      "v_min_u32_e32 v254, -2, v232\n\t"
      "global_store_dword v247, v254, s[62:63] sc1\n\t"
      "s_add_u32 s62, s62, 57344\n\t"
      "s_addc_u32 s63, s63, 0\n\t"
      "v_mfma_f32_16x16x32_bf16 v[208:211], v[12:15], v[144:147], 0\n\t"
      "global_load_dwordx4 v[144:147], v250, s[74:75] sc1\n\t"
      "v_mfma_f32_16x16x32_bf16 v[208:211], v[28:31], v[148:151], v[208:211]\n\t"
      "v_mfma_f32_16x16x32_bf16 v[208:211], v[44:47], v[152:155], v[208:211]\n\t"
      "v_mfma_f32_16x16x32_bf16 v[208:211], v[60:63], v[156:159], v[208:211]\n\t"
      "v_mfma_f32_16x16x32_bf16 v[208:211], v[76:79], v[160:163], v[208:211]\n\t"
      "global_load_dwordx4 v[148:151], v250, s[74:75] offset:1024 sc1\n\t"
      "v_mfma_f32_16x16x32_bf16 v[208:211], v[92:95], v[164:167], v[208:211]\n\t"
      "v_mfma_f32_16x16x32_bf16 v[208:211], v[108:111], v[168:171], v[208:211]\n\t"
      "ds_write_b128 v245, v[224:227] offset:34816\n\t"
      "v_mfma_f32_16x16x32_bf16 v[208:211], v[124:127], v[172:175], v[208:211]\n\t"
      "s_waitcnt vmcnt(12)\n\t"
      "v_mfma_f32_16x16x32_bf16 v[212:215], v[0:3], v[176:179], 0\n\t"
      "global_load_dwordx4 v[152:155], v250, s[74:75] offset:2048 sc1\n\t"
      "v_mfma_f32_16x16x32_bf16 v[212:215], v[16:19], v[180:183], v[212:215]\n\t"
      "s_nop 3\n\t"
      "ds_write_b128 v253, v[208:211] offset:35840\n\t"
      "v_mfma_f32_16x16x32_bf16 v[212:215], v[32:35], v[184:187], v[212:215]\n\t"
      "v_mfma_f32_16x16x32_bf16 v[212:215], v[48:51], v[188:191], v[212:215]\n\t"
      "s_waitcnt vmcnt(8)\n\t"
      "v_mfma_f32_16x16x32_bf16 v[212:215], v[64:67], v[192:195], v[212:215]\n\t"
      "global_load_dwordx4 v[156:159], v250, s[74:75] offset:3072 sc1\n\t"
      "v_mfma_f32_16x16x32_bf16 v[212:215], v[80:83], v[196:199], v[212:215]\n\t"
      "s_waitcnt lgkmcnt(0)\n\t"
      "s_barrier\n\t"
      "ds_read_b64 v[228:229], v246 offset:32768\n\t"
      "ds_read_b64 v[230:231], v246 offset:36864\n\t"
      "v_mfma_f32_16x16x32_bf16 v[212:215], v[96:99], v[200:203], v[212:215]\n\t"
      "ds_read_b64 v[232:233], v246 offset:40960\n\t"
      "ds_read_b64 v[234:235], v246 offset:45056\n\t"
      "v_mfma_f32_16x16x32_bf16 v[212:215], v[112:115], v[204:207], v[212:215]\n\t"
      "ds_read_b64 v[236:237], v246 offset:49152\n\t"
      "ds_read_b64 v[238:239], v246 offset:53248\n\t"
      "v_mfma_f32_16x16x32_bf16 v[216:219], v[4:7], v[176:179], 0\n\t"
      "ds_read_b64 v[240:241], v246 offset:57344\n\t"
      "ds_read_b64 v[242:243], v246 offset:61440\n\t"
      "global_load_dwordx4 v[160:163], v250, s[76:77] offset:-4096 sc1\n\t"
      "v_mfma_f32_16x16x32_bf16 v[216:219], v[20:23], v[180:183], v[216:219]\n\t"
      "v_mfma_f32_16x16x32_bf16 v[216:219], v[36:39], v[184:187], v[216:219]\n\t"
      "s_waitcnt lgkmcnt(4)\n\t"
      "v_add_f32_e32 v228, v228, v230\n\t"
      "v_add_f32_e32 v232, v232, v234\n\t"
      "v_cmp_u_f32_e32 vcc, v212, v212\n\t"
      "s_cbranch_vccnz LA34R_%=\n\t"
      "LA34B_%=:\n\t"
      "v_mfma_f32_16x16x32_bf16 v[216:219], v[52:55], v[188:191], v[216:219]\n\t"
      "v_add_f32_e32 v229, v229, v231\n\t"
      "v_add_f32_e32 v233, v233, v235\n\t"
      "v_mfma_f32_16x16x32_bf16 v[216:219], v[68:71], v[192:195], v[216:219]\n\t"
      "s_waitcnt lgkmcnt(0)\n\t"
      "v_add_f32_e32 v236, v236, v238\n\t"
      "v_add_f32_e32 v240, v240, v242\n\t"
      "global_load_dwordx4 v[164:167], v250, s[76:77] offset:-3072 sc1\n\t"
      "v_mfma_f32_16x16x32_bf16 v[216:219], v[84:87], v[196:199], v[216:219]\n\t"
      "v_add_f32_e32 v237, v237, v239\n\t"
      "v_add_f32_e32 v241, v241, v243\n\t"
      "v_mfma_f32_16x16x32_bf16 v[216:219], v[100:103], v[200:203], v[216:219]\n\t"
      "v_add_f32_e32 v228, v228, v232\n\t"
      "v_add_f32_e32 v236, v236, v240\n\t"
      "v_add_f32_e32 v229, v229, v233\n\t"
      "ds_write_b128 v245, v[212:215]\n\t"
      "v_mfma_f32_16x16x32_bf16 v[216:219], v[116:119], v[204:207], v[216:219]\n\t"
      "v_add_f32_e32 v237, v237, v241\n\t"
      "v_add_f32_e32 v228, v228, v248\n\t"
      "v_mfma_f32_16x16x32_bf16 v[220:223], v[8:11], v[176:179], 0\n\t"
      "v_add_f32_e32 v229, v229, v249\n\t"
      "v_add_f32_e32 v228, v228, v236\n\t"
      "v_add_f32_e32 v229, v229, v237\n\t"
      "global_load_dwordx4 v[168:171], v250, s[76:77] offset:-2048 sc1\n\t"
      "v_mfma_f32_16x16x32_bf16 v[220:223], v[24:27], v[180:183], v[220:223]\n\t"
      "v_mul_f32_e32 v230, v228, v228\n\t"
      "v_mul_f32_e32 v231, v229, v229\n\t"
      "v_mfma_f32_16x16x32_bf16 v[220:223], v[40:43], v[184:187], v[220:223]\n\t"
      "v_fmaak_f32 v230, v252, v230, 0xc0135761\n\t"
      "v_fmaak_f32 v231, v252, v231, 0xc0135761\n\t"
      "v_mul_f32_e32 v230, v228, v230\n\t"
      "v_mfma_f32_16x16x32_bf16 v[220:223], v[56:59], v[188:191], v[220:223]\n\t"
      "v_mul_f32_e32 v231, v229, v231\n\t"
      "v_exp_f32_e32 v230, v230\n\t"
      "v_mfma_f32_16x16x32_bf16 v[220:223], v[72:75], v[192:195], v[220:223]\n\t"
      "v_exp_f32_e32 v231, v231\n\t"
      "v_add_f32_e32 v230, 1.0, v230\n\t"
      "v_add_f32_e32 v231, 1.0, v231\n\t"
      "global_load_dwordx4 v[172:175], v250, s[76:77] offset:-1024 sc1\n\t"
      "v_mfma_f32_16x16x32_bf16 v[220:223], v[88:91], v[196:199], v[220:223]\n\t"
      "v_rcp_f32_e32 v230, v230\n\t"
      "v_rcp_f32_e32 v231, v231\n\t"
      "v_mfma_f32_16x16x32_bf16 v[220:223], v[104:107], v[200:203], v[220:223]\n\t"
      "v_mul_f32_e32 v228, v228, v230\n\t"
      "v_mul_f32_e32 v229, v229, v231\n\t"
      "v_cvt_pk_bf16_f32 v232, v228, v229\n\t"
      "ds_write_b128 v253, v[216:219] offset:1024\n\t"
      "v_mfma_f32_16x16x32_bf16 v[220:223], v[120:123], v[204:207], v[220:223]\n\t"
      "v_min_u32_e32 v254, -2, v232\n\t"
      "global_store_dword v247, v254, s[62:63] sc1\n\t"
      "s_add_u32 s62, s62, 57344\n\t"
      "s_addc_u32 s63, s63, 0\n\t"
      "v_mfma_f32_16x16x32_bf16 v[224:227], v[12:15], v[176:179], 0\n\t"
      "global_load_dwordx4 v[176:179], v250, s[76:77] sc1\n\t"
      "v_mfma_f32_16x16x32_bf16 v[224:227], v[28:31], v[180:183], v[224:227]\n\t"
      "v_mfma_f32_16x16x32_bf16 v[224:227], v[44:47], v[184:187], v[224:227]\n\t"
      "v_mfma_f32_16x16x32_bf16 v[224:227], v[60:63], v[188:191], v[224:227]\n\t"
      "v_mfma_f32_16x16x32_bf16 v[224:227], v[76:79], v[192:195], v[224:227]\n\t"
      "global_load_dwordx4 v[180:183], v250, s[76:77] offset:1024 sc1\n\t"
      "v_mfma_f32_16x16x32_bf16 v[224:227], v[92:95], v[196:199], v[224:227]\n\t"
      "v_mfma_f32_16x16x32_bf16 v[224:227], v[108:111], v[200:203], v[224:227]\n\t"
      "ds_write_b128 v245, v[220:223] offset:2048\n\t"
      "v_mfma_f32_16x16x32_bf16 v[224:227], v[124:127], v[204:207], v[224:227]\n\t"
      "s_waitcnt vmcnt(12)\n\t"
      "v_mfma_f32_16x16x32_bf16 v[208:211], v[0:3], v[128:131], 0\n\t"
      "global_load_dwordx4 v[184:187], v250, s[76:77] offset:2048 sc1\n\t"
      "v_mfma_f32_16x16x32_bf16 v[208:211], v[16:19], v[132:135], v[208:211]\n\t"
      "s_nop 3\n\t"
      "ds_write_b128 v253, v[224:227] offset:3072\n\t"
      "v_mfma_f32_16x16x32_bf16 v[208:211], v[32:35], v[136:139], v[208:211]\n\t"
      "v_mfma_f32_16x16x32_bf16 v[208:211], v[48:51], v[140:143], v[208:211]\n\t"
      "s_waitcnt vmcnt(8)\n\t"
      "v_mfma_f32_16x16x32_bf16 v[208:211], v[64:67], v[144:147], v[208:211]\n\t"
      "global_load_dwordx4 v[188:191], v250, s[76:77] offset:3072 sc1\n\t"
      "v_mfma_f32_16x16x32_bf16 v[208:211], v[80:83], v[148:151], v[208:211]\n\t"
      "s_waitcnt lgkmcnt(0)\n\t"
      "s_barrier\n\t"
      "ds_read_b64 v[228:229], v246\n\t"
      "ds_read_b64 v[230:231], v246 offset:4096\n\t"
      "v_mfma_f32_16x16x32_bf16 v[208:211], v[96:99], v[152:155], v[208:211]\n\t"
      "ds_read_b64 v[232:233], v246 offset:8192\n\t"
      "ds_read_b64 v[234:235], v246 offset:12288\n\t"
      "v_mfma_f32_16x16x32_bf16 v[208:211], v[112:115], v[156:159], v[208:211]\n\t"
      "ds_read_b64 v[236:237], v246 offset:16384\n\t"
      "ds_read_b64 v[238:239], v246 offset:20480\n\t"
      "v_mfma_f32_16x16x32_bf16 v[212:215], v[4:7], v[128:131], 0\n\t"
      "ds_read_b64 v[240:241], v246 offset:24576\n\t"
      "ds_read_b64 v[242:243], v246 offset:28672\n\t"
      "v_mfma_f32_16x16x32_bf16 v[212:215], v[20:23], v[132:135], v[212:215]\n\t"
      "v_mfma_f32_16x16x32_bf16 v[212:215], v[36:39], v[136:139], v[212:215]\n\t"
      "s_waitcnt lgkmcnt(4)\n\t"
      "v_add_f32_e32 v228, v228, v230\n\t"
      "v_add_f32_e32 v232, v232, v234\n\t"
      "v_cmp_u_f32_e32 vcc, v208, v208\n\t"
      "s_cbranch_vccnz LA35R_%=\n\t"
      "LA35B_%=:\n\t"
      "v_mfma_f32_16x16x32_bf16 v[212:215], v[52:55], v[140:143], v[212:215]\n\t"
      "v_add_f32_e32 v229, v229, v231\n\t"
      "v_add_f32_e32 v233, v233, v235\n\t"
      "v_mfma_f32_16x16x32_bf16 v[212:215], v[68:71], v[144:147], v[212:215]\n\t"
      "s_waitcnt lgkmcnt(0)\n\t"
      "v_add_f32_e32 v236, v236, v238\n\t"
      "v_add_f32_e32 v240, v240, v242\n\t"
      "v_mfma_f32_16x16x32_bf16 v[212:215], v[84:87], v[148:151], v[212:215]\n\t"
      "v_add_f32_e32 v237, v237, v239\n\t"
      "v_add_f32_e32 v241, v241, v243\n\t"
      "v_mfma_f32_16x16x32_bf16 v[212:215], v[100:103], v[152:155], v[212:215]\n\t"
      "v_add_f32_e32 v228, v228, v232\n\t"
      "v_add_f32_e32 v236, v236, v240\n\t"
      "v_add_f32_e32 v229, v229, v233\n\t"
      "ds_write_b128 v245, v[208:211] offset:32768\n\t"
      "v_mfma_f32_16x16x32_bf16 v[212:215], v[116:119], v[156:159], v[212:215]\n\t"
      "v_add_f32_e32 v237, v237, v241\n\t"
      "v_add_f32_e32 v228, v228, v248\n\t"
      "v_mfma_f32_16x16x32_bf16 v[216:219], v[8:11], v[128:131], 0\n\t"
      "v_add_f32_e32 v229, v229, v249\n\t"
      "v_add_f32_e32 v228, v228, v236\n\t"
      "v_add_f32_e32 v229, v229, v237\n\t"
      "v_mfma_f32_16x16x32_bf16 v[216:219], v[24:27], v[132:135], v[216:219]\n\t"
      "v_mul_f32_e32 v230, v228, v228\n\t"
      "v_mul_f32_e32 v231, v229, v229\n\t"
      "v_mfma_f32_16x16x32_bf16 v[216:219], v[40:43], v[136:139], v[216:219]\n\t"
      "v_fmaak_f32 v230, v252, v230, 0xc0135761\n\t"
      "v_fmaak_f32 v231, v252, v231, 0xc0135761\n\t"
      "v_mul_f32_e32 v230, v228, v230\n\t"
      "v_mfma_f32_16x16x32_bf16 v[216:219], v[56:59], v[140:143], v[216:219]\n\t"
      "v_mul_f32_e32 v231, v229, v231\n\t"
      "v_exp_f32_e32 v230, v230\n\t"
      "v_mfma_f32_16x16x32_bf16 v[216:219], v[72:75], v[144:147], v[216:219]\n\t"
      "v_exp_f32_e32 v231, v231\n\t"
      "v_add_f32_e32 v230, 1.0, v230\n\t"
      "v_add_f32_e32 v231, 1.0, v231\n\t"
      "v_mfma_f32_16x16x32_bf16 v[216:219], v[88:91], v[148:151], v[216:219]\n\t"
      "v_rcp_f32_e32 v230, v230\n\t"
      "v_rcp_f32_e32 v231, v231\n\t"
      "v_mfma_f32_16x16x32_bf16 v[216:219], v[104:107], v[152:155], v[216:219]\n\t"
      "v_mul_f32_e32 v228, v228, v230\n\t"
      "v_mul_f32_e32 v229, v229, v231\n\t"
      "v_cvt_pk_bf16_f32 v232, v228, v229\n\t"
      "ds_write_b128 v253, v[212:215] offset:33792\n\t"
      "v_mfma_f32_16x16x32_bf16 v[216:219], v[120:123], v[156:159], v[216:219]\n\t"
      "v_min_u32_e32 v254, -2, v232\n\t"
      "global_store_dword v247, v254, s[62:63] sc1\n\t"
      "s_add_u32 s62, s62, 57344\n\t"
      "s_addc_u32 s63, s63, 0\n\t"
      "v_mfma_f32_16x16x32_bf16 v[220:223], v[12:15], v[128:131], 0\n\t"
      "v_mfma_f32_16x16x32_bf16 v[220:223], v[28:31], v[132:135], v[220:223]\n\t"
      "v_mfma_f32_16x16x32_bf16 v[220:223], v[44:47], v[136:139], v[220:223]\n\t"
      "v_mfma_f32_16x16x32_bf16 v[220:223], v[60:63], v[140:143], v[220:223]\n\t"
      "v_mfma_f32_16x16x32_bf16 v[220:223], v[76:79], v[144:147], v[220:223]\n\t"
      "v_mfma_f32_16x16x32_bf16 v[220:223], v[92:95], v[148:151], v[220:223]\n\t"
      "v_mfma_f32_16x16x32_bf16 v[220:223], v[108:111], v[152:155], v[220:223]\n\t"
      "ds_write_b128 v245, v[216:219] offset:34816\n\t"
      "v_mfma_f32_16x16x32_bf16 v[220:223], v[124:127], v[156:159], v[220:223]\n\t"
      "s_waitcnt vmcnt(6)\n\t"
      "v_mfma_f32_16x16x32_bf16 v[224:227], v[0:3], v[160:163], 0\n\t"
      "v_mfma_f32_16x16x32_bf16 v[224:227], v[16:19], v[164:167], v[224:227]\n\t"
      "s_nop 4\n\t"
      "ds_write_b128 v253, v[220:223] offset:35840\n\t"
      "v_mfma_f32_16x16x32_bf16 v[224:227], v[32:35], v[168:171], v[224:227]\n\t"
      "v_mfma_f32_16x16x32_bf16 v[224:227], v[48:51], v[172:175], v[224:227]\n\t"
      "s_waitcnt vmcnt(1)\n\t"
      "v_mfma_f32_16x16x32_bf16 v[224:227], v[64:67], v[176:179], v[224:227]\n\t"
      "v_mfma_f32_16x16x32_bf16 v[224:227], v[80:83], v[180:183], v[224:227]\n\t"
      "s_waitcnt lgkmcnt(0)\n\t"
      "s_barrier\n\t"
      "ds_read_b64 v[228:229], v246 offset:32768\n\t"
      "ds_read_b64 v[230:231], v246 offset:36864\n\t"
      "v_mfma_f32_16x16x32_bf16 v[224:227], v[96:99], v[184:187], v[224:227]\n\t"
      "ds_read_b64 v[232:233], v246 offset:40960\n\t"
      "ds_read_b64 v[234:235], v246 offset:45056\n\t"
      "v_mfma_f32_16x16x32_bf16 v[224:227], v[112:115], v[188:191], v[224:227]\n\t"
      "ds_read_b64 v[236:237], v246 offset:49152\n\t"
      "ds_read_b64 v[238:239], v246 offset:53248\n\t"
      "v_mfma_f32_16x16x32_bf16 v[208:211], v[4:7], v[160:163], 0\n\t"
      "ds_read_b64 v[240:241], v246 offset:57344\n\t"
      "ds_read_b64 v[242:243], v246 offset:61440\n\t"
      "v_mfma_f32_16x16x32_bf16 v[208:211], v[20:23], v[164:167], v[208:211]\n\t"
      "v_mfma_f32_16x16x32_bf16 v[208:211], v[36:39], v[168:171], v[208:211]\n\t"
      "s_waitcnt lgkmcnt(4)\n\t"
      "v_add_f32_e32 v228, v228, v230\n\t"
      "v_add_f32_e32 v232, v232, v234\n\t"
      "v_cmp_u_f32_e32 vcc, v224, v224\n\t"
      "s_cbranch_vccnz LA36R_%=\n\t"
      "LA36B_%=:\n\t"
      "v_mfma_f32_16x16x32_bf16 v[208:211], v[52:55], v[172:175], v[208:211]\n\t"
      "v_add_f32_e32 v229, v229, v231\n\t"
      "v_add_f32_e32 v233, v233, v235\n\t"
      "v_mfma_f32_16x16x32_bf16 v[208:211], v[68:71], v[176:179], v[208:211]\n\t"
      "s_waitcnt lgkmcnt(0)\n\t"
      "v_add_f32_e32 v236, v236, v238\n\t"
      "v_add_f32_e32 v240, v240, v242\n\t"
      "v_mfma_f32_16x16x32_bf16 v[208:211], v[84:87], v[180:183], v[208:211]\n\t"
      "v_add_f32_e32 v237, v237, v239\n\t"
      "v_add_f32_e32 v241, v241, v243\n\t"
      "v_mfma_f32_16x16x32_bf16 v[208:211], v[100:103], v[184:187], v[208:211]\n\t"
      "v_add_f32_e32 v228, v228, v232\n\t"
      "v_add_f32_e32 v236, v236, v240\n\t"
      "v_add_f32_e32 v229, v229, v233\n\t"
      "ds_write_b128 v245, v[224:227]\n\t"
      "v_mfma_f32_16x16x32_bf16 v[208:211], v[116:119], v[188:191], v[208:211]\n\t"
      "v_add_f32_e32 v237, v237, v241\n\t"
      "v_add_f32_e32 v228, v228, v248\n\t"
      "v_mfma_f32_16x16x32_bf16 v[212:215], v[8:11], v[160:163], 0\n\t"
      "v_add_f32_e32 v229, v229, v249\n\t"
      "v_add_f32_e32 v228, v228, v236\n\t"
      "v_add_f32_e32 v229, v229, v237\n\t"
      "v_mfma_f32_16x16x32_bf16 v[212:215], v[24:27], v[164:167], v[212:215]\n\t"
      "v_mul_f32_e32 v230, v228, v228\n\t"
      "v_mul_f32_e32 v231, v229, v229\n\t"
      "v_mfma_f32_16x16x32_bf16 v[212:215], v[40:43], v[168:171], v[212:215]\n\t"
      "v_fmaak_f32 v230, v252, v230, 0xc0135761\n\t"
      "v_fmaak_f32 v231, v252, v231, 0xc0135761\n\t"
      "v_mul_f32_e32 v230, v228, v230\n\t"
      "v_mfma_f32_16x16x32_bf16 v[212:215], v[56:59], v[172:175], v[212:215]\n\t"
      "v_mul_f32_e32 v231, v229, v231\n\t"
      "v_exp_f32_e32 v230, v230\n\t"
      "v_mfma_f32_16x16x32_bf16 v[212:215], v[72:75], v[176:179], v[212:215]\n\t"
      "v_exp_f32_e32 v231, v231\n\t"
      "v_add_f32_e32 v230, 1.0, v230\n\t"
      "v_add_f32_e32 v231, 1.0, v231\n\t"
      "v_mfma_f32_16x16x32_bf16 v[212:215], v[88:91], v[180:183], v[212:215]\n\t"
      "v_rcp_f32_e32 v230, v230\n\t"
      "v_rcp_f32_e32 v231, v231\n\t"
      "v_mfma_f32_16x16x32_bf16 v[212:215], v[104:107], v[184:187], v[212:215]\n\t"
      "v_mul_f32_e32 v228, v228, v230\n\t"
      "v_mul_f32_e32 v229, v229, v231\n\t"
      "v_cvt_pk_bf16_f32 v232, v228, v229\n\t"
      "ds_write_b128 v253, v[208:211] offset:1024\n\t"
      "v_mfma_f32_16x16x32_bf16 v[212:215], v[120:123], v[188:191], v[212:215]\n\t"
      "v_min_u32_e32 v254, -2, v232\n\t"
      "global_store_dword v247, v254, s[62:63] sc1\n\t"
      "s_add_u32 s62, s62, 57344\n\t"
      "s_addc_u32 s63, s63, 0\n\t"
      "v_mfma_f32_16x16x32_bf16 v[216:219], v[12:15], v[160:163], 0\n\t"
      "v_mfma_f32_16x16x32_bf16 v[216:219], v[28:31], v[164:167], v[216:219]\n\t"
      "v_mfma_f32_16x16x32_bf16 v[216:219], v[44:47], v[168:171], v[216:219]\n\t"
      "v_mfma_f32_16x16x32_bf16 v[216:219], v[60:63], v[172:175], v[216:219]\n\t"
      "v_mfma_f32_16x16x32_bf16 v[216:219], v[76:79], v[176:179], v[216:219]\n\t"
      "v_mfma_f32_16x16x32_bf16 v[216:219], v[92:95], v[180:183], v[216:219]\n\t"
      "v_mfma_f32_16x16x32_bf16 v[216:219], v[108:111], v[184:187], v[216:219]\n\t"
      "ds_write_b128 v245, v[212:215] offset:2048\n\t"
      "v_mfma_f32_16x16x32_bf16 v[216:219], v[124:127], v[188:191], v[216:219]\n\t"
      "s_nop 7\n\t"
      "ds_write_b128 v253, v[216:219] offset:3072\n\t"
      "s_waitcnt lgkmcnt(0)\n\t"
      "s_barrier\n\t"
      "ds_read_b64 v[228:229], v246\n\t"
      "ds_read_b64 v[230:231], v246 offset:4096\n\t"
      "ds_read_b64 v[232:233], v246 offset:8192\n\t"
      "ds_read_b64 v[234:235], v246 offset:12288\n\t"
      "ds_read_b64 v[236:237], v246 offset:16384\n\t"
      "ds_read_b64 v[238:239], v246 offset:20480\n\t"
      "ds_read_b64 v[240:241], v246 offset:24576\n\t"
      "ds_read_b64 v[242:243], v246 offset:28672\n\t"
      "s_waitcnt lgkmcnt(4)\n\t"
      "v_add_f32_e32 v228, v228, v230\n\t"
      "v_add_f32_e32 v232, v232, v234\n\t"
      "v_add_f32_e32 v229, v229, v231\n\t"
      "v_add_f32_e32 v233, v233, v235\n\t"
      "s_waitcnt lgkmcnt(0)\n\t"
      "v_add_f32_e32 v236, v236, v238\n\t"
      "v_add_f32_e32 v240, v240, v242\n\t"
      "v_add_f32_e32 v237, v237, v239\n\t"
      "v_add_f32_e32 v241, v241, v243\n\t"
      "v_add_f32_e32 v228, v228, v232\n\t"
      "v_add_f32_e32 v236, v236, v240\n\t"
      "v_add_f32_e32 v229, v229, v233\n\t"
      "v_add_f32_e32 v237, v237, v241\n\t"
      "v_add_f32_e32 v228, v228, v248\n\t"
      "v_add_f32_e32 v229, v229, v249\n\t"
      "v_add_f32_e32 v228, v228, v236\n\t"
      "v_add_f32_e32 v229, v229, v237\n\t"
      "v_mul_f32_e32 v230, v228, v228\n\t"
      "v_mul_f32_e32 v231, v229, v229\n\t"
      "v_fmaak_f32 v230, v252, v230, 0xc0135761\n\t"
      "v_fmaak_f32 v231, v252, v231, 0xc0135761\n\t"
      "v_mul_f32_e32 v230, v228, v230\n\t"
      "v_mul_f32_e32 v231, v229, v231\n\t"
      "v_exp_f32_e32 v230, v230\n\t"
      "v_exp_f32_e32 v231, v231\n\t"
      "v_add_f32_e32 v230, 1.0, v230\n\t"
      "v_add_f32_e32 v231, 1.0, v231\n\t"
      "v_rcp_f32_e32 v230, v230\n\t"
      "v_rcp_f32_e32 v231, v231\n\t"
      "v_mul_f32_e32 v228, v228, v230\n\t"
      "v_mul_f32_e32 v229, v229, v231\n\t"
      "v_cvt_pk_bf16_f32 v232, v228, v229\n\t"
      "v_min_u32_e32 v254, -2, v232\n\t"
      "global_store_dword v247, v254, s[62:63] sc1\n\t"
      "s_add_u32 s62, s62, 57344\n\t"
      "s_addc_u32 s63, s63, 0\n\t"
      "s_or_b32 s34, s2, s24\n\t"
      "s_cbranch_scc1 EPB_%=\n\t"
      "v_mov_b32_e32 v228, s22\n\t"
      "v_mov_b32_e32 v229, s23\n\t"
      "v_mov_b32_e32 v230, 0\n\t"
      "s_mov_b64 exec, 1\n\t"
      "global_store_dwordx2 v230, v[228:229], s[60:61] sc1\n\t"
      "s_mov_b64 exec, -1\n\t"
      "EPB_%=:\n\t"
      "s_endpgm\n\t"
      "LPATH_%=:\n\t"
      "s_add_u32 s22, s22, 1\n\t"
      "s_addc_u32 s23, s23, 0\n\t"
      "s_add_u32 s72, s26, 1\n\t"
      "s_and_b32 s72, s72, 6\n\t"
      "s_cselect_b32 s72, 0, 2\n\t"
      "s_or_b32 s27, s27, s72\n\t"
      "s_mul_i32 s72, s81, 172032\n\t"
      "s_add_u32 s72, s72, s37\n\t"
      "s_add_u32 s40, s6, s72\n\t"
      "s_addc_u32 s41, s7, 0\n\t"
      "s_add_u32 s81, s81, 1\n\t"
      "s_cmp_eq_u32 s81, 7\n\t"
      "s_cselect_b32 s81, 0, s81\n\t"
      "s_add_u32 m0, s39, 81920\n\t"
      "s_nop 0\n\t"
      "global_load_lds_dwordx4 v148, s[40:41]\n\t"
      "global_load_lds_dwordx4 v149, s[40:41] offset:1024\n\t"
      "global_load_lds_dwordx4 v150, s[40:41] offset:2048\n\t"
      "global_load_lds_dwordx4 v151, s[40:41] offset:3072\n\t"
      "s_mul_i32 s72, s81, 172032\n\t"
      "s_add_u32 s72, s72, s37\n\t"
      "s_add_u32 s40, s6, s72\n\t"
      "s_addc_u32 s41, s7, 0\n\t"
      "s_add_u32 s81, s81, 1\n\t"
      "s_cmp_eq_u32 s81, 7\n\t"
      "s_cselect_b32 s81, 0, s81\n\t"
      "s_add_u32 m0, s39, 98304\n\t"
      "s_nop 0\n\t"
      "global_load_lds_dwordx4 v148, s[40:41]\n\t"
      "global_load_lds_dwordx4 v149, s[40:41] offset:1024\n\t"
      "s_waitcnt vmcnt(14)\n\t"
      "s_barrier\n\t"
      "v_add_u32_e32 v172, 0x10000, v224\n\t"
      "v_add_u32_e32 v173, 0x10000, v225\n\t"
      "v_add_u32_e32 v174, 0x10000, v226\n\t"
      "v_add_u32_e32 v175, 0x10000, v227\n\t"
      "s_lshl_b32 s34, s25, 8\n\t"
      "s_add_u32 s34, s34, 4096\n\t"
      "v_and_b32_e32 v136, 1, v192\n\t"
      "v_lshl_add_u32 v136, v136, 7, s34\n\t"
      "s_lshl_b32 s34, s25, 7\n\t"
      "s_add_u32 s34, s34, 16384\n\t"
      "v_mov_b32_e32 v137, s34\n\t"
      "s_lshl_b32 s34, s2, 7\n\t"
      "s_add_u32 s34, s34, 32768\n\t"
      "v_mov_b32_e32 v143, s34\n\t"
      "s_bitcmp0_b32 s27, 1\n\t"
      "s_cbranch_scc1 SK6_%=\n\t"
      "s_lshr_b32 s73, s26, 2\n\t"
      "s_lshl1_add_u32 s73, s25, s73\n\t"
      "s_lshl_b32 s72, s73, 7\n\t"
      "s_add_u32 s72, s72, 4096\n\t"
      "v_mov_b32_e32 v130, s72\n\t"
      "s_lshl2_add_u32 s75, s73, s24\n\t"
      "v_mov_b32_e32 v128, s22\n\t"
      "v_mov_b32_e32 v129, s23\n\t"
      "v_lshrrev_b32_e32 v140, 1, v244\n\t"
      "s_lshl_b32 s72, s75, 9\n\t"
      "s_add_u32 s72, s72, 12582912\n\t"
      "v_mov_b32_e32 v132, -1\n\t"
      "v_mov_b32_e32 v133, -1\n\t"
      "v_mov_b32_e32 v134, -1\n\t"
      "v_mov_b32_e32 v135, -1\n\t"
      "s_add_u32 s42, s20, s72\n\t"
      "s_addc_u32 s43, s21, 0\n\t"
      "global_store_dwordx2 v140, v[132:133], s[42:43] sc1\n\t"
      "s_lshl_b32 s72, s75, 13\n\t"
      "s_add_u32 s72, s72, 4096\n\t"
      "s_add_u32 s42, s20, s72\n\t"
      "s_addc_u32 s43, s21, 0\n\t"
      "global_store_dwordx4 v244, v[132:135], s[42:43] offset:-4096 sc1\n\t"
      "global_store_dwordx4 v244, v[132:135], s[42:43] offset:-3072 sc1\n\t"
      "global_store_dwordx4 v244, v[132:135], s[42:43] offset:-2048 sc1\n\t"
      "SK6_%=:\n\t"
      "s_waitcnt vmcnt(10)\n\t"
      "s_barrier\n\t"
      "global_load_lds_dwordx4 v150, s[40:41] offset:2048\n\t"
      "global_load_lds_dwordx4 v151, s[40:41] offset:3072\n\t"
      "s_bitcmp0_b32 s27, 1\n\t"
      "s_cbranch_scc1 SK7_%=\n\t"
      "global_store_dwordx4 v244, v[132:135], s[42:43] offset:-1024 sc1\n\t"
      "global_store_dwordx4 v244, v[132:135], s[42:43] sc1\n\t"
      "global_store_dwordx4 v244, v[132:135], s[42:43] offset:1024 sc1\n\t"
      "SK7_%=:\n\t"
      "s_waitcnt vmcnt(8)\n\t"
      "s_barrier\n\t"
      "s_mul_i32 s72, s81, 172032\n\t"
      "s_add_u32 s72, s72, s37\n\t"
      "s_add_u32 s40, s6, s72\n\t"
      "s_addc_u32 s41, s7, 0\n\t"
      "s_add_u32 s81, s81, 1\n\t"
      "s_cmp_eq_u32 s81, 7\n\t"
      "s_cselect_b32 s81, 0, s81\n\t"
      "s_add_u32 m0, s39, 114688\n\t"
      "s_nop 0\n\t"
      "global_load_lds_dwordx4 v148, s[40:41]\n\t"
      "global_load_lds_dwordx4 v149, s[40:41] offset:1024\n\t"
      "global_load_lds_dwordx4 v150, s[40:41] offset:2048\n\t"
      "global_load_lds_dwordx4 v151, s[40:41] offset:3072\n\t"
      "s_bitcmp0_b32 s27, 1\n\t"
      "s_cbranch_scc1 SK8_%=\n\t"
      "global_store_dwordx4 v244, v[132:135], s[42:43] offset:2048 sc1\n\t"
      "global_store_dwordx4 v244, v[132:135], s[42:43] offset:3072 sc1\n\t"
      "SK8_%=:\n\t"
      "s_waitcnt vmcnt(8)\n\t"
      "s_barrier\n\t"
      "s_mul_i32 s72, s81, 172032\n\t"
      "s_add_u32 s72, s72, s37\n\t"
      "s_add_u32 s40, s6, s72\n\t"
      "s_addc_u32 s41, s7, 0\n\t"
      "s_add_u32 s81, s81, 1\n\t"
      "s_cmp_eq_u32 s81, 7\n\t"
      "s_cselect_b32 s81, 0, s81\n\t"
      "s_add_u32 m0, s39, 131072\n\t"
      "s_nop 0\n\t"
      "global_load_lds_dwordx4 v148, s[40:41]\n\t"
      "global_load_lds_dwordx4 v149, s[40:41] offset:1024\n\t"
      "global_load_lds_dwordx4 v150, s[40:41] offset:2048\n\t"
      "global_load_lds_dwordx4 v151, s[40:41] offset:3072\n\t"
      "s_waitcnt vmcnt(8)\n\t"
      "s_bitcmp0_b32 s27, 1\n\t"
      "s_cbranch_scc1 SK9_%=\n\t"
      "s_waitcnt vmcnt(4)\n\t"
      "SK9_%=:\n\t"
      "s_barrier\n\t"
      "s_mul_i32 s72, s81, 172032\n\t"
      "s_add_u32 s72, s72, s37\n\t"
      "s_add_u32 s40, s6, s72\n\t"
      "s_addc_u32 s41, s7, 0\n\t"
      "s_add_u32 s81, s81, 1\n\t"
      "s_cmp_eq_u32 s81, 7\n\t"
      "s_cselect_b32 s81, 0, s81\n\t"
      "s_add_u32 m0, s39, 147456\n\t"
      "s_nop 0\n\t"
      "global_load_lds_dwordx4 v148, s[40:41]\n\t"
      "global_load_lds_dwordx4 v149, s[40:41] offset:1024\n\t"
      "global_load_lds_dwordx4 v150, s[40:41] offset:2048\n\t"
      "global_load_lds_dwordx4 v151, s[40:41] offset:3072\n\t"
      "s_bitcmp0_b32 s27, 1\n\t"
      "s_cbranch_scc1 SK10_%=\n\t"
      "s_cmp_eq_u32 s24, 0\n\t"
      "s_cselect_b64 exec, 1, 0\n\t"
      "global_store_dwordx2 v130, v[128:129], s[60:61] sc1\n\t"
      "s_mov_b64 exec, -1\n\t"
      "s_mov_b32 m0, 64512\n\t"
      "s_mul_i32 s72, s75, 13\n\t"
      "v_min_u32_e32 v141, 12, v192\n\t"
      "v_add_u32_e32 v141, s72, v141\n\t"
      "v_min_u32_e32 v141, 3135, v141\n\t"
      "v_lshlrev_b32_e32 v141, 7, v141\n\t"
      "global_load_lds_dword v141, s[8:9]\n\t"
      "SK10_%=:\n\t"
      "s_waitcnt vmcnt(8)\n\t"
      "s_barrier\n\t"
      "s_mul_i32 s72, s82, 172032\n\t"
      "s_add_u32 s72, s72, s37\n\t"
      "s_add_u32 s72, s72, 1792\n\t"
      "s_add_u32 s40, s6, s72\n\t"
      "s_addc_u32 s41, s7, 0\n\t"
      "s_add_u32 s82, s82, 1\n\t"
      "s_cmp_eq_u32 s82, 7\n\t"
      "s_cselect_b32 s82, 0, s82\n\t"
      "s_add_u32 m0, s39, 65536\n\t"
      "s_nop 0\n\t"
      "global_load_lds_dwordx4 v148, s[40:41]\n\t"
      "global_load_lds_dwordx4 v152, s[40:41] offset:1024\n\t"
      "s_mul_i32 s72, s82, 172032\n\t"
      "s_add_u32 s72, s72, s37\n\t"
      "s_add_u32 s72, s72, 1792\n\t"
      "s_add_u32 s40, s6, s72\n\t"
      "s_addc_u32 s41, s7, 0\n\t"
      "s_add_u32 s82, s82, 1\n\t"
      "s_cmp_eq_u32 s82, 7\n\t"
      "s_cselect_b32 s82, 0, s82\n\t"
      "s_add_u32 m0, s39, 67584\n\t"
      "s_nop 0\n\t"
      "global_load_lds_dwordx4 v148, s[40:41]\n\t"
      "global_load_lds_dwordx4 v152, s[40:41] offset:1024\n\t"
      "s_waitcnt vmcnt(8)\n\t"
      "s_barrier\n\t"
      "s_mul_i32 s72, s82, 172032\n\t"
      "s_add_u32 s72, s72, s37\n\t"
      "s_add_u32 s72, s72, 1792\n\t"
      "s_add_u32 s40, s6, s72\n\t"
      "s_addc_u32 s41, s7, 0\n\t"
      "s_add_u32 s82, s82, 1\n\t"
      "s_cmp_eq_u32 s82, 7\n\t"
      "s_cselect_b32 s82, 0, s82\n\t"
      "s_add_u32 m0, s39, 81920\n\t"
      "s_nop 0\n\t"
      "global_load_lds_dwordx4 v148, s[40:41]\n\t"
      "global_load_lds_dwordx4 v152, s[40:41] offset:1024\n\t"
      "s_mul_i32 s72, s82, 172032\n\t"
      "s_add_u32 s72, s72, s37\n\t"
      "s_add_u32 s72, s72, 1792\n\t"
      "s_add_u32 s40, s6, s72\n\t"
      "s_addc_u32 s41, s7, 0\n\t"
      "s_add_u32 s82, s82, 1\n\t"
      "s_cmp_eq_u32 s82, 7\n\t"
      "s_cselect_b32 s82, 0, s82\n\t"
      "s_add_u32 m0, s39, 83968\n\t"
      "s_nop 0\n\t"
      "global_load_lds_dwordx4 v148, s[40:41]\n\t"
      "global_load_lds_dwordx4 v152, s[40:41] offset:1024\n\t"
      "s_waitcnt vmcnt(8)\n\t"
      "s_barrier\n\t"
      "s_cmp_lg_u32 s24, 3\n\t"
      "s_cbranch_scc1 SK11_%=\n\t"
      "global_load_dwordx2 v[138:139], v136, s[60:61] sc1\n\t"
      "SK11_%=:\n\t"
      "s_mul_i32 s72, s82, 172032\n\t"
      "s_add_u32 s72, s72, s37\n\t"
      "s_add_u32 s72, s72, 1792\n\t"
      "s_add_u32 s40, s6, s72\n\t"
      "s_addc_u32 s41, s7, 0\n\t"
      "s_add_u32 s82, s82, 1\n\t"
      "s_cmp_eq_u32 s82, 7\n\t"
      "s_cselect_b32 s82, 0, s82\n\t"
      "s_lshr_b32 s72, s39, 1\n\t"
      "s_add_u32 m0, s72, 98304\n\t"
      "s_nop 0\n\t"
      "global_load_lds_dwordx4 v148, s[40:41]\n\t"
      "global_load_lds_dwordx4 v152, s[40:41] offset:1024\n\t"
      "global_load_dword v249, v248, s[12:13]\n\t"
      "s_lshl2_add_u32 s72, s31, s24\n\t"
      "s_and_b32 s73, s72, 0x70\n\t"
      "s_add_u32 s72, s72, s73\n\t"
      "s_mov_b32 m0, 64512\n\t"
      "s_mul_i32 s72, s72, 28672\n\t"
      "s_and_b32 s73, s2, 7\n\t"
      "s_mul_i32 s73, s73, 448\n\t"
      "s_and_b32 s73, s73, 0xffffff80\n\t"
      "s_add_u32 s72, s72, s73\n\t"
      "v_bfe_u32 v141, v192, 2, 3\n\t"
      "s_cmp_ge_u32 s31, 28\n\t"
      "s_cselect_b32 s73, 32, 0\n\t"
      "v_and_b32_e32 v142, s73, v192\n\t"
      "v_lshl_or_b32 v141, v142, 2, v141\n\t"
      "v_mul_u32_u24_e32 v141, 3584, v141\n\t"
      "v_lshl_add_u32 v141, v196, 7, v141\n\t"
      "v_add_u32_e32 v141, s72, v141\n\t"
      "global_load_lds_dword v141, s[10:11]\n\t"
      "s_waitcnt vmcnt(8)\n\t"
      "s_barrier\n\t"
      "s_mul_i32 s72, s24, 14336\n\t"
      "s_add_u32 s40, s56, s72\n\t"
      "s_addc_u32 s41, s57, 0\n\t"
      "s_lshr_b32 s72, s39, 1\n\t"
      "s_add_u32 m0, s72, 106496\n\t"
      "s_nop 0\n\t"
      "global_load_lds_dwordx4 v154, s[40:41]\n\t"
      "global_load_lds_dwordx4 v155, s[40:41] offset:1024\n\t"
      "s_waitcnt vmcnt(6)\n\t"
      "s_barrier\n\t"
      "s_mul_i32 s72, s24, 28672\n\t"
      "s_add_u32 s72, s72, 57344\n\t"
      "s_add_u32 s40, s56, s72\n\t"
      "s_addc_u32 s41, s57, 0\n\t"
      "s_lshr_b32 s72, s39, 0\n\t"
      "s_add_u32 m0, s72, 114688\n\t"
      "s_nop 0\n\t"
      "global_load_lds_dwordx4 v154, s[40:41]\n\t"
      "global_load_lds_dwordx4 v155, s[40:41] offset:1024\n\t"
      "global_load_lds_dwordx4 v156, s[40:41] offset:2048\n\t"
      "global_load_lds_dwordx4 v157, s[40:41] offset:3072\n\t"
      "s_waitcnt vmcnt(8)\n\t"
      "s_cmp_lg_u32 s24, 3\n\t"
      "s_cbranch_scc1 LF1B_%=\n\t"
      "v_cmp_ne_u64_e32 vcc, s[22:23], v[138:139]\n\t"
      "s_cbranch_vccnz LF1R_%=\n\t"
      "LF1B_%=:\n\t"
      "s_barrier\n\t"
      "s_mul_i32 s72, s24, 28672\n\t"
      "s_add_u32 s72, s72, 172032\n\t"
      "s_add_u32 s40, s56, s72\n\t"
      "s_addc_u32 s41, s57, 0\n\t"
      "s_lshr_b32 s72, s39, 0\n\t"
      "s_add_u32 m0, s72, 131072\n\t"
      "s_nop 0\n\t"
      "global_load_lds_dwordx4 v154, s[40:41]\n\t"
      "global_load_lds_dwordx4 v155, s[40:41] offset:1024\n\t"
      "global_load_lds_dwordx4 v156, s[40:41] offset:2048\n\t"
      "global_load_lds_dwordx4 v157, s[40:41] offset:3072\n\t"
      "s_mul_i32 s72, s24, 28672\n\t"
      "s_add_u32 s72, s72, 286720\n\t"
      "s_add_u32 s40, s56, s72\n\t"
      "s_addc_u32 s41, s57, 0\n\t"
      "s_lshr_b32 s72, s39, 0\n\t"
      "s_add_u32 m0, s72, 147456\n\t"
      "s_nop 0\n\t"
      "global_load_lds_dwordx4 v154, s[40:41]\n\t"
      "global_load_lds_dwordx4 v155, s[40:41] offset:1024\n\t"
      "global_load_lds_dwordx4 v156, s[40:41] offset:2048\n\t"
      "global_load_lds_dwordx4 v157, s[40:41] offset:3072\n\t"
      "s_barrier\n\t"
      "s_cmp_eq_u32 s26, 7\n\t"
      "s_cbranch_scc1 SPARE_%=\n\t"
      "s_lshl_b32 s72, s24, 10\n\t"
      "s_add_u32 s72, s72, 98304\n\t"
      "v_xor_b32_e32 v203, v197, v198\n\t"
      "v_lshl_add_u32 v203, v203, 4, s72\n\t"
      "v_lshl_add_u32 v203, v197, 7, v203\n\t"
      "ds_write_b128 v203, v[204:207]\n\t"
      "s_waitcnt lgkmcnt(0)\n\t"
      "s_bitcmp0_b32 s27, 0\n\t"
      "s_cbranch_scc1 L4_%=\n\t"
      "s_add_u32 s72, s76, 0\n\t"
      "s_add_u32 s40, s52, s72\n\t"
      "s_addc_u32 s41, s53, 0\n\t"
      "s_add_u32 m0, s39, 65536\n\t"
      "s_nop 0\n\t"
      "global_load_lds_dwordx4 v144, s[40:41]\n\t"
      "global_load_lds_dwordx4 v145, s[40:41] offset:1024\n\t"
      "global_load_lds_dwordx4 v146, s[40:41] offset:2048\n\t"
      "global_load_lds_dwordx4 v147, s[40:41] offset:3072\n\t"
      "s_add_u32 s72, s76, 229376\n\t"
      "s_add_u32 s40, s52, s72\n\t"
      "s_addc_u32 s41, s53, 0\n\t"
      "s_add_u32 m0, s39, 81920\n\t"
      "s_nop 0\n\t"
      "global_load_lds_dwordx4 v144, s[40:41]\n\t"
      "global_load_lds_dwordx4 v145, s[40:41] offset:1024\n\t"
      "global_load_lds_dwordx4 v146, s[40:41] offset:2048\n\t"
      "global_load_lds_dwordx4 v147, s[40:41] offset:3072\n\t"
      "s_waitcnt vmcnt(8)\n\t"
      "s_barrier\n\t"
      "s_barrier\n\t"
      "s_add_u32 s72, s76, 3670016\n\t"
      "s_add_u32 s40, s52, s72\n\t"
      "s_addc_u32 s41, s53, 0\n\t"
      "s_add_u32 m0, s39, 16384\n\t"
      "s_nop 0\n\t"
      "global_load_lds_dwordx4 v144, s[40:41]\n\t"
      "global_load_lds_dwordx4 v145, s[40:41] offset:1024\n\t"
      "global_load_lds_dwordx4 v146, s[40:41] offset:2048\n\t"
      "global_load_lds_dwordx4 v147, s[40:41] offset:3072\n\t"
      "s_add_u32 s72, s76, 3899392\n\t"
      "s_add_u32 s40, s52, s72\n\t"
      "s_addc_u32 s41, s53, 0\n\t"
      "s_add_u32 m0, s39, 49152\n\t"
      "s_nop 0\n\t"
      "global_load_lds_dwordx4 v144, s[40:41]\n\t"
      "global_load_lds_dwordx4 v145, s[40:41] offset:1024\n\t"
      "global_load_lds_dwordx4 v146, s[40:41] offset:2048\n\t"
      "global_load_lds_dwordx4 v147, s[40:41] offset:3072\n\t"
      "s_waitcnt vmcnt(12)\n\t"
      "ds_read_b64_tr_b16 v[0:1], v172\n\t"
      "ds_read_b64_tr_b16 v[2:3], v172 offset:512\n\t"
      "ds_read_b64_tr_b16 v[4:5], v173\n\t"
      "ds_read_b64_tr_b16 v[6:7], v173 offset:512\n\t"
      "ds_read_b64_tr_b16 v[8:9], v174\n\t"
      "ds_read_b64_tr_b16 v[10:11], v174 offset:512\n\t"
      "ds_read_b64_tr_b16 v[12:13], v175\n\t"
      "ds_read_b64_tr_b16 v[14:15], v175 offset:512\n\t"
      "s_waitcnt lgkmcnt(0)\n\t"
      "s_add_u32 s72, s77, 0\n\t"
      "s_add_u32 s40, s52, s72\n\t"
      "s_addc_u32 s41, s53, 0\n\t"
      "s_add_u32 m0, s39, 65536\n\t"
      "s_nop 0\n\t"
      "global_load_lds_dwordx4 v144, s[40:41]\n\t"
      "global_load_lds_dwordx4 v145, s[40:41] offset:1024\n\t"
      "global_load_lds_dwordx4 v146, s[40:41] offset:2048\n\t"
      "global_load_lds_dwordx4 v147, s[40:41] offset:3072\n\t"
      "s_waitcnt vmcnt(12)\n\t"
      "ds_read_b64_tr_b16 v[16:17], v172 offset:16384\n\t"
      "ds_read_b64_tr_b16 v[18:19], v172 offset:16896\n\t"
      "ds_read_b64_tr_b16 v[20:21], v173 offset:16384\n\t"
      "ds_read_b64_tr_b16 v[22:23], v173 offset:16896\n\t"
      "ds_read_b64_tr_b16 v[24:25], v174 offset:16384\n\t"
      "ds_read_b64_tr_b16 v[26:27], v174 offset:16896\n\t"
      "ds_read_b64_tr_b16 v[28:29], v175 offset:16384\n\t"
      "ds_read_b64_tr_b16 v[30:31], v175 offset:16896\n\t"
      "s_waitcnt lgkmcnt(0)\n\t"
      "s_add_u32 s72, s77, 229376\n\t"
      "s_add_u32 s40, s52, s72\n\t"
      "s_addc_u32 s41, s53, 0\n\t"
      "s_add_u32 m0, s39, 81920\n\t"
      "s_nop 0\n\t"
      "global_load_lds_dwordx4 v144, s[40:41]\n\t"
      "global_load_lds_dwordx4 v145, s[40:41] offset:1024\n\t"
      "global_load_lds_dwordx4 v146, s[40:41] offset:2048\n\t"
      "global_load_lds_dwordx4 v147, s[40:41] offset:3072\n\t"
      "s_add_u32 s72, s77, 3670016\n\t"
      "s_add_u32 s40, s52, s72\n\t"
      "s_addc_u32 s41, s53, 0\n\t"
      "s_add_u32 m0, s39, 0\n\t"
      "s_nop 0\n\t"
      "global_load_lds_dwordx4 v144, s[40:41]\n\t"
      "global_load_lds_dwordx4 v145, s[40:41] offset:1024\n\t"
      "global_load_lds_dwordx4 v146, s[40:41] offset:2048\n\t"
      "global_load_lds_dwordx4 v147, s[40:41] offset:3072\n\t"
      "s_add_u32 s72, s77, 3899392\n\t"
      "s_add_u32 s40, s52, s72\n\t"
      "s_addc_u32 s41, s53, 0\n\t"
      "s_add_u32 m0, s39, 32768\n\t"
      "s_nop 0\n\t"
      "global_load_lds_dwordx4 v144, s[40:41]\n\t"
      "global_load_lds_dwordx4 v145, s[40:41] offset:1024\n\t"
      "global_load_lds_dwordx4 v146, s[40:41] offset:2048\n\t"
      "global_load_lds_dwordx4 v147, s[40:41] offset:3072\n\t"
      "s_waitcnt vmcnt(12)\n\t"
      "ds_read_b64_tr_b16 v[32:33], v172\n\t"
      "ds_read_b64_tr_b16 v[34:35], v172 offset:512\n\t"
      "ds_read_b64_tr_b16 v[36:37], v173\n\t"
      "ds_read_b64_tr_b16 v[38:39], v173 offset:512\n\t"
      "ds_read_b64_tr_b16 v[40:41], v174\n\t"
      "ds_read_b64_tr_b16 v[42:43], v174 offset:512\n\t"
      "ds_read_b64_tr_b16 v[44:45], v175\n\t"
      "ds_read_b64_tr_b16 v[46:47], v175 offset:512\n\t"
      "s_waitcnt lgkmcnt(0)\n\t"
      "s_add_u32 s72, s78, 0\n\t"
      "s_add_u32 s40, s52, s72\n\t"
      "s_addc_u32 s41, s53, 0\n\t"
      "s_add_u32 m0, s39, 65536\n\t"
      "s_nop 0\n\t"
      "global_load_lds_dwordx4 v144, s[40:41]\n\t"
      "global_load_lds_dwordx4 v145, s[40:41] offset:1024\n\t"
      "global_load_lds_dwordx4 v146, s[40:41] offset:2048\n\t"
      "global_load_lds_dwordx4 v147, s[40:41] offset:3072\n\t"
      "s_waitcnt vmcnt(12)\n\t"
      "ds_read_b64_tr_b16 v[48:49], v172 offset:16384\n\t"
      "ds_read_b64_tr_b16 v[50:51], v172 offset:16896\n\t"
      "ds_read_b64_tr_b16 v[52:53], v173 offset:16384\n\t"
      "ds_read_b64_tr_b16 v[54:55], v173 offset:16896\n\t"
      "ds_read_b64_tr_b16 v[56:57], v174 offset:16384\n\t"
      "ds_read_b64_tr_b16 v[58:59], v174 offset:16896\n\t"
      "ds_read_b64_tr_b16 v[60:61], v175 offset:16384\n\t"
      "ds_read_b64_tr_b16 v[62:63], v175 offset:16896\n\t"
      "s_waitcnt lgkmcnt(0)\n\t"
      "s_add_u32 s72, s78, 229376\n\t"
      "s_add_u32 s40, s52, s72\n\t"
      "s_addc_u32 s41, s53, 0\n\t"
      "s_add_u32 m0, s39, 81920\n\t"
      "s_nop 0\n\t"
      "global_load_lds_dwordx4 v144, s[40:41]\n\t"
      "global_load_lds_dwordx4 v145, s[40:41] offset:1024\n\t"
      "global_load_lds_dwordx4 v146, s[40:41] offset:2048\n\t"
      "global_load_lds_dwordx4 v147, s[40:41] offset:3072\n\t"
      "s_waitcnt vmcnt(8)\n\t"
      "s_barrier\n\t"
      "s_cmp_lg_u32 s24, 3\n\t"
      "s_cbranch_scc1 SK13_%=\n\t"
      "global_load_dwordx2 v[138:139], v137, s[60:61] sc1\n\t"
      "SK13_%=:\n\t"
      "s_add_u32 s72, s79, 0\n\t"
      "s_add_u32 s40, s52, s72\n\t"
      "s_addc_u32 s41, s53, 0\n\t"
      "s_add_u32 m0, s39, 98304\n\t"
      "s_nop 0\n\t"
      "global_load_lds_dwordx4 v144, s[40:41]\n\t"
      "global_load_lds_dwordx4 v145, s[40:41] offset:1024\n\t"
      "global_load_lds_dwordx4 v146, s[40:41] offset:2048\n\t"
      "global_load_lds_dwordx4 v147, s[40:41] offset:3072\n\t"
      "s_add_u32 s72, s79, 229376\n\t"
      "s_add_u32 s40, s52, s72\n\t"
      "s_addc_u32 s41, s53, 0\n\t"
      "s_add_u32 m0, s39, 114688\n\t"
      "s_nop 0\n\t"
      "global_load_lds_dwordx4 v144, s[40:41]\n\t"
      "global_load_lds_dwordx4 v145, s[40:41] offset:1024\n\t"
      "global_load_lds_dwordx4 v146, s[40:41] offset:2048\n\t"
      "global_load_lds_dwordx4 v147, s[40:41] offset:3072\n\t"
      "s_cmp_lg_u32 s24, 3\n\t"
      "s_cbranch_scc1 LF2B_%=\n\t"
      "s_waitcnt vmcnt(8)\n\t"
      "v_cmp_ne_u64_e32 vcc, s[22:23], v[138:139]\n\t"
      "s_cbranch_vccnz LF2R_%=\n\t"
      "LF2B_%=:\n\t"
      "s_barrier\n\t"
      "s_add_u32 s72, s78, 3670016\n\t"
      "s_add_u32 s40, s52, s72\n\t"
      "s_addc_u32 s41, s53, 0\n\t"
      "s_add_u32 m0, s39, 16384\n\t"
      "s_nop 0\n\t"
      "global_load_lds_dwordx4 v144, s[40:41]\n\t"
      "global_load_lds_dwordx4 v145, s[40:41] offset:1024\n\t"
      "global_load_lds_dwordx4 v146, s[40:41] offset:2048\n\t"
      "global_load_lds_dwordx4 v147, s[40:41] offset:3072\n\t"
      "s_add_u32 s72, s78, 3899392\n\t"
      "s_add_u32 s40, s52, s72\n\t"
      "s_addc_u32 s41, s53, 0\n\t"
      "s_add_u32 m0, s39, 49152\n\t"
      "s_nop 0\n\t"
      "global_load_lds_dwordx4 v144, s[40:41]\n\t"
      "global_load_lds_dwordx4 v145, s[40:41] offset:1024\n\t"
      "global_load_lds_dwordx4 v146, s[40:41] offset:2048\n\t"
      "global_load_lds_dwordx4 v147, s[40:41] offset:3072\n\t"
      "s_add_u32 s72, s79, 3670016\n\t"
      "s_add_u32 s40, s52, s72\n\t"
      "s_addc_u32 s41, s53, 0\n\t"
      "s_add_u32 m0, s39, 0\n\t"
      "s_nop 0\n\t"
      "global_load_lds_dwordx4 v144, s[40:41]\n\t"
      "global_load_lds_dwordx4 v145, s[40:41] offset:1024\n\t"
      "global_load_lds_dwordx4 v146, s[40:41] offset:2048\n\t"
      "global_load_lds_dwordx4 v147, s[40:41] offset:3072\n\t"
      "s_add_u32 s72, s79, 3899392\n\t"
      "s_add_u32 s40, s52, s72\n\t"
      "s_addc_u32 s41, s53, 0\n\t"
      "s_add_u32 m0, s39, 32768\n\t"
      "s_nop 0\n\t"
      "global_load_lds_dwordx4 v144, s[40:41]\n\t"
      "global_load_lds_dwordx4 v145, s[40:41] offset:1024\n\t"
      "global_load_lds_dwordx4 v146, s[40:41] offset:2048\n\t"
      "global_load_lds_dwordx4 v147, s[40:41] offset:3072\n\t"
      "s_cmp_lg_u32 s24, 3\n\t"
      "s_cbranch_scc1 SK14_%=\n\t"
      "global_load_dwordx2 v[138:139], v143, s[60:61] sc1\n\t"
      "SK14_%=:\n\t"
      "s_waitcnt vmcnt(28)\n\t"
      "ds_read_b64_tr_b16 v[64:65], v172\n\t"
      "ds_read_b64_tr_b16 v[66:67], v172 offset:512\n\t"
      "ds_read_b64_tr_b16 v[68:69], v173\n\t"
      "ds_read_b64_tr_b16 v[70:71], v173 offset:512\n\t"
      "ds_read_b64_tr_b16 v[72:73], v174\n\t"
      "ds_read_b64_tr_b16 v[74:75], v174 offset:512\n\t"
      "ds_read_b64_tr_b16 v[76:77], v175\n\t"
      "ds_read_b64_tr_b16 v[78:79], v175 offset:512\n\t"
      "s_waitcnt vmcnt(24)\n\t"
      "ds_read_b64_tr_b16 v[80:81], v172 offset:16384\n\t"
      "ds_read_b64_tr_b16 v[82:83], v172 offset:16896\n\t"
      "ds_read_b64_tr_b16 v[84:85], v173 offset:16384\n\t"
      "ds_read_b64_tr_b16 v[86:87], v173 offset:16896\n\t"
      "ds_read_b64_tr_b16 v[88:89], v174 offset:16384\n\t"
      "ds_read_b64_tr_b16 v[90:91], v174 offset:16896\n\t"
      "ds_read_b64_tr_b16 v[92:93], v175 offset:16384\n\t"
      "ds_read_b64_tr_b16 v[94:95], v175 offset:16896\n\t"
      "s_waitcnt vmcnt(20)\n\t"
      "ds_read_b64_tr_b16 v[96:97], v172 offset:32768\n\t"
      "ds_read_b64_tr_b16 v[98:99], v172 offset:33280\n\t"
      "ds_read_b64_tr_b16 v[100:101], v173 offset:32768\n\t"
      "ds_read_b64_tr_b16 v[102:103], v173 offset:33280\n\t"
      "ds_read_b64_tr_b16 v[104:105], v174 offset:32768\n\t"
      "ds_read_b64_tr_b16 v[106:107], v174 offset:33280\n\t"
      "ds_read_b64_tr_b16 v[108:109], v175 offset:32768\n\t"
      "ds_read_b64_tr_b16 v[110:111], v175 offset:33280\n\t"
      "s_waitcnt vmcnt(16)\n\t"
      "ds_read_b64_tr_b16 v[112:113], v172 offset:49152\n\t"
      "ds_read_b64_tr_b16 v[114:115], v172 offset:49664\n\t"
      "ds_read_b64_tr_b16 v[116:117], v173 offset:49152\n\t"
      "ds_read_b64_tr_b16 v[118:119], v173 offset:49664\n\t"
      "ds_read_b64_tr_b16 v[120:121], v174 offset:49152\n\t"
      "ds_read_b64_tr_b16 v[122:123], v174 offset:49664\n\t"
      "ds_read_b64_tr_b16 v[124:125], v175 offset:49152\n\t"
      "ds_read_b64_tr_b16 v[126:127], v175 offset:49664\n\t"
      "s_waitcnt vmcnt(0)\n\t"
      "s_cmp_lg_u32 s24, 3\n\t"
      "s_cbranch_scc1 LF3B_%=\n\t"
      "s_waitcnt vmcnt(0)\n\t"
      "v_cmp_ne_u64_e32 vcc, s[22:23], v[138:139]\n\t"
      "s_cbranch_vccnz LF3R_%=\n\t"
      "LF3B_%=:\n\t"
      "s_waitcnt lgkmcnt(0)\n\t"
      "s_barrier\n\t"
      "s_mul_i32 s34, s32, 196608\n\t"
      "s_mul_i32 s35, s24, 8192\n\t"
      "s_add_u32 s34, s34, s35\n\t"
      "s_add_u32 s34, s34, 1048576\n\t"
      "s_add_u32 s64, s48, s34\n\t"
      "s_addc_u32 s65, s49, 0\n\t"
      "v_add_u32_e32 v250, 4096, v244\n\t"
      "global_load_dwordx4 v[128:131], v250, s[64:65] offset:-4096 sc1\n\t"
      "global_load_dwordx4 v[132:135], v250, s[64:65] offset:-3072 sc1\n\t"
      "global_load_dwordx4 v[136:139], v250, s[64:65] offset:-2048 sc1\n\t"
      "global_load_dwordx4 v[140:143], v250, s[64:65] offset:-1024 sc1\n\t"
      "global_load_dwordx4 v[144:147], v250, s[64:65] sc1\n\t"
      "global_load_dwordx4 v[148:151], v250, s[64:65] offset:1024 sc1\n\t"
      "global_load_dwordx4 v[152:155], v250, s[64:65] offset:2048 sc1\n\t"
      "global_load_dwordx4 v[156:159], v250, s[64:65] offset:3072 sc1\n\t"
      "s_waitcnt vmcnt(0) lgkmcnt(0)\n\t"
      "s_branch GST_%=\n\t"
      "G4C_%=:\n\t"
      "global_store_dwordx4 v241, v[208:211], s[20:21] offset:-2048 sc1\n\t"
      "global_store_dwordx4 v241, v[212:215], s[20:21] offset:2048 sc1\n\t"
      "s_setprio 0\n\t"
      "G4_%=:\n\t"
      "s_mul_i32 s34, s32, 401408\n\t"
      "s_mul_i32 s35, s24, 7168\n\t"
      "s_add_u32 s34, s34, s35\n\t"
      "s_add_u32 s34, s34, 1835008\n\t"
      "s_add_u32 s64, s48, s34\n\t"
      "s_addc_u32 s65, s49, 0\n\t"
      "s_and_b32 s34, s30, 3\n\t"
      "s_lshl_b32 s34, s34, 11\n\t"
      "s_add_u32 s40, s34, 0\n\t"
      "s_sub_u32 s35, s40, 7168\n\t"
      "s_min_u32 s40, s40, s35\n\t"
      "s_add_u32 s41, s34, 1024\n\t"
      "s_sub_u32 s35, s41, 7168\n\t"
      "s_min_u32 s41, s41, s35\n\t"
      "s_add_u32 s42, s34, 2048\n\t"
      "s_sub_u32 s35, s42, 7168\n\t"
      "s_min_u32 s42, s42, s35\n\t"
      "s_add_u32 s43, s34, 3072\n\t"
      "s_sub_u32 s35, s43, 7168\n\t"
      "s_min_u32 s43, s43, s35\n\t"
      "s_add_u32 s44, s34, 4096\n\t"
      "s_sub_u32 s35, s44, 7168\n\t"
      "s_min_u32 s44, s44, s35\n\t"
      "s_add_u32 s45, s34, 5120\n\t"
      "s_sub_u32 s35, s45, 7168\n\t"
      "s_min_u32 s45, s45, s35\n\t"
      "s_add_u32 s46, s34, 6144\n\t"
      "s_sub_u32 s35, s46, 7168\n\t"
      "s_min_u32 s46, s46, s35\n\t"
      "s_add_u32 s66, s64, 57344\n\t"
      "s_addc_u32 s67, s65, 0\n\t"
      "s_add_u32 s68, s66, 57344\n\t"
      "s_addc_u32 s69, s67, 0\n\t"
      "s_add_u32 s70, s68, 57344\n\t"
      "s_addc_u32 s71, s69, 0\n\t"
      "s_add_u32 s72, s70, 57344\n\t"
      "s_addc_u32 s73, s71, 0\n\t"
      "s_add_u32 s74, s72, 57344\n\t"
      "s_addc_u32 s75, s73, 0\n\t"
      "s_add_u32 s76, s74, 57344\n\t"
      "s_addc_u32 s77, s75, 0\n\t"
      "v_lshrrev_b32_e32 v240, 7, v244\n\t"
      "v_and_b32_e32 v241, 0x70, v244\n\t"
      "v_and_b32_e32 v242, 6, v240\n\t"
      "v_lshlrev_b32_e32 v242, 4, v242\n\t"
      "v_xor_b32_e32 v242, v242, v241\n\t"
      "v_lshl_or_b32 v228, v240, 12, v242\n\t"
      "s_mul_i32 s34, s24, 917504\n\t"
      "s_lshl_b32 s35, s29, 7\n\t"
      "s_add_u32 s34, s34, s35\n\t"
      "s_lshl_b32 s39, s24, 12\n\t"
      "s_add_u32 s54, s14, s34\n\t"
      "s_addc_u32 s55, s15, 0\n\t"
      "v_add_u32_e32 v229, 31744, v228\n\t"
      "v_add_u32_e32 v230, 63488, v228\n\t"
      "v_add_u32_e32 v231, 95232, v228\n\t"
      "s_lshl_b32 s37, s40, 7\n\t"
      "s_add_u32 s58, s54, s37\n\t"
      "s_addc_u32 s59, s55, 0\n\t"
      "s_add_u32 m0, s39, 0\n\t"
      "s_nop 0\n\t"
      "global_load_lds_dwordx4 v228, s[58:59]\n\t"
      "global_load_lds_dwordx4 v229, s[58:59] offset:1024\n\t"
      "global_load_lds_dwordx4 v230, s[58:59] offset:2048\n\t"
      "global_load_lds_dwordx4 v231, s[58:59] offset:3072\n\t"
      "s_lshl_b32 s37, s41, 7\n\t"
      "s_add_u32 s58, s54, s37\n\t"
      "s_addc_u32 s59, s55, 0\n\t"
      "s_add_u32 m0, s39, 32768\n\t"
      "s_nop 0\n\t"
      "global_load_lds_dwordx4 v228, s[58:59]\n\t"
      "global_load_lds_dwordx4 v229, s[58:59] offset:1024\n\t"
      "global_load_lds_dwordx4 v230, s[58:59] offset:2048\n\t"
      "global_load_lds_dwordx4 v231, s[58:59] offset:3072\n\t"
      "s_lshr_b32 s34, s24, 1\n\t"
      "s_lshl2_add_u32 s35, s29, s34\n\t"
      "s_and_b32 s36, s24, 1\n\t"
      "s_lshl4_add_u32 s35, s35, s36\n\t"
      "v_lshl_add_u32 v242, v240, 1, s35\n\t"
      "v_lshlrev_b32_e32 v248, 1, v242\n\t"
      "global_load_ushort v249, v248, s[16:17]\n\t"
      "s_movk_i32 s35, 896\n\t"
      "v_and_b32_e32 v247, 64, v244\n\t"
      "v_lshrrev_b32_e32 v247, 2, v247\n\t"
      "v_mad_u32_u24 v247, v242, s35, v247\n\t"
      "v_and_b32_e32 v242, 0x30, v244\n\t"
      "v_lshl_add_u32 v247, v242, 1, v247\n\t"
      "s_mov_b32 s82, 0x77777777\n\t"
      "s_mov_b32 s83, 0x77777777\n\t"
      "s_lshl_b32 s35, s32, 7\n\t"
      "s_add_u32 s62, s18, s35\n\t"
      "s_addc_u32 s63, s19, 0\n\t"
      "s_mul_i32 s35, s32, 96\n\t"
      "s_add_u32 s35, s35, 512\n\t"
      "s_add_u32 s18, s18, s35\n\t"
      "s_addc_u32 s19, s19, 0\n\t"
      "s_mul_i32 s35, s29, 28\n\t"
      "s_lshl2_add_u32 s35, s32, s35\n\t"
      "s_lshl1_add_u32 s35, s35, s36\n\t"
      "s_lshl2_add_u32 s35, s35, s34\n\t"
      "s_lshl_b32 s35, s35, 8\n\t"
      "s_add_u32 s35, s35, 6295552\n\t"
      "s_add_u32 s50, s20, s35\n\t"
      "s_addc_u32 s51, s21, 0\n\t"
      "v_lshrrev_b32_e32 v248, 2, v244\n\t"
      "v_bfe_u32 v242, v244, 8, 1\n\t"
      "v_lshlrev_b32_e32 v242, 4, v242\n\t"
      "v_xor_b32_e32 v245, v244, v242\n\t"
      "v_bfe_u32 v242, v244, 9, 1\n\t"
      "v_lshlrev_b32_e32 v242, 7, v242\n\t"
      "v_xor_b32_e32 v245, v245, v242\n\t"
      "s_add_u32 s35, s39, 98304\n\t"
      "v_add_u32_e32 v245, s35, v245\n\t"
      "v_lshl_or_b32 v246, v240, 1, s36\n\t"
      "v_bfe_u32 v242, v244, 5, 1\n\t"
      "v_xor_b32_e32 v246, v246, v242\n\t"
      "v_bfe_u32 v242, v244, 6, 1\n\t"
      "v_lshlrev_b32_e32 v242, 3, v242\n\t"
      "v_xor_b32_e32 v246, v246, v242\n\t"
      "v_lshlrev_b32_e32 v246, 4, v246\n\t"
      "v_and_b32_e32 v242, 0x60, v244\n\t"
      "v_lshl_or_b32 v246, v242, 3, v246\n\t"
      "v_bfe_u32 v242, v244, 4, 1\n\t"
      "v_lshl_or_b32 v246, v242, 3, v246\n\t"
      "s_lshl_b32 s34, s34, 10\n\t"
      "s_add_u32 s34, s34, 98304\n\t"
      "v_add_u32_e32 v246, s34, v246\n\t"
      "v_and_b32_e32 v240, 0x3c0, v244\n\t"
      "v_lshlrev_b32_e32 v240, 1, v240\n\t"
      "v_and_b32_e32 v241, 0x180, v244\n\t"
      "v_lshrrev_b32_e32 v241, 2, v241\n\t"
      "v_and_b32_e32 v242, 0x30, v244\n\t"
      "v_lshrrev_b32_e32 v242, 1, v242\n\t"
      "v_or3_b32 v232, v240, v241, v242\n\t"
      "v_add_u32_e32 v232, s39, v232\n\t"
      "v_min_u32_e32 v252, 432, v244\n\t"
      "v_lshlrev_b32_e32 v252, 4, v252\n\t"
      "v_xor_b32_e32 v233, 32, v232\n\t"
      "v_xor_b32_e32 v234, 64, v232\n\t"
      "v_xor_b32_e32 v235, 96, v232\n\t"
      "v_xor_b32_e32 v236, 65536, v232\n\t"
      "v_xor_b32_e32 v237, 65568, v232\n\t"
      "v_xor_b32_e32 v238, 65600, v232\n\t"
      "v_xor_b32_e32 v239, 65632, v232\n\t"
      "v_min_u32_e32 v240, 48, v244\n\t"
      "v_mul_u32_u24_e32 v240, 7, v240\n\t"
      "s_lshl_b32 s34, s29, 4\n\t"
      "v_add_u32_e32 v240, s34, v240\n\t"
      "v_subrev_u32_e32 v241, 448, v240\n\t"
      "v_min_u32_e32 v240, v240, v241\n\t"
      "v_subrev_u32_e32 v241, 448, v240\n\t"
      "v_min_u32_e32 v240, v240, v241\n\t"
      "s_and_b32 s34, s29, 7\n\t"
      "s_lshl_b32 s34, s34, 8\n\t"
      "v_lshl_add_u32 v253, v240, 7, s34\n\t"
      "s_sub_u32 s52, s64, 50176\n\t"
      "s_subb_u32 s53, s65, 0\n\t"
      "v_add_u32_e32 v143, 32768, v143\n\t"
      "s_barrier\n\t"
      "s_lshl_b32 s37, s42, 7\n\t"
      "s_add_u32 s58, s54, s37\n\t"
      "s_addc_u32 s59, s55, 0\n\t"
      "s_add_u32 m0, s39, 65536\n\t"
      "s_nop 0\n\t"
      "global_load_lds_dwordx4 v228, s[58:59]\n\t"
      "global_load_lds_dwordx4 v229, s[58:59] offset:1024\n\t"
      "global_load_lds_dwordx4 v230, s[58:59] offset:2048\n\t"
      "global_load_lds_dwordx4 v231, s[58:59] offset:3072\n\t"
      "s_lshl_b32 s37, s43, 7\n\t"
      "s_add_u32 s58, s54, s37\n\t"
      "s_addc_u32 s59, s55, 0\n\t"
      "s_add_u32 m0, s39, 98304\n\t"
      "s_nop 0\n\t"
      "global_load_lds_dwordx4 v228, s[58:59]\n\t"
      "global_load_lds_dwordx4 v229, s[58:59] offset:1024\n\t"
      "global_load_lds_dwordx4 v230, s[58:59] offset:2048\n\t"
      "global_load_lds_dwordx4 v231, s[58:59] offset:3072\n\t"
      "s_waitcnt vmcnt(13)\n\t"
      "ds_read_b64_tr_b16 v[0:1], v232\n\t"
      "ds_read_b64_tr_b16 v[2:3], v232 offset:2048\n\t"
      "ds_read_b64_tr_b16 v[4:5], v233\n\t"
      "ds_read_b64_tr_b16 v[6:7], v233 offset:2048\n\t"
      "ds_read_b64_tr_b16 v[8:9], v234\n\t"
      "ds_read_b64_tr_b16 v[10:11], v234 offset:2048\n\t"
      "ds_read_b64_tr_b16 v[12:13], v235\n\t"
      "ds_read_b64_tr_b16 v[14:15], v235 offset:2048\n\t"
      "s_waitcnt lgkmcnt(0)\n\t"
      "s_lshl_b32 s37, s44, 7\n\t"
      "s_add_u32 s58, s54, s37\n\t"
      "s_addc_u32 s59, s55, 0\n\t"
      "s_add_u32 m0, s39, 0\n\t"
      "s_nop 0\n\t"
      "global_load_lds_dwordx4 v228, s[58:59]\n\t"
      "global_load_lds_dwordx4 v229, s[58:59] offset:1024\n\t"
      "global_load_lds_dwordx4 v230, s[58:59] offset:2048\n\t"
      "global_load_lds_dwordx4 v231, s[58:59] offset:3072\n\t"
      "s_waitcnt vmcnt(13)\n\t"
      "ds_read_b64_tr_b16 v[16:17], v232 offset:32768\n\t"
      "ds_read_b64_tr_b16 v[18:19], v232 offset:34816\n\t"
      "ds_read_b64_tr_b16 v[20:21], v233 offset:32768\n\t"
      "ds_read_b64_tr_b16 v[22:23], v233 offset:34816\n\t"
      "ds_read_b64_tr_b16 v[24:25], v234 offset:32768\n\t"
      "ds_read_b64_tr_b16 v[26:27], v234 offset:34816\n\t"
      "ds_read_b64_tr_b16 v[28:29], v235 offset:32768\n\t"
      "ds_read_b64_tr_b16 v[30:31], v235 offset:34816\n\t"
      "s_cmp_lg_u32 s24, 3\n\t"
      "s_cbranch_scc1 SK18_%=\n\t"
      "global_load_dwordx2 v[138:139], v143, s[60:61] sc1\n\t"
      "SK18_%=:\n\t"
      "s_waitcnt lgkmcnt(0)\n\t"
      "s_lshl_b32 s37, s45, 7\n\t"
      "s_add_u32 s58, s54, s37\n\t"
      "s_addc_u32 s59, s55, 0\n\t"
      "s_add_u32 m0, s39, 32768\n\t"
      "s_nop 0\n\t"
      "global_load_lds_dwordx4 v228, s[58:59]\n\t"
      "global_load_lds_dwordx4 v229, s[58:59] offset:1024\n\t"
      "global_load_lds_dwordx4 v230, s[58:59] offset:2048\n\t"
      "global_load_lds_dwordx4 v231, s[58:59] offset:3072\n\t"
      "s_waitcnt vmcnt(12)\n\t"
      "ds_read_b64_tr_b16 v[32:33], v236\n\t"
      "ds_read_b64_tr_b16 v[34:35], v236 offset:2048\n\t"
      "ds_read_b64_tr_b16 v[36:37], v237\n\t"
      "ds_read_b64_tr_b16 v[38:39], v237 offset:2048\n\t"
      "ds_read_b64_tr_b16 v[40:41], v238\n\t"
      "ds_read_b64_tr_b16 v[42:43], v238 offset:2048\n\t"
      "ds_read_b64_tr_b16 v[44:45], v239\n\t"
      "ds_read_b64_tr_b16 v[46:47], v239 offset:2048\n\t"
      "s_waitcnt lgkmcnt(0)\n\t"
      "s_lshl_b32 s37, s46, 7\n\t"
      "s_add_u32 s58, s54, s37\n\t"
      "s_addc_u32 s59, s55, 0\n\t"
      "s_add_u32 m0, s39, 65536\n\t"
      "s_nop 0\n\t"
      "global_load_lds_dwordx4 v228, s[58:59]\n\t"
      "global_load_lds_dwordx4 v229, s[58:59] offset:1024\n\t"
      "global_load_lds_dwordx4 v230, s[58:59] offset:2048\n\t"
      "global_load_lds_dwordx4 v231, s[58:59] offset:3072\n\t"
      "s_waitcnt vmcnt(12)\n\t"
      "ds_read_b64_tr_b16 v[48:49], v236 offset:32768\n\t"
      "ds_read_b64_tr_b16 v[50:51], v236 offset:34816\n\t"
      "ds_read_b64_tr_b16 v[52:53], v237 offset:32768\n\t"
      "ds_read_b64_tr_b16 v[54:55], v237 offset:34816\n\t"
      "ds_read_b64_tr_b16 v[56:57], v238 offset:32768\n\t"
      "ds_read_b64_tr_b16 v[58:59], v238 offset:34816\n\t"
      "ds_read_b64_tr_b16 v[60:61], v239 offset:32768\n\t"
      "ds_read_b64_tr_b16 v[62:63], v239 offset:34816\n\t"
      "s_waitcnt vmcnt(8)\n\t"
      "ds_read_b64_tr_b16 v[64:65], v232\n\t"
      "ds_read_b64_tr_b16 v[66:67], v232 offset:2048\n\t"
      "ds_read_b64_tr_b16 v[68:69], v233\n\t"
      "ds_read_b64_tr_b16 v[70:71], v233 offset:2048\n\t"
      "ds_read_b64_tr_b16 v[72:73], v234\n\t"
      "ds_read_b64_tr_b16 v[74:75], v234 offset:2048\n\t"
      "ds_read_b64_tr_b16 v[76:77], v235\n\t"
      "ds_read_b64_tr_b16 v[78:79], v235 offset:2048\n\t"
      "s_cmp_lg_u32 s24, 3\n\t"
      "s_cbranch_scc1 LF5B_%=\n\t"
      "s_waitcnt vmcnt(8)\n\t"
      "v_cmp_ne_u64_e32 vcc, s[22:23], v[138:139]\n\t"
      "s_cbranch_vccnz LF5R_%=\n\t"
      "LF5B_%=:\n\t"
      "s_barrier\n\t"
      "s_cmp_lg_u32 s24, 3\n\t"
      "s_cbranch_scc1 HF4_%=\n\t"
      "s_lshl3_add_u32 s34, s25, s26\n\t"
      "s_lshl_b32 s34, s34, 7\n\t"
      "s_add_u32 s34, s34, 98304\n\t"
      "v_mov_b32_e32 v146, s34\n\t"
      "v_mov_b32_e32 v144, s22\n\t"
      "v_mov_b32_e32 v145, s23\n\t"
      "s_mov_b64 exec, 1\n\t"
      "global_store_dwordx2 v146, v[144:145], s[60:61] sc1\n\t"
      "s_mov_b64 exec, -1\n\t"
      "HF4_%=:\n\t"
      "s_cmp_lg_u32 s24, 7\n\t"
      "s_cbranch_scc1 PG0_%=\n\t"
      "global_load_dword v251, v253, s[52:53] offset:252 sc1\n\t"
      "PG0_%=:\n\t"
      "s_waitcnt vmcnt(4)\n\t"
      "ds_read_b64_tr_b16 v[80:81], v232 offset:32768\n\t"
      "ds_read_b64_tr_b16 v[82:83], v232 offset:34816\n\t"
      "ds_read_b64_tr_b16 v[84:85], v233 offset:32768\n\t"
      "ds_read_b64_tr_b16 v[86:87], v233 offset:34816\n\t"
      "ds_read_b64_tr_b16 v[88:89], v234 offset:32768\n\t"
      "ds_read_b64_tr_b16 v[90:91], v234 offset:34816\n\t"
      "ds_read_b64_tr_b16 v[92:93], v235 offset:32768\n\t"
      "ds_read_b64_tr_b16 v[94:95], v235 offset:34816\n\t"
      "s_waitcnt vmcnt(0)\n\t"
      "ds_read_b64_tr_b16 v[96:97], v236\n\t"
      "ds_read_b64_tr_b16 v[98:99], v236 offset:2048\n\t"
      "ds_read_b64_tr_b16 v[100:101], v237\n\t"
      "ds_read_b64_tr_b16 v[102:103], v237 offset:2048\n\t"
      "ds_read_b64_tr_b16 v[104:105], v238\n\t"
      "ds_read_b64_tr_b16 v[106:107], v238 offset:2048\n\t"
      "ds_read_b64_tr_b16 v[108:109], v239\n\t"
      "ds_read_b64_tr_b16 v[110:111], v239 offset:2048\n\t"
      "v_lshlrev_b32_e32 v249, 16, v249\n\t"
      "s_and_b32 s34, s29, 30\n\t"
      "s_lshl_b32 s34, s34, 10\n\t"
      "s_add_u32 s34, s34, 98304\n\t"
      "v_bfe_u32 v143, v244, 5, 2\n\t"
      "v_add_u32_e32 v143, 3, v143\n\t"
      "v_writelane_b32 v143, s32, 0\n\t"
      "v_writelane_b32 v143, s32, 1\n\t"
      "v_lshl_add_u32 v143, v143, 7, s34\n\t"
      "v_and_b32_e32 v142, 16, v244\n\t"
      "v_lshl_add_u32 v143, v142, 6, v143\n\t"
      "s_waitcnt vmcnt(0) lgkmcnt(0)\n\t"
      "s_cmp_lg_u32 s24, 7\n\t"
      "s_cbranch_scc1 PGB_%=\n\t"
      "v_cmp_eq_u32_e32 vcc, -1, v251\n\t"
      "s_cbranch_vccnz PGR_%=\n\t"
      "PGB_%=:\n\t"
      "s_barrier\n\t"
      "s_cmp_lg_u32 s24, 3\n\t"
      "s_cbranch_scc1 SK19_%=\n\t"
      "global_load_dwordx2 v[240:241], v143, s[60:61] sc1\n\t"
      "SK19_%=:\n\t"
      "v_mov_b32_e32 v242, v143\n\t"
      "s_add_u32 s58, s64, s40\n\t"
      "s_addc_u32 s59, s65, 0\n\t"
      "global_load_dwordx4 v[128:131], v244, s[58:59] sc1\n\t"
      "s_add_u32 s58, s64, s41\n\t"
      "s_addc_u32 s59, s65, 0\n\t"
      "global_load_dwordx4 v[132:135], v244, s[58:59] sc1\n\t"
      "s_add_u32 s58, s64, s42\n\t"
      "s_addc_u32 s59, s65, 0\n\t"
      "global_load_dwordx4 v[136:139], v244, s[58:59] sc1\n\t"
      "s_add_u32 s58, s64, s43\n\t"
      "s_addc_u32 s59, s65, 0\n\t"
      "global_load_dwordx4 v[140:143], v244, s[58:59] sc1\n\t"
      "s_add_u32 s58, s64, s44\n\t"
      "s_addc_u32 s59, s65, 0\n\t"
      "global_load_dwordx4 v[144:147], v244, s[58:59] sc1\n\t"
      "s_add_u32 s58, s64, s45\n\t"
      "s_addc_u32 s59, s65, 0\n\t"
      "global_load_dwordx4 v[148:151], v244, s[58:59] sc1\n\t"
      "s_add_u32 s58, s64, s46\n\t"
      "s_addc_u32 s59, s65, 0\n\t"
      "global_load_dwordx4 v[152:155], v244, s[58:59] sc1\n\t"
      "v_mov_b32_e32 v254, v249\n\t"
      "s_waitcnt vmcnt(6)\n\t"
      "v_mfma_f32_16x16x32_bf16 v[196:199], v[128:131], v[0:3], 0\n\t"
      "v_mfma_f32_16x16x32_bf16 v[200:203], v[128:131], v[4:7], 0\n\t"
      "v_mfma_f32_16x16x32_bf16 v[204:207], v[128:131], v[8:11], 0\n\t"
      "v_mfma_f32_16x16x32_bf16 v[208:211], v[128:131], v[12:15], 0\n\t"
      "s_waitcnt vmcnt(5)\n\t"
      "v_mfma_f32_16x16x32_bf16 v[196:199], v[132:135], v[16:19], v[196:199]\n\t"
      "v_mfma_f32_16x16x32_bf16 v[200:203], v[132:135], v[20:23], v[200:203]\n\t"
      "v_mfma_f32_16x16x32_bf16 v[204:207], v[132:135], v[24:27], v[204:207]\n\t"
      "s_cmp_lg_u32 s24, 3\n\t"
      "s_cbranch_scc1 LF8B_%=\n\t"
      "v_cmp_ne_u64_e32 vcc, s[22:23], v[240:241]\n\t"
      "s_cbranch_vccnz LF8R_%=\n\t"
      "LF8B_%=:\n\t"
      "s_barrier\n\t"
      "v_mfma_f32_16x16x32_bf16 v[208:211], v[132:135], v[28:31], v[208:211]\n\t"
      "global_load_dword v115, v248, s[50:51] offset:-4096 sc1\n\t"
      "global_load_dword v116, v248, s[50:51] offset:-2048 sc1\n\t"
      "s_waitcnt vmcnt(6)\n\t"
      "v_mfma_f32_16x16x32_bf16 v[196:199], v[136:139], v[32:35], v[196:199]\n\t"
      "global_load_dword v117, v248, s[50:51] sc1\n\t"
      "global_load_dword v118, v248, s[50:51] offset:2048 sc1\n\t"
      "s_add_u32 s58, s66, s40\n\t"
      "s_addc_u32 s59, s67, 0\n\t"
      "global_load_dwordx4 v[156:159], v244, s[58:59] sc1\n\t"
      "v_mfma_f32_16x16x32_bf16 v[200:203], v[136:139], v[36:39], v[200:203]\n\t"
      "s_sub_u32 s34, 16, s32\n\t"
      "s_lshl_b32 s34, s34, 11\n\t"
      "s_add_u32 s50, s50, s34\n\t"
      "s_addc_u32 s51, s51, 0\n\t"
      "global_load_dword v112, v248, s[50:51] offset:-4096 sc1\n\t"
      "global_load_dword v113, v248, s[50:51] offset:-2048 sc1\n\t"
      "v_mfma_f32_16x16x32_bf16 v[204:207], v[136:139], v[40:43], v[204:207]\n\t"
      "global_load_dword v114, v248, s[50:51] sc1\n\t"
      "v_mfma_f32_16x16x32_bf16 v[208:211], v[136:139], v[44:47], v[208:211]\n\t"
      "s_waitcnt vmcnt(11)\n\t"
      "v_mfma_f32_16x16x32_bf16 v[196:199], v[140:143], v[48:51], v[196:199]\n\t"
      "s_add_u32 s58, s66, s41\n\t"
      "s_addc_u32 s59, s67, 0\n\t"
      "global_load_dwordx4 v[160:163], v244, s[58:59] sc1\n\t"
      "v_mfma_f32_16x16x32_bf16 v[200:203], v[140:143], v[52:55], v[200:203]\n\t"
      "v_mfma_f32_16x16x32_bf16 v[204:207], v[140:143], v[56:59], v[204:207]\n\t"
      "v_mfma_f32_16x16x32_bf16 v[208:211], v[140:143], v[60:63], v[208:211]\n\t"
      "s_waitcnt vmcnt(11)\n\t"
      "v_mfma_f32_16x16x32_bf16 v[196:199], v[144:147], v[64:67], v[196:199]\n\t"
      "s_add_u32 s58, s66, s42\n\t"
      "s_addc_u32 s59, s67, 0\n\t"
      "global_load_dwordx4 v[164:167], v244, s[58:59] sc1\n\t"
      "v_mfma_f32_16x16x32_bf16 v[200:203], v[144:147], v[68:71], v[200:203]\n\t"
      "v_mfma_f32_16x16x32_bf16 v[204:207], v[144:147], v[72:75], v[204:207]\n\t"
      "v_mfma_f32_16x16x32_bf16 v[208:211], v[144:147], v[76:79], v[208:211]\n\t"
      "s_waitcnt vmcnt(11)\n\t"
      "v_mfma_f32_16x16x32_bf16 v[196:199], v[148:151], v[80:83], v[196:199]\n\t"
      "s_add_u32 s58, s66, s43\n\t"
      "s_addc_u32 s59, s67, 0\n\t"
      "global_load_dwordx4 v[168:171], v244, s[58:59] sc1\n\t"
      "v_mfma_f32_16x16x32_bf16 v[200:203], v[148:151], v[84:87], v[200:203]\n\t"
      "v_mfma_f32_16x16x32_bf16 v[204:207], v[148:151], v[88:91], v[204:207]\n\t"
      "v_mfma_f32_16x16x32_bf16 v[208:211], v[148:151], v[92:95], v[208:211]\n\t"
      "s_waitcnt vmcnt(11)\n\t"
      "v_mfma_f32_16x16x32_bf16 v[196:199], v[152:155], v[96:99], v[196:199]\n\t"
      "s_add_u32 s58, s66, s44\n\t"
      "s_addc_u32 s59, s67, 0\n\t"
      "global_load_dwordx4 v[172:175], v244, s[58:59] sc1\n\t"
      "v_mfma_f32_16x16x32_bf16 v[200:203], v[152:155], v[100:103], v[200:203]\n\t"
      "v_mfma_f32_16x16x32_bf16 v[204:207], v[152:155], v[104:107], v[204:207]\n\t"
      "v_mfma_f32_16x16x32_bf16 v[208:211], v[152:155], v[108:111], v[208:211]\n\t"
      "s_nop 1\n\t"
      "v_add_f32_e32 v251, v196, v197\n\t"
      "v_add_f32_e32 v251, v251, v198\n\t"
      "v_cmp_u_f32_e32 vcc, v251, v199\n\t"
      "s_cbranch_vccnz LA40R_%=\n\t"
      "LA40B_%=:\n\t"
      "ds_write_b128 v245, v[196:199]\n\t"
      "s_waitcnt vmcnt(7)\n\t"
      "v_mfma_f32_16x16x32_bf16 v[212:215], v[156:159], v[0:3], 0\n\t"
      "ds_write_b128 v245, v[200:203] offset:1024\n\t"
      "s_add_u32 s58, s66, s45\n\t"
      "s_addc_u32 s59, s67, 0\n\t"
      "global_load_dwordx4 v[176:179], v244, s[58:59] sc1\n\t"
      "v_mfma_f32_16x16x32_bf16 v[216:219], v[156:159], v[4:7], 0\n\t"
      "ds_write_b128 v245, v[204:207] offset:2048\n\t"
      "v_mfma_f32_16x16x32_bf16 v[220:223], v[156:159], v[8:11], 0\n\t"
      "ds_write_b128 v245, v[208:211] offset:3072\n\t"
      "v_mfma_f32_16x16x32_bf16 v[224:227], v[156:159], v[12:15], 0\n\t"
      "s_waitcnt vmcnt(4)\n\t"
      "v_mfma_f32_16x16x32_bf16 v[212:215], v[160:163], v[16:19], v[212:215]\n\t"
      "s_add_u32 s58, s66, s46\n\t"
      "s_addc_u32 s59, s67, 0\n\t"
      "global_load_dwordx4 v[180:183], v244, s[58:59] sc1\n\t"
      "v_mfma_f32_16x16x32_bf16 v[216:219], v[160:163], v[20:23], v[216:219]\n\t"
      "v_mfma_f32_16x16x32_bf16 v[220:223], v[160:163], v[24:27], v[220:223]\n\t"
      "s_waitcnt lgkmcnt(0)\n\t"
      "s_barrier\n\t"
      "ds_read_b64 v[228:229], v246\n\t"
      "ds_read_b64 v[230:231], v246 offset:4096\n\t"
      "s_add_u32 s58, s68, s40\n\t"
      "s_addc_u32 s59, s69, 0\n\t"
      "global_load_dwordx4 v[184:187], v244, s[58:59] sc1\n\t"
      "v_mfma_f32_16x16x32_bf16 v[224:227], v[160:163], v[28:31], v[224:227]\n\t"
      "ds_read_b64 v[232:233], v246 offset:8192\n\t"
      "ds_read_b64 v[234:235], v246 offset:12288\n\t"
      "s_waitcnt vmcnt(5)\n\t"
      "v_mfma_f32_16x16x32_bf16 v[212:215], v[164:167], v[32:35], v[212:215]\n\t"
      "ds_read_b64 v[236:237], v246 offset:16384\n\t"
      "ds_read_b64 v[238:239], v246 offset:20480\n\t"
      "v_mfma_f32_16x16x32_bf16 v[216:219], v[164:167], v[36:39], v[216:219]\n\t"
      "ds_read_b64 v[240:241], v246 offset:24576\n\t"
      "ds_read_b64 v[242:243], v246 offset:28672\n\t"
      "v_lshlrev_b32_e32 v248, 16, v112\n\t"
      "v_mfma_f32_16x16x32_bf16 v[220:223], v[164:167], v[40:43], v[220:223]\n\t"
      "v_and_b32_e32 v249, 0xffff0000, v112\n\t"
      "s_add_u32 s58, s68, s41\n\t"
      "s_addc_u32 s59, s69, 0\n\t"
      "global_load_dwordx4 v[188:191], v244, s[58:59] sc1\n\t"
      "v_mfma_f32_16x16x32_bf16 v[224:227], v[164:167], v[44:47], v[224:227]\n\t"
      "v_add_f32_e32 v248, v254, v248\n\t"
      "v_add_f32_e32 v249, v254, v249\n\t"
      "s_waitcnt vmcnt(5)\n\t"
      "v_mfma_f32_16x16x32_bf16 v[212:215], v[168:171], v[48:51], v[212:215]\n\t"
      "s_waitcnt lgkmcnt(4)\n\t"
      "v_mfma_f32_16x16x32_bf16 v[216:219], v[168:171], v[52:55], v[216:219]\n\t"
      "v_add_f32_e32 v228, v228, v230\n\t"
      "v_add_f32_e32 v232, v232, v234\n\t"
      "v_mfma_f32_16x16x32_bf16 v[220:223], v[168:171], v[56:59], v[220:223]\n\t"
      "v_add_f32_e32 v229, v229, v231\n\t"
      "s_add_u32 s58, s68, s42\n\t"
      "s_addc_u32 s59, s69, 0\n\t"
      "global_load_dwordx4 v[192:195], v244, s[58:59] sc1\n\t"
      "v_mfma_f32_16x16x32_bf16 v[224:227], v[168:171], v[60:63], v[224:227]\n\t"
      "v_add_f32_e32 v233, v233, v235\n\t"
      "s_waitcnt lgkmcnt(0)\n\t"
      "s_waitcnt vmcnt(5)\n\t"
      "v_mfma_f32_16x16x32_bf16 v[212:215], v[172:175], v[64:67], v[212:215]\n\t"
      "v_add_f32_e32 v236, v236, v238\n\t"
      "v_mfma_f32_16x16x32_bf16 v[216:219], v[172:175], v[68:71], v[216:219]\n\t"
      "v_add_f32_e32 v240, v240, v242\n\t"
      "v_add_f32_e32 v237, v237, v239\n\t"
      "v_mfma_f32_16x16x32_bf16 v[220:223], v[172:175], v[72:75], v[220:223]\n\t"
      "v_add_f32_e32 v241, v241, v243\n\t"
      "s_add_u32 s58, s68, s43\n\t"
      "s_addc_u32 s59, s69, 0\n\t"
      "global_load_dwordx4 v[128:131], v244, s[58:59] sc1\n\t"
      "v_mfma_f32_16x16x32_bf16 v[224:227], v[172:175], v[76:79], v[224:227]\n\t"
      "v_add_f32_e32 v228, v228, v232\n\t"
      "v_add_f32_e32 v236, v236, v240\n\t"
      "s_waitcnt vmcnt(5)\n\t"
      "v_mfma_f32_16x16x32_bf16 v[212:215], v[176:179], v[80:83], v[212:215]\n\t"
      "v_add_f32_e32 v229, v229, v233\n\t"
      "v_mfma_f32_16x16x32_bf16 v[216:219], v[176:179], v[84:87], v[216:219]\n\t"
      "v_add_f32_e32 v237, v237, v241\n\t"
      "v_add_f32_e32 v228, v228, v248\n\t"
      "v_mfma_f32_16x16x32_bf16 v[220:223], v[176:179], v[88:91], v[220:223]\n\t"
      "v_add_f32_e32 v229, v229, v249\n\t"
      "s_add_u32 s58, s68, s44\n\t"
      "s_addc_u32 s59, s69, 0\n\t"
      "global_load_dwordx4 v[132:135], v244, s[58:59] sc1\n\t"
      "v_mfma_f32_16x16x32_bf16 v[224:227], v[176:179], v[92:95], v[224:227]\n\t"
      "v_add_f32_e32 v228, v228, v236\n\t"
      "v_add_f32_e32 v229, v229, v237\n\t"
      "s_waitcnt vmcnt(5)\n\t"
      "v_mfma_f32_16x16x32_bf16 v[212:215], v[180:183], v[96:99], v[212:215]\n\t"
      "v_cvt_pk_bf16_f32 v232, v228, v229\n\t"
      "v_mfma_f32_16x16x32_bf16 v[216:219], v[180:183], v[100:103], v[216:219]\n\t"
      "s_mov_b32 vcc_lo, 0xeeeeeeee\n\t"
      "s_mov_b32 vcc_hi, 0xeeeeeeee\n\t"
      "v_cndmask_b32_dpp v120, v232, v120, vcc quad_perm:[0,0,0,0] row_mask:0xf bank_mask:0xf\n\t"
      "v_cndmask_b32_dpp v121, v232, v121, vcc quad_perm:[1,1,1,1] row_mask:0xf bank_mask:0xf\n\t"
      "v_cndmask_b32_dpp v122, v232, v122, vcc quad_perm:[2,2,2,2] row_mask:0xf bank_mask:0xf\n\t"
      "v_cndmask_b32_dpp v123, v232, v123, vcc quad_perm:[3,3,3,3] row_mask:0xf bank_mask:0xf\n\t"
      "v_mfma_f32_16x16x32_bf16 v[220:223], v[180:183], v[104:107], v[220:223]\n\t"
      "s_add_u32 s58, s68, s45\n\t"
      "s_addc_u32 s59, s69, 0\n\t"
      "global_load_dwordx4 v[136:139], v244, s[58:59] sc1\n\t"
      "v_mfma_f32_16x16x32_bf16 v[224:227], v[180:183], v[108:111], v[224:227]\n\t"
      "v_add_f32_e32 v251, v212, v213\n\t"
      "v_add_f32_e32 v251, v251, v214\n\t"
      "v_cmp_u_f32_e32 vcc, v251, v215\n\t"
      "s_cbranch_vccnz LA41R_%=\n\t"
      "LA41B_%=:\n\t"
      "ds_write_b128 v245, v[212:215] offset:32768\n\t"
      "s_waitcnt vmcnt(5)\n\t"
      "v_mfma_f32_16x16x32_bf16 v[196:199], v[184:187], v[0:3], 0\n\t"
      "ds_write_b128 v245, v[216:219] offset:33792\n\t"
      "v_mfma_f32_16x16x32_bf16 v[200:203], v[184:187], v[4:7], 0\n\t"
      "ds_write_b128 v245, v[220:223] offset:34816\n\t"
      "v_mfma_f32_16x16x32_bf16 v[204:207], v[184:187], v[8:11], 0\n\t"
      "ds_write_b128 v245, v[224:227] offset:35840\n\t"
      "s_add_u32 s58, s68, s46\n\t"
      "s_addc_u32 s59, s69, 0\n\t"
      "global_load_dwordx4 v[140:143], v244, s[58:59] sc1\n\t"
      "v_mfma_f32_16x16x32_bf16 v[208:211], v[184:187], v[12:15], 0\n\t"
      "s_waitcnt vmcnt(5)\n\t"
      "v_mfma_f32_16x16x32_bf16 v[196:199], v[188:191], v[16:19], v[196:199]\n\t"
      "v_mfma_f32_16x16x32_bf16 v[200:203], v[188:191], v[20:23], v[200:203]\n\t"
      "v_mfma_f32_16x16x32_bf16 v[204:207], v[188:191], v[24:27], v[204:207]\n\t"
      "s_waitcnt lgkmcnt(0)\n\t"
      "s_barrier\n\t"
      "ds_read_b64 v[228:229], v246 offset:32768\n\t"
      "ds_read_b64 v[230:231], v246 offset:36864\n\t"
      "s_add_u32 s58, s70, s40\n\t"
      "s_addc_u32 s59, s71, 0\n\t"
      "global_load_dwordx4 v[144:147], v244, s[58:59] sc1\n\t"
      "v_mfma_f32_16x16x32_bf16 v[208:211], v[188:191], v[28:31], v[208:211]\n\t"
      "ds_read_b64 v[232:233], v246 offset:40960\n\t"
      "ds_read_b64 v[234:235], v246 offset:45056\n\t"
      "s_waitcnt vmcnt(5)\n\t"
      "v_mfma_f32_16x16x32_bf16 v[196:199], v[192:195], v[32:35], v[196:199]\n\t"
      "ds_read_b64 v[236:237], v246 offset:49152\n\t"
      "ds_read_b64 v[238:239], v246 offset:53248\n\t"
      "v_mfma_f32_16x16x32_bf16 v[200:203], v[192:195], v[36:39], v[200:203]\n\t"
      "ds_read_b64 v[240:241], v246 offset:57344\n\t"
      "ds_read_b64 v[242:243], v246 offset:61440\n\t"
      "v_lshlrev_b32_e32 v248, 16, v113\n\t"
      "v_mfma_f32_16x16x32_bf16 v[204:207], v[192:195], v[40:43], v[204:207]\n\t"
      "v_and_b32_e32 v249, 0xffff0000, v113\n\t"
      "s_add_u32 s58, s70, s41\n\t"
      "s_addc_u32 s59, s71, 0\n\t"
      "global_load_dwordx4 v[148:151], v244, s[58:59] sc1\n\t"
      "v_mfma_f32_16x16x32_bf16 v[208:211], v[192:195], v[44:47], v[208:211]\n\t"
      "v_add_f32_e32 v248, v254, v248\n\t"
      "v_add_f32_e32 v249, v254, v249\n\t"
      "s_waitcnt vmcnt(5)\n\t"
      "v_mfma_f32_16x16x32_bf16 v[196:199], v[128:131], v[48:51], v[196:199]\n\t"
      "s_waitcnt lgkmcnt(4)\n\t"
      "v_mfma_f32_16x16x32_bf16 v[200:203], v[128:131], v[52:55], v[200:203]\n\t"
      "v_add_f32_e32 v228, v228, v230\n\t"
      "v_add_f32_e32 v232, v232, v234\n\t"
      "v_mfma_f32_16x16x32_bf16 v[204:207], v[128:131], v[56:59], v[204:207]\n\t"
      "v_add_f32_e32 v229, v229, v231\n\t"
      "s_add_u32 s58, s70, s42\n\t"
      "s_addc_u32 s59, s71, 0\n\t"
      "global_load_dwordx4 v[152:155], v244, s[58:59] sc1\n\t"
      "v_mfma_f32_16x16x32_bf16 v[208:211], v[128:131], v[60:63], v[208:211]\n\t"
      "v_add_f32_e32 v233, v233, v235\n\t"
      "s_waitcnt lgkmcnt(0)\n\t"
      "s_waitcnt vmcnt(5)\n\t"
      "v_mfma_f32_16x16x32_bf16 v[196:199], v[132:135], v[64:67], v[196:199]\n\t"
      "v_add_f32_e32 v236, v236, v238\n\t"
      "v_mfma_f32_16x16x32_bf16 v[200:203], v[132:135], v[68:71], v[200:203]\n\t"
      "v_add_f32_e32 v240, v240, v242\n\t"
      "v_add_f32_e32 v237, v237, v239\n\t"
      "v_mfma_f32_16x16x32_bf16 v[204:207], v[132:135], v[72:75], v[204:207]\n\t"
      "v_add_f32_e32 v241, v241, v243\n\t"
      "s_add_u32 s58, s70, s43\n\t"
      "s_addc_u32 s59, s71, 0\n\t"
      "global_load_dwordx4 v[156:159], v244, s[58:59] sc1\n\t"
      "v_mfma_f32_16x16x32_bf16 v[208:211], v[132:135], v[76:79], v[208:211]\n\t"
      "v_add_f32_e32 v228, v228, v232\n\t"
      "v_add_f32_e32 v236, v236, v240\n\t"
      "s_waitcnt vmcnt(5)\n\t"
      "v_mfma_f32_16x16x32_bf16 v[196:199], v[136:139], v[80:83], v[196:199]\n\t"
      "v_add_f32_e32 v229, v229, v233\n\t"
      "v_mfma_f32_16x16x32_bf16 v[200:203], v[136:139], v[84:87], v[200:203]\n\t"
      "v_add_f32_e32 v237, v237, v241\n\t"
      "v_add_f32_e32 v228, v228, v248\n\t"
      "v_mfma_f32_16x16x32_bf16 v[204:207], v[136:139], v[88:91], v[204:207]\n\t"
      "v_add_f32_e32 v229, v229, v249\n\t"
      "s_add_u32 s58, s70, s44\n\t"
      "s_addc_u32 s59, s71, 0\n\t"
      "global_load_dwordx4 v[160:163], v244, s[58:59] sc1\n\t"
      "v_mfma_f32_16x16x32_bf16 v[208:211], v[136:139], v[92:95], v[208:211]\n\t"
      "v_add_f32_e32 v228, v228, v236\n\t"
      "v_add_f32_e32 v229, v229, v237\n\t"
      "s_waitcnt vmcnt(5)\n\t"
      "v_mfma_f32_16x16x32_bf16 v[196:199], v[140:143], v[96:99], v[196:199]\n\t"
      "v_cvt_pk_bf16_f32 v232, v228, v229\n\t"
      "v_mfma_f32_16x16x32_bf16 v[200:203], v[140:143], v[100:103], v[200:203]\n\t"
      "s_mov_b32 vcc_lo, 0xdddddddd\n\t"
      "s_mov_b32 vcc_hi, 0xdddddddd\n\t"
      "v_cndmask_b32_dpp v120, v232, v120, vcc quad_perm:[0,0,0,0] row_mask:0xf bank_mask:0xf\n\t"
      "v_cndmask_b32_dpp v121, v232, v121, vcc quad_perm:[1,1,1,1] row_mask:0xf bank_mask:0xf\n\t"
      "v_cndmask_b32_dpp v122, v232, v122, vcc quad_perm:[2,2,2,2] row_mask:0xf bank_mask:0xf\n\t"
      "v_cndmask_b32_dpp v123, v232, v123, vcc quad_perm:[3,3,3,3] row_mask:0xf bank_mask:0xf\n\t"
      "v_mfma_f32_16x16x32_bf16 v[204:207], v[140:143], v[104:107], v[204:207]\n\t"
      "s_add_u32 s58, s70, s45\n\t"
      "s_addc_u32 s59, s71, 0\n\t"
      "global_load_dwordx4 v[164:167], v244, s[58:59] sc1\n\t"
      "v_mfma_f32_16x16x32_bf16 v[208:211], v[140:143], v[108:111], v[208:211]\n\t"
      "v_add_f32_e32 v251, v196, v197\n\t"
      "v_add_f32_e32 v251, v251, v198\n\t"
      "v_cmp_u_f32_e32 vcc, v251, v199\n\t"
      "s_cbranch_vccnz LA42R_%=\n\t"
      "LA42B_%=:\n\t"
      "ds_write_b128 v245, v[196:199]\n\t"
      "s_waitcnt vmcnt(5)\n\t"
      "v_mfma_f32_16x16x32_bf16 v[212:215], v[144:147], v[0:3], 0\n\t"
      "ds_write_b128 v245, v[200:203] offset:1024\n\t"
      "v_mfma_f32_16x16x32_bf16 v[216:219], v[144:147], v[4:7], 0\n\t"
      "ds_write_b128 v245, v[204:207] offset:2048\n\t"
      "v_mfma_f32_16x16x32_bf16 v[220:223], v[144:147], v[8:11], 0\n\t"
      "ds_write_b128 v245, v[208:211] offset:3072\n\t"
      "s_add_u32 s58, s70, s46\n\t"
      "s_addc_u32 s59, s71, 0\n\t"
      "global_load_dwordx4 v[168:171], v244, s[58:59] sc1\n\t"
      "v_mfma_f32_16x16x32_bf16 v[224:227], v[144:147], v[12:15], 0\n\t"
      "s_waitcnt vmcnt(5)\n\t"
      "v_mfma_f32_16x16x32_bf16 v[212:215], v[148:151], v[16:19], v[212:215]\n\t"
      "v_mfma_f32_16x16x32_bf16 v[216:219], v[148:151], v[20:23], v[216:219]\n\t"
      "v_mfma_f32_16x16x32_bf16 v[220:223], v[148:151], v[24:27], v[220:223]\n\t"
      "s_waitcnt lgkmcnt(0)\n\t"
      "s_barrier\n\t"
      "ds_read_b64 v[228:229], v246\n\t"
      "ds_read_b64 v[230:231], v246 offset:4096\n\t"
      "s_add_u32 s58, s72, s40\n\t"
      "s_addc_u32 s59, s73, 0\n\t"
      "global_load_dwordx4 v[172:175], v244, s[58:59] sc1\n\t"
      "v_mfma_f32_16x16x32_bf16 v[224:227], v[148:151], v[28:31], v[224:227]\n\t"
      "ds_read_b64 v[232:233], v246 offset:8192\n\t"
      "ds_read_b64 v[234:235], v246 offset:12288\n\t"
      "s_waitcnt vmcnt(5)\n\t"
      "v_mfma_f32_16x16x32_bf16 v[212:215], v[152:155], v[32:35], v[212:215]\n\t"
      "ds_read_b64 v[236:237], v246 offset:16384\n\t"
      "ds_read_b64 v[238:239], v246 offset:20480\n\t"
      "v_mfma_f32_16x16x32_bf16 v[216:219], v[152:155], v[36:39], v[216:219]\n\t"
      "ds_read_b64 v[240:241], v246 offset:24576\n\t"
      "ds_read_b64 v[242:243], v246 offset:28672\n\t"
      "v_lshlrev_b32_e32 v248, 16, v114\n\t"
      "v_mfma_f32_16x16x32_bf16 v[220:223], v[152:155], v[40:43], v[220:223]\n\t"
      "v_and_b32_e32 v249, 0xffff0000, v114\n\t"
      "s_add_u32 s58, s72, s41\n\t"
      "s_addc_u32 s59, s73, 0\n\t"
      "global_load_dwordx4 v[176:179], v244, s[58:59] sc1\n\t"
      "v_mfma_f32_16x16x32_bf16 v[224:227], v[152:155], v[44:47], v[224:227]\n\t"
      "v_add_f32_e32 v248, v254, v248\n\t"
      "v_add_f32_e32 v249, v254, v249\n\t"
      "s_waitcnt vmcnt(5)\n\t"
      "v_mfma_f32_16x16x32_bf16 v[212:215], v[156:159], v[48:51], v[212:215]\n\t"
      "s_waitcnt lgkmcnt(4)\n\t"
      "v_mfma_f32_16x16x32_bf16 v[216:219], v[156:159], v[52:55], v[216:219]\n\t"
      "v_add_f32_e32 v228, v228, v230\n\t"
      "v_add_f32_e32 v232, v232, v234\n\t"
      "v_mfma_f32_16x16x32_bf16 v[220:223], v[156:159], v[56:59], v[220:223]\n\t"
      "v_add_f32_e32 v229, v229, v231\n\t"
      "s_add_u32 s58, s72, s42\n\t"
      "s_addc_u32 s59, s73, 0\n\t"
      "global_load_dwordx4 v[180:183], v244, s[58:59] sc1\n\t"
      "v_mfma_f32_16x16x32_bf16 v[224:227], v[156:159], v[60:63], v[224:227]\n\t"
      "v_add_f32_e32 v233, v233, v235\n\t"
      "s_waitcnt lgkmcnt(0)\n\t"
      "s_waitcnt vmcnt(5)\n\t"
      "v_mfma_f32_16x16x32_bf16 v[212:215], v[160:163], v[64:67], v[212:215]\n\t"
      "v_add_f32_e32 v236, v236, v238\n\t"
      "v_mfma_f32_16x16x32_bf16 v[216:219], v[160:163], v[68:71], v[216:219]\n\t"
      "v_add_f32_e32 v240, v240, v242\n\t"
      "v_add_f32_e32 v237, v237, v239\n\t"
      "v_mfma_f32_16x16x32_bf16 v[220:223], v[160:163], v[72:75], v[220:223]\n\t"
      "v_add_f32_e32 v241, v241, v243\n\t"
      "s_add_u32 s58, s72, s43\n\t"
      "s_addc_u32 s59, s73, 0\n\t"
      "global_load_dwordx4 v[184:187], v244, s[58:59] sc1\n\t"
      "v_mfma_f32_16x16x32_bf16 v[224:227], v[160:163], v[76:79], v[224:227]\n\t"
      "v_add_f32_e32 v228, v228, v232\n\t"
      "v_add_f32_e32 v236, v236, v240\n\t"
      "s_waitcnt vmcnt(5)\n\t"
      "v_mfma_f32_16x16x32_bf16 v[212:215], v[164:167], v[80:83], v[212:215]\n\t"
      "v_add_f32_e32 v229, v229, v233\n\t"
      "v_mfma_f32_16x16x32_bf16 v[216:219], v[164:167], v[84:87], v[216:219]\n\t"
      "v_add_f32_e32 v237, v237, v241\n\t"
      "v_add_f32_e32 v228, v228, v248\n\t"
      "v_mfma_f32_16x16x32_bf16 v[220:223], v[164:167], v[88:91], v[220:223]\n\t"
      "v_add_f32_e32 v229, v229, v249\n\t"
      "s_add_u32 s58, s72, s44\n\t"
      "s_addc_u32 s59, s73, 0\n\t"
      "global_load_dwordx4 v[188:191], v244, s[58:59] sc1\n\t"
      "v_mfma_f32_16x16x32_bf16 v[224:227], v[164:167], v[92:95], v[224:227]\n\t"
      "v_add_f32_e32 v228, v228, v236\n\t"
      "v_add_f32_e32 v229, v229, v237\n\t"
      "s_waitcnt vmcnt(5)\n\t"
      "v_mfma_f32_16x16x32_bf16 v[212:215], v[168:171], v[96:99], v[212:215]\n\t"
      "v_cvt_pk_bf16_f32 v232, v228, v229\n\t"
      "v_mfma_f32_16x16x32_bf16 v[216:219], v[168:171], v[100:103], v[216:219]\n\t"
      "s_mov_b32 vcc_lo, 0xbbbbbbbb\n\t"
      "s_mov_b32 vcc_hi, 0xbbbbbbbb\n\t"
      "v_cndmask_b32_dpp v120, v232, v120, vcc quad_perm:[0,0,0,0] row_mask:0xf bank_mask:0xf\n\t"
      "v_cndmask_b32_dpp v121, v232, v121, vcc quad_perm:[1,1,1,1] row_mask:0xf bank_mask:0xf\n\t"
      "v_cndmask_b32_dpp v122, v232, v122, vcc quad_perm:[2,2,2,2] row_mask:0xf bank_mask:0xf\n\t"
      "v_cndmask_b32_dpp v123, v232, v123, vcc quad_perm:[3,3,3,3] row_mask:0xf bank_mask:0xf\n\t"
      "v_mfma_f32_16x16x32_bf16 v[220:223], v[168:171], v[104:107], v[220:223]\n\t"
      "s_add_u32 s58, s72, s45\n\t"
      "s_addc_u32 s59, s73, 0\n\t"
      "global_load_dwordx4 v[192:195], v244, s[58:59] sc1\n\t"
      "v_mfma_f32_16x16x32_bf16 v[224:227], v[168:171], v[108:111], v[224:227]\n\t"
      "v_add_f32_e32 v251, v212, v213\n\t"
      "v_add_f32_e32 v251, v251, v214\n\t"
      "v_cmp_u_f32_e32 vcc, v251, v215\n\t"
      "s_cbranch_vccnz LA43R_%=\n\t"
      "LA43B_%=:\n\t"
      "ds_write_b128 v245, v[212:215] offset:32768\n\t"
      "s_waitcnt vmcnt(5)\n\t"
      "v_mfma_f32_16x16x32_bf16 v[196:199], v[172:175], v[0:3], 0\n\t"
      "ds_write_b128 v245, v[216:219] offset:33792\n\t"
      "v_mfma_f32_16x16x32_bf16 v[200:203], v[172:175], v[4:7], 0\n\t"
      "ds_write_b128 v245, v[220:223] offset:34816\n\t"
      "v_mfma_f32_16x16x32_bf16 v[204:207], v[172:175], v[8:11], 0\n\t"
      "ds_write_b128 v245, v[224:227] offset:35840\n\t"
      "s_add_u32 s58, s72, s46\n\t"
      "s_addc_u32 s59, s73, 0\n\t"
      "global_load_dwordx4 v[128:131], v244, s[58:59] sc1\n\t"
      "v_mfma_f32_16x16x32_bf16 v[208:211], v[172:175], v[12:15], 0\n\t"
      "s_waitcnt vmcnt(5)\n\t"
      "v_mfma_f32_16x16x32_bf16 v[196:199], v[176:179], v[16:19], v[196:199]\n\t"
      "v_mfma_f32_16x16x32_bf16 v[200:203], v[176:179], v[20:23], v[200:203]\n\t"
      "v_mfma_f32_16x16x32_bf16 v[204:207], v[176:179], v[24:27], v[204:207]\n\t"
      "s_waitcnt lgkmcnt(0)\n\t"
      "s_barrier\n\t"
      "ds_read_b64 v[228:229], v246 offset:32768\n\t"
      "ds_read_b64 v[230:231], v246 offset:36864\n\t"
      "s_add_u32 s58, s74, s40\n\t"
      "s_addc_u32 s59, s75, 0\n\t"
      "global_load_dwordx4 v[132:135], v244, s[58:59] sc1\n\t"
      "v_mfma_f32_16x16x32_bf16 v[208:211], v[176:179], v[28:31], v[208:211]\n\t"
      "ds_read_b64 v[232:233], v246 offset:40960\n\t"
      "ds_read_b64 v[234:235], v246 offset:45056\n\t"
      "s_waitcnt vmcnt(5)\n\t"
      "v_mfma_f32_16x16x32_bf16 v[196:199], v[180:183], v[32:35], v[196:199]\n\t"
      "ds_read_b64 v[236:237], v246 offset:49152\n\t"
      "ds_read_b64 v[238:239], v246 offset:53248\n\t"
      "v_mfma_f32_16x16x32_bf16 v[200:203], v[180:183], v[36:39], v[200:203]\n\t"
      "ds_read_b64 v[240:241], v246 offset:57344\n\t"
      "ds_read_b64 v[242:243], v246 offset:61440\n\t"
      "v_lshlrev_b32_e32 v248, 16, v115\n\t"
      "v_mfma_f32_16x16x32_bf16 v[204:207], v[180:183], v[40:43], v[204:207]\n\t"
      "v_and_b32_e32 v249, 0xffff0000, v115\n\t"
      "s_add_u32 s58, s74, s41\n\t"
      "s_addc_u32 s59, s75, 0\n\t"
      "global_load_dwordx4 v[136:139], v244, s[58:59] sc1\n\t"
      "v_mfma_f32_16x16x32_bf16 v[208:211], v[180:183], v[44:47], v[208:211]\n\t"
      "v_add_f32_e32 v248, v254, v248\n\t"
      "v_add_f32_e32 v249, v254, v249\n\t"
      "s_waitcnt vmcnt(5)\n\t"
      "v_mfma_f32_16x16x32_bf16 v[196:199], v[184:187], v[48:51], v[196:199]\n\t"
      "s_waitcnt lgkmcnt(4)\n\t"
      "v_mfma_f32_16x16x32_bf16 v[200:203], v[184:187], v[52:55], v[200:203]\n\t"
      "v_add_f32_e32 v228, v228, v230\n\t"
      "v_add_f32_e32 v232, v232, v234\n\t"
      "v_mfma_f32_16x16x32_bf16 v[204:207], v[184:187], v[56:59], v[204:207]\n\t"
      "v_add_f32_e32 v229, v229, v231\n\t"
      "s_add_u32 s58, s74, s42\n\t"
      "s_addc_u32 s59, s75, 0\n\t"
      "global_load_dwordx4 v[140:143], v244, s[58:59] sc1\n\t"
      "v_mfma_f32_16x16x32_bf16 v[208:211], v[184:187], v[60:63], v[208:211]\n\t"
      "v_add_f32_e32 v233, v233, v235\n\t"
      "s_waitcnt lgkmcnt(0)\n\t"
      "s_waitcnt vmcnt(5)\n\t"
      "v_mfma_f32_16x16x32_bf16 v[196:199], v[188:191], v[64:67], v[196:199]\n\t"
      "v_add_f32_e32 v236, v236, v238\n\t"
      "v_mfma_f32_16x16x32_bf16 v[200:203], v[188:191], v[68:71], v[200:203]\n\t"
      "v_add_f32_e32 v240, v240, v242\n\t"
      "v_add_f32_e32 v237, v237, v239\n\t"
      "v_mfma_f32_16x16x32_bf16 v[204:207], v[188:191], v[72:75], v[204:207]\n\t"
      "v_add_f32_e32 v241, v241, v243\n\t"
      "s_add_u32 s58, s74, s43\n\t"
      "s_addc_u32 s59, s75, 0\n\t"
      "global_load_dwordx4 v[144:147], v244, s[58:59] sc1\n\t"
      "v_mfma_f32_16x16x32_bf16 v[208:211], v[188:191], v[76:79], v[208:211]\n\t"
      "v_add_f32_e32 v228, v228, v232\n\t"
      "v_add_f32_e32 v236, v236, v240\n\t"
      "s_waitcnt vmcnt(5)\n\t"
      "v_mfma_f32_16x16x32_bf16 v[196:199], v[192:195], v[80:83], v[196:199]\n\t"
      "v_add_f32_e32 v229, v229, v233\n\t"
      "v_mfma_f32_16x16x32_bf16 v[200:203], v[192:195], v[84:87], v[200:203]\n\t"
      "v_add_f32_e32 v237, v237, v241\n\t"
      "v_add_f32_e32 v228, v228, v248\n\t"
      "v_mfma_f32_16x16x32_bf16 v[204:207], v[192:195], v[88:91], v[204:207]\n\t"
      "v_add_f32_e32 v229, v229, v249\n\t"
      "s_add_u32 s58, s74, s44\n\t"
      "s_addc_u32 s59, s75, 0\n\t"
      "global_load_dwordx4 v[148:151], v244, s[58:59] sc1\n\t"
      "v_mfma_f32_16x16x32_bf16 v[208:211], v[192:195], v[92:95], v[208:211]\n\t"
      "v_add_f32_e32 v228, v228, v236\n\t"
      "v_add_f32_e32 v229, v229, v237\n\t"
      "s_waitcnt vmcnt(5)\n\t"
      "v_mfma_f32_16x16x32_bf16 v[196:199], v[128:131], v[96:99], v[196:199]\n\t"
      "v_cvt_pk_bf16_f32 v232, v228, v229\n\t"
      "v_mfma_f32_16x16x32_bf16 v[200:203], v[128:131], v[100:103], v[200:203]\n\t"
      "s_mov_b32 vcc_lo, 0xeeeeeeee\n\t"
      "s_mov_b32 vcc_hi, 0xeeeeeeee\n\t"
      "v_cndmask_b32_dpp v124, v232, v124, vcc quad_perm:[0,0,0,0] row_mask:0xf bank_mask:0xf\n\t"
      "v_cndmask_b32_dpp v125, v232, v125, vcc quad_perm:[1,1,1,1] row_mask:0xf bank_mask:0xf\n\t"
      "v_cndmask_b32_dpp v126, v232, v126, vcc quad_perm:[2,2,2,2] row_mask:0xf bank_mask:0xf\n\t"
      "v_cndmask_b32_dpp v127, v232, v127, vcc quad_perm:[3,3,3,3] row_mask:0xf bank_mask:0xf\n\t"
      "v_mfma_f32_16x16x32_bf16 v[204:207], v[128:131], v[104:107], v[204:207]\n\t"
      "s_add_u32 s58, s74, s45\n\t"
      "s_addc_u32 s59, s75, 0\n\t"
      "global_load_dwordx4 v[152:155], v244, s[58:59] sc1\n\t"
      "v_mfma_f32_16x16x32_bf16 v[208:211], v[128:131], v[108:111], v[208:211]\n\t"
      "v_add_f32_e32 v251, v196, v197\n\t"
      "v_add_f32_e32 v251, v251, v198\n\t"
      "v_cmp_u_f32_e32 vcc, v251, v199\n\t"
      "s_cbranch_vccnz LA44R_%=\n\t"
      "LA44B_%=:\n\t"
      "ds_write_b128 v245, v[196:199]\n\t"
      "s_waitcnt vmcnt(5)\n\t"
      "v_mfma_f32_16x16x32_bf16 v[212:215], v[132:135], v[0:3], 0\n\t"
      "ds_write_b128 v245, v[200:203] offset:1024\n\t"
      "v_mfma_f32_16x16x32_bf16 v[216:219], v[132:135], v[4:7], 0\n\t"
      "ds_write_b128 v245, v[204:207] offset:2048\n\t"
      "v_mfma_f32_16x16x32_bf16 v[220:223], v[132:135], v[8:11], 0\n\t"
      "ds_write_b128 v245, v[208:211] offset:3072\n\t"
      "s_add_u32 s58, s74, s46\n\t"
      "s_addc_u32 s59, s75, 0\n\t"
      "global_load_dwordx4 v[156:159], v244, s[58:59] sc1\n\t"
      "v_mfma_f32_16x16x32_bf16 v[224:227], v[132:135], v[12:15], 0\n\t"
      "s_waitcnt vmcnt(5)\n\t"
      "v_mfma_f32_16x16x32_bf16 v[212:215], v[136:139], v[16:19], v[212:215]\n\t"
      "v_mfma_f32_16x16x32_bf16 v[216:219], v[136:139], v[20:23], v[216:219]\n\t"
      "v_mfma_f32_16x16x32_bf16 v[220:223], v[136:139], v[24:27], v[220:223]\n\t"
      "s_waitcnt lgkmcnt(0)\n\t"
      "s_barrier\n\t"
      "ds_read_b64 v[228:229], v246\n\t"
      "ds_read_b64 v[230:231], v246 offset:4096\n\t"
      "s_add_u32 s58, s76, s40\n\t"
      "s_addc_u32 s59, s77, 0\n\t"
      "global_load_dwordx4 v[160:163], v244, s[58:59] sc1\n\t"
      "v_mfma_f32_16x16x32_bf16 v[224:227], v[136:139], v[28:31], v[224:227]\n\t"
      "ds_read_b64 v[232:233], v246 offset:8192\n\t"
      "ds_read_b64 v[234:235], v246 offset:12288\n\t"
      "s_waitcnt vmcnt(5)\n\t"
      "v_mfma_f32_16x16x32_bf16 v[212:215], v[140:143], v[32:35], v[212:215]\n\t"
      "ds_read_b64 v[236:237], v246 offset:16384\n\t"
      "ds_read_b64 v[238:239], v246 offset:20480\n\t"
      "v_mfma_f32_16x16x32_bf16 v[216:219], v[140:143], v[36:39], v[216:219]\n\t"
      "ds_read_b64 v[240:241], v246 offset:24576\n\t"
      "ds_read_b64 v[242:243], v246 offset:28672\n\t"
      "v_lshlrev_b32_e32 v248, 16, v116\n\t"
      "v_mfma_f32_16x16x32_bf16 v[220:223], v[140:143], v[40:43], v[220:223]\n\t"
      "v_and_b32_e32 v249, 0xffff0000, v116\n\t"
      "s_add_u32 s58, s76, s41\n\t"
      "s_addc_u32 s59, s77, 0\n\t"
      "global_load_dwordx4 v[164:167], v244, s[58:59] sc1\n\t"
      "v_mfma_f32_16x16x32_bf16 v[224:227], v[140:143], v[44:47], v[224:227]\n\t"
      "v_add_f32_e32 v248, v254, v248\n\t"
      "v_add_f32_e32 v249, v254, v249\n\t"
      "s_waitcnt vmcnt(5)\n\t"
      "v_mfma_f32_16x16x32_bf16 v[212:215], v[144:147], v[48:51], v[212:215]\n\t"
      "s_waitcnt lgkmcnt(4)\n\t"
      "v_mfma_f32_16x16x32_bf16 v[216:219], v[144:147], v[52:55], v[216:219]\n\t"
      "v_add_f32_e32 v228, v228, v230\n\t"
      "v_add_f32_e32 v232, v232, v234\n\t"
      "v_mfma_f32_16x16x32_bf16 v[220:223], v[144:147], v[56:59], v[220:223]\n\t"
      "v_add_f32_e32 v229, v229, v231\n\t"
      "s_add_u32 s58, s76, s42\n\t"
      "s_addc_u32 s59, s77, 0\n\t"
      "global_load_dwordx4 v[168:171], v244, s[58:59] sc1\n\t"
      "v_mfma_f32_16x16x32_bf16 v[224:227], v[144:147], v[60:63], v[224:227]\n\t"
      "v_add_f32_e32 v233, v233, v235\n\t"
      "s_waitcnt lgkmcnt(0)\n\t"
      "s_waitcnt vmcnt(5)\n\t"
      "v_mfma_f32_16x16x32_bf16 v[212:215], v[148:151], v[64:67], v[212:215]\n\t"
      "v_add_f32_e32 v236, v236, v238\n\t"
      "v_mfma_f32_16x16x32_bf16 v[216:219], v[148:151], v[68:71], v[216:219]\n\t"
      "v_add_f32_e32 v240, v240, v242\n\t"
      "v_add_f32_e32 v237, v237, v239\n\t"
      "v_mfma_f32_16x16x32_bf16 v[220:223], v[148:151], v[72:75], v[220:223]\n\t"
      "v_add_f32_e32 v241, v241, v243\n\t"
      "s_add_u32 s58, s76, s43\n\t"
      "s_addc_u32 s59, s77, 0\n\t"
      "global_load_dwordx4 v[172:175], v244, s[58:59] sc1\n\t"
      "v_mfma_f32_16x16x32_bf16 v[224:227], v[148:151], v[76:79], v[224:227]\n\t"
      "v_add_f32_e32 v228, v228, v232\n\t"
      "v_add_f32_e32 v236, v236, v240\n\t"
      "s_waitcnt vmcnt(5)\n\t"
      "v_mfma_f32_16x16x32_bf16 v[212:215], v[152:155], v[80:83], v[212:215]\n\t"
      "v_add_f32_e32 v229, v229, v233\n\t"
      "v_mfma_f32_16x16x32_bf16 v[216:219], v[152:155], v[84:87], v[216:219]\n\t"
      "v_add_f32_e32 v237, v237, v241\n\t"
      "v_add_f32_e32 v228, v228, v248\n\t"
      "v_mfma_f32_16x16x32_bf16 v[220:223], v[152:155], v[88:91], v[220:223]\n\t"
      "v_add_f32_e32 v229, v229, v249\n\t"
      "s_add_u32 s58, s76, s44\n\t"
      "s_addc_u32 s59, s77, 0\n\t"
      "global_load_dwordx4 v[176:179], v244, s[58:59] sc1\n\t"
      "v_mfma_f32_16x16x32_bf16 v[224:227], v[152:155], v[92:95], v[224:227]\n\t"
      "v_add_f32_e32 v228, v228, v236\n\t"
      "v_add_f32_e32 v229, v229, v237\n\t"
      "s_waitcnt vmcnt(5)\n\t"
      "v_mfma_f32_16x16x32_bf16 v[212:215], v[156:159], v[96:99], v[212:215]\n\t"
      "v_cvt_pk_bf16_f32 v232, v228, v229\n\t"
      "v_mfma_f32_16x16x32_bf16 v[216:219], v[156:159], v[100:103], v[216:219]\n\t"
      "s_mov_b32 vcc_lo, 0xdddddddd\n\t"
      "s_mov_b32 vcc_hi, 0xdddddddd\n\t"
      "v_cndmask_b32_dpp v124, v232, v124, vcc quad_perm:[0,0,0,0] row_mask:0xf bank_mask:0xf\n\t"
      "v_cndmask_b32_dpp v125, v232, v125, vcc quad_perm:[1,1,1,1] row_mask:0xf bank_mask:0xf\n\t"
      "v_cndmask_b32_dpp v126, v232, v126, vcc quad_perm:[2,2,2,2] row_mask:0xf bank_mask:0xf\n\t"
      "v_cndmask_b32_dpp v127, v232, v127, vcc quad_perm:[3,3,3,3] row_mask:0xf bank_mask:0xf\n\t"
      "v_mfma_f32_16x16x32_bf16 v[220:223], v[156:159], v[104:107], v[220:223]\n\t"
      "s_add_u32 s58, s76, s45\n\t"
      "s_addc_u32 s59, s77, 0\n\t"
      "global_load_dwordx4 v[180:183], v244, s[58:59] sc1\n\t"
      "v_mfma_f32_16x16x32_bf16 v[224:227], v[156:159], v[108:111], v[224:227]\n\t"
      "v_add_f32_e32 v251, v212, v213\n\t"
      "v_add_f32_e32 v251, v251, v214\n\t"
      "v_cmp_u_f32_e32 vcc, v251, v215\n\t"
      "s_cbranch_vccnz LA45R_%=\n\t"
      "LA45B_%=:\n\t"
      "ds_write_b128 v245, v[212:215] offset:32768\n\t"
      "s_waitcnt vmcnt(5)\n\t"
      "v_mfma_f32_16x16x32_bf16 v[196:199], v[160:163], v[0:3], 0\n\t"
      "ds_write_b128 v245, v[216:219] offset:33792\n\t"
      "v_mfma_f32_16x16x32_bf16 v[200:203], v[160:163], v[4:7], 0\n\t"
      "ds_write_b128 v245, v[220:223] offset:34816\n\t"
      "v_mfma_f32_16x16x32_bf16 v[204:207], v[160:163], v[8:11], 0\n\t"
      "ds_write_b128 v245, v[224:227] offset:35840\n\t"
      "s_add_u32 s58, s76, s46\n\t"
      "s_addc_u32 s59, s77, 0\n\t"
      "global_load_dwordx4 v[184:187], v244, s[58:59] sc1\n\t"
      "v_mfma_f32_16x16x32_bf16 v[208:211], v[160:163], v[12:15], 0\n\t"
      "s_waitcnt vmcnt(5)\n\t"
      "v_mfma_f32_16x16x32_bf16 v[196:199], v[164:167], v[16:19], v[196:199]\n\t"
      "v_mfma_f32_16x16x32_bf16 v[200:203], v[164:167], v[20:23], v[200:203]\n\t"
      "v_mfma_f32_16x16x32_bf16 v[204:207], v[164:167], v[24:27], v[204:207]\n\t"
      "s_waitcnt lgkmcnt(0)\n\t"
      "s_barrier\n\t"
      "ds_read_b64 v[228:229], v246 offset:32768\n\t"
      "ds_read_b64 v[230:231], v246 offset:36864\n\t"
      "v_mfma_f32_16x16x32_bf16 v[208:211], v[164:167], v[28:31], v[208:211]\n\t"
      "ds_read_b64 v[232:233], v246 offset:40960\n\t"
      "ds_read_b64 v[234:235], v246 offset:45056\n\t"
      "s_waitcnt vmcnt(4)\n\t"
      "v_mfma_f32_16x16x32_bf16 v[196:199], v[168:171], v[32:35], v[196:199]\n\t"
      "ds_read_b64 v[236:237], v246 offset:49152\n\t"
      "ds_read_b64 v[238:239], v246 offset:53248\n\t"
      "v_mfma_f32_16x16x32_bf16 v[200:203], v[168:171], v[36:39], v[200:203]\n\t"
      "ds_read_b64 v[240:241], v246 offset:57344\n\t"
      "ds_read_b64 v[242:243], v246 offset:61440\n\t"
      "v_lshlrev_b32_e32 v248, 16, v117\n\t"
      "v_and_b32_e32 v249, 0xffff0000, v117\n\t"
      "v_mfma_f32_16x16x32_bf16 v[204:207], v[168:171], v[40:43], v[204:207]\n\t"
      "v_add_f32_e32 v248, v254, v248\n\t"
      "v_add_f32_e32 v249, v254, v249\n\t"
      "v_mfma_f32_16x16x32_bf16 v[208:211], v[168:171], v[44:47], v[208:211]\n\t"
      "s_waitcnt lgkmcnt(4)\n\t"
      "v_add_f32_e32 v228, v228, v230\n\t"
      "v_add_f32_e32 v232, v232, v234\n\t"
      "s_waitcnt vmcnt(3)\n\t"
      "v_mfma_f32_16x16x32_bf16 v[196:199], v[172:175], v[48:51], v[196:199]\n\t"
      "v_add_f32_e32 v229, v229, v231\n\t"
      "v_add_f32_e32 v233, v233, v235\n\t"
      "v_mfma_f32_16x16x32_bf16 v[200:203], v[172:175], v[52:55], v[200:203]\n\t"
      "s_waitcnt lgkmcnt(0)\n\t"
      "v_add_f32_e32 v236, v236, v238\n\t"
      "v_add_f32_e32 v240, v240, v242\n\t"
      "v_mfma_f32_16x16x32_bf16 v[204:207], v[172:175], v[56:59], v[204:207]\n\t"
      "v_add_f32_e32 v237, v237, v239\n\t"
      "v_add_f32_e32 v241, v241, v243\n\t"
      "v_mfma_f32_16x16x32_bf16 v[208:211], v[172:175], v[60:63], v[208:211]\n\t"
      "v_add_f32_e32 v228, v228, v232\n\t"
      "v_add_f32_e32 v236, v236, v240\n\t"
      "v_add_f32_e32 v229, v229, v233\n\t"
      "s_waitcnt vmcnt(2)\n\t"
      "v_mfma_f32_16x16x32_bf16 v[196:199], v[176:179], v[64:67], v[196:199]\n\t"
      "v_add_f32_e32 v237, v237, v241\n\t"
      "v_add_f32_e32 v228, v228, v248\n\t"
      "v_mfma_f32_16x16x32_bf16 v[200:203], v[176:179], v[68:71], v[200:203]\n\t"
      "v_add_f32_e32 v229, v229, v249\n\t"
      "v_add_f32_e32 v228, v228, v236\n\t"
      "v_add_f32_e32 v229, v229, v237\n\t"
      "v_mfma_f32_16x16x32_bf16 v[204:207], v[176:179], v[72:75], v[204:207]\n\t"
      "v_cvt_pk_bf16_f32 v232, v228, v229\n\t"
      "s_mov_b32 vcc_lo, 0xbbbbbbbb\n\t"
      "s_mov_b32 vcc_hi, 0xbbbbbbbb\n\t"
      "v_cndmask_b32_dpp v124, v232, v124, vcc quad_perm:[0,0,0,0] row_mask:0xf bank_mask:0xf\n\t"
      "v_cndmask_b32_dpp v125, v232, v125, vcc quad_perm:[1,1,1,1] row_mask:0xf bank_mask:0xf\n\t"
      "v_cndmask_b32_dpp v126, v232, v126, vcc quad_perm:[2,2,2,2] row_mask:0xf bank_mask:0xf\n\t"
      "v_cndmask_b32_dpp v127, v232, v127, vcc quad_perm:[3,3,3,3] row_mask:0xf bank_mask:0xf\n\t"
      "v_mfma_f32_16x16x32_bf16 v[208:211], v[176:179], v[76:79], v[208:211]\n\t"
      "s_waitcnt vmcnt(1)\n\t"
      "v_mfma_f32_16x16x32_bf16 v[196:199], v[180:183], v[80:83], v[196:199]\n\t"
      "v_mfma_f32_16x16x32_bf16 v[200:203], v[180:183], v[84:87], v[200:203]\n\t"
      "v_mfma_f32_16x16x32_bf16 v[204:207], v[180:183], v[88:91], v[204:207]\n\t"
      "v_mfma_f32_16x16x32_bf16 v[208:211], v[180:183], v[92:95], v[208:211]\n\t"
      "s_waitcnt vmcnt(0)\n\t"
      "v_mfma_f32_16x16x32_bf16 v[196:199], v[184:187], v[96:99], v[196:199]\n\t"
      "v_mfma_f32_16x16x32_bf16 v[200:203], v[184:187], v[100:103], v[200:203]\n\t"
      "v_mfma_f32_16x16x32_bf16 v[204:207], v[184:187], v[104:107], v[204:207]\n\t"
      "v_mfma_f32_16x16x32_bf16 v[208:211], v[184:187], v[108:111], v[208:211]\n\t"
      "s_nop 4\n\t"
      "v_add_f32_e32 v251, v196, v197\n\t"
      "v_add_f32_e32 v251, v251, v198\n\t"
      "v_cmp_u_f32_e32 vcc, v251, v199\n\t"
      "s_cbranch_vccnz LA46R_%=\n\t"
      "LA46B_%=:\n\t"
      "ds_write_b128 v245, v[196:199]\n\t"
      "s_mov_b64 exec, s[82:83]\n\t"
      "global_store_dwordx4 v247, v[120:123], s[18:19] sc1\n\t"
      "s_mov_b64 exec, -1\n\t"
      "ds_write_b128 v245, v[200:203] offset:1024\n\t"
      "ds_write_b128 v245, v[204:207] offset:2048\n\t"
      "ds_write_b128 v245, v[208:211] offset:3072\n\t"
      "s_waitcnt lgkmcnt(0)\n\t"
      "s_barrier\n\t"
      "ds_read_b64 v[228:229], v246\n\t"
      "ds_read_b64 v[230:231], v246 offset:4096\n\t"
      "ds_read_b64 v[232:233], v246 offset:8192\n\t"
      "ds_read_b64 v[234:235], v246 offset:12288\n\t"
      "ds_read_b64 v[236:237], v246 offset:16384\n\t"
      "ds_read_b64 v[238:239], v246 offset:20480\n\t"
      "ds_read_b64 v[240:241], v246 offset:24576\n\t"
      "ds_read_b64 v[242:243], v246 offset:28672\n\t"
      "v_lshlrev_b32_e32 v248, 16, v118\n\t"
      "v_and_b32_e32 v249, 0xffff0000, v118\n\t"
      "v_add_f32_e32 v248, v254, v248\n\t"
      "v_add_f32_e32 v249, v254, v249\n\t"
      "s_waitcnt lgkmcnt(4)\n\t"
      "v_add_f32_e32 v228, v228, v230\n\t"
      "v_add_f32_e32 v232, v232, v234\n\t"
      "v_add_f32_e32 v229, v229, v231\n\t"
      "v_add_f32_e32 v233, v233, v235\n\t"
      "s_waitcnt lgkmcnt(0)\n\t"
      "v_add_f32_e32 v236, v236, v238\n\t"
      "v_add_f32_e32 v240, v240, v242\n\t"
      "v_add_f32_e32 v237, v237, v239\n\t"
      "v_add_f32_e32 v241, v241, v243\n\t"
      "v_add_f32_e32 v228, v228, v232\n\t"
      "v_add_f32_e32 v236, v236, v240\n\t"
      "v_add_f32_e32 v229, v229, v233\n\t"
      "v_add_f32_e32 v237, v237, v241\n\t"
      "v_add_f32_e32 v228, v228, v248\n\t"
      "v_add_f32_e32 v229, v229, v249\n\t"
      "v_add_f32_e32 v228, v228, v236\n\t"
      "v_add_f32_e32 v229, v229, v237\n\t"
      "v_cvt_pk_bf16_f32 v232, v228, v229\n\t"
      "s_mov_b32 vcc_lo, 0x77777777\n\t"
      "s_mov_b32 vcc_hi, 0x77777777\n\t"
      "v_cndmask_b32_dpp v124, v232, v124, vcc quad_perm:[0,0,0,0] row_mask:0xf bank_mask:0xf\n\t"
      "v_cndmask_b32_dpp v125, v232, v125, vcc quad_perm:[1,1,1,1] row_mask:0xf bank_mask:0xf\n\t"
      "v_cndmask_b32_dpp v126, v232, v126, vcc quad_perm:[2,2,2,2] row_mask:0xf bank_mask:0xf\n\t"
      "v_cndmask_b32_dpp v127, v232, v127, vcc quad_perm:[3,3,3,3] row_mask:0xf bank_mask:0xf\n\t"
      "global_store_dwordx4 v247, v[124:127], s[62:63] sc1\n\t"
      "s_endpgm\n\t"
      "LO0R_%=:\n\t"
      "s_mov_b32 s80, 0\n\t"
      "LO0L_%=:\n\t"
      "s_add_u32 s80, s80, 1\n\t"
      "s_cmp_lt_u32 s80, 262144\n\t"
      "s_cbranch_scc0 LO0X_%=\n\t"
      "s_waitcnt vmcnt(0)\n\t"
      "s_sleep 1\n\t"
      "global_load_dwordx4 v[128:131], v244, s[42:43] offset:-2048 sc1\n\t"
      "global_load_dwordx4 v[132:135], v244, s[42:43] offset:-1024 sc1\n\t"
      "global_load_dwordx4 v[136:139], v244, s[42:43] sc1\n\t"
      "global_load_dwordx4 v[140:143], v244, s[42:43] offset:1024 sc1\n\t"
      "global_load_dwordx4 v[144:147], v244, s[42:43] offset:2048 sc1\n\t"
      "global_load_dwordx4 v[148:151], v244, s[42:43] offset:3072 sc1\n\t"
      "global_load_dwordx4 v[152:155], v244, s[44:45] offset:-4096 sc1\n\t"
      "global_load_dwordx4 v[156:159], v244, s[44:45] offset:-3072 sc1\n\t"
      "global_load_dwordx4 v[160:163], v244, s[44:45] offset:-2048 sc1\n\t"
      "global_load_dwordx4 v[164:167], v244, s[44:45] offset:-1024 sc1\n\t"
      "global_load_dwordx4 v[168:171], v244, s[44:45] sc1\n\t"
      "global_load_dwordx4 v[172:175], v244, s[44:45] offset:1024 sc1\n\t"
      "global_load_dwordx4 v[176:179], v244, s[44:45] offset:2048 sc1\n\t"
      "global_load_dwordx4 v[180:183], v244, s[44:45] offset:3072 sc1\n\t"
      "global_load_dwordx4 v[184:187], v244, s[42:43] offset:-4096 sc1\n\t"
      "global_load_dwordx4 v[188:191], v244, s[42:43] offset:-3072 sc1\n\t"
      "s_waitcnt vmcnt(0)\n\t"
      "v_max3_u32 v232, v128, v129, v130\n\t"
      "v_max3_u32 v232, v232, v131, v132\n\t"
      "v_max3_u32 v232, v232, v133, v134\n\t"
      "v_max_u32_e32 v232, v232, v135\n\t"
      "v_cmp_eq_u32_e32 vcc, -1, v232\n\t"
      "s_cbranch_vccnz LO0L_%=\n\t"
      "LO0X_%=:\n\t"
      "s_branch LO0B_%=\n\t"
      "LO2R_%=:\n\t"
      "s_mov_b32 s80, 0\n\t"
      "LO2L_%=:\n\t"
      "s_add_u32 s80, s80, 1\n\t"
      "s_cmp_lt_u32 s80, 262144\n\t"
      "s_cbranch_scc0 LO2X_%=\n\t"
      "s_waitcnt vmcnt(0)\n\t"
      "s_sleep 1\n\t"
      "global_load_dwordx4 v[136:139], v244, s[42:43] sc1\n\t"
      "global_load_dwordx4 v[140:143], v244, s[42:43] offset:1024 sc1\n\t"
      "global_load_dwordx4 v[144:147], v244, s[42:43] offset:2048 sc1\n\t"
      "global_load_dwordx4 v[148:151], v244, s[42:43] offset:3072 sc1\n\t"
      "global_load_dwordx4 v[152:155], v244, s[44:45] offset:-4096 sc1\n\t"
      "global_load_dwordx4 v[156:159], v244, s[44:45] offset:-3072 sc1\n\t"
      "global_load_dwordx4 v[160:163], v244, s[44:45] offset:-2048 sc1\n\t"
      "global_load_dwordx4 v[164:167], v244, s[44:45] offset:-1024 sc1\n\t"
      "global_load_dwordx4 v[168:171], v244, s[44:45] sc1\n\t"
      "global_load_dwordx4 v[172:175], v244, s[44:45] offset:1024 sc1\n\t"
      "global_load_dwordx4 v[176:179], v244, s[44:45] offset:2048 sc1\n\t"
      "global_load_dwordx4 v[180:183], v244, s[44:45] offset:3072 sc1\n\t"
      "global_load_dwordx4 v[184:187], v244, s[42:43] offset:-4096 sc1\n\t"
      "global_load_dwordx4 v[188:191], v244, s[42:43] offset:-3072 sc1\n\t"
      "s_waitcnt vmcnt(0)\n\t"
      "v_max3_u32 v232, v136, v137, v138\n\t"
      "v_max3_u32 v232, v232, v139, v140\n\t"
      "v_max3_u32 v232, v232, v141, v142\n\t"
      "v_max_u32_e32 v232, v232, v143\n\t"
      "v_cmp_eq_u32_e32 vcc, -1, v232\n\t"
      "s_cbranch_vccnz LO2L_%=\n\t"
      "LO2X_%=:\n\t"
      "s_branch LO2B_%=\n\t"
      "LO4R_%=:\n\t"
      "s_mov_b32 s80, 0\n\t"
      "LO4L_%=:\n\t"
      "s_add_u32 s80, s80, 1\n\t"
      "s_cmp_lt_u32 s80, 262144\n\t"
      "s_cbranch_scc0 LO4X_%=\n\t"
      "s_waitcnt vmcnt(0)\n\t"
      "s_sleep 1\n\t"
      "global_load_dwordx4 v[144:147], v244, s[42:43] offset:2048 sc1\n\t"
      "global_load_dwordx4 v[148:151], v244, s[42:43] offset:3072 sc1\n\t"
      "global_load_dwordx4 v[152:155], v244, s[44:45] offset:-4096 sc1\n\t"
      "global_load_dwordx4 v[156:159], v244, s[44:45] offset:-3072 sc1\n\t"
      "global_load_dwordx4 v[160:163], v244, s[44:45] offset:-2048 sc1\n\t"
      "global_load_dwordx4 v[164:167], v244, s[44:45] offset:-1024 sc1\n\t"
      "global_load_dwordx4 v[168:171], v244, s[44:45] sc1\n\t"
      "global_load_dwordx4 v[172:175], v244, s[44:45] offset:1024 sc1\n\t"
      "global_load_dwordx4 v[176:179], v244, s[44:45] offset:2048 sc1\n\t"
      "global_load_dwordx4 v[180:183], v244, s[44:45] offset:3072 sc1\n\t"
      "global_load_dwordx4 v[184:187], v244, s[42:43] offset:-4096 sc1\n\t"
      "global_load_dwordx4 v[188:191], v244, s[42:43] offset:-3072 sc1\n\t"
      "s_waitcnt vmcnt(0)\n\t"
      "v_max3_u32 v232, v144, v145, v146\n\t"
      "v_max3_u32 v232, v232, v147, v148\n\t"
      "v_max3_u32 v232, v232, v149, v150\n\t"
      "v_max_u32_e32 v232, v232, v151\n\t"
      "v_cmp_eq_u32_e32 vcc, -1, v232\n\t"
      "s_cbranch_vccnz LO4L_%=\n\t"
      "LO4X_%=:\n\t"
      "s_branch LO4B_%=\n\t"
      "LO6R_%=:\n\t"
      "s_mov_b32 s80, 0\n\t"
      "LO6L_%=:\n\t"
      "s_add_u32 s80, s80, 1\n\t"
      "s_cmp_lt_u32 s80, 262144\n\t"
      "s_cbranch_scc0 LO6X_%=\n\t"
      "s_waitcnt vmcnt(0)\n\t"
      "s_sleep 1\n\t"
      "global_load_dwordx4 v[152:155], v244, s[44:45] offset:-4096 sc1\n\t"
      "global_load_dwordx4 v[156:159], v244, s[44:45] offset:-3072 sc1\n\t"
      "global_load_dwordx4 v[160:163], v244, s[44:45] offset:-2048 sc1\n\t"
      "global_load_dwordx4 v[164:167], v244, s[44:45] offset:-1024 sc1\n\t"
      "global_load_dwordx4 v[168:171], v244, s[44:45] sc1\n\t"
      "global_load_dwordx4 v[172:175], v244, s[44:45] offset:1024 sc1\n\t"
      "global_load_dwordx4 v[176:179], v244, s[44:45] offset:2048 sc1\n\t"
      "global_load_dwordx4 v[180:183], v244, s[44:45] offset:3072 sc1\n\t"
      "global_load_dwordx4 v[184:187], v244, s[42:43] offset:-4096 sc1\n\t"
      "global_load_dwordx4 v[188:191], v244, s[42:43] offset:-3072 sc1\n\t"
      "s_waitcnt vmcnt(0)\n\t"
      "v_max3_u32 v232, v152, v153, v154\n\t"
      "v_max3_u32 v232, v232, v155, v156\n\t"
      "v_max3_u32 v232, v232, v157, v158\n\t"
      "v_max_u32_e32 v232, v232, v159\n\t"
      "v_cmp_eq_u32_e32 vcc, -1, v232\n\t"
      "s_cbranch_vccnz LO6L_%=\n\t"
      "LO6X_%=:\n\t"
      "s_branch LO6B_%=\n\t"
      "LO8R_%=:\n\t"
      "s_mov_b32 s80, 0\n\t"
      "LO8L_%=:\n\t"
      "s_add_u32 s80, s80, 1\n\t"
      "s_cmp_lt_u32 s80, 262144\n\t"
      "s_cbranch_scc0 LO8X_%=\n\t"
      "s_waitcnt vmcnt(0)\n\t"
      "s_sleep 1\n\t"
      "global_load_dwordx4 v[160:163], v244, s[44:45] offset:-2048 sc1\n\t"
      "global_load_dwordx4 v[164:167], v244, s[44:45] offset:-1024 sc1\n\t"
      "global_load_dwordx4 v[168:171], v244, s[44:45] sc1\n\t"
      "global_load_dwordx4 v[172:175], v244, s[44:45] offset:1024 sc1\n\t"
      "global_load_dwordx4 v[176:179], v244, s[44:45] offset:2048 sc1\n\t"
      "global_load_dwordx4 v[180:183], v244, s[44:45] offset:3072 sc1\n\t"
      "global_load_dwordx4 v[184:187], v244, s[42:43] offset:-4096 sc1\n\t"
      "global_load_dwordx4 v[188:191], v244, s[42:43] offset:-3072 sc1\n\t"
      "s_waitcnt vmcnt(0)\n\t"
      "v_max3_u32 v232, v160, v161, v162\n\t"
      "v_max3_u32 v232, v232, v163, v164\n\t"
      "v_max3_u32 v232, v232, v165, v166\n\t"
      "v_max_u32_e32 v232, v232, v167\n\t"
      "v_cmp_eq_u32_e32 vcc, -1, v232\n\t"
      "s_cbranch_vccnz LO8L_%=\n\t"
      "LO8X_%=:\n\t"
      "s_branch LO8B_%=\n\t"
      "LO10R_%=:\n\t"
      "s_mov_b32 s80, 0\n\t"
      "LO10L_%=:\n\t"
      "s_add_u32 s80, s80, 1\n\t"
      "s_cmp_lt_u32 s80, 262144\n\t"
      "s_cbranch_scc0 LO10X_%=\n\t"
      "s_waitcnt vmcnt(0)\n\t"
      "s_sleep 1\n\t"
      "global_load_dwordx4 v[168:171], v244, s[44:45] sc1\n\t"
      "global_load_dwordx4 v[172:175], v244, s[44:45] offset:1024 sc1\n\t"
      "global_load_dwordx4 v[176:179], v244, s[44:45] offset:2048 sc1\n\t"
      "global_load_dwordx4 v[180:183], v244, s[44:45] offset:3072 sc1\n\t"
      "global_load_dwordx4 v[184:187], v244, s[42:43] offset:-4096 sc1\n\t"
      "global_load_dwordx4 v[188:191], v244, s[42:43] offset:-3072 sc1\n\t"
      "s_waitcnt vmcnt(0)\n\t"
      "v_max3_u32 v232, v168, v169, v170\n\t"
      "v_max3_u32 v232, v232, v171, v172\n\t"
      "v_max3_u32 v232, v232, v173, v174\n\t"
      "v_max_u32_e32 v232, v232, v175\n\t"
      "v_cmp_eq_u32_e32 vcc, -1, v232\n\t"
      "s_cbranch_vccnz LO10L_%=\n\t"
      "LO10X_%=:\n\t"
      "s_branch LO10B_%=\n\t"
      "LO12R_%=:\n\t"
      "s_mov_b32 s80, 0\n\t"
      "LO12L_%=:\n\t"
      "s_add_u32 s80, s80, 1\n\t"
      "s_cmp_lt_u32 s80, 262144\n\t"
      "s_cbranch_scc0 LO12X_%=\n\t"
      "s_waitcnt vmcnt(0)\n\t"
      "s_sleep 1\n\t"
      "global_load_dwordx4 v[176:179], v244, s[44:45] offset:2048 sc1\n\t"
      "global_load_dwordx4 v[180:183], v244, s[44:45] offset:3072 sc1\n\t"
      "global_load_dwordx4 v[184:187], v244, s[42:43] offset:-4096 sc1\n\t"
      "global_load_dwordx4 v[188:191], v244, s[42:43] offset:-3072 sc1\n\t"
      "s_waitcnt vmcnt(0)\n\t"
      "v_max3_u32 v232, v176, v177, v178\n\t"
      "v_max3_u32 v232, v232, v179, v180\n\t"
      "v_max3_u32 v232, v232, v181, v182\n\t"
      "v_max_u32_e32 v232, v232, v183\n\t"
      "v_cmp_eq_u32_e32 vcc, -1, v232\n\t"
      "s_cbranch_vccnz LO12L_%=\n\t"
      "LO12X_%=:\n\t"
      "s_branch LO12B_%=\n\t"
      "LO14R_%=:\n\t"
      "s_mov_b32 s80, 0\n\t"
      "LO14L_%=:\n\t"
      "s_add_u32 s80, s80, 1\n\t"
      "s_cmp_lt_u32 s80, 262144\n\t"
      "s_cbranch_scc0 LO14X_%=\n\t"
      "s_waitcnt vmcnt(0)\n\t"
      "s_sleep 1\n\t"
      "global_load_dwordx4 v[184:187], v244, s[42:43] offset:-4096 sc1\n\t"
      "global_load_dwordx4 v[188:191], v244, s[42:43] offset:-3072 sc1\n\t"
      "s_waitcnt vmcnt(0)\n\t"
      "v_max3_u32 v232, v184, v185, v186\n\t"
      "v_max3_u32 v232, v232, v187, v188\n\t"
      "v_max3_u32 v232, v232, v189, v190\n\t"
      "v_max_u32_e32 v232, v232, v191\n\t"
      "v_cmp_eq_u32_e32 vcc, -1, v232\n\t"
      "s_cbranch_vccnz LO14L_%=\n\t"
      "LO14X_%=:\n\t"
      "s_branch LO14B_%=\n\t"
      "LGX_%=:\n\t"
      "v_max3_u32 v232, v128, v129, v130\n\t"
      "v_max3_u32 v232, v232, v131, v132\n\t"
      "v_max3_u32 v232, v232, v133, v134\n\t"
      "v_max3_u32 v232, v232, v135, v136\n\t"
      "v_max3_u32 v232, v232, v137, v138\n\t"
      "v_max3_u32 v232, v232, v139, v140\n\t"
      "v_max3_u32 v232, v232, v141, v142\n\t"
      "v_max3_u32 v232, v232, v143, v144\n\t"
      "v_max3_u32 v232, v232, v145, v146\n\t"
      "v_max3_u32 v232, v232, v147, v148\n\t"
      "v_max3_u32 v232, v232, v149, v150\n\t"
      "v_max3_u32 v232, v232, v151, v152\n\t"
      "v_max3_u32 v232, v232, v153, v154\n\t"
      "v_max3_u32 v232, v232, v155, v156\n\t"
      "v_max3_u32 v232, v232, v157, v158\n\t"
      "v_max3_u32 v232, v232, v159, v160\n\t"
      "v_max3_u32 v232, v232, v161, v162\n\t"
      "v_max3_u32 v232, v232, v163, v164\n\t"
      "v_max3_u32 v232, v232, v165, v166\n\t"
      "v_max3_u32 v232, v232, v167, v168\n\t"
      "v_max3_u32 v232, v232, v169, v170\n\t"
      "v_max3_u32 v232, v232, v171, v172\n\t"
      "v_max3_u32 v232, v232, v173, v174\n\t"
      "v_max3_u32 v232, v232, v175, v176\n\t"
      "v_max3_u32 v232, v232, v177, v178\n\t"
      "v_max3_u32 v232, v232, v179, v180\n\t"
      "v_max3_u32 v232, v232, v181, v182\n\t"
      "v_max3_u32 v232, v232, v183, v184\n\t"
      "v_max3_u32 v232, v232, v185, v186\n\t"
      "v_max3_u32 v232, v232, v187, v188\n\t"
      "v_max3_u32 v232, v232, v189, v190\n\t"
      "v_max_u32_e32 v232, v232, v191\n\t"
      "v_cmp_eq_u32_e32 vcc, -1, v232\n\t"
      "s_cbranch_vccz LGB_%=\n\t"
      "LGYR_%=:\n\t"
      "s_mov_b32 s80, 0\n\t"
      "LGYL_%=:\n\t"
      "s_add_u32 s80, s80, 1\n\t"
      "s_cmp_lt_u32 s80, 262144\n\t"
      "s_cbranch_scc0 LGYX_%=\n\t"
      "s_sleep 1\n\t"
      "global_load_dwordx4 v[128:131], v244, s[42:43] offset:-2048 sc1\n\t"
      "global_load_dwordx4 v[132:135], v244, s[42:43] offset:-1024 sc1\n\t"
      "global_load_dwordx4 v[136:139], v244, s[42:43] sc1\n\t"
      "global_load_dwordx4 v[140:143], v244, s[42:43] offset:1024 sc1\n\t"
      "global_load_dwordx4 v[144:147], v244, s[42:43] offset:2048 sc1\n\t"
      "global_load_dwordx4 v[148:151], v244, s[42:43] offset:3072 sc1\n\t"
      "global_load_dwordx4 v[152:155], v244, s[44:45] offset:-4096 sc1\n\t"
      "global_load_dwordx4 v[156:159], v244, s[44:45] offset:-3072 sc1\n\t"
      "global_load_dwordx4 v[160:163], v244, s[44:45] offset:-2048 sc1\n\t"
      "global_load_dwordx4 v[164:167], v244, s[44:45] offset:-1024 sc1\n\t"
      "global_load_dwordx4 v[168:171], v244, s[44:45] sc1\n\t"
      "global_load_dwordx4 v[172:175], v244, s[44:45] offset:1024 sc1\n\t"
      "global_load_dwordx4 v[176:179], v244, s[44:45] offset:2048 sc1\n\t"
      "global_load_dwordx4 v[180:183], v244, s[44:45] offset:3072 sc1\n\t"
      "global_load_dwordx4 v[184:187], v244, s[42:43] offset:-4096 sc1\n\t"
      "global_load_dwordx4 v[188:191], v244, s[42:43] offset:-3072 sc1\n\t"
      "s_waitcnt vmcnt(0)\n\t"
      "v_max3_u32 v232, v128, v129, v130\n\t"
      "v_max3_u32 v232, v232, v131, v132\n\t"
      "v_max3_u32 v232, v232, v133, v134\n\t"
      "v_max3_u32 v232, v232, v135, v136\n\t"
      "v_max3_u32 v232, v232, v137, v138\n\t"
      "v_max3_u32 v232, v232, v139, v140\n\t"
      "v_max3_u32 v232, v232, v141, v142\n\t"
      "v_max3_u32 v232, v232, v143, v144\n\t"
      "v_max3_u32 v232, v232, v145, v146\n\t"
      "v_max3_u32 v232, v232, v147, v148\n\t"
      "v_max3_u32 v232, v232, v149, v150\n\t"
      "v_max3_u32 v232, v232, v151, v152\n\t"
      "v_max3_u32 v232, v232, v153, v154\n\t"
      "v_max3_u32 v232, v232, v155, v156\n\t"
      "v_max3_u32 v232, v232, v157, v158\n\t"
      "v_max3_u32 v232, v232, v159, v160\n\t"
      "v_max3_u32 v232, v232, v161, v162\n\t"
      "v_max3_u32 v232, v232, v163, v164\n\t"
      "v_max3_u32 v232, v232, v165, v166\n\t"
      "v_max3_u32 v232, v232, v167, v168\n\t"
      "v_max3_u32 v232, v232, v169, v170\n\t"
      "v_max3_u32 v232, v232, v171, v172\n\t"
      "v_max3_u32 v232, v232, v173, v174\n\t"
      "v_max3_u32 v232, v232, v175, v176\n\t"
      "v_max3_u32 v232, v232, v177, v178\n\t"
      "v_max3_u32 v232, v232, v179, v180\n\t"
      "v_max3_u32 v232, v232, v181, v182\n\t"
      "v_max3_u32 v232, v232, v183, v184\n\t"
      "v_max3_u32 v232, v232, v185, v186\n\t"
      "v_max3_u32 v232, v232, v187, v188\n\t"
      "v_max3_u32 v232, v232, v189, v190\n\t"
      "v_max_u32_e32 v232, v232, v191\n\t"
      "v_cmp_eq_u32_e32 vcc, -1, v232\n\t"
      "s_cbranch_vccnz LGYL_%=\n\t"
      "LGYX_%=:\n\t"
      "s_cbranch_vccnz LGB_%=\n\t"
      "ds_read_b64_tr_b16 v[0:1], v216 offset:7168\n\t"
      "ds_read_b64_tr_b16 v[2:3], v220 offset:7168\n\t"
      "ds_read_b64_tr_b16 v[4:5], v217 offset:7168\n\t"
      "ds_read_b64_tr_b16 v[6:7], v221 offset:7168\n\t"
      "ds_read_b64_tr_b16 v[8:9], v218 offset:7168\n\t"
      "ds_read_b64_tr_b16 v[10:11], v222 offset:7168\n\t"
      "ds_read_b64_tr_b16 v[12:13], v219 offset:7168\n\t"
      "ds_read_b64_tr_b16 v[14:15], v223 offset:7168\n\t"
      "ds_read_b64_tr_b16 v[16:17], v218 offset:10752\n\t"
      "ds_read_b64_tr_b16 v[18:19], v222 offset:10752\n\t"
      "ds_read_b64_tr_b16 v[20:21], v219 offset:10752\n\t"
      "ds_read_b64_tr_b16 v[22:23], v223 offset:10752\n\t"
      "ds_read_b64_tr_b16 v[24:25], v216 offset:10752\n\t"
      "ds_read_b64_tr_b16 v[26:27], v220 offset:10752\n\t"
      "ds_read_b64_tr_b16 v[28:29], v217 offset:10752\n\t"
      "ds_read_b64_tr_b16 v[30:31], v221 offset:10752\n\t"
      "ds_read_b64_tr_b16 v[32:33], v216 offset:14336\n\t"
      "ds_read_b64_tr_b16 v[34:35], v220 offset:14336\n\t"
      "ds_read_b64_tr_b16 v[36:37], v217 offset:14336\n\t"
      "ds_read_b64_tr_b16 v[38:39], v221 offset:14336\n\t"
      "ds_read_b64_tr_b16 v[40:41], v218 offset:14336\n\t"
      "ds_read_b64_tr_b16 v[42:43], v222 offset:14336\n\t"
      "ds_read_b64_tr_b16 v[44:45], v219 offset:14336\n\t"
      "ds_read_b64_tr_b16 v[46:47], v223 offset:14336\n\t"
      "ds_read_b64_tr_b16 v[48:49], v218 offset:17920\n\t"
      "ds_read_b64_tr_b16 v[50:51], v222 offset:17920\n\t"
      "ds_read_b64_tr_b16 v[52:53], v219 offset:17920\n\t"
      "ds_read_b64_tr_b16 v[54:55], v223 offset:17920\n\t"
      "ds_read_b64_tr_b16 v[56:57], v216 offset:17920\n\t"
      "ds_read_b64_tr_b16 v[58:59], v220 offset:17920\n\t"
      "ds_read_b64_tr_b16 v[60:61], v217 offset:17920\n\t"
      "ds_read_b64_tr_b16 v[62:63], v221 offset:17920\n\t"
      "ds_read_b64_tr_b16 v[64:65], v216 offset:21504\n\t"
      "ds_read_b64_tr_b16 v[66:67], v220 offset:21504\n\t"
      "ds_read_b64_tr_b16 v[68:69], v217 offset:21504\n\t"
      "ds_read_b64_tr_b16 v[70:71], v221 offset:21504\n\t"
      "ds_read_b64_tr_b16 v[72:73], v218 offset:21504\n\t"
      "ds_read_b64_tr_b16 v[74:75], v222 offset:21504\n\t"
      "ds_read_b64_tr_b16 v[76:77], v219 offset:21504\n\t"
      "ds_read_b64_tr_b16 v[78:79], v223 offset:21504\n\t"
      "ds_read_b64_tr_b16 v[80:81], v218 offset:25088\n\t"
      "ds_read_b64_tr_b16 v[82:83], v222 offset:25088\n\t"
      "ds_read_b64_tr_b16 v[84:85], v219 offset:25088\n\t"
      "ds_read_b64_tr_b16 v[86:87], v223 offset:25088\n\t"
      "ds_read_b64_tr_b16 v[88:89], v216 offset:25088\n\t"
      "ds_read_b64_tr_b16 v[90:91], v220 offset:25088\n\t"
      "ds_read_b64_tr_b16 v[92:93], v217 offset:25088\n\t"
      "ds_read_b64_tr_b16 v[94:95], v221 offset:25088\n\t"
      "ds_read_b64_tr_b16 v[96:97], v216 offset:28672\n\t"
      "ds_read_b64_tr_b16 v[98:99], v220 offset:28672\n\t"
      "ds_read_b64_tr_b16 v[100:101], v217 offset:28672\n\t"
      "ds_read_b64_tr_b16 v[102:103], v221 offset:28672\n\t"
      "ds_read_b64_tr_b16 v[104:105], v218 offset:28672\n\t"
      "ds_read_b64_tr_b16 v[106:107], v222 offset:28672\n\t"
      "ds_read_b64_tr_b16 v[108:109], v219 offset:28672\n\t"
      "ds_read_b64_tr_b16 v[110:111], v223 offset:28672\n\t"
      "ds_read_b64_tr_b16 v[112:113], v218 offset:32256\n\t"
      "ds_read_b64_tr_b16 v[114:115], v222 offset:32256\n\t"
      "ds_read_b64_tr_b16 v[116:117], v219 offset:32256\n\t"
      "ds_read_b64_tr_b16 v[118:119], v223 offset:32256\n\t"
      "ds_read_b64_tr_b16 v[120:121], v216 offset:32256\n\t"
      "ds_read_b64_tr_b16 v[122:123], v220 offset:32256\n\t"
      "ds_read_b64_tr_b16 v[124:125], v217 offset:32256\n\t"
      "ds_read_b64_tr_b16 v[126:127], v221 offset:32256\n\t"
      "v_lshlrev_b32_e32 v192, 16, v208\n\t"
      "v_and_b32_e32 v193, 0xffff0000, v208\n\t"
      "v_lshlrev_b32_e32 v194, 16, v209\n\t"
      "v_and_b32_e32 v195, 0xffff0000, v209\n\t"
      "v_lshlrev_b32_e32 v196, 16, v210\n\t"
      "v_and_b32_e32 v197, 0xffff0000, v210\n\t"
      "v_lshlrev_b32_e32 v198, 16, v211\n\t"
      "v_and_b32_e32 v199, 0xffff0000, v211\n\t"
      "v_lshlrev_b32_e32 v200, 16, v212\n\t"
      "v_and_b32_e32 v201, 0xffff0000, v212\n\t"
      "v_lshlrev_b32_e32 v202, 16, v213\n\t"
      "v_and_b32_e32 v203, 0xffff0000, v213\n\t"
      "v_lshlrev_b32_e32 v204, 16, v214\n\t"
      "v_and_b32_e32 v205, 0xffff0000, v214\n\t"
      "v_lshlrev_b32_e32 v206, 16, v215\n\t"
      "v_and_b32_e32 v207, 0xffff0000, v215\n\t"
      "s_waitcnt lgkmcnt(0)\n\t"
      "s_branch LGM_%=\n\t"
      "LSR_%=:\n\t"
      "s_mov_b32 s80, 0\n\t"
      "LSL_%=:\n\t"
      "s_add_u32 s80, s80, 1\n\t"
      "s_cmp_lt_u32 s80, 262144\n\t"
      "s_cbranch_scc0 LSX_%=\n\t"
      "s_sleep 1\n\t"
      "global_load_dwordx2 v[148:149], v231, s[46:47] sc1\n\t"
      "global_load_dwordx2 v[150:151], v231, s[46:47] offset:384 sc1\n\t"
      "s_waitcnt vmcnt(0)\n\t"
      "v_max3_u32 v232, v148, v149, v150\n\t"
      "v_max_u32_e32 v232, v232, v151\n\t"
      "v_cmp_eq_u32_e32 vcc, -1, v232\n\t"
      "s_cbranch_vccnz LSL_%=\n\t"
      "LSX_%=:\n\t"
      "s_branch LSB_%=\n\t"
      "LF1R_%=:\n\t"
      "s_mov_b32 s80, 0\n\t"
      "LF1L_%=:\n\t"
      "s_add_u32 s80, s80, 1\n\t"
      "s_cmp_lt_u32 s80, 262144\n\t"
      "s_cbranch_scc0 LF1X_%=\n\t"
      "s_sleep 2\n\t"
      "global_load_dwordx2 v[138:139], v136, s[60:61] sc1\n\t"
      "s_waitcnt vmcnt(0)\n\t"
      "v_cmp_ne_u64_e32 vcc, s[22:23], v[138:139]\n\t"
      "s_cbranch_vccnz LF1L_%=\n\t"
      "LF1X_%=:\n\t"
      "s_branch LF1B_%=\n\t"
      "LF4R_%=:\n\t"
      "s_mov_b32 s80, 0\n\t"
      "LF4L_%=:\n\t"
      "s_add_u32 s80, s80, 1\n\t"
      "s_cmp_lt_u32 s80, 262144\n\t"
      "s_cbranch_scc0 LF4X_%=\n\t"
      "s_sleep 2\n\t"
      "global_load_dwordx2 v[138:139], v137, s[60:61] sc1\n\t"
      "s_waitcnt vmcnt(0)\n\t"
      "v_cmp_ne_u64_e32 vcc, s[22:23], v[138:139]\n\t"
      "s_cbranch_vccnz LF4L_%=\n\t"
      "LF4X_%=:\n\t"
      "s_branch LF4B_%=\n\t"
      "L4_%=:\n\t"
      "s_lshl2_add_u32 s72, s31, s24\n\t"
      "s_sub_u32 s72, s72, 56\n\t"
      "s_mov_b32 m0, 64512\n\t"
      "s_lshl1_add_u32 s72, s72, 0\n\t"
      "s_mul_i32 s72, s72, 65536\n\t"
      "s_and_b32 s73, s2, 7\n\t"
      "s_mul_i32 s73, s73, 512\n\t"
      "s_and_b32 s73, s73, 0xffffff80\n\t"
      "s_add_u32 s72, s72, s73\n\t"
      "v_bfe_u32 v141, v192, 2, 3\n\t"
      "v_mul_u32_u24_e32 v141, 4096, v141\n\t"
      "v_lshl_add_u32 v141, v196, 7, v141\n\t"
      "v_add_u32_e32 v141, s72, v141\n\t"
      "global_load_lds_dword v141, s[14:15]\n\t"
      "s_waitcnt vmcnt(1)\n\t"
      "s_barrier\n\t"
      "s_barrier\n\t"
      "s_lshl2_add_u32 s72, s31, s24\n\t"
      "s_sub_u32 s72, s72, 56\n\t"
      "s_mov_b32 m0, 64512\n\t"
      "s_cmp_ge_u32 s72, 64\n\t"
      "s_cselect_b32 s73, 8, 0\n\t"
      "s_sub_u32 s73, s72, s73\n\t"
      "s_and_b32 s73, s73, 0x38\n\t"
      "s_add_u32 s72, s72, s73\n\t"
      "s_add_u32 s72, s72, 8\n\t"
      "s_mul_i32 s72, s72, 57344\n\t"
      "s_and_b32 s73, s2, 7\n\t"
      "s_mul_i32 s73, s73, 448\n\t"
      "s_and_b32 s73, s73, 0xffffff80\n\t"
      "s_add_u32 s72, s72, s73\n\t"
      "v_bfe_u32 v141, v192, 2, 4\n\t"
      "v_mul_u32_u24_e32 v141, 3584, v141\n\t"
      "v_lshl_add_u32 v141, v196, 7, v141\n\t"
      "v_add_u32_e32 v141, s72, v141\n\t"
      "global_load_lds_dword v141, s[10:11]\n\t"
      "s_barrier\n\t"
      "s_cmp_lg_u32 s24, 3\n\t"
      "s_cbranch_scc1 SK12_%=\n\t"
      "global_load_dwordx2 v[138:139], v137, s[60:61] sc1\n\t"
      "SK12_%=:\n\t"
      "s_cmp_lg_u32 s24, 3\n\t"
      "s_cbranch_scc1 LF4B_%=\n\t"
      "s_waitcnt vmcnt(0)\n\t"
      "v_cmp_ne_u64_e32 vcc, s[22:23], v[138:139]\n\t"
      "s_cbranch_vccnz LF4R_%=\n\t"
      "LF4B_%=:\n\t"
      "s_barrier\n\t"
      "s_waitcnt vmcnt(0)\n\t"
      "s_barrier\n\t"
      "s_branch G4_%=\n\t"
      "LF2R_%=:\n\t"
      "s_mov_b32 s80, 0\n\t"
      "LF2L_%=:\n\t"
      "s_add_u32 s80, s80, 1\n\t"
      "s_cmp_lt_u32 s80, 262144\n\t"
      "s_cbranch_scc0 LF2X_%=\n\t"
      "s_sleep 2\n\t"
      "global_load_dwordx2 v[138:139], v137, s[60:61] sc1\n\t"
      "s_waitcnt vmcnt(0)\n\t"
      "v_cmp_ne_u64_e32 vcc, s[22:23], v[138:139]\n\t"
      "s_cbranch_vccnz LF2L_%=\n\t"
      "LF2X_%=:\n\t"
      "s_branch LF2B_%=\n\t"
      "LF3R_%=:\n\t"
      "s_mov_b32 s80, 0\n\t"
      "LF3L_%=:\n\t"
      "s_add_u32 s80, s80, 1\n\t"
      "s_cmp_lt_u32 s80, 262144\n\t"
      "s_cbranch_scc0 LF3X_%=\n\t"
      "s_sleep 2\n\t"
      "global_load_dwordx2 v[138:139], v143, s[60:61] sc1\n\t"
      "s_waitcnt vmcnt(0)\n\t"
      "v_cmp_ne_u64_e32 vcc, s[22:23], v[138:139]\n\t"
      "s_cbranch_vccnz LF3L_%=\n\t"
      "LF3X_%=:\n\t"
      "s_branch LF3B_%=\n\t"
      "LF70R_%=:\n\t"
      "s_mov_b32 s80, 0\n\t"
      "LF70L_%=:\n\t"
      "s_add_u32 s80, s80, 1\n\t"
      "s_cmp_lt_u32 s80, 262144\n\t"
      "s_cbranch_scc0 LF70X_%=\n\t"
      "s_sleep 4\n\t"
      "global_load_dwordx2 v[138:139], v136, s[60:61] sc1\n\t"
      "s_waitcnt vmcnt(0)\n\t"
      "v_cmp_ne_u64_e32 vcc, s[22:23], v[138:139]\n\t"
      "s_cbranch_vccnz LF70L_%=\n\t"
      "LF70X_%=:\n\t"
      "s_branch LF70B_%=\n\t"
      "LF71R_%=:\n\t"
      "s_mov_b32 s80, 0\n\t"
      "LF71L_%=:\n\t"
      "s_add_u32 s80, s80, 1\n\t"
      "s_cmp_lt_u32 s80, 262144\n\t"
      "s_cbranch_scc0 LF71X_%=\n\t"
      "s_sleep 4\n\t"
      "global_load_dwordx2 v[138:139], v136, s[60:61] sc1\n\t"
      "s_waitcnt vmcnt(0)\n\t"
      "v_cmp_ne_u64_e32 vcc, s[22:23], v[138:139]\n\t"
      "s_cbranch_vccnz LF71L_%=\n\t"
      "LF71X_%=:\n\t"
      "s_branch LF71B_%=\n\t"
      "SPARE_%=:\n\t"
      "v_mov_b32_e32 v132, -1\n\t"
      "v_mov_b32_e32 v133, -1\n\t"
      "v_mov_b32_e32 v134, -1\n\t"
      "v_mov_b32_e32 v135, -1\n\t"
      "s_and_b32 s34, s25, 3\n\t"
      "s_lshl_b32 s34, s34, 15\n\t"
      "s_mul_i32 s35, s24, 917504\n\t"
      "s_add_u32 s34, s34, s35\n\t"
      "s_and_b32 s35, s2, 7\n\t"
      "s_lshl_b32 s35, s35, 9\n\t"
      "s_add_u32 s34, s34, s35\n\t"
      "v_and_b32_e32 v141, 0x1c0, v244\n\t"
      "v_lshlrev_b32_e32 v141, 6, v141\n\t"
      "v_and_b32_e32 v142, 0x30, v244\n\t"
      "v_lshl_add_u32 v141, v142, 3, v141\n\t"
      "v_add_u32_e32 v141, s34, v141\n\t"
      "s_mov_b32 m0, 64512\n\t"
      "s_lshr_b32 s34, s24, 1\n\t"
      "s_lshl_b32 s34, s34, 5\n\t"
      "s_add_u32 s34, s34, s25\n\t"
      "s_and_b32 s35, s24, 1\n\t"
      "s_lshl1_add_u32 s34, s34, s35\n\t"
      "s_lshl_b32 s34, s34, 10\n\t"
      "s_add_u32 s34, s34, 8388608\n\t"
      "s_add_u32 s40, s20, s34\n\t"
      "s_addc_u32 s41, s21, 0\n\t"
      "global_store_dwordx4 v244, v[132:135], s[40:41] sc1\n\t"
      "s_add_u32 s40, s40, 262144\n\t"
      "s_addc_u32 s41, s41, 0\n\t"
      "global_store_dwordx4 v244, v[132:135], s[40:41] sc1\n\t"
      "s_add_u32 s40, s40, 262144\n\t"
      "s_addc_u32 s41, s41, 0\n\t"
      "global_store_dwordx4 v244, v[132:135], s[40:41] sc1\n\t"
      "s_add_u32 s40, s40, 262144\n\t"
      "s_addc_u32 s41, s41, 0\n\t"
      "global_store_dwordx4 v244, v[132:135], s[40:41] sc1\n\t"
      "s_add_u32 s40, s40, 262144\n\t"
      "s_addc_u32 s41, s41, 0\n\t"
      "global_store_dwordx4 v244, v[132:135], s[40:41] sc1\n\t"
      "s_add_u32 s40, s40, 262144\n\t"
      "s_addc_u32 s41, s41, 0\n\t"
      "global_store_dwordx4 v244, v[132:135], s[40:41] sc1\n\t"
      "s_add_u32 s40, s40, 262144\n\t"
      "s_addc_u32 s41, s41, 0\n\t"
      "global_store_dwordx4 v244, v[132:135], s[40:41] sc1\n\t"
      "s_add_u32 s40, s40, 262144\n\t"
      "s_addc_u32 s41, s41, 0\n\t"
      "s_lshl_b32 s34, s25, 7\n\t"
      "s_add_u32 s34, s34, 16384\n\t"
      "v_mov_b32_e32 v131, s34\n\t"
      "s_waitcnt vmcnt(0)\n\t"
      "s_barrier\n\t"
      "s_cmp_lg_u32 s24, 0\n\t"
      "s_cbranch_scc1 SP0_%=\n\t"
      "s_mov_b64 exec, 1\n\t"
      "global_store_dwordx2 v131, v[128:129], s[60:61] sc1\n\t"
      "s_mov_b64 exec, -1\n\t"
      "SP0_%=:\n\t"
      "s_and_b32 s34, s25, 3\n\t"
      "s_lshl3_add_u32 s34, s34, s24\n\t"
      "s_cmp_lg_u32 s34, 19\n\t"
      "s_cbranch_scc1 SQ0_%=\n\t"
      "v_and_b32_e32 v136, 0x1f0, v244\n\t"
      "v_lshlrev_b32_e32 v131, 6, v136\n\t"
      "v_lshlrev_b32_e32 v136, 3, v136\n\t"
      "v_add_u32_e32 v136, 16384, v136\n\t"
      "s_and_b32 s34, s2, 7\n\t"
      "s_lshl_b32 s34, s34, 7\n\t"
      "s_add_u32 s34, s34, 32768\n\t"
      "v_add_u32_e32 v131, s34, v131\n\t"
      "s_cmp_lg_u32 s24, 3\n\t"
      "s_cbranch_scc1 SK15_%=\n\t"
      "global_load_dwordx2 v[138:139], v136, s[60:61] sc1\n\t"
      "SK15_%=:\n\t"
      "s_cmp_lg_u32 s24, 3\n\t"
      "s_cbranch_scc1 LF70B_%=\n\t"
      "s_waitcnt vmcnt(0)\n\t"
      "v_cmp_ne_u64_e32 vcc, s[22:23], v[138:139]\n\t"
      "s_cbranch_vccnz LF70R_%=\n\t"
      "LF70B_%=:\n\t"
      "global_store_dwordx2 v131, v[128:129], s[60:61] sc1\n\t"
      "SQ0_%=:\n\t"
      "s_barrier\n\t"
      "s_lshl3_add_u32 s34, s25, s24\n\t"
      "s_mul_i32 s34, s34, 7168\n\t"
      "s_add_u32 s34, s34, 10227712\n\t"
      "s_add_u32 s40, s20, s34\n\t"
      "s_addc_u32 s41, s21, 0\n\t"
      "global_store_dwordx4 v244, v[132:135], s[40:41] offset:-4096 sc1\n\t"
      "global_store_dwordx4 v244, v[132:135], s[40:41] offset:-3072 sc1\n\t"
      "global_store_dwordx4 v244, v[132:135], s[40:41] offset:-2048 sc1\n\t"
      "global_store_dwordx4 v244, v[132:135], s[40:41] offset:-1024 sc1\n\t"
      "global_store_dwordx4 v244, v[132:135], s[40:41] sc1\n\t"
      "global_store_dwordx4 v244, v[132:135], s[40:41] offset:1024 sc1\n\t"
      "global_store_dwordx4 v244, v[132:135], s[40:41] offset:2048 sc1\n\t"
      "s_lshl_b32 s34, s25, 7\n\t"
      "s_add_u32 s34, s34, 24576\n\t"
      "v_mov_b32_e32 v131, s34\n\t"
      "s_waitcnt vmcnt(0)\n\t"
      "s_barrier\n\t"
      "s_cmp_lg_u32 s24, 0\n\t"
      "s_cbranch_scc1 SP1_%=\n\t"
      "s_mov_b64 exec, 1\n\t"
      "global_store_dwordx2 v131, v[128:129], s[60:61] sc1\n\t"
      "s_mov_b64 exec, -1\n\t"
      "SP1_%=:\n\t"
      "s_and_b32 s34, s25, 3\n\t"
      "s_lshl3_add_u32 s34, s34, s24\n\t"
      "s_cmp_lg_u32 s34, 27\n\t"
      "s_cbranch_scc1 SQ1_%=\n\t"
      "v_and_b32_e32 v136, 0x1f0, v244\n\t"
      "v_lshlrev_b32_e32 v131, 6, v136\n\t"
      "v_lshlrev_b32_e32 v136, 3, v136\n\t"
      "v_add_u32_e32 v136, 24576, v136\n\t"
      "s_and_b32 s34, s2, 7\n\t"
      "s_lshl_b32 s34, s34, 7\n\t"
      "s_add_u32 s34, s34, 65536\n\t"
      "v_add_u32_e32 v131, s34, v131\n\t"
      "s_cmp_lg_u32 s24, 3\n\t"
      "s_cbranch_scc1 SK16_%=\n\t"
      "global_load_dwordx2 v[138:139], v136, s[60:61] sc1\n\t"
      "SK16_%=:\n\t"
      "s_cmp_lg_u32 s24, 3\n\t"
      "s_cbranch_scc1 LF71B_%=\n\t"
      "s_waitcnt vmcnt(0)\n\t"
      "v_cmp_ne_u64_e32 vcc, s[22:23], v[138:139]\n\t"
      "s_cbranch_vccnz LF71R_%=\n\t"
      "LF71B_%=:\n\t"
      "global_store_dwordx2 v131, v[128:129], s[60:61] sc1\n\t"
      "SQ1_%=:\n\t"
      "s_barrier\n\t"
      "s_and_b32 s35, s25, 3\n\t"
      "s_lshl2_add_u32 s31, s26, s35\n\t"
      "s_and_b32 s35, s2, 7\n\t"
      "s_mul_i32 s34, s35, 14\n\t"
      "s_lshl4_add_u32 s35, s35, -14\n\t"
      "s_cmp_lt_u32 s31, 14\n\t"
      "s_cselect_b32 s34, s34, s35\n\t"
      "s_cselect_b32 s36, 1, 0\n\t"
      "s_or_b32 s27, s27, s36\n\t"
      "s_add_u32 s34, s34, s31\n\t"
      "s_lshr_b32 s29, s34, 2\n\t"
      "s_and_b32 s32, s34, 3\n\t"
      "s_mul_i32 s30, s32, 7\n\t"
      "v_and_b32_e32 v199, 60, v192\n\t"
      "v_and_b32_e32 v200, 24, v192\n\t"
      "v_lshlrev_b32_e32 v202, 3, v196\n\t"
      "v_lshl_or_b32 v202, v200, 2, v202\n\t"
      "v_lshl_or_b32 v202, v195, 7, v202\n\t"
      "v_lshl_or_b32 v202, v193, 10, v202\n\t"
      "v_add_u32_e32 v224, s39, v202\n\t"
      "v_xor_b32_e32 v225, 32, v224\n\t"
      "v_xor_b32_e32 v226, 64, v224\n\t"
      "v_xor_b32_e32 v227, 96, v224\n\t"
      "s_lshl_b32 s34, s24, 12\n\t"
      "s_add_u32 s34, s34, 98304\n\t"
      "v_add_u32_e32 v245, s34, v244\n\t"
      "v_lshlrev_b32_e32 v199, 2, v199\n\t"
      "s_lshl_b32 s34, s24, 8\n\t"
      "v_lshl_add_u32 v247, v192, 2, s34\n\t"
      "v_and_b32_e32 v200, 2, v192\n\t"
      "v_mul_u32_u24_e32 v202, 576, v200\n\t"
      "v_xor_b32_e32 v199, v202, v199\n\t"
      "v_and_b32_e32 v202, 1, v192\n\t"
      "v_lshl_or_b32 v199, v202, 3, v199\n\t"
      "s_and_b32 s35, s34, 0x400\n\t"
      "s_add_u32 s34, s34, s35\n\t"
      "s_add_u32 s34, s34, 98304\n\t"
      "v_add_u32_e32 v246, s34, v199\n\t"
      "v_lshlrev_b32_e32 v199, 2, v202\n\t"
      "v_lshl_or_b32 v199, v200, 4, v199\n\t"
      "s_min_u32 s34, s29, 27\n\t"
      "s_lshl_b32 s34, s34, 7\n\t"
      "s_lshr_b32 s35, s24, 2\n\t"
      "s_lshl_b32 s35, s35, 6\n\t"
      "s_add_u32 s34, s34, s35\n\t"
      "s_lshr_b32 s35, s39, 9\n\t"
      "s_add_u32 s34, s34, s35\n\t"
      "v_add_u32_e32 v248, s34, v199\n\t"
      "s_add_u32 s48, s20, 8388608\n\t"
      "s_addc_u32 s49, s21, 0\n\t"
      "s_cmp_ge_u32 s31, 30\n\t"
      "s_cbranch_scc1 BYE_%=\n\t"
      "s_setprio 0\n\t"
      "s_waitcnt vmcnt(0)\n\t"
      "s_barrier\n\t"
      "s_branch G4_%=\n\t"
      "BYE_%=:\n\t"
      "s_endpgm\n\t"
      "LA30R_%=:\n\t"
      "s_mov_b32 s80, 0\n\t"
      "LA30L_%=:\n\t"
      "s_add_u32 s80, s80, 1\n\t"
      "s_cmp_lt_u32 s80, 262144\n\t"
      "s_cbranch_scc0 LA30X_%=\n\t"
      "s_waitcnt vmcnt(0)\n\t"
      "s_sleep 1\n\t"
      "global_load_dwordx4 v[128:131], v250, s[64:65] offset:-4096 sc1\n\t"
      "global_load_dwordx4 v[132:135], v250, s[64:65] offset:-3072 sc1\n\t"
      "global_load_dwordx4 v[136:139], v250, s[64:65] offset:-2048 sc1\n\t"
      "global_load_dwordx4 v[140:143], v250, s[64:65] offset:-1024 sc1\n\t"
      "global_load_dwordx4 v[144:147], v250, s[64:65] sc1\n\t"
      "global_load_dwordx4 v[148:151], v250, s[64:65] offset:1024 sc1\n\t"
      "global_load_dwordx4 v[152:155], v250, s[64:65] offset:2048 sc1\n\t"
      "global_load_dwordx4 v[156:159], v250, s[64:65] offset:3072 sc1\n\t"
      "s_waitcnt vmcnt(0)\n\t"
      "v_max3_u32 v251, v128, v129, v130\n\t"
      "v_max3_u32 v251, v251, v131, v132\n\t"
      "v_max3_u32 v251, v251, v133, v134\n\t"
      "v_max3_u32 v251, v251, v135, v136\n\t"
      "v_max3_u32 v251, v251, v137, v138\n\t"
      "v_max3_u32 v251, v251, v139, v140\n\t"
      "v_max3_u32 v251, v251, v141, v142\n\t"
      "v_max3_u32 v251, v251, v143, v144\n\t"
      "v_max3_u32 v251, v251, v145, v146\n\t"
      "v_max3_u32 v251, v251, v147, v148\n\t"
      "v_max3_u32 v251, v251, v149, v150\n\t"
      "v_max3_u32 v251, v251, v151, v152\n\t"
      "v_max3_u32 v251, v251, v153, v154\n\t"
      "v_max3_u32 v251, v251, v155, v156\n\t"
      "v_max3_u32 v251, v251, v157, v158\n\t"
      "v_max_u32_e32 v251, v251, v159\n\t"
      "v_cmp_eq_u32_e32 vcc, -1, v251\n\t"
      "s_cbranch_vccnz LA30L_%=\n\t"
      "LA30X_%=:\n\t"
      "s_branch LA30B_%=\n\t"
      "LF6R_%=:\n\t"
      "s_mov_b32 s80, 0\n\t"
      "LF6L_%=:\n\t"
      "s_add_u32 s80, s80, 1\n\t"
      "s_cmp_lt_u32 s80, 262144\n\t"
      "s_cbranch_scc0 LF6X_%=\n\t"
      "s_sleep 2\n\t"
      "global_load_dwordx2 v[228:229], v230, s[60:61] sc1\n\t"
      "s_waitcnt vmcnt(0)\n\t"
      "v_cmp_ne_u64_e32 vcc, s[22:23], v[228:229]\n\t"
      "s_cbranch_vccnz LF6L_%=\n\t"
      "LF6X_%=:\n\t"
      "s_branch LF6B_%=\n\t"
      "LA31R_%=:\n\t"
      "s_mov_b32 s80, 0\n\t"
      "LA31L_%=:\n\t"
      "s_add_u32 s80, s80, 1\n\t"
      "s_cmp_lt_u32 s80, 262144\n\t"
      "s_cbranch_scc0 LA31X_%=\n\t"
      "s_waitcnt vmcnt(0)\n\t"
      "s_sleep 1\n\t"
      "global_load_dwordx4 v[160:163], v250, s[66:67] offset:-4096 sc1\n\t"
      "global_load_dwordx4 v[164:167], v250, s[66:67] offset:-3072 sc1\n\t"
      "global_load_dwordx4 v[168:171], v250, s[66:67] offset:-2048 sc1\n\t"
      "global_load_dwordx4 v[172:175], v250, s[66:67] offset:-1024 sc1\n\t"
      "global_load_dwordx4 v[176:179], v250, s[66:67] sc1\n\t"
      "global_load_dwordx4 v[180:183], v250, s[66:67] offset:1024 sc1\n\t"
      "global_load_dwordx4 v[184:187], v250, s[66:67] offset:2048 sc1\n\t"
      "global_load_dwordx4 v[188:191], v250, s[66:67] offset:3072 sc1\n\t"
      "s_waitcnt vmcnt(0)\n\t"
      "v_max3_u32 v251, v160, v161, v162\n\t"
      "v_max3_u32 v251, v251, v163, v164\n\t"
      "v_max3_u32 v251, v251, v165, v166\n\t"
      "v_max3_u32 v251, v251, v167, v168\n\t"
      "v_max3_u32 v251, v251, v169, v170\n\t"
      "v_max3_u32 v251, v251, v171, v172\n\t"
      "v_max3_u32 v251, v251, v173, v174\n\t"
      "v_max3_u32 v251, v251, v175, v176\n\t"
      "v_max3_u32 v251, v251, v177, v178\n\t"
      "v_max3_u32 v251, v251, v179, v180\n\t"
      "v_max3_u32 v251, v251, v181, v182\n\t"
      "v_max3_u32 v251, v251, v183, v184\n\t"
      "v_max3_u32 v251, v251, v185, v186\n\t"
      "v_max3_u32 v251, v251, v187, v188\n\t"
      "v_max3_u32 v251, v251, v189, v190\n\t"
      "v_max_u32_e32 v251, v251, v191\n\t"
      "v_cmp_eq_u32_e32 vcc, -1, v251\n\t"
      "s_cbranch_vccnz LA31L_%=\n\t"
      "LA31X_%=:\n\t"
      "v_mfma_f32_16x16x32_bf16 v[224:227], v[0:3], v[160:163], 0\n\t"
      "v_mfma_f32_16x16x32_bf16 v[224:227], v[16:19], v[164:167], v[224:227]\n\t"
      "v_mfma_f32_16x16x32_bf16 v[224:227], v[32:35], v[168:171], v[224:227]\n\t"
      "v_mfma_f32_16x16x32_bf16 v[224:227], v[48:51], v[172:175], v[224:227]\n\t"
      "v_mfma_f32_16x16x32_bf16 v[224:227], v[64:67], v[176:179], v[224:227]\n\t"
      "v_mfma_f32_16x16x32_bf16 v[224:227], v[80:83], v[180:183], v[224:227]\n\t"
      "v_mfma_f32_16x16x32_bf16 v[224:227], v[96:99], v[184:187], v[224:227]\n\t"
      "v_mfma_f32_16x16x32_bf16 v[224:227], v[112:115], v[188:191], v[224:227]\n\t"
      "v_mfma_f32_16x16x32_bf16 v[208:211], v[4:7], v[160:163], 0\n\t"
      "v_mfma_f32_16x16x32_bf16 v[208:211], v[20:23], v[164:167], v[208:211]\n\t"
      "v_mfma_f32_16x16x32_bf16 v[208:211], v[36:39], v[168:171], v[208:211]\n\t"
      "s_nop 7\n\t"
      "s_branch LA31B_%=\n\t"
      "LA32R_%=:\n\t"
      "s_mov_b32 s80, 0\n\t"
      "LA32L_%=:\n\t"
      "s_add_u32 s80, s80, 1\n\t"
      "s_cmp_lt_u32 s80, 262144\n\t"
      "s_cbranch_scc0 LA32X_%=\n\t"
      "s_waitcnt vmcnt(0)\n\t"
      "s_sleep 1\n\t"
      "global_load_dwordx4 v[192:195], v250, s[68:69] offset:-4096 sc1\n\t"
      "global_load_dwordx4 v[196:199], v250, s[68:69] offset:-3072 sc1\n\t"
      "global_load_dwordx4 v[200:203], v250, s[68:69] offset:-2048 sc1\n\t"
      "global_load_dwordx4 v[204:207], v250, s[68:69] offset:-1024 sc1\n\t"
      "global_load_dwordx4 v[128:131], v250, s[68:69] sc1\n\t"
      "global_load_dwordx4 v[132:135], v250, s[68:69] offset:1024 sc1\n\t"
      "global_load_dwordx4 v[136:139], v250, s[68:69] offset:2048 sc1\n\t"
      "global_load_dwordx4 v[140:143], v250, s[68:69] offset:3072 sc1\n\t"
      "s_waitcnt vmcnt(0)\n\t"
      "v_max3_u32 v251, v192, v193, v194\n\t"
      "v_max3_u32 v251, v251, v195, v196\n\t"
      "v_max3_u32 v251, v251, v197, v198\n\t"
      "v_max3_u32 v251, v251, v199, v200\n\t"
      "v_max3_u32 v251, v251, v201, v202\n\t"
      "v_max3_u32 v251, v251, v203, v204\n\t"
      "v_max3_u32 v251, v251, v205, v206\n\t"
      "v_max3_u32 v251, v251, v207, v128\n\t"
      "v_max3_u32 v251, v251, v129, v130\n\t"
      "v_max3_u32 v251, v251, v131, v132\n\t"
      "v_max3_u32 v251, v251, v133, v134\n\t"
      "v_max3_u32 v251, v251, v135, v136\n\t"
      "v_max3_u32 v251, v251, v137, v138\n\t"
      "v_max3_u32 v251, v251, v139, v140\n\t"
      "v_max3_u32 v251, v251, v141, v142\n\t"
      "v_max_u32_e32 v251, v251, v143\n\t"
      "v_cmp_eq_u32_e32 vcc, -1, v251\n\t"
      "s_cbranch_vccnz LA32L_%=\n\t"
      "LA32X_%=:\n\t"
      "v_mfma_f32_16x16x32_bf16 v[220:223], v[0:3], v[192:195], 0\n\t"
      "v_mfma_f32_16x16x32_bf16 v[220:223], v[16:19], v[196:199], v[220:223]\n\t"
      "v_mfma_f32_16x16x32_bf16 v[220:223], v[32:35], v[200:203], v[220:223]\n\t"
      "v_mfma_f32_16x16x32_bf16 v[220:223], v[48:51], v[204:207], v[220:223]\n\t"
      "v_mfma_f32_16x16x32_bf16 v[220:223], v[64:67], v[128:131], v[220:223]\n\t"
      "v_mfma_f32_16x16x32_bf16 v[220:223], v[80:83], v[132:135], v[220:223]\n\t"
      "v_mfma_f32_16x16x32_bf16 v[220:223], v[96:99], v[136:139], v[220:223]\n\t"
      "v_mfma_f32_16x16x32_bf16 v[220:223], v[112:115], v[140:143], v[220:223]\n\t"
      "v_mfma_f32_16x16x32_bf16 v[224:227], v[4:7], v[192:195], 0\n\t"
      "v_mfma_f32_16x16x32_bf16 v[224:227], v[20:23], v[196:199], v[224:227]\n\t"
      "v_mfma_f32_16x16x32_bf16 v[224:227], v[36:39], v[200:203], v[224:227]\n\t"
      "s_nop 7\n\t"
      "s_branch LA32B_%=\n\t"
      "LA33R_%=:\n\t"
      "s_mov_b32 s80, 0\n\t"
      "LA33L_%=:\n\t"
      "s_add_u32 s80, s80, 1\n\t"
      "s_cmp_lt_u32 s80, 262144\n\t"
      "s_cbranch_scc0 LA33X_%=\n\t"
      "s_waitcnt vmcnt(0)\n\t"
      "s_sleep 1\n\t"
      "global_load_dwordx4 v[144:147], v250, s[70:71] offset:-4096 sc1\n\t"
      "global_load_dwordx4 v[148:151], v250, s[70:71] offset:-3072 sc1\n\t"
      "global_load_dwordx4 v[152:155], v250, s[70:71] offset:-2048 sc1\n\t"
      "global_load_dwordx4 v[156:159], v250, s[70:71] offset:-1024 sc1\n\t"
      "global_load_dwordx4 v[160:163], v250, s[70:71] sc1\n\t"
      "global_load_dwordx4 v[164:167], v250, s[70:71] offset:1024 sc1\n\t"
      "global_load_dwordx4 v[168:171], v250, s[70:71] offset:2048 sc1\n\t"
      "global_load_dwordx4 v[172:175], v250, s[70:71] offset:3072 sc1\n\t"
      "s_waitcnt vmcnt(0)\n\t"
      "v_max3_u32 v251, v144, v145, v146\n\t"
      "v_max3_u32 v251, v251, v147, v148\n\t"
      "v_max3_u32 v251, v251, v149, v150\n\t"
      "v_max3_u32 v251, v251, v151, v152\n\t"
      "v_max3_u32 v251, v251, v153, v154\n\t"
      "v_max3_u32 v251, v251, v155, v156\n\t"
      "v_max3_u32 v251, v251, v157, v158\n\t"
      "v_max3_u32 v251, v251, v159, v160\n\t"
      "v_max3_u32 v251, v251, v161, v162\n\t"
      "v_max3_u32 v251, v251, v163, v164\n\t"
      "v_max3_u32 v251, v251, v165, v166\n\t"
      "v_max3_u32 v251, v251, v167, v168\n\t"
      "v_max3_u32 v251, v251, v169, v170\n\t"
      "v_max3_u32 v251, v251, v171, v172\n\t"
      "v_max3_u32 v251, v251, v173, v174\n\t"
      "v_max_u32_e32 v251, v251, v175\n\t"
      "v_cmp_eq_u32_e32 vcc, -1, v251\n\t"
      "s_cbranch_vccnz LA33L_%=\n\t"
      "LA33X_%=:\n\t"
      "v_mfma_f32_16x16x32_bf16 v[216:219], v[0:3], v[144:147], 0\n\t"
      "v_mfma_f32_16x16x32_bf16 v[216:219], v[16:19], v[148:151], v[216:219]\n\t"
      "v_mfma_f32_16x16x32_bf16 v[216:219], v[32:35], v[152:155], v[216:219]\n\t"
      "v_mfma_f32_16x16x32_bf16 v[216:219], v[48:51], v[156:159], v[216:219]\n\t"
      "v_mfma_f32_16x16x32_bf16 v[216:219], v[64:67], v[160:163], v[216:219]\n\t"
      "v_mfma_f32_16x16x32_bf16 v[216:219], v[80:83], v[164:167], v[216:219]\n\t"
      "v_mfma_f32_16x16x32_bf16 v[216:219], v[96:99], v[168:171], v[216:219]\n\t"
      "v_mfma_f32_16x16x32_bf16 v[216:219], v[112:115], v[172:175], v[216:219]\n\t"
      "v_mfma_f32_16x16x32_bf16 v[220:223], v[4:7], v[144:147], 0\n\t"
      "v_mfma_f32_16x16x32_bf16 v[220:223], v[20:23], v[148:151], v[220:223]\n\t"
      "v_mfma_f32_16x16x32_bf16 v[220:223], v[36:39], v[152:155], v[220:223]\n\t"
      "s_nop 7\n\t"
      "s_branch LA33B_%=\n\t"
      "LA34R_%=:\n\t"
      "s_mov_b32 s80, 0\n\t"
      "LA34L_%=:\n\t"
      "s_add_u32 s80, s80, 1\n\t"
      "s_cmp_lt_u32 s80, 262144\n\t"
      "s_cbranch_scc0 LA34X_%=\n\t"
      "s_waitcnt vmcnt(0)\n\t"
      "s_sleep 1\n\t"
      "global_load_dwordx4 v[176:179], v250, s[72:73] offset:-4096 sc1\n\t"
      "global_load_dwordx4 v[180:183], v250, s[72:73] offset:-3072 sc1\n\t"
      "global_load_dwordx4 v[184:187], v250, s[72:73] offset:-2048 sc1\n\t"
      "global_load_dwordx4 v[188:191], v250, s[72:73] offset:-1024 sc1\n\t"
      "global_load_dwordx4 v[192:195], v250, s[72:73] sc1\n\t"
      "global_load_dwordx4 v[196:199], v250, s[72:73] offset:1024 sc1\n\t"
      "global_load_dwordx4 v[200:203], v250, s[72:73] offset:2048 sc1\n\t"
      "global_load_dwordx4 v[204:207], v250, s[72:73] offset:3072 sc1\n\t"
      "s_waitcnt vmcnt(0)\n\t"
      "v_max3_u32 v251, v176, v177, v178\n\t"
      "v_max3_u32 v251, v251, v179, v180\n\t"
      "v_max3_u32 v251, v251, v181, v182\n\t"
      "v_max3_u32 v251, v251, v183, v184\n\t"
      "v_max3_u32 v251, v251, v185, v186\n\t"
      "v_max3_u32 v251, v251, v187, v188\n\t"
      "v_max3_u32 v251, v251, v189, v190\n\t"
      "v_max3_u32 v251, v251, v191, v192\n\t"
      "v_max3_u32 v251, v251, v193, v194\n\t"
      "v_max3_u32 v251, v251, v195, v196\n\t"
      "v_max3_u32 v251, v251, v197, v198\n\t"
      "v_max3_u32 v251, v251, v199, v200\n\t"
      "v_max3_u32 v251, v251, v201, v202\n\t"
      "v_max3_u32 v251, v251, v203, v204\n\t"
      "v_max3_u32 v251, v251, v205, v206\n\t"
      "v_max_u32_e32 v251, v251, v207\n\t"
      "v_cmp_eq_u32_e32 vcc, -1, v251\n\t"
      "s_cbranch_vccnz LA34L_%=\n\t"
      "LA34X_%=:\n\t"
      "v_mfma_f32_16x16x32_bf16 v[212:215], v[0:3], v[176:179], 0\n\t"
      "v_mfma_f32_16x16x32_bf16 v[212:215], v[16:19], v[180:183], v[212:215]\n\t"
      "v_mfma_f32_16x16x32_bf16 v[212:215], v[32:35], v[184:187], v[212:215]\n\t"
      "v_mfma_f32_16x16x32_bf16 v[212:215], v[48:51], v[188:191], v[212:215]\n\t"
      "v_mfma_f32_16x16x32_bf16 v[212:215], v[64:67], v[192:195], v[212:215]\n\t"
      "v_mfma_f32_16x16x32_bf16 v[212:215], v[80:83], v[196:199], v[212:215]\n\t"
      "v_mfma_f32_16x16x32_bf16 v[212:215], v[96:99], v[200:203], v[212:215]\n\t"
      "v_mfma_f32_16x16x32_bf16 v[212:215], v[112:115], v[204:207], v[212:215]\n\t"
      "v_mfma_f32_16x16x32_bf16 v[216:219], v[4:7], v[176:179], 0\n\t"
      "v_mfma_f32_16x16x32_bf16 v[216:219], v[20:23], v[180:183], v[216:219]\n\t"
      "v_mfma_f32_16x16x32_bf16 v[216:219], v[36:39], v[184:187], v[216:219]\n\t"
      "s_nop 7\n\t"
      "s_branch LA34B_%=\n\t"
      "LA35R_%=:\n\t"
      "s_mov_b32 s80, 0\n\t"
      "LA35L_%=:\n\t"
      "s_add_u32 s80, s80, 1\n\t"
      "s_cmp_lt_u32 s80, 262144\n\t"
      "s_cbranch_scc0 LA35X_%=\n\t"
      "s_waitcnt vmcnt(0)\n\t"
      "s_sleep 1\n\t"
      "global_load_dwordx4 v[128:131], v250, s[74:75] offset:-4096 sc1\n\t"
      "global_load_dwordx4 v[132:135], v250, s[74:75] offset:-3072 sc1\n\t"
      "global_load_dwordx4 v[136:139], v250, s[74:75] offset:-2048 sc1\n\t"
      "global_load_dwordx4 v[140:143], v250, s[74:75] offset:-1024 sc1\n\t"
      "global_load_dwordx4 v[144:147], v250, s[74:75] sc1\n\t"
      "global_load_dwordx4 v[148:151], v250, s[74:75] offset:1024 sc1\n\t"
      "global_load_dwordx4 v[152:155], v250, s[74:75] offset:2048 sc1\n\t"
      "global_load_dwordx4 v[156:159], v250, s[74:75] offset:3072 sc1\n\t"
      "s_waitcnt vmcnt(0)\n\t"
      "v_max3_u32 v251, v128, v129, v130\n\t"
      "v_max3_u32 v251, v251, v131, v132\n\t"
      "v_max3_u32 v251, v251, v133, v134\n\t"
      "v_max3_u32 v251, v251, v135, v136\n\t"
      "v_max3_u32 v251, v251, v137, v138\n\t"
      "v_max3_u32 v251, v251, v139, v140\n\t"
      "v_max3_u32 v251, v251, v141, v142\n\t"
      "v_max3_u32 v251, v251, v143, v144\n\t"
      "v_max3_u32 v251, v251, v145, v146\n\t"
      "v_max3_u32 v251, v251, v147, v148\n\t"
      "v_max3_u32 v251, v251, v149, v150\n\t"
      "v_max3_u32 v251, v251, v151, v152\n\t"
      "v_max3_u32 v251, v251, v153, v154\n\t"
      "v_max3_u32 v251, v251, v155, v156\n\t"
      "v_max3_u32 v251, v251, v157, v158\n\t"
      "v_max_u32_e32 v251, v251, v159\n\t"
      "v_cmp_eq_u32_e32 vcc, -1, v251\n\t"
      "s_cbranch_vccnz LA35L_%=\n\t"
      "LA35X_%=:\n\t"
      "v_mfma_f32_16x16x32_bf16 v[208:211], v[0:3], v[128:131], 0\n\t"
      "v_mfma_f32_16x16x32_bf16 v[208:211], v[16:19], v[132:135], v[208:211]\n\t"
      "v_mfma_f32_16x16x32_bf16 v[208:211], v[32:35], v[136:139], v[208:211]\n\t"
      "v_mfma_f32_16x16x32_bf16 v[208:211], v[48:51], v[140:143], v[208:211]\n\t"
      "v_mfma_f32_16x16x32_bf16 v[208:211], v[64:67], v[144:147], v[208:211]\n\t"
      "v_mfma_f32_16x16x32_bf16 v[208:211], v[80:83], v[148:151], v[208:211]\n\t"
      "v_mfma_f32_16x16x32_bf16 v[208:211], v[96:99], v[152:155], v[208:211]\n\t"
      "v_mfma_f32_16x16x32_bf16 v[208:211], v[112:115], v[156:159], v[208:211]\n\t"
      "v_mfma_f32_16x16x32_bf16 v[212:215], v[4:7], v[128:131], 0\n\t"
      "v_mfma_f32_16x16x32_bf16 v[212:215], v[20:23], v[132:135], v[212:215]\n\t"
      "v_mfma_f32_16x16x32_bf16 v[212:215], v[36:39], v[136:139], v[212:215]\n\t"
      "s_nop 7\n\t"
      "s_branch LA35B_%=\n\t"
      "LA36R_%=:\n\t"
      "s_mov_b32 s80, 0\n\t"
      "LA36L_%=:\n\t"
      "s_add_u32 s80, s80, 1\n\t"
      "s_cmp_lt_u32 s80, 262144\n\t"
      "s_cbranch_scc0 LA36X_%=\n\t"
      "s_waitcnt vmcnt(0)\n\t"
      "s_sleep 1\n\t"
      "global_load_dwordx4 v[160:163], v250, s[76:77] offset:-4096 sc1\n\t"
      "global_load_dwordx4 v[164:167], v250, s[76:77] offset:-3072 sc1\n\t"
      "global_load_dwordx4 v[168:171], v250, s[76:77] offset:-2048 sc1\n\t"
      "global_load_dwordx4 v[172:175], v250, s[76:77] offset:-1024 sc1\n\t"
      "global_load_dwordx4 v[176:179], v250, s[76:77] sc1\n\t"
      "global_load_dwordx4 v[180:183], v250, s[76:77] offset:1024 sc1\n\t"
      "global_load_dwordx4 v[184:187], v250, s[76:77] offset:2048 sc1\n\t"
      "global_load_dwordx4 v[188:191], v250, s[76:77] offset:3072 sc1\n\t"
      "s_waitcnt vmcnt(0)\n\t"
      "v_max3_u32 v251, v160, v161, v162\n\t"
      "v_max3_u32 v251, v251, v163, v164\n\t"
      "v_max3_u32 v251, v251, v165, v166\n\t"
      "v_max3_u32 v251, v251, v167, v168\n\t"
      "v_max3_u32 v251, v251, v169, v170\n\t"
      "v_max3_u32 v251, v251, v171, v172\n\t"
      "v_max3_u32 v251, v251, v173, v174\n\t"
      "v_max3_u32 v251, v251, v175, v176\n\t"
      "v_max3_u32 v251, v251, v177, v178\n\t"
      "v_max3_u32 v251, v251, v179, v180\n\t"
      "v_max3_u32 v251, v251, v181, v182\n\t"
      "v_max3_u32 v251, v251, v183, v184\n\t"
      "v_max3_u32 v251, v251, v185, v186\n\t"
      "v_max3_u32 v251, v251, v187, v188\n\t"
      "v_max3_u32 v251, v251, v189, v190\n\t"
      "v_max_u32_e32 v251, v251, v191\n\t"
      "v_cmp_eq_u32_e32 vcc, -1, v251\n\t"
      "s_cbranch_vccnz LA36L_%=\n\t"
      "LA36X_%=:\n\t"
      "v_mfma_f32_16x16x32_bf16 v[224:227], v[0:3], v[160:163], 0\n\t"
      "v_mfma_f32_16x16x32_bf16 v[224:227], v[16:19], v[164:167], v[224:227]\n\t"
      "v_mfma_f32_16x16x32_bf16 v[224:227], v[32:35], v[168:171], v[224:227]\n\t"
      "v_mfma_f32_16x16x32_bf16 v[224:227], v[48:51], v[172:175], v[224:227]\n\t"
      "v_mfma_f32_16x16x32_bf16 v[224:227], v[64:67], v[176:179], v[224:227]\n\t"
      "v_mfma_f32_16x16x32_bf16 v[224:227], v[80:83], v[180:183], v[224:227]\n\t"
      "v_mfma_f32_16x16x32_bf16 v[224:227], v[96:99], v[184:187], v[224:227]\n\t"
      "v_mfma_f32_16x16x32_bf16 v[224:227], v[112:115], v[188:191], v[224:227]\n\t"
      "v_mfma_f32_16x16x32_bf16 v[208:211], v[4:7], v[160:163], 0\n\t"
      "v_mfma_f32_16x16x32_bf16 v[208:211], v[20:23], v[164:167], v[208:211]\n\t"
      "v_mfma_f32_16x16x32_bf16 v[208:211], v[36:39], v[168:171], v[208:211]\n\t"
      "s_nop 7\n\t"
      "s_branch LA36B_%=\n\t"
      "LF5R_%=:\n\t"
      "s_mov_b32 s80, 0\n\t"
      "LF5L_%=:\n\t"
      "s_add_u32 s80, s80, 1\n\t"
      "s_cmp_lt_u32 s80, 262144\n\t"
      "s_cbranch_scc0 LF5X_%=\n\t"
      "s_sleep 2\n\t"
      "global_load_dwordx2 v[138:139], v143, s[60:61] sc1\n\t"
      "s_waitcnt vmcnt(0)\n\t"
      "v_cmp_ne_u64_e32 vcc, s[22:23], v[138:139]\n\t"
      "s_cbranch_vccnz LF5L_%=\n\t"
      "LF5X_%=:\n\t"
      "s_branch LF5B_%=\n\t"
      "PGR_%=:\n\t"
      "s_mov_b32 s80, 0\n\t"
      "PGL_%=:\n\t"
      "s_add_u32 s80, s80, 1\n\t"
      "s_cmp_lt_u32 s80, 262144\n\t"
      "s_cbranch_scc0 PGX_%=\n\t"
      "s_sleep 2\n\t"
      "global_load_dword v251, v253, s[52:53] offset:252 sc1\n\t"
      "s_waitcnt vmcnt(0)\n\t"
      "v_cmp_eq_u32_e32 vcc, -1, v251\n\t"
      "s_cbranch_vccnz PGL_%=\n\t"
      "PGX_%=:\n\t"
      "s_sleep 8\n\t"
      "s_branch PGB_%=\n\t"
      "LF8R_%=:\n\t"
      "s_mov_b32 s80, 0\n\t"
      "LF8L_%=:\n\t"
      "s_add_u32 s80, s80, 1\n\t"
      "s_cmp_lt_u32 s80, 262144\n\t"
      "s_cbranch_scc0 LF8X_%=\n\t"
      "s_sleep 2\n\t"
      "global_load_dwordx2 v[240:241], v242, s[60:61] sc1\n\t"
      "s_waitcnt vmcnt(0)\n\t"
      "v_cmp_ne_u64_e32 vcc, s[22:23], v[240:241]\n\t"
      "s_cbranch_vccnz LF8L_%=\n\t"
      "LF8X_%=:\n\t"
      "s_branch LF8B_%=\n\t"
      "LA40R_%=:\n\t"
      "s_mov_b32 s80, 0\n\t"
      "LA40L_%=:\n\t"
      "s_add_u32 s80, s80, 1\n\t"
      "s_cmp_lt_u32 s80, 262144\n\t"
      "s_cbranch_scc0 LA40X_%=\n\t"
      "s_waitcnt vmcnt(0)\n\t"
      "PQ0R_%=:\n\t"
      "PQ0L_%=:\n\t"
      "s_add_u32 s80, s80, 1\n\t"
      "s_cmp_lt_u32 s80, 262144\n\t"
      "s_cbranch_scc0 PQ0X_%=\n\t"
      "s_sleep 1\n\t"
      "global_load_dword v251, v252, s[64:65] offset:252 sc1\n\t"
      "s_waitcnt vmcnt(0)\n\t"
      "v_cmp_eq_u32_e32 vcc, -1, v251\n\t"
      "s_cbranch_vccnz PQ0L_%=\n\t"
      "PQ0X_%=:\n\t"
      "s_add_u32 s58, s64, s40\n\t"
      "s_addc_u32 s59, s65, 0\n\t"
      "global_load_dwordx4 v[128:131], v244, s[58:59] sc1\n\t"
      "s_add_u32 s58, s64, s41\n\t"
      "s_addc_u32 s59, s65, 0\n\t"
      "global_load_dwordx4 v[132:135], v244, s[58:59] sc1\n\t"
      "s_add_u32 s58, s64, s42\n\t"
      "s_addc_u32 s59, s65, 0\n\t"
      "global_load_dwordx4 v[136:139], v244, s[58:59] sc1\n\t"
      "s_add_u32 s58, s64, s43\n\t"
      "s_addc_u32 s59, s65, 0\n\t"
      "global_load_dwordx4 v[140:143], v244, s[58:59] sc1\n\t"
      "s_add_u32 s58, s64, s44\n\t"
      "s_addc_u32 s59, s65, 0\n\t"
      "global_load_dwordx4 v[144:147], v244, s[58:59] sc1\n\t"
      "s_add_u32 s58, s64, s45\n\t"
      "s_addc_u32 s59, s65, 0\n\t"
      "global_load_dwordx4 v[148:151], v244, s[58:59] sc1\n\t"
      "s_add_u32 s58, s64, s46\n\t"
      "s_addc_u32 s59, s65, 0\n\t"
      "global_load_dwordx4 v[152:155], v244, s[58:59] sc1\n\t"
      "s_waitcnt vmcnt(0)\n\t"
      "v_max3_u32 v251, v128, v129, v130\n\t"
      "v_max3_u32 v251, v251, v131, v132\n\t"
      "v_max3_u32 v251, v251, v133, v134\n\t"
      "v_max3_u32 v251, v251, v135, v136\n\t"
      "v_max3_u32 v251, v251, v137, v138\n\t"
      "v_max3_u32 v251, v251, v139, v140\n\t"
      "v_max3_u32 v251, v251, v141, v142\n\t"
      "v_max3_u32 v251, v251, v143, v144\n\t"
      "v_max3_u32 v251, v251, v145, v146\n\t"
      "v_max3_u32 v251, v251, v147, v148\n\t"
      "v_max3_u32 v251, v251, v149, v150\n\t"
      "v_max3_u32 v251, v251, v151, v152\n\t"
      "v_max3_u32 v251, v251, v153, v154\n\t"
      "v_max_u32_e32 v251, v251, v155\n\t"
      "v_cmp_eq_u32_e32 vcc, -1, v251\n\t"
      "s_cbranch_vccnz LA40L_%=\n\t"
      "LA40X_%=:\n\t"
      "v_mfma_f32_16x16x32_bf16 v[196:199], v[128:131], v[0:3], 0\n\t"
      "v_mfma_f32_16x16x32_bf16 v[200:203], v[128:131], v[4:7], 0\n\t"
      "v_mfma_f32_16x16x32_bf16 v[204:207], v[128:131], v[8:11], 0\n\t"
      "v_mfma_f32_16x16x32_bf16 v[208:211], v[128:131], v[12:15], 0\n\t"
      "v_mfma_f32_16x16x32_bf16 v[196:199], v[132:135], v[16:19], v[196:199]\n\t"
      "v_mfma_f32_16x16x32_bf16 v[200:203], v[132:135], v[20:23], v[200:203]\n\t"
      "v_mfma_f32_16x16x32_bf16 v[204:207], v[132:135], v[24:27], v[204:207]\n\t"
      "v_mfma_f32_16x16x32_bf16 v[208:211], v[132:135], v[28:31], v[208:211]\n\t"
      "v_mfma_f32_16x16x32_bf16 v[196:199], v[136:139], v[32:35], v[196:199]\n\t"
      "v_mfma_f32_16x16x32_bf16 v[200:203], v[136:139], v[36:39], v[200:203]\n\t"
      "v_mfma_f32_16x16x32_bf16 v[204:207], v[136:139], v[40:43], v[204:207]\n\t"
      "v_mfma_f32_16x16x32_bf16 v[208:211], v[136:139], v[44:47], v[208:211]\n\t"
      "v_mfma_f32_16x16x32_bf16 v[196:199], v[140:143], v[48:51], v[196:199]\n\t"
      "v_mfma_f32_16x16x32_bf16 v[200:203], v[140:143], v[52:55], v[200:203]\n\t"
      "v_mfma_f32_16x16x32_bf16 v[204:207], v[140:143], v[56:59], v[204:207]\n\t"
      "v_mfma_f32_16x16x32_bf16 v[208:211], v[140:143], v[60:63], v[208:211]\n\t"
      "v_mfma_f32_16x16x32_bf16 v[196:199], v[144:147], v[64:67], v[196:199]\n\t"
      "v_mfma_f32_16x16x32_bf16 v[200:203], v[144:147], v[68:71], v[200:203]\n\t"
      "v_mfma_f32_16x16x32_bf16 v[204:207], v[144:147], v[72:75], v[204:207]\n\t"
      "v_mfma_f32_16x16x32_bf16 v[208:211], v[144:147], v[76:79], v[208:211]\n\t"
      "v_mfma_f32_16x16x32_bf16 v[196:199], v[148:151], v[80:83], v[196:199]\n\t"
      "v_mfma_f32_16x16x32_bf16 v[200:203], v[148:151], v[84:87], v[200:203]\n\t"
      "v_mfma_f32_16x16x32_bf16 v[204:207], v[148:151], v[88:91], v[204:207]\n\t"
      "v_mfma_f32_16x16x32_bf16 v[208:211], v[148:151], v[92:95], v[208:211]\n\t"
      "v_mfma_f32_16x16x32_bf16 v[196:199], v[152:155], v[96:99], v[196:199]\n\t"
      "v_mfma_f32_16x16x32_bf16 v[200:203], v[152:155], v[100:103], v[200:203]\n\t"
      "v_mfma_f32_16x16x32_bf16 v[204:207], v[152:155], v[104:107], v[204:207]\n\t"
      "v_mfma_f32_16x16x32_bf16 v[208:211], v[152:155], v[108:111], v[208:211]\n\t"
      "s_nop 7\n\t"
      "s_branch LA40B_%=\n\t"
      "LA41R_%=:\n\t"
      "s_mov_b32 s80, 0\n\t"
      "LA41L_%=:\n\t"
      "s_add_u32 s80, s80, 1\n\t"
      "s_cmp_lt_u32 s80, 262144\n\t"
      "s_cbranch_scc0 LA41X_%=\n\t"
      "s_waitcnt vmcnt(0)\n\t"
      "PQ1R_%=:\n\t"
      "PQ1L_%=:\n\t"
      "s_add_u32 s80, s80, 1\n\t"
      "s_cmp_lt_u32 s80, 262144\n\t"
      "s_cbranch_scc0 PQ1X_%=\n\t"
      "s_sleep 1\n\t"
      "global_load_dword v251, v252, s[66:67] offset:252 sc1\n\t"
      "s_waitcnt vmcnt(0)\n\t"
      "v_cmp_eq_u32_e32 vcc, -1, v251\n\t"
      "s_cbranch_vccnz PQ1L_%=\n\t"
      "PQ1X_%=:\n\t"
      "s_add_u32 s58, s66, s40\n\t"
      "s_addc_u32 s59, s67, 0\n\t"
      "global_load_dwordx4 v[156:159], v244, s[58:59] sc1\n\t"
      "s_add_u32 s58, s66, s41\n\t"
      "s_addc_u32 s59, s67, 0\n\t"
      "global_load_dwordx4 v[160:163], v244, s[58:59] sc1\n\t"
      "s_add_u32 s58, s66, s42\n\t"
      "s_addc_u32 s59, s67, 0\n\t"
      "global_load_dwordx4 v[164:167], v244, s[58:59] sc1\n\t"
      "s_add_u32 s58, s66, s43\n\t"
      "s_addc_u32 s59, s67, 0\n\t"
      "global_load_dwordx4 v[168:171], v244, s[58:59] sc1\n\t"
      "s_add_u32 s58, s66, s44\n\t"
      "s_addc_u32 s59, s67, 0\n\t"
      "global_load_dwordx4 v[172:175], v244, s[58:59] sc1\n\t"
      "s_add_u32 s58, s66, s45\n\t"
      "s_addc_u32 s59, s67, 0\n\t"
      "global_load_dwordx4 v[176:179], v244, s[58:59] sc1\n\t"
      "s_add_u32 s58, s66, s46\n\t"
      "s_addc_u32 s59, s67, 0\n\t"
      "global_load_dwordx4 v[180:183], v244, s[58:59] sc1\n\t"
      "s_waitcnt vmcnt(0)\n\t"
      "v_max3_u32 v251, v156, v157, v158\n\t"
      "v_max3_u32 v251, v251, v159, v160\n\t"
      "v_max3_u32 v251, v251, v161, v162\n\t"
      "v_max3_u32 v251, v251, v163, v164\n\t"
      "v_max3_u32 v251, v251, v165, v166\n\t"
      "v_max3_u32 v251, v251, v167, v168\n\t"
      "v_max3_u32 v251, v251, v169, v170\n\t"
      "v_max3_u32 v251, v251, v171, v172\n\t"
      "v_max3_u32 v251, v251, v173, v174\n\t"
      "v_max3_u32 v251, v251, v175, v176\n\t"
      "v_max3_u32 v251, v251, v177, v178\n\t"
      "v_max3_u32 v251, v251, v179, v180\n\t"
      "v_max3_u32 v251, v251, v181, v182\n\t"
      "v_max_u32_e32 v251, v251, v183\n\t"
      "v_cmp_eq_u32_e32 vcc, -1, v251\n\t"
      "s_cbranch_vccnz LA41L_%=\n\t"
      "LA41X_%=:\n\t"
      "v_mfma_f32_16x16x32_bf16 v[212:215], v[156:159], v[0:3], 0\n\t"
      "v_mfma_f32_16x16x32_bf16 v[216:219], v[156:159], v[4:7], 0\n\t"
      "v_mfma_f32_16x16x32_bf16 v[220:223], v[156:159], v[8:11], 0\n\t"
      "v_mfma_f32_16x16x32_bf16 v[224:227], v[156:159], v[12:15], 0\n\t"
      "v_mfma_f32_16x16x32_bf16 v[212:215], v[160:163], v[16:19], v[212:215]\n\t"
      "v_mfma_f32_16x16x32_bf16 v[216:219], v[160:163], v[20:23], v[216:219]\n\t"
      "v_mfma_f32_16x16x32_bf16 v[220:223], v[160:163], v[24:27], v[220:223]\n\t"
      "v_mfma_f32_16x16x32_bf16 v[224:227], v[160:163], v[28:31], v[224:227]\n\t"
      "v_mfma_f32_16x16x32_bf16 v[212:215], v[164:167], v[32:35], v[212:215]\n\t"
      "v_mfma_f32_16x16x32_bf16 v[216:219], v[164:167], v[36:39], v[216:219]\n\t"
      "v_mfma_f32_16x16x32_bf16 v[220:223], v[164:167], v[40:43], v[220:223]\n\t"
      "v_mfma_f32_16x16x32_bf16 v[224:227], v[164:167], v[44:47], v[224:227]\n\t"
      "v_mfma_f32_16x16x32_bf16 v[212:215], v[168:171], v[48:51], v[212:215]\n\t"
      "v_mfma_f32_16x16x32_bf16 v[216:219], v[168:171], v[52:55], v[216:219]\n\t"
      "v_mfma_f32_16x16x32_bf16 v[220:223], v[168:171], v[56:59], v[220:223]\n\t"
      "v_mfma_f32_16x16x32_bf16 v[224:227], v[168:171], v[60:63], v[224:227]\n\t"
      "v_mfma_f32_16x16x32_bf16 v[212:215], v[172:175], v[64:67], v[212:215]\n\t"
      "v_mfma_f32_16x16x32_bf16 v[216:219], v[172:175], v[68:71], v[216:219]\n\t"
      "v_mfma_f32_16x16x32_bf16 v[220:223], v[172:175], v[72:75], v[220:223]\n\t"
      "v_mfma_f32_16x16x32_bf16 v[224:227], v[172:175], v[76:79], v[224:227]\n\t"
      "v_mfma_f32_16x16x32_bf16 v[212:215], v[176:179], v[80:83], v[212:215]\n\t"
      "v_mfma_f32_16x16x32_bf16 v[216:219], v[176:179], v[84:87], v[216:219]\n\t"
      "v_mfma_f32_16x16x32_bf16 v[220:223], v[176:179], v[88:91], v[220:223]\n\t"
      "v_mfma_f32_16x16x32_bf16 v[224:227], v[176:179], v[92:95], v[224:227]\n\t"
      "v_mfma_f32_16x16x32_bf16 v[212:215], v[180:183], v[96:99], v[212:215]\n\t"
      "v_mfma_f32_16x16x32_bf16 v[216:219], v[180:183], v[100:103], v[216:219]\n\t"
      "v_mfma_f32_16x16x32_bf16 v[220:223], v[180:183], v[104:107], v[220:223]\n\t"
      "v_mfma_f32_16x16x32_bf16 v[224:227], v[180:183], v[108:111], v[224:227]\n\t"
      "s_nop 7\n\t"
      "s_branch LA41B_%=\n\t"
      "LA42R_%=:\n\t"
      "s_mov_b32 s80, 0\n\t"
      "LA42L_%=:\n\t"
      "s_add_u32 s80, s80, 1\n\t"
      "s_cmp_lt_u32 s80, 262144\n\t"
      "s_cbranch_scc0 LA42X_%=\n\t"
      "s_waitcnt vmcnt(0)\n\t"
      "PQ2R_%=:\n\t"
      "PQ2L_%=:\n\t"
      "s_add_u32 s80, s80, 1\n\t"
      "s_cmp_lt_u32 s80, 262144\n\t"
      "s_cbranch_scc0 PQ2X_%=\n\t"
      "s_sleep 1\n\t"
      "global_load_dword v251, v252, s[68:69] offset:252 sc1\n\t"
      "s_waitcnt vmcnt(0)\n\t"
      "v_cmp_eq_u32_e32 vcc, -1, v251\n\t"
      "s_cbranch_vccnz PQ2L_%=\n\t"
      "PQ2X_%=:\n\t"
      "s_add_u32 s58, s68, s40\n\t"
      "s_addc_u32 s59, s69, 0\n\t"
      "global_load_dwordx4 v[184:187], v244, s[58:59] sc1\n\t"
      "s_add_u32 s58, s68, s41\n\t"
      "s_addc_u32 s59, s69, 0\n\t"
      "global_load_dwordx4 v[188:191], v244, s[58:59] sc1\n\t"
      "s_add_u32 s58, s68, s42\n\t"
      "s_addc_u32 s59, s69, 0\n\t"
      "global_load_dwordx4 v[192:195], v244, s[58:59] sc1\n\t"
      "s_add_u32 s58, s68, s43\n\t"
      "s_addc_u32 s59, s69, 0\n\t"
      "global_load_dwordx4 v[128:131], v244, s[58:59] sc1\n\t"
      "s_add_u32 s58, s68, s44\n\t"
      "s_addc_u32 s59, s69, 0\n\t"
      "global_load_dwordx4 v[132:135], v244, s[58:59] sc1\n\t"
      "s_add_u32 s58, s68, s45\n\t"
      "s_addc_u32 s59, s69, 0\n\t"
      "global_load_dwordx4 v[136:139], v244, s[58:59] sc1\n\t"
      "s_add_u32 s58, s68, s46\n\t"
      "s_addc_u32 s59, s69, 0\n\t"
      "global_load_dwordx4 v[140:143], v244, s[58:59] sc1\n\t"
      "s_waitcnt vmcnt(0)\n\t"
      "v_max3_u32 v251, v184, v185, v186\n\t"
      "v_max3_u32 v251, v251, v187, v188\n\t"
      "v_max3_u32 v251, v251, v189, v190\n\t"
      "v_max3_u32 v251, v251, v191, v192\n\t"
      "v_max3_u32 v251, v251, v193, v194\n\t"
      "v_max3_u32 v251, v251, v195, v128\n\t"
      "v_max3_u32 v251, v251, v129, v130\n\t"
      "v_max3_u32 v251, v251, v131, v132\n\t"
      "v_max3_u32 v251, v251, v133, v134\n\t"
      "v_max3_u32 v251, v251, v135, v136\n\t"
      "v_max3_u32 v251, v251, v137, v138\n\t"
      "v_max3_u32 v251, v251, v139, v140\n\t"
      "v_max3_u32 v251, v251, v141, v142\n\t"
      "v_max_u32_e32 v251, v251, v143\n\t"
      "v_cmp_eq_u32_e32 vcc, -1, v251\n\t"
      "s_cbranch_vccnz LA42L_%=\n\t"
      "LA42X_%=:\n\t"
      "v_mfma_f32_16x16x32_bf16 v[196:199], v[184:187], v[0:3], 0\n\t"
      "v_mfma_f32_16x16x32_bf16 v[200:203], v[184:187], v[4:7], 0\n\t"
      "v_mfma_f32_16x16x32_bf16 v[204:207], v[184:187], v[8:11], 0\n\t"
      "v_mfma_f32_16x16x32_bf16 v[208:211], v[184:187], v[12:15], 0\n\t"
      "v_mfma_f32_16x16x32_bf16 v[196:199], v[188:191], v[16:19], v[196:199]\n\t"
      "v_mfma_f32_16x16x32_bf16 v[200:203], v[188:191], v[20:23], v[200:203]\n\t"
      "v_mfma_f32_16x16x32_bf16 v[204:207], v[188:191], v[24:27], v[204:207]\n\t"
      "v_mfma_f32_16x16x32_bf16 v[208:211], v[188:191], v[28:31], v[208:211]\n\t"
      "v_mfma_f32_16x16x32_bf16 v[196:199], v[192:195], v[32:35], v[196:199]\n\t"
      "v_mfma_f32_16x16x32_bf16 v[200:203], v[192:195], v[36:39], v[200:203]\n\t"
      "v_mfma_f32_16x16x32_bf16 v[204:207], v[192:195], v[40:43], v[204:207]\n\t"
      "v_mfma_f32_16x16x32_bf16 v[208:211], v[192:195], v[44:47], v[208:211]\n\t"
      "v_mfma_f32_16x16x32_bf16 v[196:199], v[128:131], v[48:51], v[196:199]\n\t"
      "v_mfma_f32_16x16x32_bf16 v[200:203], v[128:131], v[52:55], v[200:203]\n\t"
      "v_mfma_f32_16x16x32_bf16 v[204:207], v[128:131], v[56:59], v[204:207]\n\t"
      "v_mfma_f32_16x16x32_bf16 v[208:211], v[128:131], v[60:63], v[208:211]\n\t"
      "v_mfma_f32_16x16x32_bf16 v[196:199], v[132:135], v[64:67], v[196:199]\n\t"
      "v_mfma_f32_16x16x32_bf16 v[200:203], v[132:135], v[68:71], v[200:203]\n\t"
      "v_mfma_f32_16x16x32_bf16 v[204:207], v[132:135], v[72:75], v[204:207]\n\t"
      "v_mfma_f32_16x16x32_bf16 v[208:211], v[132:135], v[76:79], v[208:211]\n\t"
      "v_mfma_f32_16x16x32_bf16 v[196:199], v[136:139], v[80:83], v[196:199]\n\t"
      "v_mfma_f32_16x16x32_bf16 v[200:203], v[136:139], v[84:87], v[200:203]\n\t"
      "v_mfma_f32_16x16x32_bf16 v[204:207], v[136:139], v[88:91], v[204:207]\n\t"
      "v_mfma_f32_16x16x32_bf16 v[208:211], v[136:139], v[92:95], v[208:211]\n\t"
      "v_mfma_f32_16x16x32_bf16 v[196:199], v[140:143], v[96:99], v[196:199]\n\t"
      "v_mfma_f32_16x16x32_bf16 v[200:203], v[140:143], v[100:103], v[200:203]\n\t"
      "v_mfma_f32_16x16x32_bf16 v[204:207], v[140:143], v[104:107], v[204:207]\n\t"
      "v_mfma_f32_16x16x32_bf16 v[208:211], v[140:143], v[108:111], v[208:211]\n\t"
      "s_nop 7\n\t"
      "s_branch LA42B_%=\n\t"
      "LA43R_%=:\n\t"
      "s_mov_b32 s80, 0\n\t"
      "LA43L_%=:\n\t"
      "s_add_u32 s80, s80, 1\n\t"
      "s_cmp_lt_u32 s80, 262144\n\t"
      "s_cbranch_scc0 LA43X_%=\n\t"
      "s_waitcnt vmcnt(0)\n\t"
      "PQ3R_%=:\n\t"
      "PQ3L_%=:\n\t"
      "s_add_u32 s80, s80, 1\n\t"
      "s_cmp_lt_u32 s80, 262144\n\t"
      "s_cbranch_scc0 PQ3X_%=\n\t"
      "s_sleep 1\n\t"
      "global_load_dword v251, v252, s[70:71] offset:252 sc1\n\t"
      "s_waitcnt vmcnt(0)\n\t"
      "v_cmp_eq_u32_e32 vcc, -1, v251\n\t"
      "s_cbranch_vccnz PQ3L_%=\n\t"
      "PQ3X_%=:\n\t"
      "s_add_u32 s58, s70, s40\n\t"
      "s_addc_u32 s59, s71, 0\n\t"
      "global_load_dwordx4 v[144:147], v244, s[58:59] sc1\n\t"
      "s_add_u32 s58, s70, s41\n\t"
      "s_addc_u32 s59, s71, 0\n\t"
      "global_load_dwordx4 v[148:151], v244, s[58:59] sc1\n\t"
      "s_add_u32 s58, s70, s42\n\t"
      "s_addc_u32 s59, s71, 0\n\t"
      "global_load_dwordx4 v[152:155], v244, s[58:59] sc1\n\t"
      "s_add_u32 s58, s70, s43\n\t"
      "s_addc_u32 s59, s71, 0\n\t"
      "global_load_dwordx4 v[156:159], v244, s[58:59] sc1\n\t"
      "s_add_u32 s58, s70, s44\n\t"
      "s_addc_u32 s59, s71, 0\n\t"
      "global_load_dwordx4 v[160:163], v244, s[58:59] sc1\n\t"
      "s_add_u32 s58, s70, s45\n\t"
      "s_addc_u32 s59, s71, 0\n\t"
      "global_load_dwordx4 v[164:167], v244, s[58:59] sc1\n\t"
      "s_add_u32 s58, s70, s46\n\t"
      "s_addc_u32 s59, s71, 0\n\t"
      "global_load_dwordx4 v[168:171], v244, s[58:59] sc1\n\t"
      "s_waitcnt vmcnt(0)\n\t"
      "v_max3_u32 v251, v144, v145, v146\n\t"
      "v_max3_u32 v251, v251, v147, v148\n\t"
      "v_max3_u32 v251, v251, v149, v150\n\t"
      "v_max3_u32 v251, v251, v151, v152\n\t"
      "v_max3_u32 v251, v251, v153, v154\n\t"
      "v_max3_u32 v251, v251, v155, v156\n\t"
      "v_max3_u32 v251, v251, v157, v158\n\t"
      "v_max3_u32 v251, v251, v159, v160\n\t"
      "v_max3_u32 v251, v251, v161, v162\n\t"
      "v_max3_u32 v251, v251, v163, v164\n\t"
      "v_max3_u32 v251, v251, v165, v166\n\t"
      "v_max3_u32 v251, v251, v167, v168\n\t"
      "v_max3_u32 v251, v251, v169, v170\n\t"
      "v_max_u32_e32 v251, v251, v171\n\t"
      "v_cmp_eq_u32_e32 vcc, -1, v251\n\t"
      "s_cbranch_vccnz LA43L_%=\n\t"
      "LA43X_%=:\n\t"
      "v_mfma_f32_16x16x32_bf16 v[212:215], v[144:147], v[0:3], 0\n\t"
      "v_mfma_f32_16x16x32_bf16 v[216:219], v[144:147], v[4:7], 0\n\t"
      "v_mfma_f32_16x16x32_bf16 v[220:223], v[144:147], v[8:11], 0\n\t"
      "v_mfma_f32_16x16x32_bf16 v[224:227], v[144:147], v[12:15], 0\n\t"
      "v_mfma_f32_16x16x32_bf16 v[212:215], v[148:151], v[16:19], v[212:215]\n\t"
      "v_mfma_f32_16x16x32_bf16 v[216:219], v[148:151], v[20:23], v[216:219]\n\t"
      "v_mfma_f32_16x16x32_bf16 v[220:223], v[148:151], v[24:27], v[220:223]\n\t"
      "v_mfma_f32_16x16x32_bf16 v[224:227], v[148:151], v[28:31], v[224:227]\n\t"
      "v_mfma_f32_16x16x32_bf16 v[212:215], v[152:155], v[32:35], v[212:215]\n\t"
      "v_mfma_f32_16x16x32_bf16 v[216:219], v[152:155], v[36:39], v[216:219]\n\t"
      "v_mfma_f32_16x16x32_bf16 v[220:223], v[152:155], v[40:43], v[220:223]\n\t"
      "v_mfma_f32_16x16x32_bf16 v[224:227], v[152:155], v[44:47], v[224:227]\n\t"
      "v_mfma_f32_16x16x32_bf16 v[212:215], v[156:159], v[48:51], v[212:215]\n\t"
      "v_mfma_f32_16x16x32_bf16 v[216:219], v[156:159], v[52:55], v[216:219]\n\t"
      "v_mfma_f32_16x16x32_bf16 v[220:223], v[156:159], v[56:59], v[220:223]\n\t"
      "v_mfma_f32_16x16x32_bf16 v[224:227], v[156:159], v[60:63], v[224:227]\n\t"
      "v_mfma_f32_16x16x32_bf16 v[212:215], v[160:163], v[64:67], v[212:215]\n\t"
      "v_mfma_f32_16x16x32_bf16 v[216:219], v[160:163], v[68:71], v[216:219]\n\t"
      "v_mfma_f32_16x16x32_bf16 v[220:223], v[160:163], v[72:75], v[220:223]\n\t"
      "v_mfma_f32_16x16x32_bf16 v[224:227], v[160:163], v[76:79], v[224:227]\n\t"
      "v_mfma_f32_16x16x32_bf16 v[212:215], v[164:167], v[80:83], v[212:215]\n\t"
      "v_mfma_f32_16x16x32_bf16 v[216:219], v[164:167], v[84:87], v[216:219]\n\t"
      "v_mfma_f32_16x16x32_bf16 v[220:223], v[164:167], v[88:91], v[220:223]\n\t"
      "v_mfma_f32_16x16x32_bf16 v[224:227], v[164:167], v[92:95], v[224:227]\n\t"
      "v_mfma_f32_16x16x32_bf16 v[212:215], v[168:171], v[96:99], v[212:215]\n\t"
      "v_mfma_f32_16x16x32_bf16 v[216:219], v[168:171], v[100:103], v[216:219]\n\t"
      "v_mfma_f32_16x16x32_bf16 v[220:223], v[168:171], v[104:107], v[220:223]\n\t"
      "v_mfma_f32_16x16x32_bf16 v[224:227], v[168:171], v[108:111], v[224:227]\n\t"
      "s_nop 7\n\t"
      "s_branch LA43B_%=\n\t"
      "LA44R_%=:\n\t"
      "s_mov_b32 s80, 0\n\t"
      "LA44L_%=:\n\t"
      "s_add_u32 s80, s80, 1\n\t"
      "s_cmp_lt_u32 s80, 262144\n\t"
      "s_cbranch_scc0 LA44X_%=\n\t"
      "s_waitcnt vmcnt(0)\n\t"
      "PQ4R_%=:\n\t"
      "PQ4L_%=:\n\t"
      "s_add_u32 s80, s80, 1\n\t"
      "s_cmp_lt_u32 s80, 262144\n\t"
      "s_cbranch_scc0 PQ4X_%=\n\t"
      "s_sleep 1\n\t"
      "global_load_dword v251, v252, s[72:73] offset:252 sc1\n\t"
      "s_waitcnt vmcnt(0)\n\t"
      "v_cmp_eq_u32_e32 vcc, -1, v251\n\t"
      "s_cbranch_vccnz PQ4L_%=\n\t"
      "PQ4X_%=:\n\t"
      "s_add_u32 s58, s72, s40\n\t"
      "s_addc_u32 s59, s73, 0\n\t"
      "global_load_dwordx4 v[172:175], v244, s[58:59] sc1\n\t"
      "s_add_u32 s58, s72, s41\n\t"
      "s_addc_u32 s59, s73, 0\n\t"
      "global_load_dwordx4 v[176:179], v244, s[58:59] sc1\n\t"
      "s_add_u32 s58, s72, s42\n\t"
      "s_addc_u32 s59, s73, 0\n\t"
      "global_load_dwordx4 v[180:183], v244, s[58:59] sc1\n\t"
      "s_add_u32 s58, s72, s43\n\t"
      "s_addc_u32 s59, s73, 0\n\t"
      "global_load_dwordx4 v[184:187], v244, s[58:59] sc1\n\t"
      "s_add_u32 s58, s72, s44\n\t"
      "s_addc_u32 s59, s73, 0\n\t"
      "global_load_dwordx4 v[188:191], v244, s[58:59] sc1\n\t"
      "s_add_u32 s58, s72, s45\n\t"
      "s_addc_u32 s59, s73, 0\n\t"
      "global_load_dwordx4 v[192:195], v244, s[58:59] sc1\n\t"
      "s_add_u32 s58, s72, s46\n\t"
      "s_addc_u32 s59, s73, 0\n\t"
      "global_load_dwordx4 v[128:131], v244, s[58:59] sc1\n\t"
      "s_waitcnt vmcnt(0)\n\t"
      "v_max3_u32 v251, v172, v173, v174\n\t"
      "v_max3_u32 v251, v251, v175, v176\n\t"
      "v_max3_u32 v251, v251, v177, v178\n\t"
      "v_max3_u32 v251, v251, v179, v180\n\t"
      "v_max3_u32 v251, v251, v181, v182\n\t"
      "v_max3_u32 v251, v251, v183, v184\n\t"
      "v_max3_u32 v251, v251, v185, v186\n\t"
      "v_max3_u32 v251, v251, v187, v188\n\t"
      "v_max3_u32 v251, v251, v189, v190\n\t"
      "v_max3_u32 v251, v251, v191, v192\n\t"
      "v_max3_u32 v251, v251, v193, v194\n\t"
      "v_max3_u32 v251, v251, v195, v128\n\t"
      "v_max3_u32 v251, v251, v129, v130\n\t"
      "v_max_u32_e32 v251, v251, v131\n\t"
      "v_cmp_eq_u32_e32 vcc, -1, v251\n\t"
      "s_cbranch_vccnz LA44L_%=\n\t"
      "LA44X_%=:\n\t"
      "v_mfma_f32_16x16x32_bf16 v[196:199], v[172:175], v[0:3], 0\n\t"
      "v_mfma_f32_16x16x32_bf16 v[200:203], v[172:175], v[4:7], 0\n\t"
      "v_mfma_f32_16x16x32_bf16 v[204:207], v[172:175], v[8:11], 0\n\t"
      "v_mfma_f32_16x16x32_bf16 v[208:211], v[172:175], v[12:15], 0\n\t"
      "v_mfma_f32_16x16x32_bf16 v[196:199], v[176:179], v[16:19], v[196:199]\n\t"
      "v_mfma_f32_16x16x32_bf16 v[200:203], v[176:179], v[20:23], v[200:203]\n\t"
      "v_mfma_f32_16x16x32_bf16 v[204:207], v[176:179], v[24:27], v[204:207]\n\t"
      "v_mfma_f32_16x16x32_bf16 v[208:211], v[176:179], v[28:31], v[208:211]\n\t"
      "v_mfma_f32_16x16x32_bf16 v[196:199], v[180:183], v[32:35], v[196:199]\n\t"
      "v_mfma_f32_16x16x32_bf16 v[200:203], v[180:183], v[36:39], v[200:203]\n\t"
      "v_mfma_f32_16x16x32_bf16 v[204:207], v[180:183], v[40:43], v[204:207]\n\t"
      "v_mfma_f32_16x16x32_bf16 v[208:211], v[180:183], v[44:47], v[208:211]\n\t"
      "v_mfma_f32_16x16x32_bf16 v[196:199], v[184:187], v[48:51], v[196:199]\n\t"
      "v_mfma_f32_16x16x32_bf16 v[200:203], v[184:187], v[52:55], v[200:203]\n\t"
      "v_mfma_f32_16x16x32_bf16 v[204:207], v[184:187], v[56:59], v[204:207]\n\t"
      "v_mfma_f32_16x16x32_bf16 v[208:211], v[184:187], v[60:63], v[208:211]\n\t"
      "v_mfma_f32_16x16x32_bf16 v[196:199], v[188:191], v[64:67], v[196:199]\n\t"
      "v_mfma_f32_16x16x32_bf16 v[200:203], v[188:191], v[68:71], v[200:203]\n\t"
      "v_mfma_f32_16x16x32_bf16 v[204:207], v[188:191], v[72:75], v[204:207]\n\t"
      "v_mfma_f32_16x16x32_bf16 v[208:211], v[188:191], v[76:79], v[208:211]\n\t"
      "v_mfma_f32_16x16x32_bf16 v[196:199], v[192:195], v[80:83], v[196:199]\n\t"
      "v_mfma_f32_16x16x32_bf16 v[200:203], v[192:195], v[84:87], v[200:203]\n\t"
      "v_mfma_f32_16x16x32_bf16 v[204:207], v[192:195], v[88:91], v[204:207]\n\t"
      "v_mfma_f32_16x16x32_bf16 v[208:211], v[192:195], v[92:95], v[208:211]\n\t"
      "v_mfma_f32_16x16x32_bf16 v[196:199], v[128:131], v[96:99], v[196:199]\n\t"
      "v_mfma_f32_16x16x32_bf16 v[200:203], v[128:131], v[100:103], v[200:203]\n\t"
      "v_mfma_f32_16x16x32_bf16 v[204:207], v[128:131], v[104:107], v[204:207]\n\t"
      "v_mfma_f32_16x16x32_bf16 v[208:211], v[128:131], v[108:111], v[208:211]\n\t"
      "s_nop 7\n\t"
      "s_branch LA44B_%=\n\t"
      "LA45R_%=:\n\t"
      "s_mov_b32 s80, 0\n\t"
      "LA45L_%=:\n\t"
      "s_add_u32 s80, s80, 1\n\t"
      "s_cmp_lt_u32 s80, 262144\n\t"
      "s_cbranch_scc0 LA45X_%=\n\t"
      "s_waitcnt vmcnt(0)\n\t"
      "PQ5R_%=:\n\t"
      "PQ5L_%=:\n\t"
      "s_add_u32 s80, s80, 1\n\t"
      "s_cmp_lt_u32 s80, 262144\n\t"
      "s_cbranch_scc0 PQ5X_%=\n\t"
      "s_sleep 1\n\t"
      "global_load_dword v251, v252, s[74:75] offset:252 sc1\n\t"
      "s_waitcnt vmcnt(0)\n\t"
      "v_cmp_eq_u32_e32 vcc, -1, v251\n\t"
      "s_cbranch_vccnz PQ5L_%=\n\t"
      "PQ5X_%=:\n\t"
      "s_add_u32 s58, s74, s40\n\t"
      "s_addc_u32 s59, s75, 0\n\t"
      "global_load_dwordx4 v[132:135], v244, s[58:59] sc1\n\t"
      "s_add_u32 s58, s74, s41\n\t"
      "s_addc_u32 s59, s75, 0\n\t"
      "global_load_dwordx4 v[136:139], v244, s[58:59] sc1\n\t"
      "s_add_u32 s58, s74, s42\n\t"
      "s_addc_u32 s59, s75, 0\n\t"
      "global_load_dwordx4 v[140:143], v244, s[58:59] sc1\n\t"
      "s_add_u32 s58, s74, s43\n\t"
      "s_addc_u32 s59, s75, 0\n\t"
      "global_load_dwordx4 v[144:147], v244, s[58:59] sc1\n\t"
      "s_add_u32 s58, s74, s44\n\t"
      "s_addc_u32 s59, s75, 0\n\t"
      "global_load_dwordx4 v[148:151], v244, s[58:59] sc1\n\t"
      "s_add_u32 s58, s74, s45\n\t"
      "s_addc_u32 s59, s75, 0\n\t"
      "global_load_dwordx4 v[152:155], v244, s[58:59] sc1\n\t"
      "s_add_u32 s58, s74, s46\n\t"
      "s_addc_u32 s59, s75, 0\n\t"
      "global_load_dwordx4 v[156:159], v244, s[58:59] sc1\n\t"
      "s_waitcnt vmcnt(0)\n\t"
      "v_max3_u32 v251, v132, v133, v134\n\t"
      "v_max3_u32 v251, v251, v135, v136\n\t"
      "v_max3_u32 v251, v251, v137, v138\n\t"
      "v_max3_u32 v251, v251, v139, v140\n\t"
      "v_max3_u32 v251, v251, v141, v142\n\t"
      "v_max3_u32 v251, v251, v143, v144\n\t"
      "v_max3_u32 v251, v251, v145, v146\n\t"
      "v_max3_u32 v251, v251, v147, v148\n\t"
      "v_max3_u32 v251, v251, v149, v150\n\t"
      "v_max3_u32 v251, v251, v151, v152\n\t"
      "v_max3_u32 v251, v251, v153, v154\n\t"
      "v_max3_u32 v251, v251, v155, v156\n\t"
      "v_max3_u32 v251, v251, v157, v158\n\t"
      "v_max_u32_e32 v251, v251, v159\n\t"
      "v_cmp_eq_u32_e32 vcc, -1, v251\n\t"
      "s_cbranch_vccnz LA45L_%=\n\t"
      "LA45X_%=:\n\t"
      "v_mfma_f32_16x16x32_bf16 v[212:215], v[132:135], v[0:3], 0\n\t"
      "v_mfma_f32_16x16x32_bf16 v[216:219], v[132:135], v[4:7], 0\n\t"
      "v_mfma_f32_16x16x32_bf16 v[220:223], v[132:135], v[8:11], 0\n\t"
      "v_mfma_f32_16x16x32_bf16 v[224:227], v[132:135], v[12:15], 0\n\t"
      "v_mfma_f32_16x16x32_bf16 v[212:215], v[136:139], v[16:19], v[212:215]\n\t"
      "v_mfma_f32_16x16x32_bf16 v[216:219], v[136:139], v[20:23], v[216:219]\n\t"
      "v_mfma_f32_16x16x32_bf16 v[220:223], v[136:139], v[24:27], v[220:223]\n\t"
      "v_mfma_f32_16x16x32_bf16 v[224:227], v[136:139], v[28:31], v[224:227]\n\t"
      "v_mfma_f32_16x16x32_bf16 v[212:215], v[140:143], v[32:35], v[212:215]\n\t"
      "v_mfma_f32_16x16x32_bf16 v[216:219], v[140:143], v[36:39], v[216:219]\n\t"
      "v_mfma_f32_16x16x32_bf16 v[220:223], v[140:143], v[40:43], v[220:223]\n\t"
      "v_mfma_f32_16x16x32_bf16 v[224:227], v[140:143], v[44:47], v[224:227]\n\t"
      "v_mfma_f32_16x16x32_bf16 v[212:215], v[144:147], v[48:51], v[212:215]\n\t"
      "v_mfma_f32_16x16x32_bf16 v[216:219], v[144:147], v[52:55], v[216:219]\n\t"
      "v_mfma_f32_16x16x32_bf16 v[220:223], v[144:147], v[56:59], v[220:223]\n\t"
      "v_mfma_f32_16x16x32_bf16 v[224:227], v[144:147], v[60:63], v[224:227]\n\t"
      "v_mfma_f32_16x16x32_bf16 v[212:215], v[148:151], v[64:67], v[212:215]\n\t"
      "v_mfma_f32_16x16x32_bf16 v[216:219], v[148:151], v[68:71], v[216:219]\n\t"
      "v_mfma_f32_16x16x32_bf16 v[220:223], v[148:151], v[72:75], v[220:223]\n\t"
      "v_mfma_f32_16x16x32_bf16 v[224:227], v[148:151], v[76:79], v[224:227]\n\t"
      "v_mfma_f32_16x16x32_bf16 v[212:215], v[152:155], v[80:83], v[212:215]\n\t"
      "v_mfma_f32_16x16x32_bf16 v[216:219], v[152:155], v[84:87], v[216:219]\n\t"
      "v_mfma_f32_16x16x32_bf16 v[220:223], v[152:155], v[88:91], v[220:223]\n\t"
      "v_mfma_f32_16x16x32_bf16 v[224:227], v[152:155], v[92:95], v[224:227]\n\t"
      "v_mfma_f32_16x16x32_bf16 v[212:215], v[156:159], v[96:99], v[212:215]\n\t"
      "v_mfma_f32_16x16x32_bf16 v[216:219], v[156:159], v[100:103], v[216:219]\n\t"
      "v_mfma_f32_16x16x32_bf16 v[220:223], v[156:159], v[104:107], v[220:223]\n\t"
      "v_mfma_f32_16x16x32_bf16 v[224:227], v[156:159], v[108:111], v[224:227]\n\t"
      "s_nop 7\n\t"
      "s_branch LA45B_%=\n\t"
      "LA46R_%=:\n\t"
      "s_mov_b32 s80, 0\n\t"
      "LA46L_%=:\n\t"
      "s_add_u32 s80, s80, 1\n\t"
      "s_cmp_lt_u32 s80, 262144\n\t"
      "s_cbranch_scc0 LA46X_%=\n\t"
      "s_waitcnt vmcnt(0)\n\t"
      "PQ6R_%=:\n\t"
      "PQ6L_%=:\n\t"
      "s_add_u32 s80, s80, 1\n\t"
      "s_cmp_lt_u32 s80, 262144\n\t"
      "s_cbranch_scc0 PQ6X_%=\n\t"
      "s_sleep 1\n\t"
      "global_load_dword v251, v252, s[76:77] offset:252 sc1\n\t"
      "s_waitcnt vmcnt(0)\n\t"
      "v_cmp_eq_u32_e32 vcc, -1, v251\n\t"
      "s_cbranch_vccnz PQ6L_%=\n\t"
      "PQ6X_%=:\n\t"
      "s_add_u32 s58, s76, s40\n\t"
      "s_addc_u32 s59, s77, 0\n\t"
      "global_load_dwordx4 v[160:163], v244, s[58:59] sc1\n\t"
      "s_add_u32 s58, s76, s41\n\t"
      "s_addc_u32 s59, s77, 0\n\t"
      "global_load_dwordx4 v[164:167], v244, s[58:59] sc1\n\t"
      "s_add_u32 s58, s76, s42\n\t"
      "s_addc_u32 s59, s77, 0\n\t"
      "global_load_dwordx4 v[168:171], v244, s[58:59] sc1\n\t"
      "s_add_u32 s58, s76, s43\n\t"
      "s_addc_u32 s59, s77, 0\n\t"
      "global_load_dwordx4 v[172:175], v244, s[58:59] sc1\n\t"
      "s_add_u32 s58, s76, s44\n\t"
      "s_addc_u32 s59, s77, 0\n\t"
      "global_load_dwordx4 v[176:179], v244, s[58:59] sc1\n\t"
      "s_add_u32 s58, s76, s45\n\t"
      "s_addc_u32 s59, s77, 0\n\t"
      "global_load_dwordx4 v[180:183], v244, s[58:59] sc1\n\t"
      "s_add_u32 s58, s76, s46\n\t"
      "s_addc_u32 s59, s77, 0\n\t"
      "global_load_dwordx4 v[184:187], v244, s[58:59] sc1\n\t"
      "s_waitcnt vmcnt(0)\n\t"
      "v_max3_u32 v251, v160, v161, v162\n\t"
      "v_max3_u32 v251, v251, v163, v164\n\t"
      "v_max3_u32 v251, v251, v165, v166\n\t"
      "v_max3_u32 v251, v251, v167, v168\n\t"
      "v_max3_u32 v251, v251, v169, v170\n\t"
      "v_max3_u32 v251, v251, v171, v172\n\t"
      "v_max3_u32 v251, v251, v173, v174\n\t"
      "v_max3_u32 v251, v251, v175, v176\n\t"
      "v_max3_u32 v251, v251, v177, v178\n\t"
      "v_max3_u32 v251, v251, v179, v180\n\t"
      "v_max3_u32 v251, v251, v181, v182\n\t"
      "v_max3_u32 v251, v251, v183, v184\n\t"
      "v_max3_u32 v251, v251, v185, v186\n\t"
      "v_max_u32_e32 v251, v251, v187\n\t"
      "v_cmp_eq_u32_e32 vcc, -1, v251\n\t"
      "s_cbranch_vccnz LA46L_%=\n\t"
      "LA46X_%=:\n\t"
      "v_mfma_f32_16x16x32_bf16 v[196:199], v[160:163], v[0:3], 0\n\t"
      "v_mfma_f32_16x16x32_bf16 v[200:203], v[160:163], v[4:7], 0\n\t"
      "v_mfma_f32_16x16x32_bf16 v[204:207], v[160:163], v[8:11], 0\n\t"
      "v_mfma_f32_16x16x32_bf16 v[208:211], v[160:163], v[12:15], 0\n\t"
      "v_mfma_f32_16x16x32_bf16 v[196:199], v[164:167], v[16:19], v[196:199]\n\t"
      "v_mfma_f32_16x16x32_bf16 v[200:203], v[164:167], v[20:23], v[200:203]\n\t"
      "v_mfma_f32_16x16x32_bf16 v[204:207], v[164:167], v[24:27], v[204:207]\n\t"
      "v_mfma_f32_16x16x32_bf16 v[208:211], v[164:167], v[28:31], v[208:211]\n\t"
      "v_mfma_f32_16x16x32_bf16 v[196:199], v[168:171], v[32:35], v[196:199]\n\t"
      "v_mfma_f32_16x16x32_bf16 v[200:203], v[168:171], v[36:39], v[200:203]\n\t"
      "v_mfma_f32_16x16x32_bf16 v[204:207], v[168:171], v[40:43], v[204:207]\n\t"
      "v_mfma_f32_16x16x32_bf16 v[208:211], v[168:171], v[44:47], v[208:211]\n\t"
      "v_mfma_f32_16x16x32_bf16 v[196:199], v[172:175], v[48:51], v[196:199]\n\t"
      "v_mfma_f32_16x16x32_bf16 v[200:203], v[172:175], v[52:55], v[200:203]\n\t"
      "v_mfma_f32_16x16x32_bf16 v[204:207], v[172:175], v[56:59], v[204:207]\n\t"
      "v_mfma_f32_16x16x32_bf16 v[208:211], v[172:175], v[60:63], v[208:211]\n\t"
      "v_mfma_f32_16x16x32_bf16 v[196:199], v[176:179], v[64:67], v[196:199]\n\t"
      "v_mfma_f32_16x16x32_bf16 v[200:203], v[176:179], v[68:71], v[200:203]\n\t"
      "v_mfma_f32_16x16x32_bf16 v[204:207], v[176:179], v[72:75], v[204:207]\n\t"
      "v_mfma_f32_16x16x32_bf16 v[208:211], v[176:179], v[76:79], v[208:211]\n\t"
      "v_mfma_f32_16x16x32_bf16 v[196:199], v[180:183], v[80:83], v[196:199]\n\t"
      "v_mfma_f32_16x16x32_bf16 v[200:203], v[180:183], v[84:87], v[200:203]\n\t"
      "v_mfma_f32_16x16x32_bf16 v[204:207], v[180:183], v[88:91], v[204:207]\n\t"
      "v_mfma_f32_16x16x32_bf16 v[208:211], v[180:183], v[92:95], v[208:211]\n\t"
      "v_mfma_f32_16x16x32_bf16 v[196:199], v[184:187], v[96:99], v[196:199]\n\t"
      "v_mfma_f32_16x16x32_bf16 v[200:203], v[184:187], v[100:103], v[200:203]\n\t"
      "v_mfma_f32_16x16x32_bf16 v[204:207], v[184:187], v[104:107], v[204:207]\n\t"
      "v_mfma_f32_16x16x32_bf16 v[208:211], v[184:187], v[108:111], v[208:211]\n\t"
      "s_nop 7\n\t"
      "s_branch LA46B_%=\n\t"
      : "+{s[0:1]}"(ka), "+{v0}"(tid)
      : "{s2}"(blockIdx.x)
      : "memory", "vcc", "scc", "m0",
        "v1", "v2", "v3", "v4", "v5", "v6", "v7", "v8", "v9", "v10", "v11", "v12", "v13", "v14", "v15", "v16", "v17",
        "v18", "v19", "v20", "v21", "v22", "v23", "v24", "v25", "v26", "v27", "v28", "v29", "v30", "v31", "v32", "v33",
        "v34", "v35", "v36", "v37", "v38", "v39", "v40", "v41", "v42", "v43", "v44", "v45", "v46", "v47", "v48", "v49",
        "v50", "v51", "v52", "v53", "v54", "v55", "v56", "v57", "v58", "v59", "v60", "v61", "v62", "v63", "v64", "v65",
        "v66", "v67", "v68", "v69", "v70", "v71", "v72", "v73", "v74", "v75", "v76", "v77", "v78", "v79", "v80", "v81",
        "v82", "v83", "v84", "v85", "v86", "v87", "v88", "v89", "v90", "v91", "v92", "v93", "v94", "v95", "v96", "v97",
        "v98", "v99", "v100", "v101", "v102", "v103", "v104", "v105", "v106", "v107", "v108", "v109", "v110", "v111",
        "v112", "v113", "v114", "v115", "v116", "v117", "v118", "v119", "v120", "v121", "v122", "v123", "v124", "v125",
        "v126", "v127", "v128", "v129", "v130", "v131", "v132", "v133", "v134", "v135", "v136", "v137", "v138", "v139",
        "v140", "v141", "v142", "v143", "v144", "v145", "v146", "v147", "v148", "v149", "v150", "v151", "v152", "v153",
        "v154", "v155", "v156", "v157", "v158", "v159", "v160", "v161", "v162", "v163", "v164", "v165", "v166", "v167",
        "v168", "v169", "v170", "v171", "v172", "v173", "v174", "v175", "v176", "v177", "v178", "v179", "v180", "v181",
        "v182", "v183", "v184", "v185", "v186", "v187", "v188", "v189", "v190", "v191", "v192", "v193", "v194", "v195",
        "v196", "v197", "v198", "v199", "v200", "v201", "v202", "v203", "v204", "v205", "v206", "v207", "v208", "v209",
        "v210", "v211", "v212", "v213", "v214", "v215", "v216", "v217", "v218", "v219", "v220", "v221", "v222", "v223",
        "v224", "v225", "v226", "v227", "v228", "v229", "v230", "v231", "v232", "v233", "v234", "v235", "v236", "v237",
        "v238", "v239", "v240", "v241", "v242", "v243", "v244", "v245", "v246", "v247", "v248", "v249", "v250", "v251",
        "v252", "v253", "v254", "v255", "s3", "s4", "s5", "s6", "s7", "s8", "s9", "s10", "s11", "s12", "s13", "s14",
        "s15", "s16", "s17", "s18", "s19", "s20", "s21", "s22", "s23", "s24", "s25", "s26", "s27", "s28", "s29", "s30",
        "s31", "s32", "s33", "s34", "s35", "s36", "s37", "s38", "s39", "s40", "s41", "s42", "s43", "s44", "s45", "s46",
        "s47", "s48", "s49", "s50", "s51", "s52", "s53", "s54", "s55", "s56", "s57", "s58", "s59", "s60", "s61", "s62",
        "s63", "s64", "s65", "s66", "s67", "s68", "s69", "s70", "s71", "s72", "s73", "s74", "s75", "s76", "s77", "s78",
        "s79", "s80", "s81", "s82", "s83"
  );
#endif
}

extern "C" void kernel_launch(void* const* d_in, const int* in_sizes, int n_in, void* d_out, int out_size,
                              void* d_ws, size_t ws_size, hipStream_t stream) {
  (void)hipFuncSetAttribute((const void*)fused_block, hipFuncAttributeMaxDynamicSharedMemorySize, 163840);
  fused_block<<<256, 512, 163840, stream>>>((const u16*)d_in[0], (const u16*)d_in[1], (const u16*)d_in[2],
                                            (const u16*)d_in[4], (const u16*)d_in[5], (const u16*)d_in[6],
                                            (const u16*)d_in[7], (u16*)d_out, (char*)d_ws);
}
```

```cpp
#include <hip/hip_runtime.h>
#include <stdint.h>
typedef unsigned short u16;
__global__ __launch_bounds__(512, 2) void fused_block(const u16* x, const u16* wqkv, const u16* wo, const u16* w1, const u16* b1,
                                                   const u16* w2, const u16* b2, u16* out, char* ws) {
#if defined(__HIP_DEVICE_COMPILE__)
  unsigned long long ka = (unsigned long long)__builtin_amdgcn_kernarg_segment_ptr();
  unsigned tid = threadIdx.x;
  asm volatile(
      "s_load_dwordx16 s[4:19], s[0:1], 0x0\n\t"
      "s_load_dwordx2 s[20:21], s[0:1], 0x40\n\t"
      "v_lshrrev_b32_e32 v199, 6, v0\n\t"
      "v_and_b32_e32 v192, 63, v0\n\t"
      "v_readfirstlane_b32 s24, v199\n\t"
      "s_and_b32 s34, s2, 7\n\t"
      "s_lshr_b32 s36, s2, 3\n\t"
      "s_mul_i32 s35, s36, 43\n\t"
      "s_lshr_b32 s35, s35, 8\n\t"
      "s_mul_i32 s26, s35, 6\n\t"
      "s_sub_u32 s26, s36, s26\n\t"
      "s_add_u32 s26, s26, 1\n\t"
      "s_bfe_u32 s25, s2, 131076\n\t"
      "s_and_b32 s37, s36, 1\n\t"
      "s_mul_i32 s37, s37, 7\n\t"
      "s_cmp_ge_u32 s36, 24\n\t"
      "s_cselect_b32 s26, s37, s26\n\t"
      "s_cselect_b32 s35, s25, s35\n\t"
      "s_lshl2_add_u32 s25, s34, s35\n\t"
      "s_and_b32 s34, s25, 30\n\t"
      "s_lshl_b32 s34, s34, 6\n\t"
      "s_and_b32 s35, s25, 1\n\t"
      "s_add_u32 s38, s34, s35\n\t"
      "v_lshrrev_b32_e32 v197, 3, v192\n\t"
      "v_and_b32_e32 v198, 7, v192\n\t"
      "s_min_u32 s28, s26, 6\n\t"
      "s_lshl4_add_u32 s34, s24, s38\n\t"
      "s_mul_i32 s34, s34, 896\n\t"
      "s_movk_i32 s35, 1792\n\t"
      "v_lshl_add_u32 v199, v198, 4, s34\n\t"
      "v_mad_u32_u24 v199, v197, s35, v199\n\t"
      "s_lshl_b32 s34, s28, 7\n\t"
      "s_cmp_lg_u32 s24, 0\n\t"
      "s_cbranch_scc1 NTC_%=\n\t"
      "s_getpc_b64 s[58:59]\n\t"
      "s_lshr_b32 s37, s2, 3\n\t"
      "v_min_u32_e32 v200, 9, v192\n\t"
      "v_lshl_add_u32 v200, v200, 5, s37\n\t"
      "s_mov_b32 m0, 64512\n\t"
      "v_min_u32_e32 v200, 290, v200\n\t"
      "v_lshlrev_b32_e32 v200, 7, v200\n\t"
      "global_load_lds_dword v200, s[58:59]\n\t"
      "NTC_%=:\n\t"
      "v_add_u32_e32 v120, s34, v199\n\t"
      "s_add_u32 s35, s34, 128\n\t"
      "s_sub_u32 s36, s35, 896\n\t"
      "s_min_u32 s35, s35, s36\n\t"
      "v_add_u32_e32 v121, s35, v199\n\t"
      "s_waitcnt lgkmcnt(0)\n\t"
      "global_load_dwordx4 v[204:207], v120, s[4:5]\n\t"
      "s_add_u32 s35, s34, 256\n\t"
      "s_sub_u32 s36, s35, 896\n\t"
      "s_min_u32 s35, s35, s36\n\t"
      "v_add_u32_e32 v122, s35, v199\n\t"
      "global_load_dwordx4 v[4:7], v121, s[4:5]\n\t"
      "s_add_u32 s35, s34, 384\n\t"
      "s_sub_u32 s36, s35, 896\n\t"
      "s_min_u32 s35, s35, s36\n\t"
      "v_add_u32_e32 v123, s35, v199\n\t"
      "global_load_dwordx4 v[8:11], v122, s[4:5]\n\t"
      "s_add_u32 s35, s34, 512\n\t"
      "s_sub_u32 s36, s35, 896\n\t"
      "s_min_u32 s35, s35, s36\n\t"
      "v_add_u32_e32 v124, s35, v199\n\t"
      "global_load_dwordx4 v[12:15], v123, s[4:5]\n\t"
      "s_add_u32 s35, s34, 640\n\t"
      "s_sub_u32 s36, s35, 896\n\t"
      "s_min_u32 s35, s35, s36\n\t"
      "v_add_u32_e32 v125, s35, v199\n\t"
      "global_load_dwordx4 v[16:19], v124, s[4:5]\n\t"
      "s_add_u32 s35, s34, 768\n\t"
      "s_sub_u32 s36, s35, 896\n\t"
      "s_min_u32 s35, s35, s36\n\t"
      "v_add_u32_e32 v126, s35, v199\n\t"
      "global_load_dwordx4 v[20:23], v125, s[4:5]\n\t"
      "s_cmp_lt_u32 s25, 2\n\t"
      "s_cselect_b32 s34, 0, 16\n\t"
      "s_sub_u32 s34, s38, s34\n\t"
      "s_lshl1_add_u32 s34, s24, s34\n\t"
      "s_mul_i32 s34, s34, 896\n\t"
      "s_and_b32 s33, s25, 3\n\t"
      "s_lshl_b32 s33, s33, 1\n\t"
      "s_and_b32 s35, s26, 1\n\t"
      "s_add_u32 s33, s33, s35\n\t"
      "s_cmp_eq_u32 s33, 7\n\t"
      "s_cselect_b32 s33, 0, s33\n\t"
      "v_min_u32_e32 v200, 55, v192\n\t"
      "s_lshl_b32 s35, s33, 3\n\t"
      "v_add_u32_e32 v201, s35, v200\n\t"
      "v_subrev_u32_e32 v202, 56, v201\n\t"
      "v_min_u32_e32 v201, v201, v202\n\t"
      "v_lshl_add_u32 v127, v201, 4, s34\n\t"
      "s_and_b32 s39, s24, 3\n\t"
      "s_lshl_b32 s39, s39, 12\n\t"
      "s_cmp_lt_u32 s24, 4\n\t"
      "s_cselect_b32 s27, 4, 0\n\t"
      "v_lshrrev_b32_e32 v193, 4, v192\n\t"
      "v_and_b32_e32 v194, 15, v192\n\t"
      "v_bfe_u32 v195, v192, 2, 2\n\t"
      "v_and_b32_e32 v196, 3, v192\n\t"
      "v_lshlrev_b32_e32 v244, 4, v192\n\t"
      "s_lshr_b32 s34, s24, 1\n\t"
      "s_mul_i32 s34, s34, 14336\n\t"
      "s_and_b32 s35, s24, 1\n\t"
      "s_lshl_b32 s35, s35, 7\n\t"
      "s_add_u32 s34, s34, s35\n\t"
      "v_xor_b32_e32 v199, v197, v198\n\t"
      "v_lshl_add_u32 v199, v199, 4, s34\n\t"
      "v_lshl_add_u32 v199, v198, 8, v199\n\t"
      "s_sub_u32 s35, s28, s33\n\t"
      "s_cselect_b32 s36, 7, 0\n\t"
      "s_add_u32 s35, s35, s36\n\t"
      "s_lshl_b32 s35, s35, 11\n\t"
      "s_add_u32 s34, s35, 0\n\t"
      "s_sub_u32 s36, s34, 14336\n\t"
      "s_min_u32 s34, s34, s36\n\t"
      "v_add_u32_e32 v112, s34, v199\n\t"
      "s_add_u32 s34, s35, 2048\n\t"
      "s_sub_u32 s36, s34, 14336\n\t"
      "s_min_u32 s34, s34, s36\n\t"
      "v_add_u32_e32 v113, s34, v199\n\t"
      "s_add_u32 s34, s35, 4096\n\t"
      "s_sub_u32 s36, s34, 14336\n\t"
      "s_min_u32 s34, s34, s36\n\t"
      "v_add_u32_e32 v114, s34, v199\n\t"
      "s_add_u32 s34, s35, 6144\n\t"
      "s_sub_u32 s36, s34, 14336\n\t"
      "s_min_u32 s34, s34, s36\n\t"
      "v_add_u32_e32 v115, s34, v199\n\t"
      "s_add_u32 s34, s35, 8192\n\t"
      "s_sub_u32 s36, s34, 14336\n\t"
      "s_min_u32 s34, s34, s36\n\t"
      "v_add_u32_e32 v116, s34, v199\n\t"
      "s_add_u32 s34, s35, 10240\n\t"
      "s_sub_u32 s36, s34, 14336\n\t"
      "s_min_u32 s34, s34, s36\n\t"
      "v_add_u32_e32 v117, s34, v199\n\t"
      "s_add_u32 s34, s35, 12288\n\t"
      "s_sub_u32 s36, s34, 14336\n\t"
      "s_min_u32 s34, s34, s36\n\t"
      "v_add_u32_e32 v118, s34, v199\n\t"
      "v_min_u32_e32 v200, 55, v192\n\t"
      "v_lshrrev_b32_e32 v201, 2, v200\n\t"
      "v_lshlrev_b32_e32 v119, 10, v201\n\t"
      "v_cmp_lt_u32_e32 vcc, 6, v201\n\t"
      "v_mov_b32_e32 v202, 57344\n\t"
      "v_mov_b32_e32 v201, 50688\n\t"
      "v_cndmask_b32_e32 v202, v202, v201, vcc\n\t"
      "v_and_b32_e32 v201, 3, v200\n\t"
      "v_lshl_add_u32 v202, v201, 7, v202\n\t"
      "v_and_b32_e32 v200, 7, v200\n\t"
      "v_xor_b32_e32 v200, s24, v200\n\t"
      "v_lshl_add_u32 v202, v200, 4, v202\n\t"
      "v_add_u32_e32 v119, v119, v202\n\t"
      "s_bitcmp1_b32 s27, 2\n\t"
      "s_cbranch_scc1 LSET_%=\n\t"
      "v_xor_b32_e32 v199, v194, v193\n\t"
      "v_lshlrev_b32_e32 v199, 4, v199\n\t"
      "v_lshl_or_b32 v200, v193, 8, v199\n\t"
      "s_and_b32 s34, s24, 1\n\t"
      "s_mul_i32 s35, s34, 28672\n\t"
      "v_add_u32_e32 v160, s35, v200\n\t"
      "v_xor_b32_e32 v161, 64, v160\n\t"
      "v_and_b32_e32 v199, 0x70, v199\n\t"
      "v_lshl_or_b32 v199, v193, 7, v199\n\t"
      "v_add_u32_e32 v199, 57344, v199\n\t"
      "v_add_u32_e32 v200, 14336, v200\n\t"
      "s_cmp_eq_u32 s34, 1\n\t"
      "s_cselect_b64 vcc, -1, 0\n\t"
      "s_cselect_b32 s35, 0, 6656\n\t"
      "v_cndmask_b32_e32 v162, v199, v200, vcc\n\t"
      "v_xor_b32_e32 v163, 64, v162\n\t"
      "v_subrev_u32_e32 v164, s35, v162\n\t"
      "v_subrev_u32_e32 v165, s35, v163\n\t"
      "s_bfe_u32 s35, s24, 65537\n\t"
      "s_mul_i32 s35, s35, 7\n\t"
      "v_and_b32_e32 v201, 24, v192\n\t"
      "v_lshlrev_b32_e32 v201, 2, v201\n\t"
      "v_lshlrev_b32_e32 v202, 7, v195\n\t"
      "v_lshl_or_b32 v202, v193, 12, v202\n\t"
      "v_add_u32_e32 v202, 65536, v202\n\t"
      "v_add_lshl_u32 v199, v196, s35, 3\n\t"
      "v_xor_b32_e32 v199, v199, v201\n\t"
      "v_add_u32_e32 v166, v199, v202\n\t"
      "v_min_u32_e32 v199, 2, v196\n\t"
      "s_add_u32 s35, s35, 4\n\t"
      "v_add_lshl_u32 v199, v199, s35, 3\n\t"
      "v_xor_b32_e32 v199, v199, v201\n\t"
      "v_add_u32_e32 v167, v199, v202\n\t"
      "v_add_u32_e32 v168, 0x10000, v166\n\t"
      "v_add_u32_e32 v169, 0x10000, v167\n\t"
      "v_lshlrev_b32_e32 v199, 11, v193\n\t"
      "v_sub_u32_e32 v170, v166, v199\n\t"
      "v_sub_u32_e32 v171, v167, v199\n\t"
      "s_branch NORM_%=\n\t"
      "LSET_%=:\n\t"
      "s_add_u32 s60, s20, 16777216\n\t"
      "s_addc_u32 s61, s21, 0\n\t"
      "s_load_dwordx2 s[22:23], s[60:61], 0x0\n\t"
      "s_lshr_b32 s34, s39, 9\n\t"
      "v_add_u32_e32 v199, s34, v197\n\t"
      "s_and_b32 s34, s24, 1\n\t"
      "s_lshl_b32 s34, s34, 2\n\t"
      "v_and_b32_e32 v200, 2, v197\n\t"
      "v_or_b32_e32 v200, s34, v200\n\t"
      "v_xor_b32_e32 v200, v200, v198\n\t"
      "v_min_u32_e32 v200, 6, v200\n\t"
      "v_lshlrev_b32_e32 v200, 4, v200\n\t"
      "s_movk_i32 s34, 2688\n\t"
      "v_mad_u32_u24 v149, v199, s34, v200\n\t"
      "v_add_u32_e32 v148, 128, v149\n\t"
      "v_add_u32_e32 v150, 84096, v149\n\t"
      "v_add_u32_e32 v151, 83968, v149\n\t"
      "v_add_u32_e32 v152, 85120, v149\n\t"
      "s_sub_u32 s6, s6, 128\n\t"
      "s_subb_u32 s7, s7, 0\n\t"
      "s_mul_i32 s37, s26, 112\n\t"
      "s_mov_b32 s81, s33\n\t"
      "s_mov_b32 s82, s33\n\t"
      "s_and_b32 s35, s25, 3\n\t"
      "s_lshl2_add_u32 s31, s26, s35\n\t"
      "s_and_b32 s35, s2, 7\n\t"
      "s_mul_i32 s34, s35, 14\n\t"
      "s_lshl4_add_u32 s35, s35, -14\n\t"
      "s_cmp_lt_u32 s31, 14\n\t"
      "s_cselect_b32 s34, s34, s35\n\t"
      "s_cselect_b32 s36, 1, 0\n\t"
      "s_or_b32 s27, s27, s36\n\t"
      "s_add_u32 s34, s34, s31\n\t"
      "s_lshr_b32 s29, s34, 2\n\t"
      "s_and_b32 s32, s34, 3\n\t"
      "s_mul_i32 s30, s32, 7\n\t"
      "v_and_b32_e32 v199, 60, v192\n\t"
      "v_and_b32_e32 v200, 24, v192\n\t"
      "v_lshlrev_b32_e32 v202, 3, v196\n\t"
      "v_lshl_or_b32 v202, v200, 2, v202\n\t"
      "v_lshl_or_b32 v202, v195, 7, v202\n\t"
      "v_lshl_or_b32 v202, v193, 10, v202\n\t"
      "v_add_u32_e32 v224, s39, v202\n\t"
      "v_xor_b32_e32 v225, 32, v224\n\t"
      "v_xor_b32_e32 v226, 64, v224\n\t"
      "v_xor_b32_e32 v227, 96, v224\n\t"
      "s_lshl_b32 s34, s24, 12\n\t"
      "s_add_u32 s34, s34, 98304\n\t"
      "v_add_u32_e32 v245, s34, v244\n\t"
      "v_lshlrev_b32_e32 v199, 2, v199\n\t"
      "s_lshl_b32 s34, s24, 8\n\t"
      "v_lshl_add_u32 v247, v192, 2, s34\n\t"
      "v_and_b32_e32 v200, 2, v192\n\t"
      "v_mul_u32_u24_e32 v202, 576, v200\n\t"
      "v_xor_b32_e32 v199, v202, v199\n\t"
      "v_and_b32_e32 v202, 1, v192\n\t"
      "v_lshl_or_b32 v199, v202, 3, v199\n\t"
      "s_and_b32 s35, s34, 0x400\n\t"
      "s_add_u32 s34, s34, s35\n\t"
      "s_add_u32 s34, s34, 98304\n\t"
      "v_add_u32_e32 v246, s34, v199\n\t"
      "v_lshlrev_b32_e32 v199, 2, v202\n\t"
      "v_lshl_or_b32 v199, v200, 4, v199\n\t"
      "s_min_u32 s34, s29, 27\n\t"
      "s_lshl_b32 s34, s34, 7\n\t"
      "s_lshr_b32 s35, s24, 2\n\t"
      "s_lshl_b32 s35, s35, 6\n\t"
      "s_add_u32 s34, s34, s35\n\t"
      "s_lshr_b32 s35, s39, 9\n\t"
      "s_add_u32 s34, s34, s35\n\t"
      "v_add_u32_e32 v248, s34, v199\n\t"
      "s_add_u32 s48, s20, 8388608\n\t"
      "s_addc_u32 s49, s21, 0\n\t"
      "v_and_b32_e32 v199, 2, v197\n\t"
      "v_xor_b32_e32 v199, v199, v198\n\t"
      "v_lshlrev_b32_e32 v199, 4, v199\n\t"
      "s_movk_i32 s34, 7168\n\t"
      "v_mad_u32_u24 v144, v197, s34, v199\n\t"
      "v_xor_b32_e32 v199, 64, v144\n\t"
      "v_add_u32_e32 v145, 56320, v199\n\t"
      "v_add_u32_e32 v146, 112640, v144\n\t"
      "v_add_u32_e32 v147, 168960, v199\n\t"
      "v_and_b32_e32 v199, 6, v197\n\t"
      "v_xor_b32_e32 v199, v199, v198\n\t"
      "v_lshlrev_b32_e32 v199, 4, v199\n\t"
      "s_movk_i32 s35, 896\n\t"
      "v_mad_u32_u24 v154, v197, s35, v199\n\t"
      "v_add_u32_e32 v155, 6144, v154\n\t"
      "v_add_u32_e32 v156, 12288, v154\n\t"
      "v_add_u32_e32 v157, 18432, v154\n\t"
      "s_mul_i32 s34, s24, 917504\n\t"
      "s_lshl_b32 s35, s29, 7\n\t"
      "s_add_u32 s34, s34, s35\n\t"
      "s_add_u32 s52, s10, s34\n\t"
      "s_addc_u32 s53, s11, 0\n\t"
      "s_lshl_b32 s34, s28, 7\n\t"
      "s_add_u32 s56, s8, s34\n\t"
      "s_addc_u32 s57, s9, 0\n\t"
      "s_mov_b32 s34, 0\n\t"
      "s_and_b32 s35, s34, 1\n\t"
      "s_and_b32 s34, s34, 2\n\t"
      "s_lshl_b32 s34, s34, 6\n\t"
      "s_add_u32 s34, s34, s35\n\t"
      "s_mul_i32 s76, s34, 3584\n\t"
      "s_mov_b32 s34, 1\n\t"
      "s_and_b32 s35, s34, 1\n\t"
      "s_and_b32 s34, s34, 2\n\t"
      "s_lshl_b32 s34, s34, 6\n\t"
      "s_add_u32 s34, s34, s35\n\t"
      "s_mul_i32 s77, s34, 3584\n\t"
      "s_mov_b32 s34, 2\n\t"
      "s_and_b32 s35, s34, 1\n\t"
      "s_and_b32 s34, s34, 2\n\t"
      "s_lshl_b32 s34, s34, 6\n\t"
      "s_add_u32 s34, s34, s35\n\t"
      "s_mul_i32 s78, s34, 3584\n\t"
      "s_mov_b32 s34, 3\n\t"
      "s_and_b32 s35, s34, 1\n\t"
      "s_and_b32 s34, s34, 2\n\t"
      "s_lshl_b32 s34, s34, 6\n\t"
      "s_add_u32 s34, s34, s35\n\t"
      "s_mul_i32 s79, s34, 3584\n\t"
      "NORM_%=:\n\t"
      "global_load_dwordx4 v[24:27], v126, s[4:5]\n\t"
      "global_load_dwordx4 v[28:31], v127, s[4:5]\n\t"
      "v_mov_b32_e32 v199, 0x3f803f80\n\t"
      "s_waitcnt vmcnt(7)\n\t"
      "v_dot2_f32_bf16 v88, v204, v199, 0\n\t"
      "v_dot2_f32_bf16 v92, v204, v204, 0\n\t"
      "v_lshlrev_b32_e32 v32, 16, v204\n\t"
      "v_and_b32_e32 v33, 0xffff0000, v204\n\t"
      "v_dot2_f32_bf16 v89, v205, v199, 0\n\t"
      "v_dot2_f32_bf16 v93, v205, v205, 0\n\t"
      "v_lshlrev_b32_e32 v34, 16, v205\n\t"
      "v_and_b32_e32 v35, 0xffff0000, v205\n\t"
      "v_dot2_f32_bf16 v90, v206, v199, 0\n\t"
      "v_dot2_f32_bf16 v94, v206, v206, 0\n\t"
      "v_lshlrev_b32_e32 v36, 16, v206\n\t"
      "v_and_b32_e32 v37, 0xffff0000, v206\n\t"
      "v_dot2_f32_bf16 v91, v207, v199, 0\n\t"
      "v_dot2_f32_bf16 v95, v207, v207, 0\n\t"
      "v_lshlrev_b32_e32 v38, 16, v207\n\t"
      "v_and_b32_e32 v39, 0xffff0000, v207\n\t"
      "s_waitcnt vmcnt(6)\n\t"
      "v_dot2_f32_bf16 v88, v4, v199, v88\n\t"
      "v_dot2_f32_bf16 v92, v4, v4, v92\n\t"
      "v_lshlrev_b32_e32 v40, 16, v4\n\t"
      "v_and_b32_e32 v41, 0xffff0000, v4\n\t"
      "v_dot2_f32_bf16 v89, v5, v199, v89\n\t"
      "v_dot2_f32_bf16 v93, v5, v5, v93\n\t"
      "v_lshlrev_b32_e32 v42, 16, v5\n\t"
      "v_and_b32_e32 v43, 0xffff0000, v5\n\t"
      "v_dot2_f32_bf16 v90, v6, v199, v90\n\t"
      "v_dot2_f32_bf16 v94, v6, v6, v94\n\t"
      "v_lshlrev_b32_e32 v44, 16, v6\n\t"
      "v_and_b32_e32 v45, 0xffff0000, v6\n\t"
      "v_dot2_f32_bf16 v91, v7, v199, v91\n\t"
      "v_dot2_f32_bf16 v95, v7, v7, v95\n\t"
      "v_lshlrev_b32_e32 v46, 16, v7\n\t"
      "v_and_b32_e32 v47, 0xffff0000, v7\n\t"
      "s_waitcnt vmcnt(5)\n\t"
      "v_dot2_f32_bf16 v88, v8, v199, v88\n\t"
      "v_dot2_f32_bf16 v92, v8, v8, v92\n\t"
      "v_lshlrev_b32_e32 v48, 16, v8\n\t"
      "v_and_b32_e32 v49, 0xffff0000, v8\n\t"
      "v_dot2_f32_bf16 v89, v9, v199, v89\n\t"
      "v_dot2_f32_bf16 v93, v9, v9, v93\n\t"
      "v_lshlrev_b32_e32 v50, 16, v9\n\t"
      "v_and_b32_e32 v51, 0xffff0000, v9\n\t"
      "v_dot2_f32_bf16 v90, v10, v199, v90\n\t"
      "v_dot2_f32_bf16 v94, v10, v10, v94\n\t"
      "v_lshlrev_b32_e32 v52, 16, v10\n\t"
      "v_and_b32_e32 v53, 0xffff0000, v10\n\t"
      "v_dot2_f32_bf16 v91, v11, v199, v91\n\t"
      "v_dot2_f32_bf16 v95, v11, v11, v95\n\t"
      "v_lshlrev_b32_e32 v54, 16, v11\n\t"
      "v_and_b32_e32 v55, 0xffff0000, v11\n\t"
      "s_waitcnt vmcnt(4)\n\t"
      "v_dot2_f32_bf16 v88, v12, v199, v88\n\t"
      "v_dot2_f32_bf16 v92, v12, v12, v92\n\t"
      "v_lshlrev_b32_e32 v56, 16, v12\n\t"
      "v_and_b32_e32 v57, 0xffff0000, v12\n\t"
      "v_dot2_f32_bf16 v89, v13, v199, v89\n\t"
      "v_dot2_f32_bf16 v93, v13, v13, v93\n\t"
      "v_lshlrev_b32_e32 v58, 16, v13\n\t"
      "v_and_b32_e32 v59, 0xffff0000, v13\n\t"
      "v_dot2_f32_bf16 v90, v14, v199, v90\n\t"
      "v_dot2_f32_bf16 v94, v14, v14, v94\n\t"
      "v_lshlrev_b32_e32 v60, 16, v14\n\t"
      "v_and_b32_e32 v61, 0xffff0000, v14\n\t"
      "v_dot2_f32_bf16 v91, v15, v199, v91\n\t"
      "v_dot2_f32_bf16 v95, v15, v15, v95\n\t"
      "v_lshlrev_b32_e32 v62, 16, v15\n\t"
      "v_and_b32_e32 v63, 0xffff0000, v15\n\t"
      "s_waitcnt vmcnt(3)\n\t"
      "v_dot2_f32_bf16 v88, v16, v199, v88\n\t"
      "v_dot2_f32_bf16 v92, v16, v16, v92\n\t"
      "v_lshlrev_b32_e32 v64, 16, v16\n\t"
      "v_and_b32_e32 v65, 0xffff0000, v16\n\t"
      "v_dot2_f32_bf16 v89, v17, v199, v89\n\t"
      "v_dot2_f32_bf16 v93, v17, v17, v93\n\t"
      "v_lshlrev_b32_e32 v66, 16, v17\n\t"
      "v_and_b32_e32 v67, 0xffff0000, v17\n\t"
      "v_dot2_f32_bf16 v90, v18, v199, v90\n\t"
      "v_dot2_f32_bf16 v94, v18, v18, v94\n\t"
      "v_lshlrev_b32_e32 v68, 16, v18\n\t"
      "v_and_b32_e32 v69, 0xffff0000, v18\n\t"
      "v_dot2_f32_bf16 v91, v19, v199, v91\n\t"
      "v_dot2_f32_bf16 v95, v19, v19, v95\n\t"
      "v_lshlrev_b32_e32 v70, 16, v19\n\t"
      "v_and_b32_e32 v71, 0xffff0000, v19\n\t"
      "s_waitcnt vmcnt(2)\n\t"
      "v_dot2_f32_bf16 v88, v20, v199, v88\n\t"
      "v_dot2_f32_bf16 v92, v20, v20, v92\n\t"
      "v_lshlrev_b32_e32 v72, 16, v20\n\t"
      "v_and_b32_e32 v73, 0xffff0000, v20\n\t"
      "v_dot2_f32_bf16 v89, v21, v199, v89\n\t"
      "v_dot2_f32_bf16 v93, v21, v21, v93\n\t"
      "v_lshlrev_b32_e32 v74, 16, v21\n\t"
      "v_and_b32_e32 v75, 0xffff0000, v21\n\t"
      "v_dot2_f32_bf16 v90, v22, v199, v90\n\t"
      "v_dot2_f32_bf16 v94, v22, v22, v94\n\t"
      "v_lshlrev_b32_e32 v76, 16, v22\n\t"
      "v_and_b32_e32 v77, 0xffff0000, v22\n\t"
      "v_dot2_f32_bf16 v91, v23, v199, v91\n\t"
      "v_dot2_f32_bf16 v95, v23, v23, v95\n\t"
      "v_lshlrev_b32_e32 v78, 16, v23\n\t"
      "v_and_b32_e32 v79, 0xffff0000, v23\n\t"
      "s_waitcnt vmcnt(1)\n\t"
      "v_dot2_f32_bf16 v88, v24, v199, v88\n\t"
      "v_dot2_f32_bf16 v92, v24, v24, v92\n\t"
      "v_lshlrev_b32_e32 v80, 16, v24\n\t"
      "v_and_b32_e32 v81, 0xffff0000, v24\n\t"
      "v_dot2_f32_bf16 v89, v25, v199, v89\n\t"
      "v_dot2_f32_bf16 v93, v25, v25, v93\n\t"
      "v_lshlrev_b32_e32 v82, 16, v25\n\t"
      "v_and_b32_e32 v83, 0xffff0000, v25\n\t"
      "v_dot2_f32_bf16 v90, v26, v199, v90\n\t"
      "v_dot2_f32_bf16 v94, v26, v26, v94\n\t"
      "v_lshlrev_b32_e32 v84, 16, v26\n\t"
      "v_and_b32_e32 v85, 0xffff0000, v26\n\t"
      "v_dot2_f32_bf16 v91, v27, v199, v91\n\t"
      "v_dot2_f32_bf16 v95, v27, v27, v95\n\t"
      "v_lshlrev_b32_e32 v86, 16, v27\n\t"
      "v_and_b32_e32 v87, 0xffff0000, v27\n\t"
      "s_waitcnt vmcnt(0)\n\t"
      "s_bitcmp0_b32 s27, 2\n\t"
      "s_cbranch_scc1 SK1_%=\n\t"
      "s_mul_i32 s72, s81, 172032\n\t"
      "s_add_u32 s72, s72, s37\n\t"
      "s_add_u32 s40, s6, s72\n\t"
      "s_addc_u32 s41, s7, 0\n\t"
      "s_add_u32 s81, s81, 1\n\t"
      "s_cmp_eq_u32 s81, 7\n\t"
      "s_cselect_b32 s81, 0, s81\n\t"
      "s_add_u32 m0, s39, 131072\n\t"
      "s_nop 0\n\t"
      "global_load_lds_dwordx4 v148, s[40:41]\n\t"
      "global_load_lds_dwordx4 v149, s[40:41] offset:1024\n\t"
      "global_load_lds_dwordx4 v150, s[40:41] offset:2048\n\t"
      "global_load_lds_dwordx4 v151, s[40:41] offset:3072\n\t"
      "SK1_%=:\n\t"
      "v_dot2_f32_bf16 v96, v28, v199, 0\n\t"
      "v_dot2_f32_bf16 v97, v28, v28, 0\n\t"
      "v_dot2_f32_bf16 v96, v29, v199, v96\n\t"
      "v_dot2_f32_bf16 v97, v29, v29, v97\n\t"
      "v_dot2_f32_bf16 v96, v30, v199, v96\n\t"
      "v_dot2_f32_bf16 v97, v30, v30, v97\n\t"
      "v_dot2_f32_bf16 v96, v31, v199, v96\n\t"
      "v_dot2_f32_bf16 v97, v31, v31, v97\n\t"
      "v_cmp_gt_u32_e32 vcc, 56, v192\n\t"
      "v_pk_add_f32 v[88:89], v[88:89], v[90:91]\n\t"
      "v_pk_add_f32 v[92:93], v[92:93], v[94:95]\n\t"
      "v_cndmask_b32_e32 v96, 0, v96, vcc\n\t"
      "v_cndmask_b32_e32 v97, 0, v97, vcc\n\t"
      "v_add_f32_e32 v88, v88, v89\n\t"
      "v_add_f32_e32 v92, v92, v93\n\t"
      "v_add_f32_dpp v96, v96, v96 quad_perm:[1,0,3,2] row_mask:0xf bank_mask:0xf\n\t"
      "v_add_f32_dpp v97, v97, v97 quad_perm:[1,0,3,2] row_mask:0xf bank_mask:0xf\n\t"
      "v_add_f32_dpp v88, v88, v88 quad_perm:[1,0,3,2] row_mask:0xf bank_mask:0xf\n\t"
      "v_add_f32_dpp v92, v92, v92 quad_perm:[1,0,3,2] row_mask:0xf bank_mask:0xf\n\t"
      "v_add_f32_dpp v96, v96, v96 quad_perm:[2,3,0,1] row_mask:0xf bank_mask:0xf\n\t"
      "v_add_f32_dpp v97, v97, v97 quad_perm:[2,3,0,1] row_mask:0xf bank_mask:0xf\n\t"
      "v_add_f32_dpp v88, v88, v88 quad_perm:[2,3,0,1] row_mask:0xf bank_mask:0xf\n\t"
      "v_add_f32_dpp v92, v92, v92 quad_perm:[2,3,0,1] row_mask:0xf bank_mask:0xf\n\t"
      "v_add_f32_dpp v96, v96, v96 row_half_mirror row_mask:0xf bank_mask:0xf\n\t"
      "v_add_f32_dpp v97, v97, v97 row_half_mirror row_mask:0xf bank_mask:0xf\n\t"
      "v_add_f32_dpp v88, v88, v88 row_half_mirror row_mask:0xf bank_mask:0xf\n\t"
      "v_add_f32_dpp v92, v92, v92 row_half_mirror row_mask:0xf bank_mask:0xf\n\t"
      "v_mul_f32_e32 v88, 0xbb124925, v88\n\t"
      "v_add_f32_dpp v96, v96, v96 row_mirror row_mask:0xf bank_mask:0xf\n\t"
      "v_mul_f32_e32 v92, 0x3b124925, v92\n\t"
      "s_bitcmp0_b32 s27, 2\n\t"
      "s_cbranch_scc1 SK2_%=\n\t"
      "s_mul_i32 s72, s81, 172032\n\t"
      "s_add_u32 s72, s72, s37\n\t"
      "s_add_u32 s40, s6, s72\n\t"
      "s_addc_u32 s41, s7, 0\n\t"
      "s_add_u32 s81, s81, 1\n\t"
      "s_cmp_eq_u32 s81, 7\n\t"
      "s_cselect_b32 s81, 0, s81\n\t"
      "s_add_u32 m0, s39, 147456\n\t"
      "s_nop 0\n\t"
      "global_load_lds_dwordx4 v148, s[40:41]\n\t"
      "global_load_lds_dwordx4 v149, s[40:41] offset:1024\n\t"
      "SK2_%=:\n\t"
      "v_add_f32_dpp v97, v97, v97 row_mirror row_mask:0xf bank_mask:0xf\n\t"
      "v_fma_f32 v92, -v88, v88, v92\n\t"
      "s_nop 0\n\t"
      "v_permlane16_swap_b32_e32 v96, v97\n\t"
      "v_max_f32_e32 v92, 0, v92\n\t"
      "v_add_f32_e32 v96, v96, v97\n\t"
      "v_add_f32_e32 v92, 0x3727c5ac, v92\n\t"
      "v_mov_b32_e32 v97, v96\n\t"
      "v_rsq_f32_e32 v92, v92\n\t"
      "s_nop 0\n\t"
      "v_permlane32_swap_b32_e32 v96, v97\n\t"
      "v_add_f32_e32 v96, v96, v97\n\t"
      "v_mul_f32_e32 v88, v88, v92\n\t"
      "v_mov_b32_e32 v97, v96\n\t"
      "v_pk_fma_f32 v[32:33], v[32:33], v[92:93], v[88:89] op_sel_hi:[1,0,0]\n\t"
      "v_pk_fma_f32 v[34:35], v[34:35], v[92:93], v[88:89] op_sel_hi:[1,0,0]\n\t"
      "v_permlane16_swap_b32_e32 v96, v97\n\t"
      "v_pk_fma_f32 v[36:37], v[36:37], v[92:93], v[88:89] op_sel_hi:[1,0,0]\n\t"
      "v_pk_fma_f32 v[38:39], v[38:39], v[92:93], v[88:89] op_sel_hi:[1,0,0]\n\t"
      "v_mul_f32_e32 v96, 0xbb124925, v96\n\t"
      "v_cvt_pk_bf16_f32 v0, v32, v33\n\t"
      "v_cvt_pk_bf16_f32 v1, v34, v35\n\t"
      "v_cvt_pk_bf16_f32 v2, v36, v37\n\t"
      "v_cvt_pk_bf16_f32 v3, v38, v39\n\t"
      "ds_write_b128 v112, v[0:3]\n\t"
      "v_pk_fma_f32 v[40:41], v[40:41], v[92:93], v[88:89] op_sel_hi:[1,0,0]\n\t"
      "v_pk_fma_f32 v[42:43], v[42:43], v[92:93], v[88:89] op_sel_hi:[1,0,0]\n\t"
      "v_mul_f32_e32 v97, 0x3b124925, v97\n\t"
      "v_pk_fma_f32 v[44:45], v[44:45], v[92:93], v[88:89] op_sel_hi:[1,0,0]\n\t"
      "v_pk_fma_f32 v[46:47], v[46:47], v[92:93], v[88:89] op_sel_hi:[1,0,0]\n\t"
      "v_fma_f32 v97, -v96, v96, v97\n\t"
      "v_cvt_pk_bf16_f32 v4, v40, v41\n\t"
      "s_bitcmp0_b32 s27, 2\n\t"
      "s_cbranch_scc1 SK3_%=\n\t"
      "global_load_lds_dwordx4 v150, s[40:41] offset:2048\n\t"
      "global_load_lds_dwordx4 v151, s[40:41] offset:3072\n\t"
      "SK3_%=:\n\t"
      "v_cvt_pk_bf16_f32 v5, v42, v43\n\t"
      "v_cvt_pk_bf16_f32 v6, v44, v45\n\t"
      "v_cvt_pk_bf16_f32 v7, v46, v47\n\t"
      "ds_write_b128 v113, v[4:7]\n\t"
      "v_pk_fma_f32 v[48:49], v[48:49], v[92:93], v[88:89] op_sel_hi:[1,0,0]\n\t"
      "v_pk_fma_f32 v[50:51], v[50:51], v[92:93], v[88:89] op_sel_hi:[1,0,0]\n\t"
      "v_max_f32_e32 v97, 0, v97\n\t"
      "v_pk_fma_f32 v[52:53], v[52:53], v[92:93], v[88:89] op_sel_hi:[1,0,0]\n\t"
      "v_pk_fma_f32 v[54:55], v[54:55], v[92:93], v[88:89] op_sel_hi:[1,0,0]\n\t"
      "v_add_f32_e32 v97, 0x3727c5ac, v97\n\t"
      "v_cvt_pk_bf16_f32 v8, v48, v49\n\t"
      "v_cvt_pk_bf16_f32 v9, v50, v51\n\t"
      "v_cvt_pk_bf16_f32 v10, v52, v53\n\t"
      "v_cvt_pk_bf16_f32 v11, v54, v55\n\t"
      "ds_write_b128 v114, v[8:11]\n\t"
      "v_pk_fma_f32 v[56:57], v[56:57], v[92:93], v[88:89] op_sel_hi:[1,0,0]\n\t"
      "v_pk_fma_f32 v[58:59], v[58:59], v[92:93], v[88:89] op_sel_hi:[1,0,0]\n\t"
      "v_rsq_f32_e32 v97, v97\n\t"
      "v_pk_fma_f32 v[60:61], v[60:61], v[92:93], v[88:89] op_sel_hi:[1,0,0]\n\t"
      "v_pk_fma_f32 v[62:63], v[62:63], v[92:93], v[88:89] op_sel_hi:[1,0,0]\n\t"
      "v_cvt_pk_bf16_f32 v12, v56, v57\n\t"
      "v_cvt_pk_bf16_f32 v13, v58, v59\n\t"
      "v_cvt_pk_bf16_f32 v14, v60, v61\n\t"
      "v_cvt_pk_bf16_f32 v15, v62, v63\n\t"
      "ds_write_b128 v115, v[12:15]\n\t"
      "v_pk_fma_f32 v[64:65], v[64:65], v[92:93], v[88:89] op_sel_hi:[1,0,0]\n\t"
      "v_pk_fma_f32 v[66:67], v[66:67], v[92:93], v[88:89] op_sel_hi:[1,0,0]\n\t"
      "v_mul_f32_e32 v96, v96, v97\n\t"
      "v_pk_fma_f32 v[68:69], v[68:69], v[92:93], v[88:89] op_sel_hi:[1,0,0]\n\t"
      "v_pk_fma_f32 v[70:71], v[70:71], v[92:93], v[88:89] op_sel_hi:[1,0,0]\n\t"
      "s_bitcmp0_b32 s27, 2\n\t"
      "s_cbranch_scc1 SK4_%=\n\t"
      "s_mul_i32 s72, s82, 172032\n\t"
      "s_add_u32 s72, s72, s37\n\t"
      "s_add_u32 s72, s72, 1792\n\t"
      "s_add_u32 s40, s6, s72\n\t"
      "s_addc_u32 s41, s7, 0\n\t"
      "s_add_u32 s82, s82, 1\n\t"
      "s_cmp_eq_u32 s82, 7\n\t"
      "s_cselect_b32 s82, 0, s82\n\t"
      "s_add_u32 m0, s39, 65536\n\t"
      "s_nop 0\n\t"
      "global_load_lds_dwordx4 v148, s[40:41]\n\t"
      "global_load_lds_dwordx4 v152, s[40:41] offset:1024\n\t"
      "SK4_%=:\n\t"
      "v_cvt_pk_bf16_f32 v16, v64, v65\n\t"
      "v_cvt_pk_bf16_f32 v17, v66, v67\n\t"
      "v_cvt_pk_bf16_f32 v18, v68, v69\n\t"
      "v_cvt_pk_bf16_f32 v19, v70, v71\n\t"
      "ds_write_b128 v116, v[16:19]\n\t"
      "v_pk_fma_f32 v[72:73], v[72:73], v[92:93], v[88:89] op_sel_hi:[1,0,0]\n\t"
      "v_pk_fma_f32 v[74:75], v[74:75], v[92:93], v[88:89] op_sel_hi:[1,0,0]\n\t"
      "v_pk_fma_f32 v[76:77], v[76:77], v[92:93], v[88:89] op_sel_hi:[1,0,0]\n\t"
      "v_pk_fma_f32 v[78:79], v[78:79], v[92:93], v[88:89] op_sel_hi:[1,0,0]\n\t"
      "v_cvt_pk_bf16_f32 v20, v72, v73\n\t"
      "v_cvt_pk_bf16_f32 v21, v74, v75\n\t"
      "v_cvt_pk_bf16_f32 v22, v76, v77\n\t"
      "v_cvt_pk_bf16_f32 v23, v78, v79\n\t"
      "ds_write_b128 v117, v[20:23]\n\t"
      "v_pk_fma_f32 v[80:81], v[80:81], v[92:93], v[88:89] op_sel_hi:[1,0,0]\n\t"
      "v_pk_fma_f32 v[82:83], v[82:83], v[92:93], v[88:89] op_sel_hi:[1,0,0]\n\t"
      "v_pk_fma_f32 v[84:85], v[84:85], v[92:93], v[88:89] op_sel_hi:[1,0,0]\n\t"
      "v_pk_fma_f32 v[86:87], v[86:87], v[92:93], v[88:89] op_sel_hi:[1,0,0]\n\t"
      "v_cvt_pk_bf16_f32 v24, v80, v81\n\t"
      "v_cvt_pk_bf16_f32 v25, v82, v83\n\t"
      "v_cvt_pk_bf16_f32 v26, v84, v85\n\t"
      "v_cvt_pk_bf16_f32 v27, v86, v87\n\t"
      "ds_write_b128 v118, v[24:27]\n\t"
      "v_lshlrev_b32_e32 v32, 16, v28\n\t"
      "v_and_b32_e32 v33, 0xffff0000, v28\n\t"
      "v_lshlrev_b32_e32 v34, 16, v29\n\t"
      "v_and_b32_e32 v35, 0xffff0000, v29\n\t"
      "v_lshlrev_b32_e32 v36, 16, v30\n\t"
      "v_and_b32_e32 v37, 0xffff0000, v30\n\t"
      "v_lshlrev_b32_e32 v38, 16, v31\n\t"
      "s_bitcmp0_b32 s27, 2\n\t"
      "s_cbranch_scc1 SK5_%=\n\t"
      "s_mul_i32 s72, s82, 172032\n\t"
      "s_add_u32 s72, s72, s37\n\t"
      "s_add_u32 s72, s72, 1792\n\t"
      "s_add_u32 s40, s6, s72\n\t"
      "s_addc_u32 s41, s7, 0\n\t"
      "s_add_u32 s82, s82, 1\n\t"
      "s_cmp_eq_u32 s82, 7\n\t"
      "s_cselect_b32 s82, 0, s82\n\t"
      "s_add_u32 m0, s39, 67584\n\t"
      "s_nop 0\n\t"
      "global_load_lds_dwordx4 v148, s[40:41]\n\t"
      "global_load_lds_dwordx4 v152, s[40:41] offset:1024\n\t"
      "SK5_%=:\n\t"
      "v_and_b32_e32 v39, 0xffff0000, v31\n\t"
      "v_pk_fma_f32 v[32:33], v[32:33], v[96:97], v[96:97] op_sel:[0,1,0] op_sel_hi:[1,1,0]\n\t"
      "v_pk_fma_f32 v[34:35], v[34:35], v[96:97], v[96:97] op_sel:[0,1,0] op_sel_hi:[1,1,0]\n\t"
      "v_pk_fma_f32 v[36:37], v[36:37], v[96:97], v[96:97] op_sel:[0,1,0] op_sel_hi:[1,1,0]\n\t"
      "v_pk_fma_f32 v[38:39], v[38:39], v[96:97], v[96:97] op_sel:[0,1,0] op_sel_hi:[1,1,0]\n\t"
      "v_cvt_pk_bf16_f32 v28, v32, v33\n\t"
      "v_cvt_pk_bf16_f32 v29, v34, v35\n\t"
      "v_cvt_pk_bf16_f32 v30, v36, v37\n\t"
      "v_cvt_pk_bf16_f32 v31, v38, v39\n\t"
      "ds_write_b128 v119, v[28:31]\n\t"
      "s_waitcnt lgkmcnt(0)\n\t"
      "s_bitcmp1_b32 s27, 2\n\t"
      "s_cbranch_scc1 LPATH_%=\n\t"
      "s_setprio 2\n\t"
      "s_barrier\n\t"
      "ds_read_b64_tr_b16 v[76:77], v168\n\t"
      "ds_read_b64_tr_b16 v[78:79], v168 offset:512\n\t"
      "ds_read_b128 v[68:71], v160\n\t"
      "ds_read_b128 v[72:75], v160 offset:14336\n\t"
      "ds_read_b64_tr_b16 v[80:81], v169\n\t"
      "ds_read_b64_tr_b16 v[82:83], v169 offset:512\n\t"
      "ds_read_b64_tr_b16 v[84:85], v168 offset:1024\n\t"
      "ds_read_b64_tr_b16 v[86:87], v168 offset:1536\n\t"
      "ds_read_b128 v[64:67], v162\n\t"
      "ds_read_b64_tr_b16 v[88:89], v169 offset:1024\n\t"
      "ds_read_b64_tr_b16 v[90:91], v169 offset:1536\n\t"
      "ds_read_b64_tr_b16 v[104:105], v168 offset:2048\n\t"
      "ds_read_b64_tr_b16 v[106:107], v168 offset:2560\n\t"
      "ds_read_b128 v[96:99], v161 offset:1024\n\t"
      "ds_read_b128 v[100:103], v161 offset:15360\n\t"
      "ds_read_b64_tr_b16 v[108:109], v169 offset:2048\n\t"
      "ds_read_b64_tr_b16 v[110:111], v169 offset:2560\n\t"
      "ds_read_b64_tr_b16 v[112:113], v168 offset:3072\n\t"
      "ds_read_b64_tr_b16 v[114:115], v168 offset:3584\n\t"
      "ds_read_b128 v[92:95], v163 offset:1024\n\t"
      "ds_read_b64_tr_b16 v[116:117], v169 offset:3072\n\t"
      "ds_read_b64_tr_b16 v[118:119], v169 offset:3584\n\t"
      "s_waitcnt vmcnt(0)\n\t"
      "s_barrier\n\t"
      "s_waitcnt lgkmcnt(14)\n\t"
      "v_mfma_f32_16x16x32_bf16 v[0:3], v[76:79], v[68:71], 0\n\t"
      "ds_read_b64_tr_b16 v[132:133], v168 offset:16384\n\t"
      "ds_read_b64_tr_b16 v[134:135], v168 offset:16896\n\t"
      "v_mfma_f32_16x16x32_bf16 v[8:11], v[76:79], v[72:75], 0\n\t"
      "ds_read_b128 v[124:127], v160 offset:2048\n\t"
      "v_mfma_f32_16x16x32_bf16 v[4:7], v[80:83], v[68:71], 0\n\t"
      "ds_read_b128 v[128:131], v160 offset:16384\n\t"
      "v_mfma_f32_16x16x32_bf16 v[12:15], v[80:83], v[72:75], 0\n\t"
      "ds_read_b64_tr_b16 v[136:137], v169 offset:16384\n\t"
      "v_mfma_f32_16x16x32_bf16 v[24:27], v[84:87], v[68:71], 0\n\t"
      "ds_read_b64_tr_b16 v[138:139], v169 offset:16896\n\t"
      "v_mfma_f32_16x16x32_bf16 v[32:35], v[84:87], v[72:75], 0\n\t"
      "ds_read_b64_tr_b16 v[140:141], v168 offset:17408\n\t"
      "s_waitcnt lgkmcnt(14)\n\t"
      "v_mfma_f32_16x16x32_bf16 v[16:19], v[84:87], v[64:67], 0\n\t"
      "ds_read_b64_tr_b16 v[142:143], v168 offset:17920\n\t"
      "v_mfma_f32_16x16x32_bf16 v[28:31], v[88:91], v[68:71], 0\n\t"
      "ds_read_b128 v[120:123], v162 offset:2048\n\t"
      "v_mfma_f32_16x16x32_bf16 v[36:39], v[88:91], v[72:75], 0\n\t"
      "ds_read_b64_tr_b16 v[144:145], v169 offset:17408\n\t"
      "v_mfma_f32_16x16x32_bf16 v[20:23], v[88:91], v[64:67], 0\n\t"
      "ds_read_b64_tr_b16 v[146:147], v169 offset:17920\n\t"
      "s_waitcnt lgkmcnt(11)\n\t"
      "v_mfma_f32_16x16x32_bf16 v[0:3], v[104:107], v[96:99], v[0:3]\n\t"
      "ds_read_b64_tr_b16 v[76:77], v168 offset:18432\n\t"
      "ds_read_b64_tr_b16 v[78:79], v168 offset:18944\n\t"
      "v_mfma_f32_16x16x32_bf16 v[8:11], v[104:107], v[100:103], v[8:11]\n\t"
      "ds_read_b128 v[68:71], v161 offset:3072\n\t"
      "v_mfma_f32_16x16x32_bf16 v[4:7], v[108:111], v[96:99], v[4:7]\n\t"
      "ds_read_b128 v[72:75], v161 offset:17408\n\t"
      "v_mfma_f32_16x16x32_bf16 v[12:15], v[108:111], v[100:103], v[12:15]\n\t"
      "ds_read_b64_tr_b16 v[80:81], v169 offset:18432\n\t"
      "v_mfma_f32_16x16x32_bf16 v[24:27], v[112:115], v[96:99], v[24:27]\n\t"
      "ds_read_b64_tr_b16 v[82:83], v169 offset:18944\n\t"
      "v_mfma_f32_16x16x32_bf16 v[32:35], v[112:115], v[100:103], v[32:35]\n\t"
      "ds_read_b64_tr_b16 v[84:85], v168 offset:19456\n\t"
      "v_mfma_f32_16x16x32_bf16 v[16:19], v[112:115], v[92:95], v[16:19]\n\t"
      "ds_read_b64_tr_b16 v[86:87], v168 offset:19968\n\t"
      "v_mfma_f32_16x16x32_bf16 v[28:31], v[116:119], v[96:99], v[28:31]\n\t"
      "ds_read_b128 v[64:67], v163 offset:3072\n\t"
      "v_mfma_f32_16x16x32_bf16 v[36:39], v[116:119], v[100:103], v[36:39]\n\t"
      "ds_read_b64_tr_b16 v[88:89], v169 offset:19456\n\t"
      "v_mfma_f32_16x16x32_bf16 v[20:23], v[116:119], v[92:95], v[20:23]\n\t"
      "ds_read_b64_tr_b16 v[90:91], v169 offset:19968\n\t"
      "s_barrier\n\t"
      "s_waitcnt lgkmcnt(11)\n\t"
      "v_mfma_f32_16x16x32_bf16 v[0:3], v[132:135], v[124:127], v[0:3]\n\t"
      "ds_read_b64_tr_b16 v[104:105], v166\n\t"
      "v_lshlrev_b32_e32 v199, 2, v193\n\t"
      "v_mfma_f32_16x16x32_bf16 v[8:11], v[132:135], v[128:131], v[8:11]\n\t"
      "ds_read_b64_tr_b16 v[106:107], v166 offset:512\n\t"
      "v_sub_u32_e32 v199, v194, v199\n\t"
      "v_mfma_f32_16x16x32_bf16 v[4:7], v[136:139], v[124:127], v[4:7]\n\t"
      "ds_read_b128 v[96:99], v160\n\t"
      "v_mov_b32_e32 v200, 0xff800000\n\t"
      "v_mfma_f32_16x16x32_bf16 v[12:15], v[136:139], v[128:131], v[12:15]\n\t"
      "ds_read_b128 v[100:103], v160 offset:14336\n\t"
      "s_cmp_lt_u32 s25, 2\n\t"
      "v_mfma_f32_16x16x32_bf16 v[24:27], v[140:143], v[124:127], v[24:27]\n\t"
      "ds_read_b128 v[92:95], v162\n\t"
      "s_cselect_b32 s35, 0xff800000, 0\n\t"
      "v_mfma_f32_16x16x32_bf16 v[32:35], v[140:143], v[128:131], v[32:35]\n\t"
      "ds_read_b64_tr_b16 v[108:109], v167\n\t"
      "s_bitcmp0_b32 s24, 0\n\t"
      "v_mfma_f32_16x16x32_bf16 v[16:19], v[140:143], v[120:123], v[16:19]\n\t"
      "ds_read_b64_tr_b16 v[110:111], v167 offset:512\n\t"
      "s_cselect_b32 s35, s35, 0\n\t"
      "v_mfma_f32_16x16x32_bf16 v[28:31], v[144:147], v[124:127], v[28:31]\n\t"
      "v_cmp_ge_i32_e32 vcc, -9, v199\n\t"
      "v_mfma_f32_16x16x32_bf16 v[36:39], v[144:147], v[128:131], v[36:39]\n\t"
      "v_subrev_u32_e32 v201, 0, v199\n\t"
      "v_mfma_f32_16x16x32_bf16 v[20:23], v[144:147], v[120:123], v[20:23]\n\t"
      "v_cmp_gt_u32_e64 s[64:65], 8, v201\n\t"
      "s_waitcnt lgkmcnt(7)\n\t"
      "v_mfma_f32_16x16x32_bf16 v[0:3], v[76:79], v[68:71], v[0:3]\n\t"
      "ds_read_b64_tr_b16 v[132:133], v166 offset:1024\n\t"
      "v_cndmask_b32_e64 v172, v200, 0, vcc\n\t"
      "v_mfma_f32_16x16x32_bf16 v[8:11], v[76:79], v[72:75], v[8:11]\n\t"
      "ds_read_b64_tr_b16 v[134:135], v166 offset:1536\n\t"
      "v_cndmask_b32_e64 v176, v200, 0, s[64:65]\n\t"
      "v_mfma_f32_16x16x32_bf16 v[4:7], v[80:83], v[68:71], v[4:7]\n\t"
      "ds_read_b128 v[124:127], v161 offset:1024\n\t"
      "v_or_b32_e32 v180, s35, v172\n\t"
      "v_mfma_f32_16x16x32_bf16 v[12:15], v[80:83], v[72:75], v[12:15]\n\t"
      "ds_read_b128 v[128:131], v161 offset:15360\n\t"
      "v_cmp_ge_i32_e32 vcc, -8, v199\n\t"
      "v_mfma_f32_16x16x32_bf16 v[24:27], v[84:87], v[68:71], v[24:27]\n\t"
      "ds_read_b128 v[120:123], v163 offset:1024\n\t"
      "v_subrev_u32_e32 v201, 1, v199\n\t"
      "v_mfma_f32_16x16x32_bf16 v[32:35], v[84:87], v[72:75], v[32:35]\n\t"
      "ds_read_b64_tr_b16 v[136:137], v167 offset:1024\n\t"
      "v_cmp_gt_u32_e64 s[64:65], 8, v201\n\t"
      "v_mfma_f32_16x16x32_bf16 v[16:19], v[84:87], v[64:67], v[16:19]\n\t"
      "ds_read_b64_tr_b16 v[138:139], v167 offset:1536\n\t"
      "v_cndmask_b32_e64 v173, v200, 0, vcc\n\t"
      "v_mfma_f32_16x16x32_bf16 v[28:31], v[88:91], v[68:71], v[28:31]\n\t"
      "v_cndmask_b32_e64 v177, v200, 0, s[64:65]\n\t"
      "v_mfma_f32_16x16x32_bf16 v[36:39], v[88:91], v[72:75], v[36:39]\n\t"
      "v_or_b32_e32 v181, s35, v173\n\t"
      "v_mfma_f32_16x16x32_bf16 v[20:23], v[88:91], v[64:67], v[20:23]\n\t"
      "v_cmp_ge_i32_e32 vcc, -7, v199\n\t"
      "s_waitcnt lgkmcnt(7)\n\t"
      "v_mfma_f32_16x16x32_bf16 v[48:51], v[96:99], v[104:107], 0\n\t"
      "ds_read_b64_tr_b16 v[76:77], v166 offset:2048\n\t"
      "ds_read_b64_tr_b16 v[78:79], v166 offset:2560\n\t"
      "v_subrev_u32_e32 v201, 2, v199\n\t"
      "v_mfma_f32_16x16x32_bf16 v[56:59], v[100:103], v[104:107], 0\n\t"
      "ds_read_b128 v[68:71], v160 offset:2048\n\t"
      "v_cmp_gt_u32_e64 s[64:65], 8, v201\n\t"
      "v_mfma_f32_16x16x32_bf16 v[40:43], v[92:95], v[104:107], 0\n\t"
      "ds_read_b128 v[72:75], v160 offset:16384\n\t"
      "v_cndmask_b32_e64 v174, v200, 0, vcc\n\t"
      "v_mfma_f32_16x16x32_bf16 v[52:55], v[96:99], v[108:111], 0\n\t"
      "ds_read_b128 v[64:67], v162 offset:2048\n\t"
      "v_cndmask_b32_e64 v178, v200, 0, s[64:65]\n\t"
      "v_mfma_f32_16x16x32_bf16 v[60:63], v[100:103], v[108:111], 0\n\t"
      "ds_read_b64_tr_b16 v[80:81], v167 offset:2048\n\t"
      "v_or_b32_e32 v182, s35, v174\n\t"
      "v_mfma_f32_16x16x32_bf16 v[44:47], v[92:95], v[108:111], 0\n\t"
      "ds_read_b64_tr_b16 v[82:83], v167 offset:2560\n\t"
      "v_cmp_ge_i32_e32 vcc, -6, v199\n\t"
      "s_waitcnt lgkmcnt(7)\n\t"
      "v_mfma_f32_16x16x32_bf16 v[48:51], v[124:127], v[132:135], v[48:51]\n\t"
      "ds_read_b64_tr_b16 v[104:105], v166 offset:3072\n\t"
      "ds_read_b64_tr_b16 v[106:107], v166 offset:3584\n\t"
      "v_subrev_u32_e32 v201, 3, v199\n\t"
      "v_mfma_f32_16x16x32_bf16 v[56:59], v[128:131], v[132:135], v[56:59]\n\t"
      "ds_read_b128 v[96:99], v161 offset:3072\n\t"
      "v_cmp_gt_u32_e64 s[64:65], 8, v201\n\t"
      "v_mfma_f32_16x16x32_bf16 v[40:43], v[120:123], v[132:135], v[40:43]\n\t"
      "ds_read_b128 v[100:103], v161 offset:17408\n\t"
      "v_cndmask_b32_e64 v175, v200, 0, vcc\n\t"
      "v_mfma_f32_16x16x32_bf16 v[52:55], v[124:127], v[136:139], v[52:55]\n\t"
      "ds_read_b128 v[92:95], v163 offset:3072\n\t"
      "v_cndmask_b32_e64 v179, v200, 0, s[64:65]\n\t"
      "v_mfma_f32_16x16x32_bf16 v[60:63], v[128:131], v[136:139], v[60:63]\n\t"
      "ds_read_b64_tr_b16 v[108:109], v167 offset:3072\n\t"
      "v_or_b32_e32 v183, s35, v175\n\t"
      "v_mfma_f32_16x16x32_bf16 v[44:47], v[120:123], v[136:139], v[44:47]\n\t"
      "ds_read_b64_tr_b16 v[110:111], v167 offset:3584\n\t"
      "v_lshrrev_b32_e32 v201, 31, v172\n\t"
      "s_barrier\n\t"
      "s_waitcnt lgkmcnt(7)\n\t"
      "v_mfma_f32_16x16x32_bf16 v[48:51], v[68:71], v[76:79], v[48:51]\n\t"
      "ds_read_b64_tr_b16 v[132:133], v166 offset:16384\n\t"
      "ds_read_b64_tr_b16 v[134:135], v166 offset:16896\n\t"
      "ds_read_b128 v[124:127], v160 offset:4096\n\t"
      "ds_read_b128 v[128:131], v160 offset:18432\n\t"
      "ds_read_b64_tr_b16 v[136:137], v167 offset:16384\n\t"
      "ds_read_b64_tr_b16 v[138:139], v167 offset:16896\n\t"
      "v_lshrrev_b32_e32 v202, 31, v173\n\t"
      "v_mfma_f32_16x16x32_bf16 v[56:59], v[72:75], v[76:79], v[56:59]\n\t"
      "ds_read_b64_tr_b16 v[140:141], v166 offset:17408\n\t"
      "v_lshl_or_b32 v201, v202, 16, v201\n\t"
      "v_mfma_f32_16x16x32_bf16 v[40:43], v[64:67], v[76:79], v[40:43]\n\t"
      "ds_read_b64_tr_b16 v[142:143], v166 offset:17920\n\t"
      "v_mul_u32_u24_e32 v184, 0xffff, v201\n\t"
      "v_mfma_f32_16x16x32_bf16 v[52:55], v[68:71], v[80:83], v[52:55]\n\t"
      "ds_read_b128 v[120:123], v162 offset:4096\n\t"
      "v_lshrrev_b32_e32 v201, 31, v174\n\t"
      "v_mfma_f32_16x16x32_bf16 v[60:63], v[72:75], v[80:83], v[60:63]\n\t"
      "ds_read_b64_tr_b16 v[144:145], v167 offset:17408\n\t"
      "v_lshrrev_b32_e32 v202, 31, v175\n\t"
      "v_mfma_f32_16x16x32_bf16 v[44:47], v[64:67], v[80:83], v[44:47]\n\t"
      "ds_read_b64_tr_b16 v[146:147], v167 offset:17920\n\t"
      "v_lshl_or_b32 v201, v202, 16, v201\n\t"
      "s_waitcnt lgkmcnt(11)\n\t"
      "v_mfma_f32_16x16x32_bf16 v[48:51], v[96:99], v[104:107], v[48:51]\n\t"
      "ds_read_b64_tr_b16 v[76:77], v166 offset:18432\n\t"
      "ds_read_b64_tr_b16 v[78:79], v166 offset:18944\n\t"
      "ds_read_b128 v[68:71], v161 offset:5120\n\t"
      "ds_read_b128 v[72:75], v161 offset:19456\n\t"
      "ds_read_b64_tr_b16 v[80:81], v167 offset:18432\n\t"
      "ds_read_b64_tr_b16 v[82:83], v167 offset:18944\n\t"
      "v_mul_u32_u24_e32 v185, 0xffff, v201\n\t"
      "v_mfma_f32_16x16x32_bf16 v[56:59], v[100:103], v[104:107], v[56:59]\n\t"
      "ds_read_b64_tr_b16 v[84:85], v166 offset:19456\n\t"
      "s_mov_b32 s68, 0x0\n\t"
      "v_mfma_f32_16x16x32_bf16 v[40:43], v[92:95], v[104:107], v[40:43]\n\t"
      "ds_read_b64_tr_b16 v[86:87], v166 offset:19968\n\t"
      "s_mov_b32 s69, 0xffff0000\n\t"
      "v_mfma_f32_16x16x32_bf16 v[52:55], v[96:99], v[108:111], v[52:55]\n\t"
      "ds_read_b128 v[64:67], v163 offset:5120\n\t"
      "s_mov_b32 s70, 0xffff\n\t"
      "v_mfma_f32_16x16x32_bf16 v[60:63], v[100:103], v[108:111], v[60:63]\n\t"
      "ds_read_b64_tr_b16 v[88:89], v167 offset:19456\n\t"
      "s_mov_b32 s71, 0x0\n\t"
      "v_mfma_f32_16x16x32_bf16 v[44:47], v[92:95], v[108:111], v[44:47]\n\t"
      "ds_read_b64_tr_b16 v[90:91], v167 offset:19968\n\t"
      "v_lshlrev_b32_e32 v231, 3, v192\n\t"
      "s_barrier\n\t"
      "s_waitcnt lgkmcnt(11)\n\t"
      "v_mfma_f32_16x16x32_bf16 v[0:3], v[132:135], v[124:127], v[0:3]\n\t"
      "ds_read_b64_tr_b16 v[104:105], v166 offset:32768\n\t"
      "ds_read_b64_tr_b16 v[106:107], v166 offset:33280\n\t"
      "s_lshl_b32 s34, s25, 12\n\t"
      "v_mfma_f32_16x16x32_bf16 v[8:11], v[132:135], v[128:131], v[8:11]\n\t"
      "ds_read_b128 v[96:99], v160 offset:6144\n\t"
      "s_lshr_b32 s35, s39, 2\n\t"
      "v_mfma_f32_16x16x32_bf16 v[4:7], v[136:139], v[124:127], v[4:7]\n\t"
      "ds_read_b128 v[100:103], v160 offset:20480\n\t"
      "s_add_u32 s34, s34, s35\n\t"
      "v_mfma_f32_16x16x32_bf16 v[12:15], v[136:139], v[128:131], v[12:15]\n\t"
      "ds_read_b64_tr_b16 v[108:109], v167 offset:32768\n\t"
      "s_add_u32 s34, s34, 12582912\n\t"
      "v_mfma_f32_16x16x32_bf16 v[24:27], v[140:143], v[124:127], v[24:27]\n\t"
      "ds_read_b64_tr_b16 v[110:111], v167 offset:33280\n\t"
      "s_add_u32 s46, s20, s34\n\t"
      "v_mfma_f32_16x16x32_bf16 v[32:35], v[140:143], v[128:131], v[32:35]\n\t"
      "ds_read_b64_tr_b16 v[112:113], v166 offset:33792\n\t"
      "s_addc_u32 s47, s21, 0\n\t"
      "v_mfma_f32_16x16x32_bf16 v[16:19], v[140:143], v[120:123], v[16:19]\n\t"
      "ds_read_b64_tr_b16 v[114:115], v166 offset:34304\n\t"
      "s_lshl_b32 s34, s28, 7\n\t"
      "v_mfma_f32_16x16x32_bf16 v[28:31], v[144:147], v[124:127], v[28:31]\n\t"
      "ds_read_b128 v[92:95], v162 offset:6144\n\t"
      "s_add_u32 s58, s46, s34\n\t"
      "v_mfma_f32_16x16x32_bf16 v[36:39], v[144:147], v[128:131], v[36:39]\n\t"
      "ds_read_b64_tr_b16 v[116:117], v167 offset:33792\n\t"
      "s_addc_u32 s59, s47, 0\n\t"
      "v_mfma_f32_16x16x32_bf16 v[20:23], v[144:147], v[120:123], v[20:23]\n\t"
      "ds_read_b64_tr_b16 v[118:119], v167 offset:34304\n\t"
      "s_and_b32 s34, s24, 1\n\t"
      "s_waitcnt lgkmcnt(11)\n\t"
      "v_mfma_f32_16x16x32_bf16 v[0:3], v[76:79], v[68:71], v[0:3]\n\t"
      "ds_read_b64_tr_b16 v[132:133], v166 offset:34816\n\t"
      "ds_read_b64_tr_b16 v[134:135], v166 offset:35328\n\t"
      "s_lshl1_add_u32 s34, s25, s34\n\t"
      "v_mfma_f32_16x16x32_bf16 v[8:11], v[76:79], v[72:75], v[8:11]\n\t"
      "ds_read_b128 v[124:127], v161 offset:7168\n\t"
      "s_lshl_b32 s34, s34, 5\n\t"
      "v_mfma_f32_16x16x32_bf16 v[4:7], v[80:83], v[68:71], v[4:7]\n\t"
      "ds_read_b128 v[128:131], v161 offset:21504\n\t"
      "s_lshl1_add_u32 s34, s26, s34\n\t"
      "v_mfma_f32_16x16x32_bf16 v[12:15], v[80:83], v[72:75], v[12:15]\n\t"
      "ds_read_b64_tr_b16 v[136:137], v167 offset:34816\n\t"
      "s_bfe_u32 s35, s24, 65537\n\t"
      "v_mfma_f32_16x16x32_bf16 v[24:27], v[84:87], v[68:71], v[24:27]\n\t"
      "ds_read_b64_tr_b16 v[138:139], v167 offset:35328\n\t"
      "s_add_u32 s34, s34, s35\n\t"
      "v_mfma_f32_16x16x32_bf16 v[32:35], v[84:87], v[72:75], v[32:35]\n\t"
      "ds_read_b64_tr_b16 v[140:141], v166 offset:35840\n\t"
      "s_lshl_b32 s34, s34, 10\n\t"
      "v_mfma_f32_16x16x32_bf16 v[16:19], v[84:87], v[64:67], v[16:19]\n\t"
      "ds_read_b64_tr_b16 v[142:143], v166 offset:36352\n\t"
      "s_add_u32 s42, s20, s34\n\t"
      "v_mfma_f32_16x16x32_bf16 v[28:31], v[88:91], v[68:71], v[28:31]\n\t"
      "ds_read_b128 v[120:123], v165 offset:7168\n\t"
      "s_addc_u32 s43, s21, 0\n\t"
      "v_mfma_f32_16x16x32_bf16 v[36:39], v[88:91], v[72:75], v[36:39]\n\t"
      "ds_read_b64_tr_b16 v[144:145], v167 offset:35840\n\t"
      "s_add_u32 s44, s42, 16384\n\t"
      "v_mfma_f32_16x16x32_bf16 v[20:23], v[88:91], v[64:67], v[20:23]\n\t"
      "ds_read_b64_tr_b16 v[146:147], v167 offset:36352\n\t"
      "s_addc_u32 s45, s43, 0\n\t"
      "s_barrier\n\t"
      "s_waitcnt lgkmcnt(11)\n\t"
      "v_mfma_f32_16x16x32_bf16 v[0:3], v[104:107], v[96:99], v[0:3]\n\t"
      "ds_read_b64_tr_b16 v[76:77], v166 offset:49152\n\t"
      "ds_read_b64_tr_b16 v[78:79], v166 offset:49664\n\t"
      "v_and_b32_e32 v199, 60, v192\n\t"
      "v_mfma_f32_16x16x32_bf16 v[8:11], v[104:107], v[100:103], v[8:11]\n\t"
      "ds_read_b128 v[68:71], v160 offset:8192\n\t"
      "v_and_b32_e32 v200, 24, v192\n\t"
      "v_mfma_f32_16x16x32_bf16 v[4:7], v[108:111], v[96:99], v[4:7]\n\t"
      "ds_read_b128 v[72:75], v160 offset:22528\n\t"
      "v_lshlrev_b32_e32 v202, 3, v196\n\t"
      "v_mfma_f32_16x16x32_bf16 v[12:15], v[108:111], v[100:103], v[12:15]\n\t"
      "ds_read_b64_tr_b16 v[80:81], v167 offset:49152\n\t"
      "v_lshl_or_b32 v202, v200, 2, v202\n\t"
      "v_mfma_f32_16x16x32_bf16 v[24:27], v[112:115], v[96:99], v[24:27]\n\t"
      "ds_read_b64_tr_b16 v[82:83], v167 offset:49664\n\t"
      "v_lshl_or_b32 v201, v199, 5, v202\n\t"
      "v_mfma_f32_16x16x32_bf16 v[32:35], v[112:115], v[100:103], v[32:35]\n\t"
      "ds_read_b64_tr_b16 v[84:85], v166 offset:50176\n\t"
      "v_add_u32_e32 v216, 106496, v201\n\t"
      "v_mfma_f32_16x16x32_bf16 v[16:19], v[112:115], v[92:95], v[16:19]\n\t"
      "ds_read_b64_tr_b16 v[86:87], v166 offset:50688\n\t"
      "v_mov_b32_e32 v199, 2048\n\t"
      "v_mfma_f32_16x16x32_bf16 v[28:31], v[116:119], v[96:99], v[28:31]\n\t"
      "ds_read_b128 v[64:67], v164 offset:8192\n\t"
      "v_cndmask_b32_e64 v199, v199, 0, s[68:69]\n\t"
      "v_mfma_f32_16x16x32_bf16 v[36:39], v[116:119], v[100:103], v[36:39]\n\t"
      "ds_read_b64_tr_b16 v[88:89], v167 offset:50176\n\t"
      "v_add_u32_e32 v220, v216, v199\n\t"
      "v_mfma_f32_16x16x32_bf16 v[20:23], v[116:119], v[92:95], v[20:23]\n\t"
      "ds_read_b64_tr_b16 v[90:91], v167 offset:50688\n\t"
      "v_xor_b32_e32 v217, 32, v216\n\t"
      "s_waitcnt lgkmcnt(11)\n\t"
      "v_mfma_f32_16x16x32_bf16 v[0:3], v[132:135], v[124:127], v[0:3]\n\t"
      "ds_read_b64_tr_b16 v[104:105], v166 offset:51200\n\t"
      "ds_read_b64_tr_b16 v[106:107], v166 offset:51712\n\t"
      "v_xor_b32_e32 v218, 64, v216\n\t"
      "v_mfma_f32_16x16x32_bf16 v[8:11], v[132:135], v[128:131], v[8:11]\n\t"
      "ds_read_b128 v[96:99], v161 offset:9216\n\t"
      "v_xor_b32_e32 v219, 96, v216\n\t"
      "v_mfma_f32_16x16x32_bf16 v[4:7], v[136:139], v[124:127], v[4:7]\n\t"
      "ds_read_b128 v[100:103], v161 offset:23552\n\t"
      "v_xor_b32_e32 v221, 32, v220\n\t"
      "v_mfma_f32_16x16x32_bf16 v[12:15], v[136:139], v[128:131], v[12:15]\n\t"
      "ds_read_b64_tr_b16 v[108:109], v167 offset:51200\n\t"
      "v_xor_b32_e32 v222, 64, v220\n\t"
      "v_mfma_f32_16x16x32_bf16 v[24:27], v[140:143], v[124:127], v[24:27]\n\t"
      "ds_read_b64_tr_b16 v[110:111], v167 offset:51712\n\t"
      "v_xor_b32_e32 v223, 96, v220\n\t"
      "v_mfma_f32_16x16x32_bf16 v[32:35], v[140:143], v[128:131], v[32:35]\n\t"
      "ds_read_b64_tr_b16 v[112:113], v166 offset:52224\n\t"
      "s_lshr_b32 s34, s39, 1\n\t"
      "v_mfma_f32_16x16x32_bf16 v[16:19], v[140:143], v[120:123], v[16:19]\n\t"
      "ds_read_b64_tr_b16 v[114:115], v166 offset:52736\n\t"
      "s_add_u32 s34, s34, 98304\n\t"
      "v_mfma_f32_16x16x32_bf16 v[28:31], v[144:147], v[124:127], v[28:31]\n\t"
      "ds_read_b128 v[92:95], v165 offset:9216\n\t"
      "v_lshrrev_b32_e32 v199, 1, v193\n\t"
      "v_mfma_f32_16x16x32_bf16 v[36:39], v[144:147], v[128:131], v[36:39]\n\t"
      "ds_read_b64_tr_b16 v[116:117], v167 offset:52224\n\t"
      "v_xor_b32_e32 v199, v199, v198\n\t"
      "v_mfma_f32_16x16x32_bf16 v[20:23], v[144:147], v[120:123], v[20:23]\n\t"
      "ds_read_b64_tr_b16 v[118:119], v167 offset:52736\n\t"
      "v_lshlrev_b32_e32 v199, 4, v199\n\t"
      "s_barrier\n\t"
      "s_waitcnt lgkmcnt(11)\n\t"
      "v_mfma_f32_16x16x32_bf16 v[0:3], v[76:79], v[68:71], v[0:3]\n\t"
      "ds_read_b64_tr_b16 v[132:133], v168\n\t"
      "ds_read_b64_tr_b16 v[134:135], v168 offset:512\n\t"
      "v_lshl_or_b32 v199, v194, 7, v199\n\t"
      "v_mfma_f32_16x16x32_bf16 v[8:11], v[76:79], v[72:75], v[8:11]\n\t"
      "ds_read_b128 v[124:127], v160 offset:10240\n\t"
      "v_and_b32_e32 v200, 1, v193\n\t"
      "v_mfma_f32_16x16x32_bf16 v[4:7], v[80:83], v[68:71], v[4:7]\n\t"
      "ds_read_b128 v[128:131], v160 offset:24576\n\t"
      "v_lshl_or_b32 v199, v200, 3, v199\n\t"
      "v_mfma_f32_16x16x32_bf16 v[12:15], v[80:83], v[72:75], v[12:15]\n\t"
      "ds_read_b64_tr_b16 v[136:137], v169\n\t"
      "v_add_u32_e32 v228, s34, v199\n\t"
      "v_mfma_f32_16x16x32_bf16 v[24:27], v[84:87], v[68:71], v[24:27]\n\t"
      "ds_read_b64_tr_b16 v[138:139], v169 offset:512\n\t"
      "v_mfma_f32_16x16x32_bf16 v[32:35], v[84:87], v[72:75], v[32:35]\n\t"
      "ds_read_b64_tr_b16 v[140:141], v168 offset:1024\n\t"
      "v_mfma_f32_16x16x32_bf16 v[16:19], v[84:87], v[64:67], v[16:19]\n\t"
      "ds_read_b64_tr_b16 v[142:143], v168 offset:1536\n\t"
      "v_mfma_f32_16x16x32_bf16 v[28:31], v[88:91], v[68:71], v[28:31]\n\t"
      "ds_read_b128 v[120:123], v164 offset:10240\n\t"
      "v_mfma_f32_16x16x32_bf16 v[36:39], v[88:91], v[72:75], v[36:39]\n\t"
      "ds_read_b64_tr_b16 v[144:145], v169 offset:1024\n\t"
      "v_mfma_f32_16x16x32_bf16 v[20:23], v[88:91], v[64:67], v[20:23]\n\t"
      "ds_read_b64_tr_b16 v[146:147], v169 offset:1536\n\t"
      "s_waitcnt lgkmcnt(11)\n\t"
      "v_mfma_f32_16x16x32_bf16 v[0:3], v[104:107], v[96:99], v[0:3]\n\t"
      "ds_read_b64_tr_b16 v[76:77], v168 offset:2048\n\t"
      "ds_read_b64_tr_b16 v[78:79], v168 offset:2560\n\t"
      "v_mfma_f32_16x16x32_bf16 v[8:11], v[104:107], v[100:103], v[8:11]\n\t"
      "ds_read_b128 v[68:71], v161 offset:11264\n\t"
      "v_mfma_f32_16x16x32_bf16 v[4:7], v[108:111], v[96:99], v[4:7]\n\t"
      "ds_read_b128 v[72:75], v161 offset:25600\n\t"
      "v_mfma_f32_16x16x32_bf16 v[12:15], v[108:111], v[100:103], v[12:15]\n\t"
      "ds_read_b64_tr_b16 v[80:81], v169 offset:2048\n\t"
      "v_mfma_f32_16x16x32_bf16 v[24:27], v[112:115], v[96:99], v[24:27]\n\t"
      "ds_read_b64_tr_b16 v[82:83], v169 offset:2560\n\t"
      "v_mfma_f32_16x16x32_bf16 v[32:35], v[112:115], v[100:103], v[32:35]\n\t"
      "ds_read_b64_tr_b16 v[84:85], v168 offset:3072\n\t"
      "v_mfma_f32_16x16x32_bf16 v[16:19], v[112:115], v[92:95], v[16:19]\n\t"
      "ds_read_b64_tr_b16 v[86:87], v168 offset:3584\n\t"
      "v_mfma_f32_16x16x32_bf16 v[28:31], v[116:119], v[96:99], v[28:31]\n\t"
      "ds_read_b128 v[64:67], v165 offset:11264\n\t"
      "v_mfma_f32_16x16x32_bf16 v[36:39], v[116:119], v[100:103], v[36:39]\n\t"
      "ds_read_b64_tr_b16 v[88:89], v169 offset:3072\n\t"
      "v_mfma_f32_16x16x32_bf16 v[20:23], v[116:119], v[92:95], v[20:23]\n\t"
      "ds_read_b64_tr_b16 v[90:91], v169 offset:3584\n\t"
      "s_barrier\n\t"
      "s_waitcnt lgkmcnt(11)\n\t"
      "v_mfma_f32_16x16x32_bf16 v[0:3], v[132:135], v[124:127], v[0:3]\n\t"
      "ds_read_b64_tr_b16 v[104:105], v168 offset:16384\n\t"
      "ds_read_b64_tr_b16 v[106:107], v168 offset:16896\n\t"
      "v_mfma_f32_16x16x32_bf16 v[8:11], v[132:135], v[128:131], v[8:11]\n\t"
      "ds_read_b128 v[96:99], v160 offset:12288\n\t"
      "v_mfma_f32_16x16x32_bf16 v[4:7], v[136:139], v[124:127], v[4:7]\n\t"
      "ds_read_b128 v[100:103], v160 offset:26624\n\t"
      "v_mfma_f32_16x16x32_bf16 v[12:15], v[136:139], v[128:131], v[12:15]\n\t"
      "ds_read_b64_tr_b16 v[108:109], v169 offset:16384\n\t"
      "v_mfma_f32_16x16x32_bf16 v[24:27], v[140:143], v[124:127], v[24:27]\n\t"
      "ds_read_b64_tr_b16 v[110:111], v169 offset:16896\n\t"
      "v_mfma_f32_16x16x32_bf16 v[32:35], v[140:143], v[128:131], v[32:35]\n\t"
      "ds_read_b64_tr_b16 v[112:113], v168 offset:17408\n\t"
      "v_mfma_f32_16x16x32_bf16 v[16:19], v[140:143], v[120:123], v[16:19]\n\t"
      "ds_read_b64_tr_b16 v[114:115], v168 offset:17920\n\t"
      "v_mfma_f32_16x16x32_bf16 v[28:31], v[144:147], v[124:127], v[28:31]\n\t"
      "ds_read_b128 v[92:95], v164 offset:12288\n\t"
      "v_mfma_f32_16x16x32_bf16 v[36:39], v[144:147], v[128:131], v[36:39]\n\t"
      "ds_read_b64_tr_b16 v[116:117], v169 offset:17408\n\t"
      "v_mfma_f32_16x16x32_bf16 v[20:23], v[144:147], v[120:123], v[20:23]\n\t"
      "ds_read_b64_tr_b16 v[118:119], v169 offset:17920\n\t"
      "s_waitcnt lgkmcnt(11)\n\t"
      "v_mfma_f32_16x16x32_bf16 v[0:3], v[76:79], v[68:71], v[0:3]\n\t"
      "ds_read_b64_tr_b16 v[132:133], v168 offset:18432\n\t"
      "ds_read_b64_tr_b16 v[134:135], v168 offset:18944\n\t"
      "v_mfma_f32_16x16x32_bf16 v[8:11], v[76:79], v[72:75], v[8:11]\n\t"
      "ds_read_b128 v[124:127], v161 offset:13312\n\t"
      "v_mfma_f32_16x16x32_bf16 v[4:7], v[80:83], v[68:71], v[4:7]\n\t"
      "ds_read_b128 v[128:131], v161 offset:27648\n\t"
      "v_mfma_f32_16x16x32_bf16 v[12:15], v[80:83], v[72:75], v[12:15]\n\t"
      "ds_read_b64_tr_b16 v[136:137], v169 offset:18432\n\t"
      "v_mfma_f32_16x16x32_bf16 v[24:27], v[84:87], v[68:71], v[24:27]\n\t"
      "ds_read_b64_tr_b16 v[138:139], v169 offset:18944\n\t"
      "v_mfma_f32_16x16x32_bf16 v[32:35], v[84:87], v[72:75], v[32:35]\n\t"
      "ds_read_b64_tr_b16 v[140:141], v168 offset:19456\n\t"
      "v_mfma_f32_16x16x32_bf16 v[16:19], v[84:87], v[64:67], v[16:19]\n\t"
      "ds_read_b64_tr_b16 v[142:143], v168 offset:19968\n\t"
      "v_mfma_f32_16x16x32_bf16 v[28:31], v[88:91], v[68:71], v[28:31]\n\t"
      "ds_read_b128 v[120:123], v165 offset:13312\n\t"
      "v_mfma_f32_16x16x32_bf16 v[36:39], v[88:91], v[72:75], v[36:39]\n\t"
      "ds_read_b64_tr_b16 v[144:145], v169 offset:19456\n\t"
      "v_mfma_f32_16x16x32_bf16 v[20:23], v[88:91], v[64:67], v[20:23]\n\t"
      "ds_read_b64_tr_b16 v[146:147], v169 offset:19968\n\t"
      "s_barrier\n\t"
      "s_waitcnt lgkmcnt(11)\n\t"
      "v_mfma_f32_16x16x32_bf16 v[0:3], v[104:107], v[96:99], v[0:3]\n\t"
      "ds_read_b64_tr_b16 v[76:77], v166\n\t"
      "v_mfma_f32_16x16x32_bf16 v[8:11], v[104:107], v[100:103], v[8:11]\n\t"
      "ds_read_b64_tr_b16 v[78:79], v166 offset:512\n\t"
      "v_mfma_f32_16x16x32_bf16 v[4:7], v[108:111], v[96:99], v[4:7]\n\t"
      "ds_read_b128 v[68:71], v160 offset:4096\n\t"
      "v_mfma_f32_16x16x32_bf16 v[12:15], v[108:111], v[100:103], v[12:15]\n\t"
      "ds_read_b128 v[72:75], v160 offset:18432\n\t"
      "v_mfma_f32_16x16x32_bf16 v[24:27], v[112:115], v[96:99], v[24:27]\n\t"
      "ds_read_b128 v[64:67], v162 offset:4096\n\t"
      "v_mfma_f32_16x16x32_bf16 v[32:35], v[112:115], v[100:103], v[32:35]\n\t"
      "ds_read_b64_tr_b16 v[80:81], v167\n\t"
      "v_mfma_f32_16x16x32_bf16 v[16:19], v[112:115], v[92:95], v[16:19]\n\t"
      "ds_read_b64_tr_b16 v[82:83], v167 offset:512\n\t"
      "v_mfma_f32_16x16x32_bf16 v[28:31], v[116:119], v[96:99], v[28:31]\n\t"
      "v_mfma_f32_16x16x32_bf16 v[36:39], v[116:119], v[100:103], v[36:39]\n\t"
      "v_mfma_f32_16x16x32_bf16 v[20:23], v[116:119], v[92:95], v[20:23]\n\t"
      "s_waitcnt lgkmcnt(7)\n\t"
      "v_mfma_f32_16x16x32_bf16 v[0:3], v[132:135], v[124:127], v[0:3]\n\t"
      "ds_read_b64_tr_b16 v[104:105], v166 offset:1024\n\t"
      "v_mfma_f32_16x16x32_bf16 v[8:11], v[132:135], v[128:131], v[8:11]\n\t"
      "ds_read_b64_tr_b16 v[106:107], v166 offset:1536\n\t"
      "v_mfma_f32_16x16x32_bf16 v[4:7], v[136:139], v[124:127], v[4:7]\n\t"
      "ds_read_b128 v[96:99], v161 offset:5120\n\t"
      "v_mfma_f32_16x16x32_bf16 v[12:15], v[136:139], v[128:131], v[12:15]\n\t"
      "ds_read_b128 v[100:103], v161 offset:19456\n\t"
      "v_mfma_f32_16x16x32_bf16 v[24:27], v[140:143], v[124:127], v[24:27]\n\t"
      "ds_read_b128 v[92:95], v163 offset:5120\n\t"
      "v_mfma_f32_16x16x32_bf16 v[32:35], v[140:143], v[128:131], v[32:35]\n\t"
      "ds_read_b64_tr_b16 v[108:109], v167 offset:1024\n\t"
      "v_mfma_f32_16x16x32_bf16 v[16:19], v[140:143], v[120:123], v[16:19]\n\t"
      "ds_read_b64_tr_b16 v[110:111], v167 offset:1536\n\t"
      "v_mfma_f32_16x16x32_bf16 v[28:31], v[144:147], v[124:127], v[28:31]\n\t"
      "v_mfma_f32_16x16x32_bf16 v[36:39], v[144:147], v[128:131], v[36:39]\n\t"
      "v_mfma_f32_16x16x32_bf16 v[20:23], v[144:147], v[120:123], v[20:23]\n\t"
      "s_waitcnt lgkmcnt(7)\n\t"
      "v_mfma_f32_16x16x32_bf16 v[48:51], v[68:71], v[76:79], v[48:51]\n\t"
      "ds_read_b64_tr_b16 v[132:133], v166 offset:2048\n\t"
      "ds_read_b64_tr_b16 v[134:135], v166 offset:2560\n\t"
      "v_mfma_f32_16x16x32_bf16 v[56:59], v[72:75], v[76:79], v[56:59]\n\t"
      "ds_read_b128 v[124:127], v160 offset:6144\n\t"
      "v_mfma_f32_16x16x32_bf16 v[40:43], v[64:67], v[76:79], v[40:43]\n\t"
      "ds_read_b128 v[128:131], v160 offset:20480\n\t"
      "v_cvt_pk_bf16_f32 v84, v0, v1\n\t"
      "v_cvt_pk_bf16_f32 v85, v2, v3\n\t"
      "v_cvt_pk_bf16_f32 v86, v4, v5\n\t"
      "v_mfma_f32_16x16x32_bf16 v[52:55], v[68:71], v[80:83], v[52:55]\n\t"
      "ds_read_b128 v[120:123], v162 offset:6144\n\t"
      "v_cvt_pk_bf16_f32 v87, v6, v7\n\t"
      "v_cvt_pk_bf16_f32 v88, v8, v9\n\t"
      "v_cvt_pk_bf16_f32 v89, v10, v11\n\t"
      "v_mfma_f32_16x16x32_bf16 v[60:63], v[72:75], v[80:83], v[60:63]\n\t"
      "ds_read_b64_tr_b16 v[136:137], v167 offset:2048\n\t"
      "v_cvt_pk_bf16_f32 v90, v12, v13\n\t"
      "v_cvt_pk_bf16_f32 v91, v14, v15\n\t"
      "v_cvt_pk_bf16_f32 v152, v24, v25\n\t"
      "v_mfma_f32_16x16x32_bf16 v[44:47], v[64:67], v[80:83], v[44:47]\n\t"
      "ds_read_b64_tr_b16 v[138:139], v167 offset:2560\n\t"
      "v_cvt_pk_bf16_f32 v153, v26, v27\n\t"
      "v_cvt_pk_bf16_f32 v154, v28, v29\n\t"
      "v_cvt_pk_bf16_f32 v155, v30, v31\n\t"
      "s_waitcnt lgkmcnt(7)\n\t"
      "v_mfma_f32_16x16x32_bf16 v[48:51], v[96:99], v[104:107], v[48:51]\n\t"
      "ds_read_b64_tr_b16 v[76:77], v166 offset:3072\n\t"
      "ds_read_b64_tr_b16 v[78:79], v166 offset:3584\n\t"
      "v_cndmask_b32_e64 v86, v86, 0, s[68:69]\n\t"
      "v_cndmask_b32_e64 v87, v87, 0, s[68:69]\n\t"
      "v_cvt_pk_bf16_f32 v156, v32, v33\n\t"
      "v_mfma_f32_16x16x32_bf16 v[56:59], v[100:103], v[104:107], v[56:59]\n\t"
      "ds_read_b128 v[68:71], v161 offset:7168\n\t"
      "v_cvt_pk_bf16_f32 v157, v34, v35\n\t"
      "v_cvt_pk_bf16_f32 v158, v36, v37\n\t"
      "v_cvt_pk_bf16_f32 v159, v38, v39\n\t"
      "v_mfma_f32_16x16x32_bf16 v[40:43], v[92:95], v[104:107], v[40:43]\n\t"
      "ds_read_b128 v[72:75], v161 offset:21504\n\t"
      "v_cndmask_b32_e64 v90, v90, 0, s[68:69]\n\t"
      "v_cndmask_b32_e64 v91, v91, 0, s[68:69]\n\t"
      "v_cvt_pk_bf16_f32 v148, v16, v17\n\t"
      "v_mfma_f32_16x16x32_bf16 v[52:55], v[96:99], v[108:111], v[52:55]\n\t"
      "ds_read_b128 v[64:67], v165 offset:7168\n\t"
      "v_cvt_pk_bf16_f32 v149, v18, v19\n\t"
      "v_cvt_pk_bf16_f32 v150, v20, v21\n\t"
      "v_cvt_pk_bf16_f32 v151, v22, v23\n\t"
      "v_mfma_f32_16x16x32_bf16 v[60:63], v[100:103], v[108:111], v[60:63]\n\t"
      "ds_read_b64_tr_b16 v[80:81], v167 offset:3072\n\t"
      "v_mov_b32_e32 v34, 0x3e8b97f6\n\t"
      "v_mfma_f32_16x16x32_bf16 v[28:31], v[156:159], v[88:91], v[176:179]\n\t"
      "v_mfma_f32_16x16x32_bf16 v[24:27], v[152:155], v[88:91], v[172:175]\n\t"
      "v_mfma_f32_16x16x32_bf16 v[44:47], v[92:95], v[108:111], v[44:47]\n\t"
      "ds_read_b64_tr_b16 v[82:83], v167 offset:3584\n\t"
      "v_mfma_f32_16x16x32_bf16 v[20:23], v[152:155], v[84:87], v[176:179]\n\t"
      "v_mfma_f32_16x16x32_bf16 v[16:19], v[148:151], v[84:87], v[180:183]\n\t"
      "s_barrier\n\t"
      "s_waitcnt lgkmcnt(7)\n\t"
      "v_mfma_f32_16x16x32_bf16 v[48:51], v[124:127], v[132:135], v[48:51]\n\t"
      "ds_read_b64_tr_b16 v[104:105], v166 offset:16384\n\t"
      "ds_read_b64_tr_b16 v[106:107], v166 offset:16896\n\t"
      "v_mfma_f32_16x16x32_bf16 v[56:59], v[128:131], v[132:135], v[56:59]\n\t"
      "ds_read_b128 v[96:99], v160 offset:8192\n\t"
      "v_max_f32_e32 v24, v24, v28\n\t"
      "v_mfma_f32_16x16x32_bf16 v[40:43], v[120:123], v[132:135], v[40:43]\n\t"
      "ds_read_b128 v[100:103], v160 offset:22528\n\t"
      "v_max_f32_e32 v25, v25, v29\n\t"
      "v_max_f32_e32 v26, v26, v30\n\t"
      "v_max_f32_e32 v27, v27, v31\n\t"
      "v_mfma_f32_16x16x32_bf16 v[52:55], v[124:127], v[136:139], v[52:55]\n\t"
      "ds_read_b128 v[92:95], v164 offset:8192\n\t"
      "v_max_f32_e32 v16, v16, v20\n\t"
      "v_max_f32_e32 v17, v17, v21\n\t"
      "v_max_f32_e32 v18, v18, v22\n\t"
      "v_mfma_f32_16x16x32_bf16 v[60:63], v[128:131], v[136:139], v[60:63]\n\t"
      "ds_read_b64_tr_b16 v[108:109], v167 offset:16384\n\t"
      "v_max_f32_e32 v19, v19, v23\n\t"
      "v_max3_f32 v33, v24, v25, v26\n\t"
      "v_max3_f32 v32, v16, v17, v18\n\t"
      "v_mfma_f32_16x16x32_bf16 v[44:47], v[120:123], v[136:139], v[44:47]\n\t"
      "ds_read_b64_tr_b16 v[110:111], v167 offset:16896\n\t"
      "v_max_f32_e32 v33, v33, v27\n\t"
      "v_max_f32_e32 v32, v32, v19\n\t"
      "s_waitcnt lgkmcnt(7)\n\t"
      "v_mfma_f32_16x16x32_bf16 v[48:51], v[68:71], v[76:79], v[48:51]\n\t"
      "ds_read_b64_tr_b16 v[132:133], v166 offset:17408\n\t"
      "ds_read_b64_tr_b16 v[134:135], v166 offset:17920\n\t"
      "v_permlane16_swap_b32_e32 v32, v33\n\t"
      "v_max_f32_e32 v32, v32, v33\n\t"
      "v_mov_b32_e32 v33, v32\n\t"
      "v_mfma_f32_16x16x32_bf16 v[56:59], v[72:75], v[76:79], v[56:59]\n\t"
      "ds_read_b128 v[124:127], v161 offset:9216\n\t"
      "v_permlane32_swap_b32_e32 v32, v33\n\t"
      "v_max_f32_e32 v32, v32, v33\n\t"
      "v_mfma_f32_16x16x32_bf16 v[40:43], v[64:67], v[76:79], v[40:43]\n\t"
      "ds_read_b128 v[128:131], v161 offset:23552\n\t"
      "v_mov_b32_e32 v33, v32\n\t"
      "s_nop 1\n\t"
      "v_permlane16_swap_b32_e32 v32, v33\n\t"
      "v_mfma_f32_16x16x32_bf16 v[52:55], v[68:71], v[80:83], v[52:55]\n\t"
      "ds_read_b128 v[120:123], v165 offset:9216\n\t"
      "v_mul_f32_e64 v33, -v33, v34\n\t"
      "v_mul_f32_e64 v32, -v32, v34\n\t"
      "v_fma_f32 v24, v24, v34, v33\n\t"
      "v_mfma_f32_16x16x32_bf16 v[60:63], v[72:75], v[80:83], v[60:63]\n\t"
      "ds_read_b64_tr_b16 v[136:137], v167 offset:17408\n\t"
      "v_fma_f32 v25, v25, v34, v33\n\t"
      "v_fma_f32 v26, v26, v34, v33\n\t"
      "v_fma_f32 v27, v27, v34, v33\n\t"
      "v_mfma_f32_16x16x32_bf16 v[44:47], v[64:67], v[80:83], v[44:47]\n\t"
      "ds_read_b64_tr_b16 v[138:139], v167 offset:17920\n\t"
      "v_fma_f32 v16, v16, v34, v32\n\t"
      "v_fma_f32 v17, v17, v34, v32\n\t"
      "v_fma_f32 v18, v18, v34, v32\n\t"
      "s_waitcnt lgkmcnt(7)\n\t"
      "v_mfma_f32_16x16x32_bf16 v[48:51], v[96:99], v[104:107], v[48:51]\n\t"
      "ds_read_b64_tr_b16 v[76:77], v166 offset:18432\n\t"
      "ds_read_b64_tr_b16 v[78:79], v166 offset:18944\n\t"
      "v_fma_f32 v19, v19, v34, v32\n\t"
      "v_exp_f32_e32 v24, v24\n\t"
      "v_mfma_f32_16x16x32_bf16 v[56:59], v[100:103], v[104:107], v[56:59]\n\t"
      "ds_read_b128 v[68:71], v160 offset:10240\n\t"
      "v_exp_f32_e32 v25, v25\n\t"
      "v_exp_f32_e32 v26, v26\n\t"
      "v_mfma_f32_16x16x32_bf16 v[40:43], v[92:95], v[104:107], v[40:43]\n\t"
      "ds_read_b128 v[72:75], v160 offset:24576\n\t"
      "v_exp_f32_e32 v27, v27\n\t"
      "v_mfma_f32_16x16x32_bf16 v[52:55], v[96:99], v[108:111], v[52:55]\n\t"
      "ds_read_b128 v[64:67], v164 offset:10240\n\t"
      "v_exp_f32_e32 v16, v16\n\t"
      "v_exp_f32_e32 v17, v17\n\t"
      "v_mfma_f32_16x16x32_bf16 v[60:63], v[100:103], v[108:111], v[60:63]\n\t"
      "ds_read_b64_tr_b16 v[80:81], v167 offset:18432\n\t"
      "v_exp_f32_e32 v18, v18\n\t"
      "v_mfma_f32_16x16x32_bf16 v[44:47], v[92:95], v[108:111], v[44:47]\n\t"
      "ds_read_b64_tr_b16 v[82:83], v167 offset:18944\n\t"
      "v_exp_f32_e32 v19, v19\n\t"
      "v_add_f32_e32 v33, v24, v25\n\t"
      "s_waitcnt lgkmcnt(7)\n\t"
      "v_mfma_f32_16x16x32_bf16 v[48:51], v[124:127], v[132:135], v[48:51]\n\t"
      "ds_read_b64_tr_b16 v[104:105], v166 offset:19456\n\t"
      "ds_read_b64_tr_b16 v[106:107], v166 offset:19968\n\t"
      "v_add_f32_e32 v28, v26, v27\n\t"
      "v_add_f32_e32 v32, v16, v17\n\t"
      "v_add_f32_e32 v20, v18, v19\n\t"
      "v_mfma_f32_16x16x32_bf16 v[56:59], v[128:131], v[132:135], v[56:59]\n\t"
      "ds_read_b128 v[96:99], v161 offset:11264\n\t"
      "v_add_f32_e32 v33, v33, v28\n\t"
      "v_add_f32_e32 v32, v32, v20\n\t"
      "v_mfma_f32_16x16x32_bf16 v[40:43], v[120:123], v[132:135], v[40:43]\n\t"
      "ds_read_b128 v[100:103], v161 offset:25600\n\t"
      "v_permlane16_swap_b32_e32 v32, v33\n\t"
      "v_add_f32_e32 v32, v32, v33\n\t"
      "v_mov_b32_e32 v33, v32\n\t"
      "v_mfma_f32_16x16x32_bf16 v[52:55], v[124:127], v[136:139], v[52:55]\n\t"
      "ds_read_b128 v[92:95], v165 offset:11264\n\t"
      "v_permlane32_swap_b32_e32 v32, v33\n\t"
      "v_add_f32_e32 v32, v32, v33\n\t"
      "v_mfma_f32_16x16x32_bf16 v[60:63], v[128:131], v[136:139], v[60:63]\n\t"
      "ds_read_b64_tr_b16 v[108:109], v167 offset:19456\n\t"
      "v_mov_b32_e32 v33, v32\n\t"
      "s_nop 1\n\t"
      "v_permlane16_swap_b32_e32 v32, v33\n\t"
      "v_mfma_f32_16x16x32_bf16 v[44:47], v[120:123], v[136:139], v[44:47]\n\t"
      "ds_read_b64_tr_b16 v[110:111], v167 offset:19968\n\t"
      "v_rcp_f32_e32 v33, v33\n\t"
      "v_rcp_f32_e32 v32, v32\n\t"
      "s_barrier\n\t"
      "s_waitcnt lgkmcnt(7)\n\t"
      "v_mfma_f32_16x16x32_bf16 v[48:51], v[68:71], v[76:79], v[48:51]\n\t"
      "ds_read_b64_tr_b16 v[132:133], v170 offset:32768\n\t"
      "ds_read_b64_tr_b16 v[134:135], v170 offset:33280\n\t"
      "v_mul_f32_e32 v24, v24, v33\n\t"
      "v_mul_f32_e32 v25, v25, v33\n\t"
      "v_mfma_f32_16x16x32_bf16 v[56:59], v[72:75], v[76:79], v[56:59]\n\t"
      "ds_read_b128 v[124:127], v160 offset:12288\n\t"
      "v_mul_f32_e32 v26, v26, v33\n\t"
      "v_mul_f32_e32 v27, v27, v33\n\t"
      "v_mul_f32_e32 v16, v16, v32\n\t"
      "v_mfma_f32_16x16x32_bf16 v[40:43], v[64:67], v[76:79], v[40:43]\n\t"
      "ds_read_b128 v[128:131], v160 offset:26624\n\t"
      "v_mul_f32_e32 v17, v17, v32\n\t"
      "v_mul_f32_e32 v18, v18, v32\n\t"
      "v_mul_f32_e32 v19, v19, v32\n\t"
      "v_mfma_f32_16x16x32_bf16 v[52:55], v[68:71], v[80:83], v[52:55]\n\t"
      "ds_read_b128 v[120:123], v164 offset:12288\n\t"
      "v_cvt_pk_bf16_f32 v4, v24, v25\n\t"
      "v_cvt_pk_bf16_f32 v5, v26, v27\n\t"
      "v_cvt_pk_bf16_f32 v0, v16, v17\n\t"
      "v_mfma_f32_16x16x32_bf16 v[60:63], v[72:75], v[80:83], v[60:63]\n\t"
      "ds_read_b64_tr_b16 v[136:137], v171 offset:32768\n\t"
      "v_cvt_pk_bf16_f32 v1, v18, v19\n\t"
      "v_and_b32_e32 v6, v184, v4\n\t"
      "v_and_b32_e32 v7, v185, v5\n\t"
      "v_mfma_f32_16x16x32_bf16 v[44:47], v[64:67], v[80:83], v[44:47]\n\t"
      "ds_read_b64_tr_b16 v[138:139], v171 offset:33280\n\t"
      "v_and_b32_e32 v2, v184, v0\n\t"
      "v_and_b32_e32 v3, v185, v1\n\t"
      "v_xor_b32_e32 v4, v4, v6\n\t"
      "s_waitcnt lgkmcnt(7)\n\t"
      "v_mfma_f32_16x16x32_bf16 v[48:51], v[96:99], v[104:107], v[48:51]\n\t"
      "ds_read_b64_tr_b16 v[76:77], v170 offset:33792\n\t"
      "ds_read_b64_tr_b16 v[78:79], v170 offset:34304\n\t"
      "v_xor_b32_e32 v5, v5, v7\n\t"
      "v_xor_b32_e32 v0, v0, v2\n\t"
      "v_xor_b32_e32 v1, v1, v3\n\t"
      "v_mfma_f32_16x16x32_bf16 v[56:59], v[100:103], v[104:107], v[56:59]\n\t"
      "ds_read_b128 v[68:71], v161 offset:13312\n\t"
      "v_mfma_f32_16x16x32_bf16 v[40:43], v[92:95], v[104:107], v[40:43]\n\t"
      "ds_read_b128 v[72:75], v161 offset:27648\n\t"
      "v_mfma_f32_16x16x32_bf16 v[52:55], v[96:99], v[108:111], v[52:55]\n\t"
      "ds_read_b128 v[64:67], v165 offset:13312\n\t"
      "v_mfma_f32_16x16x32_bf16 v[60:63], v[100:103], v[108:111], v[60:63]\n\t"
      "ds_read_b64_tr_b16 v[80:81], v171 offset:33792\n\t"
      "v_mfma_f32_16x16x32_bf16 v[44:47], v[92:95], v[108:111], v[44:47]\n\t"
      "ds_read_b64_tr_b16 v[82:83], v171 offset:34304\n\t"
      "s_waitcnt lgkmcnt(7)\n\t"
      "v_mfma_f32_16x16x32_bf16 v[48:51], v[124:127], v[132:135], v[48:51]\n\t"
      "v_mfma_f32_16x16x32_bf16 v[56:59], v[128:131], v[132:135], v[56:59]\n\t"
      "v_mfma_f32_16x16x32_bf16 v[40:43], v[120:123], v[132:135], v[40:43]\n\t"
      "v_mfma_f32_16x16x32_bf16 v[52:55], v[124:127], v[136:139], v[52:55]\n\t"
      "v_mfma_f32_16x16x32_bf16 v[60:63], v[128:131], v[136:139], v[60:63]\n\t"
      "v_mfma_f32_16x16x32_bf16 v[44:47], v[120:123], v[136:139], v[44:47]\n\t"
      "s_waitcnt lgkmcnt(0)\n\t"
      "v_mfma_f32_16x16x32_bf16 v[48:51], v[68:71], v[76:79], v[48:51]\n\t"
      "v_mfma_f32_16x16x32_bf16 v[56:59], v[72:75], v[76:79], v[56:59]\n\t"
      "v_mfma_f32_16x16x32_bf16 v[40:43], v[64:67], v[76:79], v[40:43]\n\t"
      "v_mfma_f32_16x16x32_bf16 v[52:55], v[68:71], v[80:83], v[52:55]\n\t"
      "v_mfma_f32_16x16x32_bf16 v[60:63], v[72:75], v[80:83], v[60:63]\n\t"
      "v_mfma_f32_16x16x32_bf16 v[44:47], v[64:67], v[80:83], v[44:47]\n\t"
      "s_nop 2\n\t"
      "v_cvt_pk_bf16_f32 v66, v48, v49\n\t"
      "v_cvt_pk_bf16_f32 v67, v50, v51\n\t"
      "v_cvt_pk_bf16_f32 v68, v56, v57\n\t"
      "v_cvt_pk_bf16_f32 v69, v58, v59\n\t"
      "v_cvt_pk_bf16_f32 v64, v40, v41\n\t"
      "v_cvt_pk_bf16_f32 v65, v42, v43\n\t"
      "v_mfma_f32_16x16x32_bf16 v[88:91], v[66:69], v[4:7], 0\n\t"
      "s_nop 0\n\t"
      "v_mfma_f32_16x16x32_bf16 v[80:83], v[64:67], v[0:3], 0\n\t"
      "v_cvt_pk_bf16_f32 v72, v52, v53\n\t"
      "v_cvt_pk_bf16_f32 v73, v54, v55\n\t"
      "v_cvt_pk_bf16_f32 v74, v60, v61\n\t"
      "v_cvt_pk_bf16_f32 v75, v62, v63\n\t"
      "v_cvt_pk_bf16_f32 v70, v44, v45\n\t"
      "v_cvt_pk_bf16_f32 v71, v46, v47\n\t"
      "v_mfma_f32_16x16x32_bf16 v[92:95], v[72:75], v[4:7], 0\n\t"
      "s_nop 0\n\t"
      "v_mfma_f32_16x16x32_bf16 v[84:87], v[70:73], v[0:3], 0\n\t"
      "v_cvt_pk_bf16_f32 v152, v88, v89\n\t"
      "v_cvt_pk_bf16_f32 v153, v90, v91\n\t"
      "v_min_u32_e32 v152, -2, v152\n\t"
      "v_min_u32_e32 v153, -2, v153\n\t"
      "v_cvt_pk_bf16_f32 v148, v80, v81\n\t"
      "v_cvt_pk_bf16_f32 v149, v82, v83\n\t"
      "v_min_u32_e32 v148, -2, v148\n\t"
      "v_min_u32_e32 v149, -2, v149\n\t"
      "v_cvt_pk_bf16_f32 v154, v92, v93\n\t"
      "v_cvt_pk_bf16_f32 v155, v94, v95\n\t"
      "v_cndmask_b32_e64 v154, v154, 0, s[68:69]\n\t"
      "v_min_u32_e32 v154, -2, v154\n\t"
      "v_cndmask_b32_e64 v155, v155, 0, s[68:69]\n\t"
      "v_min_u32_e32 v155, -2, v155\n\t"
      "global_store_dwordx4 v244, v[152:155], s[44:45] sc1\n\t"
      "v_cvt_pk_bf16_f32 v150, v84, v85\n\t"
      "v_cvt_pk_bf16_f32 v151, v86, v87\n\t"
      "v_cndmask_b32_e64 v150, v150, 0, s[68:69]\n\t"
      "v_min_u32_e32 v150, -2, v150\n\t"
      "v_cndmask_b32_e64 v151, v151, 0, s[68:69]\n\t"
      "v_min_u32_e32 v151, -2, v151\n\t"
      "global_store_dwordx4 v244, v[148:151], s[42:43] sc1\n\t"
      "s_barrier\n\t"
      "s_cmp_eq_u32 s26, 7\n\t"
      "s_cbranch_scc1 SPARE_%=\n\t"
      "s_lshl_b32 s34, s25, 16\n\t"
      "s_lshl2_add_u32 s34, s39, s34\n\t"
      "s_add_u32 s34, s34, 4096\n\t"
      "s_add_u32 s42, s20, s34\n\t"
      "s_addc_u32 s43, s21, 0\n\t"
      "s_add_u32 s44, s42, 8192\n\t"
      "s_addc_u32 s45, s43, 0\n\t"
      "s_lshl_b32 s72, s24, 10\n\t"
      "s_add_u32 s72, s72, 98304\n\t"
      "v_xor_b32_e32 v203, v197, v198\n\t"
      "v_lshl_add_u32 v203, v203, 4, s72\n\t"
      "v_lshl_add_u32 v203, v197, 7, v203\n\t"
      "ds_write_b128 v203, v[204:207]\n\t"
      "s_and_b32 s35, s25, 3\n\t"
      "s_lshl2_add_u32 s31, s26, s35\n\t"
      "s_and_b32 s35, s2, 7\n\t"
      "s_mul_i32 s34, s35, 14\n\t"
      "s_lshl4_add_u32 s35, s35, -14\n\t"
      "s_cmp_lt_u32 s31, 14\n\t"
      "s_cselect_b32 s34, s34, s35\n\t"
      "s_cselect_b32 s36, 1, 0\n\t"
      "s_or_b32 s27, s27, s36\n\t"
      "s_add_u32 s34, s34, s31\n\t"
      "s_lshr_b32 s29, s34, 2\n\t"
      "s_and_b32 s32, s34, 3\n\t"
      "s_mul_i32 s30, s32, 7\n\t"
      "v_and_b32_e32 v199, 60, v192\n\t"
      "v_and_b32_e32 v200, 24, v192\n\t"
      "v_lshlrev_b32_e32 v202, 3, v196\n\t"
      "v_lshl_or_b32 v202, v200, 2, v202\n\t"
      "v_lshl_or_b32 v202, v195, 7, v202\n\t"
      "v_lshl_or_b32 v202, v193, 10, v202\n\t"
      "v_add_u32_e32 v224, s39, v202\n\t"
      "v_xor_b32_e32 v225, 32, v224\n\t"
      "v_xor_b32_e32 v226, 64, v224\n\t"
      "v_xor_b32_e32 v227, 96, v224\n\t"
      "s_lshl_b32 s34, s24, 12\n\t"
      "s_add_u32 s34, s34, 98304\n\t"
      "v_add_u32_e32 v245, s34, v244\n\t"
      "v_lshlrev_b32_e32 v199, 2, v199\n\t"
      "s_lshl_b32 s34, s24, 8\n\t"
      "v_lshl_add_u32 v247, v192, 2, s34\n\t"
      "v_and_b32_e32 v200, 2, v192\n\t"
      "v_mul_u32_u24_e32 v202, 576, v200\n\t"
      "v_xor_b32_e32 v199, v202, v199\n\t"
      "v_and_b32_e32 v202, 1, v192\n\t"
      "v_lshl_or_b32 v199, v202, 3, v199\n\t"
      "s_and_b32 s35, s34, 0x400\n\t"
      "s_add_u32 s34, s34, s35\n\t"
      "s_add_u32 s34, s34, 98304\n\t"
      "v_add_u32_e32 v246, s34, v199\n\t"
      "v_lshlrev_b32_e32 v199, 2, v202\n\t"
      "v_lshl_or_b32 v199, v200, 4, v199\n\t"
      "s_waitcnt lgkmcnt(0)\n\t"
      "s_barrier\n\t"
      "v_xor_b32_e32 v210, 32, v228\n\t"
      "v_xor_b32_e32 v212, 64, v228\n\t"
      "v_xor_b32_e32 v214, 96, v228\n\t"
      "ds_read_b64 v[208:209], v228\n\t"
      "ds_read_b64 v[210:211], v210\n\t"
      "ds_read_b64 v[212:213], v212\n\t"
      "ds_read_b64 v[214:215], v214\n\t"
      "ds_read_b64_tr_b16 v[0:1], v216 offset:7168\n\t"
      "ds_read_b64_tr_b16 v[2:3], v220 offset:7168\n\t"
      "ds_read_b64_tr_b16 v[4:5], v217 offset:7168\n\t"
      "ds_read_b64_tr_b16 v[6:7], v221 offset:7168\n\t"
      "ds_read_b64_tr_b16 v[8:9], v218 offset:7168\n\t"
      "ds_read_b64_tr_b16 v[10:11], v222 offset:7168\n\t"
      "ds_read_b64_tr_b16 v[12:13], v219 offset:7168\n\t"
      "ds_read_b64_tr_b16 v[14:15], v223 offset:7168\n\t"
      "ds_read_b64_tr_b16 v[16:17], v218 offset:10752\n\t"
      "ds_read_b64_tr_b16 v[18:19], v222 offset:10752\n\t"
      "ds_read_b64_tr_b16 v[20:21], v219 offset:10752\n\t"
      "ds_read_b64_tr_b16 v[22:23], v223 offset:10752\n\t"
      "ds_read_b64_tr_b16 v[24:25], v216 offset:10752\n\t"
      "ds_read_b64_tr_b16 v[26:27], v220 offset:10752\n\t"
      "ds_read_b64_tr_b16 v[28:29], v217 offset:10752\n\t"
      "ds_read_b64_tr_b16 v[30:31], v221 offset:10752\n\t"
      "ds_read_b64_tr_b16 v[32:33], v216 offset:14336\n\t"
      "ds_read_b64_tr_b16 v[34:35], v220 offset:14336\n\t"
      "ds_read_b64_tr_b16 v[36:37], v217 offset:14336\n\t"
      "ds_read_b64_tr_b16 v[38:39], v221 offset:14336\n\t"
      "ds_read_b64_tr_b16 v[40:41], v218 offset:14336\n\t"
      "ds_read_b64_tr_b16 v[42:43], v222 offset:14336\n\t"
      "ds_read_b64_tr_b16 v[44:45], v219 offset:14336\n\t"
      "ds_read_b64_tr_b16 v[46:47], v223 offset:14336\n\t"
      "ds_read_b64_tr_b16 v[48:49], v218 offset:17920\n\t"
      "ds_read_b64_tr_b16 v[50:51], v222 offset:17920\n\t"
      "ds_read_b64_tr_b16 v[52:53], v219 offset:17920\n\t"
      "ds_read_b64_tr_b16 v[54:55], v223 offset:17920\n\t"
      "ds_read_b64_tr_b16 v[56:57], v216 offset:17920\n\t"
      "ds_read_b64_tr_b16 v[58:59], v220 offset:17920\n\t"
      "ds_read_b64_tr_b16 v[60:61], v217 offset:17920\n\t"
      "ds_read_b64_tr_b16 v[62:63], v221 offset:17920\n\t"
      "ds_read_b64_tr_b16 v[64:65], v216 offset:21504\n\t"
      "ds_read_b64_tr_b16 v[66:67], v220 offset:21504\n\t"
      "ds_read_b64_tr_b16 v[68:69], v217 offset:21504\n\t"
      "ds_read_b64_tr_b16 v[70:71], v221 offset:21504\n\t"
      "ds_read_b64_tr_b16 v[72:73], v218 offset:21504\n\t"
      "ds_read_b64_tr_b16 v[74:75], v222 offset:21504\n\t"
      "ds_read_b64_tr_b16 v[76:77], v219 offset:21504\n\t"
      "ds_read_b64_tr_b16 v[78:79], v223 offset:21504\n\t"
      "ds_read_b64_tr_b16 v[80:81], v218 offset:25088\n\t"
      "ds_read_b64_tr_b16 v[82:83], v222 offset:25088\n\t"
      "ds_read_b64_tr_b16 v[84:85], v219 offset:25088\n\t"
      "ds_read_b64_tr_b16 v[86:87], v223 offset:25088\n\t"
      "ds_read_b64_tr_b16 v[88:89], v216 offset:25088\n\t"
      "ds_read_b64_tr_b16 v[90:91], v220 offset:25088\n\t"
      "ds_read_b64_tr_b16 v[92:93], v217 offset:25088\n\t"
      "ds_read_b64_tr_b16 v[94:95], v221 offset:25088\n\t"
      "ds_read_b64_tr_b16 v[96:97], v216 offset:28672\n\t"
      "ds_read_b64_tr_b16 v[98:99], v220 offset:28672\n\t"
      "ds_read_b64_tr_b16 v[100:101], v217 offset:28672\n\t"
      "ds_read_b64_tr_b16 v[102:103], v221 offset:28672\n\t"
      "ds_read_b64_tr_b16 v[104:105], v218 offset:28672\n\t"
      "ds_read_b64_tr_b16 v[106:107], v222 offset:28672\n\t"
      "ds_read_b64_tr_b16 v[108:109], v219 offset:28672\n\t"
      "ds_read_b64_tr_b16 v[110:111], v223 offset:28672\n\t"
      "ds_read_b64_tr_b16 v[112:113], v218 offset:32256\n\t"
      "ds_read_b64_tr_b16 v[114:115], v222 offset:32256\n\t"
      "ds_read_b64_tr_b16 v[116:117], v219 offset:32256\n\t"
      "ds_read_b64_tr_b16 v[118:119], v223 offset:32256\n\t"
      "ds_read_b64_tr_b16 v[120:121], v216 offset:32256\n\t"
      "ds_read_b64_tr_b16 v[122:123], v220 offset:32256\n\t"
      "ds_read_b64_tr_b16 v[124:125], v217 offset:32256\n\t"
      "ds_read_b64_tr_b16 v[126:127], v221 offset:32256\n\t"
      "s_sleep 2\n\t"
      "global_load_dwordx4 v[128:131], v244, s[42:43] offset:-2048 sc1\n\t"
      "global_load_dwordx4 v[132:135], v244, s[42:43] offset:-1024 sc1\n\t"
      "global_load_dwordx4 v[136:139], v244, s[42:43] sc1\n\t"
      "global_load_dwordx4 v[140:143], v244, s[42:43] offset:1024 sc1\n\t"
      "global_load_dwordx4 v[144:147], v244, s[42:43] offset:2048 sc1\n\t"
      "global_load_dwordx4 v[148:151], v244, s[42:43] offset:3072 sc1\n\t"
      "global_load_dwordx4 v[152:155], v244, s[44:45] offset:-4096 sc1\n\t"
      "global_load_dwordx4 v[156:159], v244, s[44:45] offset:-3072 sc1\n\t"
      "global_load_dwordx4 v[160:163], v244, s[44:45] offset:-2048 sc1\n\t"
      "global_load_dwordx4 v[164:167], v244, s[44:45] offset:-1024 sc1\n\t"
      "s_min_u32 s34, s29, 27\n\t"
      "s_lshl_b32 s34, s34, 7\n\t"
      "s_lshr_b32 s35, s24, 2\n\t"
      "s_lshl_b32 s35, s35, 6\n\t"
      "s_add_u32 s34, s34, s35\n\t"
      "s_lshr_b32 s35, s39, 9\n\t"
      "s_add_u32 s34, s34, s35\n\t"
      "v_add_u32_e32 v248, s34, v199\n\t"
      "s_add_u32 s48, s20, 8388608\n\t"
      "s_addc_u32 s49, s21, 0\n\t"
      "v_and_b32_e32 v199, 16, v192\n\t"
      "v_lshlrev_b32_e32 v199, 1, v199\n\t"
      "v_and_b32_e32 v200, 32, v192\n\t"
      "s_lshl_b32 s34, s28, 18\n\t"
      "s_lshl_b32 s35, s25, 11\n\t"
      "s_add_u32 s34, s34, s35\n\t"
      "s_lshr_b32 s35, s39, 3\n\t"
      "s_add_u32 s34, s34, s35\n\t"
      "v_and_b32_e32 v201, 0x1f0, v244\n\t"
      "v_lshl_or_b32 v201, v200, 11, v201\n\t"
      "v_add_u32_e32 v229, s34, v201\n\t"
      "v_add_u32_e32 v230, 131072, v229\n\t"
      "s_and_b32 s34, s25, 30\n\t"
      "s_lshr_b32 s35, s39, 13\n\t"
      "s_add_u32 s34, s34, s35\n\t"
      "s_mul_i32 s34, s34, 7\n\t"
      "s_add_u32 s34, s34, s28\n\t"
      "s_lshl_b32 s34, s34, 3\n\t"
      "s_and_b32 s35, s25, 1\n\t"
      "s_add_u32 s34, s34, s35\n\t"
      "s_lshl_b32 s34, s34, 10\n\t"
      "s_and_b32 s35, s24, 1\n\t"
      "s_lshl_b32 s35, s35, 9\n\t"
      "s_add_u32 s34, s34, s35\n\t"
      "s_add_u32 s34, s34, 6293504\n\t"
      "v_lshrrev_b32_e32 v201, 1, v200\n\t"
      "v_lshl_or_b32 v201, v199, 6, v201\n\t"
      "v_lshl_or_b32 v201, v194, 5, v201\n\t"
      "v_add_u32_e32 v241, s34, v201\n\t"
      "s_lshl_b32 s34, s24, 12\n\t"
      "s_add_u32 s34, s34, 131072\n\t"
      "v_xor_b32_e32 v201, v193, v194\n\t"
      "v_lshlrev_b32_e32 v201, 3, v201\n\t"
      "v_lshl_or_b32 v201, v194, 7, v201\n\t"
      "v_add_u32_e32 v233, s34, v201\n\t"
      "v_lshrrev_b32_e32 v199, 2, v199\n\t"
      "v_or_b32_e32 v199, v199, v195\n\t"
      "v_lshrrev_b32_e32 v200, 3, v200\n\t"
      "v_or_b32_e32 v200, v196, v200\n\t"
      "v_xor_b32_e32 v200, v200, v199\n\t"
      "v_lshlrev_b32_e32 v200, 3, v200\n\t"
      "v_lshl_or_b32 v200, v199, 7, v200\n\t"
      "v_add_u32_e32 v237, s34, v200\n\t"
      "v_xor_b32_e32 v234, 32, v233\n\t"
      "v_xor_b32_e32 v238, 32, v237\n\t"
      "v_xor_b32_e32 v235, 64, v233\n\t"
      "v_xor_b32_e32 v239, 64, v237\n\t"
      "v_xor_b32_e32 v236, 96, v233\n\t"
      "v_xor_b32_e32 v240, 96, v237\n\t"
      "s_waitcnt lgkmcnt(14)\n\t"
      "v_lshlrev_b32_e32 v192, 16, v208\n\t"
      "v_and_b32_e32 v193, 0xffff0000, v208\n\t"
      "v_lshlrev_b32_e32 v194, 16, v209\n\t"
      "v_and_b32_e32 v195, 0xffff0000, v209\n\t"
      "v_lshlrev_b32_e32 v196, 16, v210\n\t"
      "v_and_b32_e32 v197, 0xffff0000, v210\n\t"
      "v_lshlrev_b32_e32 v198, 16, v211\n\t"
      "v_and_b32_e32 v199, 0xffff0000, v211\n\t"
      "v_lshlrev_b32_e32 v200, 16, v212\n\t"
      "v_and_b32_e32 v201, 0xffff0000, v212\n\t"
      "v_lshlrev_b32_e32 v202, 16, v213\n\t"
      "v_and_b32_e32 v203, 0xffff0000, v213\n\t"
      "v_lshlrev_b32_e32 v204, 16, v214\n\t"
      "v_and_b32_e32 v205, 0xffff0000, v214\n\t"
      "v_lshlrev_b32_e32 v206, 16, v215\n\t"
      "v_and_b32_e32 v207, 0xffff0000, v215\n\t"
      "global_load_dwordx4 v[168:171], v244, s[44:45] sc1\n\t"
      "global_load_dwordx4 v[172:175], v244, s[44:45] offset:1024 sc1\n\t"
      "global_load_dwordx4 v[176:179], v244, s[44:45] offset:2048 sc1\n\t"
      "global_load_dwordx4 v[180:183], v244, s[44:45] offset:3072 sc1\n\t"
      "global_load_dwordx4 v[184:187], v244, s[42:43] offset:-4096 sc1\n\t"
      "global_load_dwordx4 v[188:191], v244, s[42:43] offset:-3072 sc1\n\t"
      "global_load_dword v249, v248, s[12:13]\n\t"
      "s_barrier\n\t"
      "s_waitcnt lgkmcnt(0)\n\t"
      "LGM_%=:\n\t"
      "s_waitcnt vmcnt(15)\n\t"
      "v_max_u32_e32 v232, v128, v132\n\t"
      "v_cmp_eq_u32_e32 vcc, -1, v232\n\t"
      "s_cbranch_vccnz LO0R_%=\n\t"
      "LO0B_%=:\n\t"
      "v_mfma_f32_16x16x32_bf16 v[192:195], v[0:3], v[128:131], v[192:195]\n\t"
      "ds_read_b64_tr_b16 v[0:1], v216 offset:35840\n\t"
      "ds_read_b64_tr_b16 v[2:3], v220 offset:35840\n\t"
      "v_mfma_f32_16x16x32_bf16 v[196:199], v[4:7], v[128:131], v[196:199]\n\t"
      "ds_read_b64_tr_b16 v[4:5], v217 offset:35840\n\t"
      "v_mfma_f32_16x16x32_bf16 v[200:203], v[8:11], v[128:131], v[200:203]\n\t"
      "ds_read_b64_tr_b16 v[6:7], v221 offset:35840\n\t"
      "v_mfma_f32_16x16x32_bf16 v[204:207], v[12:15], v[128:131], v[204:207]\n\t"
      "ds_read_b64_tr_b16 v[8:9], v218 offset:35840\n\t"
      "ds_read_b64_tr_b16 v[10:11], v222 offset:35840\n\t"
      "v_mfma_f32_16x16x32_bf16 v[192:195], v[16:19], v[132:135], v[192:195]\n\t"
      "ds_read_b64_tr_b16 v[12:13], v219 offset:35840\n\t"
      "v_mfma_f32_16x16x32_bf16 v[196:199], v[20:23], v[132:135], v[196:199]\n\t"
      "ds_read_b64_tr_b16 v[14:15], v223 offset:35840\n\t"
      "v_mfma_f32_16x16x32_bf16 v[200:203], v[24:27], v[132:135], v[200:203]\n\t"
      "ds_read_b64_tr_b16 v[16:17], v218 offset:39424\n\t"
      "ds_read_b64_tr_b16 v[18:19], v222 offset:39424\n\t"
      "v_mfma_f32_16x16x32_bf16 v[204:207], v[28:31], v[132:135], v[204:207]\n\t"
      "ds_read_b64_tr_b16 v[20:21], v219 offset:39424\n\t"
      "s_waitcnt vmcnt(13)\n\t"
      "v_max_u32_e32 v232, v136, v140\n\t"
      "v_cmp_eq_u32_e32 vcc, -1, v232\n\t"
      "s_cbranch_vccnz LO2R_%=\n\t"
      "LO2B_%=:\n\t"
      "v_mfma_f32_16x16x32_bf16 v[192:195], v[32:35], v[136:139], v[192:195]\n\t"
      "ds_read_b64_tr_b16 v[22:23], v223 offset:39424\n\t"
      "v_mfma_f32_16x16x32_bf16 v[196:199], v[36:39], v[136:139], v[196:199]\n\t"
      "ds_read_b64_tr_b16 v[24:25], v216 offset:39424\n\t"
      "ds_read_b64_tr_b16 v[26:27], v220 offset:39424\n\t"
      "v_mfma_f32_16x16x32_bf16 v[200:203], v[40:43], v[136:139], v[200:203]\n\t"
      "ds_read_b64_tr_b16 v[28:29], v217 offset:39424\n\t"
      "v_mfma_f32_16x16x32_bf16 v[204:207], v[44:47], v[136:139], v[204:207]\n\t"
      "ds_read_b64_tr_b16 v[30:31], v221 offset:39424\n\t"
      "v_mfma_f32_16x16x32_bf16 v[192:195], v[48:51], v[140:143], v[192:195]\n\t"
      "ds_read_b64_tr_b16 v[32:33], v216 offset:43008\n\t"
      "ds_read_b64_tr_b16 v[34:35], v220 offset:43008\n\t"
      "v_mfma_f32_16x16x32_bf16 v[196:199], v[52:55], v[140:143], v[196:199]\n\t"
      "ds_read_b64_tr_b16 v[36:37], v217 offset:43008\n\t"
      "v_mfma_f32_16x16x32_bf16 v[200:203], v[56:59], v[140:143], v[200:203]\n\t"
      "ds_read_b64_tr_b16 v[38:39], v221 offset:43008\n\t"
      "v_mfma_f32_16x16x32_bf16 v[204:207], v[60:63], v[140:143], v[204:207]\n\t"
      "ds_read_b64_tr_b16 v[40:41], v218 offset:43008\n\t"
      "ds_read_b64_tr_b16 v[42:43], v222 offset:43008\n\t"
      "s_waitcnt vmcnt(11)\n\t"
      "v_max_u32_e32 v232, v144, v148\n\t"
      "v_cmp_eq_u32_e32 vcc, -1, v232\n\t"
      "s_cbranch_vccnz LO4R_%=\n\t"
      "LO4B_%=:\n\t"
      "v_mfma_f32_16x16x32_bf16 v[192:195], v[64:67], v[144:147], v[192:195]\n\t"
      "ds_read_b64_tr_b16 v[44:45], v219 offset:43008\n\t"
      "v_mfma_f32_16x16x32_bf16 v[196:199], v[68:71], v[144:147], v[196:199]\n\t"
      "ds_read_b64_tr_b16 v[46:47], v223 offset:43008\n\t"
      "v_mfma_f32_16x16x32_bf16 v[200:203], v[72:75], v[144:147], v[200:203]\n\t"
      "ds_read_b64_tr_b16 v[48:49], v218 offset:46592\n\t"
      "ds_read_b64_tr_b16 v[50:51], v222 offset:46592\n\t"
      "v_mfma_f32_16x16x32_bf16 v[204:207], v[76:79], v[144:147], v[204:207]\n\t"
      "ds_read_b64_tr_b16 v[52:53], v219 offset:46592\n\t"
      "v_mfma_f32_16x16x32_bf16 v[192:195], v[80:83], v[148:151], v[192:195]\n\t"
      "ds_read_b64_tr_b16 v[54:55], v223 offset:46592\n\t"
      "v_mfma_f32_16x16x32_bf16 v[196:199], v[84:87], v[148:151], v[196:199]\n\t"
      "ds_read_b64_tr_b16 v[56:57], v216 offset:46592\n\t"
      "ds_read_b64_tr_b16 v[58:59], v220 offset:46592\n\t"
      "v_mfma_f32_16x16x32_bf16 v[200:203], v[88:91], v[148:151], v[200:203]\n\t"
      "ds_read_b64_tr_b16 v[60:61], v217 offset:46592\n\t"
      "v_mfma_f32_16x16x32_bf16 v[204:207], v[92:95], v[148:151], v[204:207]\n\t"
      "ds_read_b64_tr_b16 v[62:63], v221 offset:46592\n\t"
      "s_waitcnt vmcnt(9)\n\t"
      "v_max_u32_e32 v232, v152, v156\n\t"
      "v_cmp_eq_u32_e32 vcc, -1, v232\n\t"
      "s_cbranch_vccnz LO6R_%=\n\t"
      "LO6B_%=:\n\t"
      "v_mfma_f32_16x16x32_bf16 v[192:195], v[96:99], v[152:155], v[192:195]\n\t"
      "ds_read_b64_tr_b16 v[64:65], v216 offset:50176\n\t"
      "ds_read_b64_tr_b16 v[66:67], v220 offset:50176\n\t"
      "v_mfma_f32_16x16x32_bf16 v[196:199], v[100:103], v[152:155], v[196:199]\n\t"
      "ds_read_b64_tr_b16 v[68:69], v217 offset:50176\n\t"
      "v_mfma_f32_16x16x32_bf16 v[200:203], v[104:107], v[152:155], v[200:203]\n\t"
      "ds_read_b64_tr_b16 v[70:71], v221 offset:50176\n\t"
      "v_mfma_f32_16x16x32_bf16 v[204:207], v[108:111], v[152:155], v[204:207]\n\t"
      "ds_read_b64_tr_b16 v[72:73], v218 offset:50176\n\t"
      "ds_read_b64_tr_b16 v[74:75], v222 offset:50176\n\t"
      "v_mfma_f32_16x16x32_bf16 v[192:195], v[112:115], v[156:159], v[192:195]\n\t"
      "ds_read_b64_tr_b16 v[76:77], v219 offset:50176\n\t"
      "v_mfma_f32_16x16x32_bf16 v[196:199], v[116:119], v[156:159], v[196:199]\n\t"
      "ds_read_b64_tr_b16 v[78:79], v223 offset:50176\n\t"
      "v_mfma_f32_16x16x32_bf16 v[200:203], v[120:123], v[156:159], v[200:203]\n\t"
      "ds_read_b64_tr_b16 v[80:81], v218 offset:53760\n\t"
      "ds_read_b64_tr_b16 v[82:83], v222 offset:53760\n\t"
      "v_mfma_f32_16x16x32_bf16 v[204:207], v[124:127], v[156:159], v[204:207]\n\t"
      "ds_read_b64_tr_b16 v[84:85], v219 offset:53760\n\t"
      "s_waitcnt vmcnt(7)\n\t"
      "v_max_u32_e32 v232, v160, v164\n\t"
      "v_cmp_eq_u32_e32 vcc, -1, v232\n\t"
      "s_cbranch_vccnz LO8R_%=\n\t"
      "LO8B_%=:\n\t"
      "s_waitcnt lgkmcnt(14)\n\t"
      "v_mfma_f32_16x16x32_bf16 v[192:195], v[0:3], v[160:163], v[192:195]\n\t"
      "ds_read_b64_tr_b16 v[86:87], v223 offset:53760\n\t"
      "v_mfma_f32_16x16x32_bf16 v[196:199], v[4:7], v[160:163], v[196:199]\n\t"
      "ds_read_b64_tr_b16 v[88:89], v216 offset:53760\n\t"
      "ds_read_b64_tr_b16 v[90:91], v220 offset:53760\n\t"
      "v_mfma_f32_16x16x32_bf16 v[200:203], v[8:11], v[160:163], v[200:203]\n\t"
      "ds_read_b64_tr_b16 v[92:93], v217 offset:53760\n\t"
      "v_mfma_f32_16x16x32_bf16 v[204:207], v[12:15], v[160:163], v[204:207]\n\t"
      "ds_read_b64_tr_b16 v[94:95], v221 offset:53760\n\t"
      "v_mfma_f32_16x16x32_bf16 v[192:195], v[16:19], v[164:167], v[192:195]\n\t"
      "ds_read_b64_tr_b16 v[96:97], v216\n\t"
      "ds_read_b64_tr_b16 v[98:99], v220\n\t"
      "v_mfma_f32_16x16x32_bf16 v[196:199], v[20:23], v[164:167], v[196:199]\n\t"
      "ds_read_b64_tr_b16 v[100:101], v217\n\t"
      "v_mfma_f32_16x16x32_bf16 v[200:203], v[24:27], v[164:167], v[200:203]\n\t"
      "ds_read_b64_tr_b16 v[102:103], v221\n\t"
      "v_mfma_f32_16x16x32_bf16 v[204:207], v[28:31], v[164:167], v[204:207]\n\t"
      "ds_read_b64_tr_b16 v[104:105], v218\n\t"
      "ds_read_b64_tr_b16 v[106:107], v222\n\t"
      "s_waitcnt vmcnt(5)\n\t"
      "v_max_u32_e32 v232, v168, v172\n\t"
      "v_cmp_eq_u32_e32 vcc, -1, v232\n\t"
      "s_cbranch_vccnz LO10R_%=\n\t"
      "LO10B_%=:\n\t"
      "v_mfma_f32_16x16x32_bf16 v[192:195], v[32:35], v[168:171], v[192:195]\n\t"
      "ds_read_b64_tr_b16 v[108:109], v219\n\t"
      "v_mfma_f32_16x16x32_bf16 v[196:199], v[36:39], v[168:171], v[196:199]\n\t"
      "ds_read_b64_tr_b16 v[110:111], v223\n\t"
      "v_mfma_f32_16x16x32_bf16 v[200:203], v[40:43], v[168:171], v[200:203]\n\t"
      "ds_read_b64_tr_b16 v[112:113], v218 offset:3584\n\t"
      "ds_read_b64_tr_b16 v[114:115], v222 offset:3584\n\t"
      "v_mfma_f32_16x16x32_bf16 v[204:207], v[44:47], v[168:171], v[204:207]\n\t"
      "ds_read_b64_tr_b16 v[116:117], v219 offset:3584\n\t"
      "s_waitcnt lgkmcnt(14)\n\t"
      "v_mfma_f32_16x16x32_bf16 v[192:195], v[48:51], v[172:175], v[192:195]\n\t"
      "ds_read_b64_tr_b16 v[118:119], v223 offset:3584\n\t"
      "v_mfma_f32_16x16x32_bf16 v[196:199], v[52:55], v[172:175], v[196:199]\n\t"
      "ds_read_b64_tr_b16 v[120:121], v216 offset:3584\n\t"
      "ds_read_b64_tr_b16 v[122:123], v220 offset:3584\n\t"
      "v_mfma_f32_16x16x32_bf16 v[200:203], v[56:59], v[172:175], v[200:203]\n\t"
      "ds_read_b64_tr_b16 v[124:125], v217 offset:3584\n\t"
      "v_mfma_f32_16x16x32_bf16 v[204:207], v[60:63], v[172:175], v[204:207]\n\t"
      "ds_read_b64_tr_b16 v[126:127], v221 offset:3584\n\t"
      "s_waitcnt vmcnt(3)\n\t"
      "v_max_u32_e32 v232, v176, v180\n\t"
      "v_cmp_eq_u32_e32 vcc, -1, v232\n\t"
      "s_cbranch_vccnz LO12R_%=\n\t"
      "LO12B_%=:\n\t"
      "v_mfma_f32_16x16x32_bf16 v[192:195], v[64:67], v[176:179], v[192:195]\n\t"
      "v_mfma_f32_16x16x32_bf16 v[196:199], v[68:71], v[176:179], v[196:199]\n\t"
      "v_mfma_f32_16x16x32_bf16 v[200:203], v[72:75], v[176:179], v[200:203]\n\t"
      "v_mfma_f32_16x16x32_bf16 v[204:207], v[76:79], v[176:179], v[204:207]\n\t"
      "s_waitcnt lgkmcnt(14)\n\t"
      "v_mfma_f32_16x16x32_bf16 v[192:195], v[80:83], v[180:183], v[192:195]\n\t"
      "v_mfma_f32_16x16x32_bf16 v[196:199], v[84:87], v[180:183], v[196:199]\n\t"
      "v_mfma_f32_16x16x32_bf16 v[200:203], v[88:91], v[180:183], v[200:203]\n\t"
      "v_mfma_f32_16x16x32_bf16 v[204:207], v[92:95], v[180:183], v[204:207]\n\t"
      "s_waitcnt vmcnt(1)\n\t"
      "v_max_u32_e32 v232, v184, v188\n\t"
      "v_cmp_eq_u32_e32 vcc, -1, v232\n\t"
      "s_cbranch_vccnz LO14R_%=\n\t"
      "LO14B_%=:\n\t"
      "s_waitcnt lgkmcnt(8)\n\t"
      "v_mfma_f32_16x16x32_bf16 v[192:195], v[96:99], v[184:187], v[192:195]\n\t"
      "v_mfma_f32_16x16x32_bf16 v[196:199], v[100:103], v[184:187], v[196:199]\n\t"
      "v_mfma_f32_16x16x32_bf16 v[200:203], v[104:107], v[184:187], v[200:203]\n\t"
      "v_mfma_f32_16x16x32_bf16 v[204:207], v[108:111], v[184:187], v[204:207]\n\t"
      "s_waitcnt lgkmcnt(0)\n\t"
      "v_mfma_f32_16x16x32_bf16 v[192:195], v[112:115], v[188:191], v[192:195]\n\t"
      "v_mfma_f32_16x16x32_bf16 v[196:199], v[116:119], v[188:191], v[196:199]\n\t"
      "v_mfma_f32_16x16x32_bf16 v[200:203], v[120:123], v[188:191], v[200:203]\n\t"
      "v_mfma_f32_16x16x32_bf16 v[204:207], v[124:127], v[188:191], v[204:207]\n\t"
      "s_nop 4\n\t"
      "v_cmp_u_f32_e32 vcc, v192, v192\n\t"
      "s_cbranch_vccnz LGX_%=\n\t"
      "LGB_%=:\n\t"
      "v_cvt_pk_bf16_f32 v208, v192, v193\n\t"
      "v_cvt_pk_bf16_f32 v209, v194, v195\n\t"
      "v_cvt_pk_bf16_f32 v210, v196, v197\n\t"
      "v_cvt_pk_bf16_f32 v211, v198, v199\n\t"
      "v_cvt_pk_bf16_f32 v212, v200, v201\n\t"
      "v_cvt_pk_bf16_f32 v213, v202, v203\n\t"
      "v_cvt_pk_bf16_f32 v214, v204, v205\n\t"
      "v_cvt_pk_bf16_f32 v215, v206, v207\n\t"
      "v_mov_b32_e32 v148, 0x3f803f80\n\t"
      "v_dot2_f32_bf16 v144, v208, v148, 0\n\t"
      "v_dot2_f32_bf16 v145, v208, v208, 0\n\t"
      "v_dot2_f32_bf16 v146, v209, v148, 0\n\t"
      "v_dot2_f32_bf16 v147, v209, v209, 0\n\t"
      "v_dot2_f32_bf16 v144, v210, v148, v144\n\t"
      "v_dot2_f32_bf16 v145, v210, v210, v145\n\t"
      "v_dot2_f32_bf16 v146, v211, v148, v146\n\t"
      "v_dot2_f32_bf16 v147, v211, v211, v147\n\t"
      "v_dot2_f32_bf16 v144, v212, v148, v144\n\t"
      "v_dot2_f32_bf16 v145, v212, v212, v145\n\t"
      "v_dot2_f32_bf16 v146, v213, v148, v146\n\t"
      "v_dot2_f32_bf16 v147, v213, v213, v147\n\t"
      "v_dot2_f32_bf16 v144, v214, v148, v144\n\t"
      "v_dot2_f32_bf16 v145, v214, v214, v145\n\t"
      "v_dot2_f32_bf16 v146, v215, v148, v146\n\t"
      "v_dot2_f32_bf16 v147, v215, v215, v147\n\t"
      "v_lshlrev_b32_e32 v128, 16, v208\n\t"
      "v_and_b32_e32 v129, 0xffff0000, v208\n\t"
      "v_lshlrev_b32_e32 v130, 16, v209\n\t"
      "v_pk_add_f32 v[144:145], v[144:145], v[146:147]\n\t"
      "v_and_b32_e32 v131, 0xffff0000, v209\n\t"
      "v_lshlrev_b32_e32 v132, 16, v210\n\t"
      "v_permlane16_swap_b32_e32 v144, v145\n\t"
      "v_add_f32_e32 v144, v144, v145\n\t"
      "v_mov_b32_e32 v145, v144\n\t"
      "v_and_b32_e32 v133, 0xffff0000, v210\n\t"
      "v_lshlrev_b32_e32 v134, 16, v211\n\t"
      "v_permlane32_swap_b32_e32 v144, v145\n\t"
      "v_add_f32_e32 v144, v144, v145\n\t"
      "v_mov_b32_e32 v145, v144\n\t"
      "v_and_b32_e32 v135, 0xffff0000, v211\n\t"
      "v_lshlrev_b32_e32 v136, 16, v212\n\t"
      "v_permlane16_swap_b32_e32 v144, v145\n\t"
      "s_mov_b64 exec, s[70:71]\n\t"
      "v_min_u32_e32 v144, -2, v144\n\t"
      "v_min_u32_e32 v145, -2, v145\n\t"
      "global_store_dwordx2 v231, v[144:145], s[58:59] sc1\n\t"
      "s_mov_b64 exec, -1\n\t"
      "s_barrier\n\t"
      "v_and_b32_e32 v137, 0xffff0000, v212\n\t"
      "v_lshlrev_b32_e32 v138, 16, v213\n\t"
      "v_and_b32_e32 v139, 0xffff0000, v213\n\t"
      "v_lshlrev_b32_e32 v140, 16, v214\n\t"
      "v_and_b32_e32 v141, 0xffff0000, v214\n\t"
      "v_lshlrev_b32_e32 v142, 16, v215\n\t"
      "v_and_b32_e32 v143, 0xffff0000, v215\n\t"
      "v_permlane16_swap_b32_e32 v208, v210\n\t"
      "v_permlane16_swap_b32_e32 v209, v211\n\t"
      "v_permlane16_swap_b32_e32 v212, v214\n\t"
      "v_permlane16_swap_b32_e32 v213, v215\n\t"
      "s_bitcmp0_b32 s27, 0\n\t"
      "s_cbranch_scc1 NTK0_%=\n\t"
      "ds_read_b64_tr_b16 v[0:1], v224 offset:16384\n\t"
      "ds_read_b64_tr_b16 v[2:3], v224 offset:16896\n\t"
      "ds_read_b64_tr_b16 v[4:5], v225 offset:16384\n\t"
      "ds_read_b64_tr_b16 v[6:7], v225 offset:16896\n\t"
      "ds_read_b64_tr_b16 v[8:9], v226 offset:16384\n\t"
      "ds_read_b64_tr_b16 v[10:11], v226 offset:16896\n\t"
      "ds_read_b64_tr_b16 v[12:13], v227 offset:16384\n\t"
      "ds_read_b64_tr_b16 v[14:15], v227 offset:16896\n\t"
      "ds_read_b64_tr_b16 v[16:17], v224 offset:49152\n\t"
      "ds_read_b64_tr_b16 v[18:19], v224 offset:49664\n\t"
      "ds_read_b64_tr_b16 v[20:21], v225 offset:49152\n\t"
      "ds_read_b64_tr_b16 v[22:23], v225 offset:49664\n\t"
      "ds_read_b64_tr_b16 v[24:25], v226 offset:49152\n\t"
      "ds_read_b64_tr_b16 v[26:27], v226 offset:49664\n\t"
      "ds_read_b64_tr_b16 v[28:29], v227 offset:49152\n\t"
      "ds_read_b64_tr_b16 v[30:31], v227 offset:49664\n\t"
      "ds_read_b64_tr_b16 v[32:33], v224\n\t"
      "ds_read_b64_tr_b16 v[34:35], v224 offset:512\n\t"
      "ds_read_b64_tr_b16 v[36:37], v225\n\t"
      "ds_read_b64_tr_b16 v[38:39], v225 offset:512\n\t"
      "ds_read_b64_tr_b16 v[40:41], v226\n\t"
      "ds_read_b64_tr_b16 v[42:43], v226 offset:512\n\t"
      "ds_read_b64_tr_b16 v[44:45], v227\n\t"
      "ds_read_b64_tr_b16 v[46:47], v227 offset:512\n\t"
      "ds_read_b64_tr_b16 v[48:49], v224 offset:32768\n\t"
      "ds_read_b64_tr_b16 v[50:51], v224 offset:33280\n\t"
      "ds_read_b64_tr_b16 v[52:53], v225 offset:32768\n\t"
      "ds_read_b64_tr_b16 v[54:55], v225 offset:33280\n\t"
      "ds_read_b64_tr_b16 v[56:57], v226 offset:32768\n\t"
      "ds_read_b64_tr_b16 v[58:59], v226 offset:33280\n\t"
      "ds_read_b64_tr_b16 v[60:61], v227 offset:32768\n\t"
      "ds_read_b64_tr_b16 v[62:63], v227 offset:33280\n\t"
      "NTK0_%=:\n\t"
      "s_waitcnt lgkmcnt(0)\n\t"
      "s_barrier\n\t"
      "global_load_dwordx2 v[148:149], v231, s[46:47] sc1\n\t"
      "global_load_dwordx2 v[150:151], v231, s[46:47] offset:384 sc1\n\t"
      "s_waitcnt vmcnt(0)\n\t"
      "v_max3_u32 v232, v148, v149, v150\n\t"
      "v_max_u32_e32 v232, v232, v151\n\t"
      "v_cmp_eq_u32_e32 vcc, -1, v232\n\t"
      "s_cbranch_vccnz LSR_%=\n\t"
      "LSB_%=:\n\t"
      "v_cndmask_b32_e64 v150, v150, 0, s[70:71]\n\t"
      "v_cndmask_b32_e64 v151, v151, 0, s[70:71]\n\t"
      "v_pk_add_f32 v[144:145], v[148:149], v[150:151]\n\t"
      "s_nop 1\n\t"
      "v_permlane16_swap_b32_e32 v144, v145\n\t"
      "v_add_f32_e32 v144, v144, v145\n\t"
      "v_mov_b32_e32 v145, v144\n\t"
      "s_nop 1\n\t"
      "v_permlane32_swap_b32_e32 v144, v145\n\t"
      "v_add_f32_e32 v144, v144, v145\n\t"
      "v_mov_b32_e32 v145, v144\n\t"
      "s_nop 1\n\t"
      "v_permlane16_swap_b32_e32 v144, v145\n\t"
      "v_mul_f32_e32 v144, 0xbb124925, v144\n\t"
      "v_mul_f32_e32 v145, 0x3b124925, v145\n\t"
      "v_fma_f32 v145, -v144, v144, v145\n\t"
      "v_max_f32_e32 v145, 0, v145\n\t"
      "v_add_f32_e32 v146, 0x3727c5ac, v145\n\t"
      "v_fmac_f32_e32 v145, 0x3727c5ac, v146\n\t"
      "v_rsq_f32_e32 v145, v145\n\t"
      "s_nop 0\n\t"
      "v_mul_f32_e32 v144, v144, v145\n\t"
      "v_pk_fma_f32 v[128:129], v[128:129], v[144:145], v[144:145] op_sel:[0,1,0] op_sel_hi:[1,1,0]\n\t"
      "v_pk_fma_f32 v[130:131], v[130:131], v[144:145], v[144:145] op_sel:[0,1,0] op_sel_hi:[1,1,0]\n\t"
      "v_cvt_pk_bf16_f32 v152, v128, v129\n\t"
      "v_cvt_pk_bf16_f32 v153, v130, v131\n\t"
      "ds_write_b64 v233, v[152:153]\n\t"
      "v_pk_fma_f32 v[132:133], v[132:133], v[144:145], v[144:145] op_sel:[0,1,0] op_sel_hi:[1,1,0]\n\t"
      "v_pk_fma_f32 v[134:135], v[134:135], v[144:145], v[144:145] op_sel:[0,1,0] op_sel_hi:[1,1,0]\n\t"
      "v_cvt_pk_bf16_f32 v154, v132, v133\n\t"
      "v_cvt_pk_bf16_f32 v155, v134, v135\n\t"
      "ds_write_b64 v234, v[154:155]\n\t"
      "ds_read_b64_tr_b16 v[160:161], v237\n\t"
      "ds_read_b64_tr_b16 v[162:163], v238 offset:512\n\t"
      "v_pk_fma_f32 v[136:137], v[136:137], v[144:145], v[144:145] op_sel:[0,1,0] op_sel_hi:[1,1,0]\n\t"
      "v_pk_fma_f32 v[138:139], v[138:139], v[144:145], v[144:145] op_sel:[0,1,0] op_sel_hi:[1,1,0]\n\t"
      "v_cvt_pk_bf16_f32 v156, v136, v137\n\t"
      "v_cvt_pk_bf16_f32 v157, v138, v139\n\t"
      "ds_write_b64 v235, v[156:157]\n\t"
      "v_pk_fma_f32 v[140:141], v[140:141], v[144:145], v[144:145] op_sel:[0,1,0] op_sel_hi:[1,1,0]\n\t"
      "v_pk_fma_f32 v[142:143], v[142:143], v[144:145], v[144:145] op_sel:[0,1,0] op_sel_hi:[1,1,0]\n\t"
      "v_cvt_pk_bf16_f32 v158, v140, v141\n\t"
      "v_cvt_pk_bf16_f32 v159, v142, v143\n\t"
      "ds_write_b64 v236, v[158:159]\n\t"
      "ds_read_b64_tr_b16 v[164:165], v239\n\t"
      "ds_read_b64_tr_b16 v[166:167], v240 offset:512\n\t"
      "s_waitcnt lgkmcnt(4)\n\t"
      "v_min_u32_e32 v160, -2, v160\n\t"
      "v_min_u32_e32 v161, -2, v161\n\t"
      "v_min_u32_e32 v162, -2, v162\n\t"
      "v_min_u32_e32 v163, -2, v163\n\t"
      "global_store_dwordx4 v229, v[160:163], s[48:49] sc1\n\t"
      "s_waitcnt lgkmcnt(0)\n\t"
      "v_min_u32_e32 v164, -2, v164\n\t"
      "v_min_u32_e32 v165, -2, v165\n\t"
      "v_min_u32_e32 v166, -2, v166\n\t"
      "v_min_u32_e32 v167, -2, v167\n\t"
      "global_store_dwordx4 v230, v[164:167], s[48:49] sc1\n\t"
      "s_barrier\n\t"
      "s_bitcmp0_b32 s27, 0\n\t"
      "s_cbranch_scc1 G4C_%=\n\t"
      "s_mul_i32 s34, s32, 196608\n\t"
      "s_mul_i32 s35, s24, 8192\n\t"
      "s_add_u32 s34, s34, s35\n\t"
      "s_add_u32 s34, s34, 1048576\n\t"
      "s_add_u32 s64, s48, s34\n\t"
      "s_addc_u32 s65, s49, 0\n\t"
      "v_add_u32_e32 v250, 4096, v244\n\t"
      "global_load_dwordx4 v[128:131], v250, s[64:65] offset:-4096 sc1\n\t"
      "global_load_dwordx4 v[132:135], v250, s[64:65] offset:-3072 sc1\n\t"
      "global_load_dwordx4 v[136:139], v250, s[64:65] offset:-2048 sc1\n\t"
      "global_load_dwordx4 v[140:143], v250, s[64:65] offset:-1024 sc1\n\t"
      "global_load_dwordx4 v[144:147], v250, s[64:65] sc1\n\t"
      "global_load_dwordx4 v[148:151], v250, s[64:65] offset:1024 sc1\n\t"
      "global_load_dwordx4 v[152:155], v250, s[64:65] offset:2048 sc1\n\t"
      "global_load_dwordx4 v[156:159], v250, s[64:65] offset:3072 sc1\n\t"
      "global_store_dwordx4 v241, v[208:211], s[20:21] offset:-2048 sc1\n\t"
      "global_store_dwordx4 v241, v[212:215], s[20:21] offset:2048 sc1\n\t"
      "s_setprio 0\n\t"
      "ds_read_b64_tr_b16 v[64:65], v224 offset:16384\n\t"
      "ds_read_b64_tr_b16 v[66:67], v224 offset:16896\n\t"
      "ds_read_b64_tr_b16 v[68:69], v225 offset:16384\n\t"
      "ds_read_b64_tr_b16 v[70:71], v225 offset:16896\n\t"
      "ds_read_b64_tr_b16 v[72:73], v226 offset:16384\n\t"
      "ds_read_b64_tr_b16 v[74:75], v226 offset:16896\n\t"
      "ds_read_b64_tr_b16 v[76:77], v227 offset:16384\n\t"
      "ds_read_b64_tr_b16 v[78:79], v227 offset:16896\n\t"
      "ds_read_b64_tr_b16 v[80:81], v224 offset:49152\n\t"
      "ds_read_b64_tr_b16 v[82:83], v224 offset:49664\n\t"
      "ds_read_b64_tr_b16 v[84:85], v225 offset:49152\n\t"
      "ds_read_b64_tr_b16 v[86:87], v225 offset:49664\n\t"
      "ds_read_b64_tr_b16 v[88:89], v226 offset:49152\n\t"
      "ds_read_b64_tr_b16 v[90:91], v226 offset:49664\n\t"
      "ds_read_b64_tr_b16 v[92:93], v227 offset:49152\n\t"
      "ds_read_b64_tr_b16 v[94:95], v227 offset:49664\n\t"
      "ds_read_b64_tr_b16 v[96:97], v224\n\t"
      "ds_read_b64_tr_b16 v[98:99], v224 offset:512\n\t"
      "ds_read_b64_tr_b16 v[100:101], v225\n\t"
      "ds_read_b64_tr_b16 v[102:103], v225 offset:512\n\t"
      "ds_read_b64_tr_b16 v[104:105], v226\n\t"
      "ds_read_b64_tr_b16 v[106:107], v226 offset:512\n\t"
      "ds_read_b64_tr_b16 v[108:109], v227\n\t"
      "ds_read_b64_tr_b16 v[110:111], v227 offset:512\n\t"
      "ds_read_b64_tr_b16 v[112:113], v224 offset:32768\n\t"
      "ds_read_b64_tr_b16 v[114:115], v224 offset:33280\n\t"
      "ds_read_b64_tr_b16 v[116:117], v225 offset:32768\n\t"
      "ds_read_b64_tr_b16 v[118:119], v225 offset:33280\n\t"
      "ds_read_b64_tr_b16 v[120:121], v226 offset:32768\n\t"
      "ds_read_b64_tr_b16 v[122:123], v226 offset:33280\n\t"
      "ds_read_b64_tr_b16 v[124:125], v227 offset:32768\n\t"
      "ds_read_b64_tr_b16 v[126:127], v227 offset:33280\n\t"
      "s_waitcnt vmcnt(2) lgkmcnt(0)\n\t"
      "GST_%=:\n\t"
      "s_add_u32 s66, s64, 65536\n\t"
      "s_addc_u32 s67, s65, 0\n\t"
      "s_add_u32 s68, s66, 65536\n\t"
      "s_addc_u32 s69, s67, 0\n\t"
      "s_lshl_b32 s34, s32, 18\n\t"
      "s_lshl_b32 s35, s24, 13\n\t"
      "s_add_u32 s34, s34, s35\n\t"
      "s_add_u32 s70, s48, s34\n\t"
      "s_addc_u32 s71, s49, 0\n\t"
      "s_add_u32 s72, s70, 65536\n\t"
      "s_addc_u32 s73, s71, 0\n\t"
      "s_add_u32 s74, s72, 65536\n\t"
      "s_addc_u32 s75, s73, 0\n\t"
      "s_add_u32 s76, s74, 65536\n\t"
      "s_addc_u32 s77, s75, 0\n\t"
      "s_mul_i32 s34, s30, 28\n\t"
      "s_add_u32 s34, s34, s29\n\t"
      "s_lshl_b32 s34, s34, 11\n\t"
      "s_add_u32 s34, s34, 10223616\n\t"
      "s_add_u32 s62, s20, s34\n\t"
      "s_addc_u32 s63, s21, 0\n\t"
      "v_lshlrev_b32_e32 v248, 16, v249\n\t"
      "v_and_b32_e32 v249, 0xffff0000, v249\n\t"
      "v_mov_b32_e32 v252, 0xbdd2d3e8\n\t"
      "v_xor_b32_e32 v253, 128, v245\n\t"
      "v_max3_u32 v251, v128, v129, v130\n\t"
      "v_max3_u32 v251, v251, v131, v132\n\t"
      "v_max3_u32 v251, v251, v133, v134\n\t"
      "v_max3_u32 v251, v251, v135, v136\n\t"
      "v_max3_u32 v251, v251, v137, v138\n\t"
      "v_max3_u32 v251, v251, v139, v140\n\t"
      "v_max3_u32 v251, v251, v141, v142\n\t"
      "v_max3_u32 v251, v251, v143, v144\n\t"
      "v_max3_u32 v251, v251, v145, v146\n\t"
      "v_max3_u32 v251, v251, v147, v148\n\t"
      "v_max3_u32 v251, v251, v149, v150\n\t"
      "v_max3_u32 v251, v251, v151, v152\n\t"
      "v_max3_u32 v251, v251, v153, v154\n\t"
      "v_max3_u32 v251, v251, v155, v156\n\t"
      "v_max3_u32 v251, v251, v157, v158\n\t"
      "v_max_u32_e32 v251, v251, v159\n\t"
      "v_cmp_eq_u32_e32 vcc, -1, v251\n\t"
      "s_cbranch_vccnz LA30R_%=\n\t"
      "LA30B_%=:\n\t"
      "v_mfma_f32_16x16x32_bf16 v[208:211], v[0:3], v[128:131], 0\n\t"
      "global_load_dwordx4 v[160:163], v250, s[66:67] offset:-4096 sc1\n\t"
      "v_mfma_f32_16x16x32_bf16 v[208:211], v[16:19], v[132:135], v[208:211]\n\t"
      "v_mfma_f32_16x16x32_bf16 v[208:211], v[32:35], v[136:139], v[208:211]\n\t"
      "global_load_dwordx4 v[164:167], v250, s[66:67] offset:-3072 sc1\n\t"
      "v_mfma_f32_16x16x32_bf16 v[208:211], v[48:51], v[140:143], v[208:211]\n\t"
      "v_mfma_f32_16x16x32_bf16 v[208:211], v[64:67], v[144:147], v[208:211]\n\t"
      "global_load_dwordx4 v[168:171], v250, s[66:67] offset:-2048 sc1\n\t"
      "v_mfma_f32_16x16x32_bf16 v[208:211], v[80:83], v[148:151], v[208:211]\n\t"
      "v_mfma_f32_16x16x32_bf16 v[208:211], v[96:99], v[152:155], v[208:211]\n\t"
      "s_lshl_b32 s34, s2, 7\n\t"
      "s_add_u32 s34, s34, 65536\n\t"
      "v_mov_b32_e32 v230, s34\n\t"
      "s_cmp_lg_u32 s24, 3\n\t"
      "s_cbranch_scc1 SK23_%=\n\t"
      "global_load_dwordx2 v[228:229], v230, s[60:61] sc1\n\t"
      "SK23_%=:\n\t"
      "global_load_dwordx4 v[172:175], v250, s[66:67] offset:-1024 sc1\n\t"
      "v_mfma_f32_16x16x32_bf16 v[208:211], v[112:115], v[156:159], v[208:211]\n\t"
      "v_mfma_f32_16x16x32_bf16 v[212:215], v[4:7], v[128:131], 0\n\t"
      "global_load_dwordx4 v[176:179], v250, s[66:67] sc1\n\t"
      "v_mfma_f32_16x16x32_bf16 v[212:215], v[20:23], v[132:135], v[212:215]\n\t"
      "v_mfma_f32_16x16x32_bf16 v[212:215], v[36:39], v[136:139], v[212:215]\n\t"
      "global_load_dwordx4 v[180:183], v250, s[66:67] offset:1024 sc1\n\t"
      "v_mfma_f32_16x16x32_bf16 v[212:215], v[52:55], v[140:143], v[212:215]\n\t"
      "v_mfma_f32_16x16x32_bf16 v[212:215], v[68:71], v[144:147], v[212:215]\n\t"
      "global_load_dwordx4 v[184:187], v250, s[66:67] offset:2048 sc1\n\t"
      "v_mfma_f32_16x16x32_bf16 v[212:215], v[84:87], v[148:151], v[212:215]\n\t"
      "v_mfma_f32_16x16x32_bf16 v[212:215], v[100:103], v[152:155], v[212:215]\n\t"
      "ds_write_b128 v245, v[208:211]\n\t"
      "global_load_dwordx4 v[188:191], v250, s[66:67] offset:3072 sc1\n\t"
      "v_mfma_f32_16x16x32_bf16 v[212:215], v[116:119], v[156:159], v[212:215]\n\t"
      "v_mfma_f32_16x16x32_bf16 v[216:219], v[8:11], v[128:131], 0\n\t"
      "global_load_dwordx4 v[192:195], v250, s[68:69] offset:-4096 sc1\n\t"
      "v_mfma_f32_16x16x32_bf16 v[216:219], v[24:27], v[132:135], v[216:219]\n\t"
      "v_mfma_f32_16x16x32_bf16 v[216:219], v[40:43], v[136:139], v[216:219]\n\t"
      "global_load_dwordx4 v[196:199], v250, s[68:69] offset:-3072 sc1\n\t"
      "v_mfma_f32_16x16x32_bf16 v[216:219], v[56:59], v[140:143], v[216:219]\n\t"
      "v_mfma_f32_16x16x32_bf16 v[216:219], v[72:75], v[144:147], v[216:219]\n\t"
      "global_load_dwordx4 v[200:203], v250, s[68:69] offset:-2048 sc1\n\t"
      "v_mfma_f32_16x16x32_bf16 v[216:219], v[88:91], v[148:151], v[216:219]\n\t"
      "v_mfma_f32_16x16x32_bf16 v[216:219], v[104:107], v[152:155], v[216:219]\n\t"
      "ds_write_b128 v253, v[212:215] offset:1024\n\t"
      "global_load_dwordx4 v[204:207], v250, s[68:69] offset:-1024 sc1\n\t"
      "v_mfma_f32_16x16x32_bf16 v[216:219], v[120:123], v[156:159], v[216:219]\n\t"
      "v_mfma_f32_16x16x32_bf16 v[220:223], v[12:15], v[128:131], 0\n\t"
      "global_load_dwordx4 v[128:131], v250, s[68:69] sc1\n\t"
      "v_mfma_f32_16x16x32_bf16 v[220:223], v[28:31], v[132:135], v[220:223]\n\t"
      "v_mfma_f32_16x16x32_bf16 v[220:223], v[44:47], v[136:139], v[220:223]\n\t"
      "v_mfma_f32_16x16x32_bf16 v[220:223], v[60:63], v[140:143], v[220:223]\n\t"
      "v_mfma_f32_16x16x32_bf16 v[220:223], v[76:79], v[144:147], v[220:223]\n\t"
      "global_load_dwordx4 v[132:135], v250, s[68:69] offset:1024 sc1\n\t"
      "v_mfma_f32_16x16x32_bf16 v[220:223], v[92:95], v[148:151], v[220:223]\n\t"
      "v_mfma_f32_16x16x32_bf16 v[220:223], v[108:111], v[152:155], v[220:223]\n\t"
      "ds_write_b128 v245, v[216:219] offset:2048\n\t"
      "v_mfma_f32_16x16x32_bf16 v[220:223], v[124:127], v[156:159], v[220:223]\n\t"
      "s_waitcnt vmcnt(10)\n\t"
      "v_mfma_f32_16x16x32_bf16 v[224:227], v[0:3], v[160:163], 0\n\t"
      "global_load_dwordx4 v[136:139], v250, s[68:69] offset:2048 sc1\n\t"
      "v_mfma_f32_16x16x32_bf16 v[224:227], v[16:19], v[164:167], v[224:227]\n\t"
      "s_nop 3\n\t"
      "ds_write_b128 v253, v[220:223] offset:3072\n\t"
      "v_mfma_f32_16x16x32_bf16 v[224:227], v[32:35], v[168:171], v[224:227]\n\t"
      "v_mfma_f32_16x16x32_bf16 v[224:227], v[48:51], v[172:175], v[224:227]\n\t"
      "s_waitcnt vmcnt(7)\n\t"
      "v_mfma_f32_16x16x32_bf16 v[224:227], v[64:67], v[176:179], v[224:227]\n\t"
      "global_load_dwordx4 v[140:143], v250, s[68:69] offset:3072 sc1\n\t"
      "v_mfma_f32_16x16x32_bf16 v[224:227], v[80:83], v[180:183], v[224:227]\n\t"
      "s_cmp_lg_u32 s24, 3\n\t"
      "s_cbranch_scc1 LF6B_%=\n\t"
      "v_cmp_ne_u64_e32 vcc, s[22:23], v[228:229]\n\t"
      "s_cbranch_vccnz LF6R_%=\n\t"
      "LF6B_%=:\n\t"
      "s_waitcnt lgkmcnt(0)\n\t"
      "s_barrier\n\t"
      "s_cmp_lg_u32 s24, 3\n\t"
      "s_cbranch_scc1 HF3_%=\n\t"
      "s_lshl3_add_u32 s34, s25, s26\n\t"
      "s_lshl_b32 s34, s34, 7\n\t"
      "s_add_u32 s34, s34, 98304\n\t"
      "v_mov_b32_e32 v238, s34\n\t"
      "v_mov_b32_e32 v236, s22\n\t"
      "v_mov_b32_e32 v237, s23\n\t"
      "s_mov_b64 exec, 1\n\t"
      "global_store_dwordx2 v238, v[236:237], s[60:61] sc1\n\t"
      "s_mov_b64 exec, -1\n\t"
      "HF3_%=:\n\t"
      "ds_read_b64 v[228:229], v246\n\t"
      "ds_read_b64 v[230:231], v246 offset:4096\n\t"
      "v_mfma_f32_16x16x32_bf16 v[224:227], v[96:99], v[184:187], v[224:227]\n\t"
      "ds_read_b64 v[232:233], v246 offset:8192\n\t"
      "ds_read_b64 v[234:235], v246 offset:12288\n\t"
      "v_mfma_f32_16x16x32_bf16 v[224:227], v[112:115], v[188:191], v[224:227]\n\t"
      "ds_read_b64 v[236:237], v246 offset:16384\n\t"
      "ds_read_b64 v[238:239], v246 offset:20480\n\t"
      "v_mfma_f32_16x16x32_bf16 v[208:211], v[4:7], v[160:163], 0\n\t"
      "ds_read_b64 v[240:241], v246 offset:24576\n\t"
      "ds_read_b64 v[242:243], v246 offset:28672\n\t"
      "global_load_dwordx4 v[144:147], v250, s[70:71] offset:-4096 sc1\n\t"
      "v_mfma_f32_16x16x32_bf16 v[208:211], v[20:23], v[164:167], v[208:211]\n\t"
      "v_mfma_f32_16x16x32_bf16 v[208:211], v[36:39], v[168:171], v[208:211]\n\t"
      "s_waitcnt lgkmcnt(4)\n\t"
      "v_add_f32_e32 v228, v228, v230\n\t"
      "v_add_f32_e32 v232, v232, v234\n\t"
      "v_cmp_u_f32_e32 vcc, v224, v224\n\t"
      "s_cbranch_vccnz LA31R_%=\n\t"
      "LA31B_%=:\n\t"
      "v_mfma_f32_16x16x32_bf16 v[208:211], v[52:55], v[172:175], v[208:211]\n\t"
      "v_add_f32_e32 v229, v229, v231\n\t"
      "v_add_f32_e32 v233, v233, v235\n\t"
      "v_mfma_f32_16x16x32_bf16 v[208:211], v[68:71], v[176:179], v[208:211]\n\t"
      "s_waitcnt lgkmcnt(0)\n\t"
      "v_add_f32_e32 v236, v236, v238\n\t"
      "v_add_f32_e32 v240, v240, v242\n\t"
      "global_load_dwordx4 v[148:151], v250, s[70:71] offset:-3072 sc1\n\t"
      "v_mfma_f32_16x16x32_bf16 v[208:211], v[84:87], v[180:183], v[208:211]\n\t"
      "v_add_f32_e32 v237, v237, v239\n\t"
      "v_add_f32_e32 v241, v241, v243\n\t"
      "v_mfma_f32_16x16x32_bf16 v[208:211], v[100:103], v[184:187], v[208:211]\n\t"
      "v_add_f32_e32 v228, v228, v232\n\t"
      "v_add_f32_e32 v236, v236, v240\n\t"
      "v_add_f32_e32 v229, v229, v233\n\t"
      "ds_write_b128 v245, v[224:227] offset:32768\n\t"
      "v_mfma_f32_16x16x32_bf16 v[208:211], v[116:119], v[188:191], v[208:211]\n\t"
      "v_add_f32_e32 v237, v237, v241\n\t"
      "v_add_f32_e32 v228, v228, v248\n\t"
      "v_mfma_f32_16x16x32_bf16 v[212:215], v[8:11], v[160:163], 0\n\t"
      "v_add_f32_e32 v229, v229, v249\n\t"
      "v_add_f32_e32 v228, v228, v236\n\t"
      "v_add_f32_e32 v229, v229, v237\n\t"
      "global_load_dwordx4 v[152:155], v250, s[70:71] offset:-2048 sc1\n\t"
      "v_mfma_f32_16x16x32_bf16 v[212:215], v[24:27], v[164:167], v[212:215]\n\t"
      "v_mul_f32_e32 v230, v228, v228\n\t"
      "v_mul_f32_e32 v231, v229, v229\n\t"
      "v_mfma_f32_16x16x32_bf16 v[212:215], v[40:43], v[168:171], v[212:215]\n\t"
      "v_fmaak_f32 v230, v252, v230, 0xc0135761\n\t"
      "v_fmaak_f32 v231, v252, v231, 0xc0135761\n\t"
      "v_mul_f32_e32 v230, v228, v230\n\t"
      "v_mfma_f32_16x16x32_bf16 v[212:215], v[56:59], v[172:175], v[212:215]\n\t"
      "v_mul_f32_e32 v231, v229, v231\n\t"
      "v_exp_f32_e32 v230, v230\n\t"
      "v_mfma_f32_16x16x32_bf16 v[212:215], v[72:75], v[176:179], v[212:215]\n\t"
      "v_exp_f32_e32 v231, v231\n\t"
      "v_add_f32_e32 v230, 1.0, v230\n\t"
      "v_add_f32_e32 v231, 1.0, v231\n\t"
      "global_load_dwordx4 v[156:159], v250, s[70:71] offset:-1024 sc1\n\t"
      "v_mfma_f32_16x16x32_bf16 v[212:215], v[88:91], v[180:183], v[212:215]\n\t"
      "v_rcp_f32_e32 v230, v230\n\t"
      "v_rcp_f32_e32 v231, v231\n\t"
      "v_mfma_f32_16x16x32_bf16 v[212:215], v[104:107], v[184:187], v[212:215]\n\t"
      "v_mul_f32_e32 v228, v228, v230\n\t"
      "v_mul_f32_e32 v229, v229, v231\n\t"
      "v_cvt_pk_bf16_f32 v232, v228, v229\n\t"
      "ds_write_b128 v253, v[208:211] offset:33792\n\t"
      "v_mfma_f32_16x16x32_bf16 v[212:215], v[120:123], v[188:191], v[212:215]\n\t"
      "v_min_u32_e32 v254, -2, v232\n\t"
      "global_store_dword v247, v254, s[62:63] sc1\n\t"
      "s_add_u32 s62, s62, 57344\n\t"
      "s_addc_u32 s63, s63, 0\n\t"
      "v_mfma_f32_16x16x32_bf16 v[216:219], v[12:15], v[160:163], 0\n\t"
      "global_load_dwordx4 v[160:163], v250, s[70:71] sc1\n\t"
      "v_mfma_f32_16x16x32_bf16 v[216:219], v[28:31], v[164:167], v[216:219]\n\t"
      "v_mfma_f32_16x16x32_bf16 v[216:219], v[44:47], v[168:171], v[216:219]\n\t"
      "v_mfma_f32_16x16x32_bf16 v[216:219], v[60:63], v[172:175], v[216:219]\n\t"
      "v_mfma_f32_16x16x32_bf16 v[216:219], v[76:79], v[176:179], v[216:219]\n\t"
      "global_load_dwordx4 v[164:167], v250, s[70:71] offset:1024 sc1\n\t"
      "v_mfma_f32_16x16x32_bf16 v[216:219], v[92:95], v[180:183], v[216:219]\n\t"
      "v_mfma_f32_16x16x32_bf16 v[216:219], v[108:111], v[184:187], v[216:219]\n\t"
      "ds_write_b128 v245, v[212:215] offset:34816\n\t"
      "v_mfma_f32_16x16x32_bf16 v[216:219], v[124:127], v[188:191], v[216:219]\n\t"
      "s_waitcnt vmcnt(11)\n\t"
      "v_mfma_f32_16x16x32_bf16 v[220:223], v[0:3], v[192:195], 0\n\t"
      "global_load_dwordx4 v[168:171], v250, s[70:71] offset:2048 sc1\n\t"
      "v_mfma_f32_16x16x32_bf16 v[220:223], v[16:19], v[196:199], v[220:223]\n\t"
      "s_nop 3\n\t"
      "ds_write_b128 v253, v[216:219] offset:35840\n\t"
      "v_mfma_f32_16x16x32_bf16 v[220:223], v[32:35], v[200:203], v[220:223]\n\t"
      "v_mfma_f32_16x16x32_bf16 v[220:223], v[48:51], v[204:207], v[220:223]\n\t"
      "s_waitcnt vmcnt(8)\n\t"
      "v_mfma_f32_16x16x32_bf16 v[220:223], v[64:67], v[128:131], v[220:223]\n\t"
      "global_load_dwordx4 v[172:175], v250, s[70:71] offset:3072 sc1\n\t"
      "v_mfma_f32_16x16x32_bf16 v[220:223], v[80:83], v[132:135], v[220:223]\n\t"
      "s_waitcnt lgkmcnt(0)\n\t"
      "s_barrier\n\t"
      "ds_read_b64 v[228:229], v246 offset:32768\n\t"
      "ds_read_b64 v[230:231], v246 offset:36864\n\t"
      "v_mfma_f32_16x16x32_bf16 v[220:223], v[96:99], v[136:139], v[220:223]\n\t"
      "ds_read_b64 v[232:233], v246 offset:40960\n\t"
      "ds_read_b64 v[234:235], v246 offset:45056\n\t"
      "v_mfma_f32_16x16x32_bf16 v[220:223], v[112:115], v[140:143], v[220:223]\n\t"
      "ds_read_b64 v[236:237], v246 offset:49152\n\t"
      "ds_read_b64 v[238:239], v246 offset:53248\n\t"
      "v_mfma_f32_16x16x32_bf16 v[224:227], v[4:7], v[192:195], 0\n\t"
      "ds_read_b64 v[240:241], v246 offset:57344\n\t"
      "ds_read_b64 v[242:243], v246 offset:61440\n\t"
      "global_load_dwordx4 v[176:179], v250, s[72:73] offset:-4096 sc1\n\t"
      "v_mfma_f32_16x16x32_bf16 v[224:227], v[20:23], v[196:199], v[224:227]\n\t"
      "v_mfma_f32_16x16x32_bf16 v[224:227], v[36:39], v[200:203], v[224:227]\n\t"
      "s_waitcnt lgkmcnt(4)\n\t"
      "v_add_f32_e32 v228, v228, v230\n\t"
      "v_add_f32_e32 v232, v232, v234\n\t"
      "v_cmp_u_f32_e32 vcc, v220, v220\n\t"
      "s_cbranch_vccnz LA32R_%=\n\t"
      "LA32B_%=:\n\t"
      "v_mfma_f32_16x16x32_bf16 v[224:227], v[52:55], v[204:207], v[224:227]\n\t"
      "v_add_f32_e32 v229, v229, v231\n\t"
      "v_add_f32_e32 v233, v233, v235\n\t"
      "v_mfma_f32_16x16x32_bf16 v[224:227], v[68:71], v[128:131], v[224:227]\n\t"
      "s_waitcnt lgkmcnt(0)\n\t"
      "v_add_f32_e32 v236, v236, v238\n\t"
      "v_add_f32_e32 v240, v240, v242\n\t"
      "global_load_dwordx4 v[180:183], v250, s[72:73] offset:-3072 sc1\n\t"
      "v_mfma_f32_16x16x32_bf16 v[224:227], v[84:87], v[132:135], v[224:227]\n\t"
      "v_add_f32_e32 v237, v237, v239\n\t"
      "v_add_f32_e32 v241, v241, v243\n\t"
      "v_mfma_f32_16x16x32_bf16 v[224:227], v[100:103], v[136:139], v[224:227]\n\t"
      "v_add_f32_e32 v228, v228, v232\n\t"
      "v_add_f32_e32 v236, v236, v240\n\t"
      "v_add_f32_e32 v229, v229, v233\n\t"
      "ds_write_b128 v245, v[220:223]\n\t"
      "v_mfma_f32_16x16x32_bf16 v[224:227], v[116:119], v[140:143], v[224:227]\n\t"
      "v_add_f32_e32 v237, v237, v241\n\t"
      "v_add_f32_e32 v228, v228, v248\n\t"
      "v_mfma_f32_16x16x32_bf16 v[208:211], v[8:11], v[192:195], 0\n\t"
      "v_add_f32_e32 v229, v229, v249\n\t"
      "v_add_f32_e32 v228, v228, v236\n\t"
      "v_add_f32_e32 v229, v229, v237\n\t"
      "global_load_dwordx4 v[184:187], v250, s[72:73] offset:-2048 sc1\n\t"
      "v_mfma_f32_16x16x32_bf16 v[208:211], v[24:27], v[196:199], v[208:211]\n\t"
      "v_mul_f32_e32 v230, v228, v228\n\t"
      "v_mul_f32_e32 v231, v229, v229\n\t"
      "v_mfma_f32_16x16x32_bf16 v[208:211], v[40:43], v[200:203], v[208:211]\n\t"
      "v_fmaak_f32 v230, v252, v230, 0xc0135761\n\t"
      "v_fmaak_f32 v231, v252, v231, 0xc0135761\n\t"
      "v_mul_f32_e32 v230, v228, v230\n\t"
      "v_mfma_f32_16x16x32_bf16 v[208:211], v[56:59], v[204:207], v[208:211]\n\t"
      "v_mul_f32_e32 v231, v229, v231\n\t"
      "v_exp_f32_e32 v230, v230\n\t"
      "v_mfma_f32_16x16x32_bf16 v[208:211], v[72:75], v[128:131], v[208:211]\n\t"
      "v_exp_f32_e32 v231, v231\n\t"
      "v_add_f32_e32 v230, 1.0, v230\n\t"
      "v_add_f32_e32 v231, 1.0, v231\n\t"
      "global_load_dwordx4 v[188:191], v250, s[72:73] offset:-1024 sc1\n\t"
      "v_mfma_f32_16x16x32_bf16 v[208:211], v[88:91], v[132:135], v[208:211]\n\t"
      "v_rcp_f32_e32 v230, v230\n\t"
      "v_rcp_f32_e32 v231, v231\n\t"
      "v_mfma_f32_16x16x32_bf16 v[208:211], v[104:107], v[136:139], v[208:211]\n\t"
      "v_mul_f32_e32 v228, v228, v230\n\t"
      "v_mul_f32_e32 v229, v229, v231\n\t"
      "v_cvt_pk_bf16_f32 v232, v228, v229\n\t"
      "ds_write_b128 v253, v[224:227] offset:1024\n\t"
      "v_mfma_f32_16x16x32_bf16 v[208:211], v[120:123], v[140:143], v[208:211]\n\t"
      "v_min_u32_e32 v254, -2, v232\n\t"
      "global_store_dword v247, v254, s[62:63] sc1\n\t"
      "s_add_u32 s62, s62, 57344\n\t"
      "s_addc_u32 s63, s63, 0\n\t"
      "v_mfma_f32_16x16x32_bf16 v[212:215], v[12:15], v[192:195], 0\n\t"
      "global_load_dwordx4 v[192:195], v250, s[72:73] sc1\n\t"
      "v_mfma_f32_16x16x32_bf16 v[212:215], v[28:31], v[196:199], v[212:215]\n\t"
      "v_mfma_f32_16x16x32_bf16 v[212:215], v[44:47], v[200:203], v[212:215]\n\t"
      "v_mfma_f32_16x16x32_bf16 v[212:215], v[60:63], v[204:207], v[212:215]\n\t"
      "v_mfma_f32_16x16x32_bf16 v[212:215], v[76:79], v[128:131], v[212:215]\n\t"
      "global_load_dwordx4 v[196:199], v250, s[72:73] offset:1024 sc1\n\t"
      "v_mfma_f32_16x16x32_bf16 v[212:215], v[92:95], v[132:135], v[212:215]\n\t"
      "v_mfma_f32_16x16x32_bf16 v[212:215], v[108:111], v[136:139], v[212:215]\n\t"
      "ds_write_b128 v245, v[208:211] offset:2048\n\t"
      "v_mfma_f32_16x16x32_bf16 v[212:215], v[124:127], v[140:143], v[212:215]\n\t"
      "s_waitcnt vmcnt(12)\n\t"
      "v_mfma_f32_16x16x32_bf16 v[216:219], v[0:3], v[144:147], 0\n\t"
      "global_load_dwordx4 v[200:203], v250, s[72:73] offset:2048 sc1\n\t"
      "v_mfma_f32_16x16x32_bf16 v[216:219], v[16:19], v[148:151], v[216:219]\n\t"
      "s_nop 3\n\t"
      "ds_write_b128 v253, v[212:215] offset:3072\n\t"
      "v_mfma_f32_16x16x32_bf16 v[216:219], v[32:35], v[152:155], v[216:219]\n\t"
      "v_mfma_f32_16x16x32_bf16 v[216:219], v[48:51], v[156:159], v[216:219]\n\t"
      "s_waitcnt vmcnt(8)\n\t"
      "v_mfma_f32_16x16x32_bf16 v[216:219], v[64:67], v[160:163], v[216:219]\n\t"
      "global_load_dwordx4 v[204:207], v250, s[72:73] offset:3072 sc1\n\t"
      "v_mfma_f32_16x16x32_bf16 v[216:219], v[80:83], v[164:167], v[216:219]\n\t"
      "s_waitcnt lgkmcnt(0)\n\t"
      "s_barrier\n\t"
      "ds_read_b64 v[228:229], v246\n\t"
      "ds_read_b64 v[230:231], v246 offset:4096\n\t"
      "v_mfma_f32_16x16x32_bf16 v[216:219], v[96:99], v[168:171], v[216:219]\n\t"
      "ds_read_b64 v[232:233], v246 offset:8192\n\t"
      "ds_read_b64 v[234:235], v246 offset:12288\n\t"
      "v_mfma_f32_16x16x32_bf16 v[216:219], v[112:115], v[172:175], v[216:219]\n\t"
      "ds_read_b64 v[236:237], v246 offset:16384\n\t"
      "ds_read_b64 v[238:239], v246 offset:20480\n\t"
      "v_mfma_f32_16x16x32_bf16 v[220:223], v[4:7], v[144:147], 0\n\t"
      "ds_read_b64 v[240:241], v246 offset:24576\n\t"
      "ds_read_b64 v[242:243], v246 offset:28672\n\t"
      "global_load_dwordx4 v[128:131], v250, s[74:75] offset:-4096 sc1\n\t"
      "v_mfma_f32_16x16x32_bf16 v[220:223], v[20:23], v[148:151], v[220:223]\n\t"
      "v_mfma_f32_16x16x32_bf16 v[220:223], v[36:39], v[152:155], v[220:223]\n\t"
      "s_waitcnt lgkmcnt(4)\n\t"
      "v_add_f32_e32 v228, v228, v230\n\t"
      "v_add_f32_e32 v232, v232, v234\n\t"
      "v_cmp_u_f32_e32 vcc, v216, v216\n\t"
      "s_cbranch_vccnz LA33R_%=\n\t"
      "LA33B_%=:\n\t"
      "v_mfma_f32_16x16x32_bf16 v[220:223], v[52:55], v[156:159], v[220:223]\n\t"
      "v_add_f32_e32 v229, v229, v231\n\t"
      "v_add_f32_e32 v233, v233, v235\n\t"
      "v_mfma_f32_16x16x32_bf16 v[220:223], v[68:71], v[160:163], v[220:223]\n\t"
      "s_waitcnt lgkmcnt(0)\n\t"
      "v_add_f32_e32 v236, v236, v238\n\t"
      "v_add_f32_e32 v240, v240, v242\n\t"
      "global_load_dwordx4 v[132:135], v250, s[74:75] offset:-3072 sc1\n\t"
      "v_mfma_f32_16x16x32_bf16 v[220:223], v[84:87], v[164:167], v[220:223]\n\t"
      "v_add_f32_e32 v237, v237, v239\n\t"
      "v_add_f32_e32 v241, v241, v243\n\t"
      "v_mfma_f32_16x16x32_bf16 v[220:223], v[100:103], v[168:171], v[220:223]\n\t"
      "v_add_f32_e32 v228, v228, v232\n\t"
      "v_add_f32_e32 v236, v236, v240\n\t"
      "v_add_f32_e32 v229, v229, v233\n\t"
      "ds_write_b128 v245, v[216:219] offset:32768\n\t"
      "v_mfma_f32_16x16x32_bf16 v[220:223], v[116:119], v[172:175], v[220:223]\n\t"
      "v_add_f32_e32 v237, v237, v241\n\t"
      "v_add_f32_e32 v228, v228, v248\n\t"
      "v_mfma_f32_16x16x32_bf16 v[224:227], v[8:11], v[144:147], 0\n\t"
      "v_add_f32_e32 v229, v229, v249\n\t"
      "v_add_f32_e32 v228, v228, v236\n\t"
      "v_add_f32_e32 v229, v229, v237\n\t"
      "global_load_dwordx4 v[136:139], v250, s[74:75] offset:-2048 sc1\n\t"
      "v_mfma_f32_16x16x32_bf16 v[224:227], v[24:27], v[148:151], v[224:227]\n\t"
      "v_mul_f32_e32 v230, v228, v228\n\t"
      "v_mul_f32_e32 v231, v229, v229\n\t"
      "v_mfma_f32_16x16x32_bf16 v[224:227], v[40:43], v[152:155], v[224:227]\n\t"
      "v_fmaak_f32 v230, v252, v230, 0xc0135761\n\t"
      "v_fmaak_f32 v231, v252, v231, 0xc0135761\n\t"
      "v_mul_f32_e32 v230, v228, v230\n\t"
      "v_mfma_f32_16x16x32_bf16 v[224:227], v[56:59], v[156:159], v[224:227]\n\t"
      "v_mul_f32_e32 v231, v229, v231\n\t"
      "v_exp_f32_e32 v230, v230\n\t"
      "v_mfma_f32_16x16x32_bf16 v[224:227], v[72:75], v[160:163], v[224:227]\n\t"
      "v_exp_f32_e32 v231, v231\n\t"
      "v_add_f32_e32 v230, 1.0, v230\n\t"
      "v_add_f32_e32 v231, 1.0, v231\n\t"
      "global_load_dwordx4 v[140:143], v250, s[74:75] offset:-1024 sc1\n\t"
      "v_mfma_f32_16x16x32_bf16 v[224:227], v[88:91], v[164:167], v[224:227]\n\t"
      "v_rcp_f32_e32 v230, v230\n\t"
      "v_rcp_f32_e32 v231, v231\n\t"
      "v_mfma_f32_16x16x32_bf16 v[224:227], v[104:107], v[168:171], v[224:227]\n\t"
      "v_mul_f32_e32 v228, v228, v230\n\t"
      "v_mul_f32_e32 v229, v229, v231\n\t"
      "v_cvt_pk_bf16_f32 v232, v228, v229\n\t"
      "ds_write_b128 v253, v[220:223] offset:33792\n\t"
      "v_mfma_f32_16x16x32_bf16 v[224:227], v[120:123], v[172:175], v[224:227]\n\t"
      "v_min_u32_e32 v254, -2, v232\n\t"
      "global_store_dword v247, v254, s[62:63] sc1\n\t"
      "s_add_u32 s62, s62, 57344\n\t"
      "s_addc_u32 s63, s63, 0\n\t"
      "v_mfma_f32_16x16x32_bf16 v[208:211], v[12:15], v[144:147], 0\n\t"
      "global_load_dwordx4 v[144:147], v250, s[74:75] sc1\n\t"
      "v_mfma_f32_16x16x32_bf16 v[208:211], v[28:31], v[148:151], v[208:211]\n\t"
      "v_mfma_f32_16x16x32_bf16 v[208:211], v[44:47], v[152:155], v[208:211]\n\t"
      "v_mfma_f32_16x16x32_bf16 v[208:211], v[60:63], v[156:159], v[208:211]\n\t"
      "v_mfma_f32_16x16x32_bf16 v[208:211], v[76:79], v[160:163], v[208:211]\n\t"
      "global_load_dwordx4 v[148:151], v250, s[74:75] offset:1024 sc1\n\t"
      "v_mfma_f32_16x16x32_bf16 v[208:211], v[92:95], v[164:167], v[208:211]\n\t"
      "v_mfma_f32_16x16x32_bf16 v[208:211], v[108:111], v[168:171], v[208:211]\n\t"
      "ds_write_b128 v245, v[224:227] offset:34816\n\t"
      "v_mfma_f32_16x16x32_bf16 v[208:211], v[124:127], v[172:175], v[208:211]\n\t"
      "s_waitcnt vmcnt(12)\n\t"
      "v_mfma_f32_16x16x32_bf16 v[212:215], v[0:3], v[176:179], 0\n\t"
      "global_load_dwordx4 v[152:155], v250, s[74:75] offset:2048 sc1\n\t"
      "v_mfma_f32_16x16x32_bf16 v[212:215], v[16:19], v[180:183], v[212:215]\n\t"
      "s_nop 3\n\t"
      "ds_write_b128 v253, v[208:211] offset:35840\n\t"
      "v_mfma_f32_16x16x32_bf16 v[212:215], v[32:35], v[184:187], v[212:215]\n\t"
      "v_mfma_f32_16x16x32_bf16 v[212:215], v[48:51], v[188:191], v[212:215]\n\t"
      "s_waitcnt vmcnt(8)\n\t"
      "v_mfma_f32_16x16x32_bf16 v[212:215], v[64:67], v[192:195], v[212:215]\n\t"
      "global_load_dwordx4 v[156:159], v250, s[74:75] offset:3072 sc1\n\t"
      "v_mfma_f32_16x16x32_bf16 v[212:215], v[80:83], v[196:199], v[212:215]\n\t"
      "s_waitcnt lgkmcnt(0)\n\t"
      "s_barrier\n\t"
      "ds_read_b64 v[228:229], v246 offset:32768\n\t"
      "ds_read_b64 v[230:231], v246 offset:36864\n\t"
      "v_mfma_f32_16x16x32_bf16 v[212:215], v[96:99], v[200:203], v[212:215]\n\t"
      "ds_read_b64 v[232:233], v246 offset:40960\n\t"
      "ds_read_b64 v[234:235], v246 offset:45056\n\t"
      "v_mfma_f32_16x16x32_bf16 v[212:215], v[112:115], v[204:207], v[212:215]\n\t"
      "ds_read_b64 v[236:237], v246 offset:49152\n\t"
      "ds_read_b64 v[238:239], v246 offset:53248\n\t"
      "v_mfma_f32_16x16x32_bf16 v[216:219], v[4:7], v[176:179], 0\n\t"
      "ds_read_b64 v[240:241], v246 offset:57344\n\t"
      "ds_read_b64 v[242:243], v246 offset:61440\n\t"
      "global_load_dwordx4 v[160:163], v250, s[76:77] offset:-4096 sc1\n\t"
      "v_mfma_f32_16x16x32_bf16 v[216:219], v[20:23], v[180:183], v[216:219]\n\t"
      "v_mfma_f32_16x16x32_bf16 v[216:219], v[36:39], v[184:187], v[216:219]\n\t"
      "s_waitcnt lgkmcnt(4)\n\t"
      "v_add_f32_e32 v228, v228, v230\n\t"
      "v_add_f32_e32 v232, v232, v234\n\t"
      "v_cmp_u_f32_e32 vcc, v212, v212\n\t"
      "s_cbranch_vccnz LA34R_%=\n\t"
      "LA34B_%=:\n\t"
      "v_mfma_f32_16x16x32_bf16 v[216:219], v[52:55], v[188:191], v[216:219]\n\t"
      "v_add_f32_e32 v229, v229, v231\n\t"
      "v_add_f32_e32 v233, v233, v235\n\t"
      "v_mfma_f32_16x16x32_bf16 v[216:219], v[68:71], v[192:195], v[216:219]\n\t"
      "s_waitcnt lgkmcnt(0)\n\t"
      "v_add_f32_e32 v236, v236, v238\n\t"
      "v_add_f32_e32 v240, v240, v242\n\t"
      "global_load_dwordx4 v[164:167], v250, s[76:77] offset:-3072 sc1\n\t"
      "v_mfma_f32_16x16x32_bf16 v[216:219], v[84:87], v[196:199], v[216:219]\n\t"
      "v_add_f32_e32 v237, v237, v239\n\t"
      "v_add_f32_e32 v241, v241, v243\n\t"
      "v_mfma_f32_16x16x32_bf16 v[216:219], v[100:103], v[200:203], v[216:219]\n\t"
      "v_add_f32_e32 v228, v228, v232\n\t"
      "v_add_f32_e32 v236, v236, v240\n\t"
      "v_add_f32_e32 v229, v229, v233\n\t"
      "ds_write_b128 v245, v[212:215]\n\t"
      "v_mfma_f32_16x16x32_bf16 v[216:219], v[116:119], v[204:207], v[216:219]\n\t"
      "v_add_f32_e32 v237, v237, v241\n\t"
      "v_add_f32_e32 v228, v228, v248\n\t"
      "v_mfma_f32_16x16x32_bf16 v[220:223], v[8:11], v[176:179], 0\n\t"
      "v_add_f32_e32 v229, v229, v249\n\t"
      "v_add_f32_e32 v228, v228, v236\n\t"
      "v_add_f32_e32 v229, v229, v237\n\t"
      "global_load_dwordx4 v[168:171], v250, s[76:77] offset:-2048 sc1\n\t"
      "v_mfma_f32_16x16x32_bf16 v[220:223], v[24:27], v[180:183], v[220:223]\n\t"
      "v_mul_f32_e32 v230, v228, v228\n\t"
      "v_mul_f32_e32 v231, v229, v229\n\t"
      "v_mfma_f32_16x16x32_bf16 v[220:223], v[40:43], v[184:187], v[220:223]\n\t"
      "v_fmaak_f32 v230, v252, v230, 0xc0135761\n\t"
      "v_fmaak_f32 v231, v252, v231, 0xc0135761\n\t"
      "v_mul_f32_e32 v230, v228, v230\n\t"
      "v_mfma_f32_16x16x32_bf16 v[220:223], v[56:59], v[188:191], v[220:223]\n\t"
      "v_mul_f32_e32 v231, v229, v231\n\t"
      "v_exp_f32_e32 v230, v230\n\t"
      "v_mfma_f32_16x16x32_bf16 v[220:223], v[72:75], v[192:195], v[220:223]\n\t"
      "v_exp_f32_e32 v231, v231\n\t"
      "v_add_f32_e32 v230, 1.0, v230\n\t"
      "v_add_f32_e32 v231, 1.0, v231\n\t"
      "global_load_dwordx4 v[172:175], v250, s[76:77] offset:-1024 sc1\n\t"
      "v_mfma_f32_16x16x32_bf16 v[220:223], v[88:91], v[196:199], v[220:223]\n\t"
      "v_rcp_f32_e32 v230, v230\n\t"
      "v_rcp_f32_e32 v231, v231\n\t"
      "v_mfma_f32_16x16x32_bf16 v[220:223], v[104:107], v[200:203], v[220:223]\n\t"
      "v_mul_f32_e32 v228, v228, v230\n\t"
      "v_mul_f32_e32 v229, v229, v231\n\t"
      "v_cvt_pk_bf16_f32 v232, v228, v229\n\t"
      "ds_write_b128 v253, v[216:219] offset:1024\n\t"
      "v_mfma_f32_16x16x32_bf16 v[220:223], v[120:123], v[204:207], v[220:223]\n\t"
      "v_min_u32_e32 v254, -2, v232\n\t"
      "global_store_dword v247, v254, s[62:63] sc1\n\t"
      "s_add_u32 s62, s62, 57344\n\t"
      "s_addc_u32 s63, s63, 0\n\t"
      "v_mfma_f32_16x16x32_bf16 v[224:227], v[12:15], v[176:179], 0\n\t"
      "global_load_dwordx4 v[176:179], v250, s[76:77] sc1\n\t"
      "v_mfma_f32_16x16x32_bf16 v[224:227], v[28:31], v[180:183], v[224:227]\n\t"
      "v_mfma_f32_16x16x32_bf16 v[224:227], v[44:47], v[184:187], v[224:227]\n\t"
      "v_mfma_f32_16x16x32_bf16 v[224:227], v[60:63], v[188:191], v[224:227]\n\t"
      "v_mfma_f32_16x16x32_bf16 v[224:227], v[76:79], v[192:195], v[224:227]\n\t"
      "global_load_dwordx4 v[180:183], v250, s[76:77] offset:1024 sc1\n\t"
      "v_mfma_f32_16x16x32_bf16 v[224:227], v[92:95], v[196:199], v[224:227]\n\t"
      "v_mfma_f32_16x16x32_bf16 v[224:227], v[108:111], v[200:203], v[224:227]\n\t"
      "ds_write_b128 v245, v[220:223] offset:2048\n\t"
      "v_mfma_f32_16x16x32_bf16 v[224:227], v[124:127], v[204:207], v[224:227]\n\t"
      "s_waitcnt vmcnt(12)\n\t"
      "v_mfma_f32_16x16x32_bf16 v[208:211], v[0:3], v[128:131], 0\n\t"
      "global_load_dwordx4 v[184:187], v250, s[76:77] offset:2048 sc1\n\t"
      "v_mfma_f32_16x16x32_bf16 v[208:211], v[16:19], v[132:135], v[208:211]\n\t"
      "s_nop 3\n\t"
      "ds_write_b128 v253, v[224:227] offset:3072\n\t"
      "v_mfma_f32_16x16x32_bf16 v[208:211], v[32:35], v[136:139], v[208:211]\n\t"
      "v_mfma_f32_16x16x32_bf16 v[208:211], v[48:51], v[140:143], v[208:211]\n\t"
      "s_waitcnt vmcnt(8)\n\t"
      "v_mfma_f32_16x16x32_bf16 v[208:211], v[64:67], v[144:147], v[208:211]\n\t"
      "global_load_dwordx4 v[188:191], v250, s[76:77] offset:3072 sc1\n\t"
      "v_mfma_f32_16x16x32_bf16 v[208:211], v[80:83], v[148:151], v[208:211]\n\t"
      "s_waitcnt lgkmcnt(0)\n\t"
      "s_barrier\n\t"
      "ds_read_b64 v[228:229], v246\n\t"
      "ds_read_b64 v[230:231], v246 offset:4096\n\t"
      "v_mfma_f32_16x16x32_bf16 v[208:211], v[96:99], v[152:155], v[208:211]\n\t"
      "ds_read_b64 v[232:233], v246 offset:8192\n\t"
      "ds_read_b64 v[234:235], v246 offset:12288\n\t"
      "v_mfma_f32_16x16x32_bf16 v[208:211], v[112:115], v[156:159], v[208:211]\n\t"
      "ds_read_b64 v[236:237], v246 offset:16384\n\t"
      "ds_read_b64 v[238:239], v246 offset:20480\n\t"
      "v_mfma_f32_16x16x32_bf16 v[212:215], v[4:7], v[128:131], 0\n\t"
      "ds_read_b64 v[240:241], v246 offset:24576\n\t"
      "ds_read_b64 v[242:243], v246 offset:28672\n\t"
      "v_mfma_f32_16x16x32_bf16 v[212:215], v[20:23], v[132:135], v[212:215]\n\t"
      "v_mfma_f32_16x16x32_bf16 v[212:215], v[36:39], v[136:139], v[212:215]\n\t"
      "s_waitcnt lgkmcnt(4)\n\t"
      "v_add_f32_e32 v228, v228, v230\n\t"
      "v_add_f32_e32 v232, v232, v234\n\t"
      "v_cmp_u_f32_e32 vcc, v208, v208\n\t"
      "s_cbranch_vccnz LA35R_%=\n\t"
      "LA35B_%=:\n\t"
      "v_mfma_f32_16x16x32_bf16 v[212:215], v[52:55], v[140:143], v[212:215]\n\t"
      "v_add_f32_e32 v229, v229, v231\n\t"
      "v_add_f32_e32 v233, v233, v235\n\t"
      "v_mfma_f32_16x16x32_bf16 v[212:215], v[68:71], v[144:147], v[212:215]\n\t"
      "s_waitcnt lgkmcnt(0)\n\t"
      "v_add_f32_e32 v236, v236, v238\n\t"
      "v_add_f32_e32 v240, v240, v242\n\t"
      "v_mfma_f32_16x16x32_bf16 v[212:215], v[84:87], v[148:151], v[212:215]\n\t"
      "v_add_f32_e32 v237, v237, v239\n\t"
      "v_add_f32_e32 v241, v241, v243\n\t"
      "v_mfma_f32_16x16x32_bf16 v[212:215], v[100:103], v[152:155], v[212:215]\n\t"
      "v_add_f32_e32 v228, v228, v232\n\t"
      "v_add_f32_e32 v236, v236, v240\n\t"
      "v_add_f32_e32 v229, v229, v233\n\t"
      "ds_write_b128 v245, v[208:211] offset:32768\n\t"
      "v_mfma_f32_16x16x32_bf16 v[212:215], v[116:119], v[156:159], v[212:215]\n\t"
      "v_add_f32_e32 v237, v237, v241\n\t"
      "v_add_f32_e32 v228, v228, v248\n\t"
      "v_mfma_f32_16x16x32_bf16 v[216:219], v[8:11], v[128:131], 0\n\t"
      "v_add_f32_e32 v229, v229, v249\n\t"
      "v_add_f32_e32 v228, v228, v236\n\t"
      "v_add_f32_e32 v229, v229, v237\n\t"
      "v_mfma_f32_16x16x32_bf16 v[216:219], v[24:27], v[132:135], v[216:219]\n\t"
      "v_mul_f32_e32 v230, v228, v228\n\t"
      "v_mul_f32_e32 v231, v229, v229\n\t"
      "v_mfma_f32_16x16x32_bf16 v[216:219], v[40:43], v[136:139], v[216:219]\n\t"
      "v_fmaak_f32 v230, v252, v230, 0xc0135761\n\t"
      "v_fmaak_f32 v231, v252, v231, 0xc0135761\n\t"
      "v_mul_f32_e32 v230, v228, v230\n\t"
      "v_mfma_f32_16x16x32_bf16 v[216:219], v[56:59], v[140:143], v[216:219]\n\t"
      "v_mul_f32_e32 v231, v229, v231\n\t"
      "v_exp_f32_e32 v230, v230\n\t"
      "v_mfma_f32_16x16x32_bf16 v[216:219], v[72:75], v[144:147], v[216:219]\n\t"
      "v_exp_f32_e32 v231, v231\n\t"
      "v_add_f32_e32 v230, 1.0, v230\n\t"
      "v_add_f32_e32 v231, 1.0, v231\n\t"
      "v_mfma_f32_16x16x32_bf16 v[216:219], v[88:91], v[148:151], v[216:219]\n\t"
      "v_rcp_f32_e32 v230, v230\n\t"
      "v_rcp_f32_e32 v231, v231\n\t"
      "v_mfma_f32_16x16x32_bf16 v[216:219], v[104:107], v[152:155], v[216:219]\n\t"
      "v_mul_f32_e32 v228, v228, v230\n\t"
      "v_mul_f32_e32 v229, v229, v231\n\t"
      "v_cvt_pk_bf16_f32 v232, v228, v229\n\t"
      "ds_write_b128 v253, v[212:215] offset:33792\n\t"
      "v_mfma_f32_16x16x32_bf16 v[216:219], v[120:123], v[156:159], v[216:219]\n\t"
      "v_min_u32_e32 v254, -2, v232\n\t"
      "global_store_dword v247, v254, s[62:63] sc1\n\t"
      "s_add_u32 s62, s62, 57344\n\t"
      "s_addc_u32 s63, s63, 0\n\t"
      "v_mfma_f32_16x16x32_bf16 v[220:223], v[12:15], v[128:131], 0\n\t"
      "v_mfma_f32_16x16x32_bf16 v[220:223], v[28:31], v[132:135], v[220:223]\n\t"
      "v_mfma_f32_16x16x32_bf16 v[220:223], v[44:47], v[136:139], v[220:223]\n\t"
      "v_mfma_f32_16x16x32_bf16 v[220:223], v[60:63], v[140:143], v[220:223]\n\t"
      "v_mfma_f32_16x16x32_bf16 v[220:223], v[76:79], v[144:147], v[220:223]\n\t"
      "v_mfma_f32_16x16x32_bf16 v[220:223], v[92:95], v[148:151], v[220:223]\n\t"
      "v_mfma_f32_16x16x32_bf16 v[220:223], v[108:111], v[152:155], v[220:223]\n\t"
      "ds_write_b128 v245, v[216:219] offset:34816\n\t"
      "v_mfma_f32_16x16x32_bf16 v[220:223], v[124:127], v[156:159], v[220:223]\n\t"
      "s_waitcnt vmcnt(6)\n\t"
      "v_mfma_f32_16x16x32_bf16 v[224:227], v[0:3], v[160:163], 0\n\t"
      "v_mfma_f32_16x16x32_bf16 v[224:227], v[16:19], v[164:167], v[224:227]\n\t"
      "s_nop 4\n\t"
      "ds_write_b128 v253, v[220:223] offset:35840\n\t"
      "v_mfma_f32_16x16x32_bf16 v[224:227], v[32:35], v[168:171], v[224:227]\n\t"
      "v_mfma_f32_16x16x32_bf16 v[224:227], v[48:51], v[172:175], v[224:227]\n\t"
      "s_waitcnt vmcnt(1)\n\t"
      "v_mfma_f32_16x16x32_bf16 v[224:227], v[64:67], v[176:179], v[224:227]\n\t"
      "v_mfma_f32_16x16x32_bf16 v[224:227], v[80:83], v[180:183], v[224:227]\n\t"
      "s_waitcnt lgkmcnt(0)\n\t"
      "s_barrier\n\t"
      "ds_read_b64 v[228:229], v246 offset:32768\n\t"
      "ds_read_b64 v[230:231], v246 offset:36864\n\t"
      "v_mfma_f32_16x16x32_bf16 v[224:227], v[96:99], v[184:187], v[224:227]\n\t"
      "ds_read_b64 v[232:233], v246 offset:40960\n\t"
      "ds_read_b64 v[234:235], v246 offset:45056\n\t"
      "v_mfma_f32_16x16x32_bf16 v[224:227], v[112:115], v[188:191], v[224:227]\n\t"
      "ds_read_b64 v[236:237], v246 offset:49152\n\t"
      "ds_read_b64 v[238:239], v246 offset:53248\n\t"
      "v_mfma_f32_16x16x32_bf16 v[208:211], v[4:7], v[160:163], 0\n\t"
      "ds_read_b64 v[240:241], v246 offset:57344\n\t"
      "ds_read_b64 v[242:243], v246 offset:61440\n\t"
      "v_mfma_f32_16x16x32_bf16 v[208:211], v[20:23], v[164:167], v[208:211]\n\t"
      "v_mfma_f32_16x16x32_bf16 v[208:211], v[36:39], v[168:171], v[208:211]\n\t"
      "s_waitcnt lgkmcnt(4)\n\t"
      "v_add_f32_e32 v228, v228, v230\n\t"
      "v_add_f32_e32 v232, v232, v234\n\t"
      "v_cmp_u_f32_e32 vcc, v224, v224\n\t"
      "s_cbranch_vccnz LA36R_%=\n\t"
      "LA36B_%=:\n\t"
      "v_mfma_f32_16x16x32_bf16 v[208:211], v[52:55], v[172:175], v[208:211]\n\t"
      "v_add_f32_e32 v229, v229, v231\n\t"
      "v_add_f32_e32 v233, v233, v235\n\t"
      "v_mfma_f32_16x16x32_bf16 v[208:211], v[68:71], v[176:179], v[208:211]\n\t"
      "s_waitcnt lgkmcnt(0)\n\t"
      "v_add_f32_e32 v236, v236, v238\n\t"
      "v_add_f32_e32 v240, v240, v242\n\t"
      "v_mfma_f32_16x16x32_bf16 v[208:211], v[84:87], v[180:183], v[208:211]\n\t"
      "v_add_f32_e32 v237, v237, v239\n\t"
      "v_add_f32_e32 v241, v241, v243\n\t"
      "v_mfma_f32_16x16x32_bf16 v[208:211], v[100:103], v[184:187], v[208:211]\n\t"
      "v_add_f32_e32 v228, v228, v232\n\t"
      "v_add_f32_e32 v236, v236, v240\n\t"
      "v_add_f32_e32 v229, v229, v233\n\t"
      "ds_write_b128 v245, v[224:227]\n\t"
      "v_mfma_f32_16x16x32_bf16 v[208:211], v[116:119], v[188:191], v[208:211]\n\t"
      "v_add_f32_e32 v237, v237, v241\n\t"
      "v_add_f32_e32 v228, v228, v248\n\t"
      "v_mfma_f32_16x16x32_bf16 v[212:215], v[8:11], v[160:163], 0\n\t"
      "v_add_f32_e32 v229, v229, v249\n\t"
      "v_add_f32_e32 v228, v228, v236\n\t"
      "v_add_f32_e32 v229, v229, v237\n\t"
      "v_mfma_f32_16x16x32_bf16 v[212:215], v[24:27], v[164:167], v[212:215]\n\t"
      "v_mul_f32_e32 v230, v228, v228\n\t"
      "v_mul_f32_e32 v231, v229, v229\n\t"
      "v_mfma_f32_16x16x32_bf16 v[212:215], v[40:43], v[168:171], v[212:215]\n\t"
      "v_fmaak_f32 v230, v252, v230, 0xc0135761\n\t"
      "v_fmaak_f32 v231, v252, v231, 0xc0135761\n\t"
      "v_mul_f32_e32 v230, v228, v230\n\t"
      "v_mfma_f32_16x16x32_bf16 v[212:215], v[56:59], v[172:175], v[212:215]\n\t"
      "v_mul_f32_e32 v231, v229, v231\n\t"
      "v_exp_f32_e32 v230, v230\n\t"
      "v_mfma_f32_16x16x32_bf16 v[212:215], v[72:75], v[176:179], v[212:215]\n\t"
      "v_exp_f32_e32 v231, v231\n\t"
      "v_add_f32_e32 v230, 1.0, v230\n\t"
      "v_add_f32_e32 v231, 1.0, v231\n\t"
      "v_mfma_f32_16x16x32_bf16 v[212:215], v[88:91], v[180:183], v[212:215]\n\t"
      "v_rcp_f32_e32 v230, v230\n\t"
      "v_rcp_f32_e32 v231, v231\n\t"
      "v_mfma_f32_16x16x32_bf16 v[212:215], v[104:107], v[184:187], v[212:215]\n\t"
      "v_mul_f32_e32 v228, v228, v230\n\t"
      "v_mul_f32_e32 v229, v229, v231\n\t"
      "v_cvt_pk_bf16_f32 v232, v228, v229\n\t"
      "ds_write_b128 v253, v[208:211] offset:1024\n\t"
      "v_mfma_f32_16x16x32_bf16 v[212:215], v[120:123], v[188:191], v[212:215]\n\t"
      "v_min_u32_e32 v254, -2, v232\n\t"
      "global_store_dword v247, v254, s[62:63] sc1\n\t"
      "s_add_u32 s62, s62, 57344\n\t"
      "s_addc_u32 s63, s63, 0\n\t"
      "v_mfma_f32_16x16x32_bf16 v[216:219], v[12:15], v[160:163], 0\n\t"
      "v_mfma_f32_16x16x32_bf16 v[216:219], v[28:31], v[164:167], v[216:219]\n\t"
      "v_mfma_f32_16x16x32_bf16 v[216:219], v[44:47], v[168:171], v[216:219]\n\t"
      "v_mfma_f32_16x16x32_bf16 v[216:219], v[60:63], v[172:175], v[216:219]\n\t"
      "v_mfma_f32_16x16x32_bf16 v[216:219], v[76:79], v[176:179], v[216:219]\n\t"
      "v_mfma_f32_16x16x32_bf16 v[216:219], v[92:95], v[180:183], v[216:219]\n\t"
      "v_mfma_f32_16x16x32_bf16 v[216:219], v[108:111], v[184:187], v[216:219]\n\t"
      "ds_write_b128 v245, v[212:215] offset:2048\n\t"
      "v_mfma_f32_16x16x32_bf16 v[216:219], v[124:127], v[188:191], v[216:219]\n\t"
      "s_nop 7\n\t"
      "ds_write_b128 v253, v[216:219] offset:3072\n\t"
      "s_waitcnt lgkmcnt(0)\n\t"
      "s_barrier\n\t"
      "ds_read_b64 v[228:229], v246\n\t"
      "ds_read_b64 v[230:231], v246 offset:4096\n\t"
      "ds_read_b64 v[232:233], v246 offset:8192\n\t"
      "ds_read_b64 v[234:235], v246 offset:12288\n\t"
      "ds_read_b64 v[236:237], v246 offset:16384\n\t"
      "ds_read_b64 v[238:239], v246 offset:20480\n\t"
      "ds_read_b64 v[240:241], v246 offset:24576\n\t"
      "ds_read_b64 v[242:243], v246 offset:28672\n\t"
      "s_waitcnt lgkmcnt(4)\n\t"
      "v_add_f32_e32 v228, v228, v230\n\t"
      "v_add_f32_e32 v232, v232, v234\n\t"
      "v_add_f32_e32 v229, v229, v231\n\t"
      "v_add_f32_e32 v233, v233, v235\n\t"
      "s_waitcnt lgkmcnt(0)\n\t"
      "v_add_f32_e32 v236, v236, v238\n\t"
      "v_add_f32_e32 v240, v240, v242\n\t"
      "v_add_f32_e32 v237, v237, v239\n\t"
      "v_add_f32_e32 v241, v241, v243\n\t"
      "v_add_f32_e32 v228, v228, v232\n\t"
      "v_add_f32_e32 v236, v236, v240\n\t"
      "v_add_f32_e32 v229, v229, v233\n\t"
      "v_add_f32_e32 v237, v237, v241\n\t"
      "v_add_f32_e32 v228, v228, v248\n\t"
      "v_add_f32_e32 v229, v229, v249\n\t"
      "v_add_f32_e32 v228, v228, v236\n\t"
      "v_add_f32_e32 v229, v229, v237\n\t"
      "v_mul_f32_e32 v230, v228, v228\n\t"
      "v_mul_f32_e32 v231, v229, v229\n\t"
      "v_fmaak_f32 v230, v252, v230, 0xc0135761\n\t"
      "v_fmaak_f32 v231, v252, v231, 0xc0135761\n\t"
      "v_mul_f32_e32 v230, v228, v230\n\t"
      "v_mul_f32_e32 v231, v229, v231\n\t"
      "v_exp_f32_e32 v230, v230\n\t"
      "v_exp_f32_e32 v231, v231\n\t"
      "v_add_f32_e32 v230, 1.0, v230\n\t"
      "v_add_f32_e32 v231, 1.0, v231\n\t"
      "v_rcp_f32_e32 v230, v230\n\t"
      "v_rcp_f32_e32 v231, v231\n\t"
      "v_mul_f32_e32 v228, v228, v230\n\t"
      "v_mul_f32_e32 v229, v229, v231\n\t"
      "v_cvt_pk_bf16_f32 v232, v228, v229\n\t"
      "v_min_u32_e32 v254, -2, v232\n\t"
      "global_store_dword v247, v254, s[62:63] sc1\n\t"
      "s_add_u32 s62, s62, 57344\n\t"
      "s_addc_u32 s63, s63, 0\n\t"
      "s_or_b32 s34, s2, s24\n\t"
      "s_cbranch_scc1 EPB_%=\n\t"
      "v_mov_b32_e32 v228, s22\n\t"
      "v_mov_b32_e32 v229, s23\n\t"
      "v_mov_b32_e32 v230, 0\n\t"
      "s_mov_b64 exec, 1\n\t"
      "global_store_dwordx2 v230, v[228:229], s[60:61] sc1\n\t"
      "s_mov_b64 exec, -1\n\t"
      "EPB_%=:\n\t"
      "s_endpgm\n\t"
      "LPATH_%=:\n\t"
      "s_add_u32 s22, s22, 1\n\t"
      "s_addc_u32 s23, s23, 0\n\t"
      "s_add_u32 s72, s26, 1\n\t"
      "s_and_b32 s72, s72, 6\n\t"
      "s_cselect_b32 s72, 0, 2\n\t"
      "s_or_b32 s27, s27, s72\n\t"
      "s_mul_i32 s72, s81, 172032\n\t"
      "s_add_u32 s72, s72, s37\n\t"
      "s_add_u32 s40, s6, s72\n\t"
      "s_addc_u32 s41, s7, 0\n\t"
      "s_add_u32 s81, s81, 1\n\t"
      "s_cmp_eq_u32 s81, 7\n\t"
      "s_cselect_b32 s81, 0, s81\n\t"
      "s_add_u32 m0, s39, 81920\n\t"
      "s_nop 0\n\t"
      "global_load_lds_dwordx4 v148, s[40:41]\n\t"
      "global_load_lds_dwordx4 v149, s[40:41] offset:1024\n\t"
      "global_load_lds_dwordx4 v150, s[40:41] offset:2048\n\t"
      "global_load_lds_dwordx4 v151, s[40:41] offset:3072\n\t"
      "s_mul_i32 s72, s81, 172032\n\t"
      "s_add_u32 s72, s72, s37\n\t"
      "s_add_u32 s40, s6, s72\n\t"
      "s_addc_u32 s41, s7, 0\n\t"
      "s_add_u32 s81, s81, 1\n\t"
      "s_cmp_eq_u32 s81, 7\n\t"
      "s_cselect_b32 s81, 0, s81\n\t"
      "s_add_u32 m0, s39, 98304\n\t"
      "s_nop 0\n\t"
      "global_load_lds_dwordx4 v148, s[40:41]\n\t"
      "global_load_lds_dwordx4 v149, s[40:41] offset:1024\n\t"
      "s_bitcmp0_b32 s27, 1\n\t"
      "s_cbranch_scc1 SK6_%=\n\t"
      "s_lshr_b32 s73, s26, 2\n\t"
      "s_lshl1_add_u32 s73, s25, s73\n\t"
      "s_lshl_b32 s72, s73, 7\n\t"
      "s_add_u32 s72, s72, 4096\n\t"
      "v_mov_b32_e32 v130, s72\n\t"
      "s_lshl2_add_u32 s75, s73, s24\n\t"
      "v_mov_b32_e32 v128, s22\n\t"
      "v_mov_b32_e32 v129, s23\n\t"
      "v_lshrrev_b32_e32 v140, 1, v244\n\t"
      "s_lshl_b32 s72, s75, 9\n\t"
      "s_add_u32 s72, s72, 12582912\n\t"
      "v_mov_b32_e32 v132, -1\n\t"
      "v_mov_b32_e32 v133, -1\n\t"
      "v_mov_b32_e32 v134, -1\n\t"
      "v_mov_b32_e32 v135, -1\n\t"
      "s_add_u32 s42, s20, s72\n\t"
      "s_addc_u32 s43, s21, 0\n\t"
      "global_store_dwordx2 v140, v[132:133], s[42:43] sc1\n\t"
      "s_lshl_b32 s72, s75, 13\n\t"
      "s_add_u32 s72, s72, 4096\n\t"
      "s_add_u32 s42, s20, s72\n\t"
      "s_addc_u32 s43, s21, 0\n\t"
      "global_store_dwordx4 v244, v[132:135], s[42:43] offset:-4096 sc1\n\t"
      "global_store_dwordx4 v244, v[132:135], s[42:43] offset:-3072 sc1\n\t"
      "global_store_dwordx4 v244, v[132:135], s[42:43] offset:-2048 sc1\n\t"
      "global_store_dwordx4 v244, v[132:135], s[42:43] offset:-1024 sc1\n\t"
      "SK6_%=:\n\t"
      "s_waitcnt vmcnt(19)\n\t"
      "s_bitcmp1_b32 s27, 1\n\t"
      "s_cbranch_scc1 SK7_%=\n\t"
      "s_waitcnt vmcnt(14)\n\t"
      "SK7_%=:\n\t"
      "s_barrier\n\t"
      "v_add_u32_e32 v172, 0x10000, v224\n\t"
      "v_add_u32_e32 v173, 0x10000, v225\n\t"
      "v_add_u32_e32 v174, 0x10000, v226\n\t"
      "v_add_u32_e32 v175, 0x10000, v227\n\t"
      "s_lshl_b32 s34, s25, 8\n\t"
      "s_add_u32 s34, s34, 4096\n\t"
      "v_and_b32_e32 v136, 1, v192\n\t"
      "v_lshl_add_u32 v136, v136, 7, s34\n\t"
      "s_lshl_b32 s34, s25, 7\n\t"
      "s_add_u32 s34, s34, 16384\n\t"
      "v_mov_b32_e32 v137, s34\n\t"
      "s_lshl_b32 s34, s2, 7\n\t"
      "s_add_u32 s34, s34, 32768\n\t"
      "v_mov_b32_e32 v143, s34\n\t"
      "s_waitcnt vmcnt(15)\n\t"
      "s_bitcmp1_b32 s27, 1\n\t"
      "s_cbranch_scc1 SK8_%=\n\t"
      "s_waitcnt vmcnt(10)\n\t"
      "SK8_%=:\n\t"
      "s_barrier\n\t"
      "global_load_lds_dwordx4 v150, s[40:41] offset:2048\n\t"
      "global_load_lds_dwordx4 v151, s[40:41] offset:3072\n\t"
      "s_bitcmp0_b32 s27, 1\n\t"
      "s_cbranch_scc1 SK9_%=\n\t"
      "global_store_dwordx4 v244, v[132:135], s[42:43] sc1\n\t"
      "global_store_dwordx4 v244, v[132:135], s[42:43] offset:1024 sc1\n\t"
      "SK9_%=:\n\t"
      "s_waitcnt vmcnt(15)\n\t"
      "s_bitcmp1_b32 s27, 1\n\t"
      "s_cbranch_scc1 SK10_%=\n\t"
      "s_waitcnt vmcnt(8)\n\t"
      "SK10_%=:\n\t"
      "s_barrier\n\t"
      "s_mul_i32 s72, s81, 172032\n\t"
      "s_add_u32 s72, s72, s37\n\t"
      "s_add_u32 s40, s6, s72\n\t"
      "s_addc_u32 s41, s7, 0\n\t"
      "s_add_u32 s81, s81, 1\n\t"
      "s_cmp_eq_u32 s81, 7\n\t"
      "s_cselect_b32 s81, 0, s81\n\t"
      "s_add_u32 m0, s39, 114688\n\t"
      "s_nop 0\n\t"
      "global_load_lds_dwordx4 v148, s[40:41]\n\t"
      "global_load_lds_dwordx4 v149, s[40:41] offset:1024\n\t"
      "global_load_lds_dwordx4 v150, s[40:41] offset:2048\n\t"
      "global_load_lds_dwordx4 v151, s[40:41] offset:3072\n\t"
      "s_bitcmp0_b32 s27, 1\n\t"
      "s_cbranch_scc1 SK11_%=\n\t"
      "global_store_dwordx4 v244, v[132:135], s[42:43] offset:2048 sc1\n\t"
      "global_store_dwordx4 v244, v[132:135], s[42:43] offset:3072 sc1\n\t"
      "SK11_%=:\n\t"
      "s_waitcnt vmcnt(17)\n\t"
      "s_bitcmp1_b32 s27, 1\n\t"
      "s_cbranch_scc1 SK12_%=\n\t"
      "s_waitcnt vmcnt(8)\n\t"
      "SK12_%=:\n\t"
      "s_barrier\n\t"
      "s_mul_i32 s72, s81, 172032\n\t"
      "s_add_u32 s72, s72, s37\n\t"
      "s_add_u32 s40, s6, s72\n\t"
      "s_addc_u32 s41, s7, 0\n\t"
      "s_add_u32 s81, s81, 1\n\t"
      "s_cmp_eq_u32 s81, 7\n\t"
      "s_cselect_b32 s81, 0, s81\n\t"
      "s_add_u32 m0, s39, 131072\n\t"
      "s_nop 0\n\t"
      "global_load_lds_dwordx4 v148, s[40:41]\n\t"
      "global_load_lds_dwordx4 v149, s[40:41] offset:1024\n\t"
      "global_load_lds_dwordx4 v150, s[40:41] offset:2048\n\t"
      "global_load_lds_dwordx4 v151, s[40:41] offset:3072\n\t"
      "s_waitcnt vmcnt(12)\n\t"
      "s_bitcmp1_b32 s27, 1\n\t"
      "s_cbranch_scc1 SK13_%=\n\t"
      "s_waitcnt vmcnt(8)\n\t"
      "SK13_%=:\n\t"
      "s_bitcmp0_b32 s27, 1\n\t"
      "s_cbranch_scc1 SK14_%=\n\t"
      "s_waitcnt vmcnt(4)\n\t"
      "SK14_%=:\n\t"
      "s_barrier\n\t"
      "s_mul_i32 s72, s81, 172032\n\t"
      "s_add_u32 s72, s72, s37\n\t"
      "s_add_u32 s40, s6, s72\n\t"
      "s_addc_u32 s41, s7, 0\n\t"
      "s_add_u32 s81, s81, 1\n\t"
      "s_cmp_eq_u32 s81, 7\n\t"
      "s_cselect_b32 s81, 0, s81\n\t"
      "s_add_u32 m0, s39, 147456\n\t"
      "s_nop 0\n\t"
      "global_load_lds_dwordx4 v148, s[40:41]\n\t"
      "global_load_lds_dwordx4 v149, s[40:41] offset:1024\n\t"
      "global_load_lds_dwordx4 v150, s[40:41] offset:2048\n\t"
      "global_load_lds_dwordx4 v151, s[40:41] offset:3072\n\t"
      "s_bitcmp0_b32 s27, 1\n\t"
      "s_cbranch_scc1 SK15_%=\n\t"
      "s_cmp_eq_u32 s24, 0\n\t"
      "s_cselect_b64 exec, 1, 0\n\t"
      "global_store_dwordx2 v130, v[128:129], s[60:61] sc1\n\t"
      "s_mov_b64 exec, -1\n\t"
      "s_mov_b32 m0, 64512\n\t"
      "s_mul_i32 s72, s75, 13\n\t"
      "v_min_u32_e32 v141, 12, v192\n\t"
      "v_add_u32_e32 v141, s72, v141\n\t"
      "v_min_u32_e32 v141, 3135, v141\n\t"
      "v_lshlrev_b32_e32 v141, 7, v141\n\t"
      "global_load_lds_dword v141, s[8:9]\n\t"
      "SK15_%=:\n\t"
      "s_waitcnt vmcnt(10)\n\t"
      "s_bitcmp1_b32 s27, 1\n\t"
      "s_cbranch_scc1 SK16_%=\n\t"
      "s_waitcnt vmcnt(8)\n\t"
      "SK16_%=:\n\t"
      "s_barrier\n\t"
      "s_mul_i32 s72, s82, 172032\n\t"
      "s_add_u32 s72, s72, s37\n\t"
      "s_add_u32 s72, s72, 1792\n\t"
      "s_add_u32 s40, s6, s72\n\t"
      "s_addc_u32 s41, s7, 0\n\t"
      "s_add_u32 s82, s82, 1\n\t"
      "s_cmp_eq_u32 s82, 7\n\t"
      "s_cselect_b32 s82, 0, s82\n\t"
      "s_add_u32 m0, s39, 65536\n\t"
      "s_nop 0\n\t"
      "global_load_lds_dwordx4 v148, s[40:41]\n\t"
      "global_load_lds_dwordx4 v152, s[40:41] offset:1024\n\t"
      "s_mul_i32 s72, s82, 172032\n\t"
      "s_add_u32 s72, s72, s37\n\t"
      "s_add_u32 s72, s72, 1792\n\t"
      "s_add_u32 s40, s6, s72\n\t"
      "s_addc_u32 s41, s7, 0\n\t"
      "s_add_u32 s82, s82, 1\n\t"
      "s_cmp_eq_u32 s82, 7\n\t"
      "s_cselect_b32 s82, 0, s82\n\t"
      "s_add_u32 m0, s39, 67584\n\t"
      "s_nop 0\n\t"
      "global_load_lds_dwordx4 v148, s[40:41]\n\t"
      "global_load_lds_dwordx4 v152, s[40:41] offset:1024\n\t"
      "s_waitcnt vmcnt(8)\n\t"
      "s_barrier\n\t"
      "s_mul_i32 s72, s82, 172032\n\t"
      "s_add_u32 s72, s72, s37\n\t"
      "s_add_u32 s72, s72, 1792\n\t"
      "s_add_u32 s40, s6, s72\n\t"
      "s_addc_u32 s41, s7, 0\n\t"
      "s_add_u32 s82, s82, 1\n\t"
      "s_cmp_eq_u32 s82, 7\n\t"
      "s_cselect_b32 s82, 0, s82\n\t"
      "s_add_u32 m0, s39, 81920\n\t"
      "s_nop 0\n\t"
      "global_load_lds_dwordx4 v148, s[40:41]\n\t"
      "global_load_lds_dwordx4 v152, s[40:41] offset:1024\n\t"
      "s_mul_i32 s72, s82, 172032\n\t"
      "s_add_u32 s72, s72, s37\n\t"
      "s_add_u32 s72, s72, 1792\n\t"
      "s_add_u32 s40, s6, s72\n\t"
      "s_addc_u32 s41, s7, 0\n\t"
      "s_add_u32 s82, s82, 1\n\t"
      "s_cmp_eq_u32 s82, 7\n\t"
      "s_cselect_b32 s82, 0, s82\n\t"
      "s_add_u32 m0, s39, 83968\n\t"
      "s_nop 0\n\t"
      "global_load_lds_dwordx4 v148, s[40:41]\n\t"
      "global_load_lds_dwordx4 v152, s[40:41] offset:1024\n\t"
      "s_waitcnt vmcnt(8)\n\t"
      "s_barrier\n\t"
      "s_cmp_lg_u32 s24, 3\n\t"
      "s_cbranch_scc1 SK17_%=\n\t"
      "global_load_dwordx2 v[138:139], v136, s[60:61] sc1\n\t"
      "SK17_%=:\n\t"
      "s_mul_i32 s72, s82, 172032\n\t"
      "s_add_u32 s72, s72, s37\n\t"
      "s_add_u32 s72, s72, 1792\n\t"
      "s_add_u32 s40, s6, s72\n\t"
      "s_addc_u32 s41, s7, 0\n\t"
      "s_add_u32 s82, s82, 1\n\t"
      "s_cmp_eq_u32 s82, 7\n\t"
      "s_cselect_b32 s82, 0, s82\n\t"
      "s_lshr_b32 s72, s39, 1\n\t"
      "s_add_u32 m0, s72, 98304\n\t"
      "s_nop 0\n\t"
      "global_load_lds_dwordx4 v148, s[40:41]\n\t"
      "global_load_lds_dwordx4 v152, s[40:41] offset:1024\n\t"
      "global_load_dword v249, v248, s[12:13]\n\t"
      "s_lshl2_add_u32 s72, s31, s24\n\t"
      "s_and_b32 s73, s72, 0x70\n\t"
      "s_add_u32 s72, s72, s73\n\t"
      "s_mov_b32 m0, 64512\n\t"
      "s_mul_i32 s72, s72, 28672\n\t"
      "s_and_b32 s73, s2, 7\n\t"
      "s_mul_i32 s73, s73, 448\n\t"
      "s_and_b32 s73, s73, 0xffffff80\n\t"
      "s_add_u32 s72, s72, s73\n\t"
      "v_bfe_u32 v141, v192, 2, 3\n\t"
      "s_cmp_ge_u32 s31, 28\n\t"
      "s_cselect_b32 s73, 32, 0\n\t"
      "v_and_b32_e32 v142, s73, v192\n\t"
      "v_lshl_or_b32 v141, v142, 2, v141\n\t"
      "v_mul_u32_u24_e32 v141, 3584, v141\n\t"
      "v_lshl_add_u32 v141, v196, 7, v141\n\t"
      "v_add_u32_e32 v141, s72, v141\n\t"
      "global_load_lds_dword v141, s[10:11]\n\t"
      "s_waitcnt vmcnt(8)\n\t"
      "s_barrier\n\t"
      "s_mul_i32 s72, s24, 14336\n\t"
      "s_add_u32 s40, s56, s72\n\t"
      "s_addc_u32 s41, s57, 0\n\t"
      "s_lshr_b32 s72, s39, 1\n\t"
      "s_add_u32 m0, s72, 106496\n\t"
      "s_nop 0\n\t"
      "global_load_lds_dwordx4 v154, s[40:41]\n\t"
      "global_load_lds_dwordx4 v155, s[40:41] offset:1024\n\t"
      "s_waitcnt vmcnt(6)\n\t"
      "s_barrier\n\t"
      "s_mul_i32 s72, s24, 28672\n\t"
      "s_add_u32 s72, s72, 57344\n\t"
      "s_add_u32 s40, s56, s72\n\t"
      "s_addc_u32 s41, s57, 0\n\t"
      "s_lshr_b32 s72, s39, 0\n\t"
      "s_add_u32 m0, s72, 114688\n\t"
      "s_nop 0\n\t"
      "global_load_lds_dwordx4 v154, s[40:41]\n\t"
      "global_load_lds_dwordx4 v155, s[40:41] offset:1024\n\t"
      "global_load_lds_dwordx4 v156, s[40:41] offset:2048\n\t"
      "global_load_lds_dwordx4 v157, s[40:41] offset:3072\n\t"
      "s_waitcnt vmcnt(8)\n\t"
      "s_cmp_lg_u32 s24, 3\n\t"
      "s_cbranch_scc1 LF1B_%=\n\t"
      "v_cmp_ne_u64_e32 vcc, s[22:23], v[138:139]\n\t"
      "s_cbranch_vccnz LF1R_%=\n\t"
      "LF1B_%=:\n\t"
      "s_barrier\n\t"
      "s_mul_i32 s72, s24, 28672\n\t"
      "s_add_u32 s72, s72, 172032\n\t"
      "s_add_u32 s40, s56, s72\n\t"
      "s_addc_u32 s41, s57, 0\n\t"
      "s_lshr_b32 s72, s39, 0\n\t"
      "s_add_u32 m0, s72, 131072\n\t"
      "s_nop 0\n\t"
      "global_load_lds_dwordx4 v154, s[40:41]\n\t"
      "global_load_lds_dwordx4 v155, s[40:41] offset:1024\n\t"
      "global_load_lds_dwordx4 v156, s[40:41] offset:2048\n\t"
      "global_load_lds_dwordx4 v157, s[40:41] offset:3072\n\t"
      "s_mul_i32 s72, s24, 28672\n\t"
      "s_add_u32 s72, s72, 286720\n\t"
      "s_add_u32 s40, s56, s72\n\t"
      "s_addc_u32 s41, s57, 0\n\t"
      "s_lshr_b32 s72, s39, 0\n\t"
      "s_add_u32 m0, s72, 147456\n\t"
      "s_nop 0\n\t"
      "global_load_lds_dwordx4 v154, s[40:41]\n\t"
      "global_load_lds_dwordx4 v155, s[40:41] offset:1024\n\t"
      "global_load_lds_dwordx4 v156, s[40:41] offset:2048\n\t"
      "global_load_lds_dwordx4 v157, s[40:41] offset:3072\n\t"
      "s_barrier\n\t"
      "s_cmp_eq_u32 s26, 7\n\t"
      "s_cbranch_scc1 SPARE_%=\n\t"
      "s_lshl_b32 s72, s24, 10\n\t"
      "s_add_u32 s72, s72, 98304\n\t"
      "v_xor_b32_e32 v203, v197, v198\n\t"
      "v_lshl_add_u32 v203, v203, 4, s72\n\t"
      "v_lshl_add_u32 v203, v197, 7, v203\n\t"
      "ds_write_b128 v203, v[204:207]\n\t"
      "s_waitcnt lgkmcnt(0)\n\t"
      "s_bitcmp0_b32 s27, 0\n\t"
      "s_cbranch_scc1 L4_%=\n\t"
      "s_add_u32 s72, s76, 0\n\t"
      "s_add_u32 s40, s52, s72\n\t"
      "s_addc_u32 s41, s53, 0\n\t"
      "s_add_u32 m0, s39, 65536\n\t"
      "s_nop 0\n\t"
      "global_load_lds_dwordx4 v144, s[40:41]\n\t"
      "global_load_lds_dwordx4 v145, s[40:41] offset:1024\n\t"
      "global_load_lds_dwordx4 v146, s[40:41] offset:2048\n\t"
      "global_load_lds_dwordx4 v147, s[40:41] offset:3072\n\t"
      "s_add_u32 s72, s76, 229376\n\t"
      "s_add_u32 s40, s52, s72\n\t"
      "s_addc_u32 s41, s53, 0\n\t"
      "s_add_u32 m0, s39, 81920\n\t"
      "s_nop 0\n\t"
      "global_load_lds_dwordx4 v144, s[40:41]\n\t"
      "global_load_lds_dwordx4 v145, s[40:41] offset:1024\n\t"
      "global_load_lds_dwordx4 v146, s[40:41] offset:2048\n\t"
      "global_load_lds_dwordx4 v147, s[40:41] offset:3072\n\t"
      "s_waitcnt vmcnt(8)\n\t"
      "s_barrier\n\t"
      "s_barrier\n\t"
      "s_add_u32 s72, s76, 3670016\n\t"
      "s_add_u32 s40, s52, s72\n\t"
      "s_addc_u32 s41, s53, 0\n\t"
      "s_add_u32 m0, s39, 16384\n\t"
      "s_nop 0\n\t"
      "global_load_lds_dwordx4 v144, s[40:41]\n\t"
      "global_load_lds_dwordx4 v145, s[40:41] offset:1024\n\t"
      "global_load_lds_dwordx4 v146, s[40:41] offset:2048\n\t"
      "global_load_lds_dwordx4 v147, s[40:41] offset:3072\n\t"
      "s_add_u32 s72, s76, 3899392\n\t"
      "s_add_u32 s40, s52, s72\n\t"
      "s_addc_u32 s41, s53, 0\n\t"
      "s_add_u32 m0, s39, 49152\n\t"
      "s_nop 0\n\t"
      "global_load_lds_dwordx4 v144, s[40:41]\n\t"
      "global_load_lds_dwordx4 v145, s[40:41] offset:1024\n\t"
      "global_load_lds_dwordx4 v146, s[40:41] offset:2048\n\t"
      "global_load_lds_dwordx4 v147, s[40:41] offset:3072\n\t"
      "s_waitcnt vmcnt(12)\n\t"
      "ds_read_b64_tr_b16 v[0:1], v172\n\t"
      "ds_read_b64_tr_b16 v[2:3], v172 offset:512\n\t"
      "ds_read_b64_tr_b16 v[4:5], v173\n\t"
      "ds_read_b64_tr_b16 v[6:7], v173 offset:512\n\t"
      "ds_read_b64_tr_b16 v[8:9], v174\n\t"
      "ds_read_b64_tr_b16 v[10:11], v174 offset:512\n\t"
      "ds_read_b64_tr_b16 v[12:13], v175\n\t"
      "ds_read_b64_tr_b16 v[14:15], v175 offset:512\n\t"
      "s_waitcnt lgkmcnt(0)\n\t"
      "s_add_u32 s72, s77, 0\n\t"
      "s_add_u32 s40, s52, s72\n\t"
      "s_addc_u32 s41, s53, 0\n\t"
      "s_add_u32 m0, s39, 65536\n\t"
      "s_nop 0\n\t"
      "global_load_lds_dwordx4 v144, s[40:41]\n\t"
      "global_load_lds_dwordx4 v145, s[40:41] offset:1024\n\t"
      "global_load_lds_dwordx4 v146, s[40:41] offset:2048\n\t"
      "global_load_lds_dwordx4 v147, s[40:41] offset:3072\n\t"
      "s_waitcnt vmcnt(12)\n\t"
      "ds_read_b64_tr_b16 v[16:17], v172 offset:16384\n\t"
      "ds_read_b64_tr_b16 v[18:19], v172 offset:16896\n\t"
      "ds_read_b64_tr_b16 v[20:21], v173 offset:16384\n\t"
      "ds_read_b64_tr_b16 v[22:23], v173 offset:16896\n\t"
      "ds_read_b64_tr_b16 v[24:25], v174 offset:16384\n\t"
      "ds_read_b64_tr_b16 v[26:27], v174 offset:16896\n\t"
      "ds_read_b64_tr_b16 v[28:29], v175 offset:16384\n\t"
      "ds_read_b64_tr_b16 v[30:31], v175 offset:16896\n\t"
      "s_waitcnt lgkmcnt(0)\n\t"
      "s_add_u32 s72, s77, 229376\n\t"
      "s_add_u32 s40, s52, s72\n\t"
      "s_addc_u32 s41, s53, 0\n\t"
      "s_add_u32 m0, s39, 81920\n\t"
      "s_nop 0\n\t"
      "global_load_lds_dwordx4 v144, s[40:41]\n\t"
      "global_load_lds_dwordx4 v145, s[40:41] offset:1024\n\t"
      "global_load_lds_dwordx4 v146, s[40:41] offset:2048\n\t"
      "global_load_lds_dwordx4 v147, s[40:41] offset:3072\n\t"
      "s_add_u32 s72, s77, 3670016\n\t"
      "s_add_u32 s40, s52, s72\n\t"
      "s_addc_u32 s41, s53, 0\n\t"
      "s_add_u32 m0, s39, 0\n\t"
      "s_nop 0\n\t"
      "global_load_lds_dwordx4 v144, s[40:41]\n\t"
      "global_load_lds_dwordx4 v145, s[40:41] offset:1024\n\t"
      "global_load_lds_dwordx4 v146, s[40:41] offset:2048\n\t"
      "global_load_lds_dwordx4 v147, s[40:41] offset:3072\n\t"
      "s_add_u32 s72, s77, 3899392\n\t"
      "s_add_u32 s40, s52, s72\n\t"
      "s_addc_u32 s41, s53, 0\n\t"
      "s_add_u32 m0, s39, 32768\n\t"
      "s_nop 0\n\t"
      "global_load_lds_dwordx4 v144, s[40:41]\n\t"
      "global_load_lds_dwordx4 v145, s[40:41] offset:1024\n\t"
      "global_load_lds_dwordx4 v146, s[40:41] offset:2048\n\t"
      "global_load_lds_dwordx4 v147, s[40:41] offset:3072\n\t"
      "s_waitcnt vmcnt(12)\n\t"
      "ds_read_b64_tr_b16 v[32:33], v172\n\t"
      "ds_read_b64_tr_b16 v[34:35], v172 offset:512\n\t"
      "ds_read_b64_tr_b16 v[36:37], v173\n\t"
      "ds_read_b64_tr_b16 v[38:39], v173 offset:512\n\t"
      "ds_read_b64_tr_b16 v[40:41], v174\n\t"
      "ds_read_b64_tr_b16 v[42:43], v174 offset:512\n\t"
      "ds_read_b64_tr_b16 v[44:45], v175\n\t"
      "ds_read_b64_tr_b16 v[46:47], v175 offset:512\n\t"
      "s_waitcnt lgkmcnt(0)\n\t"
      "s_add_u32 s72, s78, 0\n\t"
      "s_add_u32 s40, s52, s72\n\t"
      "s_addc_u32 s41, s53, 0\n\t"
      "s_add_u32 m0, s39, 65536\n\t"
      "s_nop 0\n\t"
      "global_load_lds_dwordx4 v144, s[40:41]\n\t"
      "global_load_lds_dwordx4 v145, s[40:41] offset:1024\n\t"
      "global_load_lds_dwordx4 v146, s[40:41] offset:2048\n\t"
      "global_load_lds_dwordx4 v147, s[40:41] offset:3072\n\t"
      "s_waitcnt vmcnt(12)\n\t"
      "ds_read_b64_tr_b16 v[48:49], v172 offset:16384\n\t"
      "ds_read_b64_tr_b16 v[50:51], v172 offset:16896\n\t"
      "ds_read_b64_tr_b16 v[52:53], v173 offset:16384\n\t"
      "ds_read_b64_tr_b16 v[54:55], v173 offset:16896\n\t"
      "ds_read_b64_tr_b16 v[56:57], v174 offset:16384\n\t"
      "ds_read_b64_tr_b16 v[58:59], v174 offset:16896\n\t"
      "ds_read_b64_tr_b16 v[60:61], v175 offset:16384\n\t"
      "ds_read_b64_tr_b16 v[62:63], v175 offset:16896\n\t"
      "s_waitcnt lgkmcnt(0)\n\t"
      "s_add_u32 s72, s78, 229376\n\t"
      "s_add_u32 s40, s52, s72\n\t"
      "s_addc_u32 s41, s53, 0\n\t"
      "s_add_u32 m0, s39, 81920\n\t"
      "s_nop 0\n\t"
      "global_load_lds_dwordx4 v144, s[40:41]\n\t"
      "global_load_lds_dwordx4 v145, s[40:41] offset:1024\n\t"
      "global_load_lds_dwordx4 v146, s[40:41] offset:2048\n\t"
      "global_load_lds_dwordx4 v147, s[40:41] offset:3072\n\t"
      "s_waitcnt vmcnt(8)\n\t"
      "s_barrier\n\t"
      "s_cmp_lg_u32 s24, 3\n\t"
      "s_cbranch_scc1 SK19_%=\n\t"
      "global_load_dwordx2 v[138:139], v137, s[60:61] sc1\n\t"
      "SK19_%=:\n\t"
      "s_add_u32 s72, s79, 0\n\t"
      "s_add_u32 s40, s52, s72\n\t"
      "s_addc_u32 s41, s53, 0\n\t"
      "s_add_u32 m0, s39, 98304\n\t"
      "s_nop 0\n\t"
      "global_load_lds_dwordx4 v144, s[40:41]\n\t"
      "global_load_lds_dwordx4 v145, s[40:41] offset:1024\n\t"
      "global_load_lds_dwordx4 v146, s[40:41] offset:2048\n\t"
      "global_load_lds_dwordx4 v147, s[40:41] offset:3072\n\t"
      "s_add_u32 s72, s79, 229376\n\t"
      "s_add_u32 s40, s52, s72\n\t"
      "s_addc_u32 s41, s53, 0\n\t"
      "s_add_u32 m0, s39, 114688\n\t"
      "s_nop 0\n\t"
      "global_load_lds_dwordx4 v144, s[40:41]\n\t"
      "global_load_lds_dwordx4 v145, s[40:41] offset:1024\n\t"
      "global_load_lds_dwordx4 v146, s[40:41] offset:2048\n\t"
      "global_load_lds_dwordx4 v147, s[40:41] offset:3072\n\t"
      "s_cmp_lg_u32 s24, 3\n\t"
      "s_cbranch_scc1 LF2B_%=\n\t"
      "s_waitcnt vmcnt(8)\n\t"
      "v_cmp_ne_u64_e32 vcc, s[22:23], v[138:139]\n\t"
      "s_cbranch_vccnz LF2R_%=\n\t"
      "LF2B_%=:\n\t"
      "s_barrier\n\t"
      "s_add_u32 s72, s78, 3670016\n\t"
      "s_add_u32 s40, s52, s72\n\t"
      "s_addc_u32 s41, s53, 0\n\t"
      "s_add_u32 m0, s39, 16384\n\t"
      "s_nop 0\n\t"
      "global_load_lds_dwordx4 v144, s[40:41]\n\t"
      "global_load_lds_dwordx4 v145, s[40:41] offset:1024\n\t"
      "global_load_lds_dwordx4 v146, s[40:41] offset:2048\n\t"
      "global_load_lds_dwordx4 v147, s[40:41] offset:3072\n\t"
      "s_add_u32 s72, s78, 3899392\n\t"
      "s_add_u32 s40, s52, s72\n\t"
      "s_addc_u32 s41, s53, 0\n\t"
      "s_add_u32 m0, s39, 49152\n\t"
      "s_nop 0\n\t"
      "global_load_lds_dwordx4 v144, s[40:41]\n\t"
      "global_load_lds_dwordx4 v145, s[40:41] offset:1024\n\t"
      "global_load_lds_dwordx4 v146, s[40:41] offset:2048\n\t"
      "global_load_lds_dwordx4 v147, s[40:41] offset:3072\n\t"
      "s_add_u32 s72, s79, 3670016\n\t"
      "s_add_u32 s40, s52, s72\n\t"
      "s_addc_u32 s41, s53, 0\n\t"
      "s_add_u32 m0, s39, 0\n\t"
      "s_nop 0\n\t"
      "global_load_lds_dwordx4 v144, s[40:41]\n\t"
      "global_load_lds_dwordx4 v145, s[40:41] offset:1024\n\t"
      "global_load_lds_dwordx4 v146, s[40:41] offset:2048\n\t"
      "global_load_lds_dwordx4 v147, s[40:41] offset:3072\n\t"
      "s_add_u32 s72, s79, 3899392\n\t"
      "s_add_u32 s40, s52, s72\n\t"
      "s_addc_u32 s41, s53, 0\n\t"
      "s_add_u32 m0, s39, 32768\n\t"
      "s_nop 0\n\t"
      "global_load_lds_dwordx4 v144, s[40:41]\n\t"
      "global_load_lds_dwordx4 v145, s[40:41] offset:1024\n\t"
      "global_load_lds_dwordx4 v146, s[40:41] offset:2048\n\t"
      "global_load_lds_dwordx4 v147, s[40:41] offset:3072\n\t"
      "s_cmp_lg_u32 s24, 3\n\t"
      "s_cbranch_scc1 SK20_%=\n\t"
      "global_load_dwordx2 v[138:139], v143, s[60:61] sc1\n\t"
      "SK20_%=:\n\t"
      "s_waitcnt vmcnt(28)\n\t"
      "ds_read_b64_tr_b16 v[64:65], v172\n\t"
      "ds_read_b64_tr_b16 v[66:67], v172 offset:512\n\t"
      "ds_read_b64_tr_b16 v[68:69], v173\n\t"
      "ds_read_b64_tr_b16 v[70:71], v173 offset:512\n\t"
      "ds_read_b64_tr_b16 v[72:73], v174\n\t"
      "ds_read_b64_tr_b16 v[74:75], v174 offset:512\n\t"
      "ds_read_b64_tr_b16 v[76:77], v175\n\t"
      "ds_read_b64_tr_b16 v[78:79], v175 offset:512\n\t"
      "s_waitcnt vmcnt(24)\n\t"
      "ds_read_b64_tr_b16 v[80:81], v172 offset:16384\n\t"
      "ds_read_b64_tr_b16 v[82:83], v172 offset:16896\n\t"
      "ds_read_b64_tr_b16 v[84:85], v173 offset:16384\n\t"
      "ds_read_b64_tr_b16 v[86:87], v173 offset:16896\n\t"
      "ds_read_b64_tr_b16 v[88:89], v174 offset:16384\n\t"
      "ds_read_b64_tr_b16 v[90:91], v174 offset:16896\n\t"
      "ds_read_b64_tr_b16 v[92:93], v175 offset:16384\n\t"
      "ds_read_b64_tr_b16 v[94:95], v175 offset:16896\n\t"
      "s_waitcnt vmcnt(20)\n\t"
      "ds_read_b64_tr_b16 v[96:97], v172 offset:32768\n\t"
      "ds_read_b64_tr_b16 v[98:99], v172 offset:33280\n\t"
      "ds_read_b64_tr_b16 v[100:101], v173 offset:32768\n\t"
      "ds_read_b64_tr_b16 v[102:103], v173 offset:33280\n\t"
      "ds_read_b64_tr_b16 v[104:105], v174 offset:32768\n\t"
      "ds_read_b64_tr_b16 v[106:107], v174 offset:33280\n\t"
      "ds_read_b64_tr_b16 v[108:109], v175 offset:32768\n\t"
      "ds_read_b64_tr_b16 v[110:111], v175 offset:33280\n\t"
      "s_waitcnt vmcnt(16)\n\t"
      "ds_read_b64_tr_b16 v[112:113], v172 offset:49152\n\t"
      "ds_read_b64_tr_b16 v[114:115], v172 offset:49664\n\t"
      "ds_read_b64_tr_b16 v[116:117], v173 offset:49152\n\t"
      "ds_read_b64_tr_b16 v[118:119], v173 offset:49664\n\t"
      "ds_read_b64_tr_b16 v[120:121], v174 offset:49152\n\t"
      "ds_read_b64_tr_b16 v[122:123], v174 offset:49664\n\t"
      "ds_read_b64_tr_b16 v[124:125], v175 offset:49152\n\t"
      "ds_read_b64_tr_b16 v[126:127], v175 offset:49664\n\t"
      "s_waitcnt vmcnt(0)\n\t"
      "s_cmp_lg_u32 s24, 3\n\t"
      "s_cbranch_scc1 LF3B_%=\n\t"
      "s_waitcnt vmcnt(0)\n\t"
      "v_cmp_ne_u64_e32 vcc, s[22:23], v[138:139]\n\t"
      "s_cbranch_vccnz LF3R_%=\n\t"
      "LF3B_%=:\n\t"
      "s_waitcnt lgkmcnt(0)\n\t"
      "s_barrier\n\t"
      "s_mul_i32 s34, s32, 196608\n\t"
      "s_mul_i32 s35, s24, 8192\n\t"
      "s_add_u32 s34, s34, s35\n\t"
      "s_add_u32 s34, s34, 1048576\n\t"
      "s_add_u32 s64, s48, s34\n\t"
      "s_addc_u32 s65, s49, 0\n\t"
      "v_add_u32_e32 v250, 4096, v244\n\t"
      "global_load_dwordx4 v[128:131], v250, s[64:65] offset:-4096 sc1\n\t"
      "global_load_dwordx4 v[132:135], v250, s[64:65] offset:-3072 sc1\n\t"
      "global_load_dwordx4 v[136:139], v250, s[64:65] offset:-2048 sc1\n\t"
      "global_load_dwordx4 v[140:143], v250, s[64:65] offset:-1024 sc1\n\t"
      "global_load_dwordx4 v[144:147], v250, s[64:65] sc1\n\t"
      "global_load_dwordx4 v[148:151], v250, s[64:65] offset:1024 sc1\n\t"
      "global_load_dwordx4 v[152:155], v250, s[64:65] offset:2048 sc1\n\t"
      "global_load_dwordx4 v[156:159], v250, s[64:65] offset:3072 sc1\n\t"
      "s_waitcnt vmcnt(0) lgkmcnt(0)\n\t"
      "s_branch GST_%=\n\t"
      "G4C_%=:\n\t"
      "global_store_dwordx4 v241, v[208:211], s[20:21] offset:-2048 sc1\n\t"
      "global_store_dwordx4 v241, v[212:215], s[20:21] offset:2048 sc1\n\t"
      "s_setprio 0\n\t"
      "G4_%=:\n\t"
      "s_mul_i32 s34, s32, 401408\n\t"
      "s_mul_i32 s35, s24, 7168\n\t"
      "s_add_u32 s34, s34, s35\n\t"
      "s_add_u32 s34, s34, 1835008\n\t"
      "s_add_u32 s64, s48, s34\n\t"
      "s_addc_u32 s65, s49, 0\n\t"
      "s_and_b32 s34, s30, 3\n\t"
      "s_lshl_b32 s34, s34, 11\n\t"
      "s_add_u32 s40, s34, 0\n\t"
      "s_sub_u32 s35, s40, 7168\n\t"
      "s_min_u32 s40, s40, s35\n\t"
      "s_add_u32 s41, s34, 1024\n\t"
      "s_sub_u32 s35, s41, 7168\n\t"
      "s_min_u32 s41, s41, s35\n\t"
      "s_add_u32 s42, s34, 2048\n\t"
      "s_sub_u32 s35, s42, 7168\n\t"
      "s_min_u32 s42, s42, s35\n\t"
      "s_add_u32 s43, s34, 3072\n\t"
      "s_sub_u32 s35, s43, 7168\n\t"
      "s_min_u32 s43, s43, s35\n\t"
      "s_add_u32 s44, s34, 4096\n\t"
      "s_sub_u32 s35, s44, 7168\n\t"
      "s_min_u32 s44, s44, s35\n\t"
      "s_add_u32 s45, s34, 5120\n\t"
      "s_sub_u32 s35, s45, 7168\n\t"
      "s_min_u32 s45, s45, s35\n\t"
      "s_add_u32 s46, s34, 6144\n\t"
      "s_sub_u32 s35, s46, 7168\n\t"
      "s_min_u32 s46, s46, s35\n\t"
      "s_add_u32 s66, s64, 57344\n\t"
      "s_addc_u32 s67, s65, 0\n\t"
      "s_add_u32 s68, s66, 57344\n\t"
      "s_addc_u32 s69, s67, 0\n\t"
      "s_add_u32 s70, s68, 57344\n\t"
      "s_addc_u32 s71, s69, 0\n\t"
      "s_add_u32 s72, s70, 57344\n\t"
      "s_addc_u32 s73, s71, 0\n\t"
      "s_add_u32 s74, s72, 57344\n\t"
      "s_addc_u32 s75, s73, 0\n\t"
      "s_add_u32 s76, s74, 57344\n\t"
      "s_addc_u32 s77, s75, 0\n\t"
      "v_lshrrev_b32_e32 v240, 7, v244\n\t"
      "v_and_b32_e32 v241, 0x70, v244\n\t"
      "v_and_b32_e32 v242, 6, v240\n\t"
      "v_lshlrev_b32_e32 v242, 4, v242\n\t"
      "v_xor_b32_e32 v242, v242, v241\n\t"
      "v_lshl_or_b32 v228, v240, 12, v242\n\t"
      "s_mul_i32 s34, s24, 917504\n\t"
      "s_lshl_b32 s35, s29, 7\n\t"
      "s_add_u32 s34, s34, s35\n\t"
      "s_lshl_b32 s39, s24, 12\n\t"
      "s_add_u32 s54, s14, s34\n\t"
      "s_addc_u32 s55, s15, 0\n\t"
      "v_add_u32_e32 v229, 31744, v228\n\t"
      "v_add_u32_e32 v230, 63488, v228\n\t"
      "v_add_u32_e32 v231, 95232, v228\n\t"
      "s_lshl_b32 s37, s40, 7\n\t"
      "s_add_u32 s58, s54, s37\n\t"
      "s_addc_u32 s59, s55, 0\n\t"
      "s_add_u32 m0, s39, 0\n\t"
      "s_nop 0\n\t"
      "global_load_lds_dwordx4 v228, s[58:59]\n\t"
      "global_load_lds_dwordx4 v229, s[58:59] offset:1024\n\t"
      "global_load_lds_dwordx4 v230, s[58:59] offset:2048\n\t"
      "global_load_lds_dwordx4 v231, s[58:59] offset:3072\n\t"
      "s_lshl_b32 s37, s41, 7\n\t"
      "s_add_u32 s58, s54, s37\n\t"
      "s_addc_u32 s59, s55, 0\n\t"
      "s_add_u32 m0, s39, 32768\n\t"
      "s_nop 0\n\t"
      "global_load_lds_dwordx4 v228, s[58:59]\n\t"
      "global_load_lds_dwordx4 v229, s[58:59] offset:1024\n\t"
      "global_load_lds_dwordx4 v230, s[58:59] offset:2048\n\t"
      "global_load_lds_dwordx4 v231, s[58:59] offset:3072\n\t"
      "s_lshr_b32 s34, s24, 1\n\t"
      "s_lshl2_add_u32 s35, s29, s34\n\t"
      "s_and_b32 s36, s24, 1\n\t"
      "s_lshl4_add_u32 s35, s35, s36\n\t"
      "v_lshl_add_u32 v242, v240, 1, s35\n\t"
      "v_lshlrev_b32_e32 v248, 1, v242\n\t"
      "global_load_ushort v249, v248, s[16:17]\n\t"
      "s_movk_i32 s35, 896\n\t"
      "v_and_b32_e32 v247, 64, v244\n\t"
      "v_lshrrev_b32_e32 v247, 2, v247\n\t"
      "v_mad_u32_u24 v247, v242, s35, v247\n\t"
      "v_and_b32_e32 v242, 0x30, v244\n\t"
      "v_lshl_add_u32 v247, v242, 1, v247\n\t"
      "s_mov_b32 s82, 0x77777777\n\t"
      "s_mov_b32 s83, 0x77777777\n\t"
      "s_lshl_b32 s35, s32, 7\n\t"
      "s_add_u32 s62, s18, s35\n\t"
      "s_addc_u32 s63, s19, 0\n\t"
      "s_mul_i32 s35, s32, 96\n\t"
      "s_add_u32 s35, s35, 512\n\t"
      "s_add_u32 s18, s18, s35\n\t"
      "s_addc_u32 s19, s19, 0\n\t"
      "s_mul_i32 s35, s29, 28\n\t"
      "s_lshl2_add_u32 s35, s32, s35\n\t"
      "s_lshl1_add_u32 s35, s35, s36\n\t"
      "s_lshl2_add_u32 s35, s35, s34\n\t"
      "s_lshl_b32 s35, s35, 8\n\t"
      "s_add_u32 s35, s35, 6295552\n\t"
      "s_add_u32 s50, s20, s35\n\t"
      "s_addc_u32 s51, s21, 0\n\t"
      "v_lshrrev_b32_e32 v248, 2, v244\n\t"
      "v_bfe_u32 v242, v244, 8, 1\n\t"
      "v_lshlrev_b32_e32 v242, 4, v242\n\t"
      "v_xor_b32_e32 v245, v244, v242\n\t"
      "v_bfe_u32 v242, v244, 9, 1\n\t"
      "v_lshlrev_b32_e32 v242, 7, v242\n\t"
      "v_xor_b32_e32 v245, v245, v242\n\t"
      "s_add_u32 s35, s39, 98304\n\t"
      "v_add_u32_e32 v245, s35, v245\n\t"
      "v_lshl_or_b32 v246, v240, 1, s36\n\t"
      "v_bfe_u32 v242, v244, 5, 1\n\t"
      "v_xor_b32_e32 v246, v246, v242\n\t"
      "v_bfe_u32 v242, v244, 6, 1\n\t"
      "v_lshlrev_b32_e32 v242, 3, v242\n\t"
      "v_xor_b32_e32 v246, v246, v242\n\t"
      "v_lshlrev_b32_e32 v246, 4, v246\n\t"
      "v_and_b32_e32 v242, 0x60, v244\n\t"
      "v_lshl_or_b32 v246, v242, 3, v246\n\t"
      "v_bfe_u32 v242, v244, 4, 1\n\t"
      "v_lshl_or_b32 v246, v242, 3, v246\n\t"
      "s_lshl_b32 s34, s34, 10\n\t"
      "s_add_u32 s34, s34, 98304\n\t"
      "v_add_u32_e32 v246, s34, v246\n\t"
      "v_and_b32_e32 v240, 0x3c0, v244\n\t"
      "v_lshlrev_b32_e32 v240, 1, v240\n\t"
      "v_and_b32_e32 v241, 0x180, v244\n\t"
      "v_lshrrev_b32_e32 v241, 2, v241\n\t"
      "v_and_b32_e32 v242, 0x30, v244\n\t"
      "v_lshrrev_b32_e32 v242, 1, v242\n\t"
      "v_or3_b32 v232, v240, v241, v242\n\t"
      "v_add_u32_e32 v232, s39, v232\n\t"
      "v_min_u32_e32 v252, 432, v244\n\t"
      "v_lshlrev_b32_e32 v252, 4, v252\n\t"
      "v_xor_b32_e32 v233, 32, v232\n\t"
      "v_xor_b32_e32 v234, 64, v232\n\t"
      "v_xor_b32_e32 v235, 96, v232\n\t"
      "v_xor_b32_e32 v236, 65536, v232\n\t"
      "v_xor_b32_e32 v237, 65568, v232\n\t"
      "v_xor_b32_e32 v238, 65600, v232\n\t"
      "v_xor_b32_e32 v239, 65632, v232\n\t"
      "v_min_u32_e32 v240, 48, v244\n\t"
      "v_mul_u32_u24_e32 v240, 7, v240\n\t"
      "s_lshl_b32 s34, s29, 4\n\t"
      "v_add_u32_e32 v240, s34, v240\n\t"
      "v_subrev_u32_e32 v241, 448, v240\n\t"
      "v_min_u32_e32 v240, v240, v241\n\t"
      "v_subrev_u32_e32 v241, 448, v240\n\t"
      "v_min_u32_e32 v240, v240, v241\n\t"
      "s_and_b32 s34, s29, 7\n\t"
      "s_lshl_b32 s34, s34, 8\n\t"
      "v_lshl_add_u32 v253, v240, 7, s34\n\t"
      "s_sub_u32 s52, s64, 50176\n\t"
      "s_subb_u32 s53, s65, 0\n\t"
      "v_add_u32_e32 v143, 32768, v143\n\t"
      "s_barrier\n\t"
      "s_lshl_b32 s37, s42, 7\n\t"
      "s_add_u32 s58, s54, s37\n\t"
      "s_addc_u32 s59, s55, 0\n\t"
      "s_add_u32 m0, s39, 65536\n\t"
      "s_nop 0\n\t"
      "global_load_lds_dwordx4 v228, s[58:59]\n\t"
      "global_load_lds_dwordx4 v229, s[58:59] offset:1024\n\t"
      "global_load_lds_dwordx4 v230, s[58:59] offset:2048\n\t"
      "global_load_lds_dwordx4 v231, s[58:59] offset:3072\n\t"
      "s_lshl_b32 s37, s43, 7\n\t"
      "s_add_u32 s58, s54, s37\n\t"
      "s_addc_u32 s59, s55, 0\n\t"
      "s_add_u32 m0, s39, 98304\n\t"
      "s_nop 0\n\t"
      "global_load_lds_dwordx4 v228, s[58:59]\n\t"
      "global_load_lds_dwordx4 v229, s[58:59] offset:1024\n\t"
      "global_load_lds_dwordx4 v230, s[58:59] offset:2048\n\t"
      "global_load_lds_dwordx4 v231, s[58:59] offset:3072\n\t"
      "s_waitcnt vmcnt(13)\n\t"
      "ds_read_b64_tr_b16 v[0:1], v232\n\t"
      "ds_read_b64_tr_b16 v[2:3], v232 offset:2048\n\t"
      "ds_read_b64_tr_b16 v[4:5], v233\n\t"
      "ds_read_b64_tr_b16 v[6:7], v233 offset:2048\n\t"
      "ds_read_b64_tr_b16 v[8:9], v234\n\t"
      "ds_read_b64_tr_b16 v[10:11], v234 offset:2048\n\t"
      "ds_read_b64_tr_b16 v[12:13], v235\n\t"
      "ds_read_b64_tr_b16 v[14:15], v235 offset:2048\n\t"
      "s_waitcnt lgkmcnt(0)\n\t"
      "s_lshl_b32 s37, s44, 7\n\t"
      "s_add_u32 s58, s54, s37\n\t"
      "s_addc_u32 s59, s55, 0\n\t"
      "s_add_u32 m0, s39, 0\n\t"
      "s_nop 0\n\t"
      "global_load_lds_dwordx4 v228, s[58:59]\n\t"
      "global_load_lds_dwordx4 v229, s[58:59] offset:1024\n\t"
      "global_load_lds_dwordx4 v230, s[58:59] offset:2048\n\t"
      "global_load_lds_dwordx4 v231, s[58:59] offset:3072\n\t"
      "s_waitcnt vmcnt(13)\n\t"
      "ds_read_b64_tr_b16 v[16:17], v232 offset:32768\n\t"
      "ds_read_b64_tr_b16 v[18:19], v232 offset:34816\n\t"
      "ds_read_b64_tr_b16 v[20:21], v233 offset:32768\n\t"
      "ds_read_b64_tr_b16 v[22:23], v233 offset:34816\n\t"
      "ds_read_b64_tr_b16 v[24:25], v234 offset:32768\n\t"
      "ds_read_b64_tr_b16 v[26:27], v234 offset:34816\n\t"
      "ds_read_b64_tr_b16 v[28:29], v235 offset:32768\n\t"
      "ds_read_b64_tr_b16 v[30:31], v235 offset:34816\n\t"
      "s_cmp_lg_u32 s24, 3\n\t"
      "s_cbranch_scc1 SK24_%=\n\t"
      "global_load_dwordx2 v[138:139], v143, s[60:61] sc1\n\t"
      "SK24_%=:\n\t"
      "s_waitcnt lgkmcnt(0)\n\t"
      "s_lshl_b32 s37, s45, 7\n\t"
      "s_add_u32 s58, s54, s37\n\t"
      "s_addc_u32 s59, s55, 0\n\t"
      "s_add_u32 m0, s39, 32768\n\t"
      "s_nop 0\n\t"
      "global_load_lds_dwordx4 v228, s[58:59]\n\t"
      "global_load_lds_dwordx4 v229, s[58:59] offset:1024\n\t"
      "global_load_lds_dwordx4 v230, s[58:59] offset:2048\n\t"
      "global_load_lds_dwordx4 v231, s[58:59] offset:3072\n\t"
      "s_waitcnt vmcnt(12)\n\t"
      "ds_read_b64_tr_b16 v[32:33], v236\n\t"
      "ds_read_b64_tr_b16 v[34:35], v236 offset:2048\n\t"
      "ds_read_b64_tr_b16 v[36:37], v237\n\t"
      "ds_read_b64_tr_b16 v[38:39], v237 offset:2048\n\t"
      "ds_read_b64_tr_b16 v[40:41], v238\n\t"
      "ds_read_b64_tr_b16 v[42:43], v238 offset:2048\n\t"
      "ds_read_b64_tr_b16 v[44:45], v239\n\t"
      "ds_read_b64_tr_b16 v[46:47], v239 offset:2048\n\t"
      "s_waitcnt lgkmcnt(0)\n\t"
      "s_lshl_b32 s37, s46, 7\n\t"
      "s_add_u32 s58, s54, s37\n\t"
      "s_addc_u32 s59, s55, 0\n\t"
      "s_add_u32 m0, s39, 65536\n\t"
      "s_nop 0\n\t"
      "global_load_lds_dwordx4 v228, s[58:59]\n\t"
      "global_load_lds_dwordx4 v229, s[58:59] offset:1024\n\t"
      "global_load_lds_dwordx4 v230, s[58:59] offset:2048\n\t"
      "global_load_lds_dwordx4 v231, s[58:59] offset:3072\n\t"
      "s_waitcnt vmcnt(12)\n\t"
      "ds_read_b64_tr_b16 v[48:49], v236 offset:32768\n\t"
      "ds_read_b64_tr_b16 v[50:51], v236 offset:34816\n\t"
      "ds_read_b64_tr_b16 v[52:53], v237 offset:32768\n\t"
      "ds_read_b64_tr_b16 v[54:55], v237 offset:34816\n\t"
      "ds_read_b64_tr_b16 v[56:57], v238 offset:32768\n\t"
      "ds_read_b64_tr_b16 v[58:59], v238 offset:34816\n\t"
      "ds_read_b64_tr_b16 v[60:61], v239 offset:32768\n\t"
      "ds_read_b64_tr_b16 v[62:63], v239 offset:34816\n\t"
      "s_waitcnt vmcnt(8)\n\t"
      "ds_read_b64_tr_b16 v[64:65], v232\n\t"
      "ds_read_b64_tr_b16 v[66:67], v232 offset:2048\n\t"
      "ds_read_b64_tr_b16 v[68:69], v233\n\t"
      "ds_read_b64_tr_b16 v[70:71], v233 offset:2048\n\t"
      "ds_read_b64_tr_b16 v[72:73], v234\n\t"
      "ds_read_b64_tr_b16 v[74:75], v234 offset:2048\n\t"
      "ds_read_b64_tr_b16 v[76:77], v235\n\t"
      "ds_read_b64_tr_b16 v[78:79], v235 offset:2048\n\t"
      "s_cmp_lg_u32 s24, 3\n\t"
      "s_cbranch_scc1 LF5B_%=\n\t"
      "s_waitcnt vmcnt(8)\n\t"
      "v_cmp_ne_u64_e32 vcc, s[22:23], v[138:139]\n\t"
      "s_cbranch_vccnz LF5R_%=\n\t"
      "LF5B_%=:\n\t"
      "s_barrier\n\t"
      "s_cmp_lg_u32 s24, 3\n\t"
      "s_cbranch_scc1 HF4_%=\n\t"
      "s_lshl3_add_u32 s34, s25, s26\n\t"
      "s_lshl_b32 s34, s34, 7\n\t"
      "s_add_u32 s34, s34, 98304\n\t"
      "v_mov_b32_e32 v146, s34\n\t"
      "v_mov_b32_e32 v144, s22\n\t"
      "v_mov_b32_e32 v145, s23\n\t"
      "s_mov_b64 exec, 1\n\t"
      "global_store_dwordx2 v146, v[144:145], s[60:61] sc1\n\t"
      "s_mov_b64 exec, -1\n\t"
      "HF4_%=:\n\t"
      "s_cmp_lg_u32 s24, 7\n\t"
      "s_cbranch_scc1 PG0_%=\n\t"
      "global_load_dword v251, v253, s[52:53] offset:252 sc1\n\t"
      "PG0_%=:\n\t"
      "s_waitcnt vmcnt(4)\n\t"
      "ds_read_b64_tr_b16 v[80:81], v232 offset:32768\n\t"
      "ds_read_b64_tr_b16 v[82:83], v232 offset:34816\n\t"
      "ds_read_b64_tr_b16 v[84:85], v233 offset:32768\n\t"
      "ds_read_b64_tr_b16 v[86:87], v233 offset:34816\n\t"
      "ds_read_b64_tr_b16 v[88:89], v234 offset:32768\n\t"
      "ds_read_b64_tr_b16 v[90:91], v234 offset:34816\n\t"
      "ds_read_b64_tr_b16 v[92:93], v235 offset:32768\n\t"
      "ds_read_b64_tr_b16 v[94:95], v235 offset:34816\n\t"
      "s_waitcnt vmcnt(0)\n\t"
      "ds_read_b64_tr_b16 v[96:97], v236\n\t"
      "ds_read_b64_tr_b16 v[98:99], v236 offset:2048\n\t"
      "ds_read_b64_tr_b16 v[100:101], v237\n\t"
      "ds_read_b64_tr_b16 v[102:103], v237 offset:2048\n\t"
      "ds_read_b64_tr_b16 v[104:105], v238\n\t"
      "ds_read_b64_tr_b16 v[106:107], v238 offset:2048\n\t"
      "ds_read_b64_tr_b16 v[108:109], v239\n\t"
      "ds_read_b64_tr_b16 v[110:111], v239 offset:2048\n\t"
      "v_lshlrev_b32_e32 v249, 16, v249\n\t"
      "s_and_b32 s34, s29, 30\n\t"
      "s_lshl_b32 s34, s34, 10\n\t"
      "s_add_u32 s34, s34, 98304\n\t"
      "v_bfe_u32 v143, v244, 5, 2\n\t"
      "v_add_u32_e32 v143, 3, v143\n\t"
      "v_writelane_b32 v143, s32, 0\n\t"
      "v_writelane_b32 v143, s32, 1\n\t"
      "v_lshl_add_u32 v143, v143, 7, s34\n\t"
      "v_and_b32_e32 v142, 16, v244\n\t"
      "v_lshl_add_u32 v143, v142, 6, v143\n\t"
      "s_waitcnt vmcnt(0) lgkmcnt(0)\n\t"
      "s_cmp_lg_u32 s24, 7\n\t"
      "s_cbranch_scc1 PGB_%=\n\t"
      "v_cmp_eq_u32_e32 vcc, -1, v251\n\t"
      "s_cbranch_vccnz PGR_%=\n\t"
      "PGB_%=:\n\t"
      "s_barrier\n\t"
      "s_cmp_lg_u32 s24, 3\n\t"
      "s_cbranch_scc1 SK25_%=\n\t"
      "global_load_dwordx2 v[240:241], v143, s[60:61] sc1\n\t"
      "SK25_%=:\n\t"
      "v_mov_b32_e32 v242, v143\n\t"
      "s_add_u32 s58, s64, s40\n\t"
      "s_addc_u32 s59, s65, 0\n\t"
      "global_load_dwordx4 v[128:131], v244, s[58:59] sc1\n\t"
      "s_add_u32 s58, s64, s41\n\t"
      "s_addc_u32 s59, s65, 0\n\t"
      "global_load_dwordx4 v[132:135], v244, s[58:59] sc1\n\t"
      "s_add_u32 s58, s64, s42\n\t"
      "s_addc_u32 s59, s65, 0\n\t"
      "global_load_dwordx4 v[136:139], v244, s[58:59] sc1\n\t"
      "s_add_u32 s58, s64, s43\n\t"
      "s_addc_u32 s59, s65, 0\n\t"
      "global_load_dwordx4 v[140:143], v244, s[58:59] sc1\n\t"
      "s_add_u32 s58, s64, s44\n\t"
      "s_addc_u32 s59, s65, 0\n\t"
      "global_load_dwordx4 v[144:147], v244, s[58:59] sc1\n\t"
      "s_add_u32 s58, s64, s45\n\t"
      "s_addc_u32 s59, s65, 0\n\t"
      "global_load_dwordx4 v[148:151], v244, s[58:59] sc1\n\t"
      "s_add_u32 s58, s64, s46\n\t"
      "s_addc_u32 s59, s65, 0\n\t"
      "global_load_dwordx4 v[152:155], v244, s[58:59] sc1\n\t"
      "v_mov_b32_e32 v254, v249\n\t"
      "s_waitcnt vmcnt(6)\n\t"
      "v_mfma_f32_16x16x32_bf16 v[196:199], v[128:131], v[0:3], 0\n\t"
      "v_mfma_f32_16x16x32_bf16 v[200:203], v[128:131], v[4:7], 0\n\t"
      "v_mfma_f32_16x16x32_bf16 v[204:207], v[128:131], v[8:11], 0\n\t"
      "v_mfma_f32_16x16x32_bf16 v[208:211], v[128:131], v[12:15], 0\n\t"
      "s_waitcnt vmcnt(5)\n\t"
      "v_mfma_f32_16x16x32_bf16 v[196:199], v[132:135], v[16:19], v[196:199]\n\t"
      "v_mfma_f32_16x16x32_bf16 v[200:203], v[132:135], v[20:23], v[200:203]\n\t"
      "v_mfma_f32_16x16x32_bf16 v[204:207], v[132:135], v[24:27], v[204:207]\n\t"
      "s_cmp_lg_u32 s24, 3\n\t"
      "s_cbranch_scc1 LF8B_%=\n\t"
      "v_cmp_ne_u64_e32 vcc, s[22:23], v[240:241]\n\t"
      "s_cbranch_vccnz LF8R_%=\n\t"
      "LF8B_%=:\n\t"
      "s_barrier\n\t"
      "v_mfma_f32_16x16x32_bf16 v[208:211], v[132:135], v[28:31], v[208:211]\n\t"
      "global_load_dword v115, v248, s[50:51] offset:-4096 sc1\n\t"
      "global_load_dword v116, v248, s[50:51] offset:-2048 sc1\n\t"
      "s_waitcnt vmcnt(6)\n\t"
      "v_mfma_f32_16x16x32_bf16 v[196:199], v[136:139], v[32:35], v[196:199]\n\t"
      "global_load_dword v117, v248, s[50:51] sc1\n\t"
      "global_load_dword v118, v248, s[50:51] offset:2048 sc1\n\t"
      "s_add_u32 s58, s66, s40\n\t"
      "s_addc_u32 s59, s67, 0\n\t"
      "global_load_dwordx4 v[156:159], v244, s[58:59] sc1\n\t"
      "v_mfma_f32_16x16x32_bf16 v[200:203], v[136:139], v[36:39], v[200:203]\n\t"
      "s_sub_u32 s34, 16, s32\n\t"
      "s_lshl_b32 s34, s34, 11\n\t"
      "s_add_u32 s50, s50, s34\n\t"
      "s_addc_u32 s51, s51, 0\n\t"
      "global_load_dword v112, v248, s[50:51] offset:-4096 sc1\n\t"
      "global_load_dword v113, v248, s[50:51] offset:-2048 sc1\n\t"
      "v_mfma_f32_16x16x32_bf16 v[204:207], v[136:139], v[40:43], v[204:207]\n\t"
      "global_load_dword v114, v248, s[50:51] sc1\n\t"
      "v_mfma_f32_16x16x32_bf16 v[208:211], v[136:139], v[44:47], v[208:211]\n\t"
      "s_waitcnt vmcnt(11)\n\t"
      "v_mfma_f32_16x16x32_bf16 v[196:199], v[140:143], v[48:51], v[196:199]\n\t"
      "s_add_u32 s58, s66, s41\n\t"
      "s_addc_u32 s59, s67, 0\n\t"
      "global_load_dwordx4 v[160:163], v244, s[58:59] sc1\n\t"
      "v_mfma_f32_16x16x32_bf16 v[200:203], v[140:143], v[52:55], v[200:203]\n\t"
      "v_mfma_f32_16x16x32_bf16 v[204:207], v[140:143], v[56:59], v[204:207]\n\t"
      "v_mfma_f32_16x16x32_bf16 v[208:211], v[140:143], v[60:63], v[208:211]\n\t"
      "s_waitcnt vmcnt(11)\n\t"
      "v_mfma_f32_16x16x32_bf16 v[196:199], v[144:147], v[64:67], v[196:199]\n\t"
      "s_add_u32 s58, s66, s42\n\t"
      "s_addc_u32 s59, s67, 0\n\t"
      "global_load_dwordx4 v[164:167], v244, s[58:59] sc1\n\t"
      "v_mfma_f32_16x16x32_bf16 v[200:203], v[144:147], v[68:71], v[200:203]\n\t"
      "v_mfma_f32_16x16x32_bf16 v[204:207], v[144:147], v[72:75], v[204:207]\n\t"
      "v_mfma_f32_16x16x32_bf16 v[208:211], v[144:147], v[76:79], v[208:211]\n\t"
      "s_waitcnt vmcnt(11)\n\t"
      "v_mfma_f32_16x16x32_bf16 v[196:199], v[148:151], v[80:83], v[196:199]\n\t"
      "s_add_u32 s58, s66, s43\n\t"
      "s_addc_u32 s59, s67, 0\n\t"
      "global_load_dwordx4 v[168:171], v244, s[58:59] sc1\n\t"
      "v_mfma_f32_16x16x32_bf16 v[200:203], v[148:151], v[84:87], v[200:203]\n\t"
      "v_mfma_f32_16x16x32_bf16 v[204:207], v[148:151], v[88:91], v[204:207]\n\t"
      "v_mfma_f32_16x16x32_bf16 v[208:211], v[148:151], v[92:95], v[208:211]\n\t"
      "s_waitcnt vmcnt(11)\n\t"
      "v_mfma_f32_16x16x32_bf16 v[196:199], v[152:155], v[96:99], v[196:199]\n\t"
      "s_add_u32 s58, s66, s44\n\t"
      "s_addc_u32 s59, s67, 0\n\t"
      "global_load_dwordx4 v[172:175], v244, s[58:59] sc1\n\t"
      "v_mfma_f32_16x16x32_bf16 v[200:203], v[152:155], v[100:103], v[200:203]\n\t"
      "v_mfma_f32_16x16x32_bf16 v[204:207], v[152:155], v[104:107], v[204:207]\n\t"
      "v_mfma_f32_16x16x32_bf16 v[208:211], v[152:155], v[108:111], v[208:211]\n\t"
      "s_nop 1\n\t"
      "v_add_f32_e32 v251, v196, v197\n\t"
      "v_add_f32_e32 v251, v251, v198\n\t"
      "v_cmp_u_f32_e32 vcc, v251, v199\n\t"
      "s_cbranch_vccnz LA40R_%=\n\t"
      "LA40B_%=:\n\t"
      "ds_write_b128 v245, v[196:199]\n\t"
      "s_waitcnt vmcnt(7)\n\t"
      "v_mfma_f32_16x16x32_bf16 v[212:215], v[156:159], v[0:3], 0\n\t"
      "ds_write_b128 v245, v[200:203] offset:1024\n\t"
      "s_add_u32 s58, s66, s45\n\t"
      "s_addc_u32 s59, s67, 0\n\t"
      "global_load_dwordx4 v[176:179], v244, s[58:59] sc1\n\t"
      "v_mfma_f32_16x16x32_bf16 v[216:219], v[156:159], v[4:7], 0\n\t"
      "ds_write_b128 v245, v[204:207] offset:2048\n\t"
      "v_mfma_f32_16x16x32_bf16 v[220:223], v[156:159], v[8:11], 0\n\t"
      "ds_write_b128 v245, v[208:211] offset:3072\n\t"
      "v_mfma_f32_16x16x32_bf16 v[224:227], v[156:159], v[12:15], 0\n\t"
      "s_waitcnt vmcnt(4)\n\t"
      "v_mfma_f32_16x16x32_bf16 v[212:215], v[160:163], v[16:19], v[212:215]\n\t"
      "s_add_u32 s58, s66, s46\n\t"
      "s_addc_u32 s59, s67, 0\n\t"
      "global_load_dwordx4 v[180:183], v244, s[58:59] sc1\n\t"
      "v_mfma_f32_16x16x32_bf16 v[216:219], v[160:163], v[20:23], v[216:219]\n\t"
      "v_mfma_f32_16x16x32_bf16 v[220:223], v[160:163], v[24:27], v[220:223]\n\t"
      "s_waitcnt lgkmcnt(0)\n\t"
      "s_barrier\n\t"
      "ds_read_b64 v[228:229], v246\n\t"
      "ds_read_b64 v[230:231], v246 offset:4096\n\t"
      "s_add_u32 s58, s68, s40\n\t"
      "s_addc_u32 s59, s69, 0\n\t"
      "global_load_dwordx4 v[184:187], v244, s[58:59] sc1\n\t"
      "v_mfma_f32_16x16x32_bf16 v[224:227], v[160:163], v[28:31], v[224:227]\n\t"
      "ds_read_b64 v[232:233], v246 offset:8192\n\t"
      "ds_read_b64 v[234:235], v246 offset:12288\n\t"
      "s_waitcnt vmcnt(5)\n\t"
      "v_mfma_f32_16x16x32_bf16 v[212:215], v[164:167], v[32:35], v[212:215]\n\t"
      "ds_read_b64 v[236:237], v246 offset:16384\n\t"
      "ds_read_b64 v[238:239], v246 offset:20480\n\t"
      "v_mfma_f32_16x16x32_bf16 v[216:219], v[164:167], v[36:39], v[216:219]\n\t"
      "ds_read_b64 v[240:241], v246 offset:24576\n\t"
      "ds_read_b64 v[242:243], v246 offset:28672\n\t"
      "v_lshlrev_b32_e32 v248, 16, v112\n\t"
      "v_mfma_f32_16x16x32_bf16 v[220:223], v[164:167], v[40:43], v[220:223]\n\t"
      "v_and_b32_e32 v249, 0xffff0000, v112\n\t"
      "s_add_u32 s58, s68, s41\n\t"
      "s_addc_u32 s59, s69, 0\n\t"
      "global_load_dwordx4 v[188:191], v244, s[58:59] sc1\n\t"
      "v_mfma_f32_16x16x32_bf16 v[224:227], v[164:167], v[44:47], v[224:227]\n\t"
      "v_add_f32_e32 v248, v254, v248\n\t"
      "v_add_f32_e32 v249, v254, v249\n\t"
      "s_waitcnt vmcnt(5)\n\t"
      "v_mfma_f32_16x16x32_bf16 v[212:215], v[168:171], v[48:51], v[212:215]\n\t"
      "s_waitcnt lgkmcnt(4)\n\t"
      "v_mfma_f32_16x16x32_bf16 v[216:219], v[168:171], v[52:55], v[216:219]\n\t"
      "v_add_f32_e32 v228, v228, v230\n\t"
      "v_add_f32_e32 v232, v232, v234\n\t"
      "v_mfma_f32_16x16x32_bf16 v[220:223], v[168:171], v[56:59], v[220:223]\n\t"
      "v_add_f32_e32 v229, v229, v231\n\t"
      "s_add_u32 s58, s68, s42\n\t"
      "s_addc_u32 s59, s69, 0\n\t"
      "global_load_dwordx4 v[192:195], v244, s[58:59] sc1\n\t"
      "v_mfma_f32_16x16x32_bf16 v[224:227], v[168:171], v[60:63], v[224:227]\n\t"
      "v_add_f32_e32 v233, v233, v235\n\t"
      "s_waitcnt lgkmcnt(0)\n\t"
      "s_waitcnt vmcnt(5)\n\t"
      "v_mfma_f32_16x16x32_bf16 v[212:215], v[172:175], v[64:67], v[212:215]\n\t"
      "v_add_f32_e32 v236, v236, v238\n\t"
      "v_mfma_f32_16x16x32_bf16 v[216:219], v[172:175], v[68:71], v[216:219]\n\t"
      "v_add_f32_e32 v240, v240, v242\n\t"
      "v_add_f32_e32 v237, v237, v239\n\t"
      "v_mfma_f32_16x16x32_bf16 v[220:223], v[172:175], v[72:75], v[220:223]\n\t"
      "v_add_f32_e32 v241, v241, v243\n\t"
      "s_add_u32 s58, s68, s43\n\t"
      "s_addc_u32 s59, s69, 0\n\t"
      "global_load_dwordx4 v[128:131], v244, s[58:59] sc1\n\t"
      "v_mfma_f32_16x16x32_bf16 v[224:227], v[172:175], v[76:79], v[224:227]\n\t"
      "v_add_f32_e32 v228, v228, v232\n\t"
      "v_add_f32_e32 v236, v236, v240\n\t"
      "s_waitcnt vmcnt(5)\n\t"
      "v_mfma_f32_16x16x32_bf16 v[212:215], v[176:179], v[80:83], v[212:215]\n\t"
      "v_add_f32_e32 v229, v229, v233\n\t"
      "v_mfma_f32_16x16x32_bf16 v[216:219], v[176:179], v[84:87], v[216:219]\n\t"
      "v_add_f32_e32 v237, v237, v241\n\t"
      "v_add_f32_e32 v228, v228, v248\n\t"
      "v_mfma_f32_16x16x32_bf16 v[220:223], v[176:179], v[88:91], v[220:223]\n\t"
      "v_add_f32_e32 v229, v229, v249\n\t"
      "s_add_u32 s58, s68, s44\n\t"
      "s_addc_u32 s59, s69, 0\n\t"
      "global_load_dwordx4 v[132:135], v244, s[58:59] sc1\n\t"
      "v_mfma_f32_16x16x32_bf16 v[224:227], v[176:179], v[92:95], v[224:227]\n\t"
      "v_add_f32_e32 v228, v228, v236\n\t"
      "v_add_f32_e32 v229, v229, v237\n\t"
      "s_waitcnt vmcnt(5)\n\t"
      "v_mfma_f32_16x16x32_bf16 v[212:215], v[180:183], v[96:99], v[212:215]\n\t"
      "v_cvt_pk_bf16_f32 v232, v228, v229\n\t"
      "v_mfma_f32_16x16x32_bf16 v[216:219], v[180:183], v[100:103], v[216:219]\n\t"
      "s_mov_b32 vcc_lo, 0xeeeeeeee\n\t"
      "s_mov_b32 vcc_hi, 0xeeeeeeee\n\t"
      "v_cndmask_b32_dpp v120, v232, v120, vcc quad_perm:[0,0,0,0] row_mask:0xf bank_mask:0xf\n\t"
      "v_cndmask_b32_dpp v121, v232, v121, vcc quad_perm:[1,1,1,1] row_mask:0xf bank_mask:0xf\n\t"
      "v_cndmask_b32_dpp v122, v232, v122, vcc quad_perm:[2,2,2,2] row_mask:0xf bank_mask:0xf\n\t"
      "v_cndmask_b32_dpp v123, v232, v123, vcc quad_perm:[3,3,3,3] row_mask:0xf bank_mask:0xf\n\t"
      "v_mfma_f32_16x16x32_bf16 v[220:223], v[180:183], v[104:107], v[220:223]\n\t"
      "s_add_u32 s58, s68, s45\n\t"
      "s_addc_u32 s59, s69, 0\n\t"
      "global_load_dwordx4 v[136:139], v244, s[58:59] sc1\n\t"
      "v_mfma_f32_16x16x32_bf16 v[224:227], v[180:183], v[108:111], v[224:227]\n\t"
      "v_add_f32_e32 v251, v212, v213\n\t"
      "v_add_f32_e32 v251, v251, v214\n\t"
      "v_cmp_u_f32_e32 vcc, v251, v215\n\t"
      "s_cbranch_vccnz LA41R_%=\n\t"
      "LA41B_%=:\n\t"
      "ds_write_b128 v245, v[212:215] offset:32768\n\t"
      "s_waitcnt vmcnt(5)\n\t"
      "v_mfma_f32_16x16x32_bf16 v[196:199], v[184:187], v[0:3], 0\n\t"
      "ds_write_b128 v245, v[216:219] offset:33792\n\t"
      "v_mfma_f32_16x16x32_bf16 v[200:203], v[184:187], v[4:7], 0\n\t"
      "ds_write_b128 v245, v[220:223] offset:34816\n\t"
      "v_mfma_f32_16x16x32_bf16 v[204:207], v[184:187], v[8:11], 0\n\t"
      "ds_write_b128 v245, v[224:227] offset:35840\n\t"
      "s_add_u32 s58, s68, s46\n\t"
      "s_addc_u32 s59, s69, 0\n\t"
      "global_load_dwordx4 v[140:143], v244, s[58:59] sc1\n\t"
      "v_mfma_f32_16x16x32_bf16 v[208:211], v[184:187], v[12:15], 0\n\t"
      "s_waitcnt vmcnt(5)\n\t"
      "v_mfma_f32_16x16x32_bf16 v[196:199], v[188:191], v[16:19], v[196:199]\n\t"
      "v_mfma_f32_16x16x32_bf16 v[200:203], v[188:191], v[20:23], v[200:203]\n\t"
      "v_mfma_f32_16x16x32_bf16 v[204:207], v[188:191], v[24:27], v[204:207]\n\t"
      "s_waitcnt lgkmcnt(0)\n\t"
      "s_barrier\n\t"
      "ds_read_b64 v[228:229], v246 offset:32768\n\t"
      "ds_read_b64 v[230:231], v246 offset:36864\n\t"
      "s_add_u32 s58, s70, s40\n\t"
      "s_addc_u32 s59, s71, 0\n\t"
      "global_load_dwordx4 v[144:147], v244, s[58:59] sc1\n\t"
      "v_mfma_f32_16x16x32_bf16 v[208:211], v[188:191], v[28:31], v[208:211]\n\t"
      "ds_read_b64 v[232:233], v246 offset:40960\n\t"
      "ds_read_b64 v[234:235], v246 offset:45056\n\t"
      "s_waitcnt vmcnt(5)\n\t"
      "v_mfma_f32_16x16x32_bf16 v[196:199], v[192:195], v[32:35], v[196:199]\n\t"
      "ds_read_b64 v[236:237], v246 offset:49152\n\t"
      "ds_read_b64 v[238:239], v246 offset:53248\n\t"
      "v_mfma_f32_16x16x32_bf16 v[200:203], v[192:195], v[36:39], v[200:203]\n\t"
      "ds_read_b64 v[240:241], v246 offset:57344\n\t"
      "ds_read_b64 v[242:243], v246 offset:61440\n\t"
      "v_lshlrev_b32_e32 v248, 16, v113\n\t"
      "v_mfma_f32_16x16x32_bf16 v[204:207], v[192:195], v[40:43], v[204:207]\n\t"
      "v_and_b32_e32 v249, 0xffff0000, v113\n\t"
      "s_add_u32 s58, s70, s41\n\t"
      "s_addc_u32 s59, s71, 0\n\t"
      "global_load_dwordx4 v[148:151], v244, s[58:59] sc1\n\t"
      "v_mfma_f32_16x16x32_bf16 v[208:211], v[192:195], v[44:47], v[208:211]\n\t"
      "v_add_f32_e32 v248, v254, v248\n\t"
      "v_add_f32_e32 v249, v254, v249\n\t"
      "s_waitcnt vmcnt(5)\n\t"
      "v_mfma_f32_16x16x32_bf16 v[196:199], v[128:131], v[48:51], v[196:199]\n\t"
      "s_waitcnt lgkmcnt(4)\n\t"
      "v_mfma_f32_16x16x32_bf16 v[200:203], v[128:131], v[52:55], v[200:203]\n\t"
      "v_add_f32_e32 v228, v228, v230\n\t"
      "v_add_f32_e32 v232, v232, v234\n\t"
      "v_mfma_f32_16x16x32_bf16 v[204:207], v[128:131], v[56:59], v[204:207]\n\t"
      "v_add_f32_e32 v229, v229, v231\n\t"
      "s_add_u32 s58, s70, s42\n\t"
      "s_addc_u32 s59, s71, 0\n\t"
      "global_load_dwordx4 v[152:155], v244, s[58:59] sc1\n\t"
      "v_mfma_f32_16x16x32_bf16 v[208:211], v[128:131], v[60:63], v[208:211]\n\t"
      "v_add_f32_e32 v233, v233, v235\n\t"
      "s_waitcnt lgkmcnt(0)\n\t"
      "s_waitcnt vmcnt(5)\n\t"
      "v_mfma_f32_16x16x32_bf16 v[196:199], v[132:135], v[64:67], v[196:199]\n\t"
      "v_add_f32_e32 v236, v236, v238\n\t"
      "v_mfma_f32_16x16x32_bf16 v[200:203], v[132:135], v[68:71], v[200:203]\n\t"
      "v_add_f32_e32 v240, v240, v242\n\t"
      "v_add_f32_e32 v237, v237, v239\n\t"
      "v_mfma_f32_16x16x32_bf16 v[204:207], v[132:135], v[72:75], v[204:207]\n\t"
      "v_add_f32_e32 v241, v241, v243\n\t"
      "s_add_u32 s58, s70, s43\n\t"
      "s_addc_u32 s59, s71, 0\n\t"
      "global_load_dwordx4 v[156:159], v244, s[58:59] sc1\n\t"
      "v_mfma_f32_16x16x32_bf16 v[208:211], v[132:135], v[76:79], v[208:211]\n\t"
      "v_add_f32_e32 v228, v228, v232\n\t"
      "v_add_f32_e32 v236, v236, v240\n\t"
      "s_waitcnt vmcnt(5)\n\t"
      "v_mfma_f32_16x16x32_bf16 v[196:199], v[136:139], v[80:83], v[196:199]\n\t"
      "v_add_f32_e32 v229, v229, v233\n\t"
      "v_mfma_f32_16x16x32_bf16 v[200:203], v[136:139], v[84:87], v[200:203]\n\t"
      "v_add_f32_e32 v237, v237, v241\n\t"
      "v_add_f32_e32 v228, v228, v248\n\t"
      "v_mfma_f32_16x16x32_bf16 v[204:207], v[136:139], v[88:91], v[204:207]\n\t"
      "v_add_f32_e32 v229, v229, v249\n\t"
      "s_add_u32 s58, s70, s44\n\t"
      "s_addc_u32 s59, s71, 0\n\t"
      "global_load_dwordx4 v[160:163], v244, s[58:59] sc1\n\t"
      "v_mfma_f32_16x16x32_bf16 v[208:211], v[136:139], v[92:95], v[208:211]\n\t"
      "v_add_f32_e32 v228, v228, v236\n\t"
      "v_add_f32_e32 v229, v229, v237\n\t"
      "s_waitcnt vmcnt(5)\n\t"
      "v_mfma_f32_16x16x32_bf16 v[196:199], v[140:143], v[96:99], v[196:199]\n\t"
      "v_cvt_pk_bf16_f32 v232, v228, v229\n\t"
      "v_mfma_f32_16x16x32_bf16 v[200:203], v[140:143], v[100:103], v[200:203]\n\t"
      "s_mov_b32 vcc_lo, 0xdddddddd\n\t"
      "s_mov_b32 vcc_hi, 0xdddddddd\n\t"
      "v_cndmask_b32_dpp v120, v232, v120, vcc quad_perm:[0,0,0,0] row_mask:0xf bank_mask:0xf\n\t"
      "v_cndmask_b32_dpp v121, v232, v121, vcc quad_perm:[1,1,1,1] row_mask:0xf bank_mask:0xf\n\t"
      "v_cndmask_b32_dpp v122, v232, v122, vcc quad_perm:[2,2,2,2] row_mask:0xf bank_mask:0xf\n\t"
      "v_cndmask_b32_dpp v123, v232, v123, vcc quad_perm:[3,3,3,3] row_mask:0xf bank_mask:0xf\n\t"
      "v_mfma_f32_16x16x32_bf16 v[204:207], v[140:143], v[104:107], v[204:207]\n\t"
      "s_add_u32 s58, s70, s45\n\t"
      "s_addc_u32 s59, s71, 0\n\t"
      "global_load_dwordx4 v[164:167], v244, s[58:59] sc1\n\t"
      "v_mfma_f32_16x16x32_bf16 v[208:211], v[140:143], v[108:111], v[208:211]\n\t"
      "v_add_f32_e32 v251, v196, v197\n\t"
      "v_add_f32_e32 v251, v251, v198\n\t"
      "v_cmp_u_f32_e32 vcc, v251, v199\n\t"
      "s_cbranch_vccnz LA42R_%=\n\t"
      "LA42B_%=:\n\t"
      "ds_write_b128 v245, v[196:199]\n\t"
      "s_waitcnt vmcnt(5)\n\t"
      "v_mfma_f32_16x16x32_bf16 v[212:215], v[144:147], v[0:3], 0\n\t"
      "ds_write_b128 v245, v[200:203] offset:1024\n\t"
      "v_mfma_f32_16x16x32_bf16 v[216:219], v[144:147], v[4:7], 0\n\t"
      "ds_write_b128 v245, v[204:207] offset:2048\n\t"
      "v_mfma_f32_16x16x32_bf16 v[220:223], v[144:147], v[8:11], 0\n\t"
      "ds_write_b128 v245, v[208:211] offset:3072\n\t"
      "s_add_u32 s58, s70, s46\n\t"
      "s_addc_u32 s59, s71, 0\n\t"
      "global_load_dwordx4 v[168:171], v244, s[58:59] sc1\n\t"
      "v_mfma_f32_16x16x32_bf16 v[224:227], v[144:147], v[12:15], 0\n\t"
      "s_waitcnt vmcnt(5)\n\t"
      "v_mfma_f32_16x16x32_bf16 v[212:215], v[148:151], v[16:19], v[212:215]\n\t"
      "v_mfma_f32_16x16x32_bf16 v[216:219], v[148:151], v[20:23], v[216:219]\n\t"
      "v_mfma_f32_16x16x32_bf16 v[220:223], v[148:151], v[24:27], v[220:223]\n\t"
      "s_waitcnt lgkmcnt(0)\n\t"
      "s_barrier\n\t"
      "ds_read_b64 v[228:229], v246\n\t"
      "ds_read_b64 v[230:231], v246 offset:4096\n\t"
      "s_add_u32 s58, s72, s40\n\t"
      "s_addc_u32 s59, s73, 0\n\t"
      "global_load_dwordx4 v[172:175], v244, s[58:59] sc1\n\t"
      "v_mfma_f32_16x16x32_bf16 v[224:227], v[148:151], v[28:31], v[224:227]\n\t"
      "ds_read_b64 v[232:233], v246 offset:8192\n\t"
      "ds_read_b64 v[234:235], v246 offset:12288\n\t"
      "s_waitcnt vmcnt(5)\n\t"
      "v_mfma_f32_16x16x32_bf16 v[212:215], v[152:155], v[32:35], v[212:215]\n\t"
      "ds_read_b64 v[236:237], v246 offset:16384\n\t"
      "ds_read_b64 v[238:239], v246 offset:20480\n\t"
      "v_mfma_f32_16x16x32_bf16 v[216:219], v[152:155], v[36:39], v[216:219]\n\t"
      "ds_read_b64 v[240:241], v246 offset:24576\n\t"
      "ds_read_b64 v[242:243], v246 offset:28672\n\t"
      "v_lshlrev_b32_e32 v248, 16, v114\n\t"
      "v_mfma_f32_16x16x32_bf16 v[220:223], v[152:155], v[40:43], v[220:223]\n\t"
      "v_and_b32_e32 v249, 0xffff0000, v114\n\t"
      "s_add_u32 s58, s72, s41\n\t"
      "s_addc_u32 s59, s73, 0\n\t"
      "global_load_dwordx4 v[176:179], v244, s[58:59] sc1\n\t"
      "v_mfma_f32_16x16x32_bf16 v[224:227], v[152:155], v[44:47], v[224:227]\n\t"
      "v_add_f32_e32 v248, v254, v248\n\t"
      "v_add_f32_e32 v249, v254, v249\n\t"
      "s_waitcnt vmcnt(5)\n\t"
      "v_mfma_f32_16x16x32_bf16 v[212:215], v[156:159], v[48:51], v[212:215]\n\t"
      "s_waitcnt lgkmcnt(4)\n\t"
      "v_mfma_f32_16x16x32_bf16 v[216:219], v[156:159], v[52:55], v[216:219]\n\t"
      "v_add_f32_e32 v228, v228, v230\n\t"
      "v_add_f32_e32 v232, v232, v234\n\t"
      "v_mfma_f32_16x16x32_bf16 v[220:223], v[156:159], v[56:59], v[220:223]\n\t"
      "v_add_f32_e32 v229, v229, v231\n\t"
      "s_add_u32 s58, s72, s42\n\t"
      "s_addc_u32 s59, s73, 0\n\t"
      "global_load_dwordx4 v[180:183], v244, s[58:59] sc1\n\t"
      "v_mfma_f32_16x16x32_bf16 v[224:227], v[156:159], v[60:63], v[224:227]\n\t"
      "v_add_f32_e32 v233, v233, v235\n\t"
      "s_waitcnt lgkmcnt(0)\n\t"
      "s_waitcnt vmcnt(5)\n\t"
      "v_mfma_f32_16x16x32_bf16 v[212:215], v[160:163], v[64:67], v[212:215]\n\t"
      "v_add_f32_e32 v236, v236, v238\n\t"
      "v_mfma_f32_16x16x32_bf16 v[216:219], v[160:163], v[68:71], v[216:219]\n\t"
      "v_add_f32_e32 v240, v240, v242\n\t"
      "v_add_f32_e32 v237, v237, v239\n\t"
      "v_mfma_f32_16x16x32_bf16 v[220:223], v[160:163], v[72:75], v[220:223]\n\t"
      "v_add_f32_e32 v241, v241, v243\n\t"
      "s_add_u32 s58, s72, s43\n\t"
      "s_addc_u32 s59, s73, 0\n\t"
      "global_load_dwordx4 v[184:187], v244, s[58:59] sc1\n\t"
      "v_mfma_f32_16x16x32_bf16 v[224:227], v[160:163], v[76:79], v[224:227]\n\t"
      "v_add_f32_e32 v228, v228, v232\n\t"
      "v_add_f32_e32 v236, v236, v240\n\t"
      "s_waitcnt vmcnt(5)\n\t"
      "v_mfma_f32_16x16x32_bf16 v[212:215], v[164:167], v[80:83], v[212:215]\n\t"
      "v_add_f32_e32 v229, v229, v233\n\t"
      "v_mfma_f32_16x16x32_bf16 v[216:219], v[164:167], v[84:87], v[216:219]\n\t"
      "v_add_f32_e32 v237, v237, v241\n\t"
      "v_add_f32_e32 v228, v228, v248\n\t"
      "v_mfma_f32_16x16x32_bf16 v[220:223], v[164:167], v[88:91], v[220:223]\n\t"
      "v_add_f32_e32 v229, v229, v249\n\t"
      "s_add_u32 s58, s72, s44\n\t"
      "s_addc_u32 s59, s73, 0\n\t"
      "global_load_dwordx4 v[188:191], v244, s[58:59] sc1\n\t"
      "v_mfma_f32_16x16x32_bf16 v[224:227], v[164:167], v[92:95], v[224:227]\n\t"
      "v_add_f32_e32 v228, v228, v236\n\t"
      "v_add_f32_e32 v229, v229, v237\n\t"
      "s_waitcnt vmcnt(5)\n\t"
      "v_mfma_f32_16x16x32_bf16 v[212:215], v[168:171], v[96:99], v[212:215]\n\t"
      "v_cvt_pk_bf16_f32 v232, v228, v229\n\t"
      "v_mfma_f32_16x16x32_bf16 v[216:219], v[168:171], v[100:103], v[216:219]\n\t"
      "s_mov_b32 vcc_lo, 0xbbbbbbbb\n\t"
      "s_mov_b32 vcc_hi, 0xbbbbbbbb\n\t"
      "v_cndmask_b32_dpp v120, v232, v120, vcc quad_perm:[0,0,0,0] row_mask:0xf bank_mask:0xf\n\t"
      "v_cndmask_b32_dpp v121, v232, v121, vcc quad_perm:[1,1,1,1] row_mask:0xf bank_mask:0xf\n\t"
      "v_cndmask_b32_dpp v122, v232, v122, vcc quad_perm:[2,2,2,2] row_mask:0xf bank_mask:0xf\n\t"
      "v_cndmask_b32_dpp v123, v232, v123, vcc quad_perm:[3,3,3,3] row_mask:0xf bank_mask:0xf\n\t"
      "v_mfma_f32_16x16x32_bf16 v[220:223], v[168:171], v[104:107], v[220:223]\n\t"
      "s_add_u32 s58, s72, s45\n\t"
      "s_addc_u32 s59, s73, 0\n\t"
      "global_load_dwordx4 v[192:195], v244, s[58:59] sc1\n\t"
      "v_mfma_f32_16x16x32_bf16 v[224:227], v[168:171], v[108:111], v[224:227]\n\t"
      "v_add_f32_e32 v251, v212, v213\n\t"
      "v_add_f32_e32 v251, v251, v214\n\t"
      "v_cmp_u_f32_e32 vcc, v251, v215\n\t"
      "s_cbranch_vccnz LA43R_%=\n\t"
      "LA43B_%=:\n\t"
      "ds_write_b128 v245, v[212:215] offset:32768\n\t"
      "s_waitcnt vmcnt(5)\n\t"
      "v_mfma_f32_16x16x32_bf16 v[196:199], v[172:175], v[0:3], 0\n\t"
      "ds_write_b128 v245, v[216:219] offset:33792\n\t"
      "v_mfma_f32_16x16x32_bf16 v[200:203], v[172:175], v[4:7], 0\n\t"
      "ds_write_b128 v245, v[220:223] offset:34816\n\t"
      "v_mfma_f32_16x16x32_bf16 v[204:207], v[172:175], v[8:11], 0\n\t"
      "ds_write_b128 v245, v[224:227] offset:35840\n\t"
      "s_add_u32 s58, s72, s46\n\t"
      "s_addc_u32 s59, s73, 0\n\t"
      "global_load_dwordx4 v[128:131], v244, s[58:59] sc1\n\t"
      "v_mfma_f32_16x16x32_bf16 v[208:211], v[172:175], v[12:15], 0\n\t"
      "s_waitcnt vmcnt(5)\n\t"
      "v_mfma_f32_16x16x32_bf16 v[196:199], v[176:179], v[16:19], v[196:199]\n\t"
      "v_mfma_f32_16x16x32_bf16 v[200:203], v[176:179], v[20:23], v[200:203]\n\t"
      "v_mfma_f32_16x16x32_bf16 v[204:207], v[176:179], v[24:27], v[204:207]\n\t"
      "s_waitcnt lgkmcnt(0)\n\t"
      "s_barrier\n\t"
      "ds_read_b64 v[228:229], v246 offset:32768\n\t"
      "ds_read_b64 v[230:231], v246 offset:36864\n\t"
      "s_add_u32 s58, s74, s40\n\t"
      "s_addc_u32 s59, s75, 0\n\t"
      "global_load_dwordx4 v[132:135], v244, s[58:59] sc1\n\t"
      "v_mfma_f32_16x16x32_bf16 v[208:211], v[176:179], v[28:31], v[208:211]\n\t"
      "ds_read_b64 v[232:233], v246 offset:40960\n\t"
      "ds_read_b64 v[234:235], v246 offset:45056\n\t"
      "s_waitcnt vmcnt(5)\n\t"
      "v_mfma_f32_16x16x32_bf16 v[196:199], v[180:183], v[32:35], v[196:199]\n\t"
      "ds_read_b64 v[236:237], v246 offset:49152\n\t"
      "ds_read_b64 v[238:239], v246 offset:53248\n\t"
      "v_mfma_f32_16x16x32_bf16 v[200:203], v[180:183], v[36:39], v[200:203]\n\t"
      "ds_read_b64 v[240:241], v246 offset:57344\n\t"
      "ds_read_b64 v[242:243], v246 offset:61440\n\t"
      "v_lshlrev_b32_e32 v248, 16, v115\n\t"
      "v_mfma_f32_16x16x32_bf16 v[204:207], v[180:183], v[40:43], v[204:207]\n\t"
      "v_and_b32_e32 v249, 0xffff0000, v115\n\t"
      "s_add_u32 s58, s74, s41\n\t"
      "s_addc_u32 s59, s75, 0\n\t"
      "global_load_dwordx4 v[136:139], v244, s[58:59] sc1\n\t"
      "v_mfma_f32_16x16x32_bf16 v[208:211], v[180:183], v[44:47], v[208:211]\n\t"
      "v_add_f32_e32 v248, v254, v248\n\t"
      "v_add_f32_e32 v249, v254, v249\n\t"
      "s_waitcnt vmcnt(5)\n\t"
      "v_mfma_f32_16x16x32_bf16 v[196:199], v[184:187], v[48:51], v[196:199]\n\t"
      "s_waitcnt lgkmcnt(4)\n\t"
      "v_mfma_f32_16x16x32_bf16 v[200:203], v[184:187], v[52:55], v[200:203]\n\t"
      "v_add_f32_e32 v228, v228, v230\n\t"
      "v_add_f32_e32 v232, v232, v234\n\t"
      "v_mfma_f32_16x16x32_bf16 v[204:207], v[184:187], v[56:59], v[204:207]\n\t"
      "v_add_f32_e32 v229, v229, v231\n\t"
      "s_add_u32 s58, s74, s42\n\t"
      "s_addc_u32 s59, s75, 0\n\t"
      "global_load_dwordx4 v[140:143], v244, s[58:59] sc1\n\t"
      "v_mfma_f32_16x16x32_bf16 v[208:211], v[184:187], v[60:63], v[208:211]\n\t"
      "v_add_f32_e32 v233, v233, v235\n\t"
      "s_waitcnt lgkmcnt(0)\n\t"
      "s_waitcnt vmcnt(5)\n\t"
      "v_mfma_f32_16x16x32_bf16 v[196:199], v[188:191], v[64:67], v[196:199]\n\t"
      "v_add_f32_e32 v236, v236, v238\n\t"
      "v_mfma_f32_16x16x32_bf16 v[200:203], v[188:191], v[68:71], v[200:203]\n\t"
      "v_add_f32_e32 v240, v240, v242\n\t"
      "v_add_f32_e32 v237, v237, v239\n\t"
      "v_mfma_f32_16x16x32_bf16 v[204:207], v[188:191], v[72:75], v[204:207]\n\t"
      "v_add_f32_e32 v241, v241, v243\n\t"
      "s_add_u32 s58, s74, s43\n\t"
      "s_addc_u32 s59, s75, 0\n\t"
      "global_load_dwordx4 v[144:147], v244, s[58:59] sc1\n\t"
      "v_mfma_f32_16x16x32_bf16 v[208:211], v[188:191], v[76:79], v[208:211]\n\t"
      "v_add_f32_e32 v228, v228, v232\n\t"
      "v_add_f32_e32 v236, v236, v240\n\t"
      "s_waitcnt vmcnt(5)\n\t"
      "v_mfma_f32_16x16x32_bf16 v[196:199], v[192:195], v[80:83], v[196:199]\n\t"
      "v_add_f32_e32 v229, v229, v233\n\t"
      "v_mfma_f32_16x16x32_bf16 v[200:203], v[192:195], v[84:87], v[200:203]\n\t"
      "v_add_f32_e32 v237, v237, v241\n\t"
      "v_add_f32_e32 v228, v228, v248\n\t"
      "v_mfma_f32_16x16x32_bf16 v[204:207], v[192:195], v[88:91], v[204:207]\n\t"
      "v_add_f32_e32 v229, v229, v249\n\t"
      "s_add_u32 s58, s74, s44\n\t"
      "s_addc_u32 s59, s75, 0\n\t"
      "global_load_dwordx4 v[148:151], v244, s[58:59] sc1\n\t"
      "v_mfma_f32_16x16x32_bf16 v[208:211], v[192:195], v[92:95], v[208:211]\n\t"
      "v_add_f32_e32 v228, v228, v236\n\t"
      "v_add_f32_e32 v229, v229, v237\n\t"
      "s_waitcnt vmcnt(5)\n\t"
      "v_mfma_f32_16x16x32_bf16 v[196:199], v[128:131], v[96:99], v[196:199]\n\t"
      "v_cvt_pk_bf16_f32 v232, v228, v229\n\t"
      "v_mfma_f32_16x16x32_bf16 v[200:203], v[128:131], v[100:103], v[200:203]\n\t"
      "s_mov_b32 vcc_lo, 0xeeeeeeee\n\t"
      "s_mov_b32 vcc_hi, 0xeeeeeeee\n\t"
      "v_cndmask_b32_dpp v124, v232, v124, vcc quad_perm:[0,0,0,0] row_mask:0xf bank_mask:0xf\n\t"
      "v_cndmask_b32_dpp v125, v232, v125, vcc quad_perm:[1,1,1,1] row_mask:0xf bank_mask:0xf\n\t"
      "v_cndmask_b32_dpp v126, v232, v126, vcc quad_perm:[2,2,2,2] row_mask:0xf bank_mask:0xf\n\t"
      "v_cndmask_b32_dpp v127, v232, v127, vcc quad_perm:[3,3,3,3] row_mask:0xf bank_mask:0xf\n\t"
      "v_mfma_f32_16x16x32_bf16 v[204:207], v[128:131], v[104:107], v[204:207]\n\t"
      "s_add_u32 s58, s74, s45\n\t"
      "s_addc_u32 s59, s75, 0\n\t"
      "global_load_dwordx4 v[152:155], v244, s[58:59] sc1\n\t"
      "v_mfma_f32_16x16x32_bf16 v[208:211], v[128:131], v[108:111], v[208:211]\n\t"
      "v_add_f32_e32 v251, v196, v197\n\t"
      "v_add_f32_e32 v251, v251, v198\n\t"
      "v_cmp_u_f32_e32 vcc, v251, v199\n\t"
      "s_cbranch_vccnz LA44R_%=\n\t"
      "LA44B_%=:\n\t"
      "ds_write_b128 v245, v[196:199]\n\t"
      "s_waitcnt vmcnt(5)\n\t"
      "v_mfma_f32_16x16x32_bf16 v[212:215], v[132:135], v[0:3], 0\n\t"
      "ds_write_b128 v245, v[200:203] offset:1024\n\t"
      "v_mfma_f32_16x16x32_bf16 v[216:219], v[132:135], v[4:7], 0\n\t"
      "ds_write_b128 v245, v[204:207] offset:2048\n\t"
      "v_mfma_f32_16x16x32_bf16 v[220:223], v[132:135], v[8:11], 0\n\t"
      "ds_write_b128 v245, v[208:211] offset:3072\n\t"
      "s_add_u32 s58, s74, s46\n\t"
      "s_addc_u32 s59, s75, 0\n\t"
      "global_load_dwordx4 v[156:159], v244, s[58:59] sc1\n\t"
      "v_mfma_f32_16x16x32_bf16 v[224:227], v[132:135], v[12:15], 0\n\t"
      "s_waitcnt vmcnt(5)\n\t"
      "v_mfma_f32_16x16x32_bf16 v[212:215], v[136:139], v[16:19], v[212:215]\n\t"
      "v_mfma_f32_16x16x32_bf16 v[216:219], v[136:139], v[20:23], v[216:219]\n\t"
      "v_mfma_f32_16x16x32_bf16 v[220:223], v[136:139], v[24:27], v[220:223]\n\t"
      "s_waitcnt lgkmcnt(0)\n\t"
      "s_barrier\n\t"
      "ds_read_b64 v[228:229], v246\n\t"
      "ds_read_b64 v[230:231], v246 offset:4096\n\t"
      "s_add_u32 s58, s76, s40\n\t"
      "s_addc_u32 s59, s77, 0\n\t"
      "global_load_dwordx4 v[160:163], v244, s[58:59] sc1\n\t"
      "v_mfma_f32_16x16x32_bf16 v[224:227], v[136:139], v[28:31], v[224:227]\n\t"
      "ds_read_b64 v[232:233], v246 offset:8192\n\t"
      "ds_read_b64 v[234:235], v246 offset:12288\n\t"
      "s_waitcnt vmcnt(5)\n\t"
      "v_mfma_f32_16x16x32_bf16 v[212:215], v[140:143], v[32:35], v[212:215]\n\t"
      "ds_read_b64 v[236:237], v246 offset:16384\n\t"
      "ds_read_b64 v[238:239], v246 offset:20480\n\t"
      "v_mfma_f32_16x16x32_bf16 v[216:219], v[140:143], v[36:39], v[216:219]\n\t"
      "ds_read_b64 v[240:241], v246 offset:24576\n\t"
      "ds_read_b64 v[242:243], v246 offset:28672\n\t"
      "v_lshlrev_b32_e32 v248, 16, v116\n\t"
      "v_mfma_f32_16x16x32_bf16 v[220:223], v[140:143], v[40:43], v[220:223]\n\t"
      "v_and_b32_e32 v249, 0xffff0000, v116\n\t"
      "s_add_u32 s58, s76, s41\n\t"
      "s_addc_u32 s59, s77, 0\n\t"
      "global_load_dwordx4 v[164:167], v244, s[58:59] sc1\n\t"
      "v_mfma_f32_16x16x32_bf16 v[224:227], v[140:143], v[44:47], v[224:227]\n\t"
      "v_add_f32_e32 v248, v254, v248\n\t"
      "v_add_f32_e32 v249, v254, v249\n\t"
      "s_waitcnt vmcnt(5)\n\t"
      "v_mfma_f32_16x16x32_bf16 v[212:215], v[144:147], v[48:51], v[212:215]\n\t"
      "s_waitcnt lgkmcnt(4)\n\t"
      "v_mfma_f32_16x16x32_bf16 v[216:219], v[144:147], v[52:55], v[216:219]\n\t"
      "v_add_f32_e32 v228, v228, v230\n\t"
      "v_add_f32_e32 v232, v232, v234\n\t"
      "v_mfma_f32_16x16x32_bf16 v[220:223], v[144:147], v[56:59], v[220:223]\n\t"
      "v_add_f32_e32 v229, v229, v231\n\t"
      "s_add_u32 s58, s76, s42\n\t"
      "s_addc_u32 s59, s77, 0\n\t"
      "global_load_dwordx4 v[168:171], v244, s[58:59] sc1\n\t"
      "v_mfma_f32_16x16x32_bf16 v[224:227], v[144:147], v[60:63], v[224:227]\n\t"
      "v_add_f32_e32 v233, v233, v235\n\t"
      "s_waitcnt lgkmcnt(0)\n\t"
      "s_waitcnt vmcnt(5)\n\t"
      "v_mfma_f32_16x16x32_bf16 v[212:215], v[148:151], v[64:67], v[212:215]\n\t"
      "v_add_f32_e32 v236, v236, v238\n\t"
      "v_mfma_f32_16x16x32_bf16 v[216:219], v[148:151], v[68:71], v[216:219]\n\t"
      "v_add_f32_e32 v240, v240, v242\n\t"
      "v_add_f32_e32 v237, v237, v239\n\t"
      "v_mfma_f32_16x16x32_bf16 v[220:223], v[148:151], v[72:75], v[220:223]\n\t"
      "v_add_f32_e32 v241, v241, v243\n\t"
      "s_add_u32 s58, s76, s43\n\t"
      "s_addc_u32 s59, s77, 0\n\t"
      "global_load_dwordx4 v[172:175], v244, s[58:59] sc1\n\t"
      "v_mfma_f32_16x16x32_bf16 v[224:227], v[148:151], v[76:79], v[224:227]\n\t"
      "v_add_f32_e32 v228, v228, v232\n\t"
      "v_add_f32_e32 v236, v236, v240\n\t"
      "s_waitcnt vmcnt(5)\n\t"
      "v_mfma_f32_16x16x32_bf16 v[212:215], v[152:155], v[80:83], v[212:215]\n\t"
      "v_add_f32_e32 v229, v229, v233\n\t"
      "v_mfma_f32_16x16x32_bf16 v[216:219], v[152:155], v[84:87], v[216:219]\n\t"
      "v_add_f32_e32 v237, v237, v241\n\t"
      "v_add_f32_e32 v228, v228, v248\n\t"
      "v_mfma_f32_16x16x32_bf16 v[220:223], v[152:155], v[88:91], v[220:223]\n\t"
      "v_add_f32_e32 v229, v229, v249\n\t"
      "s_add_u32 s58, s76, s44\n\t"
      "s_addc_u32 s59, s77, 0\n\t"
      "global_load_dwordx4 v[176:179], v244, s[58:59] sc1\n\t"
      "v_mfma_f32_16x16x32_bf16 v[224:227], v[152:155], v[92:95], v[224:227]\n\t"
      "v_add_f32_e32 v228, v228, v236\n\t"
      "v_add_f32_e32 v229, v229, v237\n\t"
      "s_waitcnt vmcnt(5)\n\t"
      "v_mfma_f32_16x16x32_bf16 v[212:215], v[156:159], v[96:99], v[212:215]\n\t"
      "v_cvt_pk_bf16_f32 v232, v228, v229\n\t"
      "v_mfma_f32_16x16x32_bf16 v[216:219], v[156:159], v[100:103], v[216:219]\n\t"
      "s_mov_b32 vcc_lo, 0xdddddddd\n\t"
      "s_mov_b32 vcc_hi, 0xdddddddd\n\t"
      "v_cndmask_b32_dpp v124, v232, v124, vcc quad_perm:[0,0,0,0] row_mask:0xf bank_mask:0xf\n\t"
      "v_cndmask_b32_dpp v125, v232, v125, vcc quad_perm:[1,1,1,1] row_mask:0xf bank_mask:0xf\n\t"
      "v_cndmask_b32_dpp v126, v232, v126, vcc quad_perm:[2,2,2,2] row_mask:0xf bank_mask:0xf\n\t"
      "v_cndmask_b32_dpp v127, v232, v127, vcc quad_perm:[3,3,3,3] row_mask:0xf bank_mask:0xf\n\t"
      "v_mfma_f32_16x16x32_bf16 v[220:223], v[156:159], v[104:107], v[220:223]\n\t"
      "s_add_u32 s58, s76, s45\n\t"
      "s_addc_u32 s59, s77, 0\n\t"
      "global_load_dwordx4 v[180:183], v244, s[58:59] sc1\n\t"
      "v_mfma_f32_16x16x32_bf16 v[224:227], v[156:159], v[108:111], v[224:227]\n\t"
      "v_add_f32_e32 v251, v212, v213\n\t"
      "v_add_f32_e32 v251, v251, v214\n\t"
      "v_cmp_u_f32_e32 vcc, v251, v215\n\t"
      "s_cbranch_vccnz LA45R_%=\n\t"
      "LA45B_%=:\n\t"
      "ds_write_b128 v245, v[212:215] offset:32768\n\t"
      "s_waitcnt vmcnt(5)\n\t"
      "v_mfma_f32_16x16x32_bf16 v[196:199], v[160:163], v[0:3], 0\n\t"
      "ds_write_b128 v245, v[216:219] offset:33792\n\t"
      "v_mfma_f32_16x16x32_bf16 v[200:203], v[160:163], v[4:7], 0\n\t"
      "ds_write_b128 v245, v[220:223] offset:34816\n\t"
      "v_mfma_f32_16x16x32_bf16 v[204:207], v[160:163], v[8:11], 0\n\t"
      "ds_write_b128 v245, v[224:227] offset:35840\n\t"
      "s_add_u32 s58, s76, s46\n\t"
      "s_addc_u32 s59, s77, 0\n\t"
      "global_load_dwordx4 v[184:187], v244, s[58:59] sc1\n\t"
      "v_mfma_f32_16x16x32_bf16 v[208:211], v[160:163], v[12:15], 0\n\t"
      "s_waitcnt vmcnt(5)\n\t"
      "v_mfma_f32_16x16x32_bf16 v[196:199], v[164:167], v[16:19], v[196:199]\n\t"
      "v_mfma_f32_16x16x32_bf16 v[200:203], v[164:167], v[20:23], v[200:203]\n\t"
      "v_mfma_f32_16x16x32_bf16 v[204:207], v[164:167], v[24:27], v[204:207]\n\t"
      "s_waitcnt lgkmcnt(0)\n\t"
      "s_barrier\n\t"
      "ds_read_b64 v[228:229], v246 offset:32768\n\t"
      "ds_read_b64 v[230:231], v246 offset:36864\n\t"
      "v_mfma_f32_16x16x32_bf16 v[208:211], v[164:167], v[28:31], v[208:211]\n\t"
      "ds_read_b64 v[232:233], v246 offset:40960\n\t"
      "ds_read_b64 v[234:235], v246 offset:45056\n\t"
      "s_waitcnt vmcnt(4)\n\t"
      "v_mfma_f32_16x16x32_bf16 v[196:199], v[168:171], v[32:35], v[196:199]\n\t"
      "ds_read_b64 v[236:237], v246 offset:49152\n\t"
      "ds_read_b64 v[238:239], v246 offset:53248\n\t"
      "v_mfma_f32_16x16x32_bf16 v[200:203], v[168:171], v[36:39], v[200:203]\n\t"
      "ds_read_b64 v[240:241], v246 offset:57344\n\t"
      "ds_read_b64 v[242:243], v246 offset:61440\n\t"
      "v_lshlrev_b32_e32 v248, 16, v117\n\t"
      "v_and_b32_e32 v249, 0xffff0000, v117\n\t"
      "v_mfma_f32_16x16x32_bf16 v[204:207], v[168:171], v[40:43], v[204:207]\n\t"
      "v_add_f32_e32 v248, v254, v248\n\t"
      "v_add_f32_e32 v249, v254, v249\n\t"
      "v_mfma_f32_16x16x32_bf16 v[208:211], v[168:171], v[44:47], v[208:211]\n\t"
      "s_waitcnt lgkmcnt(4)\n\t"
      "v_add_f32_e32 v228, v228, v230\n\t"
      "v_add_f32_e32 v232, v232, v234\n\t"
      "s_waitcnt vmcnt(3)\n\t"
      "v_mfma_f32_16x16x32_bf16 v[196:199], v[172:175], v[48:51], v[196:199]\n\t"
      "v_add_f32_e32 v229, v229, v231\n\t"
      "v_add_f32_e32 v233, v233, v235\n\t"
      "v_mfma_f32_16x16x32_bf16 v[200:203], v[172:175], v[52:55], v[200:203]\n\t"
      "s_waitcnt lgkmcnt(0)\n\t"
      "v_add_f32_e32 v236, v236, v238\n\t"
      "v_add_f32_e32 v240, v240, v242\n\t"
      "v_mfma_f32_16x16x32_bf16 v[204:207], v[172:175], v[56:59], v[204:207]\n\t"
      "v_add_f32_e32 v237, v237, v239\n\t"
      "v_add_f32_e32 v241, v241, v243\n\t"
      "v_mfma_f32_16x16x32_bf16 v[208:211], v[172:175], v[60:63], v[208:211]\n\t"
      "v_add_f32_e32 v228, v228, v232\n\t"
      "v_add_f32_e32 v236, v236, v240\n\t"
      "v_add_f32_e32 v229, v229, v233\n\t"
      "s_waitcnt vmcnt(2)\n\t"
      "v_mfma_f32_16x16x32_bf16 v[196:199], v[176:179], v[64:67], v[196:199]\n\t"
      "v_add_f32_e32 v237, v237, v241\n\t"
      "v_add_f32_e32 v228, v228, v248\n\t"
      "v_mfma_f32_16x16x32_bf16 v[200:203], v[176:179], v[68:71], v[200:203]\n\t"
      "v_add_f32_e32 v229, v229, v249\n\t"
      "v_add_f32_e32 v228, v228, v236\n\t"
      "v_add_f32_e32 v229, v229, v237\n\t"
      "v_mfma_f32_16x16x32_bf16 v[204:207], v[176:179], v[72:75], v[204:207]\n\t"
      "v_cvt_pk_bf16_f32 v232, v228, v229\n\t"
      "s_mov_b32 vcc_lo, 0xbbbbbbbb\n\t"
      "s_mov_b32 vcc_hi, 0xbbbbbbbb\n\t"
      "v_cndmask_b32_dpp v124, v232, v124, vcc quad_perm:[0,0,0,0] row_mask:0xf bank_mask:0xf\n\t"
      "v_cndmask_b32_dpp v125, v232, v125, vcc quad_perm:[1,1,1,1] row_mask:0xf bank_mask:0xf\n\t"
      "v_cndmask_b32_dpp v126, v232, v126, vcc quad_perm:[2,2,2,2] row_mask:0xf bank_mask:0xf\n\t"
      "v_cndmask_b32_dpp v127, v232, v127, vcc quad_perm:[3,3,3,3] row_mask:0xf bank_mask:0xf\n\t"
      "v_mfma_f32_16x16x32_bf16 v[208:211], v[176:179], v[76:79], v[208:211]\n\t"
      "s_waitcnt vmcnt(1)\n\t"
      "v_mfma_f32_16x16x32_bf16 v[196:199], v[180:183], v[80:83], v[196:199]\n\t"
      "v_mfma_f32_16x16x32_bf16 v[200:203], v[180:183], v[84:87], v[200:203]\n\t"
      "v_mfma_f32_16x16x32_bf16 v[204:207], v[180:183], v[88:91], v[204:207]\n\t"
      "v_mfma_f32_16x16x32_bf16 v[208:211], v[180:183], v[92:95], v[208:211]\n\t"
      "s_waitcnt vmcnt(0)\n\t"
      "v_mfma_f32_16x16x32_bf16 v[196:199], v[184:187], v[96:99], v[196:199]\n\t"
      "v_mfma_f32_16x16x32_bf16 v[200:203], v[184:187], v[100:103], v[200:203]\n\t"
      "v_mfma_f32_16x16x32_bf16 v[204:207], v[184:187], v[104:107], v[204:207]\n\t"
      "v_mfma_f32_16x16x32_bf16 v[208:211], v[184:187], v[108:111], v[208:211]\n\t"
      "s_nop 4\n\t"
      "v_add_f32_e32 v251, v196, v197\n\t"
      "v_add_f32_e32 v251, v251, v198\n\t"
      "v_cmp_u_f32_e32 vcc, v251, v199\n\t"
      "s_cbranch_vccnz LA46R_%=\n\t"
      "LA46B_%=:\n\t"
      "ds_write_b128 v245, v[196:199]\n\t"
      "s_mov_b64 exec, s[82:83]\n\t"
      "global_store_dwordx4 v247, v[120:123], s[18:19] sc1\n\t"
      "s_mov_b64 exec, -1\n\t"
      "ds_write_b128 v245, v[200:203] offset:1024\n\t"
      "ds_write_b128 v245, v[204:207] offset:2048\n\t"
      "ds_write_b128 v245, v[208:211] offset:3072\n\t"
      "s_waitcnt lgkmcnt(0)\n\t"
      "s_barrier\n\t"
      "ds_read_b64 v[228:229], v246\n\t"
      "ds_read_b64 v[230:231], v246 offset:4096\n\t"
      "ds_read_b64 v[232:233], v246 offset:8192\n\t"
      "ds_read_b64 v[234:235], v246 offset:12288\n\t"
      "ds_read_b64 v[236:237], v246 offset:16384\n\t"
      "ds_read_b64 v[238:239], v246 offset:20480\n\t"
      "ds_read_b64 v[240:241], v246 offset:24576\n\t"
      "ds_read_b64 v[242:243], v246 offset:28672\n\t"
      "v_lshlrev_b32_e32 v248, 16, v118\n\t"
      "v_and_b32_e32 v249, 0xffff0000, v118\n\t"
      "v_add_f32_e32 v248, v254, v248\n\t"
      "v_add_f32_e32 v249, v254, v249\n\t"
      "s_waitcnt lgkmcnt(4)\n\t"
      "v_add_f32_e32 v228, v228, v230\n\t"
      "v_add_f32_e32 v232, v232, v234\n\t"
      "v_add_f32_e32 v229, v229, v231\n\t"
      "v_add_f32_e32 v233, v233, v235\n\t"
      "s_waitcnt lgkmcnt(0)\n\t"
      "v_add_f32_e32 v236, v236, v238\n\t"
      "v_add_f32_e32 v240, v240, v242\n\t"
      "v_add_f32_e32 v237, v237, v239\n\t"
      "v_add_f32_e32 v241, v241, v243\n\t"
      "v_add_f32_e32 v228, v228, v232\n\t"
      "v_add_f32_e32 v236, v236, v240\n\t"
      "v_add_f32_e32 v229, v229, v233\n\t"
      "v_add_f32_e32 v237, v237, v241\n\t"
      "v_add_f32_e32 v228, v228, v248\n\t"
      "v_add_f32_e32 v229, v229, v249\n\t"
      "v_add_f32_e32 v228, v228, v236\n\t"
      "v_add_f32_e32 v229, v229, v237\n\t"
      "v_cvt_pk_bf16_f32 v232, v228, v229\n\t"
      "s_mov_b32 vcc_lo, 0x77777777\n\t"
      "s_mov_b32 vcc_hi, 0x77777777\n\t"
      "v_cndmask_b32_dpp v124, v232, v124, vcc quad_perm:[0,0,0,0] row_mask:0xf bank_mask:0xf\n\t"
      "v_cndmask_b32_dpp v125, v232, v125, vcc quad_perm:[1,1,1,1] row_mask:0xf bank_mask:0xf\n\t"
      "v_cndmask_b32_dpp v126, v232, v126, vcc quad_perm:[2,2,2,2] row_mask:0xf bank_mask:0xf\n\t"
      "v_cndmask_b32_dpp v127, v232, v127, vcc quad_perm:[3,3,3,3] row_mask:0xf bank_mask:0xf\n\t"
      "global_store_dwordx4 v247, v[124:127], s[62:63] sc1\n\t"
      "s_endpgm\n\t"
      "LO0R_%=:\n\t"
      "s_mov_b32 s80, 0\n\t"
      "LO0L_%=:\n\t"
      "s_add_u32 s80, s80, 1\n\t"
      "s_cmp_lt_u32 s80, 262144\n\t"
      "s_cbranch_scc0 LO0X_%=\n\t"
      "s_waitcnt vmcnt(0)\n\t"
      "s_sleep 1\n\t"
      "global_load_dwordx4 v[128:131], v244, s[42:43] offset:-2048 sc1\n\t"
      "global_load_dwordx4 v[132:135], v244, s[42:43] offset:-1024 sc1\n\t"
      "global_load_dwordx4 v[136:139], v244, s[42:43] sc1\n\t"
      "global_load_dwordx4 v[140:143], v244, s[42:43] offset:1024 sc1\n\t"
      "global_load_dwordx4 v[144:147], v244, s[42:43] offset:2048 sc1\n\t"
      "global_load_dwordx4 v[148:151], v244, s[42:43] offset:3072 sc1\n\t"
      "global_load_dwordx4 v[152:155], v244, s[44:45] offset:-4096 sc1\n\t"
      "global_load_dwordx4 v[156:159], v244, s[44:45] offset:-3072 sc1\n\t"
      "global_load_dwordx4 v[160:163], v244, s[44:45] offset:-2048 sc1\n\t"
      "global_load_dwordx4 v[164:167], v244, s[44:45] offset:-1024 sc1\n\t"
      "global_load_dwordx4 v[168:171], v244, s[44:45] sc1\n\t"
      "global_load_dwordx4 v[172:175], v244, s[44:45] offset:1024 sc1\n\t"
      "global_load_dwordx4 v[176:179], v244, s[44:45] offset:2048 sc1\n\t"
      "global_load_dwordx4 v[180:183], v244, s[44:45] offset:3072 sc1\n\t"
      "global_load_dwordx4 v[184:187], v244, s[42:43] offset:-4096 sc1\n\t"
      "global_load_dwordx4 v[188:191], v244, s[42:43] offset:-3072 sc1\n\t"
      "s_waitcnt vmcnt(0)\n\t"
      "v_max3_u32 v232, v128, v129, v130\n\t"
      "v_max3_u32 v232, v232, v131, v132\n\t"
      "v_max3_u32 v232, v232, v133, v134\n\t"
      "v_max_u32_e32 v232, v232, v135\n\t"
      "v_cmp_eq_u32_e32 vcc, -1, v232\n\t"
      "s_cbranch_vccnz LO0L_%=\n\t"
      "LO0X_%=:\n\t"
      "s_branch LO0B_%=\n\t"
      "LO2R_%=:\n\t"
      "s_mov_b32 s80, 0\n\t"
      "LO2L_%=:\n\t"
      "s_add_u32 s80, s80, 1\n\t"
      "s_cmp_lt_u32 s80, 262144\n\t"
      "s_cbranch_scc0 LO2X_%=\n\t"
      "s_waitcnt vmcnt(0)\n\t"
      "s_sleep 1\n\t"
      "global_load_dwordx4 v[136:139], v244, s[42:43] sc1\n\t"
      "global_load_dwordx4 v[140:143], v244, s[42:43] offset:1024 sc1\n\t"
      "global_load_dwordx4 v[144:147], v244, s[42:43] offset:2048 sc1\n\t"
      "global_load_dwordx4 v[148:151], v244, s[42:43] offset:3072 sc1\n\t"
      "global_load_dwordx4 v[152:155], v244, s[44:45] offset:-4096 sc1\n\t"
      "global_load_dwordx4 v[156:159], v244, s[44:45] offset:-3072 sc1\n\t"
      "global_load_dwordx4 v[160:163], v244, s[44:45] offset:-2048 sc1\n\t"
      "global_load_dwordx4 v[164:167], v244, s[44:45] offset:-1024 sc1\n\t"
      "global_load_dwordx4 v[168:171], v244, s[44:45] sc1\n\t"
      "global_load_dwordx4 v[172:175], v244, s[44:45] offset:1024 sc1\n\t"
      "global_load_dwordx4 v[176:179], v244, s[44:45] offset:2048 sc1\n\t"
      "global_load_dwordx4 v[180:183], v244, s[44:45] offset:3072 sc1\n\t"
      "global_load_dwordx4 v[184:187], v244, s[42:43] offset:-4096 sc1\n\t"
      "global_load_dwordx4 v[188:191], v244, s[42:43] offset:-3072 sc1\n\t"
      "s_waitcnt vmcnt(0)\n\t"
      "v_max3_u32 v232, v136, v137, v138\n\t"
      "v_max3_u32 v232, v232, v139, v140\n\t"
      "v_max3_u32 v232, v232, v141, v142\n\t"
      "v_max_u32_e32 v232, v232, v143\n\t"
      "v_cmp_eq_u32_e32 vcc, -1, v232\n\t"
      "s_cbranch_vccnz LO2L_%=\n\t"
      "LO2X_%=:\n\t"
      "s_branch LO2B_%=\n\t"
      "LO4R_%=:\n\t"
      "s_mov_b32 s80, 0\n\t"
      "LO4L_%=:\n\t"
      "s_add_u32 s80, s80, 1\n\t"
      "s_cmp_lt_u32 s80, 262144\n\t"
      "s_cbranch_scc0 LO4X_%=\n\t"
      "s_waitcnt vmcnt(0)\n\t"
      "s_sleep 1\n\t"
      "global_load_dwordx4 v[144:147], v244, s[42:43] offset:2048 sc1\n\t"
      "global_load_dwordx4 v[148:151], v244, s[42:43] offset:3072 sc1\n\t"
      "global_load_dwordx4 v[152:155], v244, s[44:45] offset:-4096 sc1\n\t"
      "global_load_dwordx4 v[156:159], v244, s[44:45] offset:-3072 sc1\n\t"
      "global_load_dwordx4 v[160:163], v244, s[44:45] offset:-2048 sc1\n\t"
      "global_load_dwordx4 v[164:167], v244, s[44:45] offset:-1024 sc1\n\t"
      "global_load_dwordx4 v[168:171], v244, s[44:45] sc1\n\t"
      "global_load_dwordx4 v[172:175], v244, s[44:45] offset:1024 sc1\n\t"
      "global_load_dwordx4 v[176:179], v244, s[44:45] offset:2048 sc1\n\t"
      "global_load_dwordx4 v[180:183], v244, s[44:45] offset:3072 sc1\n\t"
      "global_load_dwordx4 v[184:187], v244, s[42:43] offset:-4096 sc1\n\t"
      "global_load_dwordx4 v[188:191], v244, s[42:43] offset:-3072 sc1\n\t"
      "s_waitcnt vmcnt(0)\n\t"
      "v_max3_u32 v232, v144, v145, v146\n\t"
      "v_max3_u32 v232, v232, v147, v148\n\t"
      "v_max3_u32 v232, v232, v149, v150\n\t"
      "v_max_u32_e32 v232, v232, v151\n\t"
      "v_cmp_eq_u32_e32 vcc, -1, v232\n\t"
      "s_cbranch_vccnz LO4L_%=\n\t"
      "LO4X_%=:\n\t"
      "s_branch LO4B_%=\n\t"
      "LO6R_%=:\n\t"
      "s_mov_b32 s80, 0\n\t"
      "LO6L_%=:\n\t"
      "s_add_u32 s80, s80, 1\n\t"
      "s_cmp_lt_u32 s80, 262144\n\t"
      "s_cbranch_scc0 LO6X_%=\n\t"
      "s_waitcnt vmcnt(0)\n\t"
      "s_sleep 1\n\t"
      "global_load_dwordx4 v[152:155], v244, s[44:45] offset:-4096 sc1\n\t"
      "global_load_dwordx4 v[156:159], v244, s[44:45] offset:-3072 sc1\n\t"
      "global_load_dwordx4 v[160:163], v244, s[44:45] offset:-2048 sc1\n\t"
      "global_load_dwordx4 v[164:167], v244, s[44:45] offset:-1024 sc1\n\t"
      "global_load_dwordx4 v[168:171], v244, s[44:45] sc1\n\t"
      "global_load_dwordx4 v[172:175], v244, s[44:45] offset:1024 sc1\n\t"
      "global_load_dwordx4 v[176:179], v244, s[44:45] offset:2048 sc1\n\t"
      "global_load_dwordx4 v[180:183], v244, s[44:45] offset:3072 sc1\n\t"
      "global_load_dwordx4 v[184:187], v244, s[42:43] offset:-4096 sc1\n\t"
      "global_load_dwordx4 v[188:191], v244, s[42:43] offset:-3072 sc1\n\t"
      "s_waitcnt vmcnt(0)\n\t"
      "v_max3_u32 v232, v152, v153, v154\n\t"
      "v_max3_u32 v232, v232, v155, v156\n\t"
      "v_max3_u32 v232, v232, v157, v158\n\t"
      "v_max_u32_e32 v232, v232, v159\n\t"
      "v_cmp_eq_u32_e32 vcc, -1, v232\n\t"
      "s_cbranch_vccnz LO6L_%=\n\t"
      "LO6X_%=:\n\t"
      "s_branch LO6B_%=\n\t"
      "LO8R_%=:\n\t"
      "s_mov_b32 s80, 0\n\t"
      "LO8L_%=:\n\t"
      "s_add_u32 s80, s80, 1\n\t"
      "s_cmp_lt_u32 s80, 262144\n\t"
      "s_cbranch_scc0 LO8X_%=\n\t"
      "s_waitcnt vmcnt(0)\n\t"
      "s_sleep 1\n\t"
      "global_load_dwordx4 v[160:163], v244, s[44:45] offset:-2048 sc1\n\t"
      "global_load_dwordx4 v[164:167], v244, s[44:45] offset:-1024 sc1\n\t"
      "global_load_dwordx4 v[168:171], v244, s[44:45] sc1\n\t"
      "global_load_dwordx4 v[172:175], v244, s[44:45] offset:1024 sc1\n\t"
      "global_load_dwordx4 v[176:179], v244, s[44:45] offset:2048 sc1\n\t"
      "global_load_dwordx4 v[180:183], v244, s[44:45] offset:3072 sc1\n\t"
      "global_load_dwordx4 v[184:187], v244, s[42:43] offset:-4096 sc1\n\t"
      "global_load_dwordx4 v[188:191], v244, s[42:43] offset:-3072 sc1\n\t"
      "s_waitcnt vmcnt(0)\n\t"
      "v_max3_u32 v232, v160, v161, v162\n\t"
      "v_max3_u32 v232, v232, v163, v164\n\t"
      "v_max3_u32 v232, v232, v165, v166\n\t"
      "v_max_u32_e32 v232, v232, v167\n\t"
      "v_cmp_eq_u32_e32 vcc, -1, v232\n\t"
      "s_cbranch_vccnz LO8L_%=\n\t"
      "LO8X_%=:\n\t"
      "s_branch LO8B_%=\n\t"
      "LO10R_%=:\n\t"
      "s_mov_b32 s80, 0\n\t"
      "LO10L_%=:\n\t"
      "s_add_u32 s80, s80, 1\n\t"
      "s_cmp_lt_u32 s80, 262144\n\t"
      "s_cbranch_scc0 LO10X_%=\n\t"
      "s_waitcnt vmcnt(0)\n\t"
      "s_sleep 1\n\t"
      "global_load_dwordx4 v[168:171], v244, s[44:45] sc1\n\t"
      "global_load_dwordx4 v[172:175], v244, s[44:45] offset:1024 sc1\n\t"
      "global_load_dwordx4 v[176:179], v244, s[44:45] offset:2048 sc1\n\t"
      "global_load_dwordx4 v[180:183], v244, s[44:45] offset:3072 sc1\n\t"
      "global_load_dwordx4 v[184:187], v244, s[42:43] offset:-4096 sc1\n\t"
      "global_load_dwordx4 v[188:191], v244, s[42:43] offset:-3072 sc1\n\t"
      "s_waitcnt vmcnt(0)\n\t"
      "v_max3_u32 v232, v168, v169, v170\n\t"
      "v_max3_u32 v232, v232, v171, v172\n\t"
      "v_max3_u32 v232, v232, v173, v174\n\t"
      "v_max_u32_e32 v232, v232, v175\n\t"
      "v_cmp_eq_u32_e32 vcc, -1, v232\n\t"
      "s_cbranch_vccnz LO10L_%=\n\t"
      "LO10X_%=:\n\t"
      "s_branch LO10B_%=\n\t"
      "LO12R_%=:\n\t"
      "s_mov_b32 s80, 0\n\t"
      "LO12L_%=:\n\t"
      "s_add_u32 s80, s80, 1\n\t"
      "s_cmp_lt_u32 s80, 262144\n\t"
      "s_cbranch_scc0 LO12X_%=\n\t"
      "s_waitcnt vmcnt(0)\n\t"
      "s_sleep 1\n\t"
      "global_load_dwordx4 v[176:179], v244, s[44:45] offset:2048 sc1\n\t"
      "global_load_dwordx4 v[180:183], v244, s[44:45] offset:3072 sc1\n\t"
      "global_load_dwordx4 v[184:187], v244, s[42:43] offset:-4096 sc1\n\t"
      "global_load_dwordx4 v[188:191], v244, s[42:43] offset:-3072 sc1\n\t"
      "s_waitcnt vmcnt(0)\n\t"
      "v_max3_u32 v232, v176, v177, v178\n\t"
      "v_max3_u32 v232, v232, v179, v180\n\t"
      "v_max3_u32 v232, v232, v181, v182\n\t"
      "v_max_u32_e32 v232, v232, v183\n\t"
      "v_cmp_eq_u32_e32 vcc, -1, v232\n\t"
      "s_cbranch_vccnz LO12L_%=\n\t"
      "LO12X_%=:\n\t"
      "s_branch LO12B_%=\n\t"
      "LO14R_%=:\n\t"
      "s_mov_b32 s80, 0\n\t"
      "LO14L_%=:\n\t"
      "s_add_u32 s80, s80, 1\n\t"
      "s_cmp_lt_u32 s80, 262144\n\t"
      "s_cbranch_scc0 LO14X_%=\n\t"
      "s_waitcnt vmcnt(0)\n\t"
      "s_sleep 1\n\t"
      "global_load_dwordx4 v[184:187], v244, s[42:43] offset:-4096 sc1\n\t"
      "global_load_dwordx4 v[188:191], v244, s[42:43] offset:-3072 sc1\n\t"
      "s_waitcnt vmcnt(0)\n\t"
      "v_max3_u32 v232, v184, v185, v186\n\t"
      "v_max3_u32 v232, v232, v187, v188\n\t"
      "v_max3_u32 v232, v232, v189, v190\n\t"
      "v_max_u32_e32 v232, v232, v191\n\t"
      "v_cmp_eq_u32_e32 vcc, -1, v232\n\t"
      "s_cbranch_vccnz LO14L_%=\n\t"
      "LO14X_%=:\n\t"
      "s_branch LO14B_%=\n\t"
      "LGX_%=:\n\t"
      "v_max3_u32 v232, v128, v129, v130\n\t"
      "v_max3_u32 v232, v232, v131, v132\n\t"
      "v_max3_u32 v232, v232, v133, v134\n\t"
      "v_max3_u32 v232, v232, v135, v136\n\t"
      "v_max3_u32 v232, v232, v137, v138\n\t"
      "v_max3_u32 v232, v232, v139, v140\n\t"
      "v_max3_u32 v232, v232, v141, v142\n\t"
      "v_max3_u32 v232, v232, v143, v144\n\t"
      "v_max3_u32 v232, v232, v145, v146\n\t"
      "v_max3_u32 v232, v232, v147, v148\n\t"
      "v_max3_u32 v232, v232, v149, v150\n\t"
      "v_max3_u32 v232, v232, v151, v152\n\t"
      "v_max3_u32 v232, v232, v153, v154\n\t"
      "v_max3_u32 v232, v232, v155, v156\n\t"
      "v_max3_u32 v232, v232, v157, v158\n\t"
      "v_max3_u32 v232, v232, v159, v160\n\t"
      "v_max3_u32 v232, v232, v161, v162\n\t"
      "v_max3_u32 v232, v232, v163, v164\n\t"
      "v_max3_u32 v232, v232, v165, v166\n\t"
      "v_max3_u32 v232, v232, v167, v168\n\t"
      "v_max3_u32 v232, v232, v169, v170\n\t"
      "v_max3_u32 v232, v232, v171, v172\n\t"
      "v_max3_u32 v232, v232, v173, v174\n\t"
      "v_max3_u32 v232, v232, v175, v176\n\t"
      "v_max3_u32 v232, v232, v177, v178\n\t"
      "v_max3_u32 v232, v232, v179, v180\n\t"
      "v_max3_u32 v232, v232, v181, v182\n\t"
      "v_max3_u32 v232, v232, v183, v184\n\t"
      "v_max3_u32 v232, v232, v185, v186\n\t"
      "v_max3_u32 v232, v232, v187, v188\n\t"
      "v_max3_u32 v232, v232, v189, v190\n\t"
      "v_max_u32_e32 v232, v232, v191\n\t"
      "v_cmp_eq_u32_e32 vcc, -1, v232\n\t"
      "s_cbranch_vccz LGB_%=\n\t"
      "LGYR_%=:\n\t"
      "s_mov_b32 s80, 0\n\t"
      "LGYL_%=:\n\t"
      "s_add_u32 s80, s80, 1\n\t"
      "s_cmp_lt_u32 s80, 262144\n\t"
      "s_cbranch_scc0 LGYX_%=\n\t"
      "s_sleep 1\n\t"
      "global_load_dwordx4 v[128:131], v244, s[42:43] offset:-2048 sc1\n\t"
      "global_load_dwordx4 v[132:135], v244, s[42:43] offset:-1024 sc1\n\t"
      "global_load_dwordx4 v[136:139], v244, s[42:43] sc1\n\t"
      "global_load_dwordx4 v[140:143], v244, s[42:43] offset:1024 sc1\n\t"
      "global_load_dwordx4 v[144:147], v244, s[42:43] offset:2048 sc1\n\t"
      "global_load_dwordx4 v[148:151], v244, s[42:43] offset:3072 sc1\n\t"
      "global_load_dwordx4 v[152:155], v244, s[44:45] offset:-4096 sc1\n\t"
      "global_load_dwordx4 v[156:159], v244, s[44:45] offset:-3072 sc1\n\t"
      "global_load_dwordx4 v[160:163], v244, s[44:45] offset:-2048 sc1\n\t"
      "global_load_dwordx4 v[164:167], v244, s[44:45] offset:-1024 sc1\n\t"
      "global_load_dwordx4 v[168:171], v244, s[44:45] sc1\n\t"
      "global_load_dwordx4 v[172:175], v244, s[44:45] offset:1024 sc1\n\t"
      "global_load_dwordx4 v[176:179], v244, s[44:45] offset:2048 sc1\n\t"
      "global_load_dwordx4 v[180:183], v244, s[44:45] offset:3072 sc1\n\t"
      "global_load_dwordx4 v[184:187], v244, s[42:43] offset:-4096 sc1\n\t"
      "global_load_dwordx4 v[188:191], v244, s[42:43] offset:-3072 sc1\n\t"
      "s_waitcnt vmcnt(0)\n\t"
      "v_max3_u32 v232, v128, v129, v130\n\t"
      "v_max3_u32 v232, v232, v131, v132\n\t"
      "v_max3_u32 v232, v232, v133, v134\n\t"
      "v_max3_u32 v232, v232, v135, v136\n\t"
      "v_max3_u32 v232, v232, v137, v138\n\t"
      "v_max3_u32 v232, v232, v139, v140\n\t"
      "v_max3_u32 v232, v232, v141, v142\n\t"
      "v_max3_u32 v232, v232, v143, v144\n\t"
      "v_max3_u32 v232, v232, v145, v146\n\t"
      "v_max3_u32 v232, v232, v147, v148\n\t"
      "v_max3_u32 v232, v232, v149, v150\n\t"
      "v_max3_u32 v232, v232, v151, v152\n\t"
      "v_max3_u32 v232, v232, v153, v154\n\t"
      "v_max3_u32 v232, v232, v155, v156\n\t"
      "v_max3_u32 v232, v232, v157, v158\n\t"
      "v_max3_u32 v232, v232, v159, v160\n\t"
      "v_max3_u32 v232, v232, v161, v162\n\t"
      "v_max3_u32 v232, v232, v163, v164\n\t"
      "v_max3_u32 v232, v232, v165, v166\n\t"
      "v_max3_u32 v232, v232, v167, v168\n\t"
      "v_max3_u32 v232, v232, v169, v170\n\t"
      "v_max3_u32 v232, v232, v171, v172\n\t"
      "v_max3_u32 v232, v232, v173, v174\n\t"
      "v_max3_u32 v232, v232, v175, v176\n\t"
      "v_max3_u32 v232, v232, v177, v178\n\t"
      "v_max3_u32 v232, v232, v179, v180\n\t"
      "v_max3_u32 v232, v232, v181, v182\n\t"
      "v_max3_u32 v232, v232, v183, v184\n\t"
      "v_max3_u32 v232, v232, v185, v186\n\t"
      "v_max3_u32 v232, v232, v187, v188\n\t"
      "v_max3_u32 v232, v232, v189, v190\n\t"
      "v_max_u32_e32 v232, v232, v191\n\t"
      "v_cmp_eq_u32_e32 vcc, -1, v232\n\t"
      "s_cbranch_vccnz LGYL_%=\n\t"
      "LGYX_%=:\n\t"
      "s_cbranch_vccnz LGB_%=\n\t"
      "ds_read_b64_tr_b16 v[0:1], v216 offset:7168\n\t"
      "ds_read_b64_tr_b16 v[2:3], v220 offset:7168\n\t"
      "ds_read_b64_tr_b16 v[4:5], v217 offset:7168\n\t"
      "ds_read_b64_tr_b16 v[6:7], v221 offset:7168\n\t"
      "ds_read_b64_tr_b16 v[8:9], v218 offset:7168\n\t"
      "ds_read_b64_tr_b16 v[10:11], v222 offset:7168\n\t"
      "ds_read_b64_tr_b16 v[12:13], v219 offset:7168\n\t"
      "ds_read_b64_tr_b16 v[14:15], v223 offset:7168\n\t"
      "ds_read_b64_tr_b16 v[16:17], v218 offset:10752\n\t"
      "ds_read_b64_tr_b16 v[18:19], v222 offset:10752\n\t"
      "ds_read_b64_tr_b16 v[20:21], v219 offset:10752\n\t"
      "ds_read_b64_tr_b16 v[22:23], v223 offset:10752\n\t"
      "ds_read_b64_tr_b16 v[24:25], v216 offset:10752\n\t"
      "ds_read_b64_tr_b16 v[26:27], v220 offset:10752\n\t"
      "ds_read_b64_tr_b16 v[28:29], v217 offset:10752\n\t"
      "ds_read_b64_tr_b16 v[30:31], v221 offset:10752\n\t"
      "ds_read_b64_tr_b16 v[32:33], v216 offset:14336\n\t"
      "ds_read_b64_tr_b16 v[34:35], v220 offset:14336\n\t"
      "ds_read_b64_tr_b16 v[36:37], v217 offset:14336\n\t"
      "ds_read_b64_tr_b16 v[38:39], v221 offset:14336\n\t"
      "ds_read_b64_tr_b16 v[40:41], v218 offset:14336\n\t"
      "ds_read_b64_tr_b16 v[42:43], v222 offset:14336\n\t"
      "ds_read_b64_tr_b16 v[44:45], v219 offset:14336\n\t"
      "ds_read_b64_tr_b16 v[46:47], v223 offset:14336\n\t"
      "ds_read_b64_tr_b16 v[48:49], v218 offset:17920\n\t"
      "ds_read_b64_tr_b16 v[50:51], v222 offset:17920\n\t"
      "ds_read_b64_tr_b16 v[52:53], v219 offset:17920\n\t"
      "ds_read_b64_tr_b16 v[54:55], v223 offset:17920\n\t"
      "ds_read_b64_tr_b16 v[56:57], v216 offset:17920\n\t"
      "ds_read_b64_tr_b16 v[58:59], v220 offset:17920\n\t"
      "ds_read_b64_tr_b16 v[60:61], v217 offset:17920\n\t"
      "ds_read_b64_tr_b16 v[62:63], v221 offset:17920\n\t"
      "ds_read_b64_tr_b16 v[64:65], v216 offset:21504\n\t"
      "ds_read_b64_tr_b16 v[66:67], v220 offset:21504\n\t"
      "ds_read_b64_tr_b16 v[68:69], v217 offset:21504\n\t"
      "ds_read_b64_tr_b16 v[70:71], v221 offset:21504\n\t"
      "ds_read_b64_tr_b16 v[72:73], v218 offset:21504\n\t"
      "ds_read_b64_tr_b16 v[74:75], v222 offset:21504\n\t"
      "ds_read_b64_tr_b16 v[76:77], v219 offset:21504\n\t"
      "ds_read_b64_tr_b16 v[78:79], v223 offset:21504\n\t"
      "ds_read_b64_tr_b16 v[80:81], v218 offset:25088\n\t"
      "ds_read_b64_tr_b16 v[82:83], v222 offset:25088\n\t"
      "ds_read_b64_tr_b16 v[84:85], v219 offset:25088\n\t"
      "ds_read_b64_tr_b16 v[86:87], v223 offset:25088\n\t"
      "ds_read_b64_tr_b16 v[88:89], v216 offset:25088\n\t"
      "ds_read_b64_tr_b16 v[90:91], v220 offset:25088\n\t"
      "ds_read_b64_tr_b16 v[92:93], v217 offset:25088\n\t"
      "ds_read_b64_tr_b16 v[94:95], v221 offset:25088\n\t"
      "ds_read_b64_tr_b16 v[96:97], v216 offset:28672\n\t"
      "ds_read_b64_tr_b16 v[98:99], v220 offset:28672\n\t"
      "ds_read_b64_tr_b16 v[100:101], v217 offset:28672\n\t"
      "ds_read_b64_tr_b16 v[102:103], v221 offset:28672\n\t"
      "ds_read_b64_tr_b16 v[104:105], v218 offset:28672\n\t"
      "ds_read_b64_tr_b16 v[106:107], v222 offset:28672\n\t"
      "ds_read_b64_tr_b16 v[108:109], v219 offset:28672\n\t"
      "ds_read_b64_tr_b16 v[110:111], v223 offset:28672\n\t"
      "ds_read_b64_tr_b16 v[112:113], v218 offset:32256\n\t"
      "ds_read_b64_tr_b16 v[114:115], v222 offset:32256\n\t"
      "ds_read_b64_tr_b16 v[116:117], v219 offset:32256\n\t"
      "ds_read_b64_tr_b16 v[118:119], v223 offset:32256\n\t"
      "ds_read_b64_tr_b16 v[120:121], v216 offset:32256\n\t"
      "ds_read_b64_tr_b16 v[122:123], v220 offset:32256\n\t"
      "ds_read_b64_tr_b16 v[124:125], v217 offset:32256\n\t"
      "ds_read_b64_tr_b16 v[126:127], v221 offset:32256\n\t"
      "v_lshlrev_b32_e32 v192, 16, v208\n\t"
      "v_and_b32_e32 v193, 0xffff0000, v208\n\t"
      "v_lshlrev_b32_e32 v194, 16, v209\n\t"
      "v_and_b32_e32 v195, 0xffff0000, v209\n\t"
      "v_lshlrev_b32_e32 v196, 16, v210\n\t"
      "v_and_b32_e32 v197, 0xffff0000, v210\n\t"
      "v_lshlrev_b32_e32 v198, 16, v211\n\t"
      "v_and_b32_e32 v199, 0xffff0000, v211\n\t"
      "v_lshlrev_b32_e32 v200, 16, v212\n\t"
      "v_and_b32_e32 v201, 0xffff0000, v212\n\t"
      "v_lshlrev_b32_e32 v202, 16, v213\n\t"
      "v_and_b32_e32 v203, 0xffff0000, v213\n\t"
      "v_lshlrev_b32_e32 v204, 16, v214\n\t"
      "v_and_b32_e32 v205, 0xffff0000, v214\n\t"
      "v_lshlrev_b32_e32 v206, 16, v215\n\t"
      "v_and_b32_e32 v207, 0xffff0000, v215\n\t"
      "s_waitcnt lgkmcnt(0)\n\t"
      "s_branch LGM_%=\n\t"
      "LSR_%=:\n\t"
      "s_mov_b32 s80, 0\n\t"
      "LSL_%=:\n\t"
      "s_add_u32 s80, s80, 1\n\t"
      "s_cmp_lt_u32 s80, 262144\n\t"
      "s_cbranch_scc0 LSX_%=\n\t"
      "s_sleep 1\n\t"
      "global_load_dwordx2 v[148:149], v231, s[46:47] sc1\n\t"
      "global_load_dwordx2 v[150:151], v231, s[46:47] offset:384 sc1\n\t"
      "s_waitcnt vmcnt(0)\n\t"
      "v_max3_u32 v232, v148, v149, v150\n\t"
      "v_max_u32_e32 v232, v232, v151\n\t"
      "v_cmp_eq_u32_e32 vcc, -1, v232\n\t"
      "s_cbranch_vccnz LSL_%=\n\t"
      "LSX_%=:\n\t"
      "s_branch LSB_%=\n\t"
      "LF1R_%=:\n\t"
      "s_mov_b32 s80, 0\n\t"
      "LF1L_%=:\n\t"
      "s_add_u32 s80, s80, 1\n\t"
      "s_cmp_lt_u32 s80, 262144\n\t"
      "s_cbranch_scc0 LF1X_%=\n\t"
      "s_sleep 2\n\t"
      "global_load_dwordx2 v[138:139], v136, s[60:61] sc1\n\t"
      "s_waitcnt vmcnt(0)\n\t"
      "v_cmp_ne_u64_e32 vcc, s[22:23], v[138:139]\n\t"
      "s_cbranch_vccnz LF1L_%=\n\t"
      "LF1X_%=:\n\t"
      "s_branch LF1B_%=\n\t"
      "LF4R_%=:\n\t"
      "s_mov_b32 s80, 0\n\t"
      "LF4L_%=:\n\t"
      "s_add_u32 s80, s80, 1\n\t"
      "s_cmp_lt_u32 s80, 262144\n\t"
      "s_cbranch_scc0 LF4X_%=\n\t"
      "s_sleep 2\n\t"
      "global_load_dwordx2 v[138:139], v137, s[60:61] sc1\n\t"
      "s_waitcnt vmcnt(0)\n\t"
      "v_cmp_ne_u64_e32 vcc, s[22:23], v[138:139]\n\t"
      "s_cbranch_vccnz LF4L_%=\n\t"
      "LF4X_%=:\n\t"
      "s_branch LF4B_%=\n\t"
      "L4_%=:\n\t"
      "s_lshl2_add_u32 s72, s31, s24\n\t"
      "s_sub_u32 s72, s72, 56\n\t"
      "s_mov_b32 m0, 64512\n\t"
      "s_lshl1_add_u32 s72, s72, 0\n\t"
      "s_mul_i32 s72, s72, 65536\n\t"
      "s_and_b32 s73, s2, 7\n\t"
      "s_mul_i32 s73, s73, 512\n\t"
      "s_and_b32 s73, s73, 0xffffff80\n\t"
      "s_add_u32 s72, s72, s73\n\t"
      "v_bfe_u32 v141, v192, 2, 3\n\t"
      "v_mul_u32_u24_e32 v141, 4096, v141\n\t"
      "v_lshl_add_u32 v141, v196, 7, v141\n\t"
      "v_add_u32_e32 v141, s72, v141\n\t"
      "global_load_lds_dword v141, s[14:15]\n\t"
      "s_waitcnt vmcnt(1)\n\t"
      "s_barrier\n\t"
      "s_barrier\n\t"
      "s_lshl2_add_u32 s72, s31, s24\n\t"
      "s_sub_u32 s72, s72, 56\n\t"
      "s_mov_b32 m0, 64512\n\t"
      "s_cmp_ge_u32 s72, 64\n\t"
      "s_cselect_b32 s73, 8, 0\n\t"
      "s_sub_u32 s73, s72, s73\n\t"
      "s_and_b32 s73, s73, 0x38\n\t"
      "s_add_u32 s72, s72, s73\n\t"
      "s_add_u32 s72, s72, 8\n\t"
      "s_mul_i32 s72, s72, 57344\n\t"
      "s_and_b32 s73, s2, 7\n\t"
      "s_mul_i32 s73, s73, 448\n\t"
      "s_and_b32 s73, s73, 0xffffff80\n\t"
      "s_add_u32 s72, s72, s73\n\t"
      "v_bfe_u32 v141, v192, 2, 4\n\t"
      "v_mul_u32_u24_e32 v141, 3584, v141\n\t"
      "v_lshl_add_u32 v141, v196, 7, v141\n\t"
      "v_add_u32_e32 v141, s72, v141\n\t"
      "global_load_lds_dword v141, s[10:11]\n\t"
      "s_barrier\n\t"
      "s_cmp_lg_u32 s24, 3\n\t"
      "s_cbranch_scc1 SK18_%=\n\t"
      "global_load_dwordx2 v[138:139], v137, s[60:61] sc1\n\t"
      "SK18_%=:\n\t"
      "s_cmp_lg_u32 s24, 3\n\t"
      "s_cbranch_scc1 LF4B_%=\n\t"
      "s_waitcnt vmcnt(0)\n\t"
      "v_cmp_ne_u64_e32 vcc, s[22:23], v[138:139]\n\t"
      "s_cbranch_vccnz LF4R_%=\n\t"
      "LF4B_%=:\n\t"
      "s_barrier\n\t"
      "s_waitcnt vmcnt(0)\n\t"
      "s_barrier\n\t"
      "s_branch G4_%=\n\t"
      "LF2R_%=:\n\t"
      "s_mov_b32 s80, 0\n\t"
      "LF2L_%=:\n\t"
      "s_add_u32 s80, s80, 1\n\t"
      "s_cmp_lt_u32 s80, 262144\n\t"
      "s_cbranch_scc0 LF2X_%=\n\t"
      "s_sleep 2\n\t"
      "global_load_dwordx2 v[138:139], v137, s[60:61] sc1\n\t"
      "s_waitcnt vmcnt(0)\n\t"
      "v_cmp_ne_u64_e32 vcc, s[22:23], v[138:139]\n\t"
      "s_cbranch_vccnz LF2L_%=\n\t"
      "LF2X_%=:\n\t"
      "s_branch LF2B_%=\n\t"
      "LF3R_%=:\n\t"
      "s_mov_b32 s80, 0\n\t"
      "LF3L_%=:\n\t"
      "s_add_u32 s80, s80, 1\n\t"
      "s_cmp_lt_u32 s80, 262144\n\t"
      "s_cbranch_scc0 LF3X_%=\n\t"
      "s_sleep 2\n\t"
      "global_load_dwordx2 v[138:139], v143, s[60:61] sc1\n\t"
      "s_waitcnt vmcnt(0)\n\t"
      "v_cmp_ne_u64_e32 vcc, s[22:23], v[138:139]\n\t"
      "s_cbranch_vccnz LF3L_%=\n\t"
      "LF3X_%=:\n\t"
      "s_branch LF3B_%=\n\t"
      "LF70R_%=:\n\t"
      "s_mov_b32 s80, 0\n\t"
      "LF70L_%=:\n\t"
      "s_add_u32 s80, s80, 1\n\t"
      "s_cmp_lt_u32 s80, 262144\n\t"
      "s_cbranch_scc0 LF70X_%=\n\t"
      "s_sleep 4\n\t"
      "global_load_dwordx2 v[138:139], v136, s[60:61] sc1\n\t"
      "s_waitcnt vmcnt(0)\n\t"
      "v_cmp_ne_u64_e32 vcc, s[22:23], v[138:139]\n\t"
      "s_cbranch_vccnz LF70L_%=\n\t"
      "LF70X_%=:\n\t"
      "s_branch LF70B_%=\n\t"
      "LF71R_%=:\n\t"
      "s_mov_b32 s80, 0\n\t"
      "LF71L_%=:\n\t"
      "s_add_u32 s80, s80, 1\n\t"
      "s_cmp_lt_u32 s80, 262144\n\t"
      "s_cbranch_scc0 LF71X_%=\n\t"
      "s_sleep 4\n\t"
      "global_load_dwordx2 v[138:139], v136, s[60:61] sc1\n\t"
      "s_waitcnt vmcnt(0)\n\t"
      "v_cmp_ne_u64_e32 vcc, s[22:23], v[138:139]\n\t"
      "s_cbranch_vccnz LF71L_%=\n\t"
      "LF71X_%=:\n\t"
      "s_branch LF71B_%=\n\t"
      "SPARE_%=:\n\t"
      "v_mov_b32_e32 v132, -1\n\t"
      "v_mov_b32_e32 v133, -1\n\t"
      "v_mov_b32_e32 v134, -1\n\t"
      "v_mov_b32_e32 v135, -1\n\t"
      "s_and_b32 s34, s25, 3\n\t"
      "s_lshl_b32 s34, s34, 15\n\t"
      "s_mul_i32 s35, s24, 917504\n\t"
      "s_add_u32 s34, s34, s35\n\t"
      "s_and_b32 s35, s2, 7\n\t"
      "s_lshl_b32 s35, s35, 9\n\t"
      "s_add_u32 s34, s34, s35\n\t"
      "v_and_b32_e32 v141, 0x1c0, v244\n\t"
      "v_lshlrev_b32_e32 v141, 6, v141\n\t"
      "v_and_b32_e32 v142, 0x30, v244\n\t"
      "v_lshl_add_u32 v141, v142, 3, v141\n\t"
      "v_add_u32_e32 v141, s34, v141\n\t"
      "s_mov_b32 m0, 64512\n\t"
      "s_lshr_b32 s34, s24, 1\n\t"
      "s_lshl_b32 s34, s34, 5\n\t"
      "s_add_u32 s34, s34, s25\n\t"
      "s_and_b32 s35, s24, 1\n\t"
      "s_lshl1_add_u32 s34, s34, s35\n\t"
      "s_lshl_b32 s34, s34, 10\n\t"
      "s_add_u32 s34, s34, 8388608\n\t"
      "s_add_u32 s40, s20, s34\n\t"
      "s_addc_u32 s41, s21, 0\n\t"
      "global_store_dwordx4 v244, v[132:135], s[40:41] sc1\n\t"
      "s_add_u32 s40, s40, 262144\n\t"
      "s_addc_u32 s41, s41, 0\n\t"
      "global_store_dwordx4 v244, v[132:135], s[40:41] sc1\n\t"
      "s_add_u32 s40, s40, 262144\n\t"
      "s_addc_u32 s41, s41, 0\n\t"
      "global_store_dwordx4 v244, v[132:135], s[40:41] sc1\n\t"
      "s_add_u32 s40, s40, 262144\n\t"
      "s_addc_u32 s41, s41, 0\n\t"
      "global_store_dwordx4 v244, v[132:135], s[40:41] sc1\n\t"
      "s_add_u32 s40, s40, 262144\n\t"
      "s_addc_u32 s41, s41, 0\n\t"
      "global_store_dwordx4 v244, v[132:135], s[40:41] sc1\n\t"
      "s_add_u32 s40, s40, 262144\n\t"
      "s_addc_u32 s41, s41, 0\n\t"
      "global_store_dwordx4 v244, v[132:135], s[40:41] sc1\n\t"
      "s_add_u32 s40, s40, 262144\n\t"
      "s_addc_u32 s41, s41, 0\n\t"
      "global_store_dwordx4 v244, v[132:135], s[40:41] sc1\n\t"
      "s_add_u32 s40, s40, 262144\n\t"
      "s_addc_u32 s41, s41, 0\n\t"
      "s_lshl_b32 s34, s25, 7\n\t"
      "s_add_u32 s34, s34, 16384\n\t"
      "v_mov_b32_e32 v131, s34\n\t"
      "s_waitcnt vmcnt(0)\n\t"
      "s_barrier\n\t"
      "s_cmp_lg_u32 s24, 0\n\t"
      "s_cbranch_scc1 SP0_%=\n\t"
      "s_mov_b64 exec, 1\n\t"
      "global_store_dwordx2 v131, v[128:129], s[60:61] sc1\n\t"
      "s_mov_b64 exec, -1\n\t"
      "SP0_%=:\n\t"
      "s_and_b32 s34, s25, 3\n\t"
      "s_lshl3_add_u32 s34, s34, s24\n\t"
      "s_cmp_lg_u32 s34, 19\n\t"
      "s_cbranch_scc1 SQ0_%=\n\t"
      "v_and_b32_e32 v136, 0x1f0, v244\n\t"
      "v_lshlrev_b32_e32 v131, 6, v136\n\t"
      "v_lshlrev_b32_e32 v136, 3, v136\n\t"
      "v_add_u32_e32 v136, 16384, v136\n\t"
      "s_and_b32 s34, s2, 7\n\t"
      "s_lshl_b32 s34, s34, 7\n\t"
      "s_add_u32 s34, s34, 32768\n\t"
      "v_add_u32_e32 v131, s34, v131\n\t"
      "s_cmp_lg_u32 s24, 3\n\t"
      "s_cbranch_scc1 SK21_%=\n\t"
      "global_load_dwordx2 v[138:139], v136, s[60:61] sc1\n\t"
      "SK21_%=:\n\t"
      "s_cmp_lg_u32 s24, 3\n\t"
      "s_cbranch_scc1 LF70B_%=\n\t"
      "s_waitcnt vmcnt(0)\n\t"
      "v_cmp_ne_u64_e32 vcc, s[22:23], v[138:139]\n\t"
      "s_cbranch_vccnz LF70R_%=\n\t"
      "LF70B_%=:\n\t"
      "global_store_dwordx2 v131, v[128:129], s[60:61] sc1\n\t"
      "SQ0_%=:\n\t"
      "s_barrier\n\t"
      "s_lshl3_add_u32 s34, s25, s24\n\t"
      "s_mul_i32 s34, s34, 7168\n\t"
      "s_add_u32 s34, s34, 10227712\n\t"
      "s_add_u32 s40, s20, s34\n\t"
      "s_addc_u32 s41, s21, 0\n\t"
      "global_store_dwordx4 v244, v[132:135], s[40:41] offset:-4096 sc1\n\t"
      "global_store_dwordx4 v244, v[132:135], s[40:41] offset:-3072 sc1\n\t"
      "global_store_dwordx4 v244, v[132:135], s[40:41] offset:-2048 sc1\n\t"
      "global_store_dwordx4 v244, v[132:135], s[40:41] offset:-1024 sc1\n\t"
      "global_store_dwordx4 v244, v[132:135], s[40:41] sc1\n\t"
      "global_store_dwordx4 v244, v[132:135], s[40:41] offset:1024 sc1\n\t"
      "global_store_dwordx4 v244, v[132:135], s[40:41] offset:2048 sc1\n\t"
      "s_lshl_b32 s34, s25, 7\n\t"
      "s_add_u32 s34, s34, 24576\n\t"
      "v_mov_b32_e32 v131, s34\n\t"
      "s_waitcnt vmcnt(0)\n\t"
      "s_barrier\n\t"
      "s_cmp_lg_u32 s24, 0\n\t"
      "s_cbranch_scc1 SP1_%=\n\t"
      "s_mov_b64 exec, 1\n\t"
      "global_store_dwordx2 v131, v[128:129], s[60:61] sc1\n\t"
      "s_mov_b64 exec, -1\n\t"
      "SP1_%=:\n\t"
      "s_and_b32 s34, s25, 3\n\t"
      "s_lshl3_add_u32 s34, s34, s24\n\t"
      "s_cmp_lg_u32 s34, 27\n\t"
      "s_cbranch_scc1 SQ1_%=\n\t"
      "v_and_b32_e32 v136, 0x1f0, v244\n\t"
      "v_lshlrev_b32_e32 v131, 6, v136\n\t"
      "v_lshlrev_b32_e32 v136, 3, v136\n\t"
      "v_add_u32_e32 v136, 24576, v136\n\t"
      "s_and_b32 s34, s2, 7\n\t"
      "s_lshl_b32 s34, s34, 7\n\t"
      "s_add_u32 s34, s34, 65536\n\t"
      "v_add_u32_e32 v131, s34, v131\n\t"
      "s_cmp_lg_u32 s24, 3\n\t"
      "s_cbranch_scc1 SK22_%=\n\t"
      "global_load_dwordx2 v[138:139], v136, s[60:61] sc1\n\t"
      "SK22_%=:\n\t"
      "s_cmp_lg_u32 s24, 3\n\t"
      "s_cbranch_scc1 LF71B_%=\n\t"
      "s_waitcnt vmcnt(0)\n\t"
      "v_cmp_ne_u64_e32 vcc, s[22:23], v[138:139]\n\t"
      "s_cbranch_vccnz LF71R_%=\n\t"
      "LF71B_%=:\n\t"
      "global_store_dwordx2 v131, v[128:129], s[60:61] sc1\n\t"
      "SQ1_%=:\n\t"
      "s_barrier\n\t"
      "s_and_b32 s35, s25, 3\n\t"
      "s_lshl2_add_u32 s31, s26, s35\n\t"
      "s_and_b32 s35, s2, 7\n\t"
      "s_mul_i32 s34, s35, 14\n\t"
      "s_lshl4_add_u32 s35, s35, -14\n\t"
      "s_cmp_lt_u32 s31, 14\n\t"
      "s_cselect_b32 s34, s34, s35\n\t"
      "s_cselect_b32 s36, 1, 0\n\t"
      "s_or_b32 s27, s27, s36\n\t"
      "s_add_u32 s34, s34, s31\n\t"
      "s_lshr_b32 s29, s34, 2\n\t"
      "s_and_b32 s32, s34, 3\n\t"
      "s_mul_i32 s30, s32, 7\n\t"
      "v_and_b32_e32 v199, 60, v192\n\t"
      "v_and_b32_e32 v200, 24, v192\n\t"
      "v_lshlrev_b32_e32 v202, 3, v196\n\t"
      "v_lshl_or_b32 v202, v200, 2, v202\n\t"
      "v_lshl_or_b32 v202, v195, 7, v202\n\t"
      "v_lshl_or_b32 v202, v193, 10, v202\n\t"
      "v_add_u32_e32 v224, s39, v202\n\t"
      "v_xor_b32_e32 v225, 32, v224\n\t"
      "v_xor_b32_e32 v226, 64, v224\n\t"
      "v_xor_b32_e32 v227, 96, v224\n\t"
      "s_lshl_b32 s34, s24, 12\n\t"
      "s_add_u32 s34, s34, 98304\n\t"
      "v_add_u32_e32 v245, s34, v244\n\t"
      "v_lshlrev_b32_e32 v199, 2, v199\n\t"
      "s_lshl_b32 s34, s24, 8\n\t"
      "v_lshl_add_u32 v247, v192, 2, s34\n\t"
      "v_and_b32_e32 v200, 2, v192\n\t"
      "v_mul_u32_u24_e32 v202, 576, v200\n\t"
      "v_xor_b32_e32 v199, v202, v199\n\t"
      "v_and_b32_e32 v202, 1, v192\n\t"
      "v_lshl_or_b32 v199, v202, 3, v199\n\t"
      "s_and_b32 s35, s34, 0x400\n\t"
      "s_add_u32 s34, s34, s35\n\t"
      "s_add_u32 s34, s34, 98304\n\t"
      "v_add_u32_e32 v246, s34, v199\n\t"
      "v_lshlrev_b32_e32 v199, 2, v202\n\t"
      "v_lshl_or_b32 v199, v200, 4, v199\n\t"
      "s_min_u32 s34, s29, 27\n\t"
      "s_lshl_b32 s34, s34, 7\n\t"
      "s_lshr_b32 s35, s24, 2\n\t"
      "s_lshl_b32 s35, s35, 6\n\t"
      "s_add_u32 s34, s34, s35\n\t"
      "s_lshr_b32 s35, s39, 9\n\t"
      "s_add_u32 s34, s34, s35\n\t"
      "v_add_u32_e32 v248, s34, v199\n\t"
      "s_add_u32 s48, s20, 8388608\n\t"
      "s_addc_u32 s49, s21, 0\n\t"
      "s_cmp_ge_u32 s31, 30\n\t"
      "s_cbranch_scc1 BYE_%=\n\t"
      "s_setprio 0\n\t"
      "s_waitcnt vmcnt(0)\n\t"
      "s_barrier\n\t"
      "s_branch G4_%=\n\t"
      "BYE_%=:\n\t"
      "s_endpgm\n\t"
      "LA30R_%=:\n\t"
      "s_mov_b32 s80, 0\n\t"
      "LA30L_%=:\n\t"
      "s_add_u32 s80, s80, 1\n\t"
      "s_cmp_lt_u32 s80, 262144\n\t"
      "s_cbranch_scc0 LA30X_%=\n\t"
      "s_waitcnt vmcnt(0)\n\t"
      "s_sleep 1\n\t"
      "global_load_dwordx4 v[128:131], v250, s[64:65] offset:-4096 sc1\n\t"
      "global_load_dwordx4 v[132:135], v250, s[64:65] offset:-3072 sc1\n\t"
      "global_load_dwordx4 v[136:139], v250, s[64:65] offset:-2048 sc1\n\t"
      "global_load_dwordx4 v[140:143], v250, s[64:65] offset:-1024 sc1\n\t"
      "global_load_dwordx4 v[144:147], v250, s[64:65] sc1\n\t"
      "global_load_dwordx4 v[148:151], v250, s[64:65] offset:1024 sc1\n\t"
      "global_load_dwordx4 v[152:155], v250, s[64:65] offset:2048 sc1\n\t"
      "global_load_dwordx4 v[156:159], v250, s[64:65] offset:3072 sc1\n\t"
      "s_waitcnt vmcnt(0)\n\t"
      "v_max3_u32 v251, v128, v129, v130\n\t"
      "v_max3_u32 v251, v251, v131, v132\n\t"
      "v_max3_u32 v251, v251, v133, v134\n\t"
      "v_max3_u32 v251, v251, v135, v136\n\t"
      "v_max3_u32 v251, v251, v137, v138\n\t"
      "v_max3_u32 v251, v251, v139, v140\n\t"
      "v_max3_u32 v251, v251, v141, v142\n\t"
      "v_max3_u32 v251, v251, v143, v144\n\t"
      "v_max3_u32 v251, v251, v145, v146\n\t"
      "v_max3_u32 v251, v251, v147, v148\n\t"
      "v_max3_u32 v251, v251, v149, v150\n\t"
      "v_max3_u32 v251, v251, v151, v152\n\t"
      "v_max3_u32 v251, v251, v153, v154\n\t"
      "v_max3_u32 v251, v251, v155, v156\n\t"
      "v_max3_u32 v251, v251, v157, v158\n\t"
      "v_max_u32_e32 v251, v251, v159\n\t"
      "v_cmp_eq_u32_e32 vcc, -1, v251\n\t"
      "s_cbranch_vccnz LA30L_%=\n\t"
      "LA30X_%=:\n\t"
      "s_branch LA30B_%=\n\t"
      "LF6R_%=:\n\t"
      "s_mov_b32 s80, 0\n\t"
      "LF6L_%=:\n\t"
      "s_add_u32 s80, s80, 1\n\t"
      "s_cmp_lt_u32 s80, 262144\n\t"
      "s_cbranch_scc0 LF6X_%=\n\t"
      "s_sleep 2\n\t"
      "global_load_dwordx2 v[228:229], v230, s[60:61] sc1\n\t"
      "s_waitcnt vmcnt(0)\n\t"
      "v_cmp_ne_u64_e32 vcc, s[22:23], v[228:229]\n\t"
      "s_cbranch_vccnz LF6L_%=\n\t"
      "LF6X_%=:\n\t"
      "s_branch LF6B_%=\n\t"
      "LA31R_%=:\n\t"
      "s_mov_b32 s80, 0\n\t"
      "LA31L_%=:\n\t"
      "s_add_u32 s80, s80, 1\n\t"
      "s_cmp_lt_u32 s80, 262144\n\t"
      "s_cbranch_scc0 LA31X_%=\n\t"
      "s_waitcnt vmcnt(0)\n\t"
      "s_sleep 1\n\t"
      "global_load_dwordx4 v[160:163], v250, s[66:67] offset:-4096 sc1\n\t"
      "global_load_dwordx4 v[164:167], v250, s[66:67] offset:-3072 sc1\n\t"
      "global_load_dwordx4 v[168:171], v250, s[66:67] offset:-2048 sc1\n\t"
      "global_load_dwordx4 v[172:175], v250, s[66:67] offset:-1024 sc1\n\t"
      "global_load_dwordx4 v[176:179], v250, s[66:67] sc1\n\t"
      "global_load_dwordx4 v[180:183], v250, s[66:67] offset:1024 sc1\n\t"
      "global_load_dwordx4 v[184:187], v250, s[66:67] offset:2048 sc1\n\t"
      "global_load_dwordx4 v[188:191], v250, s[66:67] offset:3072 sc1\n\t"
      "s_waitcnt vmcnt(0)\n\t"
      "v_max3_u32 v251, v160, v161, v162\n\t"
      "v_max3_u32 v251, v251, v163, v164\n\t"
      "v_max3_u32 v251, v251, v165, v166\n\t"
      "v_max3_u32 v251, v251, v167, v168\n\t"
      "v_max3_u32 v251, v251, v169, v170\n\t"
      "v_max3_u32 v251, v251, v171, v172\n\t"
      "v_max3_u32 v251, v251, v173, v174\n\t"
      "v_max3_u32 v251, v251, v175, v176\n\t"
      "v_max3_u32 v251, v251, v177, v178\n\t"
      "v_max3_u32 v251, v251, v179, v180\n\t"
      "v_max3_u32 v251, v251, v181, v182\n\t"
      "v_max3_u32 v251, v251, v183, v184\n\t"
      "v_max3_u32 v251, v251, v185, v186\n\t"
      "v_max3_u32 v251, v251, v187, v188\n\t"
      "v_max3_u32 v251, v251, v189, v190\n\t"
      "v_max_u32_e32 v251, v251, v191\n\t"
      "v_cmp_eq_u32_e32 vcc, -1, v251\n\t"
      "s_cbranch_vccnz LA31L_%=\n\t"
      "LA31X_%=:\n\t"
      "v_mfma_f32_16x16x32_bf16 v[224:227], v[0:3], v[160:163], 0\n\t"
      "v_mfma_f32_16x16x32_bf16 v[224:227], v[16:19], v[164:167], v[224:227]\n\t"
      "v_mfma_f32_16x16x32_bf16 v[224:227], v[32:35], v[168:171], v[224:227]\n\t"
      "v_mfma_f32_16x16x32_bf16 v[224:227], v[48:51], v[172:175], v[224:227]\n\t"
      "v_mfma_f32_16x16x32_bf16 v[224:227], v[64:67], v[176:179], v[224:227]\n\t"
      "v_mfma_f32_16x16x32_bf16 v[224:227], v[80:83], v[180:183], v[224:227]\n\t"
      "v_mfma_f32_16x16x32_bf16 v[224:227], v[96:99], v[184:187], v[224:227]\n\t"
      "v_mfma_f32_16x16x32_bf16 v[224:227], v[112:115], v[188:191], v[224:227]\n\t"
      "v_mfma_f32_16x16x32_bf16 v[208:211], v[4:7], v[160:163], 0\n\t"
      "v_mfma_f32_16x16x32_bf16 v[208:211], v[20:23], v[164:167], v[208:211]\n\t"
      "v_mfma_f32_16x16x32_bf16 v[208:211], v[36:39], v[168:171], v[208:211]\n\t"
      "s_nop 7\n\t"
      "s_branch LA31B_%=\n\t"
      "LA32R_%=:\n\t"
      "s_mov_b32 s80, 0\n\t"
      "LA32L_%=:\n\t"
      "s_add_u32 s80, s80, 1\n\t"
      "s_cmp_lt_u32 s80, 262144\n\t"
      "s_cbranch_scc0 LA32X_%=\n\t"
      "s_waitcnt vmcnt(0)\n\t"
      "s_sleep 1\n\t"
      "global_load_dwordx4 v[192:195], v250, s[68:69] offset:-4096 sc1\n\t"
      "global_load_dwordx4 v[196:199], v250, s[68:69] offset:-3072 sc1\n\t"
      "global_load_dwordx4 v[200:203], v250, s[68:69] offset:-2048 sc1\n\t"
      "global_load_dwordx4 v[204:207], v250, s[68:69] offset:-1024 sc1\n\t"
      "global_load_dwordx4 v[128:131], v250, s[68:69] sc1\n\t"
      "global_load_dwordx4 v[132:135], v250, s[68:69] offset:1024 sc1\n\t"
      "global_load_dwordx4 v[136:139], v250, s[68:69] offset:2048 sc1\n\t"
      "global_load_dwordx4 v[140:143], v250, s[68:69] offset:3072 sc1\n\t"
      "s_waitcnt vmcnt(0)\n\t"
      "v_max3_u32 v251, v192, v193, v194\n\t"
      "v_max3_u32 v251, v251, v195, v196\n\t"
      "v_max3_u32 v251, v251, v197, v198\n\t"
      "v_max3_u32 v251, v251, v199, v200\n\t"
      "v_max3_u32 v251, v251, v201, v202\n\t"
      "v_max3_u32 v251, v251, v203, v204\n\t"
      "v_max3_u32 v251, v251, v205, v206\n\t"
      "v_max3_u32 v251, v251, v207, v128\n\t"
      "v_max3_u32 v251, v251, v129, v130\n\t"
      "v_max3_u32 v251, v251, v131, v132\n\t"
      "v_max3_u32 v251, v251, v133, v134\n\t"
      "v_max3_u32 v251, v251, v135, v136\n\t"
      "v_max3_u32 v251, v251, v137, v138\n\t"
      "v_max3_u32 v251, v251, v139, v140\n\t"
      "v_max3_u32 v251, v251, v141, v142\n\t"
      "v_max_u32_e32 v251, v251, v143\n\t"
      "v_cmp_eq_u32_e32 vcc, -1, v251\n\t"
      "s_cbranch_vccnz LA32L_%=\n\t"
      "LA32X_%=:\n\t"
      "v_mfma_f32_16x16x32_bf16 v[220:223], v[0:3], v[192:195], 0\n\t"
      "v_mfma_f32_16x16x32_bf16 v[220:223], v[16:19], v[196:199], v[220:223]\n\t"
      "v_mfma_f32_16x16x32_bf16 v[220:223], v[32:35], v[200:203], v[220:223]\n\t"
      "v_mfma_f32_16x16x32_bf16 v[220:223], v[48:51], v[204:207], v[220:223]\n\t"
      "v_mfma_f32_16x16x32_bf16 v[220:223], v[64:67], v[128:131], v[220:223]\n\t"
      "v_mfma_f32_16x16x32_bf16 v[220:223], v[80:83], v[132:135], v[220:223]\n\t"
      "v_mfma_f32_16x16x32_bf16 v[220:223], v[96:99], v[136:139], v[220:223]\n\t"
      "v_mfma_f32_16x16x32_bf16 v[220:223], v[112:115], v[140:143], v[220:223]\n\t"
      "v_mfma_f32_16x16x32_bf16 v[224:227], v[4:7], v[192:195], 0\n\t"
      "v_mfma_f32_16x16x32_bf16 v[224:227], v[20:23], v[196:199], v[224:227]\n\t"
      "v_mfma_f32_16x16x32_bf16 v[224:227], v[36:39], v[200:203], v[224:227]\n\t"
      "s_nop 7\n\t"
      "s_branch LA32B_%=\n\t"
      "LA33R_%=:\n\t"
      "s_mov_b32 s80, 0\n\t"
      "LA33L_%=:\n\t"
      "s_add_u32 s80, s80, 1\n\t"
      "s_cmp_lt_u32 s80, 262144\n\t"
      "s_cbranch_scc0 LA33X_%=\n\t"
      "s_waitcnt vmcnt(0)\n\t"
      "s_sleep 1\n\t"
      "global_load_dwordx4 v[144:147], v250, s[70:71] offset:-4096 sc1\n\t"
      "global_load_dwordx4 v[148:151], v250, s[70:71] offset:-3072 sc1\n\t"
      "global_load_dwordx4 v[152:155], v250, s[70:71] offset:-2048 sc1\n\t"
      "global_load_dwordx4 v[156:159], v250, s[70:71] offset:-1024 sc1\n\t"
      "global_load_dwordx4 v[160:163], v250, s[70:71] sc1\n\t"
      "global_load_dwordx4 v[164:167], v250, s[70:71] offset:1024 sc1\n\t"
      "global_load_dwordx4 v[168:171], v250, s[70:71] offset:2048 sc1\n\t"
      "global_load_dwordx4 v[172:175], v250, s[70:71] offset:3072 sc1\n\t"
      "s_waitcnt vmcnt(0)\n\t"
      "v_max3_u32 v251, v144, v145, v146\n\t"
      "v_max3_u32 v251, v251, v147, v148\n\t"
      "v_max3_u32 v251, v251, v149, v150\n\t"
      "v_max3_u32 v251, v251, v151, v152\n\t"
      "v_max3_u32 v251, v251, v153, v154\n\t"
      "v_max3_u32 v251, v251, v155, v156\n\t"
      "v_max3_u32 v251, v251, v157, v158\n\t"
      "v_max3_u32 v251, v251, v159, v160\n\t"
      "v_max3_u32 v251, v251, v161, v162\n\t"
      "v_max3_u32 v251, v251, v163, v164\n\t"
      "v_max3_u32 v251, v251, v165, v166\n\t"
      "v_max3_u32 v251, v251, v167, v168\n\t"
      "v_max3_u32 v251, v251, v169, v170\n\t"
      "v_max3_u32 v251, v251, v171, v172\n\t"
      "v_max3_u32 v251, v251, v173, v174\n\t"
      "v_max_u32_e32 v251, v251, v175\n\t"
      "v_cmp_eq_u32_e32 vcc, -1, v251\n\t"
      "s_cbranch_vccnz LA33L_%=\n\t"
      "LA33X_%=:\n\t"
      "v_mfma_f32_16x16x32_bf16 v[216:219], v[0:3], v[144:147], 0\n\t"
      "v_mfma_f32_16x16x32_bf16 v[216:219], v[16:19], v[148:151], v[216:219]\n\t"
      "v_mfma_f32_16x16x32_bf16 v[216:219], v[32:35], v[152:155], v[216:219]\n\t"
      "v_mfma_f32_16x16x32_bf16 v[216:219], v[48:51], v[156:159], v[216:219]\n\t"
      "v_mfma_f32_16x16x32_bf16 v[216:219], v[64:67], v[160:163], v[216:219]\n\t"
      "v_mfma_f32_16x16x32_bf16 v[216:219], v[80:83], v[164:167], v[216:219]\n\t"
      "v_mfma_f32_16x16x32_bf16 v[216:219], v[96:99], v[168:171], v[216:219]\n\t"
      "v_mfma_f32_16x16x32_bf16 v[216:219], v[112:115], v[172:175], v[216:219]\n\t"
      "v_mfma_f32_16x16x32_bf16 v[220:223], v[4:7], v[144:147], 0\n\t"
      "v_mfma_f32_16x16x32_bf16 v[220:223], v[20:23], v[148:151], v[220:223]\n\t"
      "v_mfma_f32_16x16x32_bf16 v[220:223], v[36:39], v[152:155], v[220:223]\n\t"
      "s_nop 7\n\t"
      "s_branch LA33B_%=\n\t"
      "LA34R_%=:\n\t"
      "s_mov_b32 s80, 0\n\t"
      "LA34L_%=:\n\t"
      "s_add_u32 s80, s80, 1\n\t"
      "s_cmp_lt_u32 s80, 262144\n\t"
      "s_cbranch_scc0 LA34X_%=\n\t"
      "s_waitcnt vmcnt(0)\n\t"
      "s_sleep 1\n\t"
      "global_load_dwordx4 v[176:179], v250, s[72:73] offset:-4096 sc1\n\t"
      "global_load_dwordx4 v[180:183], v250, s[72:73] offset:-3072 sc1\n\t"
      "global_load_dwordx4 v[184:187], v250, s[72:73] offset:-2048 sc1\n\t"
      "global_load_dwordx4 v[188:191], v250, s[72:73] offset:-1024 sc1\n\t"
      "global_load_dwordx4 v[192:195], v250, s[72:73] sc1\n\t"
      "global_load_dwordx4 v[196:199], v250, s[72:73] offset:1024 sc1\n\t"
      "global_load_dwordx4 v[200:203], v250, s[72:73] offset:2048 sc1\n\t"
      "global_load_dwordx4 v[204:207], v250, s[72:73] offset:3072 sc1\n\t"
      "s_waitcnt vmcnt(0)\n\t"
      "v_max3_u32 v251, v176, v177, v178\n\t"
      "v_max3_u32 v251, v251, v179, v180\n\t"
      "v_max3_u32 v251, v251, v181, v182\n\t"
      "v_max3_u32 v251, v251, v183, v184\n\t"
      "v_max3_u32 v251, v251, v185, v186\n\t"
      "v_max3_u32 v251, v251, v187, v188\n\t"
      "v_max3_u32 v251, v251, v189, v190\n\t"
      "v_max3_u32 v251, v251, v191, v192\n\t"
      "v_max3_u32 v251, v251, v193, v194\n\t"
      "v_max3_u32 v251, v251, v195, v196\n\t"
      "v_max3_u32 v251, v251, v197, v198\n\t"
      "v_max3_u32 v251, v251, v199, v200\n\t"
      "v_max3_u32 v251, v251, v201, v202\n\t"
      "v_max3_u32 v251, v251, v203, v204\n\t"
      "v_max3_u32 v251, v251, v205, v206\n\t"
      "v_max_u32_e32 v251, v251, v207\n\t"
      "v_cmp_eq_u32_e32 vcc, -1, v251\n\t"
      "s_cbranch_vccnz LA34L_%=\n\t"
      "LA34X_%=:\n\t"
      "v_mfma_f32_16x16x32_bf16 v[212:215], v[0:3], v[176:179], 0\n\t"
      "v_mfma_f32_16x16x32_bf16 v[212:215], v[16:19], v[180:183], v[212:215]\n\t"
      "v_mfma_f32_16x16x32_bf16 v[212:215], v[32:35], v[184:187], v[212:215]\n\t"
      "v_mfma_f32_16x16x32_bf16 v[212:215], v[48:51], v[188:191], v[212:215]\n\t"
      "v_mfma_f32_16x16x32_bf16 v[212:215], v[64:67], v[192:195], v[212:215]\n\t"
      "v_mfma_f32_16x16x32_bf16 v[212:215], v[80:83], v[196:199], v[212:215]\n\t"
      "v_mfma_f32_16x16x32_bf16 v[212:215], v[96:99], v[200:203], v[212:215]\n\t"
      "v_mfma_f32_16x16x32_bf16 v[212:215], v[112:115], v[204:207], v[212:215]\n\t"
      "v_mfma_f32_16x16x32_bf16 v[216:219], v[4:7], v[176:179], 0\n\t"
      "v_mfma_f32_16x16x32_bf16 v[216:219], v[20:23], v[180:183], v[216:219]\n\t"
      "v_mfma_f32_16x16x32_bf16 v[216:219], v[36:39], v[184:187], v[216:219]\n\t"
      "s_nop 7\n\t"
      "s_branch LA34B_%=\n\t"
      "LA35R_%=:\n\t"
      "s_mov_b32 s80, 0\n\t"
      "LA35L_%=:\n\t"
      "s_add_u32 s80, s80, 1\n\t"
      "s_cmp_lt_u32 s80, 262144\n\t"
      "s_cbranch_scc0 LA35X_%=\n\t"
      "s_waitcnt vmcnt(0)\n\t"
      "s_sleep 1\n\t"
      "global_load_dwordx4 v[128:131], v250, s[74:75] offset:-4096 sc1\n\t"
      "global_load_dwordx4 v[132:135], v250, s[74:75] offset:-3072 sc1\n\t"
      "global_load_dwordx4 v[136:139], v250, s[74:75] offset:-2048 sc1\n\t"
      "global_load_dwordx4 v[140:143], v250, s[74:75] offset:-1024 sc1\n\t"
      "global_load_dwordx4 v[144:147], v250, s[74:75] sc1\n\t"
      "global_load_dwordx4 v[148:151], v250, s[74:75] offset:1024 sc1\n\t"
      "global_load_dwordx4 v[152:155], v250, s[74:75] offset:2048 sc1\n\t"
      "global_load_dwordx4 v[156:159], v250, s[74:75] offset:3072 sc1\n\t"
      "s_waitcnt vmcnt(0)\n\t"
      "v_max3_u32 v251, v128, v129, v130\n\t"
      "v_max3_u32 v251, v251, v131, v132\n\t"
      "v_max3_u32 v251, v251, v133, v134\n\t"
      "v_max3_u32 v251, v251, v135, v136\n\t"
      "v_max3_u32 v251, v251, v137, v138\n\t"
      "v_max3_u32 v251, v251, v139, v140\n\t"
      "v_max3_u32 v251, v251, v141, v142\n\t"
      "v_max3_u32 v251, v251, v143, v144\n\t"
      "v_max3_u32 v251, v251, v145, v146\n\t"
      "v_max3_u32 v251, v251, v147, v148\n\t"
      "v_max3_u32 v251, v251, v149, v150\n\t"
      "v_max3_u32 v251, v251, v151, v152\n\t"
      "v_max3_u32 v251, v251, v153, v154\n\t"
      "v_max3_u32 v251, v251, v155, v156\n\t"
      "v_max3_u32 v251, v251, v157, v158\n\t"
      "v_max_u32_e32 v251, v251, v159\n\t"
      "v_cmp_eq_u32_e32 vcc, -1, v251\n\t"
      "s_cbranch_vccnz LA35L_%=\n\t"
      "LA35X_%=:\n\t"
      "v_mfma_f32_16x16x32_bf16 v[208:211], v[0:3], v[128:131], 0\n\t"
      "v_mfma_f32_16x16x32_bf16 v[208:211], v[16:19], v[132:135], v[208:211]\n\t"
      "v_mfma_f32_16x16x32_bf16 v[208:211], v[32:35], v[136:139], v[208:211]\n\t"
      "v_mfma_f32_16x16x32_bf16 v[208:211], v[48:51], v[140:143], v[208:211]\n\t"
      "v_mfma_f32_16x16x32_bf16 v[208:211], v[64:67], v[144:147], v[208:211]\n\t"
      "v_mfma_f32_16x16x32_bf16 v[208:211], v[80:83], v[148:151], v[208:211]\n\t"
      "v_mfma_f32_16x16x32_bf16 v[208:211], v[96:99], v[152:155], v[208:211]\n\t"
      "v_mfma_f32_16x16x32_bf16 v[208:211], v[112:115], v[156:159], v[208:211]\n\t"
      "v_mfma_f32_16x16x32_bf16 v[212:215], v[4:7], v[128:131], 0\n\t"
      "v_mfma_f32_16x16x32_bf16 v[212:215], v[20:23], v[132:135], v[212:215]\n\t"
      "v_mfma_f32_16x16x32_bf16 v[212:215], v[36:39], v[136:139], v[212:215]\n\t"
      "s_nop 7\n\t"
      "s_branch LA35B_%=\n\t"
      "LA36R_%=:\n\t"
      "s_mov_b32 s80, 0\n\t"
      "LA36L_%=:\n\t"
      "s_add_u32 s80, s80, 1\n\t"
      "s_cmp_lt_u32 s80, 262144\n\t"
      "s_cbranch_scc0 LA36X_%=\n\t"
      "s_waitcnt vmcnt(0)\n\t"
      "s_sleep 1\n\t"
      "global_load_dwordx4 v[160:163], v250, s[76:77] offset:-4096 sc1\n\t"
      "global_load_dwordx4 v[164:167], v250, s[76:77] offset:-3072 sc1\n\t"
      "global_load_dwordx4 v[168:171], v250, s[76:77] offset:-2048 sc1\n\t"
      "global_load_dwordx4 v[172:175], v250, s[76:77] offset:-1024 sc1\n\t"
      "global_load_dwordx4 v[176:179], v250, s[76:77] sc1\n\t"
      "global_load_dwordx4 v[180:183], v250, s[76:77] offset:1024 sc1\n\t"
      "global_load_dwordx4 v[184:187], v250, s[76:77] offset:2048 sc1\n\t"
      "global_load_dwordx4 v[188:191], v250, s[76:77] offset:3072 sc1\n\t"
      "s_waitcnt vmcnt(0)\n\t"
      "v_max3_u32 v251, v160, v161, v162\n\t"
      "v_max3_u32 v251, v251, v163, v164\n\t"
      "v_max3_u32 v251, v251, v165, v166\n\t"
      "v_max3_u32 v251, v251, v167, v168\n\t"
      "v_max3_u32 v251, v251, v169, v170\n\t"
      "v_max3_u32 v251, v251, v171, v172\n\t"
      "v_max3_u32 v251, v251, v173, v174\n\t"
      "v_max3_u32 v251, v251, v175, v176\n\t"
      "v_max3_u32 v251, v251, v177, v178\n\t"
      "v_max3_u32 v251, v251, v179, v180\n\t"
      "v_max3_u32 v251, v251, v181, v182\n\t"
      "v_max3_u32 v251, v251, v183, v184\n\t"
      "v_max3_u32 v251, v251, v185, v186\n\t"
      "v_max3_u32 v251, v251, v187, v188\n\t"
      "v_max3_u32 v251, v251, v189, v190\n\t"
      "v_max_u32_e32 v251, v251, v191\n\t"
      "v_cmp_eq_u32_e32 vcc, -1, v251\n\t"
      "s_cbranch_vccnz LA36L_%=\n\t"
      "LA36X_%=:\n\t"
      "v_mfma_f32_16x16x32_bf16 v[224:227], v[0:3], v[160:163], 0\n\t"
      "v_mfma_f32_16x16x32_bf16 v[224:227], v[16:19], v[164:167], v[224:227]\n\t"
      "v_mfma_f32_16x16x32_bf16 v[224:227], v[32:35], v[168:171], v[224:227]\n\t"
      "v_mfma_f32_16x16x32_bf16 v[224:227], v[48:51], v[172:175], v[224:227]\n\t"
      "v_mfma_f32_16x16x32_bf16 v[224:227], v[64:67], v[176:179], v[224:227]\n\t"
      "v_mfma_f32_16x16x32_bf16 v[224:227], v[80:83], v[180:183], v[224:227]\n\t"
      "v_mfma_f32_16x16x32_bf16 v[224:227], v[96:99], v[184:187], v[224:227]\n\t"
      "v_mfma_f32_16x16x32_bf16 v[224:227], v[112:115], v[188:191], v[224:227]\n\t"
      "v_mfma_f32_16x16x32_bf16 v[208:211], v[4:7], v[160:163], 0\n\t"
      "v_mfma_f32_16x16x32_bf16 v[208:211], v[20:23], v[164:167], v[208:211]\n\t"
      "v_mfma_f32_16x16x32_bf16 v[208:211], v[36:39], v[168:171], v[208:211]\n\t"
      "s_nop 7\n\t"
      "s_branch LA36B_%=\n\t"
      "LF5R_%=:\n\t"
      "s_mov_b32 s80, 0\n\t"
      "LF5L_%=:\n\t"
      "s_add_u32 s80, s80, 1\n\t"
      "s_cmp_lt_u32 s80, 262144\n\t"
      "s_cbranch_scc0 LF5X_%=\n\t"
      "s_sleep 2\n\t"
      "global_load_dwordx2 v[138:139], v143, s[60:61] sc1\n\t"
      "s_waitcnt vmcnt(0)\n\t"
      "v_cmp_ne_u64_e32 vcc, s[22:23], v[138:139]\n\t"
      "s_cbranch_vccnz LF5L_%=\n\t"
      "LF5X_%=:\n\t"
      "s_branch LF5B_%=\n\t"
      "PGR_%=:\n\t"
      "s_mov_b32 s80, 0\n\t"
      "PGL_%=:\n\t"
      "s_add_u32 s80, s80, 1\n\t"
      "s_cmp_lt_u32 s80, 262144\n\t"
      "s_cbranch_scc0 PGX_%=\n\t"
      "s_sleep 2\n\t"
      "global_load_dword v251, v253, s[52:53] offset:252 sc1\n\t"
      "s_waitcnt vmcnt(0)\n\t"
      "v_cmp_eq_u32_e32 vcc, -1, v251\n\t"
      "s_cbranch_vccnz PGL_%=\n\t"
      "PGX_%=:\n\t"
      "s_sleep 8\n\t"
      "s_branch PGB_%=\n\t"
      "LF8R_%=:\n\t"
      "s_mov_b32 s80, 0\n\t"
      "LF8L_%=:\n\t"
      "s_add_u32 s80, s80, 1\n\t"
      "s_cmp_lt_u32 s80, 262144\n\t"
      "s_cbranch_scc0 LF8X_%=\n\t"
      "s_sleep 2\n\t"
      "global_load_dwordx2 v[240:241], v242, s[60:61] sc1\n\t"
      "s_waitcnt vmcnt(0)\n\t"
      "v_cmp_ne_u64_e32 vcc, s[22:23], v[240:241]\n\t"
      "s_cbranch_vccnz LF8L_%=\n\t"
      "LF8X_%=:\n\t"
      "s_branch LF8B_%=\n\t"
      "LA40R_%=:\n\t"
      "s_mov_b32 s80, 0\n\t"
      "LA40L_%=:\n\t"
      "s_add_u32 s80, s80, 1\n\t"
      "s_cmp_lt_u32 s80, 262144\n\t"
      "s_cbranch_scc0 LA40X_%=\n\t"
      "s_waitcnt vmcnt(0)\n\t"
      "PQ0R_%=:\n\t"
      "PQ0L_%=:\n\t"
      "s_add_u32 s80, s80, 1\n\t"
      "s_cmp_lt_u32 s80, 262144\n\t"
      "s_cbranch_scc0 PQ0X_%=\n\t"
      "s_sleep 1\n\t"
      "global_load_dword v251, v252, s[64:65] offset:252 sc1\n\t"
      "s_waitcnt vmcnt(0)\n\t"
      "v_cmp_eq_u32_e32 vcc, -1, v251\n\t"
      "s_cbranch_vccnz PQ0L_%=\n\t"
      "PQ0X_%=:\n\t"
      "s_add_u32 s58, s64, s40\n\t"
      "s_addc_u32 s59, s65, 0\n\t"
      "global_load_dwordx4 v[128:131], v244, s[58:59] sc1\n\t"
      "s_add_u32 s58, s64, s41\n\t"
      "s_addc_u32 s59, s65, 0\n\t"
      "global_load_dwordx4 v[132:135], v244, s[58:59] sc1\n\t"
      "s_add_u32 s58, s64, s42\n\t"
      "s_addc_u32 s59, s65, 0\n\t"
      "global_load_dwordx4 v[136:139], v244, s[58:59] sc1\n\t"
      "s_add_u32 s58, s64, s43\n\t"
      "s_addc_u32 s59, s65, 0\n\t"
      "global_load_dwordx4 v[140:143], v244, s[58:59] sc1\n\t"
      "s_add_u32 s58, s64, s44\n\t"
      "s_addc_u32 s59, s65, 0\n\t"
      "global_load_dwordx4 v[144:147], v244, s[58:59] sc1\n\t"
      "s_add_u32 s58, s64, s45\n\t"
      "s_addc_u32 s59, s65, 0\n\t"
      "global_load_dwordx4 v[148:151], v244, s[58:59] sc1\n\t"
      "s_add_u32 s58, s64, s46\n\t"
      "s_addc_u32 s59, s65, 0\n\t"
      "global_load_dwordx4 v[152:155], v244, s[58:59] sc1\n\t"
      "s_waitcnt vmcnt(0)\n\t"
      "v_max3_u32 v251, v128, v129, v130\n\t"
      "v_max3_u32 v251, v251, v131, v132\n\t"
      "v_max3_u32 v251, v251, v133, v134\n\t"
      "v_max3_u32 v251, v251, v135, v136\n\t"
      "v_max3_u32 v251, v251, v137, v138\n\t"
      "v_max3_u32 v251, v251, v139, v140\n\t"
      "v_max3_u32 v251, v251, v141, v142\n\t"
      "v_max3_u32 v251, v251, v143, v144\n\t"
      "v_max3_u32 v251, v251, v145, v146\n\t"
      "v_max3_u32 v251, v251, v147, v148\n\t"
      "v_max3_u32 v251, v251, v149, v150\n\t"
      "v_max3_u32 v251, v251, v151, v152\n\t"
      "v_max3_u32 v251, v251, v153, v154\n\t"
      "v_max_u32_e32 v251, v251, v155\n\t"
      "v_cmp_eq_u32_e32 vcc, -1, v251\n\t"
      "s_cbranch_vccnz LA40L_%=\n\t"
      "LA40X_%=:\n\t"
      "v_mfma_f32_16x16x32_bf16 v[196:199], v[128:131], v[0:3], 0\n\t"
      "v_mfma_f32_16x16x32_bf16 v[200:203], v[128:131], v[4:7], 0\n\t"
      "v_mfma_f32_16x16x32_bf16 v[204:207], v[128:131], v[8:11], 0\n\t"
      "v_mfma_f32_16x16x32_bf16 v[208:211], v[128:131], v[12:15], 0\n\t"
      "v_mfma_f32_16x16x32_bf16 v[196:199], v[132:135], v[16:19], v[196:199]\n\t"
      "v_mfma_f32_16x16x32_bf16 v[200:203], v[132:135], v[20:23], v[200:203]\n\t"
      "v_mfma_f32_16x16x32_bf16 v[204:207], v[132:135], v[24:27], v[204:207]\n\t"
      "v_mfma_f32_16x16x32_bf16 v[208:211], v[132:135], v[28:31], v[208:211]\n\t"
      "v_mfma_f32_16x16x32_bf16 v[196:199], v[136:139], v[32:35], v[196:199]\n\t"
      "v_mfma_f32_16x16x32_bf16 v[200:203], v[136:139], v[36:39], v[200:203]\n\t"
      "v_mfma_f32_16x16x32_bf16 v[204:207], v[136:139], v[40:43], v[204:207]\n\t"
      "v_mfma_f32_16x16x32_bf16 v[208:211], v[136:139], v[44:47], v[208:211]\n\t"
      "v_mfma_f32_16x16x32_bf16 v[196:199], v[140:143], v[48:51], v[196:199]\n\t"
      "v_mfma_f32_16x16x32_bf16 v[200:203], v[140:143], v[52:55], v[200:203]\n\t"
      "v_mfma_f32_16x16x32_bf16 v[204:207], v[140:143], v[56:59], v[204:207]\n\t"
      "v_mfma_f32_16x16x32_bf16 v[208:211], v[140:143], v[60:63], v[208:211]\n\t"
      "v_mfma_f32_16x16x32_bf16 v[196:199], v[144:147], v[64:67], v[196:199]\n\t"
      "v_mfma_f32_16x16x32_bf16 v[200:203], v[144:147], v[68:71], v[200:203]\n\t"
      "v_mfma_f32_16x16x32_bf16 v[204:207], v[144:147], v[72:75], v[204:207]\n\t"
      "v_mfma_f32_16x16x32_bf16 v[208:211], v[144:147], v[76:79], v[208:211]\n\t"
      "v_mfma_f32_16x16x32_bf16 v[196:199], v[148:151], v[80:83], v[196:199]\n\t"
      "v_mfma_f32_16x16x32_bf16 v[200:203], v[148:151], v[84:87], v[200:203]\n\t"
      "v_mfma_f32_16x16x32_bf16 v[204:207], v[148:151], v[88:91], v[204:207]\n\t"
      "v_mfma_f32_16x16x32_bf16 v[208:211], v[148:151], v[92:95], v[208:211]\n\t"
      "v_mfma_f32_16x16x32_bf16 v[196:199], v[152:155], v[96:99], v[196:199]\n\t"
      "v_mfma_f32_16x16x32_bf16 v[200:203], v[152:155], v[100:103], v[200:203]\n\t"
      "v_mfma_f32_16x16x32_bf16 v[204:207], v[152:155], v[104:107], v[204:207]\n\t"
      "v_mfma_f32_16x16x32_bf16 v[208:211], v[152:155], v[108:111], v[208:211]\n\t"
      "s_nop 7\n\t"
      "s_branch LA40B_%=\n\t"
      "LA41R_%=:\n\t"
      "s_mov_b32 s80, 0\n\t"
      "LA41L_%=:\n\t"
      "s_add_u32 s80, s80, 1\n\t"
      "s_cmp_lt_u32 s80, 262144\n\t"
      "s_cbranch_scc0 LA41X_%=\n\t"
      "s_waitcnt vmcnt(0)\n\t"
      "PQ1R_%=:\n\t"
      "PQ1L_%=:\n\t"
      "s_add_u32 s80, s80, 1\n\t"
      "s_cmp_lt_u32 s80, 262144\n\t"
      "s_cbranch_scc0 PQ1X_%=\n\t"
      "s_sleep 1\n\t"
      "global_load_dword v251, v252, s[66:67] offset:252 sc1\n\t"
      "s_waitcnt vmcnt(0)\n\t"
      "v_cmp_eq_u32_e32 vcc, -1, v251\n\t"
      "s_cbranch_vccnz PQ1L_%=\n\t"
      "PQ1X_%=:\n\t"
      "s_add_u32 s58, s66, s40\n\t"
      "s_addc_u32 s59, s67, 0\n\t"
      "global_load_dwordx4 v[156:159], v244, s[58:59] sc1\n\t"
      "s_add_u32 s58, s66, s41\n\t"
      "s_addc_u32 s59, s67, 0\n\t"
      "global_load_dwordx4 v[160:163], v244, s[58:59] sc1\n\t"
      "s_add_u32 s58, s66, s42\n\t"
      "s_addc_u32 s59, s67, 0\n\t"
      "global_load_dwordx4 v[164:167], v244, s[58:59] sc1\n\t"
      "s_add_u32 s58, s66, s43\n\t"
      "s_addc_u32 s59, s67, 0\n\t"
      "global_load_dwordx4 v[168:171], v244, s[58:59] sc1\n\t"
      "s_add_u32 s58, s66, s44\n\t"
      "s_addc_u32 s59, s67, 0\n\t"
      "global_load_dwordx4 v[172:175], v244, s[58:59] sc1\n\t"
      "s_add_u32 s58, s66, s45\n\t"
      "s_addc_u32 s59, s67, 0\n\t"
      "global_load_dwordx4 v[176:179], v244, s[58:59] sc1\n\t"
      "s_add_u32 s58, s66, s46\n\t"
      "s_addc_u32 s59, s67, 0\n\t"
      "global_load_dwordx4 v[180:183], v244, s[58:59] sc1\n\t"
      "s_waitcnt vmcnt(0)\n\t"
      "v_max3_u32 v251, v156, v157, v158\n\t"
      "v_max3_u32 v251, v251, v159, v160\n\t"
      "v_max3_u32 v251, v251, v161, v162\n\t"
      "v_max3_u32 v251, v251, v163, v164\n\t"
      "v_max3_u32 v251, v251, v165, v166\n\t"
      "v_max3_u32 v251, v251, v167, v168\n\t"
      "v_max3_u32 v251, v251, v169, v170\n\t"
      "v_max3_u32 v251, v251, v171, v172\n\t"
      "v_max3_u32 v251, v251, v173, v174\n\t"
      "v_max3_u32 v251, v251, v175, v176\n\t"
      "v_max3_u32 v251, v251, v177, v178\n\t"
      "v_max3_u32 v251, v251, v179, v180\n\t"
      "v_max3_u32 v251, v251, v181, v182\n\t"
      "v_max_u32_e32 v251, v251, v183\n\t"
      "v_cmp_eq_u32_e32 vcc, -1, v251\n\t"
      "s_cbranch_vccnz LA41L_%=\n\t"
      "LA41X_%=:\n\t"
      "v_mfma_f32_16x16x32_bf16 v[212:215], v[156:159], v[0:3], 0\n\t"
      "v_mfma_f32_16x16x32_bf16 v[216:219], v[156:159], v[4:7], 0\n\t"
      "v_mfma_f32_16x16x32_bf16 v[220:223], v[156:159], v[8:11], 0\n\t"
      "v_mfma_f32_16x16x32_bf16 v[224:227], v[156:159], v[12:15], 0\n\t"
      "v_mfma_f32_16x16x32_bf16 v[212:215], v[160:163], v[16:19], v[212:215]\n\t"
      "v_mfma_f32_16x16x32_bf16 v[216:219], v[160:163], v[20:23], v[216:219]\n\t"
      "v_mfma_f32_16x16x32_bf16 v[220:223], v[160:163], v[24:27], v[220:223]\n\t"
      "v_mfma_f32_16x16x32_bf16 v[224:227], v[160:163], v[28:31], v[224:227]\n\t"
      "v_mfma_f32_16x16x32_bf16 v[212:215], v[164:167], v[32:35], v[212:215]\n\t"
      "v_mfma_f32_16x16x32_bf16 v[216:219], v[164:167], v[36:39], v[216:219]\n\t"
      "v_mfma_f32_16x16x32_bf16 v[220:223], v[164:167], v[40:43], v[220:223]\n\t"
      "v_mfma_f32_16x16x32_bf16 v[224:227], v[164:167], v[44:47], v[224:227]\n\t"
      "v_mfma_f32_16x16x32_bf16 v[212:215], v[168:171], v[48:51], v[212:215]\n\t"
      "v_mfma_f32_16x16x32_bf16 v[216:219], v[168:171], v[52:55], v[216:219]\n\t"
      "v_mfma_f32_16x16x32_bf16 v[220:223], v[168:171], v[56:59], v[220:223]\n\t"
      "v_mfma_f32_16x16x32_bf16 v[224:227], v[168:171], v[60:63], v[224:227]\n\t"
      "v_mfma_f32_16x16x32_bf16 v[212:215], v[172:175], v[64:67], v[212:215]\n\t"
      "v_mfma_f32_16x16x32_bf16 v[216:219], v[172:175], v[68:71], v[216:219]\n\t"
      "v_mfma_f32_16x16x32_bf16 v[220:223], v[172:175], v[72:75], v[220:223]\n\t"
      "v_mfma_f32_16x16x32_bf16 v[224:227], v[172:175], v[76:79], v[224:227]\n\t"
      "v_mfma_f32_16x16x32_bf16 v[212:215], v[176:179], v[80:83], v[212:215]\n\t"
      "v_mfma_f32_16x16x32_bf16 v[216:219], v[176:179], v[84:87], v[216:219]\n\t"
      "v_mfma_f32_16x16x32_bf16 v[220:223], v[176:179], v[88:91], v[220:223]\n\t"
      "v_mfma_f32_16x16x32_bf16 v[224:227], v[176:179], v[92:95], v[224:227]\n\t"
      "v_mfma_f32_16x16x32_bf16 v[212:215], v[180:183], v[96:99], v[212:215]\n\t"
      "v_mfma_f32_16x16x32_bf16 v[216:219], v[180:183], v[100:103], v[216:219]\n\t"
      "v_mfma_f32_16x16x32_bf16 v[220:223], v[180:183], v[104:107], v[220:223]\n\t"
      "v_mfma_f32_16x16x32_bf16 v[224:227], v[180:183], v[108:111], v[224:227]\n\t"
      "s_nop 7\n\t"
      "s_branch LA41B_%=\n\t"
      "LA42R_%=:\n\t"
      "s_mov_b32 s80, 0\n\t"
      "LA42L_%=:\n\t"
      "s_add_u32 s80, s80, 1\n\t"
      "s_cmp_lt_u32 s80, 262144\n\t"
      "s_cbranch_scc0 LA42X_%=\n\t"
      "s_waitcnt vmcnt(0)\n\t"
      "PQ2R_%=:\n\t"
      "PQ2L_%=:\n\t"
      "s_add_u32 s80, s80, 1\n\t"
      "s_cmp_lt_u32 s80, 262144\n\t"
      "s_cbranch_scc0 PQ2X_%=\n\t"
      "s_sleep 1\n\t"
      "global_load_dword v251, v252, s[68:69] offset:252 sc1\n\t"
      "s_waitcnt vmcnt(0)\n\t"
      "v_cmp_eq_u32_e32 vcc, -1, v251\n\t"
      "s_cbranch_vccnz PQ2L_%=\n\t"
      "PQ2X_%=:\n\t"
      "s_add_u32 s58, s68, s40\n\t"
      "s_addc_u32 s59, s69, 0\n\t"
      "global_load_dwordx4 v[184:187], v244, s[58:59] sc1\n\t"
      "s_add_u32 s58, s68, s41\n\t"
      "s_addc_u32 s59, s69, 0\n\t"
      "global_load_dwordx4 v[188:191], v244, s[58:59] sc1\n\t"
      "s_add_u32 s58, s68, s42\n\t"
      "s_addc_u32 s59, s69, 0\n\t"
      "global_load_dwordx4 v[192:195], v244, s[58:59] sc1\n\t"
      "s_add_u32 s58, s68, s43\n\t"
      "s_addc_u32 s59, s69, 0\n\t"
      "global_load_dwordx4 v[128:131], v244, s[58:59] sc1\n\t"
      "s_add_u32 s58, s68, s44\n\t"
      "s_addc_u32 s59, s69, 0\n\t"
      "global_load_dwordx4 v[132:135], v244, s[58:59] sc1\n\t"
      "s_add_u32 s58, s68, s45\n\t"
      "s_addc_u32 s59, s69, 0\n\t"
      "global_load_dwordx4 v[136:139], v244, s[58:59] sc1\n\t"
      "s_add_u32 s58, s68, s46\n\t"
      "s_addc_u32 s59, s69, 0\n\t"
      "global_load_dwordx4 v[140:143], v244, s[58:59] sc1\n\t"
      "s_waitcnt vmcnt(0)\n\t"
      "v_max3_u32 v251, v184, v185, v186\n\t"
      "v_max3_u32 v251, v251, v187, v188\n\t"
      "v_max3_u32 v251, v251, v189, v190\n\t"
      "v_max3_u32 v251, v251, v191, v192\n\t"
      "v_max3_u32 v251, v251, v193, v194\n\t"
      "v_max3_u32 v251, v251, v195, v128\n\t"
      "v_max3_u32 v251, v251, v129, v130\n\t"
      "v_max3_u32 v251, v251, v131, v132\n\t"
      "v_max3_u32 v251, v251, v133, v134\n\t"
      "v_max3_u32 v251, v251, v135, v136\n\t"
      "v_max3_u32 v251, v251, v137, v138\n\t"
      "v_max3_u32 v251, v251, v139, v140\n\t"
      "v_max3_u32 v251, v251, v141, v142\n\t"
      "v_max_u32_e32 v251, v251, v143\n\t"
      "v_cmp_eq_u32_e32 vcc, -1, v251\n\t"
      "s_cbranch_vccnz LA42L_%=\n\t"
      "LA42X_%=:\n\t"
      "v_mfma_f32_16x16x32_bf16 v[196:199], v[184:187], v[0:3], 0\n\t"
      "v_mfma_f32_16x16x32_bf16 v[200:203], v[184:187], v[4:7], 0\n\t"
      "v_mfma_f32_16x16x32_bf16 v[204:207], v[184:187], v[8:11], 0\n\t"
      "v_mfma_f32_16x16x32_bf16 v[208:211], v[184:187], v[12:15], 0\n\t"
      "v_mfma_f32_16x16x32_bf16 v[196:199], v[188:191], v[16:19], v[196:199]\n\t"
      "v_mfma_f32_16x16x32_bf16 v[200:203], v[188:191], v[20:23], v[200:203]\n\t"
      "v_mfma_f32_16x16x32_bf16 v[204:207], v[188:191], v[24:27], v[204:207]\n\t"
      "v_mfma_f32_16x16x32_bf16 v[208:211], v[188:191], v[28:31], v[208:211]\n\t"
      "v_mfma_f32_16x16x32_bf16 v[196:199], v[192:195], v[32:35], v[196:199]\n\t"
      "v_mfma_f32_16x16x32_bf16 v[200:203], v[192:195], v[36:39], v[200:203]\n\t"
      "v_mfma_f32_16x16x32_bf16 v[204:207], v[192:195], v[40:43], v[204:207]\n\t"
      "v_mfma_f32_16x16x32_bf16 v[208:211], v[192:195], v[44:47], v[208:211]\n\t"
      "v_mfma_f32_16x16x32_bf16 v[196:199], v[128:131], v[48:51], v[196:199]\n\t"
      "v_mfma_f32_16x16x32_bf16 v[200:203], v[128:131], v[52:55], v[200:203]\n\t"
      "v_mfma_f32_16x16x32_bf16 v[204:207], v[128:131], v[56:59], v[204:207]\n\t"
      "v_mfma_f32_16x16x32_bf16 v[208:211], v[128:131], v[60:63], v[208:211]\n\t"
      "v_mfma_f32_16x16x32_bf16 v[196:199], v[132:135], v[64:67], v[196:199]\n\t"
      "v_mfma_f32_16x16x32_bf16 v[200:203], v[132:135], v[68:71], v[200:203]\n\t"
      "v_mfma_f32_16x16x32_bf16 v[204:207], v[132:135], v[72:75], v[204:207]\n\t"
      "v_mfma_f32_16x16x32_bf16 v[208:211], v[132:135], v[76:79], v[208:211]\n\t"
      "v_mfma_f32_16x16x32_bf16 v[196:199], v[136:139], v[80:83], v[196:199]\n\t"
      "v_mfma_f32_16x16x32_bf16 v[200:203], v[136:139], v[84:87], v[200:203]\n\t"
      "v_mfma_f32_16x16x32_bf16 v[204:207], v[136:139], v[88:91], v[204:207]\n\t"
      "v_mfma_f32_16x16x32_bf16 v[208:211], v[136:139], v[92:95], v[208:211]\n\t"
      "v_mfma_f32_16x16x32_bf16 v[196:199], v[140:143], v[96:99], v[196:199]\n\t"
      "v_mfma_f32_16x16x32_bf16 v[200:203], v[140:143], v[100:103], v[200:203]\n\t"
      "v_mfma_f32_16x16x32_bf16 v[204:207], v[140:143], v[104:107], v[204:207]\n\t"
      "v_mfma_f32_16x16x32_bf16 v[208:211], v[140:143], v[108:111], v[208:211]\n\t"
      "s_nop 7\n\t"
      "s_branch LA42B_%=\n\t"
      "LA43R_%=:\n\t"
      "s_mov_b32 s80, 0\n\t"
      "LA43L_%=:\n\t"
      "s_add_u32 s80, s80, 1\n\t"
      "s_cmp_lt_u32 s80, 262144\n\t"
      "s_cbranch_scc0 LA43X_%=\n\t"
      "s_waitcnt vmcnt(0)\n\t"
      "PQ3R_%=:\n\t"
      "PQ3L_%=:\n\t"
      "s_add_u32 s80, s80, 1\n\t"
      "s_cmp_lt_u32 s80, 262144\n\t"
      "s_cbranch_scc0 PQ3X_%=\n\t"
      "s_sleep 1\n\t"
      "global_load_dword v251, v252, s[70:71] offset:252 sc1\n\t"
      "s_waitcnt vmcnt(0)\n\t"
      "v_cmp_eq_u32_e32 vcc, -1, v251\n\t"
      "s_cbranch_vccnz PQ3L_%=\n\t"
      "PQ3X_%=:\n\t"
      "s_add_u32 s58, s70, s40\n\t"
      "s_addc_u32 s59, s71, 0\n\t"
      "global_load_dwordx4 v[144:147], v244, s[58:59] sc1\n\t"
      "s_add_u32 s58, s70, s41\n\t"
      "s_addc_u32 s59, s71, 0\n\t"
      "global_load_dwordx4 v[148:151], v244, s[58:59] sc1\n\t"
      "s_add_u32 s58, s70, s42\n\t"
      "s_addc_u32 s59, s71, 0\n\t"
      "global_load_dwordx4 v[152:155], v244, s[58:59] sc1\n\t"
      "s_add_u32 s58, s70, s43\n\t"
      "s_addc_u32 s59, s71, 0\n\t"
      "global_load_dwordx4 v[156:159], v244, s[58:59] sc1\n\t"
      "s_add_u32 s58, s70, s44\n\t"
      "s_addc_u32 s59, s71, 0\n\t"
      "global_load_dwordx4 v[160:163], v244, s[58:59] sc1\n\t"
      "s_add_u32 s58, s70, s45\n\t"
      "s_addc_u32 s59, s71, 0\n\t"
      "global_load_dwordx4 v[164:167], v244, s[58:59] sc1\n\t"
      "s_add_u32 s58, s70, s46\n\t"
      "s_addc_u32 s59, s71, 0\n\t"
      "global_load_dwordx4 v[168:171], v244, s[58:59] sc1\n\t"
      "s_waitcnt vmcnt(0)\n\t"
      "v_max3_u32 v251, v144, v145, v146\n\t"
      "v_max3_u32 v251, v251, v147, v148\n\t"
      "v_max3_u32 v251, v251, v149, v150\n\t"
      "v_max3_u32 v251, v251, v151, v152\n\t"
      "v_max3_u32 v251, v251, v153, v154\n\t"
      "v_max3_u32 v251, v251, v155, v156\n\t"
      "v_max3_u32 v251, v251, v157, v158\n\t"
      "v_max3_u32 v251, v251, v159, v160\n\t"
      "v_max3_u32 v251, v251, v161, v162\n\t"
      "v_max3_u32 v251, v251, v163, v164\n\t"
      "v_max3_u32 v251, v251, v165, v166\n\t"
      "v_max3_u32 v251, v251, v167, v168\n\t"
      "v_max3_u32 v251, v251, v169, v170\n\t"
      "v_max_u32_e32 v251, v251, v171\n\t"
      "v_cmp_eq_u32_e32 vcc, -1, v251\n\t"
      "s_cbranch_vccnz LA43L_%=\n\t"
      "LA43X_%=:\n\t"
      "v_mfma_f32_16x16x32_bf16 v[212:215], v[144:147], v[0:3], 0\n\t"
      "v_mfma_f32_16x16x32_bf16 v[216:219], v[144:147], v[4:7], 0\n\t"
      "v_mfma_f32_16x16x32_bf16 v[220:223], v[144:147], v[8:11], 0\n\t"
      "v_mfma_f32_16x16x32_bf16 v[224:227], v[144:147], v[12:15], 0\n\t"
      "v_mfma_f32_16x16x32_bf16 v[212:215], v[148:151], v[16:19], v[212:215]\n\t"
      "v_mfma_f32_16x16x32_bf16 v[216:219], v[148:151], v[20:23], v[216:219]\n\t"
      "v_mfma_f32_16x16x32_bf16 v[220:223], v[148:151], v[24:27], v[220:223]\n\t"
      "v_mfma_f32_16x16x32_bf16 v[224:227], v[148:151], v[28:31], v[224:227]\n\t"
      "v_mfma_f32_16x16x32_bf16 v[212:215], v[152:155], v[32:35], v[212:215]\n\t"
      "v_mfma_f32_16x16x32_bf16 v[216:219], v[152:155], v[36:39], v[216:219]\n\t"
      "v_mfma_f32_16x16x32_bf16 v[220:223], v[152:155], v[40:43], v[220:223]\n\t"
      "v_mfma_f32_16x16x32_bf16 v[224:227], v[152:155], v[44:47], v[224:227]\n\t"
      "v_mfma_f32_16x16x32_bf16 v[212:215], v[156:159], v[48:51], v[212:215]\n\t"
      "v_mfma_f32_16x16x32_bf16 v[216:219], v[156:159], v[52:55], v[216:219]\n\t"
      "v_mfma_f32_16x16x32_bf16 v[220:223], v[156:159], v[56:59], v[220:223]\n\t"
      "v_mfma_f32_16x16x32_bf16 v[224:227], v[156:159], v[60:63], v[224:227]\n\t"
      "v_mfma_f32_16x16x32_bf16 v[212:215], v[160:163], v[64:67], v[212:215]\n\t"
      "v_mfma_f32_16x16x32_bf16 v[216:219], v[160:163], v[68:71], v[216:219]\n\t"
      "v_mfma_f32_16x16x32_bf16 v[220:223], v[160:163], v[72:75], v[220:223]\n\t"
      "v_mfma_f32_16x16x32_bf16 v[224:227], v[160:163], v[76:79], v[224:227]\n\t"
      "v_mfma_f32_16x16x32_bf16 v[212:215], v[164:167], v[80:83], v[212:215]\n\t"
      "v_mfma_f32_16x16x32_bf16 v[216:219], v[164:167], v[84:87], v[216:219]\n\t"
      "v_mfma_f32_16x16x32_bf16 v[220:223], v[164:167], v[88:91], v[220:223]\n\t"
      "v_mfma_f32_16x16x32_bf16 v[224:227], v[164:167], v[92:95], v[224:227]\n\t"
      "v_mfma_f32_16x16x32_bf16 v[212:215], v[168:171], v[96:99], v[212:215]\n\t"
      "v_mfma_f32_16x16x32_bf16 v[216:219], v[168:171], v[100:103], v[216:219]\n\t"
      "v_mfma_f32_16x16x32_bf16 v[220:223], v[168:171], v[104:107], v[220:223]\n\t"
      "v_mfma_f32_16x16x32_bf16 v[224:227], v[168:171], v[108:111], v[224:227]\n\t"
      "s_nop 7\n\t"
      "s_branch LA43B_%=\n\t"
      "LA44R_%=:\n\t"
      "s_mov_b32 s80, 0\n\t"
      "LA44L_%=:\n\t"
      "s_add_u32 s80, s80, 1\n\t"
      "s_cmp_lt_u32 s80, 262144\n\t"
      "s_cbranch_scc0 LA44X_%=\n\t"
      "s_waitcnt vmcnt(0)\n\t"
      "PQ4R_%=:\n\t"
      "PQ4L_%=:\n\t"
      "s_add_u32 s80, s80, 1\n\t"
      "s_cmp_lt_u32 s80, 262144\n\t"
      "s_cbranch_scc0 PQ4X_%=\n\t"
      "s_sleep 1\n\t"
      "global_load_dword v251, v252, s[72:73] offset:252 sc1\n\t"
      "s_waitcnt vmcnt(0)\n\t"
      "v_cmp_eq_u32_e32 vcc, -1, v251\n\t"
      "s_cbranch_vccnz PQ4L_%=\n\t"
      "PQ4X_%=:\n\t"
      "s_add_u32 s58, s72, s40\n\t"
      "s_addc_u32 s59, s73, 0\n\t"
      "global_load_dwordx4 v[172:175], v244, s[58:59] sc1\n\t"
      "s_add_u32 s58, s72, s41\n\t"
      "s_addc_u32 s59, s73, 0\n\t"
      "global_load_dwordx4 v[176:179], v244, s[58:59] sc1\n\t"
      "s_add_u32 s58, s72, s42\n\t"
      "s_addc_u32 s59, s73, 0\n\t"
      "global_load_dwordx4 v[180:183], v244, s[58:59] sc1\n\t"
      "s_add_u32 s58, s72, s43\n\t"
      "s_addc_u32 s59, s73, 0\n\t"
      "global_load_dwordx4 v[184:187], v244, s[58:59] sc1\n\t"
      "s_add_u32 s58, s72, s44\n\t"
      "s_addc_u32 s59, s73, 0\n\t"
      "global_load_dwordx4 v[188:191], v244, s[58:59] sc1\n\t"
      "s_add_u32 s58, s72, s45\n\t"
      "s_addc_u32 s59, s73, 0\n\t"
      "global_load_dwordx4 v[192:195], v244, s[58:59] sc1\n\t"
      "s_add_u32 s58, s72, s46\n\t"
      "s_addc_u32 s59, s73, 0\n\t"
      "global_load_dwordx4 v[128:131], v244, s[58:59] sc1\n\t"
      "s_waitcnt vmcnt(0)\n\t"
      "v_max3_u32 v251, v172, v173, v174\n\t"
      "v_max3_u32 v251, v251, v175, v176\n\t"
      "v_max3_u32 v251, v251, v177, v178\n\t"
      "v_max3_u32 v251, v251, v179, v180\n\t"
      "v_max3_u32 v251, v251, v181, v182\n\t"
      "v_max3_u32 v251, v251, v183, v184\n\t"
      "v_max3_u32 v251, v251, v185, v186\n\t"
      "v_max3_u32 v251, v251, v187, v188\n\t"
      "v_max3_u32 v251, v251, v189, v190\n\t"
      "v_max3_u32 v251, v251, v191, v192\n\t"
      "v_max3_u32 v251, v251, v193, v194\n\t"
      "v_max3_u32 v251, v251, v195, v128\n\t"
      "v_max3_u32 v251, v251, v129, v130\n\t"
      "v_max_u32_e32 v251, v251, v131\n\t"
      "v_cmp_eq_u32_e32 vcc, -1, v251\n\t"
      "s_cbranch_vccnz LA44L_%=\n\t"
      "LA44X_%=:\n\t"
      "v_mfma_f32_16x16x32_bf16 v[196:199], v[172:175], v[0:3], 0\n\t"
      "v_mfma_f32_16x16x32_bf16 v[200:203], v[172:175], v[4:7], 0\n\t"
      "v_mfma_f32_16x16x32_bf16 v[204:207], v[172:175], v[8:11], 0\n\t"
      "v_mfma_f32_16x16x32_bf16 v[208:211], v[172:175], v[12:15], 0\n\t"
      "v_mfma_f32_16x16x32_bf16 v[196:199], v[176:179], v[16:19], v[196:199]\n\t"
      "v_mfma_f32_16x16x32_bf16 v[200:203], v[176:179], v[20:23], v[200:203]\n\t"
      "v_mfma_f32_16x16x32_bf16 v[204:207], v[176:179], v[24:27], v[204:207]\n\t"
      "v_mfma_f32_16x16x32_bf16 v[208:211], v[176:179], v[28:31], v[208:211]\n\t"
      "v_mfma_f32_16x16x32_bf16 v[196:199], v[180:183], v[32:35], v[196:199]\n\t"
      "v_mfma_f32_16x16x32_bf16 v[200:203], v[180:183], v[36:39], v[200:203]\n\t"
      "v_mfma_f32_16x16x32_bf16 v[204:207], v[180:183], v[40:43], v[204:207]\n\t"
      "v_mfma_f32_16x16x32_bf16 v[208:211], v[180:183], v[44:47], v[208:211]\n\t"
      "v_mfma_f32_16x16x32_bf16 v[196:199], v[184:187], v[48:51], v[196:199]\n\t"
      "v_mfma_f32_16x16x32_bf16 v[200:203], v[184:187], v[52:55], v[200:203]\n\t"
      "v_mfma_f32_16x16x32_bf16 v[204:207], v[184:187], v[56:59], v[204:207]\n\t"
      "v_mfma_f32_16x16x32_bf16 v[208:211], v[184:187], v[60:63], v[208:211]\n\t"
      "v_mfma_f32_16x16x32_bf16 v[196:199], v[188:191], v[64:67], v[196:199]\n\t"
      "v_mfma_f32_16x16x32_bf16 v[200:203], v[188:191], v[68:71], v[200:203]\n\t"
      "v_mfma_f32_16x16x32_bf16 v[204:207], v[188:191], v[72:75], v[204:207]\n\t"
      "v_mfma_f32_16x16x32_bf16 v[208:211], v[188:191], v[76:79], v[208:211]\n\t"
      "v_mfma_f32_16x16x32_bf16 v[196:199], v[192:195], v[80:83], v[196:199]\n\t"
      "v_mfma_f32_16x16x32_bf16 v[200:203], v[192:195], v[84:87], v[200:203]\n\t"
      "v_mfma_f32_16x16x32_bf16 v[204:207], v[192:195], v[88:91], v[204:207]\n\t"
      "v_mfma_f32_16x16x32_bf16 v[208:211], v[192:195], v[92:95], v[208:211]\n\t"
      "v_mfma_f32_16x16x32_bf16 v[196:199], v[128:131], v[96:99], v[196:199]\n\t"
      "v_mfma_f32_16x16x32_bf16 v[200:203], v[128:131], v[100:103], v[200:203]\n\t"
      "v_mfma_f32_16x16x32_bf16 v[204:207], v[128:131], v[104:107], v[204:207]\n\t"
      "v_mfma_f32_16x16x32_bf16 v[208:211], v[128:131], v[108:111], v[208:211]\n\t"
      "s_nop 7\n\t"
      "s_branch LA44B_%=\n\t"
      "LA45R_%=:\n\t"
      "s_mov_b32 s80, 0\n\t"
      "LA45L_%=:\n\t"
      "s_add_u32 s80, s80, 1\n\t"
      "s_cmp_lt_u32 s80, 262144\n\t"
      "s_cbranch_scc0 LA45X_%=\n\t"
      "s_waitcnt vmcnt(0)\n\t"
      "PQ5R_%=:\n\t"
      "PQ5L_%=:\n\t"
      "s_add_u32 s80, s80, 1\n\t"
      "s_cmp_lt_u32 s80, 262144\n\t"
      "s_cbranch_scc0 PQ5X_%=\n\t"
      "s_sleep 1\n\t"
      "global_load_dword v251, v252, s[74:75] offset:252 sc1\n\t"
      "s_waitcnt vmcnt(0)\n\t"
      "v_cmp_eq_u32_e32 vcc, -1, v251\n\t"
      "s_cbranch_vccnz PQ5L_%=\n\t"
      "PQ5X_%=:\n\t"
      "s_add_u32 s58, s74, s40\n\t"
      "s_addc_u32 s59, s75, 0\n\t"
      "global_load_dwordx4 v[132:135], v244, s[58:59] sc1\n\t"
      "s_add_u32 s58, s74, s41\n\t"
      "s_addc_u32 s59, s75, 0\n\t"
      "global_load_dwordx4 v[136:139], v244, s[58:59] sc1\n\t"
      "s_add_u32 s58, s74, s42\n\t"
      "s_addc_u32 s59, s75, 0\n\t"
      "global_load_dwordx4 v[140:143], v244, s[58:59] sc1\n\t"
      "s_add_u32 s58, s74, s43\n\t"
      "s_addc_u32 s59, s75, 0\n\t"
      "global_load_dwordx4 v[144:147], v244, s[58:59] sc1\n\t"
      "s_add_u32 s58, s74, s44\n\t"
      "s_addc_u32 s59, s75, 0\n\t"
      "global_load_dwordx4 v[148:151], v244, s[58:59] sc1\n\t"
      "s_add_u32 s58, s74, s45\n\t"
      "s_addc_u32 s59, s75, 0\n\t"
      "global_load_dwordx4 v[152:155], v244, s[58:59] sc1\n\t"
      "s_add_u32 s58, s74, s46\n\t"
      "s_addc_u32 s59, s75, 0\n\t"
      "global_load_dwordx4 v[156:159], v244, s[58:59] sc1\n\t"
      "s_waitcnt vmcnt(0)\n\t"
      "v_max3_u32 v251, v132, v133, v134\n\t"
      "v_max3_u32 v251, v251, v135, v136\n\t"
      "v_max3_u32 v251, v251, v137, v138\n\t"
      "v_max3_u32 v251, v251, v139, v140\n\t"
      "v_max3_u32 v251, v251, v141, v142\n\t"
      "v_max3_u32 v251, v251, v143, v144\n\t"
      "v_max3_u32 v251, v251, v145, v146\n\t"
      "v_max3_u32 v251, v251, v147, v148\n\t"
      "v_max3_u32 v251, v251, v149, v150\n\t"
      "v_max3_u32 v251, v251, v151, v152\n\t"
      "v_max3_u32 v251, v251, v153, v154\n\t"
      "v_max3_u32 v251, v251, v155, v156\n\t"
      "v_max3_u32 v251, v251, v157, v158\n\t"
      "v_max_u32_e32 v251, v251, v159\n\t"
      "v_cmp_eq_u32_e32 vcc, -1, v251\n\t"
      "s_cbranch_vccnz LA45L_%=\n\t"
      "LA45X_%=:\n\t"
      "v_mfma_f32_16x16x32_bf16 v[212:215], v[132:135], v[0:3], 0\n\t"
      "v_mfma_f32_16x16x32_bf16 v[216:219], v[132:135], v[4:7], 0\n\t"
      "v_mfma_f32_16x16x32_bf16 v[220:223], v[132:135], v[8:11], 0\n\t"
      "v_mfma_f32_16x16x32_bf16 v[224:227], v[132:135], v[12:15], 0\n\t"
      "v_mfma_f32_16x16x32_bf16 v[212:215], v[136:139], v[16:19], v[212:215]\n\t"
      "v_mfma_f32_16x16x32_bf16 v[216:219], v[136:139], v[20:23], v[216:219]\n\t"
      "v_mfma_f32_16x16x32_bf16 v[220:223], v[136:139], v[24:27], v[220:223]\n\t"
      "v_mfma_f32_16x16x32_bf16 v[224:227], v[136:139], v[28:31], v[224:227]\n\t"
      "v_mfma_f32_16x16x32_bf16 v[212:215], v[140:143], v[32:35], v[212:215]\n\t"
      "v_mfma_f32_16x16x32_bf16 v[216:219], v[140:143], v[36:39], v[216:219]\n\t"
      "v_mfma_f32_16x16x32_bf16 v[220:223], v[140:143], v[40:43], v[220:223]\n\t"
      "v_mfma_f32_16x16x32_bf16 v[224:227], v[140:143], v[44:47], v[224:227]\n\t"
      "v_mfma_f32_16x16x32_bf16 v[212:215], v[144:147], v[48:51], v[212:215]\n\t"
      "v_mfma_f32_16x16x32_bf16 v[216:219], v[144:147], v[52:55], v[216:219]\n\t"
      "v_mfma_f32_16x16x32_bf16 v[220:223], v[144:147], v[56:59], v[220:223]\n\t"
      "v_mfma_f32_16x16x32_bf16 v[224:227], v[144:147], v[60:63], v[224:227]\n\t"
      "v_mfma_f32_16x16x32_bf16 v[212:215], v[148:151], v[64:67], v[212:215]\n\t"
      "v_mfma_f32_16x16x32_bf16 v[216:219], v[148:151], v[68:71], v[216:219]\n\t"
      "v_mfma_f32_16x16x32_bf16 v[220:223], v[148:151], v[72:75], v[220:223]\n\t"
      "v_mfma_f32_16x16x32_bf16 v[224:227], v[148:151], v[76:79], v[224:227]\n\t"
      "v_mfma_f32_16x16x32_bf16 v[212:215], v[152:155], v[80:83], v[212:215]\n\t"
      "v_mfma_f32_16x16x32_bf16 v[216:219], v[152:155], v[84:87], v[216:219]\n\t"
      "v_mfma_f32_16x16x32_bf16 v[220:223], v[152:155], v[88:91], v[220:223]\n\t"
      "v_mfma_f32_16x16x32_bf16 v[224:227], v[152:155], v[92:95], v[224:227]\n\t"
      "v_mfma_f32_16x16x32_bf16 v[212:215], v[156:159], v[96:99], v[212:215]\n\t"
      "v_mfma_f32_16x16x32_bf16 v[216:219], v[156:159], v[100:103], v[216:219]\n\t"
      "v_mfma_f32_16x16x32_bf16 v[220:223], v[156:159], v[104:107], v[220:223]\n\t"
      "v_mfma_f32_16x16x32_bf16 v[224:227], v[156:159], v[108:111], v[224:227]\n\t"
      "s_nop 7\n\t"
      "s_branch LA45B_%=\n\t"
      "LA46R_%=:\n\t"
      "s_mov_b32 s80, 0\n\t"
      "LA46L_%=:\n\t"
      "s_add_u32 s80, s80, 1\n\t"
      "s_cmp_lt_u32 s80, 262144\n\t"
      "s_cbranch_scc0 LA46X_%=\n\t"
      "s_waitcnt vmcnt(0)\n\t"
      "PQ6R_%=:\n\t"
      "PQ6L_%=:\n\t"
      "s_add_u32 s80, s80, 1\n\t"
      "s_cmp_lt_u32 s80, 262144\n\t"
      "s_cbranch_scc0 PQ6X_%=\n\t"
      "s_sleep 1\n\t"
      "global_load_dword v251, v252, s[76:77] offset:252 sc1\n\t"
      "s_waitcnt vmcnt(0)\n\t"
      "v_cmp_eq_u32_e32 vcc, -1, v251\n\t"
      "s_cbranch_vccnz PQ6L_%=\n\t"
      "PQ6X_%=:\n\t"
      "s_add_u32 s58, s76, s40\n\t"
      "s_addc_u32 s59, s77, 0\n\t"
      "global_load_dwordx4 v[160:163], v244, s[58:59] sc1\n\t"
      "s_add_u32 s58, s76, s41\n\t"
      "s_addc_u32 s59, s77, 0\n\t"
      "global_load_dwordx4 v[164:167], v244, s[58:59] sc1\n\t"
      "s_add_u32 s58, s76, s42\n\t"
      "s_addc_u32 s59, s77, 0\n\t"
      "global_load_dwordx4 v[168:171], v244, s[58:59] sc1\n\t"
      "s_add_u32 s58, s76, s43\n\t"
      "s_addc_u32 s59, s77, 0\n\t"
      "global_load_dwordx4 v[172:175], v244, s[58:59] sc1\n\t"
      "s_add_u32 s58, s76, s44\n\t"
      "s_addc_u32 s59, s77, 0\n\t"
      "global_load_dwordx4 v[176:179], v244, s[58:59] sc1\n\t"
      "s_add_u32 s58, s76, s45\n\t"
      "s_addc_u32 s59, s77, 0\n\t"
      "global_load_dwordx4 v[180:183], v244, s[58:59] sc1\n\t"
      "s_add_u32 s58, s76, s46\n\t"
      "s_addc_u32 s59, s77, 0\n\t"
      "global_load_dwordx4 v[184:187], v244, s[58:59] sc1\n\t"
      "s_waitcnt vmcnt(0)\n\t"
      "v_max3_u32 v251, v160, v161, v162\n\t"
      "v_max3_u32 v251, v251, v163, v164\n\t"
      "v_max3_u32 v251, v251, v165, v166\n\t"
      "v_max3_u32 v251, v251, v167, v168\n\t"
      "v_max3_u32 v251, v251, v169, v170\n\t"
      "v_max3_u32 v251, v251, v171, v172\n\t"
      "v_max3_u32 v251, v251, v173, v174\n\t"
      "v_max3_u32 v251, v251, v175, v176\n\t"
      "v_max3_u32 v251, v251, v177, v178\n\t"
      "v_max3_u32 v251, v251, v179, v180\n\t"
      "v_max3_u32 v251, v251, v181, v182\n\t"
      "v_max3_u32 v251, v251, v183, v184\n\t"
      "v_max3_u32 v251, v251, v185, v186\n\t"
      "v_max_u32_e32 v251, v251, v187\n\t"
      "v_cmp_eq_u32_e32 vcc, -1, v251\n\t"
      "s_cbranch_vccnz LA46L_%=\n\t"
      "LA46X_%=:\n\t"
      "v_mfma_f32_16x16x32_bf16 v[196:199], v[160:163], v[0:3], 0\n\t"
      "v_mfma_f32_16x16x32_bf16 v[200:203], v[160:163], v[4:7], 0\n\t"
      "v_mfma_f32_16x16x32_bf16 v[204:207], v[160:163], v[8:11], 0\n\t"
      "v_mfma_f32_16x16x32_bf16 v[208:211], v[160:163], v[12:15], 0\n\t"
      "v_mfma_f32_16x16x32_bf16 v[196:199], v[164:167], v[16:19], v[196:199]\n\t"
      "v_mfma_f32_16x16x32_bf16 v[200:203], v[164:167], v[20:23], v[200:203]\n\t"
      "v_mfma_f32_16x16x32_bf16 v[204:207], v[164:167], v[24:27], v[204:207]\n\t"
      "v_mfma_f32_16x16x32_bf16 v[208:211], v[164:167], v[28:31], v[208:211]\n\t"
      "v_mfma_f32_16x16x32_bf16 v[196:199], v[168:171], v[32:35], v[196:199]\n\t"
      "v_mfma_f32_16x16x32_bf16 v[200:203], v[168:171], v[36:39], v[200:203]\n\t"
      "v_mfma_f32_16x16x32_bf16 v[204:207], v[168:171], v[40:43], v[204:207]\n\t"
      "v_mfma_f32_16x16x32_bf16 v[208:211], v[168:171], v[44:47], v[208:211]\n\t"
      "v_mfma_f32_16x16x32_bf16 v[196:199], v[172:175], v[48:51], v[196:199]\n\t"
      "v_mfma_f32_16x16x32_bf16 v[200:203], v[172:175], v[52:55], v[200:203]\n\t"
      "v_mfma_f32_16x16x32_bf16 v[204:207], v[172:175], v[56:59], v[204:207]\n\t"
      "v_mfma_f32_16x16x32_bf16 v[208:211], v[172:175], v[60:63], v[208:211]\n\t"
      "v_mfma_f32_16x16x32_bf16 v[196:199], v[176:179], v[64:67], v[196:199]\n\t"
      "v_mfma_f32_16x16x32_bf16 v[200:203], v[176:179], v[68:71], v[200:203]\n\t"
      "v_mfma_f32_16x16x32_bf16 v[204:207], v[176:179], v[72:75], v[204:207]\n\t"
      "v_mfma_f32_16x16x32_bf16 v[208:211], v[176:179], v[76:79], v[208:211]\n\t"
      "v_mfma_f32_16x16x32_bf16 v[196:199], v[180:183], v[80:83], v[196:199]\n\t"
      "v_mfma_f32_16x16x32_bf16 v[200:203], v[180:183], v[84:87], v[200:203]\n\t"
      "v_mfma_f32_16x16x32_bf16 v[204:207], v[180:183], v[88:91], v[204:207]\n\t"
      "v_mfma_f32_16x16x32_bf16 v[208:211], v[180:183], v[92:95], v[208:211]\n\t"
      "v_mfma_f32_16x16x32_bf16 v[196:199], v[184:187], v[96:99], v[196:199]\n\t"
      "v_mfma_f32_16x16x32_bf16 v[200:203], v[184:187], v[100:103], v[200:203]\n\t"
      "v_mfma_f32_16x16x32_bf16 v[204:207], v[184:187], v[104:107], v[204:207]\n\t"
      "v_mfma_f32_16x16x32_bf16 v[208:211], v[184:187], v[108:111], v[208:211]\n\t"
      "s_nop 7\n\t"
      "s_branch LA46B_%=\n\t"
      : "+{s[0:1]}"(ka), "+{v0}"(tid)
      : "{s2}"(blockIdx.x)
      : "memory", "vcc", "scc", "m0",
        "v1", "v2", "v3", "v4", "v5", "v6", "v7", "v8", "v9", "v10", "v11", "v12", "v13", "v14", "v15", "v16", "v17",
        "v18", "v19", "v20", "v21", "v22", "v23", "v24", "v25", "v26", "v27", "v28", "v29", "v30", "v31", "v32", "v33",
        "v34", "v35", "v36", "v37", "v38", "v39", "v40", "v41", "v42", "v43", "v44", "v45", "v46", "v47", "v48", "v49",
        "v50", "v51", "v52", "v53", "v54", "v55", "v56", "v57", "v58", "v59", "v60", "v61", "v62", "v63", "v64", "v65",
        "v66", "v67", "v68", "v69", "v70", "v71", "v72", "v73", "v74", "v75", "v76", "v77", "v78", "v79", "v80", "v81",
        "v82", "v83", "v84", "v85", "v86", "v87", "v88", "v89", "v90", "v91", "v92", "v93", "v94", "v95", "v96", "v97",
        "v98", "v99", "v100", "v101", "v102", "v103", "v104", "v105", "v106", "v107", "v108", "v109", "v110", "v111",
        "v112", "v113", "v114", "v115", "v116", "v117", "v118", "v119", "v120", "v121", "v122", "v123", "v124", "v125",
        "v126", "v127", "v128", "v129", "v130", "v131", "v132", "v133", "v134", "v135", "v136", "v137", "v138", "v139",
        "v140", "v141", "v142", "v143", "v144", "v145", "v146", "v147", "v148", "v149", "v150", "v151", "v152", "v153",
        "v154", "v155", "v156", "v157", "v158", "v159", "v160", "v161", "v162", "v163", "v164", "v165", "v166", "v167",
        "v168", "v169", "v170", "v171", "v172", "v173", "v174", "v175", "v176", "v177", "v178", "v179", "v180", "v181",
        "v182", "v183", "v184", "v185", "v186", "v187", "v188", "v189", "v190", "v191", "v192", "v193", "v194", "v195",
        "v196", "v197", "v198", "v199", "v200", "v201", "v202", "v203", "v204", "v205", "v206", "v207", "v208", "v209",
        "v210", "v211", "v212", "v213", "v214", "v215", "v216", "v217", "v218", "v219", "v220", "v221", "v222", "v223",
        "v224", "v225", "v226", "v227", "v228", "v229", "v230", "v231", "v232", "v233", "v234", "v235", "v236", "v237",
        "v238", "v239", "v240", "v241", "v242", "v243", "v244", "v245", "v246", "v247", "v248", "v249", "v250", "v251",
        "v252", "v253", "v254", "v255", "s3", "s4", "s5", "s6", "s7", "s8", "s9", "s10", "s11", "s12", "s13", "s14",
        "s15", "s16", "s17", "s18", "s19", "s20", "s21", "s22", "s23", "s24", "s25", "s26", "s27", "s28", "s29", "s30",
        "s31", "s32", "s33", "s34", "s35", "s36", "s37", "s38", "s39", "s40", "s41", "s42", "s43", "s44", "s45", "s46",
        "s47", "s48", "s49", "s50", "s51", "s52", "s53", "s54", "s55", "s56", "s57", "s58", "s59", "s60", "s61", "s62",
        "s63", "s64", "s65", "s66", "s67", "s68", "s69", "s70", "s71", "s72", "s73", "s74", "s75", "s76", "s77", "s78",
        "s79", "s80", "s81", "s82", "s83"
  );
#endif
}

extern "C" void kernel_launch(void* const* d_in, const int* in_sizes, int n_in, void* d_out, int out_size,
                              void* d_ws, size_t ws_size, hipStream_t stream) {
  (void)hipFuncSetAttribute((const void*)fused_block, hipFuncAttributeMaxDynamicSharedMemorySize, 163840);
  fused_block<<<256, 512, 163840, stream>>>((const u16*)d_in[0], (const u16*)d_in[1], (const u16*)d_in[2],
                                            (const u16*)d_in[4], (const u16*)d_in[5], (const u16*)d_in[6],
                                            (const u16*)d_in[7], (u16*)d_out, (char*)d_ws);
}
```

```cpp
#include <hip/hip_runtime.h>
#include <stdint.h>
typedef unsigned short u16;
__global__ __launch_bounds__(512, 2) void fused_block(const u16* x, const u16* wqkv, const u16* wo, const u16* w1, const u16* b1,
                                                   const u16* w2, const u16* b2, u16* out, char* ws) {
#if defined(__HIP_DEVICE_COMPILE__)
  unsigned long long ka = (unsigned long long)__builtin_amdgcn_kernarg_segment_ptr();
  unsigned tid = threadIdx.x;
  asm volatile(
      "s_load_dwordx16 s[4:19], s[0:1], 0x0\n\t"
      "s_load_dwordx2 s[20:21], s[0:1], 0x40\n\t"
      "v_lshrrev_b32_e32 v199, 6, v0\n\t"
      "v_and_b32_e32 v192, 63, v0\n\t"
      "v_readfirstlane_b32 s24, v199\n\t"
      "s_and_b32 s34, s2, 7\n\t"
      "s_lshr_b32 s36, s2, 3\n\t"
      "s_mul_i32 s35, s36, 43\n\t"
      "s_lshr_b32 s35, s35, 8\n\t"
      "s_mul_i32 s26, s35, 6\n\t"
      "s_sub_u32 s26, s36, s26\n\t"
      "s_add_u32 s26, s26, 1\n\t"
      "s_bfe_u32 s25, s2, 131076\n\t"
      "s_and_b32 s37, s36, 1\n\t"
      "s_mul_i32 s37, s37, 7\n\t"
      "s_cmp_ge_u32 s36, 24\n\t"
      "s_cselect_b32 s26, s37, s26\n\t"
      "s_cselect_b32 s35, s25, s35\n\t"
      "s_lshl2_add_u32 s25, s34, s35\n\t"
      "s_and_b32 s34, s25, 30\n\t"
      "s_lshl_b32 s34, s34, 6\n\t"
      "s_and_b32 s35, s25, 1\n\t"
      "s_add_u32 s38, s34, s35\n\t"
      "v_lshrrev_b32_e32 v197, 3, v192\n\t"
      "v_and_b32_e32 v198, 7, v192\n\t"
      "s_min_u32 s28, s26, 6\n\t"
      "s_lshl4_add_u32 s34, s24, s38\n\t"
      "s_mul_i32 s34, s34, 896\n\t"
      "s_movk_i32 s35, 1792\n\t"
      "v_lshl_add_u32 v199, v198, 4, s34\n\t"
      "v_mad_u32_u24 v199, v197, s35, v199\n\t"
      "s_lshl_b32 s34, s28, 7\n\t"
      "s_cmp_lg_u32 s24, 0\n\t"
      "s_cbranch_scc1 NTC_%=\n\t"
      "s_getpc_b64 s[58:59]\n\t"
      "s_lshr_b32 s37, s2, 3\n\t"
      "v_min_u32_e32 v200, 9, v192\n\t"
      "v_lshl_add_u32 v200, v200, 5, s37\n\t"
      "s_mov_b32 m0, 64512\n\t"
      "v_min_u32_e32 v200, 290, v200\n\t"
      "v_lshlrev_b32_e32 v200, 7, v200\n\t"
      "global_load_lds_dword v200, s[58:59]\n\t"
      "NTC_%=:\n\t"
      "v_add_u32_e32 v120, s34, v199\n\t"
      "s_add_u32 s35, s34, 128\n\t"
      "s_sub_u32 s36, s35, 896\n\t"
      "s_min_u32 s35, s35, s36\n\t"
      "v_add_u32_e32 v121, s35, v199\n\t"
      "s_waitcnt lgkmcnt(0)\n\t"
      "global_load_dwordx4 v[204:207], v120, s[4:5]\n\t"
      "s_add_u32 s35, s34, 256\n\t"
      "s_sub_u32 s36, s35, 896\n\t"
      "s_min_u32 s35, s35, s36\n\t"
      "v_add_u32_e32 v122, s35, v199\n\t"
      "global_load_dwordx4 v[4:7], v121, s[4:5]\n\t"
      "s_add_u32 s35, s34, 384\n\t"
      "s_sub_u32 s36, s35, 896\n\t"
      "s_min_u32 s35, s35, s36\n\t"
      "v_add_u32_e32 v123, s35, v199\n\t"
      "global_load_dwordx4 v[8:11], v122, s[4:5]\n\t"
      "s_add_u32 s35, s34, 512\n\t"
      "s_sub_u32 s36, s35, 896\n\t"
      "s_min_u32 s35, s35, s36\n\t"
      "v_add_u32_e32 v124, s35, v199\n\t"
      "global_load_dwordx4 v[12:15], v123, s[4:5]\n\t"
      "s_add_u32 s35, s34, 640\n\t"
      "s_sub_u32 s36, s35, 896\n\t"
      "s_min_u32 s35, s35, s36\n\t"
      "v_add_u32_e32 v125, s35, v199\n\t"
      "s_add_u32 s35, s34, 768\n\t"
      "s_sub_u32 s36, s35, 896\n\t"
      "s_min_u32 s35, s35, s36\n\t"
      "v_add_u32_e32 v126, s35, v199\n\t"
      "s_cmp_lt_u32 s25, 2\n\t"
      "s_cselect_b32 s34, 0, 16\n\t"
      "s_sub_u32 s34, s38, s34\n\t"
      "s_lshl1_add_u32 s34, s24, s34\n\t"
      "s_mul_i32 s34, s34, 896\n\t"
      "s_and_b32 s33, s25, 3\n\t"
      "s_lshl_b32 s33, s33, 1\n\t"
      "s_and_b32 s35, s26, 1\n\t"
      "s_add_u32 s33, s33, s35\n\t"
      "s_cmp_eq_u32 s33, 7\n\t"
      "s_cselect_b32 s33, 0, s33\n\t"
      "v_min_u32_e32 v200, 55, v192\n\t"
      "s_lshl_b32 s35, s33, 3\n\t"
      "v_add_u32_e32 v201, s35, v200\n\t"
      "v_subrev_u32_e32 v202, 56, v201\n\t"
      "v_min_u32_e32 v201, v201, v202\n\t"
      "v_lshl_add_u32 v127, v201, 4, s34\n\t"
      "s_and_b32 s39, s24, 3\n\t"
      "s_lshl_b32 s39, s39, 12\n\t"
      "s_cmp_lt_u32 s24, 4\n\t"
      "s_cselect_b32 s27, 4, 0\n\t"
      "v_lshrrev_b32_e32 v193, 4, v192\n\t"
      "v_and_b32_e32 v194, 15, v192\n\t"
      "v_bfe_u32 v195, v192, 2, 2\n\t"
      "v_and_b32_e32 v196, 3, v192\n\t"
      "v_lshlrev_b32_e32 v244, 4, v192\n\t"
      "s_lshr_b32 s34, s24, 1\n\t"
      "s_mul_i32 s34, s34, 14336\n\t"
      "s_and_b32 s35, s24, 1\n\t"
      "s_lshl_b32 s35, s35, 7\n\t"
      "s_add_u32 s34, s34, s35\n\t"
      "v_xor_b32_e32 v199, v197, v198\n\t"
      "v_lshl_add_u32 v199, v199, 4, s34\n\t"
      "v_lshl_add_u32 v199, v198, 8, v199\n\t"
      "s_sub_u32 s35, s28, s33\n\t"
      "s_cselect_b32 s36, 7, 0\n\t"
      "s_add_u32 s35, s35, s36\n\t"
      "s_lshl_b32 s35, s35, 11\n\t"
      "s_add_u32 s34, s35, 0\n\t"
      "s_sub_u32 s36, s34, 14336\n\t"
      "s_min_u32 s34, s34, s36\n\t"
      "v_add_u32_e32 v112, s34, v199\n\t"
      "s_add_u32 s34, s35, 2048\n\t"
      "s_sub_u32 s36, s34, 14336\n\t"
      "s_min_u32 s34, s34, s36\n\t"
      "v_add_u32_e32 v113, s34, v199\n\t"
      "s_add_u32 s34, s35, 4096\n\t"
      "s_sub_u32 s36, s34, 14336\n\t"
      "s_min_u32 s34, s34, s36\n\t"
      "v_add_u32_e32 v114, s34, v199\n\t"
      "s_add_u32 s34, s35, 6144\n\t"
      "s_sub_u32 s36, s34, 14336\n\t"
      "s_min_u32 s34, s34, s36\n\t"
      "v_add_u32_e32 v115, s34, v199\n\t"
      "s_add_u32 s34, s35, 8192\n\t"
      "s_sub_u32 s36, s34, 14336\n\t"
      "s_min_u32 s34, s34, s36\n\t"
      "v_add_u32_e32 v116, s34, v199\n\t"
      "s_add_u32 s34, s35, 10240\n\t"
      "s_sub_u32 s36, s34, 14336\n\t"
      "s_min_u32 s34, s34, s36\n\t"
      "v_add_u32_e32 v117, s34, v199\n\t"
      "s_add_u32 s34, s35, 12288\n\t"
      "s_sub_u32 s36, s34, 14336\n\t"
      "s_min_u32 s34, s34, s36\n\t"
      "v_add_u32_e32 v118, s34, v199\n\t"
      "v_min_u32_e32 v200, 55, v192\n\t"
      "v_lshrrev_b32_e32 v201, 2, v200\n\t"
      "v_lshlrev_b32_e32 v119, 10, v201\n\t"
      "v_cmp_lt_u32_e32 vcc, 6, v201\n\t"
      "v_mov_b32_e32 v202, 57344\n\t"
      "v_mov_b32_e32 v201, 50688\n\t"
      "v_cndmask_b32_e32 v202, v202, v201, vcc\n\t"
      "v_and_b32_e32 v201, 3, v200\n\t"
      "v_lshl_add_u32 v202, v201, 7, v202\n\t"
      "v_and_b32_e32 v200, 7, v200\n\t"
      "v_xor_b32_e32 v200, s24, v200\n\t"
      "v_lshl_add_u32 v202, v200, 4, v202\n\t"
      "v_add_u32_e32 v119, v119, v202\n\t"
      "s_bitcmp1_b32 s27, 2\n\t"
      "s_cbranch_scc1 LSET_%=\n\t"
      "v_xor_b32_e32 v199, v194, v193\n\t"
      "v_lshlrev_b32_e32 v199, 4, v199\n\t"
      "v_lshl_or_b32 v200, v193, 8, v199\n\t"
      "s_and_b32 s34, s24, 1\n\t"
      "s_mul_i32 s35, s34, 28672\n\t"
      "v_add_u32_e32 v160, s35, v200\n\t"
      "v_xor_b32_e32 v161, 64, v160\n\t"
      "v_and_b32_e32 v199, 0x70, v199\n\t"
      "v_lshl_or_b32 v199, v193, 7, v199\n\t"
      "v_add_u32_e32 v199, 57344, v199\n\t"
      "v_add_u32_e32 v200, 14336, v200\n\t"
      "s_cmp_eq_u32 s34, 1\n\t"
      "s_cselect_b64 vcc, -1, 0\n\t"
      "s_cselect_b32 s35, 0, 6656\n\t"
      "v_cndmask_b32_e32 v162, v199, v200, vcc\n\t"
      "v_xor_b32_e32 v163, 64, v162\n\t"
      "v_subrev_u32_e32 v164, s35, v162\n\t"
      "v_subrev_u32_e32 v165, s35, v163\n\t"
      "s_bfe_u32 s35, s24, 65537\n\t"
      "s_mul_i32 s35, s35, 7\n\t"
      "v_and_b32_e32 v201, 24, v192\n\t"
      "v_lshlrev_b32_e32 v201, 2, v201\n\t"
      "v_lshlrev_b32_e32 v202, 7, v195\n\t"
      "v_lshl_or_b32 v202, v193, 12, v202\n\t"
      "v_add_u32_e32 v202, 65536, v202\n\t"
      "v_add_lshl_u32 v199, v196, s35, 3\n\t"
      "v_xor_b32_e32 v199, v199, v201\n\t"
      "v_add_u32_e32 v166, v199, v202\n\t"
      "v_min_u32_e32 v199, 2, v196\n\t"
      "s_add_u32 s35, s35, 4\n\t"
      "v_add_lshl_u32 v199, v199, s35, 3\n\t"
      "v_xor_b32_e32 v199, v199, v201\n\t"
      "v_add_u32_e32 v167, v199, v202\n\t"
      "v_add_u32_e32 v168, 0x10000, v166\n\t"
      "v_add_u32_e32 v169, 0x10000, v167\n\t"
      "v_lshlrev_b32_e32 v199, 11, v193\n\t"
      "v_sub_u32_e32 v170, v166, v199\n\t"
      "v_sub_u32_e32 v171, v167, v199\n\t"
      "s_branch NORM_%=\n\t"
      "LSET_%=:\n\t"
      "s_add_u32 s60, s20, 16777216\n\t"
      "s_addc_u32 s61, s21, 0\n\t"
      "s_load_dwordx2 s[22:23], s[60:61], 0x0\n\t"
      "s_lshr_b32 s34, s39, 9\n\t"
      "v_add_u32_e32 v199, s34, v197\n\t"
      "s_and_b32 s34, s24, 1\n\t"
      "s_lshl_b32 s34, s34, 2\n\t"
      "v_and_b32_e32 v200, 2, v197\n\t"
      "v_or_b32_e32 v200, s34, v200\n\t"
      "v_xor_b32_e32 v200, v200, v198\n\t"
      "v_min_u32_e32 v200, 6, v200\n\t"
      "v_lshlrev_b32_e32 v200, 4, v200\n\t"
      "s_movk_i32 s34, 2688\n\t"
      "v_mad_u32_u24 v149, v199, s34, v200\n\t"
      "v_add_u32_e32 v148, 128, v149\n\t"
      "v_add_u32_e32 v150, 84096, v149\n\t"
      "v_add_u32_e32 v151, 83968, v149\n\t"
      "v_add_u32_e32 v152, 85120, v149\n\t"
      "s_sub_u32 s6, s6, 128\n\t"
      "s_subb_u32 s7, s7, 0\n\t"
      "s_mul_i32 s37, s26, 112\n\t"
      "s_mov_b32 s81, s33\n\t"
      "s_mov_b32 s82, s33\n\t"
      "s_and_b32 s35, s25, 3\n\t"
      "s_lshl2_add_u32 s31, s26, s35\n\t"
      "s_and_b32 s35, s2, 7\n\t"
      "s_mul_i32 s34, s35, 14\n\t"
      "s_lshl4_add_u32 s35, s35, -14\n\t"
      "s_cmp_lt_u32 s31, 14\n\t"
      "s_cselect_b32 s34, s34, s35\n\t"
      "s_cselect_b32 s36, 1, 0\n\t"
      "s_or_b32 s27, s27, s36\n\t"
      "s_add_u32 s34, s34, s31\n\t"
      "s_lshr_b32 s29, s34, 2\n\t"
      "s_and_b32 s32, s34, 3\n\t"
      "s_mul_i32 s30, s32, 7\n\t"
      "v_and_b32_e32 v199, 60, v192\n\t"
      "v_and_b32_e32 v200, 24, v192\n\t"
      "v_lshlrev_b32_e32 v202, 3, v196\n\t"
      "v_lshl_or_b32 v202, v200, 2, v202\n\t"
      "v_lshl_or_b32 v202, v195, 7, v202\n\t"
      "v_lshl_or_b32 v202, v193, 10, v202\n\t"
      "v_add_u32_e32 v224, s39, v202\n\t"
      "v_xor_b32_e32 v225, 32, v224\n\t"
      "v_xor_b32_e32 v226, 64, v224\n\t"
      "v_xor_b32_e32 v227, 96, v224\n\t"
      "s_lshl_b32 s34, s24, 12\n\t"
      "s_add_u32 s34, s34, 98304\n\t"
      "v_add_u32_e32 v245, s34, v244\n\t"
      "v_lshlrev_b32_e32 v199, 2, v199\n\t"
      "s_lshl_b32 s34, s24, 8\n\t"
      "v_lshl_add_u32 v247, v192, 2, s34\n\t"
      "v_and_b32_e32 v200, 2, v192\n\t"
      "v_mul_u32_u24_e32 v202, 576, v200\n\t"
      "v_xor_b32_e32 v199, v202, v199\n\t"
      "v_and_b32_e32 v202, 1, v192\n\t"
      "v_lshl_or_b32 v199, v202, 3, v199\n\t"
      "s_and_b32 s35, s34, 0x400\n\t"
      "s_add_u32 s34, s34, s35\n\t"
      "s_add_u32 s34, s34, 98304\n\t"
      "v_add_u32_e32 v246, s34, v199\n\t"
      "v_lshlrev_b32_e32 v199, 2, v202\n\t"
      "v_lshl_or_b32 v199, v200, 4, v199\n\t"
      "s_min_u32 s34, s29, 27\n\t"
      "s_lshl_b32 s34, s34, 7\n\t"
      "s_lshr_b32 s35, s24, 2\n\t"
      "s_lshl_b32 s35, s35, 6\n\t"
      "s_add_u32 s34, s34, s35\n\t"
      "s_lshr_b32 s35, s39, 9\n\t"
      "s_add_u32 s34, s34, s35\n\t"
      "v_add_u32_e32 v248, s34, v199\n\t"
      "s_add_u32 s48, s20, 8388608\n\t"
      "s_addc_u32 s49, s21, 0\n\t"
      "v_and_b32_e32 v199, 2, v197\n\t"
      "v_xor_b32_e32 v199, v199, v198\n\t"
      "v_lshlrev_b32_e32 v199, 4, v199\n\t"
      "s_movk_i32 s34, 7168\n\t"
      "v_mad_u32_u24 v144, v197, s34, v199\n\t"
      "v_xor_b32_e32 v199, 64, v144\n\t"
      "v_add_u32_e32 v145, 56320, v199\n\t"
      "v_add_u32_e32 v146, 112640, v144\n\t"
      "v_add_u32_e32 v147, 168960, v199\n\t"
      "v_and_b32_e32 v199, 6, v197\n\t"
      "v_xor_b32_e32 v199, v199, v198\n\t"
      "v_lshlrev_b32_e32 v199, 4, v199\n\t"
      "s_movk_i32 s35, 896\n\t"
      "v_mad_u32_u24 v154, v197, s35, v199\n\t"
      "v_add_u32_e32 v155, 6144, v154\n\t"
      "v_add_u32_e32 v156, 12288, v154\n\t"
      "v_add_u32_e32 v157, 18432, v154\n\t"
      "s_mul_i32 s34, s24, 917504\n\t"
      "s_lshl_b32 s35, s29, 7\n\t"
      "s_add_u32 s34, s34, s35\n\t"
      "s_add_u32 s52, s10, s34\n\t"
      "s_addc_u32 s53, s11, 0\n\t"
      "s_lshl_b32 s34, s28, 7\n\t"
      "s_add_u32 s56, s8, s34\n\t"
      "s_addc_u32 s57, s9, 0\n\t"
      "s_mov_b32 s34, 0\n\t"
      "s_and_b32 s35, s34, 1\n\t"
      "s_and_b32 s34, s34, 2\n\t"
      "s_lshl_b32 s34, s34, 6\n\t"
      "s_add_u32 s34, s34, s35\n\t"
      "s_mul_i32 s76, s34, 3584\n\t"
      "s_mov_b32 s34, 1\n\t"
      "s_and_b32 s35, s34, 1\n\t"
      "s_and_b32 s34, s34, 2\n\t"
      "s_lshl_b32 s34, s34, 6\n\t"
      "s_add_u32 s34, s34, s35\n\t"
      "s_mul_i32 s77, s34, 3584\n\t"
      "s_mov_b32 s34, 2\n\t"
      "s_and_b32 s35, s34, 1\n\t"
      "s_and_b32 s34, s34, 2\n\t"
      "s_lshl_b32 s34, s34, 6\n\t"
      "s_add_u32 s34, s34, s35\n\t"
      "s_mul_i32 s78, s34, 3584\n\t"
      "s_mov_b32 s34, 3\n\t"
      "s_and_b32 s35, s34, 1\n\t"
      "s_and_b32 s34, s34, 2\n\t"
      "s_lshl_b32 s34, s34, 6\n\t"
      "s_add_u32 s34, s34, s35\n\t"
      "s_mul_i32 s79, s34, 3584\n\t"
      "NORM_%=:\n\t"
      "global_load_dwordx4 v[16:19], v124, s[4:5]\n\t"
      "global_load_dwordx4 v[20:23], v125, s[4:5]\n\t"
      "global_load_dwordx4 v[24:27], v126, s[4:5]\n\t"
      "global_load_dwordx4 v[28:31], v127, s[4:5]\n\t"
      "v_mov_b32_e32 v199, 0x3f803f80\n\t"
      "s_waitcnt vmcnt(7)\n\t"
      "v_dot2_f32_bf16 v88, v204, v199, 0\n\t"
      "v_dot2_f32_bf16 v92, v204, v204, 0\n\t"
      "v_lshlrev_b32_e32 v32, 16, v204\n\t"
      "v_and_b32_e32 v33, 0xffff0000, v204\n\t"
      "v_dot2_f32_bf16 v89, v205, v199, 0\n\t"
      "v_dot2_f32_bf16 v93, v205, v205, 0\n\t"
      "v_lshlrev_b32_e32 v34, 16, v205\n\t"
      "v_and_b32_e32 v35, 0xffff0000, v205\n\t"
      "v_dot2_f32_bf16 v90, v206, v199, 0\n\t"
      "v_dot2_f32_bf16 v94, v206, v206, 0\n\t"
      "v_lshlrev_b32_e32 v36, 16, v206\n\t"
      "v_and_b32_e32 v37, 0xffff0000, v206\n\t"
      "v_dot2_f32_bf16 v91, v207, v199, 0\n\t"
      "v_dot2_f32_bf16 v95, v207, v207, 0\n\t"
      "v_lshlrev_b32_e32 v38, 16, v207\n\t"
      "v_and_b32_e32 v39, 0xffff0000, v207\n\t"
      "s_waitcnt vmcnt(6)\n\t"
      "v_dot2_f32_bf16 v88, v4, v199, v88\n\t"
      "v_dot2_f32_bf16 v92, v4, v4, v92\n\t"
      "v_lshlrev_b32_e32 v40, 16, v4\n\t"
      "v_and_b32_e32 v41, 0xffff0000, v4\n\t"
      "v_dot2_f32_bf16 v89, v5, v199, v89\n\t"
      "v_dot2_f32_bf16 v93, v5, v5, v93\n\t"
      "v_lshlrev_b32_e32 v42, 16, v5\n\t"
      "v_and_b32_e32 v43, 0xffff0000, v5\n\t"
      "v_dot2_f32_bf16 v90, v6, v199, v90\n\t"
      "v_dot2_f32_bf16 v94, v6, v6, v94\n\t"
      "v_lshlrev_b32_e32 v44, 16, v6\n\t"
      "v_and_b32_e32 v45, 0xffff0000, v6\n\t"
      "v_dot2_f32_bf16 v91, v7, v199, v91\n\t"
      "v_dot2_f32_bf16 v95, v7, v7, v95\n\t"
      "v_lshlrev_b32_e32 v46, 16, v7\n\t"
      "v_and_b32_e32 v47, 0xffff0000, v7\n\t"
      "s_waitcnt vmcnt(5)\n\t"
      "v_dot2_f32_bf16 v88, v8, v199, v88\n\t"
      "v_dot2_f32_bf16 v92, v8, v8, v92\n\t"
      "v_lshlrev_b32_e32 v48, 16, v8\n\t"
      "v_and_b32_e32 v49, 0xffff0000, v8\n\t"
      "v_dot2_f32_bf16 v89, v9, v199, v89\n\t"
      "v_dot2_f32_bf16 v93, v9, v9, v93\n\t"
      "v_lshlrev_b32_e32 v50, 16, v9\n\t"
      "v_and_b32_e32 v51, 0xffff0000, v9\n\t"
      "v_dot2_f32_bf16 v90, v10, v199, v90\n\t"
      "v_dot2_f32_bf16 v94, v10, v10, v94\n\t"
      "v_lshlrev_b32_e32 v52, 16, v10\n\t"
      "v_and_b32_e32 v53, 0xffff0000, v10\n\t"
      "v_dot2_f32_bf16 v91, v11, v199, v91\n\t"
      "v_dot2_f32_bf16 v95, v11, v11, v95\n\t"
      "v_lshlrev_b32_e32 v54, 16, v11\n\t"
      "v_and_b32_e32 v55, 0xffff0000, v11\n\t"
      "s_waitcnt vmcnt(4)\n\t"
      "v_dot2_f32_bf16 v88, v12, v199, v88\n\t"
      "v_dot2_f32_bf16 v92, v12, v12, v92\n\t"
      "v_lshlrev_b32_e32 v56, 16, v12\n\t"
      "v_and_b32_e32 v57, 0xffff0000, v12\n\t"
      "v_dot2_f32_bf16 v89, v13, v199, v89\n\t"
      "v_dot2_f32_bf16 v93, v13, v13, v93\n\t"
      "v_lshlrev_b32_e32 v58, 16, v13\n\t"
      "v_and_b32_e32 v59, 0xffff0000, v13\n\t"
      "v_dot2_f32_bf16 v90, v14, v199, v90\n\t"
      "v_dot2_f32_bf16 v94, v14, v14, v94\n\t"
      "v_lshlrev_b32_e32 v60, 16, v14\n\t"
      "v_and_b32_e32 v61, 0xffff0000, v14\n\t"
      "v_dot2_f32_bf16 v91, v15, v199, v91\n\t"
      "v_dot2_f32_bf16 v95, v15, v15, v95\n\t"
      "v_lshlrev_b32_e32 v62, 16, v15\n\t"
      "v_and_b32_e32 v63, 0xffff0000, v15\n\t"
      "s_waitcnt vmcnt(3)\n\t"
      "v_dot2_f32_bf16 v88, v16, v199, v88\n\t"
      "v_dot2_f32_bf16 v92, v16, v16, v92\n\t"
      "v_lshlrev_b32_e32 v64, 16, v16\n\t"
      "v_and_b32_e32 v65, 0xffff0000, v16\n\t"
      "v_dot2_f32_bf16 v89, v17, v199, v89\n\t"
      "v_dot2_f32_bf16 v93, v17, v17, v93\n\t"
      "v_lshlrev_b32_e32 v66, 16, v17\n\t"
      "v_and_b32_e32 v67, 0xffff0000, v17\n\t"
      "v_dot2_f32_bf16 v90, v18, v199, v90\n\t"
      "v_dot2_f32_bf16 v94, v18, v18, v94\n\t"
      "v_lshlrev_b32_e32 v68, 16, v18\n\t"
      "v_and_b32_e32 v69, 0xffff0000, v18\n\t"
      "v_dot2_f32_bf16 v91, v19, v199, v91\n\t"
      "v_dot2_f32_bf16 v95, v19, v19, v95\n\t"
      "v_lshlrev_b32_e32 v70, 16, v19\n\t"
      "v_and_b32_e32 v71, 0xffff0000, v19\n\t"
      "s_waitcnt vmcnt(2)\n\t"
      "v_dot2_f32_bf16 v88, v20, v199, v88\n\t"
      "v_dot2_f32_bf16 v92, v20, v20, v92\n\t"
      "v_lshlrev_b32_e32 v72, 16, v20\n\t"
      "v_and_b32_e32 v73, 0xffff0000, v20\n\t"
      "v_dot2_f32_bf16 v89, v21, v199, v89\n\t"
      "v_dot2_f32_bf16 v93, v21, v21, v93\n\t"
      "v_lshlrev_b32_e32 v74, 16, v21\n\t"
      "v_and_b32_e32 v75, 0xffff0000, v21\n\t"
      "v_dot2_f32_bf16 v90, v22, v199, v90\n\t"
      "v_dot2_f32_bf16 v94, v22, v22, v94\n\t"
      "v_lshlrev_b32_e32 v76, 16, v22\n\t"
      "v_and_b32_e32 v77, 0xffff0000, v22\n\t"
      "v_dot2_f32_bf16 v91, v23, v199, v91\n\t"
      "v_dot2_f32_bf16 v95, v23, v23, v95\n\t"
      "v_lshlrev_b32_e32 v78, 16, v23\n\t"
      "v_and_b32_e32 v79, 0xffff0000, v23\n\t"
      "s_waitcnt vmcnt(1)\n\t"
      "v_dot2_f32_bf16 v88, v24, v199, v88\n\t"
      "v_dot2_f32_bf16 v92, v24, v24, v92\n\t"
      "v_lshlrev_b32_e32 v80, 16, v24\n\t"
      "v_and_b32_e32 v81, 0xffff0000, v24\n\t"
      "v_dot2_f32_bf16 v89, v25, v199, v89\n\t"
      "v_dot2_f32_bf16 v93, v25, v25, v93\n\t"
      "v_lshlrev_b32_e32 v82, 16, v25\n\t"
      "v_and_b32_e32 v83, 0xffff0000, v25\n\t"
      "v_dot2_f32_bf16 v90, v26, v199, v90\n\t"
      "v_dot2_f32_bf16 v94, v26, v26, v94\n\t"
      "v_lshlrev_b32_e32 v84, 16, v26\n\t"
      "v_and_b32_e32 v85, 0xffff0000, v26\n\t"
      "v_dot2_f32_bf16 v91, v27, v199, v91\n\t"
      "v_dot2_f32_bf16 v95, v27, v27, v95\n\t"
      "v_lshlrev_b32_e32 v86, 16, v27\n\t"
      "v_and_b32_e32 v87, 0xffff0000, v27\n\t"
      "s_waitcnt vmcnt(0)\n\t"
      "s_bitcmp0_b32 s27, 2\n\t"
      "s_cbranch_scc1 SK1_%=\n\t"
      "s_mul_i32 s72, s81, 172032\n\t"
      "s_add_u32 s72, s72, s37\n\t"
      "s_add_u32 s40, s6, s72\n\t"
      "s_addc_u32 s41, s7, 0\n\t"
      "s_add_u32 s81, s81, 1\n\t"
      "s_cmp_eq_u32 s81, 7\n\t"
      "s_cselect_b32 s81, 0, s81\n\t"
      "s_add_u32 m0, s39, 131072\n\t"
      "s_nop 0\n\t"
      "global_load_lds_dwordx4 v148, s[40:41]\n\t"
      "global_load_lds_dwordx4 v149, s[40:41] offset:1024\n\t"
      "global_load_lds_dwordx4 v150, s[40:41] offset:2048\n\t"
      "global_load_lds_dwordx4 v151, s[40:41] offset:3072\n\t"
      "SK1_%=:\n\t"
      "v_dot2_f32_bf16 v96, v28, v199, 0\n\t"
      "v_dot2_f32_bf16 v97, v28, v28, 0\n\t"
      "v_dot2_f32_bf16 v96, v29, v199, v96\n\t"
      "v_dot2_f32_bf16 v97, v29, v29, v97\n\t"
      "v_dot2_f32_bf16 v96, v30, v199, v96\n\t"
      "v_dot2_f32_bf16 v97, v30, v30, v97\n\t"
      "v_dot2_f32_bf16 v96, v31, v199, v96\n\t"
      "v_dot2_f32_bf16 v97, v31, v31, v97\n\t"
      "v_cmp_gt_u32_e32 vcc, 56, v192\n\t"
      "v_pk_add_f32 v[88:89], v[88:89], v[90:91]\n\t"
      "v_pk_add_f32 v[92:93], v[92:93], v[94:95]\n\t"
      "v_cndmask_b32_e32 v96, 0, v96, vcc\n\t"
      "v_cndmask_b32_e32 v97, 0, v97, vcc\n\t"
      "v_add_f32_e32 v88, v88, v89\n\t"
      "v_add_f32_e32 v92, v92, v93\n\t"
      "v_add_f32_dpp v96, v96, v96 quad_perm:[1,0,3,2] row_mask:0xf bank_mask:0xf\n\t"
      "v_add_f32_dpp v97, v97, v97 quad_perm:[1,0,3,2] row_mask:0xf bank_mask:0xf\n\t"
      "v_add_f32_dpp v88, v88, v88 quad_perm:[1,0,3,2] row_mask:0xf bank_mask:0xf\n\t"
      "v_add_f32_dpp v92, v92, v92 quad_perm:[1,0,3,2] row_mask:0xf bank_mask:0xf\n\t"
      "v_add_f32_dpp v96, v96, v96 quad_perm:[2,3,0,1] row_mask:0xf bank_mask:0xf\n\t"
      "v_add_f32_dpp v97, v97, v97 quad_perm:[2,3,0,1] row_mask:0xf bank_mask:0xf\n\t"
      "v_add_f32_dpp v88, v88, v88 quad_perm:[2,3,0,1] row_mask:0xf bank_mask:0xf\n\t"
      "v_add_f32_dpp v92, v92, v92 quad_perm:[2,3,0,1] row_mask:0xf bank_mask:0xf\n\t"
      "v_add_f32_dpp v96, v96, v96 row_half_mirror row_mask:0xf bank_mask:0xf\n\t"
      "v_add_f32_dpp v97, v97, v97 row_half_mirror row_mask:0xf bank_mask:0xf\n\t"
      "v_add_f32_dpp v88, v88, v88 row_half_mirror row_mask:0xf bank_mask:0xf\n\t"
      "v_add_f32_dpp v92, v92, v92 row_half_mirror row_mask:0xf bank_mask:0xf\n\t"
      "v_mul_f32_e32 v88, 0xbb124925, v88\n\t"
      "v_add_f32_dpp v96, v96, v96 row_mirror row_mask:0xf bank_mask:0xf\n\t"
      "v_mul_f32_e32 v92, 0x3b124925, v92\n\t"
      "s_bitcmp0_b32 s27, 2\n\t"
      "s_cbranch_scc1 SK2_%=\n\t"
      "s_mul_i32 s72, s81, 172032\n\t"
      "s_add_u32 s72, s72, s37\n\t"
      "s_add_u32 s40, s6, s72\n\t"
      "s_addc_u32 s41, s7, 0\n\t"
      "s_add_u32 s81, s81, 1\n\t"
      "s_cmp_eq_u32 s81, 7\n\t"
      "s_cselect_b32 s81, 0, s81\n\t"
      "s_add_u32 m0, s39, 147456\n\t"
      "s_nop 0\n\t"
      "global_load_lds_dwordx4 v148, s[40:41]\n\t"
      "global_load_lds_dwordx4 v149, s[40:41] offset:1024\n\t"
      "SK2_%=:\n\t"
      "v_add_f32_dpp v97, v97, v97 row_mirror row_mask:0xf bank_mask:0xf\n\t"
      "v_fma_f32 v92, -v88, v88, v92\n\t"
      "s_nop 0\n\t"
      "v_permlane16_swap_b32_e32 v96, v97\n\t"
      "v_max_f32_e32 v92, 0, v92\n\t"
      "v_add_f32_e32 v96, v96, v97\n\t"
      "v_add_f32_e32 v92, 0x3727c5ac, v92\n\t"
      "v_mov_b32_e32 v97, v96\n\t"
      "v_rsq_f32_e32 v92, v92\n\t"
      "s_nop 0\n\t"
      "v_permlane32_swap_b32_e32 v96, v97\n\t"
      "v_add_f32_e32 v96, v96, v97\n\t"
      "v_mul_f32_e32 v88, v88, v92\n\t"
      "v_mov_b32_e32 v97, v96\n\t"
      "v_pk_fma_f32 v[32:33], v[32:33], v[92:93], v[88:89] op_sel_hi:[1,0,0]\n\t"
      "v_pk_fma_f32 v[34:35], v[34:35], v[92:93], v[88:89] op_sel_hi:[1,0,0]\n\t"
      "v_permlane16_swap_b32_e32 v96, v97\n\t"
      "v_pk_fma_f32 v[36:37], v[36:37], v[92:93], v[88:89] op_sel_hi:[1,0,0]\n\t"
      "v_pk_fma_f32 v[38:39], v[38:39], v[92:93], v[88:89] op_sel_hi:[1,0,0]\n\t"
      "v_mul_f32_e32 v96, 0xbb124925, v96\n\t"
      "v_cvt_pk_bf16_f32 v0, v32, v33\n\t"
      "v_cvt_pk_bf16_f32 v1, v34, v35\n\t"
      "v_cvt_pk_bf16_f32 v2, v36, v37\n\t"
      "v_cvt_pk_bf16_f32 v3, v38, v39\n\t"
      "ds_write_b128 v112, v[0:3]\n\t"
      "v_pk_fma_f32 v[40:41], v[40:41], v[92:93], v[88:89] op_sel_hi:[1,0,0]\n\t"
      "v_pk_fma_f32 v[42:43], v[42:43], v[92:93], v[88:89] op_sel_hi:[1,0,0]\n\t"
      "v_mul_f32_e32 v97, 0x3b124925, v97\n\t"
      "v_pk_fma_f32 v[44:45], v[44:45], v[92:93], v[88:89] op_sel_hi:[1,0,0]\n\t"
      "v_pk_fma_f32 v[46:47], v[46:47], v[92:93], v[88:89] op_sel_hi:[1,0,0]\n\t"
      "v_fma_f32 v97, -v96, v96, v97\n\t"
      "v_cvt_pk_bf16_f32 v4, v40, v41\n\t"
      "s_bitcmp0_b32 s27, 2\n\t"
      "s_cbranch_scc1 SK3_%=\n\t"
      "global_load_lds_dwordx4 v150, s[40:41] offset:2048\n\t"
      "global_load_lds_dwordx4 v151, s[40:41] offset:3072\n\t"
      "SK3_%=:\n\t"
      "v_cvt_pk_bf16_f32 v5, v42, v43\n\t"
      "v_cvt_pk_bf16_f32 v6, v44, v45\n\t"
      "v_cvt_pk_bf16_f32 v7, v46, v47\n\t"
      "ds_write_b128 v113, v[4:7]\n\t"
      "v_pk_fma_f32 v[48:49], v[48:49], v[92:93], v[88:89] op_sel_hi:[1,0,0]\n\t"
      "v_pk_fma_f32 v[50:51], v[50:51], v[92:93], v[88:89] op_sel_hi:[1,0,0]\n\t"
      "v_max_f32_e32 v97, 0, v97\n\t"
      "v_pk_fma_f32 v[52:53], v[52:53], v[92:93], v[88:89] op_sel_hi:[1,0,0]\n\t"
      "v_pk_fma_f32 v[54:55], v[54:55], v[92:93], v[88:89] op_sel_hi:[1,0,0]\n\t"
      "v_add_f32_e32 v97, 0x3727c5ac, v97\n\t"
      "v_cvt_pk_bf16_f32 v8, v48, v49\n\t"
      "v_cvt_pk_bf16_f32 v9, v50, v51\n\t"
      "v_cvt_pk_bf16_f32 v10, v52, v53\n\t"
      "v_cvt_pk_bf16_f32 v11, v54, v55\n\t"
      "ds_write_b128 v114, v[8:11]\n\t"
      "v_pk_fma_f32 v[56:57], v[56:57], v[92:93], v[88:89] op_sel_hi:[1,0,0]\n\t"
      "v_pk_fma_f32 v[58:59], v[58:59], v[92:93], v[88:89] op_sel_hi:[1,0,0]\n\t"
      "v_rsq_f32_e32 v97, v97\n\t"
      "v_pk_fma_f32 v[60:61], v[60:61], v[92:93], v[88:89] op_sel_hi:[1,0,0]\n\t"
      "v_pk_fma_f32 v[62:63], v[62:63], v[92:93], v[88:89] op_sel_hi:[1,0,0]\n\t"
      "v_cvt_pk_bf16_f32 v12, v56, v57\n\t"
      "v_cvt_pk_bf16_f32 v13, v58, v59\n\t"
      "v_cvt_pk_bf16_f32 v14, v60, v61\n\t"
      "v_cvt_pk_bf16_f32 v15, v62, v63\n\t"
      "ds_write_b128 v115, v[12:15]\n\t"
      "v_pk_fma_f32 v[64:65], v[64:65], v[92:93], v[88:89] op_sel_hi:[1,0,0]\n\t"
      "v_pk_fma_f32 v[66:67], v[66:67], v[92:93], v[88:89] op_sel_hi:[1,0,0]\n\t"
      "v_mul_f32_e32 v96, v96, v97\n\t"
      "v_pk_fma_f32 v[68:69], v[68:69], v[92:93], v[88:89] op_sel_hi:[1,0,0]\n\t"
      "v_pk_fma_f32 v[70:71], v[70:71], v[92:93], v[88:89] op_sel_hi:[1,0,0]\n\t"
      "s_bitcmp0_b32 s27, 2\n\t"
      "s_cbranch_scc1 SK4_%=\n\t"
      "s_mul_i32 s72, s82, 172032\n\t"
      "s_add_u32 s72, s72, s37\n\t"
      "s_add_u32 s72, s72, 1792\n\t"
      "s_add_u32 s40, s6, s72\n\t"
      "s_addc_u32 s41, s7, 0\n\t"
      "s_add_u32 s82, s82, 1\n\t"
      "s_cmp_eq_u32 s82, 7\n\t"
      "s_cselect_b32 s82, 0, s82\n\t"
      "s_add_u32 m0, s39, 65536\n\t"
      "s_nop 0\n\t"
      "global_load_lds_dwordx4 v148, s[40:41]\n\t"
      "global_load_lds_dwordx4 v152, s[40:41] offset:1024\n\t"
      "SK4_%=:\n\t"
      "v_cvt_pk_bf16_f32 v16, v64, v65\n\t"
      "v_cvt_pk_bf16_f32 v17, v66, v67\n\t"
      "v_cvt_pk_bf16_f32 v18, v68, v69\n\t"
      "v_cvt_pk_bf16_f32 v19, v70, v71\n\t"
      "ds_write_b128 v116, v[16:19]\n\t"
      "v_pk_fma_f32 v[72:73], v[72:73], v[92:93], v[88:89] op_sel_hi:[1,0,0]\n\t"
      "v_pk_fma_f32 v[74:75], v[74:75], v[92:93], v[88:89] op_sel_hi:[1,0,0]\n\t"
      "v_pk_fma_f32 v[76:77], v[76:77], v[92:93], v[88:89] op_sel_hi:[1,0,0]\n\t"
      "v_pk_fma_f32 v[78:79], v[78:79], v[92:93], v[88:89] op_sel_hi:[1,0,0]\n\t"
      "v_cvt_pk_bf16_f32 v20, v72, v73\n\t"
      "v_cvt_pk_bf16_f32 v21, v74, v75\n\t"
      "v_cvt_pk_bf16_f32 v22, v76, v77\n\t"
      "v_cvt_pk_bf16_f32 v23, v78, v79\n\t"
      "ds_write_b128 v117, v[20:23]\n\t"
      "v_pk_fma_f32 v[80:81], v[80:81], v[92:93], v[88:89] op_sel_hi:[1,0,0]\n\t"
      "v_pk_fma_f32 v[82:83], v[82:83], v[92:93], v[88:89] op_sel_hi:[1,0,0]\n\t"
      "v_pk_fma_f32 v[84:85], v[84:85], v[92:93], v[88:89] op_sel_hi:[1,0,0]\n\t"
      "v_pk_fma_f32 v[86:87], v[86:87], v[92:93], v[88:89] op_sel_hi:[1,0,0]\n\t"
      "v_cvt_pk_bf16_f32 v24, v80, v81\n\t"
      "v_cvt_pk_bf16_f32 v25, v82, v83\n\t"
      "v_cvt_pk_bf16_f32 v26, v84, v85\n\t"
      "v_cvt_pk_bf16_f32 v27, v86, v87\n\t"
      "ds_write_b128 v118, v[24:27]\n\t"
      "v_lshlrev_b32_e32 v32, 16, v28\n\t"
      "v_and_b32_e32 v33, 0xffff0000, v28\n\t"
      "v_lshlrev_b32_e32 v34, 16, v29\n\t"
      "v_and_b32_e32 v35, 0xffff0000, v29\n\t"
      "v_lshlrev_b32_e32 v36, 16, v30\n\t"
      "v_and_b32_e32 v37, 0xffff0000, v30\n\t"
      "v_lshlrev_b32_e32 v38, 16, v31\n\t"
      "s_bitcmp0_b32 s27, 2\n\t"
      "s_cbranch_scc1 SK5_%=\n\t"
      "s_mul_i32 s72, s82, 172032\n\t"
      "s_add_u32 s72, s72, s37\n\t"
      "s_add_u32 s72, s72, 1792\n\t"
      "s_add_u32 s40, s6, s72\n\t"
      "s_addc_u32 s41, s7, 0\n\t"
      "s_add_u32 s82, s82, 1\n\t"
      "s_cmp_eq_u32 s82, 7\n\t"
      "s_cselect_b32 s82, 0, s82\n\t"
      "s_add_u32 m0, s39, 67584\n\t"
      "s_nop 0\n\t"
      "global_load_lds_dwordx4 v148, s[40:41]\n\t"
      "global_load_lds_dwordx4 v152, s[40:41] offset:1024\n\t"
      "SK5_%=:\n\t"
      "v_and_b32_e32 v39, 0xffff0000, v31\n\t"
      "v_pk_fma_f32 v[32:33], v[32:33], v[96:97], v[96:97] op_sel:[0,1,0] op_sel_hi:[1,1,0]\n\t"
      "v_pk_fma_f32 v[34:35], v[34:35], v[96:97], v[96:97] op_sel:[0,1,0] op_sel_hi:[1,1,0]\n\t"
      "v_pk_fma_f32 v[36:37], v[36:37], v[96:97], v[96:97] op_sel:[0,1,0] op_sel_hi:[1,1,0]\n\t"
      "v_pk_fma_f32 v[38:39], v[38:39], v[96:97], v[96:97] op_sel:[0,1,0] op_sel_hi:[1,1,0]\n\t"
      "v_cvt_pk_bf16_f32 v28, v32, v33\n\t"
      "v_cvt_pk_bf16_f32 v29, v34, v35\n\t"
      "v_cvt_pk_bf16_f32 v30, v36, v37\n\t"
      "v_cvt_pk_bf16_f32 v31, v38, v39\n\t"
      "ds_write_b128 v119, v[28:31]\n\t"
      "s_waitcnt lgkmcnt(0)\n\t"
      "s_bitcmp1_b32 s27, 2\n\t"
      "s_cbranch_scc1 LPATH_%=\n\t"
      "s_setprio 2\n\t"
      "s_barrier\n\t"
      "ds_read_b64_tr_b16 v[76:77], v168\n\t"
      "ds_read_b64_tr_b16 v[78:79], v168 offset:512\n\t"
      "ds_read_b128 v[68:71], v160\n\t"
      "ds_read_b128 v[72:75], v160 offset:14336\n\t"
      "ds_read_b64_tr_b16 v[80:81], v169\n\t"
      "ds_read_b64_tr_b16 v[82:83], v169 offset:512\n\t"
      "ds_read_b64_tr_b16 v[84:85], v168 offset:1024\n\t"
      "ds_read_b64_tr_b16 v[86:87], v168 offset:1536\n\t"
      "ds_read_b128 v[64:67], v162\n\t"
      "ds_read_b64_tr_b16 v[88:89], v169 offset:1024\n\t"
      "ds_read_b64_tr_b16 v[90:91], v169 offset:1536\n\t"
      "ds_read_b64_tr_b16 v[104:105], v168 offset:2048\n\t"
      "ds_read_b64_tr_b16 v[106:107], v168 offset:2560\n\t"
      "ds_read_b128 v[96:99], v161 offset:1024\n\t"
      "ds_read_b128 v[100:103], v161 offset:15360\n\t"
      "ds_read_b64_tr_b16 v[108:109], v169 offset:2048\n\t"
      "ds_read_b64_tr_b16 v[110:111], v169 offset:2560\n\t"
      "ds_read_b64_tr_b16 v[112:113], v168 offset:3072\n\t"
      "ds_read_b64_tr_b16 v[114:115], v168 offset:3584\n\t"
      "ds_read_b128 v[92:95], v163 offset:1024\n\t"
      "ds_read_b64_tr_b16 v[116:117], v169 offset:3072\n\t"
      "ds_read_b64_tr_b16 v[118:119], v169 offset:3584\n\t"
      "s_waitcnt vmcnt(0)\n\t"
      "s_barrier\n\t"
      "s_waitcnt lgkmcnt(14)\n\t"
      "v_mfma_f32_16x16x32_bf16 v[0:3], v[76:79], v[68:71], 0\n\t"
      "ds_read_b64_tr_b16 v[132:133], v168 offset:16384\n\t"
      "ds_read_b64_tr_b16 v[134:135], v168 offset:16896\n\t"
      "v_mfma_f32_16x16x32_bf16 v[8:11], v[76:79], v[72:75], 0\n\t"
      "ds_read_b128 v[124:127], v160 offset:2048\n\t"
      "v_mfma_f32_16x16x32_bf16 v[4:7], v[80:83], v[68:71], 0\n\t"
      "ds_read_b128 v[128:131], v160 offset:16384\n\t"
      "v_mfma_f32_16x16x32_bf16 v[12:15], v[80:83], v[72:75], 0\n\t"
      "ds_read_b64_tr_b16 v[136:137], v169 offset:16384\n\t"
      "v_mfma_f32_16x16x32_bf16 v[24:27], v[84:87], v[68:71], 0\n\t"
      "ds_read_b64_tr_b16 v[138:139], v169 offset:16896\n\t"
      "v_mfma_f32_16x16x32_bf16 v[32:35], v[84:87], v[72:75], 0\n\t"
      "ds_read_b64_tr_b16 v[140:141], v168 offset:17408\n\t"
      "s_waitcnt lgkmcnt(14)\n\t"
      "v_mfma_f32_16x16x32_bf16 v[16:19], v[84:87], v[64:67], 0\n\t"
      "ds_read_b64_tr_b16 v[142:143], v168 offset:17920\n\t"
      "v_mfma_f32_16x16x32_bf16 v[28:31], v[88:91], v[68:71], 0\n\t"
      "ds_read_b128 v[120:123], v162 offset:2048\n\t"
      "v_mfma_f32_16x16x32_bf16 v[36:39], v[88:91], v[72:75], 0\n\t"
      "ds_read_b64_tr_b16 v[144:145], v169 offset:17408\n\t"
      "v_mfma_f32_16x16x32_bf16 v[20:23], v[88:91], v[64:67], 0\n\t"
      "ds_read_b64_tr_b16 v[146:147], v169 offset:17920\n\t"
      "s_waitcnt lgkmcnt(11)\n\t"
      "v_mfma_f32_16x16x32_bf16 v[0:3], v[104:107], v[96:99], v[0:3]\n\t"
      "ds_read_b64_tr_b16 v[76:77], v168 offset:18432\n\t"
      "ds_read_b64_tr_b16 v[78:79], v168 offset:18944\n\t"
      "v_mfma_f32_16x16x32_bf16 v[8:11], v[104:107], v[100:103], v[8:11]\n\t"
      "ds_read_b128 v[68:71], v161 offset:3072\n\t"
      "v_mfma_f32_16x16x32_bf16 v[4:7], v[108:111], v[96:99], v[4:7]\n\t"
      "ds_read_b128 v[72:75], v161 offset:17408\n\t"
      "v_mfma_f32_16x16x32_bf16 v[12:15], v[108:111], v[100:103], v[12:15]\n\t"
      "ds_read_b64_tr_b16 v[80:81], v169 offset:18432\n\t"
      "v_mfma_f32_16x16x32_bf16 v[24:27], v[112:115], v[96:99], v[24:27]\n\t"
      "ds_read_b64_tr_b16 v[82:83], v169 offset:18944\n\t"
      "v_mfma_f32_16x16x32_bf16 v[32:35], v[112:115], v[100:103], v[32:35]\n\t"
      "ds_read_b64_tr_b16 v[84:85], v168 offset:19456\n\t"
      "v_mfma_f32_16x16x32_bf16 v[16:19], v[112:115], v[92:95], v[16:19]\n\t"
      "ds_read_b64_tr_b16 v[86:87], v168 offset:19968\n\t"
      "v_mfma_f32_16x16x32_bf16 v[28:31], v[116:119], v[96:99], v[28:31]\n\t"
      "ds_read_b128 v[64:67], v163 offset:3072\n\t"
      "v_mfma_f32_16x16x32_bf16 v[36:39], v[116:119], v[100:103], v[36:39]\n\t"
      "ds_read_b64_tr_b16 v[88:89], v169 offset:19456\n\t"
      "v_mfma_f32_16x16x32_bf16 v[20:23], v[116:119], v[92:95], v[20:23]\n\t"
      "ds_read_b64_tr_b16 v[90:91], v169 offset:19968\n\t"
      "s_barrier\n\t"
      "s_waitcnt lgkmcnt(11)\n\t"
      "v_mfma_f32_16x16x32_bf16 v[0:3], v[132:135], v[124:127], v[0:3]\n\t"
      "ds_read_b64_tr_b16 v[104:105], v166\n\t"
      "v_lshlrev_b32_e32 v199, 2, v193\n\t"
      "v_mfma_f32_16x16x32_bf16 v[8:11], v[132:135], v[128:131], v[8:11]\n\t"
      "ds_read_b64_tr_b16 v[106:107], v166 offset:512\n\t"
      "v_sub_u32_e32 v199, v194, v199\n\t"
      "v_mfma_f32_16x16x32_bf16 v[4:7], v[136:139], v[124:127], v[4:7]\n\t"
      "ds_read_b128 v[96:99], v160\n\t"
      "v_mov_b32_e32 v200, 0xff800000\n\t"
      "v_mfma_f32_16x16x32_bf16 v[12:15], v[136:139], v[128:131], v[12:15]\n\t"
      "ds_read_b128 v[100:103], v160 offset:14336\n\t"
      "s_cmp_lt_u32 s25, 2\n\t"
      "v_mfma_f32_16x16x32_bf16 v[24:27], v[140:143], v[124:127], v[24:27]\n\t"
      "ds_read_b128 v[92:95], v162\n\t"
      "s_cselect_b32 s35, 0xff800000, 0\n\t"
      "v_mfma_f32_16x16x32_bf16 v[32:35], v[140:143], v[128:131], v[32:35]\n\t"
      "ds_read_b64_tr_b16 v[108:109], v167\n\t"
      "s_bitcmp0_b32 s24, 0\n\t"
      "v_mfma_f32_16x16x32_bf16 v[16:19], v[140:143], v[120:123], v[16:19]\n\t"
      "ds_read_b64_tr_b16 v[110:111], v167 offset:512\n\t"
      "s_cselect_b32 s35, s35, 0\n\t"
      "v_mfma_f32_16x16x32_bf16 v[28:31], v[144:147], v[124:127], v[28:31]\n\t"
      "v_cmp_ge_i32_e32 vcc, -9, v199\n\t"
      "v_mfma_f32_16x16x32_bf16 v[36:39], v[144:147], v[128:131], v[36:39]\n\t"
      "v_subrev_u32_e32 v201, 0, v199\n\t"
      "v_mfma_f32_16x16x32_bf16 v[20:23], v[144:147], v[120:123], v[20:23]\n\t"
      "v_cmp_gt_u32_e64 s[64:65], 8, v201\n\t"
      "s_waitcnt lgkmcnt(7)\n\t"
      "v_mfma_f32_16x16x32_bf16 v[0:3], v[76:79], v[68:71], v[0:3]\n\t"
      "ds_read_b64_tr_b16 v[132:133], v166 offset:1024\n\t"
      "v_cndmask_b32_e64 v172, v200, 0, vcc\n\t"
      "v_mfma_f32_16x16x32_bf16 v[8:11], v[76:79], v[72:75], v[8:11]\n\t"
      "ds_read_b64_tr_b16 v[134:135], v166 offset:1536\n\t"
      "v_cndmask_b32_e64 v176, v200, 0, s[64:65]\n\t"
      "v_mfma_f32_16x16x32_bf16 v[4:7], v[80:83], v[68:71], v[4:7]\n\t"
      "ds_read_b128 v[124:127], v161 offset:1024\n\t"
      "v_or_b32_e32 v180, s35, v172\n\t"
      "v_mfma_f32_16x16x32_bf16 v[12:15], v[80:83], v[72:75], v[12:15]\n\t"
      "ds_read_b128 v[128:131], v161 offset:15360\n\t"
      "v_cmp_ge_i32_e32 vcc, -8, v199\n\t"
      "v_mfma_f32_16x16x32_bf16 v[24:27], v[84:87], v[68:71], v[24:27]\n\t"
      "ds_read_b128 v[120:123], v163 offset:1024\n\t"
      "v_subrev_u32_e32 v201, 1, v199\n\t"
      "v_mfma_f32_16x16x32_bf16 v[32:35], v[84:87], v[72:75], v[32:35]\n\t"
      "ds_read_b64_tr_b16 v[136:137], v167 offset:1024\n\t"
      "v_cmp_gt_u32_e64 s[64:65], 8, v201\n\t"
      "v_mfma_f32_16x16x32_bf16 v[16:19], v[84:87], v[64:67], v[16:19]\n\t"
      "ds_read_b64_tr_b16 v[138:139], v167 offset:1536\n\t"
      "v_cndmask_b32_e64 v173, v200, 0, vcc\n\t"
      "v_mfma_f32_16x16x32_bf16 v[28:31], v[88:91], v[68:71], v[28:31]\n\t"
      "v_cndmask_b32_e64 v177, v200, 0, s[64:65]\n\t"
      "v_mfma_f32_16x16x32_bf16 v[36:39], v[88:91], v[72:75], v[36:39]\n\t"
      "v_or_b32_e32 v181, s35, v173\n\t"
      "v_mfma_f32_16x16x32_bf16 v[20:23], v[88:91], v[64:67], v[20:23]\n\t"
      "v_cmp_ge_i32_e32 vcc, -7, v199\n\t"
      "s_waitcnt lgkmcnt(7)\n\t"
      "v_mfma_f32_16x16x32_bf16 v[48:51], v[96:99], v[104:107], 0\n\t"
      "ds_read_b64_tr_b16 v[76:77], v166 offset:2048\n\t"
      "ds_read_b64_tr_b16 v[78:79], v166 offset:2560\n\t"
      "v_subrev_u32_e32 v201, 2, v199\n\t"
      "v_mfma_f32_16x16x32_bf16 v[56:59], v[100:103], v[104:107], 0\n\t"
      "ds_read_b128 v[68:71], v160 offset:2048\n\t"
      "v_cmp_gt_u32_e64 s[64:65], 8, v201\n\t"
      "v_mfma_f32_16x16x32_bf16 v[40:43], v[92:95], v[104:107], 0\n\t"
      "ds_read_b128 v[72:75], v160 offset:16384\n\t"
      "v_cndmask_b32_e64 v174, v200, 0, vcc\n\t"
      "v_mfma_f32_16x16x32_bf16 v[52:55], v[96:99], v[108:111], 0\n\t"
      "ds_read_b128 v[64:67], v162 offset:2048\n\t"
      "v_cndmask_b32_e64 v178, v200, 0, s[64:65]\n\t"
      "v_mfma_f32_16x16x32_bf16 v[60:63], v[100:103], v[108:111], 0\n\t"
      "ds_read_b64_tr_b16 v[80:81], v167 offset:2048\n\t"
      "v_or_b32_e32 v182, s35, v174\n\t"
      "v_mfma_f32_16x16x32_bf16 v[44:47], v[92:95], v[108:111], 0\n\t"
      "ds_read_b64_tr_b16 v[82:83], v167 offset:2560\n\t"
      "v_cmp_ge_i32_e32 vcc, -6, v199\n\t"
      "s_waitcnt lgkmcnt(7)\n\t"
      "v_mfma_f32_16x16x32_bf16 v[48:51], v[124:127], v[132:135], v[48:51]\n\t"
      "ds_read_b64_tr_b16 v[104:105], v166 offset:3072\n\t"
      "ds_read_b64_tr_b16 v[106:107], v166 offset:3584\n\t"
      "v_subrev_u32_e32 v201, 3, v199\n\t"
      "v_mfma_f32_16x16x32_bf16 v[56:59], v[128:131], v[132:135], v[56:59]\n\t"
      "ds_read_b128 v[96:99], v161 offset:3072\n\t"
      "v_cmp_gt_u32_e64 s[64:65], 8, v201\n\t"
      "v_mfma_f32_16x16x32_bf16 v[40:43], v[120:123], v[132:135], v[40:43]\n\t"
      "ds_read_b128 v[100:103], v161 offset:17408\n\t"
      "v_cndmask_b32_e64 v175, v200, 0, vcc\n\t"
      "v_mfma_f32_16x16x32_bf16 v[52:55], v[124:127], v[136:139], v[52:55]\n\t"
      "ds_read_b128 v[92:95], v163 offset:3072\n\t"
      "v_cndmask_b32_e64 v179, v200, 0, s[64:65]\n\t"
      "v_mfma_f32_16x16x32_bf16 v[60:63], v[128:131], v[136:139], v[60:63]\n\t"
      "ds_read_b64_tr_b16 v[108:109], v167 offset:3072\n\t"
      "v_or_b32_e32 v183, s35, v175\n\t"
      "v_mfma_f32_16x16x32_bf16 v[44:47], v[120:123], v[136:139], v[44:47]\n\t"
      "ds_read_b64_tr_b16 v[110:111], v167 offset:3584\n\t"
      "v_lshrrev_b32_e32 v201, 31, v172\n\t"
      "s_barrier\n\t"
      "s_waitcnt lgkmcnt(7)\n\t"
      "v_mfma_f32_16x16x32_bf16 v[48:51], v[68:71], v[76:79], v[48:51]\n\t"
      "ds_read_b64_tr_b16 v[132:133], v166 offset:16384\n\t"
      "ds_read_b64_tr_b16 v[134:135], v166 offset:16896\n\t"
      "ds_read_b128 v[124:127], v160 offset:4096\n\t"
      "ds_read_b128 v[128:131], v160 offset:18432\n\t"
      "ds_read_b64_tr_b16 v[136:137], v167 offset:16384\n\t"
      "ds_read_b64_tr_b16 v[138:139], v167 offset:16896\n\t"
      "v_lshrrev_b32_e32 v202, 31, v173\n\t"
      "v_mfma_f32_16x16x32_bf16 v[56:59], v[72:75], v[76:79], v[56:59]\n\t"
      "ds_read_b64_tr_b16 v[140:141], v166 offset:17408\n\t"
      "v_lshl_or_b32 v201, v202, 16, v201\n\t"
      "v_mfma_f32_16x16x32_bf16 v[40:43], v[64:67], v[76:79], v[40:43]\n\t"
      "ds_read_b64_tr_b16 v[142:143], v166 offset:17920\n\t"
      "v_mul_u32_u24_e32 v184, 0xffff, v201\n\t"
      "v_mfma_f32_16x16x32_bf16 v[52:55], v[68:71], v[80:83], v[52:55]\n\t"
      "ds_read_b128 v[120:123], v162 offset:4096\n\t"
      "v_lshrrev_b32_e32 v201, 31, v174\n\t"
      "v_mfma_f32_16x16x32_bf16 v[60:63], v[72:75], v[80:83], v[60:63]\n\t"
      "ds_read_b64_tr_b16 v[144:145], v167 offset:17408\n\t"
      "v_lshrrev_b32_e32 v202, 31, v175\n\t"
      "v_mfma_f32_16x16x32_bf16 v[44:47], v[64:67], v[80:83], v[44:47]\n\t"
      "ds_read_b64_tr_b16 v[146:147], v167 offset:17920\n\t"
      "v_lshl_or_b32 v201, v202, 16, v201\n\t"
      "s_waitcnt lgkmcnt(11)\n\t"
      "v_mfma_f32_16x16x32_bf16 v[48:51], v[96:99], v[104:107], v[48:51]\n\t"
      "ds_read_b64_tr_b16 v[76:77], v166 offset:18432\n\t"
      "ds_read_b64_tr_b16 v[78:79], v166 offset:18944\n\t"
      "ds_read_b128 v[68:71], v161 offset:5120\n\t"
      "ds_read_b128 v[72:75], v161 offset:19456\n\t"
      "ds_read_b64_tr_b16 v[80:81], v167 offset:18432\n\t"
      "ds_read_b64_tr_b16 v[82:83], v167 offset:18944\n\t"
      "v_mul_u32_u24_e32 v185, 0xffff, v201\n\t"
      "v_mfma_f32_16x16x32_bf16 v[56:59], v[100:103], v[104:107], v[56:59]\n\t"
      "ds_read_b64_tr_b16 v[84:85], v166 offset:19456\n\t"
      "s_mov_b32 s68, 0x0\n\t"
      "v_mfma_f32_16x16x32_bf16 v[40:43], v[92:95], v[104:107], v[40:43]\n\t"
      "ds_read_b64_tr_b16 v[86:87], v166 offset:19968\n\t"
      "s_mov_b32 s69, 0xffff0000\n\t"
      "v_mfma_f32_16x16x32_bf16 v[52:55], v[96:99], v[108:111], v[52:55]\n\t"
      "ds_read_b128 v[64:67], v163 offset:5120\n\t"
      "s_mov_b32 s70, 0xffff\n\t"
      "v_mfma_f32_16x16x32_bf16 v[60:63], v[100:103], v[108:111], v[60:63]\n\t"
      "ds_read_b64_tr_b16 v[88:89], v167 offset:19456\n\t"
      "s_mov_b32 s71, 0x0\n\t"
      "v_mfma_f32_16x16x32_bf16 v[44:47], v[92:95], v[108:111], v[44:47]\n\t"
      "ds_read_b64_tr_b16 v[90:91], v167 offset:19968\n\t"
      "v_lshlrev_b32_e32 v231, 3, v192\n\t"
      "s_barrier\n\t"
      "s_waitcnt lgkmcnt(11)\n\t"
      "v_mfma_f32_16x16x32_bf16 v[0:3], v[132:135], v[124:127], v[0:3]\n\t"
      "ds_read_b64_tr_b16 v[104:105], v166 offset:32768\n\t"
      "ds_read_b64_tr_b16 v[106:107], v166 offset:33280\n\t"
      "s_lshl_b32 s34, s25, 12\n\t"
      "v_mfma_f32_16x16x32_bf16 v[8:11], v[132:135], v[128:131], v[8:11]\n\t"
      "ds_read_b128 v[96:99], v160 offset:6144\n\t"
      "s_lshr_b32 s35, s39, 2\n\t"
      "v_mfma_f32_16x16x32_bf16 v[4:7], v[136:139], v[124:127], v[4:7]\n\t"
      "ds_read_b128 v[100:103], v160 offset:20480\n\t"
      "s_add_u32 s34, s34, s35\n\t"
      "v_mfma_f32_16x16x32_bf16 v[12:15], v[136:139], v[128:131], v[12:15]\n\t"
      "ds_read_b64_tr_b16 v[108:109], v167 offset:32768\n\t"
      "s_add_u32 s34, s34, 12582912\n\t"
      "v_mfma_f32_16x16x32_bf16 v[24:27], v[140:143], v[124:127], v[24:27]\n\t"
      "ds_read_b64_tr_b16 v[110:111], v167 offset:33280\n\t"
      "s_add_u32 s46, s20, s34\n\t"
      "v_mfma_f32_16x16x32_bf16 v[32:35], v[140:143], v[128:131], v[32:35]\n\t"
      "ds_read_b64_tr_b16 v[112:113], v166 offset:33792\n\t"
      "s_addc_u32 s47, s21, 0\n\t"
      "v_mfma_f32_16x16x32_bf16 v[16:19], v[140:143], v[120:123], v[16:19]\n\t"
      "ds_read_b64_tr_b16 v[114:115], v166 offset:34304\n\t"
      "s_lshl_b32 s34, s28, 7\n\t"
      "v_mfma_f32_16x16x32_bf16 v[28:31], v[144:147], v[124:127], v[28:31]\n\t"
      "ds_read_b128 v[92:95], v162 offset:6144\n\t"
      "s_add_u32 s58, s46, s34\n\t"
      "v_mfma_f32_16x16x32_bf16 v[36:39], v[144:147], v[128:131], v[36:39]\n\t"
      "ds_read_b64_tr_b16 v[116:117], v167 offset:33792\n\t"
      "s_addc_u32 s59, s47, 0\n\t"
      "v_mfma_f32_16x16x32_bf16 v[20:23], v[144:147], v[120:123], v[20:23]\n\t"
      "ds_read_b64_tr_b16 v[118:119], v167 offset:34304\n\t"
      "s_and_b32 s34, s24, 1\n\t"
      "s_waitcnt lgkmcnt(11)\n\t"
      "v_mfma_f32_16x16x32_bf16 v[0:3], v[76:79], v[68:71], v[0:3]\n\t"
      "ds_read_b64_tr_b16 v[132:133], v166 offset:34816\n\t"
      "ds_read_b64_tr_b16 v[134:135], v166 offset:35328\n\t"
      "s_lshl1_add_u32 s34, s25, s34\n\t"
      "v_mfma_f32_16x16x32_bf16 v[8:11], v[76:79], v[72:75], v[8:11]\n\t"
      "ds_read_b128 v[124:127], v161 offset:7168\n\t"
      "s_lshl_b32 s34, s34, 5\n\t"
      "v_mfma_f32_16x16x32_bf16 v[4:7], v[80:83], v[68:71], v[4:7]\n\t"
      "ds_read_b128 v[128:131], v161 offset:21504\n\t"
      "s_lshl1_add_u32 s34, s26, s34\n\t"
      "v_mfma_f32_16x16x32_bf16 v[12:15], v[80:83], v[72:75], v[12:15]\n\t"
      "ds_read_b64_tr_b16 v[136:137], v167 offset:34816\n\t"
      "s_bfe_u32 s35, s24, 65537\n\t"
      "v_mfma_f32_16x16x32_bf16 v[24:27], v[84:87], v[68:71], v[24:27]\n\t"
      "ds_read_b64_tr_b16 v[138:139], v167 offset:35328\n\t"
      "s_add_u32 s34, s34, s35\n\t"
      "v_mfma_f32_16x16x32_bf16 v[32:35], v[84:87], v[72:75], v[32:35]\n\t"
      "ds_read_b64_tr_b16 v[140:141], v166 offset:35840\n\t"
      "s_lshl_b32 s34, s34, 10\n\t"
      "v_mfma_f32_16x16x32_bf16 v[16:19], v[84:87], v[64:67], v[16:19]\n\t"
      "ds_read_b64_tr_b16 v[142:143], v166 offset:36352\n\t"
      "s_add_u32 s42, s20, s34\n\t"
      "v_mfma_f32_16x16x32_bf16 v[28:31], v[88:91], v[68:71], v[28:31]\n\t"
      "ds_read_b128 v[120:123], v165 offset:7168\n\t"
      "s_addc_u32 s43, s21, 0\n\t"
      "v_mfma_f32_16x16x32_bf16 v[36:39], v[88:91], v[72:75], v[36:39]\n\t"
      "ds_read_b64_tr_b16 v[144:145], v167 offset:35840\n\t"
      "s_add_u32 s44, s42, 16384\n\t"
      "v_mfma_f32_16x16x32_bf16 v[20:23], v[88:91], v[64:67], v[20:23]\n\t"
      "ds_read_b64_tr_b16 v[146:147], v167 offset:36352\n\t"
      "s_addc_u32 s45, s43, 0\n\t"
      "s_barrier\n\t"
      "s_waitcnt lgkmcnt(11)\n\t"
      "v_mfma_f32_16x16x32_bf16 v[0:3], v[104:107], v[96:99], v[0:3]\n\t"
      "ds_read_b64_tr_b16 v[76:77], v166 offset:49152\n\t"
      "ds_read_b64_tr_b16 v[78:79], v166 offset:49664\n\t"
      "v_and_b32_e32 v199, 60, v192\n\t"
      "v_mfma_f32_16x16x32_bf16 v[8:11], v[104:107], v[100:103], v[8:11]\n\t"
      "ds_read_b128 v[68:71], v160 offset:8192\n\t"
      "v_and_b32_e32 v200, 24, v192\n\t"
      "v_mfma_f32_16x16x32_bf16 v[4:7], v[108:111], v[96:99], v[4:7]\n\t"
      "ds_read_b128 v[72:75], v160 offset:22528\n\t"
      "v_lshlrev_b32_e32 v202, 3, v196\n\t"
      "v_mfma_f32_16x16x32_bf16 v[12:15], v[108:111], v[100:103], v[12:15]\n\t"
      "ds_read_b64_tr_b16 v[80:81], v167 offset:49152\n\t"
      "v_lshl_or_b32 v202, v200, 2, v202\n\t"
      "v_mfma_f32_16x16x32_bf16 v[24:27], v[112:115], v[96:99], v[24:27]\n\t"
      "ds_read_b64_tr_b16 v[82:83], v167 offset:49664\n\t"
      "v_lshl_or_b32 v201, v199, 5, v202\n\t"
      "v_mfma_f32_16x16x32_bf16 v[32:35], v[112:115], v[100:103], v[32:35]\n\t"
      "ds_read_b64_tr_b16 v[84:85], v166 offset:50176\n\t"
      "v_add_u32_e32 v216, 106496, v201\n\t"
      "v_mfma_f32_16x16x32_bf16 v[16:19], v[112:115], v[92:95], v[16:19]\n\t"
      "ds_read_b64_tr_b16 v[86:87], v166 offset:50688\n\t"
      "v_mov_b32_e32 v199, 2048\n\t"
      "v_mfma_f32_16x16x32_bf16 v[28:31], v[116:119], v[96:99], v[28:31]\n\t"
      "ds_read_b128 v[64:67], v164 offset:8192\n\t"
      "v_cndmask_b32_e64 v199, v199, 0, s[68:69]\n\t"
      "v_mfma_f32_16x16x32_bf16 v[36:39], v[116:119], v[100:103], v[36:39]\n\t"
      "ds_read_b64_tr_b16 v[88:89], v167 offset:50176\n\t"
      "v_add_u32_e32 v220, v216, v199\n\t"
      "v_mfma_f32_16x16x32_bf16 v[20:23], v[116:119], v[92:95], v[20:23]\n\t"
      "ds_read_b64_tr_b16 v[90:91], v167 offset:50688\n\t"
      "v_xor_b32_e32 v217, 32, v216\n\t"
      "s_waitcnt lgkmcnt(11)\n\t"
      "v_mfma_f32_16x16x32_bf16 v[0:3], v[132:135], v[124:127], v[0:3]\n\t"
      "ds_read_b64_tr_b16 v[104:105], v166 offset:51200\n\t"
      "ds_read_b64_tr_b16 v[106:107], v166 offset:51712\n\t"
      "v_xor_b32_e32 v218, 64, v216\n\t"
      "v_mfma_f32_16x16x32_bf16 v[8:11], v[132:135], v[128:131], v[8:11]\n\t"
      "ds_read_b128 v[96:99], v161 offset:9216\n\t"
      "v_xor_b32_e32 v219, 96, v216\n\t"
      "v_mfma_f32_16x16x32_bf16 v[4:7], v[136:139], v[124:127], v[4:7]\n\t"
      "ds_read_b128 v[100:103], v161 offset:23552\n\t"
      "v_xor_b32_e32 v221, 32, v220\n\t"
      "v_mfma_f32_16x16x32_bf16 v[12:15], v[136:139], v[128:131], v[12:15]\n\t"
      "ds_read_b64_tr_b16 v[108:109], v167 offset:51200\n\t"
      "v_xor_b32_e32 v222, 64, v220\n\t"
      "v_mfma_f32_16x16x32_bf16 v[24:27], v[140:143], v[124:127], v[24:27]\n\t"
      "ds_read_b64_tr_b16 v[110:111], v167 offset:51712\n\t"
      "v_xor_b32_e32 v223, 96, v220\n\t"
      "v_mfma_f32_16x16x32_bf16 v[32:35], v[140:143], v[128:131], v[32:35]\n\t"
      "ds_read_b64_tr_b16 v[112:113], v166 offset:52224\n\t"
      "s_lshr_b32 s34, s39, 1\n\t"
      "v_mfma_f32_16x16x32_bf16 v[16:19], v[140:143], v[120:123], v[16:19]\n\t"
      "ds_read_b64_tr_b16 v[114:115], v166 offset:52736\n\t"
      "s_add_u32 s34, s34, 98304\n\t"
      "v_mfma_f32_16x16x32_bf16 v[28:31], v[144:147], v[124:127], v[28:31]\n\t"
      "ds_read_b128 v[92:95], v165 offset:9216\n\t"
      "v_lshrrev_b32_e32 v199, 1, v193\n\t"
      "v_mfma_f32_16x16x32_bf16 v[36:39], v[144:147], v[128:131], v[36:39]\n\t"
      "ds_read_b64_tr_b16 v[116:117], v167 offset:52224\n\t"
      "v_xor_b32_e32 v199, v199, v198\n\t"
      "v_mfma_f32_16x16x32_bf16 v[20:23], v[144:147], v[120:123], v[20:23]\n\t"
      "ds_read_b64_tr_b16 v[118:119], v167 offset:52736\n\t"
      "v_lshlrev_b32_e32 v199, 4, v199\n\t"
      "s_barrier\n\t"
      "s_waitcnt lgkmcnt(11)\n\t"
      "v_mfma_f32_16x16x32_bf16 v[0:3], v[76:79], v[68:71], v[0:3]\n\t"
      "ds_read_b64_tr_b16 v[132:133], v168\n\t"
      "ds_read_b64_tr_b16 v[134:135], v168 offset:512\n\t"
      "v_lshl_or_b32 v199, v194, 7, v199\n\t"
      "v_mfma_f32_16x16x32_bf16 v[8:11], v[76:79], v[72:75], v[8:11]\n\t"
      "ds_read_b128 v[124:127], v160 offset:10240\n\t"
      "v_and_b32_e32 v200, 1, v193\n\t"
      "v_mfma_f32_16x16x32_bf16 v[4:7], v[80:83], v[68:71], v[4:7]\n\t"
      "ds_read_b128 v[128:131], v160 offset:24576\n\t"
      "v_lshl_or_b32 v199, v200, 3, v199\n\t"
      "v_mfma_f32_16x16x32_bf16 v[12:15], v[80:83], v[72:75], v[12:15]\n\t"
      "ds_read_b64_tr_b16 v[136:137], v169\n\t"
      "v_add_u32_e32 v228, s34, v199\n\t"
      "v_mfma_f32_16x16x32_bf16 v[24:27], v[84:87], v[68:71], v[24:27]\n\t"
      "ds_read_b64_tr_b16 v[138:139], v169 offset:512\n\t"
      "v_mfma_f32_16x16x32_bf16 v[32:35], v[84:87], v[72:75], v[32:35]\n\t"
      "ds_read_b64_tr_b16 v[140:141], v168 offset:1024\n\t"
      "v_mfma_f32_16x16x32_bf16 v[16:19], v[84:87], v[64:67], v[16:19]\n\t"
      "ds_read_b64_tr_b16 v[142:143], v168 offset:1536\n\t"
      "v_mfma_f32_16x16x32_bf16 v[28:31], v[88:91], v[68:71], v[28:31]\n\t"
      "ds_read_b128 v[120:123], v164 offset:10240\n\t"
      "v_mfma_f32_16x16x32_bf16 v[36:39], v[88:91], v[72:75], v[36:39]\n\t"
      "ds_read_b64_tr_b16 v[144:145], v169 offset:1024\n\t"
      "v_mfma_f32_16x16x32_bf16 v[20:23], v[88:91], v[64:67], v[20:23]\n\t"
      "ds_read_b64_tr_b16 v[146:147], v169 offset:1536\n\t"
      "s_waitcnt lgkmcnt(11)\n\t"
      "v_mfma_f32_16x16x32_bf16 v[0:3], v[104:107], v[96:99], v[0:3]\n\t"
      "ds_read_b64_tr_b16 v[76:77], v168 offset:2048\n\t"
      "ds_read_b64_tr_b16 v[78:79], v168 offset:2560\n\t"
      "v_mfma_f32_16x16x32_bf16 v[8:11], v[104:107], v[100:103], v[8:11]\n\t"
      "ds_read_b128 v[68:71], v161 offset:11264\n\t"
      "v_mfma_f32_16x16x32_bf16 v[4:7], v[108:111], v[96:99], v[4:7]\n\t"
      "ds_read_b128 v[72:75], v161 offset:25600\n\t"
      "v_mfma_f32_16x16x32_bf16 v[12:15], v[108:111], v[100:103], v[12:15]\n\t"
      "ds_read_b64_tr_b16 v[80:81], v169 offset:2048\n\t"
      "v_mfma_f32_16x16x32_bf16 v[24:27], v[112:115], v[96:99], v[24:27]\n\t"
      "ds_read_b64_tr_b16 v[82:83], v169 offset:2560\n\t"
      "v_mfma_f32_16x16x32_bf16 v[32:35], v[112:115], v[100:103], v[32:35]\n\t"
      "ds_read_b64_tr_b16 v[84:85], v168 offset:3072\n\t"
      "v_mfma_f32_16x16x32_bf16 v[16:19], v[112:115], v[92:95], v[16:19]\n\t"
      "ds_read_b64_tr_b16 v[86:87], v168 offset:3584\n\t"
      "v_mfma_f32_16x16x32_bf16 v[28:31], v[116:119], v[96:99], v[28:31]\n\t"
      "ds_read_b128 v[64:67], v165 offset:11264\n\t"
      "v_mfma_f32_16x16x32_bf16 v[36:39], v[116:119], v[100:103], v[36:39]\n\t"
      "ds_read_b64_tr_b16 v[88:89], v169 offset:3072\n\t"
      "v_mfma_f32_16x16x32_bf16 v[20:23], v[116:119], v[92:95], v[20:23]\n\t"
      "ds_read_b64_tr_b16 v[90:91], v169 offset:3584\n\t"
      "s_barrier\n\t"
      "s_waitcnt lgkmcnt(11)\n\t"
      "v_mfma_f32_16x16x32_bf16 v[0:3], v[132:135], v[124:127], v[0:3]\n\t"
      "ds_read_b64_tr_b16 v[104:105], v168 offset:16384\n\t"
      "ds_read_b64_tr_b16 v[106:107], v168 offset:16896\n\t"
      "v_mfma_f32_16x16x32_bf16 v[8:11], v[132:135], v[128:131], v[8:11]\n\t"
      "ds_read_b128 v[96:99], v160 offset:12288\n\t"
      "v_mfma_f32_16x16x32_bf16 v[4:7], v[136:139], v[124:127], v[4:7]\n\t"
      "ds_read_b128 v[100:103], v160 offset:26624\n\t"
      "v_mfma_f32_16x16x32_bf16 v[12:15], v[136:139], v[128:131], v[12:15]\n\t"
      "ds_read_b64_tr_b16 v[108:109], v169 offset:16384\n\t"
      "v_mfma_f32_16x16x32_bf16 v[24:27], v[140:143], v[124:127], v[24:27]\n\t"
      "ds_read_b64_tr_b16 v[110:111], v169 offset:16896\n\t"
      "v_mfma_f32_16x16x32_bf16 v[32:35], v[140:143], v[128:131], v[32:35]\n\t"
      "ds_read_b64_tr_b16 v[112:113], v168 offset:17408\n\t"
      "v_mfma_f32_16x16x32_bf16 v[16:19], v[140:143], v[120:123], v[16:19]\n\t"
      "ds_read_b64_tr_b16 v[114:115], v168 offset:17920\n\t"
      "v_mfma_f32_16x16x32_bf16 v[28:31], v[144:147], v[124:127], v[28:31]\n\t"
      "ds_read_b128 v[92:95], v164 offset:12288\n\t"
      "v_mfma_f32_16x16x32_bf16 v[36:39], v[144:147], v[128:131], v[36:39]\n\t"
      "ds_read_b64_tr_b16 v[116:117], v169 offset:17408\n\t"
      "v_mfma_f32_16x16x32_bf16 v[20:23], v[144:147], v[120:123], v[20:23]\n\t"
      "ds_read_b64_tr_b16 v[118:119], v169 offset:17920\n\t"
      "s_waitcnt lgkmcnt(11)\n\t"
      "v_mfma_f32_16x16x32_bf16 v[0:3], v[76:79], v[68:71], v[0:3]\n\t"
      "ds_read_b64_tr_b16 v[132:133], v168 offset:18432\n\t"
      "ds_read_b64_tr_b16 v[134:135], v168 offset:18944\n\t"
      "v_mfma_f32_16x16x32_bf16 v[8:11], v[76:79], v[72:75], v[8:11]\n\t"
      "ds_read_b128 v[124:127], v161 offset:13312\n\t"
      "v_mfma_f32_16x16x32_bf16 v[4:7], v[80:83], v[68:71], v[4:7]\n\t"
      "ds_read_b128 v[128:131], v161 offset:27648\n\t"
      "v_mfma_f32_16x16x32_bf16 v[12:15], v[80:83], v[72:75], v[12:15]\n\t"
      "ds_read_b64_tr_b16 v[136:137], v169 offset:18432\n\t"
      "v_mfma_f32_16x16x32_bf16 v[24:27], v[84:87], v[68:71], v[24:27]\n\t"
      "ds_read_b64_tr_b16 v[138:139], v169 offset:18944\n\t"
      "v_mfma_f32_16x16x32_bf16 v[32:35], v[84:87], v[72:75], v[32:35]\n\t"
      "ds_read_b64_tr_b16 v[140:141], v168 offset:19456\n\t"
      "v_mfma_f32_16x16x32_bf16 v[16:19], v[84:87], v[64:67], v[16:19]\n\t"
      "ds_read_b64_tr_b16 v[142:143], v168 offset:19968\n\t"
      "v_mfma_f32_16x16x32_bf16 v[28:31], v[88:91], v[68:71], v[28:31]\n\t"
      "ds_read_b128 v[120:123], v165 offset:13312\n\t"
      "v_mfma_f32_16x16x32_bf16 v[36:39], v[88:91], v[72:75], v[36:39]\n\t"
      "ds_read_b64_tr_b16 v[144:145], v169 offset:19456\n\t"
      "v_mfma_f32_16x16x32_bf16 v[20:23], v[88:91], v[64:67], v[20:23]\n\t"
      "ds_read_b64_tr_b16 v[146:147], v169 offset:19968\n\t"
      "s_barrier\n\t"
      "s_waitcnt lgkmcnt(11)\n\t"
      "v_mfma_f32_16x16x32_bf16 v[0:3], v[104:107], v[96:99], v[0:3]\n\t"
      "ds_read_b64_tr_b16 v[76:77], v166\n\t"
      "v_mfma_f32_16x16x32_bf16 v[8:11], v[104:107], v[100:103], v[8:11]\n\t"
      "ds_read_b64_tr_b16 v[78:79], v166 offset:512\n\t"
      "v_mfma_f32_16x16x32_bf16 v[4:7], v[108:111], v[96:99], v[4:7]\n\t"
      "ds_read_b128 v[68:71], v160 offset:4096\n\t"
      "v_mfma_f32_16x16x32_bf16 v[12:15], v[108:111], v[100:103], v[12:15]\n\t"
      "ds_read_b128 v[72:75], v160 offset:18432\n\t"
      "v_mfma_f32_16x16x32_bf16 v[24:27], v[112:115], v[96:99], v[24:27]\n\t"
      "ds_read_b128 v[64:67], v162 offset:4096\n\t"
      "v_mfma_f32_16x16x32_bf16 v[32:35], v[112:115], v[100:103], v[32:35]\n\t"
      "ds_read_b64_tr_b16 v[80:81], v167\n\t"
      "v_mfma_f32_16x16x32_bf16 v[16:19], v[112:115], v[92:95], v[16:19]\n\t"
      "ds_read_b64_tr_b16 v[82:83], v167 offset:512\n\t"
      "v_mfma_f32_16x16x32_bf16 v[28:31], v[116:119], v[96:99], v[28:31]\n\t"
      "v_mfma_f32_16x16x32_bf16 v[36:39], v[116:119], v[100:103], v[36:39]\n\t"
      "v_mfma_f32_16x16x32_bf16 v[20:23], v[116:119], v[92:95], v[20:23]\n\t"
      "s_waitcnt lgkmcnt(7)\n\t"
      "v_mfma_f32_16x16x32_bf16 v[0:3], v[132:135], v[124:127], v[0:3]\n\t"
      "ds_read_b64_tr_b16 v[104:105], v166 offset:1024\n\t"
      "v_mfma_f32_16x16x32_bf16 v[8:11], v[132:135], v[128:131], v[8:11]\n\t"
      "ds_read_b64_tr_b16 v[106:107], v166 offset:1536\n\t"
      "v_mfma_f32_16x16x32_bf16 v[4:7], v[136:139], v[124:127], v[4:7]\n\t"
      "ds_read_b128 v[96:99], v161 offset:5120\n\t"
      "v_mfma_f32_16x16x32_bf16 v[12:15], v[136:139], v[128:131], v[12:15]\n\t"
      "ds_read_b128 v[100:103], v161 offset:19456\n\t"
      "v_mfma_f32_16x16x32_bf16 v[24:27], v[140:143], v[124:127], v[24:27]\n\t"
      "ds_read_b128 v[92:95], v163 offset:5120\n\t"
      "v_mfma_f32_16x16x32_bf16 v[32:35], v[140:143], v[128:131], v[32:35]\n\t"
      "ds_read_b64_tr_b16 v[108:109], v167 offset:1024\n\t"
      "v_mfma_f32_16x16x32_bf16 v[16:19], v[140:143], v[120:123], v[16:19]\n\t"
      "ds_read_b64_tr_b16 v[110:111], v167 offset:1536\n\t"
      "v_mfma_f32_16x16x32_bf16 v[28:31], v[144:147], v[124:127], v[28:31]\n\t"
      "v_mfma_f32_16x16x32_bf16 v[36:39], v[144:147], v[128:131], v[36:39]\n\t"
      "v_mfma_f32_16x16x32_bf16 v[20:23], v[144:147], v[120:123], v[20:23]\n\t"
      "s_waitcnt lgkmcnt(7)\n\t"
      "v_mfma_f32_16x16x32_bf16 v[48:51], v[68:71], v[76:79], v[48:51]\n\t"
      "ds_read_b64_tr_b16 v[132:133], v166 offset:2048\n\t"
      "ds_read_b64_tr_b16 v[134:135], v166 offset:2560\n\t"
      "v_mfma_f32_16x16x32_bf16 v[56:59], v[72:75], v[76:79], v[56:59]\n\t"
      "ds_read_b128 v[124:127], v160 offset:6144\n\t"
      "v_mfma_f32_16x16x32_bf16 v[40:43], v[64:67], v[76:79], v[40:43]\n\t"
      "ds_read_b128 v[128:131], v160 offset:20480\n\t"
      "v_cvt_pk_bf16_f32 v84, v0, v1\n\t"
      "v_cvt_pk_bf16_f32 v85, v2, v3\n\t"
      "v_cvt_pk_bf16_f32 v86, v4, v5\n\t"
      "v_cvt_pk_bf16_f32 v87, v6, v7\n\t"
      "v_mfma_f32_16x16x32_bf16 v[52:55], v[68:71], v[80:83], v[52:55]\n\t"
      "ds_read_b128 v[120:123], v162 offset:6144\n\t"
      "v_cvt_pk_bf16_f32 v88, v8, v9\n\t"
      "v_cvt_pk_bf16_f32 v89, v10, v11\n\t"
      "v_cvt_pk_bf16_f32 v90, v12, v13\n\t"
      "v_cvt_pk_bf16_f32 v91, v14, v15\n\t"
      "v_mfma_f32_16x16x32_bf16 v[60:63], v[72:75], v[80:83], v[60:63]\n\t"
      "ds_read_b64_tr_b16 v[136:137], v167 offset:2048\n\t"
      "v_cvt_pk_bf16_f32 v152, v24, v25\n\t"
      "v_cvt_pk_bf16_f32 v153, v26, v27\n\t"
      "v_cvt_pk_bf16_f32 v154, v28, v29\n\t"
      "v_cvt_pk_bf16_f32 v155, v30, v31\n\t"
      "v_mfma_f32_16x16x32_bf16 v[44:47], v[64:67], v[80:83], v[44:47]\n\t"
      "ds_read_b64_tr_b16 v[138:139], v167 offset:2560\n\t"
      "v_cndmask_b32_e64 v86, v86, 0, s[68:69]\n\t"
      "v_cndmask_b32_e64 v87, v87, 0, s[68:69]\n\t"
      "v_cvt_pk_bf16_f32 v156, v32, v33\n\t"
      "v_cvt_pk_bf16_f32 v157, v34, v35\n\t"
      "s_waitcnt lgkmcnt(7)\n\t"
      "v_mfma_f32_16x16x32_bf16 v[48:51], v[96:99], v[104:107], v[48:51]\n\t"
      "ds_read_b64_tr_b16 v[76:77], v166 offset:3072\n\t"
      "ds_read_b64_tr_b16 v[78:79], v166 offset:3584\n\t"
      "v_cvt_pk_bf16_f32 v158, v36, v37\n\t"
      "v_cvt_pk_bf16_f32 v159, v38, v39\n\t"
      "v_cndmask_b32_e64 v90, v90, 0, s[68:69]\n\t"
      "v_cndmask_b32_e64 v91, v91, 0, s[68:69]\n\t"
      "v_mfma_f32_16x16x32_bf16 v[56:59], v[100:103], v[104:107], v[56:59]\n\t"
      "ds_read_b128 v[68:71], v161 offset:7168\n\t"
      "v_cvt_pk_bf16_f32 v148, v16, v17\n\t"
      "v_cvt_pk_bf16_f32 v149, v18, v19\n\t"
      "v_cvt_pk_bf16_f32 v150, v20, v21\n\t"
      "v_cvt_pk_bf16_f32 v151, v22, v23\n\t"
      "v_mfma_f32_16x16x32_bf16 v[40:43], v[92:95], v[104:107], v[40:43]\n\t"
      "ds_read_b128 v[72:75], v161 offset:21504\n\t"
      "v_mov_b32_e32 v34, 0x3e8b97f6\n\t"
      "v_mfma_f32_16x16x32_bf16 v[28:31], v[156:159], v[88:91], v[176:179]\n\t"
      "v_mfma_f32_16x16x32_bf16 v[24:27], v[152:155], v[88:91], v[172:175]\n\t"
      "v_mfma_f32_16x16x32_bf16 v[20:23], v[152:155], v[84:87], v[176:179]\n\t"
      "v_mfma_f32_16x16x32_bf16 v[52:55], v[96:99], v[108:111], v[52:55]\n\t"
      "ds_read_b128 v[64:67], v165 offset:7168\n\t"
      "v_mfma_f32_16x16x32_bf16 v[16:19], v[148:151], v[84:87], v[180:183]\n\t"
      "v_mfma_f32_16x16x32_bf16 v[60:63], v[100:103], v[108:111], v[60:63]\n\t"
      "ds_read_b64_tr_b16 v[80:81], v167 offset:3072\n\t"
      "s_nop 1\n\t"
      "v_max_f32_e32 v24, v24, v28\n\t"
      "v_mfma_f32_16x16x32_bf16 v[44:47], v[92:95], v[108:111], v[44:47]\n\t"
      "ds_read_b64_tr_b16 v[82:83], v167 offset:3584\n\t"
      "v_max_f32_e32 v25, v25, v29\n\t"
      "v_max_f32_e32 v26, v26, v30\n\t"
      "v_max_f32_e32 v27, v27, v31\n\t"
      "v_max_f32_e32 v16, v16, v20\n\t"
      "s_barrier\n\t"
      "s_waitcnt lgkmcnt(7)\n\t"
      "v_mfma_f32_16x16x32_bf16 v[48:51], v[124:127], v[132:135], v[48:51]\n\t"
      "ds_read_b64_tr_b16 v[104:105], v166 offset:16384\n\t"
      "ds_read_b64_tr_b16 v[106:107], v166 offset:16896\n\t"
      "v_max_f32_e32 v17, v17, v21\n\t"
      "v_max_f32_e32 v18, v18, v22\n\t"
      "v_max_f32_e32 v19, v19, v23\n\t"
      "v_max3_f32 v33, v24, v25, v26\n\t"
      "v_mfma_f32_16x16x32_bf16 v[56:59], v[128:131], v[132:135], v[56:59]\n\t"
      "ds_read_b128 v[96:99], v160 offset:8192\n\t"
      "v_max3_f32 v32, v16, v17, v18\n\t"
      "v_max_f32_e32 v33, v33, v27\n\t"
      "v_max_f32_e32 v32, v32, v19\n\t"
      "v_mfma_f32_16x16x32_bf16 v[40:43], v[120:123], v[132:135], v[40:43]\n\t"
      "ds_read_b128 v[100:103], v160 offset:22528\n\t"
      "v_permlane16_swap_b32_e32 v32, v33\n\t"
      "v_max_f32_e32 v32, v32, v33\n\t"
      "v_mov_b32_e32 v33, v32\n\t"
      "v_mfma_f32_16x16x32_bf16 v[52:55], v[124:127], v[136:139], v[52:55]\n\t"
      "ds_read_b128 v[92:95], v164 offset:8192\n\t"
      "v_permlane32_swap_b32_e32 v32, v33\n\t"
      "v_max_f32_e32 v32, v32, v33\n\t"
      "v_mov_b32_e32 v33, v32\n\t"
      "v_mfma_f32_16x16x32_bf16 v[60:63], v[128:131], v[136:139], v[60:63]\n\t"
      "ds_read_b64_tr_b16 v[108:109], v167 offset:16384\n\t"
      "v_permlane16_swap_b32_e32 v32, v33\n\t"
      "v_mul_f32_e64 v33, -v33, v34\n\t"
      "v_mul_f32_e64 v32, -v32, v34\n\t"
      "v_fma_f32 v24, v24, v34, v33\n\t"
      "v_mfma_f32_16x16x32_bf16 v[44:47], v[120:123], v[136:139], v[44:47]\n\t"
      "ds_read_b64_tr_b16 v[110:111], v167 offset:16896\n\t"
      "v_fma_f32 v25, v25, v34, v33\n\t"
      "v_fma_f32 v26, v26, v34, v33\n\t"
      "v_fma_f32 v27, v27, v34, v33\n\t"
      "v_fma_f32 v16, v16, v34, v32\n\t"
      "s_waitcnt lgkmcnt(7)\n\t"
      "v_mfma_f32_16x16x32_bf16 v[48:51], v[68:71], v[76:79], v[48:51]\n\t"
      "ds_read_b64_tr_b16 v[132:133], v166 offset:17408\n\t"
      "ds_read_b64_tr_b16 v[134:135], v166 offset:17920\n\t"
      "v_fma_f32 v17, v17, v34, v32\n\t"
      "v_fma_f32 v18, v18, v34, v32\n\t"
      "v_fma_f32 v19, v19, v34, v32\n\t"
      "v_exp_f32_e32 v24, v24\n\t"
      "v_mfma_f32_16x16x32_bf16 v[56:59], v[72:75], v[76:79], v[56:59]\n\t"
      "ds_read_b128 v[124:127], v161 offset:9216\n\t"
      "v_exp_f32_e32 v25, v25\n\t"
      "v_exp_f32_e32 v26, v26\n\t"
      "v_mfma_f32_16x16x32_bf16 v[40:43], v[64:67], v[76:79], v[40:43]\n\t"
      "ds_read_b128 v[128:131], v161 offset:23552\n\t"
      "v_exp_f32_e32 v27, v27\n\t"
      "v_exp_f32_e32 v16, v16\n\t"
      "v_mfma_f32_16x16x32_bf16 v[52:55], v[68:71], v[80:83], v[52:55]\n\t"
      "ds_read_b128 v[120:123], v165 offset:9216\n\t"
      "v_exp_f32_e32 v17, v17\n\t"
      "v_exp_f32_e32 v18, v18\n\t"
      "v_mfma_f32_16x16x32_bf16 v[60:63], v[72:75], v[80:83], v[60:63]\n\t"
      "ds_read_b64_tr_b16 v[136:137], v167 offset:17408\n\t"
      "v_exp_f32_e32 v19, v19\n\t"
      "v_add_f32_e32 v33, v24, v25\n\t"
      "v_mfma_f32_16x16x32_bf16 v[44:47], v[64:67], v[80:83], v[44:47]\n\t"
      "ds_read_b64_tr_b16 v[138:139], v167 offset:17920\n\t"
      "v_add_f32_e32 v28, v26, v27\n\t"
      "v_add_f32_e32 v32, v16, v17\n\t"
      "v_add_f32_e32 v20, v18, v19\n\t"
      "v_add_f32_e32 v33, v33, v28\n\t"
      "s_waitcnt lgkmcnt(7)\n\t"
      "v_mfma_f32_16x16x32_bf16 v[48:51], v[96:99], v[104:107], v[48:51]\n\t"
      "ds_read_b64_tr_b16 v[76:77], v166 offset:18432\n\t"
      "ds_read_b64_tr_b16 v[78:79], v166 offset:18944\n\t"
      "v_add_f32_e32 v32, v32, v20\n\t"
      "s_nop 1\n\t"
      "v_permlane16_swap_b32_e32 v32, v33\n\t"
      "v_add_f32_e32 v32, v32, v33\n\t"
      "v_mfma_f32_16x16x32_bf16 v[56:59], v[100:103], v[104:107], v[56:59]\n\t"
      "ds_read_b128 v[68:71], v160 offset:10240\n\t"
      "v_mov_b32_e32 v33, v32\n\t"
      "s_nop 1\n\t"
      "v_permlane32_swap_b32_e32 v32, v33\n\t"
      "v_add_f32_e32 v32, v32, v33\n\t"
      "v_mfma_f32_16x16x32_bf16 v[40:43], v[92:95], v[104:107], v[40:43]\n\t"
      "ds_read_b128 v[72:75], v160 offset:24576\n\t"
      "v_mov_b32_e32 v33, v32\n\t"
      "s_nop 1\n\t"
      "v_permlane16_swap_b32_e32 v32, v33\n\t"
      "v_rcp_f32_e32 v33, v33\n\t"
      "v_mfma_f32_16x16x32_bf16 v[52:55], v[96:99], v[108:111], v[52:55]\n\t"
      "ds_read_b128 v[64:67], v164 offset:10240\n\t"
      "v_rcp_f32_e32 v32, v32\n\t"
      "v_mul_f32_e32 v24, v24, v33\n\t"
      "v_mfma_f32_16x16x32_bf16 v[60:63], v[100:103], v[108:111], v[60:63]\n\t"
      "ds_read_b64_tr_b16 v[80:81], v167 offset:18432\n\t"
      "v_mul_f32_e32 v25, v25, v33\n\t"
      "v_mul_f32_e32 v26, v26, v33\n\t"
      "v_mul_f32_e32 v27, v27, v33\n\t"
      "v_mul_f32_e32 v16, v16, v32\n\t"
      "v_mfma_f32_16x16x32_bf16 v[44:47], v[92:95], v[108:111], v[44:47]\n\t"
      "ds_read_b64_tr_b16 v[82:83], v167 offset:18944\n\t"
      "v_mul_f32_e32 v17, v17, v32\n\t"
      "v_mul_f32_e32 v18, v18, v32\n\t"
      "v_mul_f32_e32 v19, v19, v32\n\t"
      "v_cvt_pk_bf16_f32 v4, v24, v25\n\t"
      "s_waitcnt lgkmcnt(7)\n\t"
      "v_mfma_f32_16x16x32_bf16 v[48:51], v[124:127], v[132:135], v[48:51]\n\t"
      "ds_read_b64_tr_b16 v[104:105], v166 offset:19456\n\t"
      "ds_read_b64_tr_b16 v[106:107], v166 offset:19968\n\t"
      "v_cvt_pk_bf16_f32 v5, v26, v27\n\t"
      "v_cvt_pk_bf16_f32 v0, v16, v17\n\t"
      "v_cvt_pk_bf16_f32 v1, v18, v19\n\t"
      "v_and_b32_e32 v6, v184, v4\n\t"
      "v_mfma_f32_16x16x32_bf16 v[56:59], v[128:131], v[132:135], v[56:59]\n\t"
      "ds_read_b128 v[96:99], v161 offset:11264\n\t"
      "v_and_b32_e32 v7, v185, v5\n\t"
      "v_and_b32_e32 v2, v184, v0\n\t"
      "v_and_b32_e32 v3, v185, v1\n\t"
      "v_xor_b32_e32 v4, v4, v6\n\t"
      "v_mfma_f32_16x16x32_bf16 v[40:43], v[120:123], v[132:135], v[40:43]\n\t"
      "ds_read_b128 v[100:103], v161 offset:25600\n\t"
      "v_xor_b32_e32 v5, v5, v7\n\t"
      "v_xor_b32_e32 v0, v0, v2\n\t"
      "v_xor_b32_e32 v1, v1, v3\n\t"
      "v_mfma_f32_16x16x32_bf16 v[52:55], v[124:127], v[136:139], v[52:55]\n\t"
      "ds_read_b128 v[92:95], v165 offset:11264\n\t"
      "v_mfma_f32_16x16x32_bf16 v[60:63], v[128:131], v[136:139], v[60:63]\n\t"
      "ds_read_b64_tr_b16 v[108:109], v167 offset:19456\n\t"
      "v_mfma_f32_16x16x32_bf16 v[44:47], v[120:123], v[136:139], v[44:47]\n\t"
      "ds_read_b64_tr_b16 v[110:111], v167 offset:19968\n\t"
      "s_barrier\n\t"
      "s_waitcnt lgkmcnt(7)\n\t"
      "v_mfma_f32_16x16x32_bf16 v[48:51], v[68:71], v[76:79], v[48:51]\n\t"
      "ds_read_b64_tr_b16 v[132:133], v170 offset:32768\n\t"
      "ds_read_b64_tr_b16 v[134:135], v170 offset:33280\n\t"
      "v_mfma_f32_16x16x32_bf16 v[56:59], v[72:75], v[76:79], v[56:59]\n\t"
      "ds_read_b128 v[124:127], v160 offset:12288\n\t"
      "v_mfma_f32_16x16x32_bf16 v[40:43], v[64:67], v[76:79], v[40:43]\n\t"
      "ds_read_b128 v[128:131], v160 offset:26624\n\t"
      "v_mfma_f32_16x16x32_bf16 v[52:55], v[68:71], v[80:83], v[52:55]\n\t"
      "ds_read_b128 v[120:123], v164 offset:12288\n\t"
      "v_mfma_f32_16x16x32_bf16 v[60:63], v[72:75], v[80:83], v[60:63]\n\t"
      "ds_read_b64_tr_b16 v[136:137], v171 offset:32768\n\t"
      "v_mfma_f32_16x16x32_bf16 v[44:47], v[64:67], v[80:83], v[44:47]\n\t"
      "ds_read_b64_tr_b16 v[138:139], v171 offset:33280\n\t"
      "s_waitcnt lgkmcnt(7)\n\t"
      "v_mfma_f32_16x16x32_bf16 v[48:51], v[96:99], v[104:107], v[48:51]\n\t"
      "ds_read_b64_tr_b16 v[76:77], v170 offset:33792\n\t"
      "ds_read_b64_tr_b16 v[78:79], v170 offset:34304\n\t"
      "v_mfma_f32_16x16x32_bf16 v[56:59], v[100:103], v[104:107], v[56:59]\n\t"
      "ds_read_b128 v[68:71], v161 offset:13312\n\t"
      "v_mfma_f32_16x16x32_bf16 v[40:43], v[92:95], v[104:107], v[40:43]\n\t"
      "ds_read_b128 v[72:75], v161 offset:27648\n\t"
      "v_mfma_f32_16x16x32_bf16 v[52:55], v[96:99], v[108:111], v[52:55]\n\t"
      "ds_read_b128 v[64:67], v165 offset:13312\n\t"
      "v_mfma_f32_16x16x32_bf16 v[60:63], v[100:103], v[108:111], v[60:63]\n\t"
      "ds_read_b64_tr_b16 v[80:81], v171 offset:33792\n\t"
      "v_mfma_f32_16x16x32_bf16 v[44:47], v[92:95], v[108:111], v[44:47]\n\t"
      "ds_read_b64_tr_b16 v[82:83], v171 offset:34304\n\t"
      "s_waitcnt lgkmcnt(7)\n\t"
      "v_mfma_f32_16x16x32_bf16 v[48:51], v[124:127], v[132:135], v[48:51]\n\t"
      "v_mfma_f32_16x16x32_bf16 v[56:59], v[128:131], v[132:135], v[56:59]\n\t"
      "v_mfma_f32_16x16x32_bf16 v[40:43], v[120:123], v[132:135], v[40:43]\n\t"
      "v_mfma_f32_16x16x32_bf16 v[52:55], v[124:127], v[136:139], v[52:55]\n\t"
      "v_mfma_f32_16x16x32_bf16 v[60:63], v[128:131], v[136:139], v[60:63]\n\t"
      "v_mfma_f32_16x16x32_bf16 v[44:47], v[120:123], v[136:139], v[44:47]\n\t"
      "s_waitcnt lgkmcnt(0)\n\t"
      "v_mfma_f32_16x16x32_bf16 v[48:51], v[68:71], v[76:79], v[48:51]\n\t"
      "v_mfma_f32_16x16x32_bf16 v[56:59], v[72:75], v[76:79], v[56:59]\n\t"
      "v_mfma_f32_16x16x32_bf16 v[40:43], v[64:67], v[76:79], v[40:43]\n\t"
      "v_mfma_f32_16x16x32_bf16 v[52:55], v[68:71], v[80:83], v[52:55]\n\t"
      "v_mfma_f32_16x16x32_bf16 v[60:63], v[72:75], v[80:83], v[60:63]\n\t"
      "v_mfma_f32_16x16x32_bf16 v[44:47], v[64:67], v[80:83], v[44:47]\n\t"
      "s_nop 2\n\t"
      "v_cvt_pk_bf16_f32 v66, v48, v49\n\t"
      "v_cvt_pk_bf16_f32 v67, v50, v51\n\t"
      "v_cvt_pk_bf16_f32 v68, v56, v57\n\t"
      "v_cvt_pk_bf16_f32 v69, v58, v59\n\t"
      "v_cvt_pk_bf16_f32 v64, v40, v41\n\t"
      "v_cvt_pk_bf16_f32 v65, v42, v43\n\t"
      "v_mfma_f32_16x16x32_bf16 v[88:91], v[66:69], v[4:7], 0\n\t"
      "s_nop 0\n\t"
      "v_mfma_f32_16x16x32_bf16 v[80:83], v[64:67], v[0:3], 0\n\t"
      "v_cvt_pk_bf16_f32 v72, v52, v53\n\t"
      "v_cvt_pk_bf16_f32 v73, v54, v55\n\t"
      "v_cvt_pk_bf16_f32 v74, v60, v61\n\t"
      "v_cvt_pk_bf16_f32 v75, v62, v63\n\t"
      "v_cvt_pk_bf16_f32 v70, v44, v45\n\t"
      "v_cvt_pk_bf16_f32 v71, v46, v47\n\t"
      "v_mfma_f32_16x16x32_bf16 v[92:95], v[72:75], v[4:7], 0\n\t"
      "s_nop 0\n\t"
      "v_mfma_f32_16x16x32_bf16 v[84:87], v[70:73], v[0:3], 0\n\t"
      "v_cvt_pk_bf16_f32 v152, v88, v89\n\t"
      "v_cvt_pk_bf16_f32 v153, v90, v91\n\t"
      "v_min_u32_e32 v152, -2, v152\n\t"
      "v_min_u32_e32 v153, -2, v153\n\t"
      "v_cvt_pk_bf16_f32 v148, v80, v81\n\t"
      "v_cvt_pk_bf16_f32 v149, v82, v83\n\t"
      "v_min_u32_e32 v148, -2, v148\n\t"
      "v_min_u32_e32 v149, -2, v149\n\t"
      "v_cvt_pk_bf16_f32 v154, v92, v93\n\t"
      "v_cvt_pk_bf16_f32 v155, v94, v95\n\t"
      "v_cndmask_b32_e64 v154, v154, 0, s[68:69]\n\t"
      "v_min_u32_e32 v154, -2, v154\n\t"
      "v_cndmask_b32_e64 v155, v155, 0, s[68:69]\n\t"
      "v_min_u32_e32 v155, -2, v155\n\t"
      "global_store_dwordx4 v244, v[152:155], s[44:45] sc1\n\t"
      "v_cvt_pk_bf16_f32 v150, v84, v85\n\t"
      "v_cvt_pk_bf16_f32 v151, v86, v87\n\t"
      "v_cndmask_b32_e64 v150, v150, 0, s[68:69]\n\t"
      "v_min_u32_e32 v150, -2, v150\n\t"
      "v_cndmask_b32_e64 v151, v151, 0, s[68:69]\n\t"
      "v_min_u32_e32 v151, -2, v151\n\t"
      "global_store_dwordx4 v244, v[148:151], s[42:43] sc1\n\t"
      "s_barrier\n\t"
      "s_cmp_eq_u32 s26, 7\n\t"
      "s_cbranch_scc1 SPARE_%=\n\t"
      "s_lshl_b32 s34, s25, 16\n\t"
      "s_lshl2_add_u32 s34, s39, s34\n\t"
      "s_add_u32 s34, s34, 4096\n\t"
      "s_add_u32 s42, s20, s34\n\t"
      "s_addc_u32 s43, s21, 0\n\t"
      "s_add_u32 s44, s42, 8192\n\t"
      "s_addc_u32 s45, s43, 0\n\t"
      "s_lshl_b32 s72, s24, 10\n\t"
      "s_add_u32 s72, s72, 98304\n\t"
      "v_xor_b32_e32 v203, v197, v198\n\t"
      "v_lshl_add_u32 v203, v203, 4, s72\n\t"
      "v_lshl_add_u32 v203, v197, 7, v203\n\t"
      "ds_write_b128 v203, v[204:207]\n\t"
      "s_and_b32 s35, s25, 3\n\t"
      "s_lshl2_add_u32 s31, s26, s35\n\t"
      "s_and_b32 s35, s2, 7\n\t"
      "s_mul_i32 s34, s35, 14\n\t"
      "s_lshl4_add_u32 s35, s35, -14\n\t"
      "s_cmp_lt_u32 s31, 14\n\t"
      "s_cselect_b32 s34, s34, s35\n\t"
      "s_cselect_b32 s36, 1, 0\n\t"
      "s_or_b32 s27, s27, s36\n\t"
      "s_add_u32 s34, s34, s31\n\t"
      "s_lshr_b32 s29, s34, 2\n\t"
      "s_and_b32 s32, s34, 3\n\t"
      "s_mul_i32 s30, s32, 7\n\t"
      "v_and_b32_e32 v199, 60, v192\n\t"
      "v_and_b32_e32 v200, 24, v192\n\t"
      "v_lshlrev_b32_e32 v202, 3, v196\n\t"
      "v_lshl_or_b32 v202, v200, 2, v202\n\t"
      "v_lshl_or_b32 v202, v195, 7, v202\n\t"
      "v_lshl_or_b32 v202, v193, 10, v202\n\t"
      "v_add_u32_e32 v224, s39, v202\n\t"
      "v_xor_b32_e32 v225, 32, v224\n\t"
      "v_xor_b32_e32 v226, 64, v224\n\t"
      "v_xor_b32_e32 v227, 96, v224\n\t"
      "s_lshl_b32 s34, s24, 12\n\t"
      "s_add_u32 s34, s34, 98304\n\t"
      "v_add_u32_e32 v245, s34, v244\n\t"
      "v_lshlrev_b32_e32 v199, 2, v199\n\t"
      "s_lshl_b32 s34, s24, 8\n\t"
      "v_lshl_add_u32 v247, v192, 2, s34\n\t"
      "v_and_b32_e32 v200, 2, v192\n\t"
      "v_mul_u32_u24_e32 v202, 576, v200\n\t"
      "v_xor_b32_e32 v199, v202, v199\n\t"
      "v_and_b32_e32 v202, 1, v192\n\t"
      "v_lshl_or_b32 v199, v202, 3, v199\n\t"
      "s_and_b32 s35, s34, 0x400\n\t"
      "s_add_u32 s34, s34, s35\n\t"
      "s_add_u32 s34, s34, 98304\n\t"
      "v_add_u32_e32 v246, s34, v199\n\t"
      "v_lshlrev_b32_e32 v199, 2, v202\n\t"
      "v_lshl_or_b32 v199, v200, 4, v199\n\t"
      "s_min_u32 s34, s29, 27\n\t"
      "s_lshl_b32 s34, s34, 7\n\t"
      "s_lshr_b32 s35, s24, 2\n\t"
      "s_lshl_b32 s35, s35, 6\n\t"
      "s_add_u32 s34, s34, s35\n\t"
      "s_lshr_b32 s35, s39, 9\n\t"
      "s_add_u32 s34, s34, s35\n\t"
      "v_add_u32_e32 v248, s34, v199\n\t"
      "s_add_u32 s48, s20, 8388608\n\t"
      "s_addc_u32 s49, s21, 0\n\t"
      "v_and_b32_e32 v199, 16, v192\n\t"
      "v_lshlrev_b32_e32 v199, 1, v199\n\t"
      "v_and_b32_e32 v200, 32, v192\n\t"
      "s_lshl_b32 s34, s28, 18\n\t"
      "s_lshl_b32 s35, s25, 11\n\t"
      "s_add_u32 s34, s34, s35\n\t"
      "s_lshr_b32 s35, s39, 3\n\t"
      "s_add_u32 s34, s34, s35\n\t"
      "v_and_b32_e32 v201, 0x1f0, v244\n\t"
      "v_lshl_or_b32 v201, v200, 11, v201\n\t"
      "s_waitcnt lgkmcnt(0)\n\t"
      "s_barrier\n\t"
      "v_xor_b32_e32 v210, 32, v228\n\t"
      "v_xor_b32_e32 v212, 64, v228\n\t"
      "v_xor_b32_e32 v214, 96, v228\n\t"
      "ds_read_b64 v[208:209], v228\n\t"
      "ds_read_b64 v[210:211], v210\n\t"
      "ds_read_b64 v[212:213], v212\n\t"
      "ds_read_b64 v[214:215], v214\n\t"
      "ds_read_b64_tr_b16 v[0:1], v216 offset:7168\n\t"
      "ds_read_b64_tr_b16 v[2:3], v220 offset:7168\n\t"
      "ds_read_b64_tr_b16 v[4:5], v217 offset:7168\n\t"
      "ds_read_b64_tr_b16 v[6:7], v221 offset:7168\n\t"
      "ds_read_b64_tr_b16 v[8:9], v218 offset:7168\n\t"
      "ds_read_b64_tr_b16 v[10:11], v222 offset:7168\n\t"
      "ds_read_b64_tr_b16 v[12:13], v219 offset:7168\n\t"
      "ds_read_b64_tr_b16 v[14:15], v223 offset:7168\n\t"
      "ds_read_b64_tr_b16 v[16:17], v218 offset:10752\n\t"
      "ds_read_b64_tr_b16 v[18:19], v222 offset:10752\n\t"
      "ds_read_b64_tr_b16 v[20:21], v219 offset:10752\n\t"
      "ds_read_b64_tr_b16 v[22:23], v223 offset:10752\n\t"
      "ds_read_b64_tr_b16 v[24:25], v216 offset:10752\n\t"
      "ds_read_b64_tr_b16 v[26:27], v220 offset:10752\n\t"
      "ds_read_b64_tr_b16 v[28:29], v217 offset:10752\n\t"
      "ds_read_b64_tr_b16 v[30:31], v221 offset:10752\n\t"
      "ds_read_b64_tr_b16 v[32:33], v216 offset:14336\n\t"
      "ds_read_b64_tr_b16 v[34:35], v220 offset:14336\n\t"
      "ds_read_b64_tr_b16 v[36:37], v217 offset:14336\n\t"
      "ds_read_b64_tr_b16 v[38:39], v221 offset:14336\n\t"
      "ds_read_b64_tr_b16 v[40:41], v218 offset:14336\n\t"
      "ds_read_b64_tr_b16 v[42:43], v222 offset:14336\n\t"
      "ds_read_b64_tr_b16 v[44:45], v219 offset:14336\n\t"
      "ds_read_b64_tr_b16 v[46:47], v223 offset:14336\n\t"
      "ds_read_b64_tr_b16 v[48:49], v218 offset:17920\n\t"
      "ds_read_b64_tr_b16 v[50:51], v222 offset:17920\n\t"
      "ds_read_b64_tr_b16 v[52:53], v219 offset:17920\n\t"
      "ds_read_b64_tr_b16 v[54:55], v223 offset:17920\n\t"
      "ds_read_b64_tr_b16 v[56:57], v216 offset:17920\n\t"
      "ds_read_b64_tr_b16 v[58:59], v220 offset:17920\n\t"
      "ds_read_b64_tr_b16 v[60:61], v217 offset:17920\n\t"
      "ds_read_b64_tr_b16 v[62:63], v221 offset:17920\n\t"
      "ds_read_b64_tr_b16 v[64:65], v216 offset:21504\n\t"
      "ds_read_b64_tr_b16 v[66:67], v220 offset:21504\n\t"
      "ds_read_b64_tr_b16 v[68:69], v217 offset:21504\n\t"
      "ds_read_b64_tr_b16 v[70:71], v221 offset:21504\n\t"
      "ds_read_b64_tr_b16 v[72:73], v218 offset:21504\n\t"
      "ds_read_b64_tr_b16 v[74:75], v222 offset:21504\n\t"
      "ds_read_b64_tr_b16 v[76:77], v219 offset:21504\n\t"
      "ds_read_b64_tr_b16 v[78:79], v223 offset:21504\n\t"
      "ds_read_b64_tr_b16 v[80:81], v218 offset:25088\n\t"
      "ds_read_b64_tr_b16 v[82:83], v222 offset:25088\n\t"
      "ds_read_b64_tr_b16 v[84:85], v219 offset:25088\n\t"
      "ds_read_b64_tr_b16 v[86:87], v223 offset:25088\n\t"
      "ds_read_b64_tr_b16 v[88:89], v216 offset:25088\n\t"
      "ds_read_b64_tr_b16 v[90:91], v220 offset:25088\n\t"
      "ds_read_b64_tr_b16 v[92:93], v217 offset:25088\n\t"
      "ds_read_b64_tr_b16 v[94:95], v221 offset:25088\n\t"
      "ds_read_b64_tr_b16 v[96:97], v216 offset:28672\n\t"
      "ds_read_b64_tr_b16 v[98:99], v220 offset:28672\n\t"
      "ds_read_b64_tr_b16 v[100:101], v217 offset:28672\n\t"
      "ds_read_b64_tr_b16 v[102:103], v221 offset:28672\n\t"
      "ds_read_b64_tr_b16 v[104:105], v218 offset:28672\n\t"
      "ds_read_b64_tr_b16 v[106:107], v222 offset:28672\n\t"
      "ds_read_b64_tr_b16 v[108:109], v219 offset:28672\n\t"
      "ds_read_b64_tr_b16 v[110:111], v223 offset:28672\n\t"
      "ds_read_b64_tr_b16 v[112:113], v218 offset:32256\n\t"
      "ds_read_b64_tr_b16 v[114:115], v222 offset:32256\n\t"
      "ds_read_b64_tr_b16 v[116:117], v219 offset:32256\n\t"
      "ds_read_b64_tr_b16 v[118:119], v223 offset:32256\n\t"
      "ds_read_b64_tr_b16 v[120:121], v216 offset:32256\n\t"
      "ds_read_b64_tr_b16 v[122:123], v220 offset:32256\n\t"
      "ds_read_b64_tr_b16 v[124:125], v217 offset:32256\n\t"
      "ds_read_b64_tr_b16 v[126:127], v221 offset:32256\n\t"
      "s_sleep 2\n\t"
      "global_load_dwordx4 v[128:131], v244, s[42:43] offset:-2048 sc1\n\t"
      "global_load_dwordx4 v[132:135], v244, s[42:43] offset:-1024 sc1\n\t"
      "global_load_dwordx4 v[136:139], v244, s[42:43] sc1\n\t"
      "global_load_dwordx4 v[140:143], v244, s[42:43] offset:1024 sc1\n\t"
      "global_load_dwordx4 v[144:147], v244, s[42:43] offset:2048 sc1\n\t"
      "global_load_dwordx4 v[148:151], v244, s[42:43] offset:3072 sc1\n\t"
      "global_load_dwordx4 v[152:155], v244, s[44:45] offset:-4096 sc1\n\t"
      "global_load_dwordx4 v[156:159], v244, s[44:45] offset:-3072 sc1\n\t"
      "global_load_dwordx4 v[160:163], v244, s[44:45] offset:-2048 sc1\n\t"
      "global_load_dwordx4 v[164:167], v244, s[44:45] offset:-1024 sc1\n\t"
      "v_add_u32_e32 v229, s34, v201\n\t"
      "v_add_u32_e32 v230, 131072, v229\n\t"
      "s_and_b32 s34, s25, 30\n\t"
      "s_lshr_b32 s35, s39, 13\n\t"
      "s_add_u32 s34, s34, s35\n\t"
      "s_mul_i32 s34, s34, 7\n\t"
      "s_add_u32 s34, s34, s28\n\t"
      "s_lshl_b32 s34, s34, 3\n\t"
      "s_and_b32 s35, s25, 1\n\t"
      "s_add_u32 s34, s34, s35\n\t"
      "s_lshl_b32 s34, s34, 10\n\t"
      "s_and_b32 s35, s24, 1\n\t"
      "s_lshl_b32 s35, s35, 9\n\t"
      "s_add_u32 s34, s34, s35\n\t"
      "s_add_u32 s34, s34, 6293504\n\t"
      "v_lshrrev_b32_e32 v201, 1, v200\n\t"
      "v_lshl_or_b32 v201, v199, 6, v201\n\t"
      "v_lshl_or_b32 v201, v194, 5, v201\n\t"
      "v_add_u32_e32 v241, s34, v201\n\t"
      "s_lshl_b32 s34, s24, 12\n\t"
      "s_add_u32 s34, s34, 131072\n\t"
      "v_xor_b32_e32 v201, v193, v194\n\t"
      "v_lshlrev_b32_e32 v201, 3, v201\n\t"
      "v_lshl_or_b32 v201, v194, 7, v201\n\t"
      "v_add_u32_e32 v233, s34, v201\n\t"
      "v_lshrrev_b32_e32 v199, 2, v199\n\t"
      "v_or_b32_e32 v199, v199, v195\n\t"
      "v_lshrrev_b32_e32 v200, 3, v200\n\t"
      "v_or_b32_e32 v200, v196, v200\n\t"
      "v_xor_b32_e32 v200, v200, v199\n\t"
      "v_lshlrev_b32_e32 v200, 3, v200\n\t"
      "v_lshl_or_b32 v200, v199, 7, v200\n\t"
      "v_add_u32_e32 v237, s34, v200\n\t"
      "v_xor_b32_e32 v234, 32, v233\n\t"
      "v_xor_b32_e32 v238, 32, v237\n\t"
      "v_xor_b32_e32 v235, 64, v233\n\t"
      "v_xor_b32_e32 v239, 64, v237\n\t"
      "v_xor_b32_e32 v236, 96, v233\n\t"
      "v_xor_b32_e32 v240, 96, v237\n\t"
      "s_waitcnt lgkmcnt(14)\n\t"
      "v_lshlrev_b32_e32 v192, 16, v208\n\t"
      "v_and_b32_e32 v193, 0xffff0000, v208\n\t"
      "v_lshlrev_b32_e32 v194, 16, v209\n\t"
      "v_and_b32_e32 v195, 0xffff0000, v209\n\t"
      "v_lshlrev_b32_e32 v196, 16, v210\n\t"
      "v_and_b32_e32 v197, 0xffff0000, v210\n\t"
      "v_lshlrev_b32_e32 v198, 16, v211\n\t"
      "v_and_b32_e32 v199, 0xffff0000, v211\n\t"
      "v_lshlrev_b32_e32 v200, 16, v212\n\t"
      "v_and_b32_e32 v201, 0xffff0000, v212\n\t"
      "v_lshlrev_b32_e32 v202, 16, v213\n\t"
      "v_and_b32_e32 v203, 0xffff0000, v213\n\t"
      "v_lshlrev_b32_e32 v204, 16, v214\n\t"
      "v_and_b32_e32 v205, 0xffff0000, v214\n\t"
      "v_lshlrev_b32_e32 v206, 16, v215\n\t"
      "v_and_b32_e32 v207, 0xffff0000, v215\n\t"
      "global_load_dwordx4 v[168:171], v244, s[44:45] sc1\n\t"
      "global_load_dwordx4 v[172:175], v244, s[44:45] offset:1024 sc1\n\t"
      "global_load_dwordx4 v[176:179], v244, s[44:45] offset:2048 sc1\n\t"
      "global_load_dwordx4 v[180:183], v244, s[44:45] offset:3072 sc1\n\t"
      "global_load_dwordx4 v[184:187], v244, s[42:43] offset:-4096 sc1\n\t"
      "global_load_dwordx4 v[188:191], v244, s[42:43] offset:-3072 sc1\n\t"
      "global_load_dword v249, v248, s[12:13]\n\t"
      "s_barrier\n\t"
      "s_waitcnt lgkmcnt(0)\n\t"
      "LGM_%=:\n\t"
      "s_waitcnt vmcnt(15)\n\t"
      "v_max_u32_e32 v232, v128, v132\n\t"
      "v_cmp_eq_u32_e32 vcc, -1, v232\n\t"
      "s_cbranch_vccnz LO0R_%=\n\t"
      "LO0B_%=:\n\t"
      "v_mfma_f32_16x16x32_bf16 v[192:195], v[0:3], v[128:131], v[192:195]\n\t"
      "ds_read_b64_tr_b16 v[0:1], v216 offset:35840\n\t"
      "ds_read_b64_tr_b16 v[2:3], v220 offset:35840\n\t"
      "v_mfma_f32_16x16x32_bf16 v[196:199], v[4:7], v[128:131], v[196:199]\n\t"
      "ds_read_b64_tr_b16 v[4:5], v217 offset:35840\n\t"
      "v_mfma_f32_16x16x32_bf16 v[200:203], v[8:11], v[128:131], v[200:203]\n\t"
      "ds_read_b64_tr_b16 v[6:7], v221 offset:35840\n\t"
      "ds_read_b64_tr_b16 v[8:9], v218 offset:35840\n\t"
      "v_mfma_f32_16x16x32_bf16 v[204:207], v[12:15], v[128:131], v[204:207]\n\t"
      "ds_read_b64_tr_b16 v[10:11], v222 offset:35840\n\t"
      "v_mfma_f32_16x16x32_bf16 v[192:195], v[16:19], v[132:135], v[192:195]\n\t"
      "ds_read_b64_tr_b16 v[12:13], v219 offset:35840\n\t"
      "ds_read_b64_tr_b16 v[14:15], v223 offset:35840\n\t"
      "v_mfma_f32_16x16x32_bf16 v[196:199], v[20:23], v[132:135], v[196:199]\n\t"
      "ds_read_b64_tr_b16 v[16:17], v218 offset:39424\n\t"
      "v_mfma_f32_16x16x32_bf16 v[200:203], v[24:27], v[132:135], v[200:203]\n\t"
      "ds_read_b64_tr_b16 v[18:19], v222 offset:39424\n\t"
      "ds_read_b64_tr_b16 v[20:21], v219 offset:39424\n\t"
      "v_mfma_f32_16x16x32_bf16 v[204:207], v[28:31], v[132:135], v[204:207]\n\t"
      "ds_read_b64_tr_b16 v[22:23], v223 offset:39424\n\t"
      "s_waitcnt vmcnt(13)\n\t"
      "v_max_u32_e32 v232, v136, v140\n\t"
      "v_cmp_eq_u32_e32 vcc, -1, v232\n\t"
      "s_cbranch_vccnz LO2R_%=\n\t"
      "LO2B_%=:\n\t"
      "v_mfma_f32_16x16x32_bf16 v[192:195], v[32:35], v[136:139], v[192:195]\n\t"
      "ds_read_b64_tr_b16 v[24:25], v216 offset:39424\n\t"
      "ds_read_b64_tr_b16 v[26:27], v220 offset:39424\n\t"
      "v_mfma_f32_16x16x32_bf16 v[196:199], v[36:39], v[136:139], v[196:199]\n\t"
      "ds_read_b64_tr_b16 v[28:29], v217 offset:39424\n\t"
      "v_mfma_f32_16x16x32_bf16 v[200:203], v[40:43], v[136:139], v[200:203]\n\t"
      "ds_read_b64_tr_b16 v[30:31], v221 offset:39424\n\t"
      "ds_read_b64_tr_b16 v[32:33], v216 offset:43008\n\t"
      "v_mfma_f32_16x16x32_bf16 v[204:207], v[44:47], v[136:139], v[204:207]\n\t"
      "ds_read_b64_tr_b16 v[34:35], v220 offset:43008\n\t"
      "v_mfma_f32_16x16x32_bf16 v[192:195], v[48:51], v[140:143], v[192:195]\n\t"
      "ds_read_b64_tr_b16 v[36:37], v217 offset:43008\n\t"
      "ds_read_b64_tr_b16 v[38:39], v221 offset:43008\n\t"
      "v_mfma_f32_16x16x32_bf16 v[196:199], v[52:55], v[140:143], v[196:199]\n\t"
      "ds_read_b64_tr_b16 v[40:41], v218 offset:43008\n\t"
      "v_mfma_f32_16x16x32_bf16 v[200:203], v[56:59], v[140:143], v[200:203]\n\t"
      "ds_read_b64_tr_b16 v[42:43], v222 offset:43008\n\t"
      "ds_read_b64_tr_b16 v[44:45], v219 offset:43008\n\t"
      "v_mfma_f32_16x16x32_bf16 v[204:207], v[60:63], v[140:143], v[204:207]\n\t"
      "ds_read_b64_tr_b16 v[46:47], v223 offset:43008\n\t"
      "s_waitcnt vmcnt(11)\n\t"
      "v_max_u32_e32 v232, v144, v148\n\t"
      "v_cmp_eq_u32_e32 vcc, -1, v232\n\t"
      "s_cbranch_vccnz LO4R_%=\n\t"
      "LO4B_%=:\n\t"
      "v_mfma_f32_16x16x32_bf16 v[192:195], v[64:67], v[144:147], v[192:195]\n\t"
      "ds_read_b64_tr_b16 v[48:49], v218 offset:46592\n\t"
      "ds_read_b64_tr_b16 v[50:51], v222 offset:46592\n\t"
      "v_mfma_f32_16x16x32_bf16 v[196:199], v[68:71], v[144:147], v[196:199]\n\t"
      "ds_read_b64_tr_b16 v[52:53], v219 offset:46592\n\t"
      "v_mfma_f32_16x16x32_bf16 v[200:203], v[72:75], v[144:147], v[200:203]\n\t"
      "ds_read_b64_tr_b16 v[54:55], v223 offset:46592\n\t"
      "ds_read_b64_tr_b16 v[56:57], v216 offset:46592\n\t"
      "v_mfma_f32_16x16x32_bf16 v[204:207], v[76:79], v[144:147], v[204:207]\n\t"
      "ds_read_b64_tr_b16 v[58:59], v220 offset:46592\n\t"
      "v_mfma_f32_16x16x32_bf16 v[192:195], v[80:83], v[148:151], v[192:195]\n\t"
      "ds_read_b64_tr_b16 v[60:61], v217 offset:46592\n\t"
      "ds_read_b64_tr_b16 v[62:63], v221 offset:46592\n\t"
      "v_mfma_f32_16x16x32_bf16 v[196:199], v[84:87], v[148:151], v[196:199]\n\t"
      "ds_read_b64_tr_b16 v[64:65], v216 offset:50176\n\t"
      "v_mfma_f32_16x16x32_bf16 v[200:203], v[88:91], v[148:151], v[200:203]\n\t"
      "ds_read_b64_tr_b16 v[66:67], v220 offset:50176\n\t"
      "ds_read_b64_tr_b16 v[68:69], v217 offset:50176\n\t"
      "v_mfma_f32_16x16x32_bf16 v[204:207], v[92:95], v[148:151], v[204:207]\n\t"
      "ds_read_b64_tr_b16 v[70:71], v221 offset:50176\n\t"
      "s_waitcnt vmcnt(9)\n\t"
      "v_max_u32_e32 v232, v152, v156\n\t"
      "v_cmp_eq_u32_e32 vcc, -1, v232\n\t"
      "s_cbranch_vccnz LO6R_%=\n\t"
      "LO6B_%=:\n\t"
      "v_mfma_f32_16x16x32_bf16 v[192:195], v[96:99], v[152:155], v[192:195]\n\t"
      "ds_read_b64_tr_b16 v[72:73], v218 offset:50176\n\t"
      "ds_read_b64_tr_b16 v[74:75], v222 offset:50176\n\t"
      "v_mfma_f32_16x16x32_bf16 v[196:199], v[100:103], v[152:155], v[196:199]\n\t"
      "ds_read_b64_tr_b16 v[76:77], v219 offset:50176\n\t"
      "v_mfma_f32_16x16x32_bf16 v[200:203], v[104:107], v[152:155], v[200:203]\n\t"
      "ds_read_b64_tr_b16 v[78:79], v223 offset:50176\n\t"
      "ds_read_b64_tr_b16 v[80:81], v218 offset:53760\n\t"
      "v_mfma_f32_16x16x32_bf16 v[204:207], v[108:111], v[152:155], v[204:207]\n\t"
      "ds_read_b64_tr_b16 v[82:83], v222 offset:53760\n\t"
      "v_mfma_f32_16x16x32_bf16 v[192:195], v[112:115], v[156:159], v[192:195]\n\t"
      "ds_read_b64_tr_b16 v[84:85], v219 offset:53760\n\t"
      "ds_read_b64_tr_b16 v[86:87], v223 offset:53760\n\t"
      "v_mfma_f32_16x16x32_bf16 v[196:199], v[116:119], v[156:159], v[196:199]\n\t"
      "ds_read_b64_tr_b16 v[88:89], v216 offset:53760\n\t"
      "v_mfma_f32_16x16x32_bf16 v[200:203], v[120:123], v[156:159], v[200:203]\n\t"
      "ds_read_b64_tr_b16 v[90:91], v220 offset:53760\n\t"
      "ds_read_b64_tr_b16 v[92:93], v217 offset:53760\n\t"
      "v_mfma_f32_16x16x32_bf16 v[204:207], v[124:127], v[156:159], v[204:207]\n\t"
      "ds_read_b64_tr_b16 v[94:95], v221 offset:53760\n\t"
      "s_waitcnt vmcnt(7)\n\t"
      "v_max_u32_e32 v232, v160, v164\n\t"
      "v_cmp_eq_u32_e32 vcc, -1, v232\n\t"
      "s_cbranch_vccnz LO8R_%=\n\t"
      "LO8B_%=:\n\t"
      "s_waitcnt lgkmcnt(14)\n\t"
      "v_mfma_f32_16x16x32_bf16 v[192:195], v[0:3], v[160:163], v[192:195]\n\t"
      "ds_read_b64_tr_b16 v[96:97], v216\n\t"
      "ds_read_b64_tr_b16 v[98:99], v220\n\t"
      "v_mfma_f32_16x16x32_bf16 v[196:199], v[4:7], v[160:163], v[196:199]\n\t"
      "ds_read_b64_tr_b16 v[100:101], v217\n\t"
      "v_mfma_f32_16x16x32_bf16 v[200:203], v[8:11], v[160:163], v[200:203]\n\t"
      "ds_read_b64_tr_b16 v[102:103], v221\n\t"
      "ds_read_b64_tr_b16 v[104:105], v218\n\t"
      "v_mfma_f32_16x16x32_bf16 v[204:207], v[12:15], v[160:163], v[204:207]\n\t"
      "ds_read_b64_tr_b16 v[106:107], v222\n\t"
      "v_mfma_f32_16x16x32_bf16 v[192:195], v[16:19], v[164:167], v[192:195]\n\t"
      "ds_read_b64_tr_b16 v[108:109], v219\n\t"
      "ds_read_b64_tr_b16 v[110:111], v223\n\t"
      "v_mfma_f32_16x16x32_bf16 v[196:199], v[20:23], v[164:167], v[196:199]\n\t"
      "ds_read_b64_tr_b16 v[112:113], v218 offset:3584\n\t"
      "v_mfma_f32_16x16x32_bf16 v[200:203], v[24:27], v[164:167], v[200:203]\n\t"
      "ds_read_b64_tr_b16 v[114:115], v222 offset:3584\n\t"
      "ds_read_b64_tr_b16 v[116:117], v219 offset:3584\n\t"
      "v_mfma_f32_16x16x32_bf16 v[204:207], v[28:31], v[164:167], v[204:207]\n\t"
      "ds_read_b64_tr_b16 v[118:119], v223 offset:3584\n\t"
      "s_waitcnt vmcnt(5)\n\t"
      "v_max_u32_e32 v232, v168, v172\n\t"
      "v_cmp_eq_u32_e32 vcc, -1, v232\n\t"
      "s_cbranch_vccnz LO10R_%=\n\t"
      "LO10B_%=:\n\t"
      "v_mfma_f32_16x16x32_bf16 v[192:195], v[32:35], v[168:171], v[192:195]\n\t"
      "ds_read_b64_tr_b16 v[120:121], v216 offset:3584\n\t"
      "ds_read_b64_tr_b16 v[122:123], v220 offset:3584\n\t"
      "v_mfma_f32_16x16x32_bf16 v[196:199], v[36:39], v[168:171], v[196:199]\n\t"
      "ds_read_b64_tr_b16 v[124:125], v217 offset:3584\n\t"
      "v_mfma_f32_16x16x32_bf16 v[200:203], v[40:43], v[168:171], v[200:203]\n\t"
      "ds_read_b64_tr_b16 v[126:127], v221 offset:3584\n\t"
      "v_mfma_f32_16x16x32_bf16 v[204:207], v[44:47], v[168:171], v[204:207]\n\t"
      "v_mfma_f32_16x16x32_bf16 v[192:195], v[48:51], v[172:175], v[192:195]\n\t"
      "v_mfma_f32_16x16x32_bf16 v[196:199], v[52:55], v[172:175], v[196:199]\n\t"
      "v_mfma_f32_16x16x32_bf16 v[200:203], v[56:59], v[172:175], v[200:203]\n\t"
      "v_mfma_f32_16x16x32_bf16 v[204:207], v[60:63], v[172:175], v[204:207]\n\t"
      "s_waitcnt vmcnt(3)\n\t"
      "v_max_u32_e32 v232, v176, v180\n\t"
      "v_cmp_eq_u32_e32 vcc, -1, v232\n\t"
      "s_cbranch_vccnz LO12R_%=\n\t"
      "LO12B_%=:\n\t"
      "s_waitcnt lgkmcnt(14)\n\t"
      "v_mfma_f32_16x16x32_bf16 v[192:195], v[64:67], v[176:179], v[192:195]\n\t"
      "v_mfma_f32_16x16x32_bf16 v[196:199], v[68:71], v[176:179], v[196:199]\n\t"
      "v_mfma_f32_16x16x32_bf16 v[200:203], v[72:75], v[176:179], v[200:203]\n\t"
      "v_mfma_f32_16x16x32_bf16 v[204:207], v[76:79], v[176:179], v[204:207]\n\t"
      "v_mfma_f32_16x16x32_bf16 v[192:195], v[80:83], v[180:183], v[192:195]\n\t"
      "v_mfma_f32_16x16x32_bf16 v[196:199], v[84:87], v[180:183], v[196:199]\n\t"
      "v_mfma_f32_16x16x32_bf16 v[200:203], v[88:91], v[180:183], v[200:203]\n\t"
      "v_mfma_f32_16x16x32_bf16 v[204:207], v[92:95], v[180:183], v[204:207]\n\t"
      "s_waitcnt vmcnt(1)\n\t"
      "v_max_u32_e32 v232, v184, v188\n\t"
      "v_cmp_eq_u32_e32 vcc, -1, v232\n\t"
      "s_cbranch_vccnz LO14R_%=\n\t"
      "LO14B_%=:\n\t"
      "s_waitcnt lgkmcnt(8)\n\t"
      "v_mfma_f32_16x16x32_bf16 v[192:195], v[96:99], v[184:187], v[192:195]\n\t"
      "v_mfma_f32_16x16x32_bf16 v[196:199], v[100:103], v[184:187], v[196:199]\n\t"
      "v_mfma_f32_16x16x32_bf16 v[200:203], v[104:107], v[184:187], v[200:203]\n\t"
      "v_mfma_f32_16x16x32_bf16 v[204:207], v[108:111], v[184:187], v[204:207]\n\t"
      "s_waitcnt lgkmcnt(0)\n\t"
      "v_mfma_f32_16x16x32_bf16 v[192:195], v[112:115], v[188:191], v[192:195]\n\t"
      "v_mfma_f32_16x16x32_bf16 v[196:199], v[116:119], v[188:191], v[196:199]\n\t"
      "v_mfma_f32_16x16x32_bf16 v[200:203], v[120:123], v[188:191], v[200:203]\n\t"
      "v_mfma_f32_16x16x32_bf16 v[204:207], v[124:127], v[188:191], v[204:207]\n\t"
      "s_nop 4\n\t"
      "v_cmp_u_f32_e32 vcc, v192, v192\n\t"
      "s_cbranch_vccnz LGX_%=\n\t"
      "LGB_%=:\n\t"
      "v_cvt_pk_bf16_f32 v208, v192, v193\n\t"
      "v_cvt_pk_bf16_f32 v209, v194, v195\n\t"
      "v_cvt_pk_bf16_f32 v210, v196, v197\n\t"
      "v_cvt_pk_bf16_f32 v211, v198, v199\n\t"
      "v_cvt_pk_bf16_f32 v212, v200, v201\n\t"
      "v_cvt_pk_bf16_f32 v213, v202, v203\n\t"
      "v_cvt_pk_bf16_f32 v214, v204, v205\n\t"
      "v_cvt_pk_bf16_f32 v215, v206, v207\n\t"
      "v_mov_b32_e32 v148, 0x3f803f80\n\t"
      "v_dot2_f32_bf16 v144, v208, v148, 0\n\t"
      "v_dot2_f32_bf16 v145, v208, v208, 0\n\t"
      "v_dot2_f32_bf16 v146, v209, v148, 0\n\t"
      "v_dot2_f32_bf16 v147, v209, v209, 0\n\t"
      "v_dot2_f32_bf16 v144, v210, v148, v144\n\t"
      "v_dot2_f32_bf16 v145, v210, v210, v145\n\t"
      "v_dot2_f32_bf16 v146, v211, v148, v146\n\t"
      "v_dot2_f32_bf16 v147, v211, v211, v147\n\t"
      "v_dot2_f32_bf16 v144, v212, v148, v144\n\t"
      "v_dot2_f32_bf16 v145, v212, v212, v145\n\t"
      "v_dot2_f32_bf16 v146, v213, v148, v146\n\t"
      "v_dot2_f32_bf16 v147, v213, v213, v147\n\t"
      "v_dot2_f32_bf16 v144, v214, v148, v144\n\t"
      "v_dot2_f32_bf16 v145, v214, v214, v145\n\t"
      "v_dot2_f32_bf16 v146, v215, v148, v146\n\t"
      "v_dot2_f32_bf16 v147, v215, v215, v147\n\t"
      "v_lshlrev_b32_e32 v128, 16, v208\n\t"
      "v_and_b32_e32 v129, 0xffff0000, v208\n\t"
      "v_lshlrev_b32_e32 v130, 16, v209\n\t"
      "v_pk_add_f32 v[144:145], v[144:145], v[146:147]\n\t"
      "v_and_b32_e32 v131, 0xffff0000, v209\n\t"
      "v_lshlrev_b32_e32 v132, 16, v210\n\t"
      "v_permlane16_swap_b32_e32 v144, v145\n\t"
      "v_add_f32_e32 v144, v144, v145\n\t"
      "v_mov_b32_e32 v145, v144\n\t"
      "v_and_b32_e32 v133, 0xffff0000, v210\n\t"
      "v_lshlrev_b32_e32 v134, 16, v211\n\t"
      "v_permlane32_swap_b32_e32 v144, v145\n\t"
      "v_add_f32_e32 v144, v144, v145\n\t"
      "v_mov_b32_e32 v145, v144\n\t"
      "v_and_b32_e32 v135, 0xffff0000, v211\n\t"
      "v_lshlrev_b32_e32 v136, 16, v212\n\t"
      "v_permlane16_swap_b32_e32 v144, v145\n\t"
      "s_mov_b64 exec, s[70:71]\n\t"
      "v_min_u32_e32 v144, -2, v144\n\t"
      "v_min_u32_e32 v145, -2, v145\n\t"
      "global_store_dwordx2 v231, v[144:145], s[58:59] sc1\n\t"
      "s_mov_b64 exec, -1\n\t"
      "s_barrier\n\t"
      "v_and_b32_e32 v137, 0xffff0000, v212\n\t"
      "v_lshlrev_b32_e32 v138, 16, v213\n\t"
      "v_and_b32_e32 v139, 0xffff0000, v213\n\t"
      "v_lshlrev_b32_e32 v140, 16, v214\n\t"
      "v_and_b32_e32 v141, 0xffff0000, v214\n\t"
      "v_lshlrev_b32_e32 v142, 16, v215\n\t"
      "v_and_b32_e32 v143, 0xffff0000, v215\n\t"
      "v_permlane16_swap_b32_e32 v208, v210\n\t"
      "v_permlane16_swap_b32_e32 v209, v211\n\t"
      "v_permlane16_swap_b32_e32 v212, v214\n\t"
      "v_permlane16_swap_b32_e32 v213, v215\n\t"
      "s_bitcmp0_b32 s27, 0\n\t"
      "s_cbranch_scc1 NTK0_%=\n\t"
      "ds_read_b64_tr_b16 v[0:1], v224 offset:16384\n\t"
      "ds_read_b64_tr_b16 v[2:3], v224 offset:16896\n\t"
      "ds_read_b64_tr_b16 v[4:5], v225 offset:16384\n\t"
      "ds_read_b64_tr_b16 v[6:7], v225 offset:16896\n\t"
      "ds_read_b64_tr_b16 v[8:9], v226 offset:16384\n\t"
      "ds_read_b64_tr_b16 v[10:11], v226 offset:16896\n\t"
      "ds_read_b64_tr_b16 v[12:13], v227 offset:16384\n\t"
      "ds_read_b64_tr_b16 v[14:15], v227 offset:16896\n\t"
      "ds_read_b64_tr_b16 v[16:17], v224 offset:49152\n\t"
      "ds_read_b64_tr_b16 v[18:19], v224 offset:49664\n\t"
      "ds_read_b64_tr_b16 v[20:21], v225 offset:49152\n\t"
      "ds_read_b64_tr_b16 v[22:23], v225 offset:49664\n\t"
      "ds_read_b64_tr_b16 v[24:25], v226 offset:49152\n\t"
      "ds_read_b64_tr_b16 v[26:27], v226 offset:49664\n\t"
      "ds_read_b64_tr_b16 v[28:29], v227 offset:49152\n\t"
      "ds_read_b64_tr_b16 v[30:31], v227 offset:49664\n\t"
      "ds_read_b64_tr_b16 v[32:33], v224\n\t"
      "ds_read_b64_tr_b16 v[34:35], v224 offset:512\n\t"
      "ds_read_b64_tr_b16 v[36:37], v225\n\t"
      "ds_read_b64_tr_b16 v[38:39], v225 offset:512\n\t"
      "ds_read_b64_tr_b16 v[40:41], v226\n\t"
      "ds_read_b64_tr_b16 v[42:43], v226 offset:512\n\t"
      "ds_read_b64_tr_b16 v[44:45], v227\n\t"
      "ds_read_b64_tr_b16 v[46:47], v227 offset:512\n\t"
      "ds_read_b64_tr_b16 v[48:49], v224 offset:32768\n\t"
      "ds_read_b64_tr_b16 v[50:51], v224 offset:33280\n\t"
      "ds_read_b64_tr_b16 v[52:53], v225 offset:32768\n\t"
      "ds_read_b64_tr_b16 v[54:55], v225 offset:33280\n\t"
      "ds_read_b64_tr_b16 v[56:57], v226 offset:32768\n\t"
      "ds_read_b64_tr_b16 v[58:59], v226 offset:33280\n\t"
      "ds_read_b64_tr_b16 v[60:61], v227 offset:32768\n\t"
      "ds_read_b64_tr_b16 v[62:63], v227 offset:33280\n\t"
      "NTK0_%=:\n\t"
      "s_waitcnt lgkmcnt(0)\n\t"
      "s_barrier\n\t"
      "global_load_dwordx2 v[148:149], v231, s[46:47] sc1\n\t"
      "global_load_dwordx2 v[150:151], v231, s[46:47] offset:384 sc1\n\t"
      "s_waitcnt vmcnt(0)\n\t"
      "v_max3_u32 v232, v148, v149, v150\n\t"
      "v_max_u32_e32 v232, v232, v151\n\t"
      "v_cmp_eq_u32_e32 vcc, -1, v232\n\t"
      "s_cbranch_vccnz LSR_%=\n\t"
      "LSB_%=:\n\t"
      "v_cndmask_b32_e64 v150, v150, 0, s[70:71]\n\t"
      "v_cndmask_b32_e64 v151, v151, 0, s[70:71]\n\t"
      "v_pk_add_f32 v[144:145], v[148:149], v[150:151]\n\t"
      "s_nop 1\n\t"
      "v_permlane16_swap_b32_e32 v144, v145\n\t"
      "v_add_f32_e32 v144, v144, v145\n\t"
      "v_mov_b32_e32 v145, v144\n\t"
      "s_nop 1\n\t"
      "v_permlane32_swap_b32_e32 v144, v145\n\t"
      "v_add_f32_e32 v144, v144, v145\n\t"
      "v_mov_b32_e32 v145, v144\n\t"
      "s_nop 1\n\t"
      "v_permlane16_swap_b32_e32 v144, v145\n\t"
      "v_mul_f32_e32 v144, 0xbb124925, v144\n\t"
      "v_mul_f32_e32 v145, 0x3b124925, v145\n\t"
      "v_fma_f32 v145, -v144, v144, v145\n\t"
      "v_max_f32_e32 v145, 0, v145\n\t"
      "v_add_f32_e32 v146, 0x3727c5ac, v145\n\t"
      "v_fmac_f32_e32 v145, 0x3727c5ac, v146\n\t"
      "v_rsq_f32_e32 v145, v145\n\t"
      "s_nop 0\n\t"
      "v_mul_f32_e32 v144, v144, v145\n\t"
      "v_pk_fma_f32 v[128:129], v[128:129], v[144:145], v[144:145] op_sel:[0,1,0] op_sel_hi:[1,1,0]\n\t"
      "v_pk_fma_f32 v[130:131], v[130:131], v[144:145], v[144:145] op_sel:[0,1,0] op_sel_hi:[1,1,0]\n\t"
      "v_cvt_pk_bf16_f32 v152, v128, v129\n\t"
      "v_cvt_pk_bf16_f32 v153, v130, v131\n\t"
      "ds_write_b64 v233, v[152:153]\n\t"
      "v_pk_fma_f32 v[132:133], v[132:133], v[144:145], v[144:145] op_sel:[0,1,0] op_sel_hi:[1,1,0]\n\t"
      "v_pk_fma_f32 v[134:135], v[134:135], v[144:145], v[144:145] op_sel:[0,1,0] op_sel_hi:[1,1,0]\n\t"
      "v_cvt_pk_bf16_f32 v154, v132, v133\n\t"
      "v_cvt_pk_bf16_f32 v155, v134, v135\n\t"
      "ds_write_b64 v234, v[154:155]\n\t"
      "ds_read_b64_tr_b16 v[160:161], v237\n\t"
      "ds_read_b64_tr_b16 v[162:163], v238 offset:512\n\t"
      "v_pk_fma_f32 v[136:137], v[136:137], v[144:145], v[144:145] op_sel:[0,1,0] op_sel_hi:[1,1,0]\n\t"
      "v_pk_fma_f32 v[138:139], v[138:139], v[144:145], v[144:145] op_sel:[0,1,0] op_sel_hi:[1,1,0]\n\t"
      "v_cvt_pk_bf16_f32 v156, v136, v137\n\t"
      "v_cvt_pk_bf16_f32 v157, v138, v139\n\t"
      "ds_write_b64 v235, v[156:157]\n\t"
      "v_pk_fma_f32 v[140:141], v[140:141], v[144:145], v[144:145] op_sel:[0,1,0] op_sel_hi:[1,1,0]\n\t"
      "v_pk_fma_f32 v[142:143], v[142:143], v[144:145], v[144:145] op_sel:[0,1,0] op_sel_hi:[1,1,0]\n\t"
      "v_cvt_pk_bf16_f32 v158, v140, v141\n\t"
      "v_cvt_pk_bf16_f32 v159, v142, v143\n\t"
      "ds_write_b64 v236, v[158:159]\n\t"
      "ds_read_b64_tr_b16 v[164:165], v239\n\t"
      "ds_read_b64_tr_b16 v[166:167], v240 offset:512\n\t"
      "s_waitcnt lgkmcnt(4)\n\t"
      "v_min_u32_e32 v160, -2, v160\n\t"
      "v_min_u32_e32 v161, -2, v161\n\t"
      "v_min_u32_e32 v162, -2, v162\n\t"
      "v_min_u32_e32 v163, -2, v163\n\t"
      "global_store_dwordx4 v229, v[160:163], s[48:49] sc1\n\t"
      "s_waitcnt lgkmcnt(0)\n\t"
      "v_min_u32_e32 v164, -2, v164\n\t"
      "v_min_u32_e32 v165, -2, v165\n\t"
      "v_min_u32_e32 v166, -2, v166\n\t"
      "v_min_u32_e32 v167, -2, v167\n\t"
      "global_store_dwordx4 v230, v[164:167], s[48:49] sc1\n\t"
      "s_barrier\n\t"
      "s_bitcmp0_b32 s27, 0\n\t"
      "s_cbranch_scc1 G4C_%=\n\t"
      "s_mul_i32 s34, s32, 196608\n\t"
      "s_mul_i32 s35, s24, 8192\n\t"
      "s_add_u32 s34, s34, s35\n\t"
      "s_add_u32 s34, s34, 1048576\n\t"
      "s_add_u32 s64, s48, s34\n\t"
      "s_addc_u32 s65, s49, 0\n\t"
      "v_add_u32_e32 v250, 4096, v244\n\t"
      "global_load_dwordx4 v[128:131], v250, s[64:65] offset:-4096 sc1\n\t"
      "global_load_dwordx4 v[132:135], v250, s[64:65] offset:-3072 sc1\n\t"
      "global_load_dwordx4 v[136:139], v250, s[64:65] offset:-2048 sc1\n\t"
      "global_load_dwordx4 v[140:143], v250, s[64:65] offset:-1024 sc1\n\t"
      "global_load_dwordx4 v[144:147], v250, s[64:65] sc1\n\t"
      "global_load_dwordx4 v[148:151], v250, s[64:65] offset:1024 sc1\n\t"
      "global_load_dwordx4 v[152:155], v250, s[64:65] offset:2048 sc1\n\t"
      "global_load_dwordx4 v[156:159], v250, s[64:65] offset:3072 sc1\n\t"
      "global_store_dwordx4 v241, v[208:211], s[20:21] offset:-2048 sc1\n\t"
      "global_store_dwordx4 v241, v[212:215], s[20:21] offset:2048 sc1\n\t"
      "s_setprio 0\n\t"
      "ds_read_b64_tr_b16 v[64:65], v224 offset:16384\n\t"
      "ds_read_b64_tr_b16 v[66:67], v224 offset:16896\n\t"
      "ds_read_b64_tr_b16 v[68:69], v225 offset:16384\n\t"
      "ds_read_b64_tr_b16 v[70:71], v225 offset:16896\n\t"
      "ds_read_b64_tr_b16 v[72:73], v226 offset:16384\n\t"
      "ds_read_b64_tr_b16 v[74:75], v226 offset:16896\n\t"
      "ds_read_b64_tr_b16 v[76:77], v227 offset:16384\n\t"
      "ds_read_b64_tr_b16 v[78:79], v227 offset:16896\n\t"
      "ds_read_b64_tr_b16 v[80:81], v224 offset:49152\n\t"
      "ds_read_b64_tr_b16 v[82:83], v224 offset:49664\n\t"
      "ds_read_b64_tr_b16 v[84:85], v225 offset:49152\n\t"
      "ds_read_b64_tr_b16 v[86:87], v225 offset:49664\n\t"
      "ds_read_b64_tr_b16 v[88:89], v226 offset:49152\n\t"
      "ds_read_b64_tr_b16 v[90:91], v226 offset:49664\n\t"
      "ds_read_b64_tr_b16 v[92:93], v227 offset:49152\n\t"
      "ds_read_b64_tr_b16 v[94:95], v227 offset:49664\n\t"
      "ds_read_b64_tr_b16 v[96:97], v224\n\t"
      "ds_read_b64_tr_b16 v[98:99], v224 offset:512\n\t"
      "ds_read_b64_tr_b16 v[100:101], v225\n\t"
      "ds_read_b64_tr_b16 v[102:103], v225 offset:512\n\t"
      "ds_read_b64_tr_b16 v[104:105], v226\n\t"
      "ds_read_b64_tr_b16 v[106:107], v226 offset:512\n\t"
      "ds_read_b64_tr_b16 v[108:109], v227\n\t"
      "ds_read_b64_tr_b16 v[110:111], v227 offset:512\n\t"
      "ds_read_b64_tr_b16 v[112:113], v224 offset:32768\n\t"
      "ds_read_b64_tr_b16 v[114:115], v224 offset:33280\n\t"
      "ds_read_b64_tr_b16 v[116:117], v225 offset:32768\n\t"
      "ds_read_b64_tr_b16 v[118:119], v225 offset:33280\n\t"
      "ds_read_b64_tr_b16 v[120:121], v226 offset:32768\n\t"
      "ds_read_b64_tr_b16 v[122:123], v226 offset:33280\n\t"
      "ds_read_b64_tr_b16 v[124:125], v227 offset:32768\n\t"
      "ds_read_b64_tr_b16 v[126:127], v227 offset:33280\n\t"
      "s_waitcnt vmcnt(2) lgkmcnt(0)\n\t"
      "GST_%=:\n\t"
      "s_add_u32 s66, s64, 65536\n\t"
      "s_addc_u32 s67, s65, 0\n\t"
      "s_add_u32 s68, s66, 65536\n\t"
      "s_addc_u32 s69, s67, 0\n\t"
      "s_lshl_b32 s34, s32, 18\n\t"
      "s_lshl_b32 s35, s24, 13\n\t"
      "s_add_u32 s34, s34, s35\n\t"
      "s_add_u32 s70, s48, s34\n\t"
      "s_addc_u32 s71, s49, 0\n\t"
      "s_add_u32 s72, s70, 65536\n\t"
      "s_addc_u32 s73, s71, 0\n\t"
      "s_add_u32 s74, s72, 65536\n\t"
      "s_addc_u32 s75, s73, 0\n\t"
      "s_add_u32 s76, s74, 65536\n\t"
      "s_addc_u32 s77, s75, 0\n\t"
      "s_mul_i32 s34, s30, 28\n\t"
      "s_add_u32 s34, s34, s29\n\t"
      "s_lshl_b32 s34, s34, 11\n\t"
      "s_add_u32 s34, s34, 10223616\n\t"
      "s_add_u32 s62, s20, s34\n\t"
      "s_addc_u32 s63, s21, 0\n\t"
      "v_lshlrev_b32_e32 v248, 16, v249\n\t"
      "v_and_b32_e32 v249, 0xffff0000, v249\n\t"
      "v_mov_b32_e32 v252, 0xbdd2d3e8\n\t"
      "v_xor_b32_e32 v253, 128, v245\n\t"
      "v_max3_u32 v251, v128, v129, v130\n\t"
      "v_max3_u32 v251, v251, v131, v132\n\t"
      "v_max3_u32 v251, v251, v133, v134\n\t"
      "v_max3_u32 v251, v251, v135, v136\n\t"
      "v_max3_u32 v251, v251, v137, v138\n\t"
      "v_max3_u32 v251, v251, v139, v140\n\t"
      "v_max3_u32 v251, v251, v141, v142\n\t"
      "v_max3_u32 v251, v251, v143, v144\n\t"
      "v_max3_u32 v251, v251, v145, v146\n\t"
      "v_max3_u32 v251, v251, v147, v148\n\t"
      "v_max3_u32 v251, v251, v149, v150\n\t"
      "v_max3_u32 v251, v251, v151, v152\n\t"
      "v_max3_u32 v251, v251, v153, v154\n\t"
      "v_max3_u32 v251, v251, v155, v156\n\t"
      "v_max3_u32 v251, v251, v157, v158\n\t"
      "v_max_u32_e32 v251, v251, v159\n\t"
      "v_cmp_eq_u32_e32 vcc, -1, v251\n\t"
      "s_cbranch_vccnz LA30R_%=\n\t"
      "LA30B_%=:\n\t"
      "v_mfma_f32_16x16x32_bf16 v[208:211], v[0:3], v[128:131], 0\n\t"
      "global_load_dwordx4 v[160:163], v250, s[66:67] offset:-4096 sc1\n\t"
      "v_mfma_f32_16x16x32_bf16 v[208:211], v[16:19], v[132:135], v[208:211]\n\t"
      "v_mfma_f32_16x16x32_bf16 v[208:211], v[32:35], v[136:139], v[208:211]\n\t"
      "global_load_dwordx4 v[164:167], v250, s[66:67] offset:-3072 sc1\n\t"
      "v_mfma_f32_16x16x32_bf16 v[208:211], v[48:51], v[140:143], v[208:211]\n\t"
      "v_mfma_f32_16x16x32_bf16 v[208:211], v[64:67], v[144:147], v[208:211]\n\t"
      "global_load_dwordx4 v[168:171], v250, s[66:67] offset:-2048 sc1\n\t"
      "v_mfma_f32_16x16x32_bf16 v[208:211], v[80:83], v[148:151], v[208:211]\n\t"
      "s_lshl_b32 s34, s2, 7\n\t"
      "s_add_u32 s34, s34, 65536\n\t"
      "v_mov_b32_e32 v230, s34\n\t"
      "s_cmp_lg_u32 s24, 3\n\t"
      "s_cbranch_scc1 SK17_%=\n\t"
      "global_load_dwordx2 v[228:229], v230, s[60:61] sc1\n\t"
      "SK17_%=:\n\t"
      "v_mfma_f32_16x16x32_bf16 v[208:211], v[96:99], v[152:155], v[208:211]\n\t"
      "global_load_dwordx4 v[172:175], v250, s[66:67] offset:-1024 sc1\n\t"
      "v_mfma_f32_16x16x32_bf16 v[208:211], v[112:115], v[156:159], v[208:211]\n\t"
      "v_mfma_f32_16x16x32_bf16 v[212:215], v[4:7], v[128:131], 0\n\t"
      "global_load_dwordx4 v[176:179], v250, s[66:67] sc1\n\t"
      "v_mfma_f32_16x16x32_bf16 v[212:215], v[20:23], v[132:135], v[212:215]\n\t"
      "v_mfma_f32_16x16x32_bf16 v[212:215], v[36:39], v[136:139], v[212:215]\n\t"
      "global_load_dwordx4 v[180:183], v250, s[66:67] offset:1024 sc1\n\t"
      "v_mfma_f32_16x16x32_bf16 v[212:215], v[52:55], v[140:143], v[212:215]\n\t"
      "v_mfma_f32_16x16x32_bf16 v[212:215], v[68:71], v[144:147], v[212:215]\n\t"
      "global_load_dwordx4 v[184:187], v250, s[66:67] offset:2048 sc1\n\t"
      "v_mfma_f32_16x16x32_bf16 v[212:215], v[84:87], v[148:151], v[212:215]\n\t"
      "ds_write_b128 v245, v[208:211]\n\t"
      "v_mfma_f32_16x16x32_bf16 v[212:215], v[100:103], v[152:155], v[212:215]\n\t"
      "global_load_dwordx4 v[188:191], v250, s[66:67] offset:3072 sc1\n\t"
      "v_mfma_f32_16x16x32_bf16 v[212:215], v[116:119], v[156:159], v[212:215]\n\t"
      "v_mfma_f32_16x16x32_bf16 v[216:219], v[8:11], v[128:131], 0\n\t"
      "global_load_dwordx4 v[192:195], v250, s[68:69] offset:-4096 sc1\n\t"
      "v_mfma_f32_16x16x32_bf16 v[216:219], v[24:27], v[132:135], v[216:219]\n\t"
      "v_mfma_f32_16x16x32_bf16 v[216:219], v[40:43], v[136:139], v[216:219]\n\t"
      "global_load_dwordx4 v[196:199], v250, s[68:69] offset:-3072 sc1\n\t"
      "v_mfma_f32_16x16x32_bf16 v[216:219], v[56:59], v[140:143], v[216:219]\n\t"
      "v_mfma_f32_16x16x32_bf16 v[216:219], v[72:75], v[144:147], v[216:219]\n\t"
      "global_load_dwordx4 v[200:203], v250, s[68:69] offset:-2048 sc1\n\t"
      "v_mfma_f32_16x16x32_bf16 v[216:219], v[88:91], v[148:151], v[216:219]\n\t"
      "ds_write_b128 v253, v[212:215] offset:1024\n\t"
      "v_mfma_f32_16x16x32_bf16 v[216:219], v[104:107], v[152:155], v[216:219]\n\t"
      "global_load_dwordx4 v[204:207], v250, s[68:69] offset:-1024 sc1\n\t"
      "v_mfma_f32_16x16x32_bf16 v[216:219], v[120:123], v[156:159], v[216:219]\n\t"
      "v_mfma_f32_16x16x32_bf16 v[220:223], v[12:15], v[128:131], 0\n\t"
      "global_load_dwordx4 v[128:131], v250, s[68:69] sc1\n\t"
      "v_mfma_f32_16x16x32_bf16 v[220:223], v[28:31], v[132:135], v[220:223]\n\t"
      "v_mfma_f32_16x16x32_bf16 v[220:223], v[44:47], v[136:139], v[220:223]\n\t"
      "v_mfma_f32_16x16x32_bf16 v[220:223], v[60:63], v[140:143], v[220:223]\n\t"
      "v_mfma_f32_16x16x32_bf16 v[220:223], v[76:79], v[144:147], v[220:223]\n\t"
      "global_load_dwordx4 v[132:135], v250, s[68:69] offset:1024 sc1\n\t"
      "v_mfma_f32_16x16x32_bf16 v[220:223], v[92:95], v[148:151], v[220:223]\n\t"
      "ds_write_b128 v245, v[216:219] offset:2048\n\t"
      "v_mfma_f32_16x16x32_bf16 v[220:223], v[108:111], v[152:155], v[220:223]\n\t"
      "v_mfma_f32_16x16x32_bf16 v[220:223], v[124:127], v[156:159], v[220:223]\n\t"
      "s_waitcnt vmcnt(10)\n\t"
      "v_mfma_f32_16x16x32_bf16 v[224:227], v[0:3], v[160:163], 0\n\t"
      "global_load_dwordx4 v[136:139], v250, s[68:69] offset:2048 sc1\n\t"
      "v_mfma_f32_16x16x32_bf16 v[224:227], v[16:19], v[164:167], v[224:227]\n\t"
      "s_nop 3\n\t"
      "ds_write_b128 v253, v[220:223] offset:3072\n\t"
      "v_mfma_f32_16x16x32_bf16 v[224:227], v[32:35], v[168:171], v[224:227]\n\t"
      "v_mfma_f32_16x16x32_bf16 v[224:227], v[48:51], v[172:175], v[224:227]\n\t"
      "s_waitcnt vmcnt(7)\n\t"
      "v_mfma_f32_16x16x32_bf16 v[224:227], v[64:67], v[176:179], v[224:227]\n\t"
      "global_load_dwordx4 v[140:143], v250, s[68:69] offset:3072 sc1\n\t"
      "s_cmp_lg_u32 s24, 3\n\t"
      "s_cbranch_scc1 LF6B_%=\n\t"
      "v_cmp_ne_u64_e32 vcc, s[22:23], v[228:229]\n\t"
      "s_cbranch_vccnz LF6R_%=\n\t"
      "LF6B_%=:\n\t"
      "s_waitcnt lgkmcnt(0)\n\t"
      "s_barrier\n\t"
      "s_cmp_lg_u32 s24, 3\n\t"
      "s_cbranch_scc1 HF3_%=\n\t"
      "s_lshl3_add_u32 s34, s25, s26\n\t"
      "s_lshl_b32 s34, s34, 7\n\t"
      "s_add_u32 s34, s34, 98304\n\t"
      "v_mov_b32_e32 v238, s34\n\t"
      "v_mov_b32_e32 v236, s22\n\t"
      "v_mov_b32_e32 v237, s23\n\t"
      "s_mov_b64 exec, 1\n\t"
      "global_store_dwordx2 v238, v[236:237], s[60:61] sc1\n\t"
      "s_mov_b64 exec, -1\n\t"
      "HF3_%=:\n\t"
      "ds_read_b64 v[228:229], v246\n\t"
      "ds_read_b64 v[230:231], v246 offset:4096\n\t"
      "s_setprio 1\n\t"
      "v_mfma_f32_16x16x32_bf16 v[224:227], v[80:83], v[180:183], v[224:227]\n\t"
      "ds_read_b64 v[232:233], v246 offset:8192\n\t"
      "ds_read_b64 v[234:235], v246 offset:12288\n\t"
      "v_mfma_f32_16x16x32_bf16 v[224:227], v[96:99], v[184:187], v[224:227]\n\t"
      "ds_read_b64 v[236:237], v246 offset:16384\n\t"
      "ds_read_b64 v[238:239], v246 offset:20480\n\t"
      "v_mfma_f32_16x16x32_bf16 v[224:227], v[112:115], v[188:191], v[224:227]\n\t"
      "ds_read_b64 v[240:241], v246 offset:24576\n\t"
      "ds_read_b64 v[242:243], v246 offset:28672\n\t"
      "v_mfma_f32_16x16x32_bf16 v[208:211], v[4:7], v[160:163], 0\n\t"
      "global_load_dwordx4 v[144:147], v250, s[70:71] offset:-4096 sc1\n\t"
      "v_mfma_f32_16x16x32_bf16 v[208:211], v[20:23], v[164:167], v[208:211]\n\t"
      "s_waitcnt lgkmcnt(4)\n\t"
      "v_add_f32_e32 v228, v228, v230\n\t"
      "v_add_f32_e32 v232, v232, v234\n\t"
      "v_mfma_f32_16x16x32_bf16 v[208:211], v[36:39], v[168:171], v[208:211]\n\t"
      "v_add_f32_e32 v229, v229, v231\n\t"
      "v_add_f32_e32 v233, v233, v235\n\t"
      "v_add_f32_e32 v228, v228, v232\n\t"
      "v_cmp_u_f32_e32 vcc, v224, v224\n\t"
      "s_cbranch_vccnz LA31R_%=\n\t"
      "LA31B_%=:\n\t"
      "v_mfma_f32_16x16x32_bf16 v[208:211], v[52:55], v[172:175], v[208:211]\n\t"
      "v_add_f32_e32 v229, v229, v233\n\t"
      "s_waitcnt lgkmcnt(0)\n\t"
      "v_add_f32_e32 v236, v236, v238\n\t"
      "v_mfma_f32_16x16x32_bf16 v[208:211], v[68:71], v[176:179], v[208:211]\n\t"
      "v_add_f32_e32 v240, v240, v242\n\t"
      "v_add_f32_e32 v237, v237, v239\n\t"
      "v_add_f32_e32 v241, v241, v243\n\t"
      "global_load_dwordx4 v[148:151], v250, s[70:71] offset:-3072 sc1\n\t"
      "v_mfma_f32_16x16x32_bf16 v[208:211], v[84:87], v[180:183], v[208:211]\n\t"
      "v_add_f32_e32 v228, v228, v248\n\t"
      "v_add_f32_e32 v236, v236, v240\n\t"
      "ds_write_b128 v245, v[224:227] offset:32768\n\t"
      "v_mfma_f32_16x16x32_bf16 v[208:211], v[100:103], v[184:187], v[208:211]\n\t"
      "v_add_f32_e32 v229, v229, v249\n\t"
      "v_add_f32_e32 v237, v237, v241\n\t"
      "v_add_f32_e32 v228, v228, v236\n\t"
      "v_mfma_f32_16x16x32_bf16 v[208:211], v[116:119], v[188:191], v[208:211]\n\t"
      "v_add_f32_e32 v229, v229, v237\n\t"
      "v_mul_f32_e32 v230, v228, v228\n\t"
      "v_mul_f32_e32 v231, v229, v229\n\t"
      "v_mfma_f32_16x16x32_bf16 v[212:215], v[8:11], v[160:163], 0\n\t"
      "v_fmaak_f32 v230, v252, v230, 0xc0135761\n\t"
      "v_fmaak_f32 v231, v252, v231, 0xc0135761\n\t"
      "v_mul_f32_e32 v230, v228, v230\n\t"
      "global_load_dwordx4 v[152:155], v250, s[70:71] offset:-2048 sc1\n\t"
      "v_mfma_f32_16x16x32_bf16 v[212:215], v[24:27], v[164:167], v[212:215]\n\t"
      "v_mul_f32_e32 v231, v229, v231\n\t"
      "v_exp_f32_e32 v230, v230\n\t"
      "v_exp_f32_e32 v231, v231\n\t"
      "v_mfma_f32_16x16x32_bf16 v[212:215], v[40:43], v[168:171], v[212:215]\n\t"
      "v_add_f32_e32 v230, 1.0, v230\n\t"
      "v_add_f32_e32 v231, 1.0, v231\n\t"
      "v_mfma_f32_16x16x32_bf16 v[212:215], v[56:59], v[172:175], v[212:215]\n\t"
      "v_rcp_f32_e32 v230, v230\n\t"
      "v_rcp_f32_e32 v231, v231\n\t"
      "v_mul_f32_e32 v228, v228, v230\n\t"
      "v_mfma_f32_16x16x32_bf16 v[212:215], v[72:75], v[176:179], v[212:215]\n\t"
      "v_mul_f32_e32 v229, v229, v231\n\t"
      "v_cvt_pk_bf16_f32 v230, v228, v229\n\t"
      "v_min_u32_e32 v254, -2, v230\n\t"
      "global_load_dwordx4 v[156:159], v250, s[70:71] offset:-1024 sc1\n\t"
      "v_mfma_f32_16x16x32_bf16 v[212:215], v[88:91], v[180:183], v[212:215]\n\t"
      "global_store_dword v247, v254, s[62:63] sc1\n\t"
      "s_add_u32 s62, s62, 57344\n\t"
      "s_addc_u32 s63, s63, 0\n\t"
      "s_setprio 0\n\t"
      "ds_write_b128 v253, v[208:211] offset:33792\n\t"
      "v_mfma_f32_16x16x32_bf16 v[212:215], v[104:107], v[184:187], v[212:215]\n\t"
      "v_mfma_f32_16x16x32_bf16 v[212:215], v[120:123], v[188:191], v[212:215]\n\t"
      "v_mfma_f32_16x16x32_bf16 v[216:219], v[12:15], v[160:163], 0\n\t"
      "global_load_dwordx4 v[160:163], v250, s[70:71] sc1\n\t"
      "v_mfma_f32_16x16x32_bf16 v[216:219], v[28:31], v[164:167], v[216:219]\n\t"
      "v_mfma_f32_16x16x32_bf16 v[216:219], v[44:47], v[168:171], v[216:219]\n\t"
      "v_mfma_f32_16x16x32_bf16 v[216:219], v[60:63], v[172:175], v[216:219]\n\t"
      "v_mfma_f32_16x16x32_bf16 v[216:219], v[76:79], v[176:179], v[216:219]\n\t"
      "global_load_dwordx4 v[164:167], v250, s[70:71] offset:1024 sc1\n\t"
      "v_mfma_f32_16x16x32_bf16 v[216:219], v[92:95], v[180:183], v[216:219]\n\t"
      "ds_write_b128 v245, v[212:215] offset:34816\n\t"
      "v_mfma_f32_16x16x32_bf16 v[216:219], v[108:111], v[184:187], v[216:219]\n\t"
      "v_mfma_f32_16x16x32_bf16 v[216:219], v[124:127], v[188:191], v[216:219]\n\t"
      "s_waitcnt vmcnt(11)\n\t"
      "v_mfma_f32_16x16x32_bf16 v[220:223], v[0:3], v[192:195], 0\n\t"
      "global_load_dwordx4 v[168:171], v250, s[70:71] offset:2048 sc1\n\t"
      "v_mfma_f32_16x16x32_bf16 v[220:223], v[16:19], v[196:199], v[220:223]\n\t"
      "s_nop 3\n\t"
      "ds_write_b128 v253, v[216:219] offset:35840\n\t"
      "v_mfma_f32_16x16x32_bf16 v[220:223], v[32:35], v[200:203], v[220:223]\n\t"
      "v_mfma_f32_16x16x32_bf16 v[220:223], v[48:51], v[204:207], v[220:223]\n\t"
      "s_waitcnt vmcnt(8)\n\t"
      "v_mfma_f32_16x16x32_bf16 v[220:223], v[64:67], v[128:131], v[220:223]\n\t"
      "global_load_dwordx4 v[172:175], v250, s[70:71] offset:3072 sc1\n\t"
      "s_waitcnt lgkmcnt(0)\n\t"
      "s_barrier\n\t"
      "ds_read_b64 v[228:229], v246 offset:32768\n\t"
      "ds_read_b64 v[230:231], v246 offset:36864\n\t"
      "s_setprio 1\n\t"
      "v_mfma_f32_16x16x32_bf16 v[220:223], v[80:83], v[132:135], v[220:223]\n\t"
      "ds_read_b64 v[232:233], v246 offset:40960\n\t"
      "ds_read_b64 v[234:235], v246 offset:45056\n\t"
      "v_mfma_f32_16x16x32_bf16 v[220:223], v[96:99], v[136:139], v[220:223]\n\t"
      "ds_read_b64 v[236:237], v246 offset:49152\n\t"
      "ds_read_b64 v[238:239], v246 offset:53248\n\t"
      "v_mfma_f32_16x16x32_bf16 v[220:223], v[112:115], v[140:143], v[220:223]\n\t"
      "ds_read_b64 v[240:241], v246 offset:57344\n\t"
      "ds_read_b64 v[242:243], v246 offset:61440\n\t"
      "v_mfma_f32_16x16x32_bf16 v[224:227], v[4:7], v[192:195], 0\n\t"
      "global_load_dwordx4 v[176:179], v250, s[72:73] offset:-4096 sc1\n\t"
      "v_mfma_f32_16x16x32_bf16 v[224:227], v[20:23], v[196:199], v[224:227]\n\t"
      "s_waitcnt lgkmcnt(4)\n\t"
      "v_add_f32_e32 v228, v228, v230\n\t"
      "v_add_f32_e32 v232, v232, v234\n\t"
      "v_mfma_f32_16x16x32_bf16 v[224:227], v[36:39], v[200:203], v[224:227]\n\t"
      "v_add_f32_e32 v229, v229, v231\n\t"
      "v_add_f32_e32 v233, v233, v235\n\t"
      "v_add_f32_e32 v228, v228, v232\n\t"
      "v_cmp_u_f32_e32 vcc, v220, v220\n\t"
      "s_cbranch_vccnz LA32R_%=\n\t"
      "LA32B_%=:\n\t"
      "v_mfma_f32_16x16x32_bf16 v[224:227], v[52:55], v[204:207], v[224:227]\n\t"
      "v_add_f32_e32 v229, v229, v233\n\t"
      "s_waitcnt lgkmcnt(0)\n\t"
      "v_add_f32_e32 v236, v236, v238\n\t"
      "v_mfma_f32_16x16x32_bf16 v[224:227], v[68:71], v[128:131], v[224:227]\n\t"
      "v_add_f32_e32 v240, v240, v242\n\t"
      "v_add_f32_e32 v237, v237, v239\n\t"
      "v_add_f32_e32 v241, v241, v243\n\t"
      "global_load_dwordx4 v[180:183], v250, s[72:73] offset:-3072 sc1\n\t"
      "v_mfma_f32_16x16x32_bf16 v[224:227], v[84:87], v[132:135], v[224:227]\n\t"
      "v_add_f32_e32 v228, v228, v248\n\t"
      "v_add_f32_e32 v236, v236, v240\n\t"
      "ds_write_b128 v245, v[220:223]\n\t"
      "v_mfma_f32_16x16x32_bf16 v[224:227], v[100:103], v[136:139], v[224:227]\n\t"
      "v_add_f32_e32 v229, v229, v249\n\t"
      "v_add_f32_e32 v237, v237, v241\n\t"
      "v_add_f32_e32 v228, v228, v236\n\t"
      "v_mfma_f32_16x16x32_bf16 v[224:227], v[116:119], v[140:143], v[224:227]\n\t"
      "v_add_f32_e32 v229, v229, v237\n\t"
      "v_mul_f32_e32 v230, v228, v228\n\t"
      "v_mul_f32_e32 v231, v229, v229\n\t"
      "v_mfma_f32_16x16x32_bf16 v[208:211], v[8:11], v[192:195], 0\n\t"
      "v_fmaak_f32 v230, v252, v230, 0xc0135761\n\t"
      "v_fmaak_f32 v231, v252, v231, 0xc0135761\n\t"
      "v_mul_f32_e32 v230, v228, v230\n\t"
      "global_load_dwordx4 v[184:187], v250, s[72:73] offset:-2048 sc1\n\t"
      "v_mfma_f32_16x16x32_bf16 v[208:211], v[24:27], v[196:199], v[208:211]\n\t"
      "v_mul_f32_e32 v231, v229, v231\n\t"
      "v_exp_f32_e32 v230, v230\n\t"
      "v_exp_f32_e32 v231, v231\n\t"
      "v_mfma_f32_16x16x32_bf16 v[208:211], v[40:43], v[200:203], v[208:211]\n\t"
      "v_add_f32_e32 v230, 1.0, v230\n\t"
      "v_add_f32_e32 v231, 1.0, v231\n\t"
      "v_mfma_f32_16x16x32_bf16 v[208:211], v[56:59], v[204:207], v[208:211]\n\t"
      "v_rcp_f32_e32 v230, v230\n\t"
      "v_rcp_f32_e32 v231, v231\n\t"
      "v_mul_f32_e32 v228, v228, v230\n\t"
      "v_mfma_f32_16x16x32_bf16 v[208:211], v[72:75], v[128:131], v[208:211]\n\t"
      "v_mul_f32_e32 v229, v229, v231\n\t"
      "v_cvt_pk_bf16_f32 v230, v228, v229\n\t"
      "v_min_u32_e32 v254, -2, v230\n\t"
      "global_load_dwordx4 v[188:191], v250, s[72:73] offset:-1024 sc1\n\t"
      "v_mfma_f32_16x16x32_bf16 v[208:211], v[88:91], v[132:135], v[208:211]\n\t"
      "global_store_dword v247, v254, s[62:63] sc1\n\t"
      "s_add_u32 s62, s62, 57344\n\t"
      "s_addc_u32 s63, s63, 0\n\t"
      "s_setprio 0\n\t"
      "ds_write_b128 v253, v[224:227] offset:1024\n\t"
      "v_mfma_f32_16x16x32_bf16 v[208:211], v[104:107], v[136:139], v[208:211]\n\t"
      "v_mfma_f32_16x16x32_bf16 v[208:211], v[120:123], v[140:143], v[208:211]\n\t"
      "v_mfma_f32_16x16x32_bf16 v[212:215], v[12:15], v[192:195], 0\n\t"
      "global_load_dwordx4 v[192:195], v250, s[72:73] sc1\n\t"
      "v_mfma_f32_16x16x32_bf16 v[212:215], v[28:31], v[196:199], v[212:215]\n\t"
      "v_mfma_f32_16x16x32_bf16 v[212:215], v[44:47], v[200:203], v[212:215]\n\t"
      "v_mfma_f32_16x16x32_bf16 v[212:215], v[60:63], v[204:207], v[212:215]\n\t"
      "v_mfma_f32_16x16x32_bf16 v[212:215], v[76:79], v[128:131], v[212:215]\n\t"
      "global_load_dwordx4 v[196:199], v250, s[72:73] offset:1024 sc1\n\t"
      "v_mfma_f32_16x16x32_bf16 v[212:215], v[92:95], v[132:135], v[212:215]\n\t"
      "ds_write_b128 v245, v[208:211] offset:2048\n\t"
      "v_mfma_f32_16x16x32_bf16 v[212:215], v[108:111], v[136:139], v[212:215]\n\t"
      "v_mfma_f32_16x16x32_bf16 v[212:215], v[124:127], v[140:143], v[212:215]\n\t"
      "s_waitcnt vmcnt(12)\n\t"
      "v_mfma_f32_16x16x32_bf16 v[216:219], v[0:3], v[144:147], 0\n\t"
      "global_load_dwordx4 v[200:203], v250, s[72:73] offset:2048 sc1\n\t"
      "v_mfma_f32_16x16x32_bf16 v[216:219], v[16:19], v[148:151], v[216:219]\n\t"
      "s_nop 3\n\t"
      "ds_write_b128 v253, v[212:215] offset:3072\n\t"
      "v_mfma_f32_16x16x32_bf16 v[216:219], v[32:35], v[152:155], v[216:219]\n\t"
      "v_mfma_f32_16x16x32_bf16 v[216:219], v[48:51], v[156:159], v[216:219]\n\t"
      "s_waitcnt vmcnt(8)\n\t"
      "v_mfma_f32_16x16x32_bf16 v[216:219], v[64:67], v[160:163], v[216:219]\n\t"
      "global_load_dwordx4 v[204:207], v250, s[72:73] offset:3072 sc1\n\t"
      "s_waitcnt lgkmcnt(0)\n\t"
      "s_barrier\n\t"
      "ds_read_b64 v[228:229], v246\n\t"
      "ds_read_b64 v[230:231], v246 offset:4096\n\t"
      "s_setprio 1\n\t"
      "v_mfma_f32_16x16x32_bf16 v[216:219], v[80:83], v[164:167], v[216:219]\n\t"
      "ds_read_b64 v[232:233], v246 offset:8192\n\t"
      "ds_read_b64 v[234:235], v246 offset:12288\n\t"
      "v_mfma_f32_16x16x32_bf16 v[216:219], v[96:99], v[168:171], v[216:219]\n\t"
      "ds_read_b64 v[236:237], v246 offset:16384\n\t"
      "ds_read_b64 v[238:239], v246 offset:20480\n\t"
      "v_mfma_f32_16x16x32_bf16 v[216:219], v[112:115], v[172:175], v[216:219]\n\t"
      "ds_read_b64 v[240:241], v246 offset:24576\n\t"
      "ds_read_b64 v[242:243], v246 offset:28672\n\t"
      "v_mfma_f32_16x16x32_bf16 v[220:223], v[4:7], v[144:147], 0\n\t"
      "global_load_dwordx4 v[128:131], v250, s[74:75] offset:-4096 sc1\n\t"
      "v_mfma_f32_16x16x32_bf16 v[220:223], v[20:23], v[148:151], v[220:223]\n\t"
      "s_waitcnt lgkmcnt(4)\n\t"
      "v_add_f32_e32 v228, v228, v230\n\t"
      "v_add_f32_e32 v232, v232, v234\n\t"
      "v_mfma_f32_16x16x32_bf16 v[220:223], v[36:39], v[152:155], v[220:223]\n\t"
      "v_add_f32_e32 v229, v229, v231\n\t"
      "v_add_f32_e32 v233, v233, v235\n\t"
      "v_add_f32_e32 v228, v228, v232\n\t"
      "v_cmp_u_f32_e32 vcc, v216, v216\n\t"
      "s_cbranch_vccnz LA33R_%=\n\t"
      "LA33B_%=:\n\t"
      "v_mfma_f32_16x16x32_bf16 v[220:223], v[52:55], v[156:159], v[220:223]\n\t"
      "v_add_f32_e32 v229, v229, v233\n\t"
      "s_waitcnt lgkmcnt(0)\n\t"
      "v_add_f32_e32 v236, v236, v238\n\t"
      "v_mfma_f32_16x16x32_bf16 v[220:223], v[68:71], v[160:163], v[220:223]\n\t"
      "v_add_f32_e32 v240, v240, v242\n\t"
      "v_add_f32_e32 v237, v237, v239\n\t"
      "v_add_f32_e32 v241, v241, v243\n\t"
      "global_load_dwordx4 v[132:135], v250, s[74:75] offset:-3072 sc1\n\t"
      "v_mfma_f32_16x16x32_bf16 v[220:223], v[84:87], v[164:167], v[220:223]\n\t"
      "v_add_f32_e32 v228, v228, v248\n\t"
      "v_add_f32_e32 v236, v236, v240\n\t"
      "ds_write_b128 v245, v[216:219] offset:32768\n\t"
      "v_mfma_f32_16x16x32_bf16 v[220:223], v[100:103], v[168:171], v[220:223]\n\t"
      "v_add_f32_e32 v229, v229, v249\n\t"
      "v_add_f32_e32 v237, v237, v241\n\t"
      "v_add_f32_e32 v228, v228, v236\n\t"
      "v_mfma_f32_16x16x32_bf16 v[220:223], v[116:119], v[172:175], v[220:223]\n\t"
      "v_add_f32_e32 v229, v229, v237\n\t"
      "v_mul_f32_e32 v230, v228, v228\n\t"
      "v_mul_f32_e32 v231, v229, v229\n\t"
      "v_mfma_f32_16x16x32_bf16 v[224:227], v[8:11], v[144:147], 0\n\t"
      "v_fmaak_f32 v230, v252, v230, 0xc0135761\n\t"
      "v_fmaak_f32 v231, v252, v231, 0xc0135761\n\t"
      "v_mul_f32_e32 v230, v228, v230\n\t"
      "global_load_dwordx4 v[136:139], v250, s[74:75] offset:-2048 sc1\n\t"
      "v_mfma_f32_16x16x32_bf16 v[224:227], v[24:27], v[148:151], v[224:227]\n\t"
      "v_mul_f32_e32 v231, v229, v231\n\t"
      "v_exp_f32_e32 v230, v230\n\t"
      "v_exp_f32_e32 v231, v231\n\t"
      "v_mfma_f32_16x16x32_bf16 v[224:227], v[40:43], v[152:155], v[224:227]\n\t"
      "v_add_f32_e32 v230, 1.0, v230\n\t"
      "v_add_f32_e32 v231, 1.0, v231\n\t"
      "v_mfma_f32_16x16x32_bf16 v[224:227], v[56:59], v[156:159], v[224:227]\n\t"
      "v_rcp_f32_e32 v230, v230\n\t"
      "v_rcp_f32_e32 v231, v231\n\t"
      "v_mul_f32_e32 v228, v228, v230\n\t"
      "v_mfma_f32_16x16x32_bf16 v[224:227], v[72:75], v[160:163], v[224:227]\n\t"
      "v_mul_f32_e32 v229, v229, v231\n\t"
      "v_cvt_pk_bf16_f32 v230, v228, v229\n\t"
      "v_min_u32_e32 v254, -2, v230\n\t"
      "global_load_dwordx4 v[140:143], v250, s[74:75] offset:-1024 sc1\n\t"
      "v_mfma_f32_16x16x32_bf16 v[224:227], v[88:91], v[164:167], v[224:227]\n\t"
      "global_store_dword v247, v254, s[62:63] sc1\n\t"
      "s_add_u32 s62, s62, 57344\n\t"
      "s_addc_u32 s63, s63, 0\n\t"
      "s_setprio 0\n\t"
      "ds_write_b128 v253, v[220:223] offset:33792\n\t"
      "v_mfma_f32_16x16x32_bf16 v[224:227], v[104:107], v[168:171], v[224:227]\n\t"
      "v_mfma_f32_16x16x32_bf16 v[224:227], v[120:123], v[172:175], v[224:227]\n\t"
      "v_mfma_f32_16x16x32_bf16 v[208:211], v[12:15], v[144:147], 0\n\t"
      "global_load_dwordx4 v[144:147], v250, s[74:75] sc1\n\t"
      "v_mfma_f32_16x16x32_bf16 v[208:211], v[28:31], v[148:151], v[208:211]\n\t"
      "v_mfma_f32_16x16x32_bf16 v[208:211], v[44:47], v[152:155], v[208:211]\n\t"
      "v_mfma_f32_16x16x32_bf16 v[208:211], v[60:63], v[156:159], v[208:211]\n\t"
      "v_mfma_f32_16x16x32_bf16 v[208:211], v[76:79], v[160:163], v[208:211]\n\t"
      "global_load_dwordx4 v[148:151], v250, s[74:75] offset:1024 sc1\n\t"
      "v_mfma_f32_16x16x32_bf16 v[208:211], v[92:95], v[164:167], v[208:211]\n\t"
      "ds_write_b128 v245, v[224:227] offset:34816\n\t"
      "v_mfma_f32_16x16x32_bf16 v[208:211], v[108:111], v[168:171], v[208:211]\n\t"
      "v_mfma_f32_16x16x32_bf16 v[208:211], v[124:127], v[172:175], v[208:211]\n\t"
      "s_waitcnt vmcnt(12)\n\t"
      "v_mfma_f32_16x16x32_bf16 v[212:215], v[0:3], v[176:179], 0\n\t"
      "global_load_dwordx4 v[152:155], v250, s[74:75] offset:2048 sc1\n\t"
      "v_mfma_f32_16x16x32_bf16 v[212:215], v[16:19], v[180:183], v[212:215]\n\t"
      "s_nop 3\n\t"
      "ds_write_b128 v253, v[208:211] offset:35840\n\t"
      "v_mfma_f32_16x16x32_bf16 v[212:215], v[32:35], v[184:187], v[212:215]\n\t"
      "v_mfma_f32_16x16x32_bf16 v[212:215], v[48:51], v[188:191], v[212:215]\n\t"
      "s_waitcnt vmcnt(8)\n\t"
      "v_mfma_f32_16x16x32_bf16 v[212:215], v[64:67], v[192:195], v[212:215]\n\t"
      "global_load_dwordx4 v[156:159], v250, s[74:75] offset:3072 sc1\n\t"
      "s_waitcnt lgkmcnt(0)\n\t"
      "s_barrier\n\t"
      "ds_read_b64 v[228:229], v246 offset:32768\n\t"
      "ds_read_b64 v[230:231], v246 offset:36864\n\t"
      "s_setprio 1\n\t"
      "v_mfma_f32_16x16x32_bf16 v[212:215], v[80:83], v[196:199], v[212:215]\n\t"
      "ds_read_b64 v[232:233], v246 offset:40960\n\t"
      "ds_read_b64 v[234:235], v246 offset:45056\n\t"
      "v_mfma_f32_16x16x32_bf16 v[212:215], v[96:99], v[200:203], v[212:215]\n\t"
      "ds_read_b64 v[236:237], v246 offset:49152\n\t"
      "ds_read_b64 v[238:239], v246 offset:53248\n\t"
      "v_mfma_f32_16x16x32_bf16 v[212:215], v[112:115], v[204:207], v[212:215]\n\t"
      "ds_read_b64 v[240:241], v246 offset:57344\n\t"
      "ds_read_b64 v[242:243], v246 offset:61440\n\t"
      "v_mfma_f32_16x16x32_bf16 v[216:219], v[4:7], v[176:179], 0\n\t"
      "global_load_dwordx4 v[160:163], v250, s[76:77] offset:-4096 sc1\n\t"
      "v_mfma_f32_16x16x32_bf16 v[216:219], v[20:23], v[180:183], v[216:219]\n\t"
      "s_waitcnt lgkmcnt(4)\n\t"
      "v_add_f32_e32 v228, v228, v230\n\t"
      "v_add_f32_e32 v232, v232, v234\n\t"
      "v_mfma_f32_16x16x32_bf16 v[216:219], v[36:39], v[184:187], v[216:219]\n\t"
      "v_add_f32_e32 v229, v229, v231\n\t"
      "v_add_f32_e32 v233, v233, v235\n\t"
      "v_add_f32_e32 v228, v228, v232\n\t"
      "v_cmp_u_f32_e32 vcc, v212, v212\n\t"
      "s_cbranch_vccnz LA34R_%=\n\t"
      "LA34B_%=:\n\t"
      "v_mfma_f32_16x16x32_bf16 v[216:219], v[52:55], v[188:191], v[216:219]\n\t"
      "v_add_f32_e32 v229, v229, v233\n\t"
      "s_waitcnt lgkmcnt(0)\n\t"
      "v_add_f32_e32 v236, v236, v238\n\t"
      "v_mfma_f32_16x16x32_bf16 v[216:219], v[68:71], v[192:195], v[216:219]\n\t"
      "v_add_f32_e32 v240, v240, v242\n\t"
      "v_add_f32_e32 v237, v237, v239\n\t"
      "v_add_f32_e32 v241, v241, v243\n\t"
      "global_load_dwordx4 v[164:167], v250, s[76:77] offset:-3072 sc1\n\t"
      "v_mfma_f32_16x16x32_bf16 v[216:219], v[84:87], v[196:199], v[216:219]\n\t"
      "v_add_f32_e32 v228, v228, v248\n\t"
      "v_add_f32_e32 v236, v236, v240\n\t"
      "ds_write_b128 v245, v[212:215]\n\t"
      "v_mfma_f32_16x16x32_bf16 v[216:219], v[100:103], v[200:203], v[216:219]\n\t"
      "v_add_f32_e32 v229, v229, v249\n\t"
      "v_add_f32_e32 v237, v237, v241\n\t"
      "v_add_f32_e32 v228, v228, v236\n\t"
      "v_mfma_f32_16x16x32_bf16 v[216:219], v[116:119], v[204:207], v[216:219]\n\t"
      "v_add_f32_e32 v229, v229, v237\n\t"
      "v_mul_f32_e32 v230, v228, v228\n\t"
      "v_mul_f32_e32 v231, v229, v229\n\t"
      "v_mfma_f32_16x16x32_bf16 v[220:223], v[8:11], v[176:179], 0\n\t"
      "v_fmaak_f32 v230, v252, v230, 0xc0135761\n\t"
      "v_fmaak_f32 v231, v252, v231, 0xc0135761\n\t"
      "v_mul_f32_e32 v230, v228, v230\n\t"
      "global_load_dwordx4 v[168:171], v250, s[76:77] offset:-2048 sc1\n\t"
      "v_mfma_f32_16x16x32_bf16 v[220:223], v[24:27], v[180:183], v[220:223]\n\t"
      "v_mul_f32_e32 v231, v229, v231\n\t"
      "v_exp_f32_e32 v230, v230\n\t"
      "v_exp_f32_e32 v231, v231\n\t"
      "v_mfma_f32_16x16x32_bf16 v[220:223], v[40:43], v[184:187], v[220:223]\n\t"
      "v_add_f32_e32 v230, 1.0, v230\n\t"
      "v_add_f32_e32 v231, 1.0, v231\n\t"
      "v_mfma_f32_16x16x32_bf16 v[220:223], v[56:59], v[188:191], v[220:223]\n\t"
      "v_rcp_f32_e32 v230, v230\n\t"
      "v_rcp_f32_e32 v231, v231\n\t"
      "v_mul_f32_e32 v228, v228, v230\n\t"
      "v_mfma_f32_16x16x32_bf16 v[220:223], v[72:75], v[192:195], v[220:223]\n\t"
      "v_mul_f32_e32 v229, v229, v231\n\t"
      "v_cvt_pk_bf16_f32 v230, v228, v229\n\t"
      "v_min_u32_e32 v254, -2, v230\n\t"
      "global_load_dwordx4 v[172:175], v250, s[76:77] offset:-1024 sc1\n\t"
      "v_mfma_f32_16x16x32_bf16 v[220:223], v[88:91], v[196:199], v[220:223]\n\t"
      "global_store_dword v247, v254, s[62:63] sc1\n\t"
      "s_add_u32 s62, s62, 57344\n\t"
      "s_addc_u32 s63, s63, 0\n\t"
      "s_setprio 0\n\t"
      "ds_write_b128 v253, v[216:219] offset:1024\n\t"
      "v_mfma_f32_16x16x32_bf16 v[220:223], v[104:107], v[200:203], v[220:223]\n\t"
      "v_mfma_f32_16x16x32_bf16 v[220:223], v[120:123], v[204:207], v[220:223]\n\t"
      "v_mfma_f32_16x16x32_bf16 v[224:227], v[12:15], v[176:179], 0\n\t"
      "global_load_dwordx4 v[176:179], v250, s[76:77] sc1\n\t"
      "v_mfma_f32_16x16x32_bf16 v[224:227], v[28:31], v[180:183], v[224:227]\n\t"
      "v_mfma_f32_16x16x32_bf16 v[224:227], v[44:47], v[184:187], v[224:227]\n\t"
      "v_mfma_f32_16x16x32_bf16 v[224:227], v[60:63], v[188:191], v[224:227]\n\t"
      "v_mfma_f32_16x16x32_bf16 v[224:227], v[76:79], v[192:195], v[224:227]\n\t"
      "global_load_dwordx4 v[180:183], v250, s[76:77] offset:1024 sc1\n\t"
      "v_mfma_f32_16x16x32_bf16 v[224:227], v[92:95], v[196:199], v[224:227]\n\t"
      "ds_write_b128 v245, v[220:223] offset:2048\n\t"
      "v_mfma_f32_16x16x32_bf16 v[224:227], v[108:111], v[200:203], v[224:227]\n\t"
      "v_mfma_f32_16x16x32_bf16 v[224:227], v[124:127], v[204:207], v[224:227]\n\t"
      "s_waitcnt vmcnt(12)\n\t"
      "v_mfma_f32_16x16x32_bf16 v[208:211], v[0:3], v[128:131], 0\n\t"
      "global_load_dwordx4 v[184:187], v250, s[76:77] offset:2048 sc1\n\t"
      "v_mfma_f32_16x16x32_bf16 v[208:211], v[16:19], v[132:135], v[208:211]\n\t"
      "s_nop 3\n\t"
      "ds_write_b128 v253, v[224:227] offset:3072\n\t"
      "v_mfma_f32_16x16x32_bf16 v[208:211], v[32:35], v[136:139], v[208:211]\n\t"
      "v_mfma_f32_16x16x32_bf16 v[208:211], v[48:51], v[140:143], v[208:211]\n\t"
      "s_waitcnt vmcnt(8)\n\t"
      "v_mfma_f32_16x16x32_bf16 v[208:211], v[64:67], v[144:147], v[208:211]\n\t"
      "global_load_dwordx4 v[188:191], v250, s[76:77] offset:3072 sc1\n\t"
      "s_waitcnt lgkmcnt(0)\n\t"
      "s_barrier\n\t"
      "ds_read_b64 v[228:229], v246\n\t"
      "ds_read_b64 v[230:231], v246 offset:4096\n\t"
      "s_setprio 1\n\t"
      "v_mfma_f32_16x16x32_bf16 v[208:211], v[80:83], v[148:151], v[208:211]\n\t"
      "ds_read_b64 v[232:233], v246 offset:8192\n\t"
      "ds_read_b64 v[234:235], v246 offset:12288\n\t"
      "v_mfma_f32_16x16x32_bf16 v[208:211], v[96:99], v[152:155], v[208:211]\n\t"
      "ds_read_b64 v[236:237], v246 offset:16384\n\t"
      "ds_read_b64 v[238:239], v246 offset:20480\n\t"
      "v_mfma_f32_16x16x32_bf16 v[208:211], v[112:115], v[156:159], v[208:211]\n\t"
      "ds_read_b64 v[240:241], v246 offset:24576\n\t"
      "ds_read_b64 v[242:243], v246 offset:28672\n\t"
      "v_mfma_f32_16x16x32_bf16 v[212:215], v[4:7], v[128:131], 0\n\t"
      "v_mfma_f32_16x16x32_bf16 v[212:215], v[20:23], v[132:135], v[212:215]\n\t"
      "s_waitcnt lgkmcnt(4)\n\t"
      "v_add_f32_e32 v228, v228, v230\n\t"
      "v_add_f32_e32 v232, v232, v234\n\t"
      "v_mfma_f32_16x16x32_bf16 v[212:215], v[36:39], v[136:139], v[212:215]\n\t"
      "v_add_f32_e32 v229, v229, v231\n\t"
      "v_add_f32_e32 v233, v233, v235\n\t"
      "v_add_f32_e32 v228, v228, v232\n\t"
      "v_cmp_u_f32_e32 vcc, v208, v208\n\t"
      "s_cbranch_vccnz LA35R_%=\n\t"
      "LA35B_%=:\n\t"
      "v_mfma_f32_16x16x32_bf16 v[212:215], v[52:55], v[140:143], v[212:215]\n\t"
      "v_add_f32_e32 v229, v229, v233\n\t"
      "s_waitcnt lgkmcnt(0)\n\t"
      "v_add_f32_e32 v236, v236, v238\n\t"
      "v_mfma_f32_16x16x32_bf16 v[212:215], v[68:71], v[144:147], v[212:215]\n\t"
      "v_add_f32_e32 v240, v240, v242\n\t"
      "v_add_f32_e32 v237, v237, v239\n\t"
      "v_add_f32_e32 v241, v241, v243\n\t"
      "v_mfma_f32_16x16x32_bf16 v[212:215], v[84:87], v[148:151], v[212:215]\n\t"
      "v_add_f32_e32 v228, v228, v248\n\t"
      "v_add_f32_e32 v236, v236, v240\n\t"
      "ds_write_b128 v245, v[208:211] offset:32768\n\t"
      "v_mfma_f32_16x16x32_bf16 v[212:215], v[100:103], v[152:155], v[212:215]\n\t"
      "v_add_f32_e32 v229, v229, v249\n\t"
      "v_add_f32_e32 v237, v237, v241\n\t"
      "v_add_f32_e32 v228, v228, v236\n\t"
      "v_mfma_f32_16x16x32_bf16 v[212:215], v[116:119], v[156:159], v[212:215]\n\t"
      "v_add_f32_e32 v229, v229, v237\n\t"
      "v_mul_f32_e32 v230, v228, v228\n\t"
      "v_mul_f32_e32 v231, v229, v229\n\t"
      "v_mfma_f32_16x16x32_bf16 v[216:219], v[8:11], v[128:131], 0\n\t"
      "v_fmaak_f32 v230, v252, v230, 0xc0135761\n\t"
      "v_fmaak_f32 v231, v252, v231, 0xc0135761\n\t"
      "v_mul_f32_e32 v230, v228, v230\n\t"
      "v_mfma_f32_16x16x32_bf16 v[216:219], v[24:27], v[132:135], v[216:219]\n\t"
      "v_mul_f32_e32 v231, v229, v231\n\t"
      "v_exp_f32_e32 v230, v230\n\t"
      "v_exp_f32_e32 v231, v231\n\t"
      "v_mfma_f32_16x16x32_bf16 v[216:219], v[40:43], v[136:139], v[216:219]\n\t"
      "v_add_f32_e32 v230, 1.0, v230\n\t"
      "v_add_f32_e32 v231, 1.0, v231\n\t"
      "v_mfma_f32_16x16x32_bf16 v[216:219], v[56:59], v[140:143], v[216:219]\n\t"
      "v_rcp_f32_e32 v230, v230\n\t"
      "v_rcp_f32_e32 v231, v231\n\t"
      "v_mul_f32_e32 v228, v228, v230\n\t"
      "v_mfma_f32_16x16x32_bf16 v[216:219], v[72:75], v[144:147], v[216:219]\n\t"
      "v_mul_f32_e32 v229, v229, v231\n\t"
      "v_cvt_pk_bf16_f32 v230, v228, v229\n\t"
      "v_min_u32_e32 v254, -2, v230\n\t"
      "v_mfma_f32_16x16x32_bf16 v[216:219], v[88:91], v[148:151], v[216:219]\n\t"
      "global_store_dword v247, v254, s[62:63] sc1\n\t"
      "s_add_u32 s62, s62, 57344\n\t"
      "s_addc_u32 s63, s63, 0\n\t"
      "s_setprio 0\n\t"
      "ds_write_b128 v253, v[212:215] offset:33792\n\t"
      "v_mfma_f32_16x16x32_bf16 v[216:219], v[104:107], v[152:155], v[216:219]\n\t"
      "v_mfma_f32_16x16x32_bf16 v[216:219], v[120:123], v[156:159], v[216:219]\n\t"
      "v_mfma_f32_16x16x32_bf16 v[220:223], v[12:15], v[128:131], 0\n\t"
      "v_mfma_f32_16x16x32_bf16 v[220:223], v[28:31], v[132:135], v[220:223]\n\t"
      "v_mfma_f32_16x16x32_bf16 v[220:223], v[44:47], v[136:139], v[220:223]\n\t"
      "v_mfma_f32_16x16x32_bf16 v[220:223], v[60:63], v[140:143], v[220:223]\n\t"
      "v_mfma_f32_16x16x32_bf16 v[220:223], v[76:79], v[144:147], v[220:223]\n\t"
      "v_mfma_f32_16x16x32_bf16 v[220:223], v[92:95], v[148:151], v[220:223]\n\t"
      "s_nop 1\n\t"
      "ds_write_b128 v245, v[216:219] offset:34816\n\t"
      "v_mfma_f32_16x16x32_bf16 v[220:223], v[108:111], v[152:155], v[220:223]\n\t"
      "v_mfma_f32_16x16x32_bf16 v[220:223], v[124:127], v[156:159], v[220:223]\n\t"
      "s_waitcnt vmcnt(6)\n\t"
      "v_mfma_f32_16x16x32_bf16 v[224:227], v[0:3], v[160:163], 0\n\t"
      "v_mfma_f32_16x16x32_bf16 v[224:227], v[16:19], v[164:167], v[224:227]\n\t"
      "s_nop 4\n\t"
      "ds_write_b128 v253, v[220:223] offset:35840\n\t"
      "v_mfma_f32_16x16x32_bf16 v[224:227], v[32:35], v[168:171], v[224:227]\n\t"
      "v_mfma_f32_16x16x32_bf16 v[224:227], v[48:51], v[172:175], v[224:227]\n\t"
      "s_waitcnt vmcnt(1)\n\t"
      "v_mfma_f32_16x16x32_bf16 v[224:227], v[64:67], v[176:179], v[224:227]\n\t"
      "s_waitcnt lgkmcnt(0)\n\t"
      "s_barrier\n\t"
      "ds_read_b64 v[228:229], v246 offset:32768\n\t"
      "ds_read_b64 v[230:231], v246 offset:36864\n\t"
      "s_setprio 1\n\t"
      "v_mfma_f32_16x16x32_bf16 v[224:227], v[80:83], v[180:183], v[224:227]\n\t"
      "ds_read_b64 v[232:233], v246 offset:40960\n\t"
      "ds_read_b64 v[234:235], v246 offset:45056\n\t"
      "v_mfma_f32_16x16x32_bf16 v[224:227], v[96:99], v[184:187], v[224:227]\n\t"
      "ds_read_b64 v[236:237], v246 offset:49152\n\t"
      "ds_read_b64 v[238:239], v246 offset:53248\n\t"
      "v_mfma_f32_16x16x32_bf16 v[224:227], v[112:115], v[188:191], v[224:227]\n\t"
      "ds_read_b64 v[240:241], v246 offset:57344\n\t"
      "ds_read_b64 v[242:243], v246 offset:61440\n\t"
      "v_mfma_f32_16x16x32_bf16 v[208:211], v[4:7], v[160:163], 0\n\t"
      "v_mfma_f32_16x16x32_bf16 v[208:211], v[20:23], v[164:167], v[208:211]\n\t"
      "s_waitcnt lgkmcnt(4)\n\t"
      "v_add_f32_e32 v228, v228, v230\n\t"
      "v_add_f32_e32 v232, v232, v234\n\t"
      "v_mfma_f32_16x16x32_bf16 v[208:211], v[36:39], v[168:171], v[208:211]\n\t"
      "v_add_f32_e32 v229, v229, v231\n\t"
      "v_add_f32_e32 v233, v233, v235\n\t"
      "v_add_f32_e32 v228, v228, v232\n\t"
      "v_cmp_u_f32_e32 vcc, v224, v224\n\t"
      "s_cbranch_vccnz LA36R_%=\n\t"
      "LA36B_%=:\n\t"
      "v_mfma_f32_16x16x32_bf16 v[208:211], v[52:55], v[172:175], v[208:211]\n\t"
      "v_add_f32_e32 v229, v229, v233\n\t"
      "s_waitcnt lgkmcnt(0)\n\t"
      "v_add_f32_e32 v236, v236, v238\n\t"
      "v_mfma_f32_16x16x32_bf16 v[208:211], v[68:71], v[176:179], v[208:211]\n\t"
      "v_add_f32_e32 v240, v240, v242\n\t"
      "v_add_f32_e32 v237, v237, v239\n\t"
      "v_add_f32_e32 v241, v241, v243\n\t"
      "v_mfma_f32_16x16x32_bf16 v[208:211], v[84:87], v[180:183], v[208:211]\n\t"
      "v_add_f32_e32 v228, v228, v248\n\t"
      "v_add_f32_e32 v236, v236, v240\n\t"
      "ds_write_b128 v245, v[224:227]\n\t"
      "v_mfma_f32_16x16x32_bf16 v[208:211], v[100:103], v[184:187], v[208:211]\n\t"
      "v_add_f32_e32 v229, v229, v249\n\t"
      "v_add_f32_e32 v237, v237, v241\n\t"
      "v_add_f32_e32 v228, v228, v236\n\t"
      "v_mfma_f32_16x16x32_bf16 v[208:211], v[116:119], v[188:191], v[208:211]\n\t"
      "v_add_f32_e32 v229, v229, v237\n\t"
      "v_mul_f32_e32 v230, v228, v228\n\t"
      "v_mul_f32_e32 v231, v229, v229\n\t"
      "v_mfma_f32_16x16x32_bf16 v[212:215], v[8:11], v[160:163], 0\n\t"
      "v_fmaak_f32 v230, v252, v230, 0xc0135761\n\t"
      "v_fmaak_f32 v231, v252, v231, 0xc0135761\n\t"
      "v_mul_f32_e32 v230, v228, v230\n\t"
      "v_mfma_f32_16x16x32_bf16 v[212:215], v[24:27], v[164:167], v[212:215]\n\t"
      "v_mul_f32_e32 v231, v229, v231\n\t"
      "v_exp_f32_e32 v230, v230\n\t"
      "v_exp_f32_e32 v231, v231\n\t"
      "v_mfma_f32_16x16x32_bf16 v[212:215], v[40:43], v[168:171], v[212:215]\n\t"
      "v_add_f32_e32 v230, 1.0, v230\n\t"
      "v_add_f32_e32 v231, 1.0, v231\n\t"
      "v_mfma_f32_16x16x32_bf16 v[212:215], v[56:59], v[172:175], v[212:215]\n\t"
      "v_rcp_f32_e32 v230, v230\n\t"
      "v_rcp_f32_e32 v231, v231\n\t"
      "v_mul_f32_e32 v228, v228, v230\n\t"
      "v_mfma_f32_16x16x32_bf16 v[212:215], v[72:75], v[176:179], v[212:215]\n\t"
      "v_mul_f32_e32 v229, v229, v231\n\t"
      "v_cvt_pk_bf16_f32 v230, v228, v229\n\t"
      "v_min_u32_e32 v254, -2, v230\n\t"
      "v_mfma_f32_16x16x32_bf16 v[212:215], v[88:91], v[180:183], v[212:215]\n\t"
      "global_store_dword v247, v254, s[62:63] sc1\n\t"
      "s_add_u32 s62, s62, 57344\n\t"
      "s_addc_u32 s63, s63, 0\n\t"
      "s_setprio 0\n\t"
      "ds_write_b128 v253, v[208:211] offset:1024\n\t"
      "v_mfma_f32_16x16x32_bf16 v[212:215], v[104:107], v[184:187], v[212:215]\n\t"
      "v_mfma_f32_16x16x32_bf16 v[212:215], v[120:123], v[188:191], v[212:215]\n\t"
      "v_mfma_f32_16x16x32_bf16 v[216:219], v[12:15], v[160:163], 0\n\t"
      "v_mfma_f32_16x16x32_bf16 v[216:219], v[28:31], v[164:167], v[216:219]\n\t"
      "v_mfma_f32_16x16x32_bf16 v[216:219], v[44:47], v[168:171], v[216:219]\n\t"
      "v_mfma_f32_16x16x32_bf16 v[216:219], v[60:63], v[172:175], v[216:219]\n\t"
      "v_mfma_f32_16x16x32_bf16 v[216:219], v[76:79], v[176:179], v[216:219]\n\t"
      "v_mfma_f32_16x16x32_bf16 v[216:219], v[92:95], v[180:183], v[216:219]\n\t"
      "s_nop 1\n\t"
      "ds_write_b128 v245, v[212:215] offset:2048\n\t"
      "v_mfma_f32_16x16x32_bf16 v[216:219], v[108:111], v[184:187], v[216:219]\n\t"
      "v_mfma_f32_16x16x32_bf16 v[216:219], v[124:127], v[188:191], v[216:219]\n\t"
      "s_nop 7\n\t"
      "ds_write_b128 v253, v[216:219] offset:3072\n\t"
      "s_waitcnt lgkmcnt(0)\n\t"
      "s_barrier\n\t"
      "ds_read_b64 v[228:229], v246\n\t"
      "ds_read_b64 v[230:231], v246 offset:4096\n\t"
      "ds_read_b64 v[232:233], v246 offset:8192\n\t"
      "ds_read_b64 v[234:235], v246 offset:12288\n\t"
      "ds_read_b64 v[236:237], v246 offset:16384\n\t"
      "ds_read_b64 v[238:239], v246 offset:20480\n\t"
      "ds_read_b64 v[240:241], v246 offset:24576\n\t"
      "ds_read_b64 v[242:243], v246 offset:28672\n\t"
      "s_waitcnt lgkmcnt(4)\n\t"
      "v_add_f32_e32 v228, v228, v230\n\t"
      "v_add_f32_e32 v232, v232, v234\n\t"
      "v_add_f32_e32 v229, v229, v231\n\t"
      "v_add_f32_e32 v233, v233, v235\n\t"
      "v_add_f32_e32 v228, v228, v232\n\t"
      "v_add_f32_e32 v229, v229, v233\n\t"
      "s_waitcnt lgkmcnt(0)\n\t"
      "v_add_f32_e32 v236, v236, v238\n\t"
      "v_add_f32_e32 v240, v240, v242\n\t"
      "v_add_f32_e32 v237, v237, v239\n\t"
      "v_add_f32_e32 v241, v241, v243\n\t"
      "v_add_f32_e32 v228, v228, v248\n\t"
      "v_add_f32_e32 v236, v236, v240\n\t"
      "v_add_f32_e32 v229, v229, v249\n\t"
      "v_add_f32_e32 v237, v237, v241\n\t"
      "v_add_f32_e32 v228, v228, v236\n\t"
      "v_add_f32_e32 v229, v229, v237\n\t"
      "v_mul_f32_e32 v230, v228, v228\n\t"
      "v_mul_f32_e32 v231, v229, v229\n\t"
      "v_fmaak_f32 v230, v252, v230, 0xc0135761\n\t"
      "v_fmaak_f32 v231, v252, v231, 0xc0135761\n\t"
      "v_mul_f32_e32 v230, v228, v230\n\t"
      "v_mul_f32_e32 v231, v229, v231\n\t"
      "v_exp_f32_e32 v230, v230\n\t"
      "v_exp_f32_e32 v231, v231\n\t"
      "v_add_f32_e32 v230, 1.0, v230\n\t"
      "v_add_f32_e32 v231, 1.0, v231\n\t"
      "v_rcp_f32_e32 v230, v230\n\t"
      "v_rcp_f32_e32 v231, v231\n\t"
      "v_mul_f32_e32 v228, v228, v230\n\t"
      "v_mul_f32_e32 v229, v229, v231\n\t"
      "v_cvt_pk_bf16_f32 v230, v228, v229\n\t"
      "v_min_u32_e32 v254, -2, v230\n\t"
      "global_store_dword v247, v254, s[62:63] sc1\n\t"
      "s_add_u32 s62, s62, 57344\n\t"
      "s_addc_u32 s63, s63, 0\n\t"
      "s_or_b32 s34, s2, s24\n\t"
      "s_cbranch_scc1 EPB_%=\n\t"
      "v_mov_b32_e32 v228, s22\n\t"
      "v_mov_b32_e32 v229, s23\n\t"
      "v_mov_b32_e32 v230, 0\n\t"
      "s_mov_b64 exec, 1\n\t"
      "global_store_dwordx2 v230, v[228:229], s[60:61] sc1\n\t"
      "s_mov_b64 exec, -1\n\t"
      "EPB_%=:\n\t"
      "s_endpgm\n\t"
      "LPATH_%=:\n\t"
      "s_add_u32 s22, s22, 1\n\t"
      "s_addc_u32 s23, s23, 0\n\t"
      "s_add_u32 s72, s26, 1\n\t"
      "s_and_b32 s72, s72, 6\n\t"
      "s_cselect_b32 s72, 0, 2\n\t"
      "s_or_b32 s27, s27, s72\n\t"
      "s_mul_i32 s72, s81, 172032\n\t"
      "s_add_u32 s72, s72, s37\n\t"
      "s_add_u32 s40, s6, s72\n\t"
      "s_addc_u32 s41, s7, 0\n\t"
      "s_add_u32 s81, s81, 1\n\t"
      "s_cmp_eq_u32 s81, 7\n\t"
      "s_cselect_b32 s81, 0, s81\n\t"
      "s_add_u32 m0, s39, 81920\n\t"
      "s_nop 0\n\t"
      "global_load_lds_dwordx4 v148, s[40:41]\n\t"
      "global_load_lds_dwordx4 v149, s[40:41] offset:1024\n\t"
      "global_load_lds_dwordx4 v150, s[40:41] offset:2048\n\t"
      "global_load_lds_dwordx4 v151, s[40:41] offset:3072\n\t"
      "s_mul_i32 s72, s81, 172032\n\t"
      "s_add_u32 s72, s72, s37\n\t"
      "s_add_u32 s40, s6, s72\n\t"
      "s_addc_u32 s41, s7, 0\n\t"
      "s_add_u32 s81, s81, 1\n\t"
      "s_cmp_eq_u32 s81, 7\n\t"
      "s_cselect_b32 s81, 0, s81\n\t"
      "s_add_u32 m0, s39, 98304\n\t"
      "s_nop 0\n\t"
      "global_load_lds_dwordx4 v148, s[40:41]\n\t"
      "global_load_lds_dwordx4 v149, s[40:41] offset:1024\n\t"
      "s_waitcnt vmcnt(14)\n\t"
      "s_barrier\n\t"
      "v_add_u32_e32 v172, 0x10000, v224\n\t"
      "v_add_u32_e32 v173, 0x10000, v225\n\t"
      "v_add_u32_e32 v174, 0x10000, v226\n\t"
      "v_add_u32_e32 v175, 0x10000, v227\n\t"
      "s_lshl_b32 s34, s25, 8\n\t"
      "s_add_u32 s34, s34, 4096\n\t"
      "v_and_b32_e32 v136, 1, v192\n\t"
      "v_lshl_add_u32 v136, v136, 7, s34\n\t"
      "s_lshl_b32 s34, s25, 7\n\t"
      "s_add_u32 s34, s34, 16384\n\t"
      "v_mov_b32_e32 v137, s34\n\t"
      "s_lshl_b32 s34, s2, 7\n\t"
      "s_add_u32 s34, s34, 32768\n\t"
      "v_mov_b32_e32 v143, s34\n\t"
      "s_bitcmp0_b32 s27, 1\n\t"
      "s_cbranch_scc1 SK6_%=\n\t"
      "s_lshr_b32 s73, s26, 2\n\t"
      "s_lshl1_add_u32 s73, s25, s73\n\t"
      "s_lshl_b32 s72, s73, 7\n\t"
      "s_add_u32 s72, s72, 4096\n\t"
      "v_mov_b32_e32 v130, s72\n\t"
      "s_lshl2_add_u32 s75, s73, s24\n\t"
      "v_mov_b32_e32 v128, s22\n\t"
      "v_mov_b32_e32 v129, s23\n\t"
      "v_lshrrev_b32_e32 v140, 1, v244\n\t"
      "s_lshl_b32 s72, s75, 9\n\t"
      "s_add_u32 s72, s72, 12582912\n\t"
      "v_mov_b32_e32 v132, -1\n\t"
      "v_mov_b32_e32 v133, -1\n\t"
      "v_mov_b32_e32 v134, -1\n\t"
      "v_mov_b32_e32 v135, -1\n\t"
      "s_add_u32 s42, s20, s72\n\t"
      "s_addc_u32 s43, s21, 0\n\t"
      "global_store_dwordx2 v140, v[132:133], s[42:43] sc1\n\t"
      "s_lshl_b32 s72, s75, 13\n\t"
      "s_add_u32 s72, s72, 4096\n\t"
      "s_add_u32 s42, s20, s72\n\t"
      "s_addc_u32 s43, s21, 0\n\t"
      "global_store_dwordx4 v244, v[132:135], s[42:43] offset:-4096 sc1\n\t"
      "global_store_dwordx4 v244, v[132:135], s[42:43] offset:-3072 sc1\n\t"
      "global_store_dwordx4 v244, v[132:135], s[42:43] offset:-2048 sc1\n\t"
      "SK6_%=:\n\t"
      "s_waitcnt vmcnt(10)\n\t"
      "s_barrier\n\t"
      "global_load_lds_dwordx4 v150, s[40:41] offset:2048\n\t"
      "global_load_lds_dwordx4 v151, s[40:41] offset:3072\n\t"
      "s_bitcmp0_b32 s27, 1\n\t"
      "s_cbranch_scc1 SK7_%=\n\t"
      "global_store_dwordx4 v244, v[132:135], s[42:43] offset:-1024 sc1\n\t"
      "global_store_dwordx4 v244, v[132:135], s[42:43] sc1\n\t"
      "global_store_dwordx4 v244, v[132:135], s[42:43] offset:1024 sc1\n\t"
      "SK7_%=:\n\t"
      "s_waitcnt vmcnt(8)\n\t"
      "s_barrier\n\t"
      "s_mul_i32 s72, s81, 172032\n\t"
      "s_add_u32 s72, s72, s37\n\t"
      "s_add_u32 s40, s6, s72\n\t"
      "s_addc_u32 s41, s7, 0\n\t"
      "s_add_u32 s81, s81, 1\n\t"
      "s_cmp_eq_u32 s81, 7\n\t"
      "s_cselect_b32 s81, 0, s81\n\t"
      "s_add_u32 m0, s39, 114688\n\t"
      "s_nop 0\n\t"
      "global_load_lds_dwordx4 v148, s[40:41]\n\t"
      "global_load_lds_dwordx4 v149, s[40:41] offset:1024\n\t"
      "global_load_lds_dwordx4 v150, s[40:41] offset:2048\n\t"
      "global_load_lds_dwordx4 v151, s[40:41] offset:3072\n\t"
      "s_bitcmp0_b32 s27, 1\n\t"
      "s_cbranch_scc1 SK8_%=\n\t"
      "global_store_dwordx4 v244, v[132:135], s[42:43] offset:2048 sc1\n\t"
      "global_store_dwordx4 v244, v[132:135], s[42:43] offset:3072 sc1\n\t"
      "SK8_%=:\n\t"
      "s_waitcnt vmcnt(8)\n\t"
      "s_barrier\n\t"
      "s_mul_i32 s72, s81, 172032\n\t"
      "s_add_u32 s72, s72, s37\n\t"
      "s_add_u32 s40, s6, s72\n\t"
      "s_addc_u32 s41, s7, 0\n\t"
      "s_add_u32 s81, s81, 1\n\t"
      "s_cmp_eq_u32 s81, 7\n\t"
      "s_cselect_b32 s81, 0, s81\n\t"
      "s_add_u32 m0, s39, 131072\n\t"
      "s_nop 0\n\t"
      "global_load_lds_dwordx4 v148, s[40:41]\n\t"
      "global_load_lds_dwordx4 v149, s[40:41] offset:1024\n\t"
      "global_load_lds_dwordx4 v150, s[40:41] offset:2048\n\t"
      "global_load_lds_dwordx4 v151, s[40:41] offset:3072\n\t"
      "s_waitcnt vmcnt(8)\n\t"
      "s_bitcmp0_b32 s27, 1\n\t"
      "s_cbranch_scc1 SK9_%=\n\t"
      "s_waitcnt vmcnt(4)\n\t"
      "SK9_%=:\n\t"
      "s_barrier\n\t"
      "s_mul_i32 s72, s81, 172032\n\t"
      "s_add_u32 s72, s72, s37\n\t"
      "s_add_u32 s40, s6, s72\n\t"
      "s_addc_u32 s41, s7, 0\n\t"
      "s_add_u32 s81, s81, 1\n\t"
      "s_cmp_eq_u32 s81, 7\n\t"
      "s_cselect_b32 s81, 0, s81\n\t"
      "s_add_u32 m0, s39, 147456\n\t"
      "s_nop 0\n\t"
      "global_load_lds_dwordx4 v148, s[40:41]\n\t"
      "global_load_lds_dwordx4 v149, s[40:41] offset:1024\n\t"
      "global_load_lds_dwordx4 v150, s[40:41] offset:2048\n\t"
      "global_load_lds_dwordx4 v151, s[40:41] offset:3072\n\t"
      "s_bitcmp0_b32 s27, 1\n\t"
      "s_cbranch_scc1 SK10_%=\n\t"
      "s_cmp_eq_u32 s24, 0\n\t"
      "s_cselect_b64 exec, 1, 0\n\t"
      "global_store_dwordx2 v130, v[128:129], s[60:61] sc1\n\t"
      "s_mov_b64 exec, -1\n\t"
      "s_mov_b32 m0, 64512\n\t"
      "s_mul_i32 s72, s75, 13\n\t"
      "v_min_u32_e32 v141, 12, v192\n\t"
      "v_add_u32_e32 v141, s72, v141\n\t"
      "v_min_u32_e32 v141, 3135, v141\n\t"
      "v_lshlrev_b32_e32 v141, 7, v141\n\t"
      "global_load_lds_dword v141, s[8:9]\n\t"
      "SK10_%=:\n\t"
      "s_waitcnt vmcnt(8)\n\t"
      "s_barrier\n\t"
      "s_mul_i32 s72, s82, 172032\n\t"
      "s_add_u32 s72, s72, s37\n\t"
      "s_add_u32 s72, s72, 1792\n\t"
      "s_add_u32 s40, s6, s72\n\t"
      "s_addc_u32 s41, s7, 0\n\t"
      "s_add_u32 s82, s82, 1\n\t"
      "s_cmp_eq_u32 s82, 7\n\t"
      "s_cselect_b32 s82, 0, s82\n\t"
      "s_add_u32 m0, s39, 65536\n\t"
      "s_nop 0\n\t"
      "global_load_lds_dwordx4 v148, s[40:41]\n\t"
      "global_load_lds_dwordx4 v152, s[40:41] offset:1024\n\t"
      "s_mul_i32 s72, s82, 172032\n\t"
      "s_add_u32 s72, s72, s37\n\t"
      "s_add_u32 s72, s72, 1792\n\t"
      "s_add_u32 s40, s6, s72\n\t"
      "s_addc_u32 s41, s7, 0\n\t"
      "s_add_u32 s82, s82, 1\n\t"
      "s_cmp_eq_u32 s82, 7\n\t"
      "s_cselect_b32 s82, 0, s82\n\t"
      "s_add_u32 m0, s39, 67584\n\t"
      "s_nop 0\n\t"
      "global_load_lds_dwordx4 v148, s[40:41]\n\t"
      "global_load_lds_dwordx4 v152, s[40:41] offset:1024\n\t"
      "s_waitcnt vmcnt(8)\n\t"
      "s_barrier\n\t"
      "s_mul_i32 s72, s82, 172032\n\t"
      "s_add_u32 s72, s72, s37\n\t"
      "s_add_u32 s72, s72, 1792\n\t"
      "s_add_u32 s40, s6, s72\n\t"
      "s_addc_u32 s41, s7, 0\n\t"
      "s_add_u32 s82, s82, 1\n\t"
      "s_cmp_eq_u32 s82, 7\n\t"
      "s_cselect_b32 s82, 0, s82\n\t"
      "s_add_u32 m0, s39, 81920\n\t"
      "s_nop 0\n\t"
      "global_load_lds_dwordx4 v148, s[40:41]\n\t"
      "global_load_lds_dwordx4 v152, s[40:41] offset:1024\n\t"
      "s_mul_i32 s72, s82, 172032\n\t"
      "s_add_u32 s72, s72, s37\n\t"
      "s_add_u32 s72, s72, 1792\n\t"
      "s_add_u32 s40, s6, s72\n\t"
      "s_addc_u32 s41, s7, 0\n\t"
      "s_add_u32 s82, s82, 1\n\t"
      "s_cmp_eq_u32 s82, 7\n\t"
      "s_cselect_b32 s82, 0, s82\n\t"
      "s_add_u32 m0, s39, 83968\n\t"
      "s_nop 0\n\t"
      "global_load_lds_dwordx4 v148, s[40:41]\n\t"
      "global_load_lds_dwordx4 v152, s[40:41] offset:1024\n\t"
      "s_waitcnt vmcnt(8)\n\t"
      "s_barrier\n\t"
      "s_cmp_lg_u32 s24, 3\n\t"
      "s_cbranch_scc1 SK11_%=\n\t"
      "global_load_dwordx2 v[138:139], v136, s[60:61] sc1\n\t"
      "SK11_%=:\n\t"
      "s_mul_i32 s72, s82, 172032\n\t"
      "s_add_u32 s72, s72, s37\n\t"
      "s_add_u32 s72, s72, 1792\n\t"
      "s_add_u32 s40, s6, s72\n\t"
      "s_addc_u32 s41, s7, 0\n\t"
      "s_add_u32 s82, s82, 1\n\t"
      "s_cmp_eq_u32 s82, 7\n\t"
      "s_cselect_b32 s82, 0, s82\n\t"
      "s_lshr_b32 s72, s39, 1\n\t"
      "s_add_u32 m0, s72, 98304\n\t"
      "s_nop 0\n\t"
      "global_load_lds_dwordx4 v148, s[40:41]\n\t"
      "global_load_lds_dwordx4 v152, s[40:41] offset:1024\n\t"
      "global_load_dword v249, v248, s[12:13]\n\t"
      "s_lshl2_add_u32 s72, s31, s24\n\t"
      "s_and_b32 s73, s72, 0x70\n\t"
      "s_add_u32 s72, s72, s73\n\t"
      "s_mov_b32 m0, 64512\n\t"
      "s_mul_i32 s72, s72, 28672\n\t"
      "s_and_b32 s73, s2, 7\n\t"
      "s_mul_i32 s73, s73, 448\n\t"
      "s_and_b32 s73, s73, 0xffffff80\n\t"
      "s_add_u32 s72, s72, s73\n\t"
      "v_bfe_u32 v141, v192, 2, 3\n\t"
      "s_cmp_ge_u32 s31, 28\n\t"
      "s_cselect_b32 s73, 32, 0\n\t"
      "v_and_b32_e32 v142, s73, v192\n\t"
      "v_lshl_or_b32 v141, v142, 2, v141\n\t"
      "v_mul_u32_u24_e32 v141, 3584, v141\n\t"
      "v_lshl_add_u32 v141, v196, 7, v141\n\t"
      "v_add_u32_e32 v141, s72, v141\n\t"
      "global_load_lds_dword v141, s[10:11]\n\t"
      "s_waitcnt vmcnt(8)\n\t"
      "s_barrier\n\t"
      "s_mul_i32 s72, s24, 14336\n\t"
      "s_add_u32 s40, s56, s72\n\t"
      "s_addc_u32 s41, s57, 0\n\t"
      "s_lshr_b32 s72, s39, 1\n\t"
      "s_add_u32 m0, s72, 106496\n\t"
      "s_nop 0\n\t"
      "global_load_lds_dwordx4 v154, s[40:41]\n\t"
      "global_load_lds_dwordx4 v155, s[40:41] offset:1024\n\t"
      "s_waitcnt vmcnt(6)\n\t"
      "s_barrier\n\t"
      "s_mul_i32 s72, s24, 28672\n\t"
      "s_add_u32 s72, s72, 57344\n\t"
      "s_add_u32 s40, s56, s72\n\t"
      "s_addc_u32 s41, s57, 0\n\t"
      "s_lshr_b32 s72, s39, 0\n\t"
      "s_add_u32 m0, s72, 114688\n\t"
      "s_nop 0\n\t"
      "global_load_lds_dwordx4 v154, s[40:41]\n\t"
      "global_load_lds_dwordx4 v155, s[40:41] offset:1024\n\t"
      "global_load_lds_dwordx4 v156, s[40:41] offset:2048\n\t"
      "global_load_lds_dwordx4 v157, s[40:41] offset:3072\n\t"
      "s_waitcnt vmcnt(8)\n\t"
      "s_cmp_lg_u32 s24, 3\n\t"
      "s_cbranch_scc1 LF1B_%=\n\t"
      "v_cmp_ne_u64_e32 vcc, s[22:23], v[138:139]\n\t"
      "s_cbranch_vccnz LF1R_%=\n\t"
      "LF1B_%=:\n\t"
      "s_barrier\n\t"
      "s_mul_i32 s72, s24, 28672\n\t"
      "s_add_u32 s72, s72, 172032\n\t"
      "s_add_u32 s40, s56, s72\n\t"
      "s_addc_u32 s41, s57, 0\n\t"
      "s_lshr_b32 s72, s39, 0\n\t"
      "s_add_u32 m0, s72, 131072\n\t"
      "s_nop 0\n\t"
      "global_load_lds_dwordx4 v154, s[40:41]\n\t"
      "global_load_lds_dwordx4 v155, s[40:41] offset:1024\n\t"
      "global_load_lds_dwordx4 v156, s[40:41] offset:2048\n\t"
      "global_load_lds_dwordx4 v157, s[40:41] offset:3072\n\t"
      "s_mul_i32 s72, s24, 28672\n\t"
      "s_add_u32 s72, s72, 286720\n\t"
      "s_add_u32 s40, s56, s72\n\t"
      "s_addc_u32 s41, s57, 0\n\t"
      "s_lshr_b32 s72, s39, 0\n\t"
      "s_add_u32 m0, s72, 147456\n\t"
      "s_nop 0\n\t"
      "global_load_lds_dwordx4 v154, s[40:41]\n\t"
      "global_load_lds_dwordx4 v155, s[40:41] offset:1024\n\t"
      "global_load_lds_dwordx4 v156, s[40:41] offset:2048\n\t"
      "global_load_lds_dwordx4 v157, s[40:41] offset:3072\n\t"
      "s_barrier\n\t"
      "s_cmp_eq_u32 s26, 7\n\t"
      "s_cbranch_scc1 SPARE_%=\n\t"
      "s_lshl_b32 s72, s24, 10\n\t"
      "s_add_u32 s72, s72, 98304\n\t"
      "v_xor_b32_e32 v203, v197, v198\n\t"
      "v_lshl_add_u32 v203, v203, 4, s72\n\t"
      "v_lshl_add_u32 v203, v197, 7, v203\n\t"
      "ds_write_b128 v203, v[204:207]\n\t"
      "s_waitcnt lgkmcnt(0)\n\t"
      "s_bitcmp0_b32 s27, 0\n\t"
      "s_cbranch_scc1 L4_%=\n\t"
      "s_add_u32 s72, s76, 0\n\t"
      "s_add_u32 s40, s52, s72\n\t"
      "s_addc_u32 s41, s53, 0\n\t"
      "s_add_u32 m0, s39, 65536\n\t"
      "s_nop 0\n\t"
      "global_load_lds_dwordx4 v144, s[40:41]\n\t"
      "global_load_lds_dwordx4 v145, s[40:41] offset:1024\n\t"
      "global_load_lds_dwordx4 v146, s[40:41] offset:2048\n\t"
      "global_load_lds_dwordx4 v147, s[40:41] offset:3072\n\t"
      "s_add_u32 s72, s76, 229376\n\t"
      "s_add_u32 s40, s52, s72\n\t"
      "s_addc_u32 s41, s53, 0\n\t"
      "s_add_u32 m0, s39, 81920\n\t"
      "s_nop 0\n\t"
      "global_load_lds_dwordx4 v144, s[40:41]\n\t"
      "global_load_lds_dwordx4 v145, s[40:41] offset:1024\n\t"
      "global_load_lds_dwordx4 v146, s[40:41] offset:2048\n\t"
      "global_load_lds_dwordx4 v147, s[40:41] offset:3072\n\t"
      "s_waitcnt vmcnt(8)\n\t"
      "s_barrier\n\t"
      "s_barrier\n\t"
      "s_add_u32 s72, s76, 3670016\n\t"
      "s_add_u32 s40, s52, s72\n\t"
      "s_addc_u32 s41, s53, 0\n\t"
      "s_add_u32 m0, s39, 16384\n\t"
      "s_nop 0\n\t"
      "global_load_lds_dwordx4 v144, s[40:41]\n\t"
      "global_load_lds_dwordx4 v145, s[40:41] offset:1024\n\t"
      "global_load_lds_dwordx4 v146, s[40:41] offset:2048\n\t"
      "global_load_lds_dwordx4 v147, s[40:41] offset:3072\n\t"
      "s_add_u32 s72, s76, 3899392\n\t"
      "s_add_u32 s40, s52, s72\n\t"
      "s_addc_u32 s41, s53, 0\n\t"
      "s_add_u32 m0, s39, 49152\n\t"
      "s_nop 0\n\t"
      "global_load_lds_dwordx4 v144, s[40:41]\n\t"
      "global_load_lds_dwordx4 v145, s[40:41] offset:1024\n\t"
      "global_load_lds_dwordx4 v146, s[40:41] offset:2048\n\t"
      "global_load_lds_dwordx4 v147, s[40:41] offset:3072\n\t"
      "s_waitcnt vmcnt(12)\n\t"
      "ds_read_b64_tr_b16 v[0:1], v172\n\t"
      "ds_read_b64_tr_b16 v[2:3], v172 offset:512\n\t"
      "ds_read_b64_tr_b16 v[4:5], v173\n\t"
      "ds_read_b64_tr_b16 v[6:7], v173 offset:512\n\t"
      "ds_read_b64_tr_b16 v[8:9], v174\n\t"
      "ds_read_b64_tr_b16 v[10:11], v174 offset:512\n\t"
      "ds_read_b64_tr_b16 v[12:13], v175\n\t"
      "ds_read_b64_tr_b16 v[14:15], v175 offset:512\n\t"
      "s_waitcnt lgkmcnt(0)\n\t"
      "s_add_u32 s72, s77, 0\n\t"
      "s_add_u32 s40, s52, s72\n\t"
      "s_addc_u32 s41, s53, 0\n\t"
      "s_add_u32 m0, s39, 65536\n\t"
      "s_nop 0\n\t"
      "global_load_lds_dwordx4 v144, s[40:41]\n\t"
      "global_load_lds_dwordx4 v145, s[40:41] offset:1024\n\t"
      "global_load_lds_dwordx4 v146, s[40:41] offset:2048\n\t"
      "global_load_lds_dwordx4 v147, s[40:41] offset:3072\n\t"
      "s_waitcnt vmcnt(12)\n\t"
      "ds_read_b64_tr_b16 v[16:17], v172 offset:16384\n\t"
      "ds_read_b64_tr_b16 v[18:19], v172 offset:16896\n\t"
      "ds_read_b64_tr_b16 v[20:21], v173 offset:16384\n\t"
      "ds_read_b64_tr_b16 v[22:23], v173 offset:16896\n\t"
      "ds_read_b64_tr_b16 v[24:25], v174 offset:16384\n\t"
      "ds_read_b64_tr_b16 v[26:27], v174 offset:16896\n\t"
      "ds_read_b64_tr_b16 v[28:29], v175 offset:16384\n\t"
      "ds_read_b64_tr_b16 v[30:31], v175 offset:16896\n\t"
      "s_waitcnt lgkmcnt(0)\n\t"
      "s_add_u32 s72, s77, 229376\n\t"
      "s_add_u32 s40, s52, s72\n\t"
      "s_addc_u32 s41, s53, 0\n\t"
      "s_add_u32 m0, s39, 81920\n\t"
      "s_nop 0\n\t"
      "global_load_lds_dwordx4 v144, s[40:41]\n\t"
      "global_load_lds_dwordx4 v145, s[40:41] offset:1024\n\t"
      "global_load_lds_dwordx4 v146, s[40:41] offset:2048\n\t"
      "global_load_lds_dwordx4 v147, s[40:41] offset:3072\n\t"
      "s_add_u32 s72, s77, 3670016\n\t"
      "s_add_u32 s40, s52, s72\n\t"
      "s_addc_u32 s41, s53, 0\n\t"
      "s_add_u32 m0, s39, 0\n\t"
      "s_nop 0\n\t"
      "global_load_lds_dwordx4 v144, s[40:41]\n\t"
      "global_load_lds_dwordx4 v145, s[40:41] offset:1024\n\t"
      "global_load_lds_dwordx4 v146, s[40:41] offset:2048\n\t"
      "global_load_lds_dwordx4 v147, s[40:41] offset:3072\n\t"
      "s_add_u32 s72, s77, 3899392\n\t"
      "s_add_u32 s40, s52, s72\n\t"
      "s_addc_u32 s41, s53, 0\n\t"
      "s_add_u32 m0, s39, 32768\n\t"
      "s_nop 0\n\t"
      "global_load_lds_dwordx4 v144, s[40:41]\n\t"
      "global_load_lds_dwordx4 v145, s[40:41] offset:1024\n\t"
      "global_load_lds_dwordx4 v146, s[40:41] offset:2048\n\t"
      "global_load_lds_dwordx4 v147, s[40:41] offset:3072\n\t"
      "s_waitcnt vmcnt(12)\n\t"
      "ds_read_b64_tr_b16 v[32:33], v172\n\t"
      "ds_read_b64_tr_b16 v[34:35], v172 offset:512\n\t"
      "ds_read_b64_tr_b16 v[36:37], v173\n\t"
      "ds_read_b64_tr_b16 v[38:39], v173 offset:512\n\t"
      "ds_read_b64_tr_b16 v[40:41], v174\n\t"
      "ds_read_b64_tr_b16 v[42:43], v174 offset:512\n\t"
      "ds_read_b64_tr_b16 v[44:45], v175\n\t"
      "ds_read_b64_tr_b16 v[46:47], v175 offset:512\n\t"
      "s_waitcnt lgkmcnt(0)\n\t"
      "s_add_u32 s72, s78, 0\n\t"
      "s_add_u32 s40, s52, s72\n\t"
      "s_addc_u32 s41, s53, 0\n\t"
      "s_add_u32 m0, s39, 65536\n\t"
      "s_nop 0\n\t"
      "global_load_lds_dwordx4 v144, s[40:41]\n\t"
      "global_load_lds_dwordx4 v145, s[40:41] offset:1024\n\t"
      "global_load_lds_dwordx4 v146, s[40:41] offset:2048\n\t"
      "global_load_lds_dwordx4 v147, s[40:41] offset:3072\n\t"
      "s_waitcnt vmcnt(12)\n\t"
      "ds_read_b64_tr_b16 v[48:49], v172 offset:16384\n\t"
      "ds_read_b64_tr_b16 v[50:51], v172 offset:16896\n\t"
      "ds_read_b64_tr_b16 v[52:53], v173 offset:16384\n\t"
      "ds_read_b64_tr_b16 v[54:55], v173 offset:16896\n\t"
      "ds_read_b64_tr_b16 v[56:57], v174 offset:16384\n\t"
      "ds_read_b64_tr_b16 v[58:59], v174 offset:16896\n\t"
      "ds_read_b64_tr_b16 v[60:61], v175 offset:16384\n\t"
      "ds_read_b64_tr_b16 v[62:63], v175 offset:16896\n\t"
      "s_waitcnt lgkmcnt(0)\n\t"
      "s_add_u32 s72, s78, 229376\n\t"
      "s_add_u32 s40, s52, s72\n\t"
      "s_addc_u32 s41, s53, 0\n\t"
      "s_add_u32 m0, s39, 81920\n\t"
      "s_nop 0\n\t"
      "global_load_lds_dwordx4 v144, s[40:41]\n\t"
      "global_load_lds_dwordx4 v145, s[40:41] offset:1024\n\t"
      "global_load_lds_dwordx4 v146, s[40:41] offset:2048\n\t"
      "global_load_lds_dwordx4 v147, s[40:41] offset:3072\n\t"
      "s_waitcnt vmcnt(8)\n\t"
      "s_barrier\n\t"
      "s_cmp_lg_u32 s24, 3\n\t"
      "s_cbranch_scc1 SK13_%=\n\t"
      "global_load_dwordx2 v[138:139], v137, s[60:61] sc1\n\t"
      "SK13_%=:\n\t"
      "s_add_u32 s72, s79, 0\n\t"
      "s_add_u32 s40, s52, s72\n\t"
      "s_addc_u32 s41, s53, 0\n\t"
      "s_add_u32 m0, s39, 98304\n\t"
      "s_nop 0\n\t"
      "global_load_lds_dwordx4 v144, s[40:41]\n\t"
      "global_load_lds_dwordx4 v145, s[40:41] offset:1024\n\t"
      "global_load_lds_dwordx4 v146, s[40:41] offset:2048\n\t"
      "global_load_lds_dwordx4 v147, s[40:41] offset:3072\n\t"
      "s_add_u32 s72, s79, 229376\n\t"
      "s_add_u32 s40, s52, s72\n\t"
      "s_addc_u32 s41, s53, 0\n\t"
      "s_add_u32 m0, s39, 114688\n\t"
      "s_nop 0\n\t"
      "global_load_lds_dwordx4 v144, s[40:41]\n\t"
      "global_load_lds_dwordx4 v145, s[40:41] offset:1024\n\t"
      "global_load_lds_dwordx4 v146, s[40:41] offset:2048\n\t"
      "global_load_lds_dwordx4 v147, s[40:41] offset:3072\n\t"
      "s_cmp_lg_u32 s24, 3\n\t"
      "s_cbranch_scc1 LF2B_%=\n\t"
      "s_waitcnt vmcnt(8)\n\t"
      "v_cmp_ne_u64_e32 vcc, s[22:23], v[138:139]\n\t"
      "s_cbranch_vccnz LF2R_%=\n\t"
      "LF2B_%=:\n\t"
      "s_barrier\n\t"
      "s_add_u32 s72, s78, 3670016\n\t"
      "s_add_u32 s40, s52, s72\n\t"
      "s_addc_u32 s41, s53, 0\n\t"
      "s_add_u32 m0, s39, 16384\n\t"
      "s_nop 0\n\t"
      "global_load_lds_dwordx4 v144, s[40:41]\n\t"
      "global_load_lds_dwordx4 v145, s[40:41] offset:1024\n\t"
      "global_load_lds_dwordx4 v146, s[40:41] offset:2048\n\t"
      "global_load_lds_dwordx4 v147, s[40:41] offset:3072\n\t"
      "s_add_u32 s72, s78, 3899392\n\t"
      "s_add_u32 s40, s52, s72\n\t"
      "s_addc_u32 s41, s53, 0\n\t"
      "s_add_u32 m0, s39, 49152\n\t"
      "s_nop 0\n\t"
      "global_load_lds_dwordx4 v144, s[40:41]\n\t"
      "global_load_lds_dwordx4 v145, s[40:41] offset:1024\n\t"
      "global_load_lds_dwordx4 v146, s[40:41] offset:2048\n\t"
      "global_load_lds_dwordx4 v147, s[40:41] offset:3072\n\t"
      "s_add_u32 s72, s79, 3670016\n\t"
      "s_add_u32 s40, s52, s72\n\t"
      "s_addc_u32 s41, s53, 0\n\t"
      "s_add_u32 m0, s39, 0\n\t"
      "s_nop 0\n\t"
      "global_load_lds_dwordx4 v144, s[40:41]\n\t"
      "global_load_lds_dwordx4 v145, s[40:41] offset:1024\n\t"
      "global_load_lds_dwordx4 v146, s[40:41] offset:2048\n\t"
      "global_load_lds_dwordx4 v147, s[40:41] offset:3072\n\t"
      "s_add_u32 s72, s79, 3899392\n\t"
      "s_add_u32 s40, s52, s72\n\t"
      "s_addc_u32 s41, s53, 0\n\t"
      "s_add_u32 m0, s39, 32768\n\t"
      "s_nop 0\n\t"
      "global_load_lds_dwordx4 v144, s[40:41]\n\t"
      "global_load_lds_dwordx4 v145, s[40:41] offset:1024\n\t"
      "global_load_lds_dwordx4 v146, s[40:41] offset:2048\n\t"
      "global_load_lds_dwordx4 v147, s[40:41] offset:3072\n\t"
      "s_cmp_lg_u32 s24, 3\n\t"
      "s_cbranch_scc1 SK14_%=\n\t"
      "global_load_dwordx2 v[138:139], v143, s[60:61] sc1\n\t"
      "SK14_%=:\n\t"
      "s_waitcnt vmcnt(28)\n\t"
      "ds_read_b64_tr_b16 v[64:65], v172\n\t"
      "ds_read_b64_tr_b16 v[66:67], v172 offset:512\n\t"
      "ds_read_b64_tr_b16 v[68:69], v173\n\t"
      "ds_read_b64_tr_b16 v[70:71], v173 offset:512\n\t"
      "ds_read_b64_tr_b16 v[72:73], v174\n\t"
      "ds_read_b64_tr_b16 v[74:75], v174 offset:512\n\t"
      "ds_read_b64_tr_b16 v[76:77], v175\n\t"
      "ds_read_b64_tr_b16 v[78:79], v175 offset:512\n\t"
      "s_waitcnt vmcnt(24)\n\t"
      "ds_read_b64_tr_b16 v[80:81], v172 offset:16384\n\t"
      "ds_read_b64_tr_b16 v[82:83], v172 offset:16896\n\t"
      "ds_read_b64_tr_b16 v[84:85], v173 offset:16384\n\t"
      "ds_read_b64_tr_b16 v[86:87], v173 offset:16896\n\t"
      "ds_read_b64_tr_b16 v[88:89], v174 offset:16384\n\t"
      "ds_read_b64_tr_b16 v[90:91], v174 offset:16896\n\t"
      "ds_read_b64_tr_b16 v[92:93], v175 offset:16384\n\t"
      "ds_read_b64_tr_b16 v[94:95], v175 offset:16896\n\t"
      "s_waitcnt vmcnt(20)\n\t"
      "ds_read_b64_tr_b16 v[96:97], v172 offset:32768\n\t"
      "ds_read_b64_tr_b16 v[98:99], v172 offset:33280\n\t"
      "ds_read_b64_tr_b16 v[100:101], v173 offset:32768\n\t"
      "ds_read_b64_tr_b16 v[102:103], v173 offset:33280\n\t"
      "ds_read_b64_tr_b16 v[104:105], v174 offset:32768\n\t"
      "ds_read_b64_tr_b16 v[106:107], v174 offset:33280\n\t"
      "ds_read_b64_tr_b16 v[108:109], v175 offset:32768\n\t"
      "ds_read_b64_tr_b16 v[110:111], v175 offset:33280\n\t"
      "s_waitcnt vmcnt(16)\n\t"
      "ds_read_b64_tr_b16 v[112:113], v172 offset:49152\n\t"
      "ds_read_b64_tr_b16 v[114:115], v172 offset:49664\n\t"
      "ds_read_b64_tr_b16 v[116:117], v173 offset:49152\n\t"
      "ds_read_b64_tr_b16 v[118:119], v173 offset:49664\n\t"
      "ds_read_b64_tr_b16 v[120:121], v174 offset:49152\n\t"
      "ds_read_b64_tr_b16 v[122:123], v174 offset:49664\n\t"
      "ds_read_b64_tr_b16 v[124:125], v175 offset:49152\n\t"
      "ds_read_b64_tr_b16 v[126:127], v175 offset:49664\n\t"
      "s_waitcnt vmcnt(0)\n\t"
      "s_cmp_lg_u32 s24, 3\n\t"
      "s_cbranch_scc1 LF3B_%=\n\t"
      "s_waitcnt vmcnt(0)\n\t"
      "v_cmp_ne_u64_e32 vcc, s[22:23], v[138:139]\n\t"
      "s_cbranch_vccnz LF3R_%=\n\t"
      "LF3B_%=:\n\t"
      "s_waitcnt lgkmcnt(0)\n\t"
      "s_barrier\n\t"
      "s_mul_i32 s34, s32, 196608\n\t"
      "s_mul_i32 s35, s24, 8192\n\t"
      "s_add_u32 s34, s34, s35\n\t"
      "s_add_u32 s34, s34, 1048576\n\t"
      "s_add_u32 s64, s48, s34\n\t"
      "s_addc_u32 s65, s49, 0\n\t"
      "v_add_u32_e32 v250, 4096, v244\n\t"
      "global_load_dwordx4 v[128:131], v250, s[64:65] offset:-4096 sc1\n\t"
      "global_load_dwordx4 v[132:135], v250, s[64:65] offset:-3072 sc1\n\t"
      "global_load_dwordx4 v[136:139], v250, s[64:65] offset:-2048 sc1\n\t"
      "global_load_dwordx4 v[140:143], v250, s[64:65] offset:-1024 sc1\n\t"
      "global_load_dwordx4 v[144:147], v250, s[64:65] sc1\n\t"
      "global_load_dwordx4 v[148:151], v250, s[64:65] offset:1024 sc1\n\t"
      "global_load_dwordx4 v[152:155], v250, s[64:65] offset:2048 sc1\n\t"
      "global_load_dwordx4 v[156:159], v250, s[64:65] offset:3072 sc1\n\t"
      "s_waitcnt vmcnt(0) lgkmcnt(0)\n\t"
      "s_branch GST_%=\n\t"
      "G4C_%=:\n\t"
      "global_store_dwordx4 v241, v[208:211], s[20:21] offset:-2048 sc1\n\t"
      "global_store_dwordx4 v241, v[212:215], s[20:21] offset:2048 sc1\n\t"
      "s_setprio 0\n\t"
      "G4_%=:\n\t"
      "s_mul_i32 s34, s32, 401408\n\t"
      "s_mul_i32 s35, s24, 7168\n\t"
      "s_add_u32 s34, s34, s35\n\t"
      "s_add_u32 s34, s34, 1835008\n\t"
      "s_add_u32 s64, s48, s34\n\t"
      "s_addc_u32 s65, s49, 0\n\t"
      "s_and_b32 s34, s30, 3\n\t"
      "s_lshl_b32 s34, s34, 11\n\t"
      "s_add_u32 s40, s34, 0\n\t"
      "s_sub_u32 s35, s40, 7168\n\t"
      "s_min_u32 s40, s40, s35\n\t"
      "s_add_u32 s41, s34, 1024\n\t"
      "s_sub_u32 s35, s41, 7168\n\t"
      "s_min_u32 s41, s41, s35\n\t"
      "s_add_u32 s42, s34, 2048\n\t"
      "s_sub_u32 s35, s42, 7168\n\t"
      "s_min_u32 s42, s42, s35\n\t"
      "s_add_u32 s43, s34, 3072\n\t"
      "s_sub_u32 s35, s43, 7168\n\t"
      "s_min_u32 s43, s43, s35\n\t"
      "s_add_u32 s44, s34, 4096\n\t"
      "s_sub_u32 s35, s44, 7168\n\t"
      "s_min_u32 s44, s44, s35\n\t"
      "s_add_u32 s45, s34, 5120\n\t"
      "s_sub_u32 s35, s45, 7168\n\t"
      "s_min_u32 s45, s45, s35\n\t"
      "s_add_u32 s46, s34, 6144\n\t"
      "s_sub_u32 s35, s46, 7168\n\t"
      "s_min_u32 s46, s46, s35\n\t"
      "s_add_u32 s66, s64, 57344\n\t"
      "s_addc_u32 s67, s65, 0\n\t"
      "s_add_u32 s68, s66, 57344\n\t"
      "s_addc_u32 s69, s67, 0\n\t"
      "s_add_u32 s70, s68, 57344\n\t"
      "s_addc_u32 s71, s69, 0\n\t"
      "s_add_u32 s72, s70, 57344\n\t"
      "s_addc_u32 s73, s71, 0\n\t"
      "s_add_u32 s74, s72, 57344\n\t"
      "s_addc_u32 s75, s73, 0\n\t"
      "s_add_u32 s76, s74, 57344\n\t"
      "s_addc_u32 s77, s75, 0\n\t"
      "v_lshrrev_b32_e32 v240, 7, v244\n\t"
      "v_and_b32_e32 v241, 0x70, v244\n\t"
      "v_and_b32_e32 v242, 6, v240\n\t"
      "v_lshlrev_b32_e32 v242, 4, v242\n\t"
      "v_xor_b32_e32 v242, v242, v241\n\t"
      "v_lshl_or_b32 v228, v240, 12, v242\n\t"
      "s_mul_i32 s34, s24, 917504\n\t"
      "s_lshl_b32 s35, s29, 7\n\t"
      "s_add_u32 s34, s34, s35\n\t"
      "s_lshl_b32 s39, s24, 12\n\t"
      "s_add_u32 s54, s14, s34\n\t"
      "s_addc_u32 s55, s15, 0\n\t"
      "v_add_u32_e32 v229, 31744, v228\n\t"
      "v_add_u32_e32 v230, 63488, v228\n\t"
      "v_add_u32_e32 v231, 95232, v228\n\t"
      "s_lshl_b32 s37, s40, 7\n\t"
      "s_add_u32 s58, s54, s37\n\t"
      "s_addc_u32 s59, s55, 0\n\t"
      "s_add_u32 m0, s39, 0\n\t"
      "s_nop 0\n\t"
      "global_load_lds_dwordx4 v228, s[58:59]\n\t"
      "global_load_lds_dwordx4 v229, s[58:59] offset:1024\n\t"
      "global_load_lds_dwordx4 v230, s[58:59] offset:2048\n\t"
      "global_load_lds_dwordx4 v231, s[58:59] offset:3072\n\t"
      "s_lshl_b32 s37, s41, 7\n\t"
      "s_add_u32 s58, s54, s37\n\t"
      "s_addc_u32 s59, s55, 0\n\t"
      "s_add_u32 m0, s39, 32768\n\t"
      "s_nop 0\n\t"
      "global_load_lds_dwordx4 v228, s[58:59]\n\t"
      "global_load_lds_dwordx4 v229, s[58:59] offset:1024\n\t"
      "global_load_lds_dwordx4 v230, s[58:59] offset:2048\n\t"
      "global_load_lds_dwordx4 v231, s[58:59] offset:3072\n\t"
      "s_lshr_b32 s34, s24, 1\n\t"
      "s_lshl2_add_u32 s35, s29, s34\n\t"
      "s_and_b32 s36, s24, 1\n\t"
      "s_lshl4_add_u32 s35, s35, s36\n\t"
      "v_lshl_add_u32 v242, v240, 1, s35\n\t"
      "v_lshlrev_b32_e32 v248, 1, v242\n\t"
      "global_load_ushort v249, v248, s[16:17]\n\t"
      "s_movk_i32 s35, 896\n\t"
      "v_and_b32_e32 v247, 64, v244\n\t"
      "v_lshrrev_b32_e32 v247, 2, v247\n\t"
      "v_mad_u32_u24 v247, v242, s35, v247\n\t"
      "v_and_b32_e32 v242, 0x30, v244\n\t"
      "v_lshl_add_u32 v247, v242, 1, v247\n\t"
      "s_mov_b32 s82, 0x77777777\n\t"
      "s_mov_b32 s83, 0x77777777\n\t"
      "s_lshl_b32 s35, s32, 7\n\t"
      "s_add_u32 s62, s18, s35\n\t"
      "s_addc_u32 s63, s19, 0\n\t"
      "s_mul_i32 s35, s32, 96\n\t"
      "s_add_u32 s35, s35, 512\n\t"
      "s_add_u32 s18, s18, s35\n\t"
      "s_addc_u32 s19, s19, 0\n\t"
      "s_mul_i32 s35, s29, 28\n\t"
      "s_lshl2_add_u32 s35, s32, s35\n\t"
      "s_lshl1_add_u32 s35, s35, s36\n\t"
      "s_lshl2_add_u32 s35, s35, s34\n\t"
      "s_lshl_b32 s35, s35, 8\n\t"
      "s_add_u32 s35, s35, 6295552\n\t"
      "s_add_u32 s50, s20, s35\n\t"
      "s_addc_u32 s51, s21, 0\n\t"
      "v_lshrrev_b32_e32 v248, 2, v244\n\t"
      "v_bfe_u32 v242, v244, 8, 1\n\t"
      "v_lshlrev_b32_e32 v242, 4, v242\n\t"
      "v_xor_b32_e32 v245, v244, v242\n\t"
      "v_bfe_u32 v242, v244, 9, 1\n\t"
      "v_lshlrev_b32_e32 v242, 7, v242\n\t"
      "v_xor_b32_e32 v245, v245, v242\n\t"
      "s_add_u32 s35, s39, 98304\n\t"
      "v_add_u32_e32 v245, s35, v245\n\t"
      "v_lshl_or_b32 v246, v240, 1, s36\n\t"
      "v_bfe_u32 v242, v244, 5, 1\n\t"
      "v_xor_b32_e32 v246, v246, v242\n\t"
      "v_bfe_u32 v242, v244, 6, 1\n\t"
      "v_lshlrev_b32_e32 v242, 3, v242\n\t"
      "v_xor_b32_e32 v246, v246, v242\n\t"
      "v_lshlrev_b32_e32 v246, 4, v246\n\t"
      "v_and_b32_e32 v242, 0x60, v244\n\t"
      "v_lshl_or_b32 v246, v242, 3, v246\n\t"
      "v_bfe_u32 v242, v244, 4, 1\n\t"
      "v_lshl_or_b32 v246, v242, 3, v246\n\t"
      "s_lshl_b32 s34, s34, 10\n\t"
      "s_add_u32 s34, s34, 98304\n\t"
      "v_add_u32_e32 v246, s34, v246\n\t"
      "v_and_b32_e32 v240, 0x3c0, v244\n\t"
      "v_lshlrev_b32_e32 v240, 1, v240\n\t"
      "v_and_b32_e32 v241, 0x180, v244\n\t"
      "v_lshrrev_b32_e32 v241, 2, v241\n\t"
      "v_and_b32_e32 v242, 0x30, v244\n\t"
      "v_lshrrev_b32_e32 v242, 1, v242\n\t"
      "v_or3_b32 v232, v240, v241, v242\n\t"
      "v_add_u32_e32 v232, s39, v232\n\t"
      "v_min_u32_e32 v252, 432, v244\n\t"
      "v_lshlrev_b32_e32 v252, 4, v252\n\t"
      "v_xor_b32_e32 v233, 32, v232\n\t"
      "v_xor_b32_e32 v234, 64, v232\n\t"
      "v_xor_b32_e32 v235, 96, v232\n\t"
      "v_xor_b32_e32 v236, 65536, v232\n\t"
      "v_xor_b32_e32 v237, 65568, v232\n\t"
      "v_xor_b32_e32 v238, 65600, v232\n\t"
      "v_xor_b32_e32 v239, 65632, v232\n\t"
      "v_min_u32_e32 v240, 48, v244\n\t"
      "v_mul_u32_u24_e32 v240, 7, v240\n\t"
      "s_lshl_b32 s34, s29, 4\n\t"
      "v_add_u32_e32 v240, s34, v240\n\t"
      "v_subrev_u32_e32 v241, 448, v240\n\t"
      "v_min_u32_e32 v240, v240, v241\n\t"
      "v_subrev_u32_e32 v241, 448, v240\n\t"
      "v_min_u32_e32 v240, v240, v241\n\t"
      "s_and_b32 s34, s29, 7\n\t"
      "s_lshl_b32 s34, s34, 8\n\t"
      "v_lshl_add_u32 v253, v240, 7, s34\n\t"
      "s_sub_u32 s52, s64, 0\n\t"
      "s_subb_u32 s53, s65, 0\n\t"
      "v_add_u32_e32 v143, 32768, v143\n\t"
      "s_barrier\n\t"
      "s_lshl_b32 s37, s42, 7\n\t"
      "s_add_u32 s58, s54, s37\n\t"
      "s_addc_u32 s59, s55, 0\n\t"
      "s_add_u32 m0, s39, 65536\n\t"
      "s_nop 0\n\t"
      "global_load_lds_dwordx4 v228, s[58:59]\n\t"
      "global_load_lds_dwordx4 v229, s[58:59] offset:1024\n\t"
      "global_load_lds_dwordx4 v230, s[58:59] offset:2048\n\t"
      "global_load_lds_dwordx4 v231, s[58:59] offset:3072\n\t"
      "s_lshl_b32 s37, s43, 7\n\t"
      "s_add_u32 s58, s54, s37\n\t"
      "s_addc_u32 s59, s55, 0\n\t"
      "s_add_u32 m0, s39, 98304\n\t"
      "s_nop 0\n\t"
      "global_load_lds_dwordx4 v228, s[58:59]\n\t"
      "global_load_lds_dwordx4 v229, s[58:59] offset:1024\n\t"
      "global_load_lds_dwordx4 v230, s[58:59] offset:2048\n\t"
      "global_load_lds_dwordx4 v231, s[58:59] offset:3072\n\t"
      "s_waitcnt vmcnt(13)\n\t"
      "ds_read_b64_tr_b16 v[0:1], v232\n\t"
      "ds_read_b64_tr_b16 v[2:3], v232 offset:2048\n\t"
      "ds_read_b64_tr_b16 v[4:5], v233\n\t"
      "ds_read_b64_tr_b16 v[6:7], v233 offset:2048\n\t"
      "ds_read_b64_tr_b16 v[8:9], v234\n\t"
      "ds_read_b64_tr_b16 v[10:11], v234 offset:2048\n\t"
      "ds_read_b64_tr_b16 v[12:13], v235\n\t"
      "ds_read_b64_tr_b16 v[14:15], v235 offset:2048\n\t"
      "s_waitcnt lgkmcnt(0)\n\t"
      "s_lshl_b32 s37, s44, 7\n\t"
      "s_add_u32 s58, s54, s37\n\t"
      "s_addc_u32 s59, s55, 0\n\t"
      "s_add_u32 m0, s39, 0\n\t"
      "s_nop 0\n\t"
      "global_load_lds_dwordx4 v228, s[58:59]\n\t"
      "global_load_lds_dwordx4 v229, s[58:59] offset:1024\n\t"
      "global_load_lds_dwordx4 v230, s[58:59] offset:2048\n\t"
      "global_load_lds_dwordx4 v231, s[58:59] offset:3072\n\t"
      "s_waitcnt vmcnt(13)\n\t"
      "ds_read_b64_tr_b16 v[16:17], v232 offset:32768\n\t"
      "ds_read_b64_tr_b16 v[18:19], v232 offset:34816\n\t"
      "ds_read_b64_tr_b16 v[20:21], v233 offset:32768\n\t"
      "ds_read_b64_tr_b16 v[22:23], v233 offset:34816\n\t"
      "ds_read_b64_tr_b16 v[24:25], v234 offset:32768\n\t"
      "ds_read_b64_tr_b16 v[26:27], v234 offset:34816\n\t"
      "ds_read_b64_tr_b16 v[28:29], v235 offset:32768\n\t"
      "ds_read_b64_tr_b16 v[30:31], v235 offset:34816\n\t"
      "s_cmp_lg_u32 s24, 3\n\t"
      "s_cbranch_scc1 SK18_%=\n\t"
      "global_load_dwordx2 v[138:139], v143, s[60:61] sc1\n\t"
      "SK18_%=:\n\t"
      "s_waitcnt lgkmcnt(0)\n\t"
      "s_lshl_b32 s37, s45, 7\n\t"
      "s_add_u32 s58, s54, s37\n\t"
      "s_addc_u32 s59, s55, 0\n\t"
      "s_add_u32 m0, s39, 32768\n\t"
      "s_nop 0\n\t"
      "global_load_lds_dwordx4 v228, s[58:59]\n\t"
      "global_load_lds_dwordx4 v229, s[58:59] offset:1024\n\t"
      "global_load_lds_dwordx4 v230, s[58:59] offset:2048\n\t"
      "global_load_lds_dwordx4 v231, s[58:59] offset:3072\n\t"
      "s_waitcnt vmcnt(12)\n\t"
      "ds_read_b64_tr_b16 v[32:33], v236\n\t"
      "ds_read_b64_tr_b16 v[34:35], v236 offset:2048\n\t"
      "ds_read_b64_tr_b16 v[36:37], v237\n\t"
      "ds_read_b64_tr_b16 v[38:39], v237 offset:2048\n\t"
      "ds_read_b64_tr_b16 v[40:41], v238\n\t"
      "ds_read_b64_tr_b16 v[42:43], v238 offset:2048\n\t"
      "ds_read_b64_tr_b16 v[44:45], v239\n\t"
      "ds_read_b64_tr_b16 v[46:47], v239 offset:2048\n\t"
      "s_waitcnt lgkmcnt(0)\n\t"
      "s_lshl_b32 s37, s46, 7\n\t"
      "s_add_u32 s58, s54, s37\n\t"
      "s_addc_u32 s59, s55, 0\n\t"
      "s_add_u32 m0, s39, 65536\n\t"
      "s_nop 0\n\t"
      "global_load_lds_dwordx4 v228, s[58:59]\n\t"
      "global_load_lds_dwordx4 v229, s[58:59] offset:1024\n\t"
      "global_load_lds_dwordx4 v230, s[58:59] offset:2048\n\t"
      "global_load_lds_dwordx4 v231, s[58:59] offset:3072\n\t"
      "s_waitcnt vmcnt(12)\n\t"
      "ds_read_b64_tr_b16 v[48:49], v236 offset:32768\n\t"
      "ds_read_b64_tr_b16 v[50:51], v236 offset:34816\n\t"
      "ds_read_b64_tr_b16 v[52:53], v237 offset:32768\n\t"
      "ds_read_b64_tr_b16 v[54:55], v237 offset:34816\n\t"
      "ds_read_b64_tr_b16 v[56:57], v238 offset:32768\n\t"
      "ds_read_b64_tr_b16 v[58:59], v238 offset:34816\n\t"
      "ds_read_b64_tr_b16 v[60:61], v239 offset:32768\n\t"
      "ds_read_b64_tr_b16 v[62:63], v239 offset:34816\n\t"
      "s_waitcnt vmcnt(8)\n\t"
      "ds_read_b64_tr_b16 v[64:65], v232\n\t"
      "ds_read_b64_tr_b16 v[66:67], v232 offset:2048\n\t"
      "ds_read_b64_tr_b16 v[68:69], v233\n\t"
      "ds_read_b64_tr_b16 v[70:71], v233 offset:2048\n\t"
      "ds_read_b64_tr_b16 v[72:73], v234\n\t"
      "ds_read_b64_tr_b16 v[74:75], v234 offset:2048\n\t"
      "ds_read_b64_tr_b16 v[76:77], v235\n\t"
      "ds_read_b64_tr_b16 v[78:79], v235 offset:2048\n\t"
      "s_cmp_lg_u32 s24, 3\n\t"
      "s_cbranch_scc1 LF5B_%=\n\t"
      "s_waitcnt vmcnt(8)\n\t"
      "v_cmp_ne_u64_e32 vcc, s[22:23], v[138:139]\n\t"
      "s_cbranch_vccnz LF5R_%=\n\t"
      "LF5B_%=:\n\t"
      "s_barrier\n\t"
      "s_cmp_lg_u32 s24, 3\n\t"
      "s_cbranch_scc1 HF4_%=\n\t"
      "s_lshl3_add_u32 s34, s25, s26\n\t"
      "s_lshl_b32 s34, s34, 7\n\t"
      "s_add_u32 s34, s34, 98304\n\t"
      "v_mov_b32_e32 v146, s34\n\t"
      "v_mov_b32_e32 v144, s22\n\t"
      "v_mov_b32_e32 v145, s23\n\t"
      "s_mov_b64 exec, 1\n\t"
      "global_store_dwordx2 v146, v[144:145], s[60:61] sc1\n\t"
      "s_mov_b64 exec, -1\n\t"
      "HF4_%=:\n\t"
      "s_cmp_lg_u32 s24, 0\n\t"
      "s_cbranch_scc1 PG0_%=\n\t"
      "global_load_dword v251, v253, s[52:53] offset:252 sc1\n\t"
      "PG0_%=:\n\t"
      "s_waitcnt vmcnt(4)\n\t"
      "ds_read_b64_tr_b16 v[80:81], v232 offset:32768\n\t"
      "ds_read_b64_tr_b16 v[82:83], v232 offset:34816\n\t"
      "ds_read_b64_tr_b16 v[84:85], v233 offset:32768\n\t"
      "ds_read_b64_tr_b16 v[86:87], v233 offset:34816\n\t"
      "ds_read_b64_tr_b16 v[88:89], v234 offset:32768\n\t"
      "ds_read_b64_tr_b16 v[90:91], v234 offset:34816\n\t"
      "ds_read_b64_tr_b16 v[92:93], v235 offset:32768\n\t"
      "ds_read_b64_tr_b16 v[94:95], v235 offset:34816\n\t"
      "s_waitcnt vmcnt(0)\n\t"
      "ds_read_b64_tr_b16 v[96:97], v236\n\t"
      "ds_read_b64_tr_b16 v[98:99], v236 offset:2048\n\t"
      "ds_read_b64_tr_b16 v[100:101], v237\n\t"
      "ds_read_b64_tr_b16 v[102:103], v237 offset:2048\n\t"
      "ds_read_b64_tr_b16 v[104:105], v238\n\t"
      "ds_read_b64_tr_b16 v[106:107], v238 offset:2048\n\t"
      "ds_read_b64_tr_b16 v[108:109], v239\n\t"
      "ds_read_b64_tr_b16 v[110:111], v239 offset:2048\n\t"
      "v_lshlrev_b32_e32 v249, 16, v249\n\t"
      "s_and_b32 s34, s29, 30\n\t"
      "s_lshl_b32 s34, s34, 10\n\t"
      "s_add_u32 s34, s34, 98304\n\t"
      "v_bfe_u32 v143, v244, 5, 2\n\t"
      "v_add_u32_e32 v143, 3, v143\n\t"
      "v_writelane_b32 v143, s32, 0\n\t"
      "v_writelane_b32 v143, s32, 1\n\t"
      "v_lshl_add_u32 v143, v143, 7, s34\n\t"
      "v_and_b32_e32 v142, 16, v244\n\t"
      "v_lshl_add_u32 v143, v142, 6, v143\n\t"
      "s_waitcnt vmcnt(0) lgkmcnt(0)\n\t"
      "s_cmp_lg_u32 s24, 0\n\t"
      "s_cbranch_scc1 PGB_%=\n\t"
      "v_cmp_eq_u32_e32 vcc, -1, v251\n\t"
      "s_cbranch_vccnz PGR_%=\n\t"
      "PGB_%=:\n\t"
      "s_barrier\n\t"
      "s_cmp_lg_u32 s24, 3\n\t"
      "s_cbranch_scc1 SK19_%=\n\t"
      "global_load_dwordx2 v[240:241], v143, s[60:61] sc1\n\t"
      "SK19_%=:\n\t"
      "v_mov_b32_e32 v242, v143\n\t"
      "s_add_u32 s58, s64, s40\n\t"
      "s_addc_u32 s59, s65, 0\n\t"
      "global_load_dwordx4 v[128:131], v244, s[58:59] sc1\n\t"
      "s_add_u32 s58, s64, s41\n\t"
      "s_addc_u32 s59, s65, 0\n\t"
      "global_load_dwordx4 v[132:135], v244, s[58:59] sc1\n\t"
      "s_add_u32 s58, s64, s42\n\t"
      "s_addc_u32 s59, s65, 0\n\t"
      "global_load_dwordx4 v[136:139], v244, s[58:59] sc1\n\t"
      "s_add_u32 s58, s64, s43\n\t"
      "s_addc_u32 s59, s65, 0\n\t"
      "global_load_dwordx4 v[140:143], v244, s[58:59] sc1\n\t"
      "s_add_u32 s58, s64, s44\n\t"
      "s_addc_u32 s59, s65, 0\n\t"
      "global_load_dwordx4 v[144:147], v244, s[58:59] sc1\n\t"
      "s_add_u32 s58, s64, s45\n\t"
      "s_addc_u32 s59, s65, 0\n\t"
      "global_load_dwordx4 v[148:151], v244, s[58:59] sc1\n\t"
      "s_add_u32 s58, s64, s46\n\t"
      "s_addc_u32 s59, s65, 0\n\t"
      "global_load_dwordx4 v[152:155], v244, s[58:59] sc1\n\t"
      "v_mov_b32_e32 v254, v249\n\t"
      "s_waitcnt vmcnt(6)\n\t"
      "v_mfma_f32_16x16x32_bf16 v[196:199], v[128:131], v[0:3], 0\n\t"
      "v_mfma_f32_16x16x32_bf16 v[200:203], v[128:131], v[4:7], 0\n\t"
      "v_mfma_f32_16x16x32_bf16 v[204:207], v[128:131], v[8:11], 0\n\t"
      "v_mfma_f32_16x16x32_bf16 v[208:211], v[128:131], v[12:15], 0\n\t"
      "s_waitcnt vmcnt(5)\n\t"
      "v_mfma_f32_16x16x32_bf16 v[196:199], v[132:135], v[16:19], v[196:199]\n\t"
      "v_mfma_f32_16x16x32_bf16 v[200:203], v[132:135], v[20:23], v[200:203]\n\t"
      "v_mfma_f32_16x16x32_bf16 v[204:207], v[132:135], v[24:27], v[204:207]\n\t"
      "s_cmp_lg_u32 s24, 3\n\t"
      "s_cbranch_scc1 LF8B_%=\n\t"
      "v_cmp_ne_u64_e32 vcc, s[22:23], v[240:241]\n\t"
      "s_cbranch_vccnz LF8R_%=\n\t"
      "LF8B_%=:\n\t"
      "s_barrier\n\t"
      "v_mfma_f32_16x16x32_bf16 v[208:211], v[132:135], v[28:31], v[208:211]\n\t"
      "global_load_dword v115, v248, s[50:51] offset:-4096 sc1\n\t"
      "global_load_dword v116, v248, s[50:51] offset:-2048 sc1\n\t"
      "s_waitcnt vmcnt(6)\n\t"
      "v_mfma_f32_16x16x32_bf16 v[196:199], v[136:139], v[32:35], v[196:199]\n\t"
      "global_load_dword v117, v248, s[50:51] sc1\n\t"
      "global_load_dword v118, v248, s[50:51] offset:2048 sc1\n\t"
      "v_mfma_f32_16x16x32_bf16 v[200:203], v[136:139], v[36:39], v[200:203]\n\t"
      "s_sub_u32 s34, 16, s32\n\t"
      "s_lshl_b32 s34, s34, 11\n\t"
      "s_add_u32 s50, s50, s34\n\t"
      "s_addc_u32 s51, s51, 0\n\t"
      "global_load_dword v112, v248, s[50:51] offset:-4096 sc1\n\t"
      "global_load_dword v113, v248, s[50:51] offset:-2048 sc1\n\t"
      "v_mfma_f32_16x16x32_bf16 v[204:207], v[136:139], v[40:43], v[204:207]\n\t"
      "global_load_dword v114, v248, s[50:51] sc1\n\t"
      "s_add_u32 s58, s66, s40\n\t"
      "s_addc_u32 s59, s67, 0\n\t"
      "global_load_dwordx4 v[156:159], v244, s[58:59] sc1\n\t"
      "v_mfma_f32_16x16x32_bf16 v[208:211], v[136:139], v[44:47], v[208:211]\n\t"
      "s_waitcnt vmcnt(11)\n\t"
      "v_mfma_f32_16x16x32_bf16 v[196:199], v[140:143], v[48:51], v[196:199]\n\t"
      "v_mfma_f32_16x16x32_bf16 v[200:203], v[140:143], v[52:55], v[200:203]\n\t"
      "v_mfma_f32_16x16x32_bf16 v[204:207], v[140:143], v[56:59], v[204:207]\n\t"
      "s_add_u32 s58, s66, s41\n\t"
      "s_addc_u32 s59, s67, 0\n\t"
      "global_load_dwordx4 v[160:163], v244, s[58:59] sc1\n\t"
      "v_mfma_f32_16x16x32_bf16 v[208:211], v[140:143], v[60:63], v[208:211]\n\t"
      "s_waitcnt vmcnt(11)\n\t"
      "v_mfma_f32_16x16x32_bf16 v[196:199], v[144:147], v[64:67], v[196:199]\n\t"
      "v_mfma_f32_16x16x32_bf16 v[200:203], v[144:147], v[68:71], v[200:203]\n\t"
      "v_mfma_f32_16x16x32_bf16 v[204:207], v[144:147], v[72:75], v[204:207]\n\t"
      "s_add_u32 s58, s66, s42\n\t"
      "s_addc_u32 s59, s67, 0\n\t"
      "global_load_dwordx4 v[164:167], v244, s[58:59] sc1\n\t"
      "v_mfma_f32_16x16x32_bf16 v[208:211], v[144:147], v[76:79], v[208:211]\n\t"
      "s_waitcnt vmcnt(11)\n\t"
      "v_mfma_f32_16x16x32_bf16 v[196:199], v[148:151], v[80:83], v[196:199]\n\t"
      "v_mfma_f32_16x16x32_bf16 v[200:203], v[148:151], v[84:87], v[200:203]\n\t"
      "v_mfma_f32_16x16x32_bf16 v[204:207], v[148:151], v[88:91], v[204:207]\n\t"
      "s_add_u32 s58, s66, s43\n\t"
      "s_addc_u32 s59, s67, 0\n\t"
      "global_load_dwordx4 v[168:171], v244, s[58:59] sc1\n\t"
      "v_mfma_f32_16x16x32_bf16 v[208:211], v[148:151], v[92:95], v[208:211]\n\t"
      "s_waitcnt vmcnt(11)\n\t"
      "v_mfma_f32_16x16x32_bf16 v[196:199], v[152:155], v[96:99], v[196:199]\n\t"
      "v_mfma_f32_16x16x32_bf16 v[200:203], v[152:155], v[100:103], v[200:203]\n\t"
      "v_mfma_f32_16x16x32_bf16 v[204:207], v[152:155], v[104:107], v[204:207]\n\t"
      "s_add_u32 s58, s66, s44\n\t"
      "s_addc_u32 s59, s67, 0\n\t"
      "global_load_dwordx4 v[172:175], v244, s[58:59] sc1\n\t"
      "v_mfma_f32_16x16x32_bf16 v[208:211], v[152:155], v[108:111], v[208:211]\n\t"
      "s_nop 1\n\t"
      "v_add_f32_e32 v251, v196, v197\n\t"
      "v_add_f32_e32 v251, v251, v198\n\t"
      "v_cmp_u_f32_e32 vcc, v251, v199\n\t"
      "s_cbranch_vccnz LA40R_%=\n\t"
      "LA40B_%=:\n\t"
      "ds_write_b128 v245, v[196:199]\n\t"
      "s_waitcnt vmcnt(4)\n\t"
      "v_mfma_f32_16x16x32_bf16 v[212:215], v[156:159], v[0:3], 0\n\t"
      "ds_write_b128 v245, v[200:203] offset:1024\n\t"
      "v_mfma_f32_16x16x32_bf16 v[216:219], v[156:159], v[4:7], 0\n\t"
      "ds_write_b128 v245, v[204:207] offset:2048\n\t"
      "v_mfma_f32_16x16x32_bf16 v[220:223], v[156:159], v[8:11], 0\n\t"
      "ds_write_b128 v245, v[208:211] offset:3072\n\t"
      "s_add_u32 s58, s66, s45\n\t"
      "s_addc_u32 s59, s67, 0\n\t"
      "global_load_dwordx4 v[176:179], v244, s[58:59] sc1\n\t"
      "v_mfma_f32_16x16x32_bf16 v[224:227], v[156:159], v[12:15], 0\n\t"
      "s_waitcnt vmcnt(4)\n\t"
      "v_mfma_f32_16x16x32_bf16 v[212:215], v[160:163], v[16:19], v[212:215]\n\t"
      "v_mfma_f32_16x16x32_bf16 v[216:219], v[160:163], v[20:23], v[216:219]\n\t"
      "v_mfma_f32_16x16x32_bf16 v[220:223], v[160:163], v[24:27], v[220:223]\n\t"
      "s_waitcnt lgkmcnt(0)\n\t"
      "s_barrier\n\t"
      "ds_read_b64 v[228:229], v246\n\t"
      "ds_read_b64 v[230:231], v246 offset:4096\n\t"
      "s_add_u32 s58, s66, s46\n\t"
      "s_addc_u32 s59, s67, 0\n\t"
      "global_load_dwordx4 v[180:183], v244, s[58:59] sc1\n\t"
      "v_mfma_f32_16x16x32_bf16 v[224:227], v[160:163], v[28:31], v[224:227]\n\t"
      "ds_read_b64 v[232:233], v246 offset:8192\n\t"
      "ds_read_b64 v[234:235], v246 offset:12288\n\t"
      "s_waitcnt vmcnt(4)\n\t"
      "v_mfma_f32_16x16x32_bf16 v[212:215], v[164:167], v[32:35], v[212:215]\n\t"
      "ds_read_b64 v[236:237], v246 offset:16384\n\t"
      "ds_read_b64 v[238:239], v246 offset:20480\n\t"
      "v_mfma_f32_16x16x32_bf16 v[216:219], v[164:167], v[36:39], v[216:219]\n\t"
      "ds_read_b64 v[240:241], v246 offset:24576\n\t"
      "ds_read_b64 v[242:243], v246 offset:28672\n\t"
      "v_lshlrev_b32_e32 v248, 16, v112\n\t"
      "s_add_u32 s58, s68, s40\n\t"
      "s_addc_u32 s59, s69, 0\n\t"
      "global_load_dwordx4 v[184:187], v244, s[58:59] sc1\n\t"
      "v_mfma_f32_16x16x32_bf16 v[220:223], v[164:167], v[40:43], v[220:223]\n\t"
      "v_and_b32_e32 v249, 0xffff0000, v112\n\t"
      "v_mfma_f32_16x16x32_bf16 v[224:227], v[164:167], v[44:47], v[224:227]\n\t"
      "v_add_f32_e32 v248, v254, v248\n\t"
      "v_add_f32_e32 v249, v254, v249\n\t"
      "s_waitcnt vmcnt(4)\n\t"
      "v_mfma_f32_16x16x32_bf16 v[212:215], v[168:171], v[48:51], v[212:215]\n\t"
      "s_waitcnt lgkmcnt(4)\n\t"
      "v_mfma_f32_16x16x32_bf16 v[216:219], v[168:171], v[52:55], v[216:219]\n\t"
      "v_add_f32_e32 v228, v228, v230\n\t"
      "v_add_f32_e32 v232, v232, v234\n\t"
      "s_add_u32 s58, s68, s41\n\t"
      "s_addc_u32 s59, s69, 0\n\t"
      "global_load_dwordx4 v[188:191], v244, s[58:59] sc1\n\t"
      "v_mfma_f32_16x16x32_bf16 v[220:223], v[168:171], v[56:59], v[220:223]\n\t"
      "v_add_f32_e32 v229, v229, v231\n\t"
      "v_mfma_f32_16x16x32_bf16 v[224:227], v[168:171], v[60:63], v[224:227]\n\t"
      "v_add_f32_e32 v233, v233, v235\n\t"
      "v_add_f32_e32 v228, v228, v232\n\t"
      "s_waitcnt vmcnt(4)\n\t"
      "v_mfma_f32_16x16x32_bf16 v[212:215], v[172:175], v[64:67], v[212:215]\n\t"
      "v_add_f32_e32 v229, v229, v233\n\t"
      "v_mfma_f32_16x16x32_bf16 v[216:219], v[172:175], v[68:71], v[216:219]\n\t"
      "s_waitcnt lgkmcnt(0)\n\t"
      "v_add_f32_e32 v236, v236, v238\n\t"
      "s_add_u32 s58, s68, s42\n\t"
      "s_addc_u32 s59, s69, 0\n\t"
      "global_load_dwordx4 v[192:195], v244, s[58:59] sc1\n\t"
      "v_mfma_f32_16x16x32_bf16 v[220:223], v[172:175], v[72:75], v[220:223]\n\t"
      "v_add_f32_e32 v240, v240, v242\n\t"
      "v_mfma_f32_16x16x32_bf16 v[224:227], v[172:175], v[76:79], v[224:227]\n\t"
      "v_add_f32_e32 v237, v237, v239\n\t"
      "v_add_f32_e32 v241, v241, v243\n\t"
      "s_waitcnt vmcnt(4)\n\t"
      "v_mfma_f32_16x16x32_bf16 v[212:215], v[176:179], v[80:83], v[212:215]\n\t"
      "v_add_f32_e32 v228, v228, v248\n\t"
      "v_mfma_f32_16x16x32_bf16 v[216:219], v[176:179], v[84:87], v[216:219]\n\t"
      "v_add_f32_e32 v236, v236, v240\n\t"
      "v_add_f32_e32 v229, v229, v249\n\t"
      "s_add_u32 s58, s68, s43\n\t"
      "s_addc_u32 s59, s69, 0\n\t"
      "global_load_dwordx4 v[128:131], v244, s[58:59] sc1\n\t"
      "v_mfma_f32_16x16x32_bf16 v[220:223], v[176:179], v[88:91], v[220:223]\n\t"
      "v_add_f32_e32 v237, v237, v241\n\t"
      "v_mfma_f32_16x16x32_bf16 v[224:227], v[176:179], v[92:95], v[224:227]\n\t"
      "v_add_f32_e32 v228, v228, v236\n\t"
      "v_add_f32_e32 v229, v229, v237\n\t"
      "s_waitcnt vmcnt(4)\n\t"
      "v_mfma_f32_16x16x32_bf16 v[212:215], v[180:183], v[96:99], v[212:215]\n\t"
      "v_cvt_pk_bf16_f32 v232, v228, v229\n\t"
      "v_mfma_f32_16x16x32_bf16 v[216:219], v[180:183], v[100:103], v[216:219]\n\t"
      "s_mov_b32 vcc_lo, 0xeeeeeeee\n\t"
      "s_mov_b32 vcc_hi, 0xeeeeeeee\n\t"
      "v_cndmask_b32_dpp v120, v232, v120, vcc quad_perm:[0,0,0,0] row_mask:0xf bank_mask:0xf\n\t"
      "v_cndmask_b32_dpp v121, v232, v121, vcc quad_perm:[1,1,1,1] row_mask:0xf bank_mask:0xf\n\t"
      "v_cndmask_b32_dpp v122, v232, v122, vcc quad_perm:[2,2,2,2] row_mask:0xf bank_mask:0xf\n\t"
      "v_cndmask_b32_dpp v123, v232, v123, vcc quad_perm:[3,3,3,3] row_mask:0xf bank_mask:0xf\n\t"
      "s_add_u32 s58, s68, s44\n\t"
      "s_addc_u32 s59, s69, 0\n\t"
      "global_load_dwordx4 v[132:135], v244, s[58:59] sc1\n\t"
      "v_mfma_f32_16x16x32_bf16 v[220:223], v[180:183], v[104:107], v[220:223]\n\t"
      "v_mfma_f32_16x16x32_bf16 v[224:227], v[180:183], v[108:111], v[224:227]\n\t"
      "v_add_f32_e32 v251, v212, v213\n\t"
      "v_add_f32_e32 v251, v251, v214\n\t"
      "v_cmp_u_f32_e32 vcc, v251, v215\n\t"
      "s_cbranch_vccnz LA41R_%=\n\t"
      "LA41B_%=:\n\t"
      "ds_write_b128 v245, v[212:215] offset:32768\n\t"
      "s_waitcnt vmcnt(4)\n\t"
      "v_mfma_f32_16x16x32_bf16 v[196:199], v[184:187], v[0:3], 0\n\t"
      "ds_write_b128 v245, v[216:219] offset:33792\n\t"
      "v_mfma_f32_16x16x32_bf16 v[200:203], v[184:187], v[4:7], 0\n\t"
      "ds_write_b128 v245, v[220:223] offset:34816\n\t"
      "s_add_u32 s58, s68, s45\n\t"
      "s_addc_u32 s59, s69, 0\n\t"
      "global_load_dwordx4 v[136:139], v244, s[58:59] sc1\n\t"
      "v_mfma_f32_16x16x32_bf16 v[204:207], v[184:187], v[8:11], 0\n\t"
      "ds_write_b128 v245, v[224:227] offset:35840\n\t"
      "v_mfma_f32_16x16x32_bf16 v[208:211], v[184:187], v[12:15], 0\n\t"
      "s_waitcnt vmcnt(4)\n\t"
      "v_mfma_f32_16x16x32_bf16 v[196:199], v[188:191], v[16:19], v[196:199]\n\t"
      "v_mfma_f32_16x16x32_bf16 v[200:203], v[188:191], v[20:23], v[200:203]\n\t"
      "s_add_u32 s58, s68, s46\n\t"
      "s_addc_u32 s59, s69, 0\n\t"
      "global_load_dwordx4 v[140:143], v244, s[58:59] sc1\n\t"
      "v_mfma_f32_16x16x32_bf16 v[204:207], v[188:191], v[24:27], v[204:207]\n\t"
      "s_waitcnt lgkmcnt(0)\n\t"
      "s_barrier\n\t"
      "ds_read_b64 v[228:229], v246 offset:32768\n\t"
      "ds_read_b64 v[230:231], v246 offset:36864\n\t"
      "v_mfma_f32_16x16x32_bf16 v[208:211], v[188:191], v[28:31], v[208:211]\n\t"
      "ds_read_b64 v[232:233], v246 offset:40960\n\t"
      "ds_read_b64 v[234:235], v246 offset:45056\n\t"
      "s_waitcnt vmcnt(4)\n\t"
      "v_mfma_f32_16x16x32_bf16 v[196:199], v[192:195], v[32:35], v[196:199]\n\t"
      "ds_read_b64 v[236:237], v246 offset:49152\n\t"
      "ds_read_b64 v[238:239], v246 offset:53248\n\t"
      "v_mfma_f32_16x16x32_bf16 v[200:203], v[192:195], v[36:39], v[200:203]\n\t"
      "ds_read_b64 v[240:241], v246 offset:57344\n\t"
      "ds_read_b64 v[242:243], v246 offset:61440\n\t"
      "v_lshlrev_b32_e32 v248, 16, v113\n\t"
      "s_add_u32 s58, s70, s40\n\t"
      "s_addc_u32 s59, s71, 0\n\t"
      "global_load_dwordx4 v[144:147], v244, s[58:59] sc1\n\t"
      "v_mfma_f32_16x16x32_bf16 v[204:207], v[192:195], v[40:43], v[204:207]\n\t"
      "v_and_b32_e32 v249, 0xffff0000, v113\n\t"
      "v_mfma_f32_16x16x32_bf16 v[208:211], v[192:195], v[44:47], v[208:211]\n\t"
      "v_add_f32_e32 v248, v254, v248\n\t"
      "v_add_f32_e32 v249, v254, v249\n\t"
      "s_waitcnt vmcnt(4)\n\t"
      "v_mfma_f32_16x16x32_bf16 v[196:199], v[128:131], v[48:51], v[196:199]\n\t"
      "s_waitcnt lgkmcnt(4)\n\t"
      "v_mfma_f32_16x16x32_bf16 v[200:203], v[128:131], v[52:55], v[200:203]\n\t"
      "v_add_f32_e32 v228, v228, v230\n\t"
      "v_add_f32_e32 v232, v232, v234\n\t"
      "s_add_u32 s58, s70, s41\n\t"
      "s_addc_u32 s59, s71, 0\n\t"
      "global_load_dwordx4 v[148:151], v244, s[58:59] sc1\n\t"
      "v_mfma_f32_16x16x32_bf16 v[204:207], v[128:131], v[56:59], v[204:207]\n\t"
      "v_add_f32_e32 v229, v229, v231\n\t"
      "v_mfma_f32_16x16x32_bf16 v[208:211], v[128:131], v[60:63], v[208:211]\n\t"
      "v_add_f32_e32 v233, v233, v235\n\t"
      "v_add_f32_e32 v228, v228, v232\n\t"
      "s_waitcnt vmcnt(4)\n\t"
      "v_mfma_f32_16x16x32_bf16 v[196:199], v[132:135], v[64:67], v[196:199]\n\t"
      "v_add_f32_e32 v229, v229, v233\n\t"
      "v_mfma_f32_16x16x32_bf16 v[200:203], v[132:135], v[68:71], v[200:203]\n\t"
      "s_waitcnt lgkmcnt(0)\n\t"
      "v_add_f32_e32 v236, v236, v238\n\t"
      "s_add_u32 s58, s70, s42\n\t"
      "s_addc_u32 s59, s71, 0\n\t"
      "global_load_dwordx4 v[152:155], v244, s[58:59] sc1\n\t"
      "v_mfma_f32_16x16x32_bf16 v[204:207], v[132:135], v[72:75], v[204:207]\n\t"
      "v_add_f32_e32 v240, v240, v242\n\t"
      "v_mfma_f32_16x16x32_bf16 v[208:211], v[132:135], v[76:79], v[208:211]\n\t"
      "v_add_f32_e32 v237, v237, v239\n\t"
      "v_add_f32_e32 v241, v241, v243\n\t"
      "s_waitcnt vmcnt(4)\n\t"
      "v_mfma_f32_16x16x32_bf16 v[196:199], v[136:139], v[80:83], v[196:199]\n\t"
      "v_add_f32_e32 v228, v228, v248\n\t"
      "v_mfma_f32_16x16x32_bf16 v[200:203], v[136:139], v[84:87], v[200:203]\n\t"
      "v_add_f32_e32 v236, v236, v240\n\t"
      "v_add_f32_e32 v229, v229, v249\n\t"
      "s_add_u32 s58, s70, s43\n\t"
      "s_addc_u32 s59, s71, 0\n\t"
      "global_load_dwordx4 v[156:159], v244, s[58:59] sc1\n\t"
      "v_mfma_f32_16x16x32_bf16 v[204:207], v[136:139], v[88:91], v[204:207]\n\t"
      "v_add_f32_e32 v237, v237, v241\n\t"
      "v_mfma_f32_16x16x32_bf16 v[208:211], v[136:139], v[92:95], v[208:211]\n\t"
      "v_add_f32_e32 v228, v228, v236\n\t"
      "v_add_f32_e32 v229, v229, v237\n\t"
      "s_waitcnt vmcnt(4)\n\t"
      "v_mfma_f32_16x16x32_bf16 v[196:199], v[140:143], v[96:99], v[196:199]\n\t"
      "v_cvt_pk_bf16_f32 v232, v228, v229\n\t"
      "v_mfma_f32_16x16x32_bf16 v[200:203], v[140:143], v[100:103], v[200:203]\n\t"
      "s_mov_b32 vcc_lo, 0xdddddddd\n\t"
      "s_mov_b32 vcc_hi, 0xdddddddd\n\t"
      "v_cndmask_b32_dpp v120, v232, v120, vcc quad_perm:[0,0,0,0] row_mask:0xf bank_mask:0xf\n\t"
      "v_cndmask_b32_dpp v121, v232, v121, vcc quad_perm:[1,1,1,1] row_mask:0xf bank_mask:0xf\n\t"
      "v_cndmask_b32_dpp v122, v232, v122, vcc quad_perm:[2,2,2,2] row_mask:0xf bank_mask:0xf\n\t"
      "v_cndmask_b32_dpp v123, v232, v123, vcc quad_perm:[3,3,3,3] row_mask:0xf bank_mask:0xf\n\t"
      "s_add_u32 s58, s70, s44\n\t"
      "s_addc_u32 s59, s71, 0\n\t"
      "global_load_dwordx4 v[160:163], v244, s[58:59] sc1\n\t"
      "v_mfma_f32_16x16x32_bf16 v[204:207], v[140:143], v[104:107], v[204:207]\n\t"
      "v_mfma_f32_16x16x32_bf16 v[208:211], v[140:143], v[108:111], v[208:211]\n\t"
      "v_add_f32_e32 v251, v196, v197\n\t"
      "v_add_f32_e32 v251, v251, v198\n\t"
      "v_cmp_u_f32_e32 vcc, v251, v199\n\t"
      "s_cbranch_vccnz LA42R_%=\n\t"
      "LA42B_%=:\n\t"
      "ds_write_b128 v245, v[196:199]\n\t"
      "s_waitcnt vmcnt(4)\n\t"
      "v_mfma_f32_16x16x32_bf16 v[212:215], v[144:147], v[0:3], 0\n\t"
      "ds_write_b128 v245, v[200:203] offset:1024\n\t"
      "v_mfma_f32_16x16x32_bf16 v[216:219], v[144:147], v[4:7], 0\n\t"
      "ds_write_b128 v245, v[204:207] offset:2048\n\t"
      "s_add_u32 s58, s70, s45\n\t"
      "s_addc_u32 s59, s71, 0\n\t"
      "global_load_dwordx4 v[164:167], v244, s[58:59] sc1\n\t"
      "v_mfma_f32_16x16x32_bf16 v[220:223], v[144:147], v[8:11], 0\n\t"
      "ds_write_b128 v245, v[208:211] offset:3072\n\t"
      "v_mfma_f32_16x16x32_bf16 v[224:227], v[144:147], v[12:15], 0\n\t"
      "s_waitcnt vmcnt(4)\n\t"
      "v_mfma_f32_16x16x32_bf16 v[212:215], v[148:151], v[16:19], v[212:215]\n\t"
      "v_mfma_f32_16x16x32_bf16 v[216:219], v[148:151], v[20:23], v[216:219]\n\t"
      "s_add_u32 s58, s70, s46\n\t"
      "s_addc_u32 s59, s71, 0\n\t"
      "global_load_dwordx4 v[168:171], v244, s[58:59] sc1\n\t"
      "v_mfma_f32_16x16x32_bf16 v[220:223], v[148:151], v[24:27], v[220:223]\n\t"
      "s_waitcnt lgkmcnt(0)\n\t"
      "s_barrier\n\t"
      "ds_read_b64 v[228:229], v246\n\t"
      "ds_read_b64 v[230:231], v246 offset:4096\n\t"
      "v_mfma_f32_16x16x32_bf16 v[224:227], v[148:151], v[28:31], v[224:227]\n\t"
      "ds_read_b64 v[232:233], v246 offset:8192\n\t"
      "ds_read_b64 v[234:235], v246 offset:12288\n\t"
      "s_waitcnt vmcnt(4)\n\t"
      "v_mfma_f32_16x16x32_bf16 v[212:215], v[152:155], v[32:35], v[212:215]\n\t"
      "ds_read_b64 v[236:237], v246 offset:16384\n\t"
      "ds_read_b64 v[238:239], v246 offset:20480\n\t"
      "v_mfma_f32_16x16x32_bf16 v[216:219], v[152:155], v[36:39], v[216:219]\n\t"
      "ds_read_b64 v[240:241], v246 offset:24576\n\t"
      "ds_read_b64 v[242:243], v246 offset:28672\n\t"
      "v_lshlrev_b32_e32 v248, 16, v114\n\t"
      "s_add_u32 s58, s72, s40\n\t"
      "s_addc_u32 s59, s73, 0\n\t"
      "global_load_dwordx4 v[172:175], v244, s[58:59] sc1\n\t"
      "v_mfma_f32_16x16x32_bf16 v[220:223], v[152:155], v[40:43], v[220:223]\n\t"
      "v_and_b32_e32 v249, 0xffff0000, v114\n\t"
      "v_mfma_f32_16x16x32_bf16 v[224:227], v[152:155], v[44:47], v[224:227]\n\t"
      "v_add_f32_e32 v248, v254, v248\n\t"
      "v_add_f32_e32 v249, v254, v249\n\t"
      "s_waitcnt vmcnt(4)\n\t"
      "v_mfma_f32_16x16x32_bf16 v[212:215], v[156:159], v[48:51], v[212:215]\n\t"
      "s_waitcnt lgkmcnt(4)\n\t"
      "v_mfma_f32_16x16x32_bf16 v[216:219], v[156:159], v[52:55], v[216:219]\n\t"
      "v_add_f32_e32 v228, v228, v230\n\t"
      "v_add_f32_e32 v232, v232, v234\n\t"
      "s_add_u32 s58, s72, s41\n\t"
      "s_addc_u32 s59, s73, 0\n\t"
      "global_load_dwordx4 v[176:179], v244, s[58:59] sc1\n\t"
      "v_mfma_f32_16x16x32_bf16 v[220:223], v[156:159], v[56:59], v[220:223]\n\t"
      "v_add_f32_e32 v229, v229, v231\n\t"
      "v_mfma_f32_16x16x32_bf16 v[224:227], v[156:159], v[60:63], v[224:227]\n\t"
      "v_add_f32_e32 v233, v233, v235\n\t"
      "v_add_f32_e32 v228, v228, v232\n\t"
      "s_waitcnt vmcnt(4)\n\t"
      "v_mfma_f32_16x16x32_bf16 v[212:215], v[160:163], v[64:67], v[212:215]\n\t"
      "v_add_f32_e32 v229, v229, v233\n\t"
      "v_mfma_f32_16x16x32_bf16 v[216:219], v[160:163], v[68:71], v[216:219]\n\t"
      "s_waitcnt lgkmcnt(0)\n\t"
      "v_add_f32_e32 v236, v236, v238\n\t"
      "s_add_u32 s58, s72, s42\n\t"
      "s_addc_u32 s59, s73, 0\n\t"
      "global_load_dwordx4 v[180:183], v244, s[58:59] sc1\n\t"
      "v_mfma_f32_16x16x32_bf16 v[220:223], v[160:163], v[72:75], v[220:223]\n\t"
      "v_add_f32_e32 v240, v240, v242\n\t"
      "v_mfma_f32_16x16x32_bf16 v[224:227], v[160:163], v[76:79], v[224:227]\n\t"
      "v_add_f32_e32 v237, v237, v239\n\t"
      "v_add_f32_e32 v241, v241, v243\n\t"
      "s_waitcnt vmcnt(4)\n\t"
      "v_mfma_f32_16x16x32_bf16 v[212:215], v[164:167], v[80:83], v[212:215]\n\t"
      "v_add_f32_e32 v228, v228, v248\n\t"
      "v_mfma_f32_16x16x32_bf16 v[216:219], v[164:167], v[84:87], v[216:219]\n\t"
      "v_add_f32_e32 v236, v236, v240\n\t"
      "v_add_f32_e32 v229, v229, v249\n\t"
      "s_add_u32 s58, s72, s43\n\t"
      "s_addc_u32 s59, s73, 0\n\t"
      "global_load_dwordx4 v[184:187], v244, s[58:59] sc1\n\t"
      "v_mfma_f32_16x16x32_bf16 v[220:223], v[164:167], v[88:91], v[220:223]\n\t"
      "v_add_f32_e32 v237, v237, v241\n\t"
      "v_mfma_f32_16x16x32_bf16 v[224:227], v[164:167], v[92:95], v[224:227]\n\t"
      "v_add_f32_e32 v228, v228, v236\n\t"
      "v_add_f32_e32 v229, v229, v237\n\t"
      "s_waitcnt vmcnt(4)\n\t"
      "v_mfma_f32_16x16x32_bf16 v[212:215], v[168:171], v[96:99], v[212:215]\n\t"
      "v_cvt_pk_bf16_f32 v232, v228, v229\n\t"
      "v_mfma_f32_16x16x32_bf16 v[216:219], v[168:171], v[100:103], v[216:219]\n\t"
      "s_mov_b32 vcc_lo, 0xbbbbbbbb\n\t"
      "s_mov_b32 vcc_hi, 0xbbbbbbbb\n\t"
      "v_cndmask_b32_dpp v120, v232, v120, vcc quad_perm:[0,0,0,0] row_mask:0xf bank_mask:0xf\n\t"
      "v_cndmask_b32_dpp v121, v232, v121, vcc quad_perm:[1,1,1,1] row_mask:0xf bank_mask:0xf\n\t"
      "v_cndmask_b32_dpp v122, v232, v122, vcc quad_perm:[2,2,2,2] row_mask:0xf bank_mask:0xf\n\t"
      "v_cndmask_b32_dpp v123, v232, v123, vcc quad_perm:[3,3,3,3] row_mask:0xf bank_mask:0xf\n\t"
      "s_add_u32 s58, s72, s44\n\t"
      "s_addc_u32 s59, s73, 0\n\t"
      "global_load_dwordx4 v[188:191], v244, s[58:59] sc1\n\t"
      "v_mfma_f32_16x16x32_bf16 v[220:223], v[168:171], v[104:107], v[220:223]\n\t"
      "v_mfma_f32_16x16x32_bf16 v[224:227], v[168:171], v[108:111], v[224:227]\n\t"
      "v_add_f32_e32 v251, v212, v213\n\t"
      "v_add_f32_e32 v251, v251, v214\n\t"
      "v_cmp_u_f32_e32 vcc, v251, v215\n\t"
      "s_cbranch_vccnz LA43R_%=\n\t"
      "LA43B_%=:\n\t"
      "ds_write_b128 v245, v[212:215] offset:32768\n\t"
      "s_waitcnt vmcnt(4)\n\t"
      "v_mfma_f32_16x16x32_bf16 v[196:199], v[172:175], v[0:3], 0\n\t"
      "ds_write_b128 v245, v[216:219] offset:33792\n\t"
      "v_mfma_f32_16x16x32_bf16 v[200:203], v[172:175], v[4:7], 0\n\t"
      "ds_write_b128 v245, v[220:223] offset:34816\n\t"
      "s_add_u32 s58, s72, s45\n\t"
      "s_addc_u32 s59, s73, 0\n\t"
      "global_load_dwordx4 v[192:195], v244, s[58:59] sc1\n\t"
      "v_mfma_f32_16x16x32_bf16 v[204:207], v[172:175], v[8:11], 0\n\t"
      "ds_write_b128 v245, v[224:227] offset:35840\n\t"
      "v_mfma_f32_16x16x32_bf16 v[208:211], v[172:175], v[12:15], 0\n\t"
      "s_waitcnt vmcnt(4)\n\t"
      "v_mfma_f32_16x16x32_bf16 v[196:199], v[176:179], v[16:19], v[196:199]\n\t"
      "v_mfma_f32_16x16x32_bf16 v[200:203], v[176:179], v[20:23], v[200:203]\n\t"
      "s_add_u32 s58, s72, s46\n\t"
      "s_addc_u32 s59, s73, 0\n\t"
      "global_load_dwordx4 v[128:131], v244, s[58:59] sc1\n\t"
      "v_mfma_f32_16x16x32_bf16 v[204:207], v[176:179], v[24:27], v[204:207]\n\t"
      "s_waitcnt lgkmcnt(0)\n\t"
      "s_barrier\n\t"
      "ds_read_b64 v[228:229], v246 offset:32768\n\t"
      "ds_read_b64 v[230:231], v246 offset:36864\n\t"
      "v_mfma_f32_16x16x32_bf16 v[208:211], v[176:179], v[28:31], v[208:211]\n\t"
      "ds_read_b64 v[232:233], v246 offset:40960\n\t"
      "ds_read_b64 v[234:235], v246 offset:45056\n\t"
      "s_waitcnt vmcnt(4)\n\t"
      "v_mfma_f32_16x16x32_bf16 v[196:199], v[180:183], v[32:35], v[196:199]\n\t"
      "ds_read_b64 v[236:237], v246 offset:49152\n\t"
      "ds_read_b64 v[238:239], v246 offset:53248\n\t"
      "v_mfma_f32_16x16x32_bf16 v[200:203], v[180:183], v[36:39], v[200:203]\n\t"
      "ds_read_b64 v[240:241], v246 offset:57344\n\t"
      "ds_read_b64 v[242:243], v246 offset:61440\n\t"
      "v_lshlrev_b32_e32 v248, 16, v115\n\t"
      "s_add_u32 s58, s74, s40\n\t"
      "s_addc_u32 s59, s75, 0\n\t"
      "global_load_dwordx4 v[132:135], v244, s[58:59] sc1\n\t"
      "v_mfma_f32_16x16x32_bf16 v[204:207], v[180:183], v[40:43], v[204:207]\n\t"
      "v_and_b32_e32 v249, 0xffff0000, v115\n\t"
      "v_mfma_f32_16x16x32_bf16 v[208:211], v[180:183], v[44:47], v[208:211]\n\t"
      "v_add_f32_e32 v248, v254, v248\n\t"
      "v_add_f32_e32 v249, v254, v249\n\t"
      "s_waitcnt vmcnt(4)\n\t"
      "v_mfma_f32_16x16x32_bf16 v[196:199], v[184:187], v[48:51], v[196:199]\n\t"
      "s_waitcnt lgkmcnt(4)\n\t"
      "v_mfma_f32_16x16x32_bf16 v[200:203], v[184:187], v[52:55], v[200:203]\n\t"
      "v_add_f32_e32 v228, v228, v230\n\t"
      "v_add_f32_e32 v232, v232, v234\n\t"
      "s_add_u32 s58, s74, s41\n\t"
      "s_addc_u32 s59, s75, 0\n\t"
      "global_load_dwordx4 v[136:139], v244, s[58:59] sc1\n\t"
      "v_mfma_f32_16x16x32_bf16 v[204:207], v[184:187], v[56:59], v[204:207]\n\t"
      "v_add_f32_e32 v229, v229, v231\n\t"
      "v_mfma_f32_16x16x32_bf16 v[208:211], v[184:187], v[60:63], v[208:211]\n\t"
      "v_add_f32_e32 v233, v233, v235\n\t"
      "v_add_f32_e32 v228, v228, v232\n\t"
      "s_waitcnt vmcnt(4)\n\t"
      "v_mfma_f32_16x16x32_bf16 v[196:199], v[188:191], v[64:67], v[196:199]\n\t"
      "v_add_f32_e32 v229, v229, v233\n\t"
      "v_mfma_f32_16x16x32_bf16 v[200:203], v[188:191], v[68:71], v[200:203]\n\t"
      "s_waitcnt lgkmcnt(0)\n\t"
      "v_add_f32_e32 v236, v236, v238\n\t"
      "s_add_u32 s58, s74, s42\n\t"
      "s_addc_u32 s59, s75, 0\n\t"
      "global_load_dwordx4 v[140:143], v244, s[58:59] sc1\n\t"
      "v_mfma_f32_16x16x32_bf16 v[204:207], v[188:191], v[72:75], v[204:207]\n\t"
      "v_add_f32_e32 v240, v240, v242\n\t"
      "v_mfma_f32_16x16x32_bf16 v[208:211], v[188:191], v[76:79], v[208:211]\n\t"
      "v_add_f32_e32 v237, v237, v239\n\t"
      "v_add_f32_e32 v241, v241, v243\n\t"
      "s_waitcnt vmcnt(4)\n\t"
      "v_mfma_f32_16x16x32_bf16 v[196:199], v[192:195], v[80:83], v[196:199]\n\t"
      "v_add_f32_e32 v228, v228, v248\n\t"
      "v_mfma_f32_16x16x32_bf16 v[200:203], v[192:195], v[84:87], v[200:203]\n\t"
      "v_add_f32_e32 v236, v236, v240\n\t"
      "v_add_f32_e32 v229, v229, v249\n\t"
      "s_add_u32 s58, s74, s43\n\t"
      "s_addc_u32 s59, s75, 0\n\t"
      "global_load_dwordx4 v[144:147], v244, s[58:59] sc1\n\t"
      "v_mfma_f32_16x16x32_bf16 v[204:207], v[192:195], v[88:91], v[204:207]\n\t"
      "v_add_f32_e32 v237, v237, v241\n\t"
      "v_mfma_f32_16x16x32_bf16 v[208:211], v[192:195], v[92:95], v[208:211]\n\t"
      "v_add_f32_e32 v228, v228, v236\n\t"
      "v_add_f32_e32 v229, v229, v237\n\t"
      "s_waitcnt vmcnt(4)\n\t"
      "v_mfma_f32_16x16x32_bf16 v[196:199], v[128:131], v[96:99], v[196:199]\n\t"
      "v_cvt_pk_bf16_f32 v232, v228, v229\n\t"
      "v_mfma_f32_16x16x32_bf16 v[200:203], v[128:131], v[100:103], v[200:203]\n\t"
      "s_mov_b32 vcc_lo, 0xeeeeeeee\n\t"
      "s_mov_b32 vcc_hi, 0xeeeeeeee\n\t"
      "v_cndmask_b32_dpp v124, v232, v124, vcc quad_perm:[0,0,0,0] row_mask:0xf bank_mask:0xf\n\t"
      "v_cndmask_b32_dpp v125, v232, v125, vcc quad_perm:[1,1,1,1] row_mask:0xf bank_mask:0xf\n\t"
      "v_cndmask_b32_dpp v126, v232, v126, vcc quad_perm:[2,2,2,2] row_mask:0xf bank_mask:0xf\n\t"
      "v_cndmask_b32_dpp v127, v232, v127, vcc quad_perm:[3,3,3,3] row_mask:0xf bank_mask:0xf\n\t"
      "s_add_u32 s58, s74, s44\n\t"
      "s_addc_u32 s59, s75, 0\n\t"
      "global_load_dwordx4 v[148:151], v244, s[58:59] sc1\n\t"
      "v_mfma_f32_16x16x32_bf16 v[204:207], v[128:131], v[104:107], v[204:207]\n\t"
      "v_mfma_f32_16x16x32_bf16 v[208:211], v[128:131], v[108:111], v[208:211]\n\t"
      "v_add_f32_e32 v251, v196, v197\n\t"
      "v_add_f32_e32 v251, v251, v198\n\t"
      "v_cmp_u_f32_e32 vcc, v251, v199\n\t"
      "s_cbranch_vccnz LA44R_%=\n\t"
      "LA44B_%=:\n\t"
      "ds_write_b128 v245, v[196:199]\n\t"
      "s_waitcnt vmcnt(4)\n\t"
      "v_mfma_f32_16x16x32_bf16 v[212:215], v[132:135], v[0:3], 0\n\t"
      "ds_write_b128 v245, v[200:203] offset:1024\n\t"
      "v_mfma_f32_16x16x32_bf16 v[216:219], v[132:135], v[4:7], 0\n\t"
      "ds_write_b128 v245, v[204:207] offset:2048\n\t"
      "s_add_u32 s58, s74, s45\n\t"
      "s_addc_u32 s59, s75, 0\n\t"
      "global_load_dwordx4 v[152:155], v244, s[58:59] sc1\n\t"
      "v_mfma_f32_16x16x32_bf16 v[220:223], v[132:135], v[8:11], 0\n\t"
      "ds_write_b128 v245, v[208:211] offset:3072\n\t"
      "v_mfma_f32_16x16x32_bf16 v[224:227], v[132:135], v[12:15], 0\n\t"
      "s_waitcnt vmcnt(4)\n\t"
      "v_mfma_f32_16x16x32_bf16 v[212:215], v[136:139], v[16:19], v[212:215]\n\t"
      "v_mfma_f32_16x16x32_bf16 v[216:219], v[136:139], v[20:23], v[216:219]\n\t"
      "s_add_u32 s58, s74, s46\n\t"
      "s_addc_u32 s59, s75, 0\n\t"
      "global_load_dwordx4 v[156:159], v244, s[58:59] sc1\n\t"
      "v_mfma_f32_16x16x32_bf16 v[220:223], v[136:139], v[24:27], v[220:223]\n\t"
      "s_waitcnt lgkmcnt(0)\n\t"
      "s_barrier\n\t"
      "ds_read_b64 v[228:229], v246\n\t"
      "ds_read_b64 v[230:231], v246 offset:4096\n\t"
      "v_mfma_f32_16x16x32_bf16 v[224:227], v[136:139], v[28:31], v[224:227]\n\t"
      "ds_read_b64 v[232:233], v246 offset:8192\n\t"
      "ds_read_b64 v[234:235], v246 offset:12288\n\t"
      "s_waitcnt vmcnt(4)\n\t"
      "v_mfma_f32_16x16x32_bf16 v[212:215], v[140:143], v[32:35], v[212:215]\n\t"
      "ds_read_b64 v[236:237], v246 offset:16384\n\t"
      "ds_read_b64 v[238:239], v246 offset:20480\n\t"
      "s_add_u32 s58, s76, s40\n\t"
      "s_addc_u32 s59, s77, 0\n\t"
      "global_load_dwordx4 v[160:163], v244, s[58:59] sc1\n\t"
      "v_mfma_f32_16x16x32_bf16 v[216:219], v[140:143], v[36:39], v[216:219]\n\t"
      "ds_read_b64 v[240:241], v246 offset:24576\n\t"
      "ds_read_b64 v[242:243], v246 offset:28672\n\t"
      "v_lshlrev_b32_e32 v248, 16, v116\n\t"
      "v_mfma_f32_16x16x32_bf16 v[220:223], v[140:143], v[40:43], v[220:223]\n\t"
      "v_and_b32_e32 v249, 0xffff0000, v116\n\t"
      "v_mfma_f32_16x16x32_bf16 v[224:227], v[140:143], v[44:47], v[224:227]\n\t"
      "v_add_f32_e32 v248, v254, v248\n\t"
      "v_add_f32_e32 v249, v254, v249\n\t"
      "s_waitcnt vmcnt(4)\n\t"
      "v_mfma_f32_16x16x32_bf16 v[212:215], v[144:147], v[48:51], v[212:215]\n\t"
      "s_waitcnt lgkmcnt(4)\n\t"
      "s_add_u32 s58, s76, s41\n\t"
      "s_addc_u32 s59, s77, 0\n\t"
      "global_load_dwordx4 v[164:167], v244, s[58:59] sc1\n\t"
      "v_mfma_f32_16x16x32_bf16 v[216:219], v[144:147], v[52:55], v[216:219]\n\t"
      "v_add_f32_e32 v228, v228, v230\n\t"
      "v_add_f32_e32 v232, v232, v234\n\t"
      "v_mfma_f32_16x16x32_bf16 v[220:223], v[144:147], v[56:59], v[220:223]\n\t"
      "v_add_f32_e32 v229, v229, v231\n\t"
      "v_mfma_f32_16x16x32_bf16 v[224:227], v[144:147], v[60:63], v[224:227]\n\t"
      "v_add_f32_e32 v233, v233, v235\n\t"
      "v_add_f32_e32 v228, v228, v232\n\t"
      "s_waitcnt vmcnt(4)\n\t"
      "v_mfma_f32_16x16x32_bf16 v[212:215], v[148:151], v[64:67], v[212:215]\n\t"
      "v_add_f32_e32 v229, v229, v233\n\t"
      "s_add_u32 s58, s76, s42\n\t"
      "s_addc_u32 s59, s77, 0\n\t"
      "global_load_dwordx4 v[168:171], v244, s[58:59] sc1\n\t"
      "v_mfma_f32_16x16x32_bf16 v[216:219], v[148:151], v[68:71], v[216:219]\n\t"
      "s_waitcnt lgkmcnt(0)\n\t"
      "v_add_f32_e32 v236, v236, v238\n\t"
      "v_mfma_f32_16x16x32_bf16 v[220:223], v[148:151], v[72:75], v[220:223]\n\t"
      "v_add_f32_e32 v240, v240, v242\n\t"
      "v_mfma_f32_16x16x32_bf16 v[224:227], v[148:151], v[76:79], v[224:227]\n\t"
      "v_add_f32_e32 v237, v237, v239\n\t"
      "v_add_f32_e32 v241, v241, v243\n\t"
      "s_waitcnt vmcnt(4)\n\t"
      "v_mfma_f32_16x16x32_bf16 v[212:215], v[152:155], v[80:83], v[212:215]\n\t"
      "v_add_f32_e32 v228, v228, v248\n\t"
      "s_add_u32 s58, s76, s43\n\t"
      "s_addc_u32 s59, s77, 0\n\t"
      "global_load_dwordx4 v[172:175], v244, s[58:59] sc1\n\t"
      "v_mfma_f32_16x16x32_bf16 v[216:219], v[152:155], v[84:87], v[216:219]\n\t"
      "v_add_f32_e32 v236, v236, v240\n\t"
      "v_add_f32_e32 v229, v229, v249\n\t"
      "v_mfma_f32_16x16x32_bf16 v[220:223], v[152:155], v[88:91], v[220:223]\n\t"
      "v_add_f32_e32 v237, v237, v241\n\t"
      "v_mfma_f32_16x16x32_bf16 v[224:227], v[152:155], v[92:95], v[224:227]\n\t"
      "v_add_f32_e32 v228, v228, v236\n\t"
      "v_add_f32_e32 v229, v229, v237\n\t"
      "s_waitcnt vmcnt(4)\n\t"
      "v_mfma_f32_16x16x32_bf16 v[212:215], v[156:159], v[96:99], v[212:215]\n\t"
      "v_cvt_pk_bf16_f32 v232, v228, v229\n\t"
      "s_add_u32 s58, s76, s44\n\t"
      "s_addc_u32 s59, s77, 0\n\t"
      "global_load_dwordx4 v[176:179], v244, s[58:59] sc1\n\t"
      "v_mfma_f32_16x16x32_bf16 v[216:219], v[156:159], v[100:103], v[216:219]\n\t"
      "s_mov_b32 vcc_lo, 0xdddddddd\n\t"
      "s_mov_b32 vcc_hi, 0xdddddddd\n\t"
      "v_cndmask_b32_dpp v124, v232, v124, vcc quad_perm:[0,0,0,0] row_mask:0xf bank_mask:0xf\n\t"
      "v_cndmask_b32_dpp v125, v232, v125, vcc quad_perm:[1,1,1,1] row_mask:0xf bank_mask:0xf\n\t"
      "v_cndmask_b32_dpp v126, v232, v126, vcc quad_perm:[2,2,2,2] row_mask:0xf bank_mask:0xf\n\t"
      "v_cndmask_b32_dpp v127, v232, v127, vcc quad_perm:[3,3,3,3] row_mask:0xf bank_mask:0xf\n\t"
      "v_mfma_f32_16x16x32_bf16 v[220:223], v[156:159], v[104:107], v[220:223]\n\t"
      "v_mfma_f32_16x16x32_bf16 v[224:227], v[156:159], v[108:111], v[224:227]\n\t"
      "v_add_f32_e32 v251, v212, v213\n\t"
      "v_add_f32_e32 v251, v251, v214\n\t"
      "v_cmp_u_f32_e32 vcc, v251, v215\n\t"
      "s_cbranch_vccnz LA45R_%=\n\t"
      "LA45B_%=:\n\t"
      "ds_write_b128 v245, v[212:215] offset:32768\n\t"
      "s_waitcnt vmcnt(4)\n\t"
      "v_mfma_f32_16x16x32_bf16 v[196:199], v[160:163], v[0:3], 0\n\t"
      "ds_write_b128 v245, v[216:219] offset:33792\n\t"
      "s_add_u32 s58, s76, s45\n\t"
      "s_addc_u32 s59, s77, 0\n\t"
      "global_load_dwordx4 v[180:183], v244, s[58:59] sc1\n\t"
      "v_mfma_f32_16x16x32_bf16 v[200:203], v[160:163], v[4:7], 0\n\t"
      "ds_write_b128 v245, v[220:223] offset:34816\n\t"
      "v_mfma_f32_16x16x32_bf16 v[204:207], v[160:163], v[8:11], 0\n\t"
      "ds_write_b128 v245, v[224:227] offset:35840\n\t"
      "v_mfma_f32_16x16x32_bf16 v[208:211], v[160:163], v[12:15], 0\n\t"
      "s_waitcnt vmcnt(4)\n\t"
      "v_mfma_f32_16x16x32_bf16 v[196:199], v[164:167], v[16:19], v[196:199]\n\t"
      "s_add_u32 s58, s76, s46\n\t"
      "s_addc_u32 s59, s77, 0\n\t"
      "global_load_dwordx4 v[184:187], v244, s[58:59] sc1\n\t"
      "v_mfma_f32_16x16x32_bf16 v[200:203], v[164:167], v[20:23], v[200:203]\n\t"
      "v_mfma_f32_16x16x32_bf16 v[204:207], v[164:167], v[24:27], v[204:207]\n\t"
      "s_waitcnt lgkmcnt(0)\n\t"
      "s_barrier\n\t"
      "ds_read_b64 v[228:229], v246 offset:32768\n\t"
      "ds_read_b64 v[230:231], v246 offset:36864\n\t"
      "v_mfma_f32_16x16x32_bf16 v[208:211], v[164:167], v[28:31], v[208:211]\n\t"
      "ds_read_b64 v[232:233], v246 offset:40960\n\t"
      "ds_read_b64 v[234:235], v246 offset:45056\n\t"
      "s_waitcnt vmcnt(4)\n\t"
      "v_mfma_f32_16x16x32_bf16 v[196:199], v[168:171], v[32:35], v[196:199]\n\t"
      "ds_read_b64 v[236:237], v246 offset:49152\n\t"
      "ds_read_b64 v[238:239], v246 offset:53248\n\t"
      "v_mfma_f32_16x16x32_bf16 v[200:203], v[168:171], v[36:39], v[200:203]\n\t"
      "ds_read_b64 v[240:241], v246 offset:57344\n\t"
      "ds_read_b64 v[242:243], v246 offset:61440\n\t"
      "v_lshlrev_b32_e32 v248, 16, v117\n\t"
      "v_and_b32_e32 v249, 0xffff0000, v117\n\t"
      "v_mfma_f32_16x16x32_bf16 v[204:207], v[168:171], v[40:43], v[204:207]\n\t"
      "v_add_f32_e32 v248, v254, v248\n\t"
      "v_add_f32_e32 v249, v254, v249\n\t"
      "v_mfma_f32_16x16x32_bf16 v[208:211], v[168:171], v[44:47], v[208:211]\n\t"
      "s_waitcnt lgkmcnt(4)\n\t"
      "v_add_f32_e32 v228, v228, v230\n\t"
      "v_add_f32_e32 v232, v232, v234\n\t"
      "s_waitcnt vmcnt(3)\n\t"
      "v_mfma_f32_16x16x32_bf16 v[196:199], v[172:175], v[48:51], v[196:199]\n\t"
      "v_add_f32_e32 v229, v229, v231\n\t"
      "v_add_f32_e32 v233, v233, v235\n\t"
      "v_mfma_f32_16x16x32_bf16 v[200:203], v[172:175], v[52:55], v[200:203]\n\t"
      "v_add_f32_e32 v228, v228, v232\n\t"
      "v_add_f32_e32 v229, v229, v233\n\t"
      "s_waitcnt lgkmcnt(0)\n\t"
      "v_mfma_f32_16x16x32_bf16 v[204:207], v[172:175], v[56:59], v[204:207]\n\t"
      "v_add_f32_e32 v236, v236, v238\n\t"
      "v_add_f32_e32 v240, v240, v242\n\t"
      "v_mfma_f32_16x16x32_bf16 v[208:211], v[172:175], v[60:63], v[208:211]\n\t"
      "v_add_f32_e32 v237, v237, v239\n\t"
      "v_add_f32_e32 v241, v241, v243\n\t"
      "v_add_f32_e32 v228, v228, v248\n\t"
      "s_waitcnt vmcnt(2)\n\t"
      "v_mfma_f32_16x16x32_bf16 v[196:199], v[176:179], v[64:67], v[196:199]\n\t"
      "v_add_f32_e32 v236, v236, v240\n\t"
      "v_add_f32_e32 v229, v229, v249\n\t"
      "v_mfma_f32_16x16x32_bf16 v[200:203], v[176:179], v[68:71], v[200:203]\n\t"
      "v_add_f32_e32 v237, v237, v241\n\t"
      "v_add_f32_e32 v228, v228, v236\n\t"
      "v_add_f32_e32 v229, v229, v237\n\t"
      "v_mfma_f32_16x16x32_bf16 v[204:207], v[176:179], v[72:75], v[204:207]\n\t"
      "v_cvt_pk_bf16_f32 v232, v228, v229\n\t"
      "s_mov_b32 vcc_lo, 0xbbbbbbbb\n\t"
      "s_mov_b32 vcc_hi, 0xbbbbbbbb\n\t"
      "v_cndmask_b32_dpp v124, v232, v124, vcc quad_perm:[0,0,0,0] row_mask:0xf bank_mask:0xf\n\t"
      "v_cndmask_b32_dpp v125, v232, v125, vcc quad_perm:[1,1,1,1] row_mask:0xf bank_mask:0xf\n\t"
      "v_cndmask_b32_dpp v126, v232, v126, vcc quad_perm:[2,2,2,2] row_mask:0xf bank_mask:0xf\n\t"
      "v_cndmask_b32_dpp v127, v232, v127, vcc quad_perm:[3,3,3,3] row_mask:0xf bank_mask:0xf\n\t"
      "v_mfma_f32_16x16x32_bf16 v[208:211], v[176:179], v[76:79], v[208:211]\n\t"
      "s_waitcnt vmcnt(1)\n\t"
      "v_mfma_f32_16x16x32_bf16 v[196:199], v[180:183], v[80:83], v[196:199]\n\t"
      "v_mfma_f32_16x16x32_bf16 v[200:203], v[180:183], v[84:87], v[200:203]\n\t"
      "v_mfma_f32_16x16x32_bf16 v[204:207], v[180:183], v[88:91], v[204:207]\n\t"
      "v_mfma_f32_16x16x32_bf16 v[208:211], v[180:183], v[92:95], v[208:211]\n\t"
      "s_waitcnt vmcnt(0)\n\t"
      "v_mfma_f32_16x16x32_bf16 v[196:199], v[184:187], v[96:99], v[196:199]\n\t"
      "v_mfma_f32_16x16x32_bf16 v[200:203], v[184:187], v[100:103], v[200:203]\n\t"
      "v_mfma_f32_16x16x32_bf16 v[204:207], v[184:187], v[104:107], v[204:207]\n\t"
      "v_mfma_f32_16x16x32_bf16 v[208:211], v[184:187], v[108:111], v[208:211]\n\t"
      "s_nop 4\n\t"
      "v_add_f32_e32 v251, v196, v197\n\t"
      "v_add_f32_e32 v251, v251, v198\n\t"
      "v_cmp_u_f32_e32 vcc, v251, v199\n\t"
      "s_cbranch_vccnz LA46R_%=\n\t"
      "LA46B_%=:\n\t"
      "ds_write_b128 v245, v[196:199]\n\t"
      "s_mov_b64 exec, s[82:83]\n\t"
      "global_store_dwordx4 v247, v[120:123], s[18:19] sc1\n\t"
      "s_mov_b64 exec, -1\n\t"
      "ds_write_b128 v245, v[200:203] offset:1024\n\t"
      "ds_write_b128 v245, v[204:207] offset:2048\n\t"
      "ds_write_b128 v245, v[208:211] offset:3072\n\t"
      "s_waitcnt lgkmcnt(0)\n\t"
      "s_barrier\n\t"
      "ds_read_b64 v[228:229], v246\n\t"
      "ds_read_b64 v[230:231], v246 offset:4096\n\t"
      "ds_read_b64 v[232:233], v246 offset:8192\n\t"
      "ds_read_b64 v[234:235], v246 offset:12288\n\t"
      "ds_read_b64 v[236:237], v246 offset:16384\n\t"
      "ds_read_b64 v[238:239], v246 offset:20480\n\t"
      "ds_read_b64 v[240:241], v246 offset:24576\n\t"
      "ds_read_b64 v[242:243], v246 offset:28672\n\t"
      "v_lshlrev_b32_e32 v248, 16, v118\n\t"
      "v_and_b32_e32 v249, 0xffff0000, v118\n\t"
      "v_add_f32_e32 v248, v254, v248\n\t"
      "v_add_f32_e32 v249, v254, v249\n\t"
      "s_waitcnt lgkmcnt(4)\n\t"
      "v_add_f32_e32 v228, v228, v230\n\t"
      "v_add_f32_e32 v232, v232, v234\n\t"
      "v_add_f32_e32 v229, v229, v231\n\t"
      "v_add_f32_e32 v233, v233, v235\n\t"
      "v_add_f32_e32 v228, v228, v232\n\t"
      "v_add_f32_e32 v229, v229, v233\n\t"
      "s_waitcnt lgkmcnt(0)\n\t"
      "v_add_f32_e32 v236, v236, v238\n\t"
      "v_add_f32_e32 v240, v240, v242\n\t"
      "v_add_f32_e32 v237, v237, v239\n\t"
      "v_add_f32_e32 v241, v241, v243\n\t"
      "v_add_f32_e32 v228, v228, v248\n\t"
      "v_add_f32_e32 v236, v236, v240\n\t"
      "v_add_f32_e32 v229, v229, v249\n\t"
      "v_add_f32_e32 v237, v237, v241\n\t"
      "v_add_f32_e32 v228, v228, v236\n\t"
      "v_add_f32_e32 v229, v229, v237\n\t"
      "v_cvt_pk_bf16_f32 v232, v228, v229\n\t"
      "s_mov_b32 vcc_lo, 0x77777777\n\t"
      "s_mov_b32 vcc_hi, 0x77777777\n\t"
      "v_cndmask_b32_dpp v124, v232, v124, vcc quad_perm:[0,0,0,0] row_mask:0xf bank_mask:0xf\n\t"
      "v_cndmask_b32_dpp v125, v232, v125, vcc quad_perm:[1,1,1,1] row_mask:0xf bank_mask:0xf\n\t"
      "v_cndmask_b32_dpp v126, v232, v126, vcc quad_perm:[2,2,2,2] row_mask:0xf bank_mask:0xf\n\t"
      "v_cndmask_b32_dpp v127, v232, v127, vcc quad_perm:[3,3,3,3] row_mask:0xf bank_mask:0xf\n\t"
      "global_store_dwordx4 v247, v[124:127], s[62:63] sc1\n\t"
      "s_endpgm\n\t"
      "LO0R_%=:\n\t"
      "s_mov_b32 s80, 0\n\t"
      "LO0L_%=:\n\t"
      "s_add_u32 s80, s80, 1\n\t"
      "s_cmp_lt_u32 s80, 262144\n\t"
      "s_cbranch_scc0 LO0X_%=\n\t"
      "s_waitcnt vmcnt(0)\n\t"
      "s_sleep 1\n\t"
      "global_load_dwordx4 v[128:131], v244, s[42:43] offset:-2048 sc1\n\t"
      "global_load_dwordx4 v[132:135], v244, s[42:43] offset:-1024 sc1\n\t"
      "global_load_dwordx4 v[136:139], v244, s[42:43] sc1\n\t"
      "global_load_dwordx4 v[140:143], v244, s[42:43] offset:1024 sc1\n\t"
      "global_load_dwordx4 v[144:147], v244, s[42:43] offset:2048 sc1\n\t"
      "global_load_dwordx4 v[148:151], v244, s[42:43] offset:3072 sc1\n\t"
      "global_load_dwordx4 v[152:155], v244, s[44:45] offset:-4096 sc1\n\t"
      "global_load_dwordx4 v[156:159], v244, s[44:45] offset:-3072 sc1\n\t"
      "global_load_dwordx4 v[160:163], v244, s[44:45] offset:-2048 sc1\n\t"
      "global_load_dwordx4 v[164:167], v244, s[44:45] offset:-1024 sc1\n\t"
      "global_load_dwordx4 v[168:171], v244, s[44:45] sc1\n\t"
      "global_load_dwordx4 v[172:175], v244, s[44:45] offset:1024 sc1\n\t"
      "global_load_dwordx4 v[176:179], v244, s[44:45] offset:2048 sc1\n\t"
      "global_load_dwordx4 v[180:183], v244, s[44:45] offset:3072 sc1\n\t"
      "global_load_dwordx4 v[184:187], v244, s[42:43] offset:-4096 sc1\n\t"
      "global_load_dwordx4 v[188:191], v244, s[42:43] offset:-3072 sc1\n\t"
      "s_waitcnt vmcnt(0)\n\t"
      "v_max3_u32 v232, v128, v129, v130\n\t"
      "v_max3_u32 v232, v232, v131, v132\n\t"
      "v_max3_u32 v232, v232, v133, v134\n\t"
      "v_max_u32_e32 v232, v232, v135\n\t"
      "v_cmp_eq_u32_e32 vcc, -1, v232\n\t"
      "s_cbranch_vccnz LO0L_%=\n\t"
      "LO0X_%=:\n\t"
      "s_branch LO0B_%=\n\t"
      "LO2R_%=:\n\t"
      "s_mov_b32 s80, 0\n\t"
      "LO2L_%=:\n\t"
      "s_add_u32 s80, s80, 1\n\t"
      "s_cmp_lt_u32 s80, 262144\n\t"
      "s_cbranch_scc0 LO2X_%=\n\t"
      "s_waitcnt vmcnt(0)\n\t"
      "s_sleep 1\n\t"
      "global_load_dwordx4 v[136:139], v244, s[42:43] sc1\n\t"
      "global_load_dwordx4 v[140:143], v244, s[42:43] offset:1024 sc1\n\t"
      "global_load_dwordx4 v[144:147], v244, s[42:43] offset:2048 sc1\n\t"
      "global_load_dwordx4 v[148:151], v244, s[42:43] offset:3072 sc1\n\t"
      "global_load_dwordx4 v[152:155], v244, s[44:45] offset:-4096 sc1\n\t"
      "global_load_dwordx4 v[156:159], v244, s[44:45] offset:-3072 sc1\n\t"
      "global_load_dwordx4 v[160:163], v244, s[44:45] offset:-2048 sc1\n\t"
      "global_load_dwordx4 v[164:167], v244, s[44:45] offset:-1024 sc1\n\t"
      "global_load_dwordx4 v[168:171], v244, s[44:45] sc1\n\t"
      "global_load_dwordx4 v[172:175], v244, s[44:45] offset:1024 sc1\n\t"
      "global_load_dwordx4 v[176:179], v244, s[44:45] offset:2048 sc1\n\t"
      "global_load_dwordx4 v[180:183], v244, s[44:45] offset:3072 sc1\n\t"
      "global_load_dwordx4 v[184:187], v244, s[42:43] offset:-4096 sc1\n\t"
      "global_load_dwordx4 v[188:191], v244, s[42:43] offset:-3072 sc1\n\t"
      "s_waitcnt vmcnt(0)\n\t"
      "v_max3_u32 v232, v136, v137, v138\n\t"
      "v_max3_u32 v232, v232, v139, v140\n\t"
      "v_max3_u32 v232, v232, v141, v142\n\t"
      "v_max_u32_e32 v232, v232, v143\n\t"
      "v_cmp_eq_u32_e32 vcc, -1, v232\n\t"
      "s_cbranch_vccnz LO2L_%=\n\t"
      "LO2X_%=:\n\t"
      "s_branch LO2B_%=\n\t"
      "LO4R_%=:\n\t"
      "s_mov_b32 s80, 0\n\t"
      "LO4L_%=:\n\t"
      "s_add_u32 s80, s80, 1\n\t"
      "s_cmp_lt_u32 s80, 262144\n\t"
      "s_cbranch_scc0 LO4X_%=\n\t"
      "s_waitcnt vmcnt(0)\n\t"
      "s_sleep 1\n\t"
      "global_load_dwordx4 v[144:147], v244, s[42:43] offset:2048 sc1\n\t"
      "global_load_dwordx4 v[148:151], v244, s[42:43] offset:3072 sc1\n\t"
      "global_load_dwordx4 v[152:155], v244, s[44:45] offset:-4096 sc1\n\t"
      "global_load_dwordx4 v[156:159], v244, s[44:45] offset:-3072 sc1\n\t"
      "global_load_dwordx4 v[160:163], v244, s[44:45] offset:-2048 sc1\n\t"
      "global_load_dwordx4 v[164:167], v244, s[44:45] offset:-1024 sc1\n\t"
      "global_load_dwordx4 v[168:171], v244, s[44:45] sc1\n\t"
      "global_load_dwordx4 v[172:175], v244, s[44:45] offset:1024 sc1\n\t"
      "global_load_dwordx4 v[176:179], v244, s[44:45] offset:2048 sc1\n\t"
      "global_load_dwordx4 v[180:183], v244, s[44:45] offset:3072 sc1\n\t"
      "global_load_dwordx4 v[184:187], v244, s[42:43] offset:-4096 sc1\n\t"
      "global_load_dwordx4 v[188:191], v244, s[42:43] offset:-3072 sc1\n\t"
      "s_waitcnt vmcnt(0)\n\t"
      "v_max3_u32 v232, v144, v145, v146\n\t"
      "v_max3_u32 v232, v232, v147, v148\n\t"
      "v_max3_u32 v232, v232, v149, v150\n\t"
      "v_max_u32_e32 v232, v232, v151\n\t"
      "v_cmp_eq_u32_e32 vcc, -1, v232\n\t"
      "s_cbranch_vccnz LO4L_%=\n\t"
      "LO4X_%=:\n\t"
      "s_branch LO4B_%=\n\t"
      "LO6R_%=:\n\t"
      "s_mov_b32 s80, 0\n\t"
      "LO6L_%=:\n\t"
      "s_add_u32 s80, s80, 1\n\t"
      "s_cmp_lt_u32 s80, 262144\n\t"
      "s_cbranch_scc0 LO6X_%=\n\t"
      "s_waitcnt vmcnt(0)\n\t"
      "s_sleep 1\n\t"
      "global_load_dwordx4 v[152:155], v244, s[44:45] offset:-4096 sc1\n\t"
      "global_load_dwordx4 v[156:159], v244, s[44:45] offset:-3072 sc1\n\t"
      "global_load_dwordx4 v[160:163], v244, s[44:45] offset:-2048 sc1\n\t"
      "global_load_dwordx4 v[164:167], v244, s[44:45] offset:-1024 sc1\n\t"
      "global_load_dwordx4 v[168:171], v244, s[44:45] sc1\n\t"
      "global_load_dwordx4 v[172:175], v244, s[44:45] offset:1024 sc1\n\t"
      "global_load_dwordx4 v[176:179], v244, s[44:45] offset:2048 sc1\n\t"
      "global_load_dwordx4 v[180:183], v244, s[44:45] offset:3072 sc1\n\t"
      "global_load_dwordx4 v[184:187], v244, s[42:43] offset:-4096 sc1\n\t"
      "global_load_dwordx4 v[188:191], v244, s[42:43] offset:-3072 sc1\n\t"
      "s_waitcnt vmcnt(0)\n\t"
      "v_max3_u32 v232, v152, v153, v154\n\t"
      "v_max3_u32 v232, v232, v155, v156\n\t"
      "v_max3_u32 v232, v232, v157, v158\n\t"
      "v_max_u32_e32 v232, v232, v159\n\t"
      "v_cmp_eq_u32_e32 vcc, -1, v232\n\t"
      "s_cbranch_vccnz LO6L_%=\n\t"
      "LO6X_%=:\n\t"
      "s_branch LO6B_%=\n\t"
      "LO8R_%=:\n\t"
      "s_mov_b32 s80, 0\n\t"
      "LO8L_%=:\n\t"
      "s_add_u32 s80, s80, 1\n\t"
      "s_cmp_lt_u32 s80, 262144\n\t"
      "s_cbranch_scc0 LO8X_%=\n\t"
      "s_waitcnt vmcnt(0)\n\t"
      "s_sleep 1\n\t"
      "global_load_dwordx4 v[160:163], v244, s[44:45] offset:-2048 sc1\n\t"
      "global_load_dwordx4 v[164:167], v244, s[44:45] offset:-1024 sc1\n\t"
      "global_load_dwordx4 v[168:171], v244, s[44:45] sc1\n\t"
      "global_load_dwordx4 v[172:175], v244, s[44:45] offset:1024 sc1\n\t"
      "global_load_dwordx4 v[176:179], v244, s[44:45] offset:2048 sc1\n\t"
      "global_load_dwordx4 v[180:183], v244, s[44:45] offset:3072 sc1\n\t"
      "global_load_dwordx4 v[184:187], v244, s[42:43] offset:-4096 sc1\n\t"
      "global_load_dwordx4 v[188:191], v244, s[42:43] offset:-3072 sc1\n\t"
      "s_waitcnt vmcnt(0)\n\t"
      "v_max3_u32 v232, v160, v161, v162\n\t"
      "v_max3_u32 v232, v232, v163, v164\n\t"
      "v_max3_u32 v232, v232, v165, v166\n\t"
      "v_max_u32_e32 v232, v232, v167\n\t"
      "v_cmp_eq_u32_e32 vcc, -1, v232\n\t"
      "s_cbranch_vccnz LO8L_%=\n\t"
      "LO8X_%=:\n\t"
      "s_branch LO8B_%=\n\t"
      "LO10R_%=:\n\t"
      "s_mov_b32 s80, 0\n\t"
      "LO10L_%=:\n\t"
      "s_add_u32 s80, s80, 1\n\t"
      "s_cmp_lt_u32 s80, 262144\n\t"
      "s_cbranch_scc0 LO10X_%=\n\t"
      "s_waitcnt vmcnt(0)\n\t"
      "s_sleep 1\n\t"
      "global_load_dwordx4 v[168:171], v244, s[44:45] sc1\n\t"
      "global_load_dwordx4 v[172:175], v244, s[44:45] offset:1024 sc1\n\t"
      "global_load_dwordx4 v[176:179], v244, s[44:45] offset:2048 sc1\n\t"
      "global_load_dwordx4 v[180:183], v244, s[44:45] offset:3072 sc1\n\t"
      "global_load_dwordx4 v[184:187], v244, s[42:43] offset:-4096 sc1\n\t"
      "global_load_dwordx4 v[188:191], v244, s[42:43] offset:-3072 sc1\n\t"
      "s_waitcnt vmcnt(0)\n\t"
      "v_max3_u32 v232, v168, v169, v170\n\t"
      "v_max3_u32 v232, v232, v171, v172\n\t"
      "v_max3_u32 v232, v232, v173, v174\n\t"
      "v_max_u32_e32 v232, v232, v175\n\t"
      "v_cmp_eq_u32_e32 vcc, -1, v232\n\t"
      "s_cbranch_vccnz LO10L_%=\n\t"
      "LO10X_%=:\n\t"
      "s_branch LO10B_%=\n\t"
      "LO12R_%=:\n\t"
      "s_mov_b32 s80, 0\n\t"
      "LO12L_%=:\n\t"
      "s_add_u32 s80, s80, 1\n\t"
      "s_cmp_lt_u32 s80, 262144\n\t"
      "s_cbranch_scc0 LO12X_%=\n\t"
      "s_waitcnt vmcnt(0)\n\t"
      "s_sleep 1\n\t"
      "global_load_dwordx4 v[176:179], v244, s[44:45] offset:2048 sc1\n\t"
      "global_load_dwordx4 v[180:183], v244, s[44:45] offset:3072 sc1\n\t"
      "global_load_dwordx4 v[184:187], v244, s[42:43] offset:-4096 sc1\n\t"
      "global_load_dwordx4 v[188:191], v244, s[42:43] offset:-3072 sc1\n\t"
      "s_waitcnt vmcnt(0)\n\t"
      "v_max3_u32 v232, v176, v177, v178\n\t"
      "v_max3_u32 v232, v232, v179, v180\n\t"
      "v_max3_u32 v232, v232, v181, v182\n\t"
      "v_max_u32_e32 v232, v232, v183\n\t"
      "v_cmp_eq_u32_e32 vcc, -1, v232\n\t"
      "s_cbranch_vccnz LO12L_%=\n\t"
      "LO12X_%=:\n\t"
      "s_branch LO12B_%=\n\t"
      "LO14R_%=:\n\t"
      "s_mov_b32 s80, 0\n\t"
      "LO14L_%=:\n\t"
      "s_add_u32 s80, s80, 1\n\t"
      "s_cmp_lt_u32 s80, 262144\n\t"
      "s_cbranch_scc0 LO14X_%=\n\t"
      "s_waitcnt vmcnt(0)\n\t"
      "s_sleep 1\n\t"
      "global_load_dwordx4 v[184:187], v244, s[42:43] offset:-4096 sc1\n\t"
      "global_load_dwordx4 v[188:191], v244, s[42:43] offset:-3072 sc1\n\t"
      "s_waitcnt vmcnt(0)\n\t"
      "v_max3_u32 v232, v184, v185, v186\n\t"
      "v_max3_u32 v232, v232, v187, v188\n\t"
      "v_max3_u32 v232, v232, v189, v190\n\t"
      "v_max_u32_e32 v232, v232, v191\n\t"
      "v_cmp_eq_u32_e32 vcc, -1, v232\n\t"
      "s_cbranch_vccnz LO14L_%=\n\t"
      "LO14X_%=:\n\t"
      "s_branch LO14B_%=\n\t"
      "LGX_%=:\n\t"
      "v_max3_u32 v232, v128, v129, v130\n\t"
      "v_max3_u32 v232, v232, v131, v132\n\t"
      "v_max3_u32 v232, v232, v133, v134\n\t"
      "v_max3_u32 v232, v232, v135, v136\n\t"
      "v_max3_u32 v232, v232, v137, v138\n\t"
      "v_max3_u32 v232, v232, v139, v140\n\t"
      "v_max3_u32 v232, v232, v141, v142\n\t"
      "v_max3_u32 v232, v232, v143, v144\n\t"
      "v_max3_u32 v232, v232, v145, v146\n\t"
      "v_max3_u32 v232, v232, v147, v148\n\t"
      "v_max3_u32 v232, v232, v149, v150\n\t"
      "v_max3_u32 v232, v232, v151, v152\n\t"
      "v_max3_u32 v232, v232, v153, v154\n\t"
      "v_max3_u32 v232, v232, v155, v156\n\t"
      "v_max3_u32 v232, v232, v157, v158\n\t"
      "v_max3_u32 v232, v232, v159, v160\n\t"
      "v_max3_u32 v232, v232, v161, v162\n\t"
      "v_max3_u32 v232, v232, v163, v164\n\t"
      "v_max3_u32 v232, v232, v165, v166\n\t"
      "v_max3_u32 v232, v232, v167, v168\n\t"
      "v_max3_u32 v232, v232, v169, v170\n\t"
      "v_max3_u32 v232, v232, v171, v172\n\t"
      "v_max3_u32 v232, v232, v173, v174\n\t"
      "v_max3_u32 v232, v232, v175, v176\n\t"
      "v_max3_u32 v232, v232, v177, v178\n\t"
      "v_max3_u32 v232, v232, v179, v180\n\t"
      "v_max3_u32 v232, v232, v181, v182\n\t"
      "v_max3_u32 v232, v232, v183, v184\n\t"
      "v_max3_u32 v232, v232, v185, v186\n\t"
      "v_max3_u32 v232, v232, v187, v188\n\t"
      "v_max3_u32 v232, v232, v189, v190\n\t"
      "v_max_u32_e32 v232, v232, v191\n\t"
      "v_cmp_eq_u32_e32 vcc, -1, v232\n\t"
      "s_cbranch_vccz LGB_%=\n\t"
      "LGYR_%=:\n\t"
      "s_mov_b32 s80, 0\n\t"
      "LGYL_%=:\n\t"
      "s_add_u32 s80, s80, 1\n\t"
      "s_cmp_lt_u32 s80, 262144\n\t"
      "s_cbranch_scc0 LGYX_%=\n\t"
      "s_sleep 1\n\t"
      "global_load_dwordx4 v[128:131], v244, s[42:43] offset:-2048 sc1\n\t"
      "global_load_dwordx4 v[132:135], v244, s[42:43] offset:-1024 sc1\n\t"
      "global_load_dwordx4 v[136:139], v244, s[42:43] sc1\n\t"
      "global_load_dwordx4 v[140:143], v244, s[42:43] offset:1024 sc1\n\t"
      "global_load_dwordx4 v[144:147], v244, s[42:43] offset:2048 sc1\n\t"
      "global_load_dwordx4 v[148:151], v244, s[42:43] offset:3072 sc1\n\t"
      "global_load_dwordx4 v[152:155], v244, s[44:45] offset:-4096 sc1\n\t"
      "global_load_dwordx4 v[156:159], v244, s[44:45] offset:-3072 sc1\n\t"
      "global_load_dwordx4 v[160:163], v244, s[44:45] offset:-2048 sc1\n\t"
      "global_load_dwordx4 v[164:167], v244, s[44:45] offset:-1024 sc1\n\t"
      "global_load_dwordx4 v[168:171], v244, s[44:45] sc1\n\t"
      "global_load_dwordx4 v[172:175], v244, s[44:45] offset:1024 sc1\n\t"
      "global_load_dwordx4 v[176:179], v244, s[44:45] offset:2048 sc1\n\t"
      "global_load_dwordx4 v[180:183], v244, s[44:45] offset:3072 sc1\n\t"
      "global_load_dwordx4 v[184:187], v244, s[42:43] offset:-4096 sc1\n\t"
      "global_load_dwordx4 v[188:191], v244, s[42:43] offset:-3072 sc1\n\t"
      "s_waitcnt vmcnt(0)\n\t"
      "v_max3_u32 v232, v128, v129, v130\n\t"
      "v_max3_u32 v232, v232, v131, v132\n\t"
      "v_max3_u32 v232, v232, v133, v134\n\t"
      "v_max3_u32 v232, v232, v135, v136\n\t"
      "v_max3_u32 v232, v232, v137, v138\n\t"
      "v_max3_u32 v232, v232, v139, v140\n\t"
      "v_max3_u32 v232, v232, v141, v142\n\t"
      "v_max3_u32 v232, v232, v143, v144\n\t"
      "v_max3_u32 v232, v232, v145, v146\n\t"
      "v_max3_u32 v232, v232, v147, v148\n\t"
      "v_max3_u32 v232, v232, v149, v150\n\t"
      "v_max3_u32 v232, v232, v151, v152\n\t"
      "v_max3_u32 v232, v232, v153, v154\n\t"
      "v_max3_u32 v232, v232, v155, v156\n\t"
      "v_max3_u32 v232, v232, v157, v158\n\t"
      "v_max3_u32 v232, v232, v159, v160\n\t"
      "v_max3_u32 v232, v232, v161, v162\n\t"
      "v_max3_u32 v232, v232, v163, v164\n\t"
      "v_max3_u32 v232, v232, v165, v166\n\t"
      "v_max3_u32 v232, v232, v167, v168\n\t"
      "v_max3_u32 v232, v232, v169, v170\n\t"
      "v_max3_u32 v232, v232, v171, v172\n\t"
      "v_max3_u32 v232, v232, v173, v174\n\t"
      "v_max3_u32 v232, v232, v175, v176\n\t"
      "v_max3_u32 v232, v232, v177, v178\n\t"
      "v_max3_u32 v232, v232, v179, v180\n\t"
      "v_max3_u32 v232, v232, v181, v182\n\t"
      "v_max3_u32 v232, v232, v183, v184\n\t"
      "v_max3_u32 v232, v232, v185, v186\n\t"
      "v_max3_u32 v232, v232, v187, v188\n\t"
      "v_max3_u32 v232, v232, v189, v190\n\t"
      "v_max_u32_e32 v232, v232, v191\n\t"
      "v_cmp_eq_u32_e32 vcc, -1, v232\n\t"
      "s_cbranch_vccnz LGYL_%=\n\t"
      "LGYX_%=:\n\t"
      "s_cbranch_vccnz LGB_%=\n\t"
      "ds_read_b64_tr_b16 v[0:1], v216 offset:7168\n\t"
      "ds_read_b64_tr_b16 v[2:3], v220 offset:7168\n\t"
      "ds_read_b64_tr_b16 v[4:5], v217 offset:7168\n\t"
      "ds_read_b64_tr_b16 v[6:7], v221 offset:7168\n\t"
      "ds_read_b64_tr_b16 v[8:9], v218 offset:7168\n\t"
      "ds_read_b64_tr_b16 v[10:11], v222 offset:7168\n\t"
      "ds_read_b64_tr_b16 v[12:13], v219 offset:7168\n\t"
      "ds_read_b64_tr_b16 v[14:15], v223 offset:7168\n\t"
      "ds_read_b64_tr_b16 v[16:17], v218 offset:10752\n\t"
      "ds_read_b64_tr_b16 v[18:19], v222 offset:10752\n\t"
      "ds_read_b64_tr_b16 v[20:21], v219 offset:10752\n\t"
      "ds_read_b64_tr_b16 v[22:23], v223 offset:10752\n\t"
      "ds_read_b64_tr_b16 v[24:25], v216 offset:10752\n\t"
      "ds_read_b64_tr_b16 v[26:27], v220 offset:10752\n\t"
      "ds_read_b64_tr_b16 v[28:29], v217 offset:10752\n\t"
      "ds_read_b64_tr_b16 v[30:31], v221 offset:10752\n\t"
      "ds_read_b64_tr_b16 v[32:33], v216 offset:14336\n\t"
      "ds_read_b64_tr_b16 v[34:35], v220 offset:14336\n\t"
      "ds_read_b64_tr_b16 v[36:37], v217 offset:14336\n\t"
      "ds_read_b64_tr_b16 v[38:39], v221 offset:14336\n\t"
      "ds_read_b64_tr_b16 v[40:41], v218 offset:14336\n\t"
      "ds_read_b64_tr_b16 v[42:43], v222 offset:14336\n\t"
      "ds_read_b64_tr_b16 v[44:45], v219 offset:14336\n\t"
      "ds_read_b64_tr_b16 v[46:47], v223 offset:14336\n\t"
      "ds_read_b64_tr_b16 v[48:49], v218 offset:17920\n\t"
      "ds_read_b64_tr_b16 v[50:51], v222 offset:17920\n\t"
      "ds_read_b64_tr_b16 v[52:53], v219 offset:17920\n\t"
      "ds_read_b64_tr_b16 v[54:55], v223 offset:17920\n\t"
      "ds_read_b64_tr_b16 v[56:57], v216 offset:17920\n\t"
      "ds_read_b64_tr_b16 v[58:59], v220 offset:17920\n\t"
      "ds_read_b64_tr_b16 v[60:61], v217 offset:17920\n\t"
      "ds_read_b64_tr_b16 v[62:63], v221 offset:17920\n\t"
      "ds_read_b64_tr_b16 v[64:65], v216 offset:21504\n\t"
      "ds_read_b64_tr_b16 v[66:67], v220 offset:21504\n\t"
      "ds_read_b64_tr_b16 v[68:69], v217 offset:21504\n\t"
      "ds_read_b64_tr_b16 v[70:71], v221 offset:21504\n\t"
      "ds_read_b64_tr_b16 v[72:73], v218 offset:21504\n\t"
      "ds_read_b64_tr_b16 v[74:75], v222 offset:21504\n\t"
      "ds_read_b64_tr_b16 v[76:77], v219 offset:21504\n\t"
      "ds_read_b64_tr_b16 v[78:79], v223 offset:21504\n\t"
      "ds_read_b64_tr_b16 v[80:81], v218 offset:25088\n\t"
      "ds_read_b64_tr_b16 v[82:83], v222 offset:25088\n\t"
      "ds_read_b64_tr_b16 v[84:85], v219 offset:25088\n\t"
      "ds_read_b64_tr_b16 v[86:87], v223 offset:25088\n\t"
      "ds_read_b64_tr_b16 v[88:89], v216 offset:25088\n\t"
      "ds_read_b64_tr_b16 v[90:91], v220 offset:25088\n\t"
      "ds_read_b64_tr_b16 v[92:93], v217 offset:25088\n\t"
      "ds_read_b64_tr_b16 v[94:95], v221 offset:25088\n\t"
      "ds_read_b64_tr_b16 v[96:97], v216 offset:28672\n\t"
      "ds_read_b64_tr_b16 v[98:99], v220 offset:28672\n\t"
      "ds_read_b64_tr_b16 v[100:101], v217 offset:28672\n\t"
      "ds_read_b64_tr_b16 v[102:103], v221 offset:28672\n\t"
      "ds_read_b64_tr_b16 v[104:105], v218 offset:28672\n\t"
      "ds_read_b64_tr_b16 v[106:107], v222 offset:28672\n\t"
      "ds_read_b64_tr_b16 v[108:109], v219 offset:28672\n\t"
      "ds_read_b64_tr_b16 v[110:111], v223 offset:28672\n\t"
      "ds_read_b64_tr_b16 v[112:113], v218 offset:32256\n\t"
      "ds_read_b64_tr_b16 v[114:115], v222 offset:32256\n\t"
      "ds_read_b64_tr_b16 v[116:117], v219 offset:32256\n\t"
      "ds_read_b64_tr_b16 v[118:119], v223 offset:32256\n\t"
      "ds_read_b64_tr_b16 v[120:121], v216 offset:32256\n\t"
      "ds_read_b64_tr_b16 v[122:123], v220 offset:32256\n\t"
      "ds_read_b64_tr_b16 v[124:125], v217 offset:32256\n\t"
      "ds_read_b64_tr_b16 v[126:127], v221 offset:32256\n\t"
      "v_lshlrev_b32_e32 v192, 16, v208\n\t"
      "v_and_b32_e32 v193, 0xffff0000, v208\n\t"
      "v_lshlrev_b32_e32 v194, 16, v209\n\t"
      "v_and_b32_e32 v195, 0xffff0000, v209\n\t"
      "v_lshlrev_b32_e32 v196, 16, v210\n\t"
      "v_and_b32_e32 v197, 0xffff0000, v210\n\t"
      "v_lshlrev_b32_e32 v198, 16, v211\n\t"
      "v_and_b32_e32 v199, 0xffff0000, v211\n\t"
      "v_lshlrev_b32_e32 v200, 16, v212\n\t"
      "v_and_b32_e32 v201, 0xffff0000, v212\n\t"
      "v_lshlrev_b32_e32 v202, 16, v213\n\t"
      "v_and_b32_e32 v203, 0xffff0000, v213\n\t"
      "v_lshlrev_b32_e32 v204, 16, v214\n\t"
      "v_and_b32_e32 v205, 0xffff0000, v214\n\t"
      "v_lshlrev_b32_e32 v206, 16, v215\n\t"
      "v_and_b32_e32 v207, 0xffff0000, v215\n\t"
      "s_waitcnt lgkmcnt(0)\n\t"
      "s_branch LGM_%=\n\t"
      "LSR_%=:\n\t"
      "s_mov_b32 s80, 0\n\t"
      "LSL_%=:\n\t"
      "s_add_u32 s80, s80, 1\n\t"
      "s_cmp_lt_u32 s80, 262144\n\t"
      "s_cbranch_scc0 LSX_%=\n\t"
      "s_sleep 1\n\t"
      "global_load_dwordx2 v[148:149], v231, s[46:47] sc1\n\t"
      "global_load_dwordx2 v[150:151], v231, s[46:47] offset:384 sc1\n\t"
      "s_waitcnt vmcnt(0)\n\t"
      "v_max3_u32 v232, v148, v149, v150\n\t"
      "v_max_u32_e32 v232, v232, v151\n\t"
      "v_cmp_eq_u32_e32 vcc, -1, v232\n\t"
      "s_cbranch_vccnz LSL_%=\n\t"
      "LSX_%=:\n\t"
      "s_branch LSB_%=\n\t"
      "LF1R_%=:\n\t"
      "s_mov_b32 s80, 0\n\t"
      "LF1L_%=:\n\t"
      "s_add_u32 s80, s80, 1\n\t"
      "s_cmp_lt_u32 s80, 262144\n\t"
      "s_cbranch_scc0 LF1X_%=\n\t"
      "s_sleep 2\n\t"
      "global_load_dwordx2 v[138:139], v136, s[60:61] sc1\n\t"
      "s_waitcnt vmcnt(0)\n\t"
      "v_cmp_ne_u64_e32 vcc, s[22:23], v[138:139]\n\t"
      "s_cbranch_vccnz LF1L_%=\n\t"
      "LF1X_%=:\n\t"
      "s_branch LF1B_%=\n\t"
      "LF4R_%=:\n\t"
      "s_mov_b32 s80, 0\n\t"
      "LF4L_%=:\n\t"
      "s_add_u32 s80, s80, 1\n\t"
      "s_cmp_lt_u32 s80, 262144\n\t"
      "s_cbranch_scc0 LF4X_%=\n\t"
      "s_sleep 2\n\t"
      "global_load_dwordx2 v[138:139], v137, s[60:61] sc1\n\t"
      "s_waitcnt vmcnt(0)\n\t"
      "v_cmp_ne_u64_e32 vcc, s[22:23], v[138:139]\n\t"
      "s_cbranch_vccnz LF4L_%=\n\t"
      "LF4X_%=:\n\t"
      "s_branch LF4B_%=\n\t"
      "L4_%=:\n\t"
      "s_lshl2_add_u32 s72, s31, s24\n\t"
      "s_sub_u32 s72, s72, 56\n\t"
      "s_mov_b32 m0, 64512\n\t"
      "s_lshl1_add_u32 s72, s72, 0\n\t"
      "s_mul_i32 s72, s72, 65536\n\t"
      "s_and_b32 s73, s2, 7\n\t"
      "s_mul_i32 s73, s73, 512\n\t"
      "s_and_b32 s73, s73, 0xffffff80\n\t"
      "s_add_u32 s72, s72, s73\n\t"
      "v_bfe_u32 v141, v192, 2, 3\n\t"
      "v_mul_u32_u24_e32 v141, 4096, v141\n\t"
      "v_lshl_add_u32 v141, v196, 7, v141\n\t"
      "v_add_u32_e32 v141, s72, v141\n\t"
      "global_load_lds_dword v141, s[14:15]\n\t"
      "s_waitcnt vmcnt(1)\n\t"
      "s_barrier\n\t"
      "s_barrier\n\t"
      "s_lshl2_add_u32 s72, s31, s24\n\t"
      "s_sub_u32 s72, s72, 56\n\t"
      "s_mov_b32 m0, 64512\n\t"
      "s_cmp_ge_u32 s72, 64\n\t"
      "s_cselect_b32 s73, 8, 0\n\t"
      "s_sub_u32 s73, s72, s73\n\t"
      "s_and_b32 s73, s73, 0x38\n\t"
      "s_add_u32 s72, s72, s73\n\t"
      "s_add_u32 s72, s72, 8\n\t"
      "s_mul_i32 s72, s72, 57344\n\t"
      "s_and_b32 s73, s2, 7\n\t"
      "s_mul_i32 s73, s73, 448\n\t"
      "s_and_b32 s73, s73, 0xffffff80\n\t"
      "s_add_u32 s72, s72, s73\n\t"
      "v_bfe_u32 v141, v192, 2, 4\n\t"
      "v_mul_u32_u24_e32 v141, 3584, v141\n\t"
      "v_lshl_add_u32 v141, v196, 7, v141\n\t"
      "v_add_u32_e32 v141, s72, v141\n\t"
      "global_load_lds_dword v141, s[10:11]\n\t"
      "s_barrier\n\t"
      "s_cmp_lg_u32 s24, 3\n\t"
      "s_cbranch_scc1 SK12_%=\n\t"
      "global_load_dwordx2 v[138:139], v137, s[60:61] sc1\n\t"
      "SK12_%=:\n\t"
      "s_cmp_lg_u32 s24, 3\n\t"
      "s_cbranch_scc1 LF4B_%=\n\t"
      "s_waitcnt vmcnt(0)\n\t"
      "v_cmp_ne_u64_e32 vcc, s[22:23], v[138:139]\n\t"
      "s_cbranch_vccnz LF4R_%=\n\t"
      "LF4B_%=:\n\t"
      "s_barrier\n\t"
      "s_waitcnt vmcnt(0)\n\t"
      "s_barrier\n\t"
      "s_branch G4_%=\n\t"
      "LF2R_%=:\n\t"
      "s_mov_b32 s80, 0\n\t"
      "LF2L_%=:\n\t"
      "s_add_u32 s80, s80, 1\n\t"
      "s_cmp_lt_u32 s80, 262144\n\t"
      "s_cbranch_scc0 LF2X_%=\n\t"
      "s_sleep 2\n\t"
      "global_load_dwordx2 v[138:139], v137, s[60:61] sc1\n\t"
      "s_waitcnt vmcnt(0)\n\t"
      "v_cmp_ne_u64_e32 vcc, s[22:23], v[138:139]\n\t"
      "s_cbranch_vccnz LF2L_%=\n\t"
      "LF2X_%=:\n\t"
      "s_branch LF2B_%=\n\t"
      "LF3R_%=:\n\t"
      "s_mov_b32 s80, 0\n\t"
      "LF3L_%=:\n\t"
      "s_add_u32 s80, s80, 1\n\t"
      "s_cmp_lt_u32 s80, 262144\n\t"
      "s_cbranch_scc0 LF3X_%=\n\t"
      "s_sleep 2\n\t"
      "global_load_dwordx2 v[138:139], v143, s[60:61] sc1\n\t"
      "s_waitcnt vmcnt(0)\n\t"
      "v_cmp_ne_u64_e32 vcc, s[22:23], v[138:139]\n\t"
      "s_cbranch_vccnz LF3L_%=\n\t"
      "LF3X_%=:\n\t"
      "s_branch LF3B_%=\n\t"
      "LF70R_%=:\n\t"
      "s_mov_b32 s80, 0\n\t"
      "LF70L_%=:\n\t"
      "s_add_u32 s80, s80, 1\n\t"
      "s_cmp_lt_u32 s80, 262144\n\t"
      "s_cbranch_scc0 LF70X_%=\n\t"
      "s_sleep 4\n\t"
      "global_load_dwordx2 v[138:139], v136, s[60:61] sc1\n\t"
      "s_waitcnt vmcnt(0)\n\t"
      "v_cmp_ne_u64_e32 vcc, s[22:23], v[138:139]\n\t"
      "s_cbranch_vccnz LF70L_%=\n\t"
      "LF70X_%=:\n\t"
      "s_branch LF70B_%=\n\t"
      "LF71R_%=:\n\t"
      "s_mov_b32 s80, 0\n\t"
      "LF71L_%=:\n\t"
      "s_add_u32 s80, s80, 1\n\t"
      "s_cmp_lt_u32 s80, 262144\n\t"
      "s_cbranch_scc0 LF71X_%=\n\t"
      "s_sleep 4\n\t"
      "global_load_dwordx2 v[138:139], v136, s[60:61] sc1\n\t"
      "s_waitcnt vmcnt(0)\n\t"
      "v_cmp_ne_u64_e32 vcc, s[22:23], v[138:139]\n\t"
      "s_cbranch_vccnz LF71L_%=\n\t"
      "LF71X_%=:\n\t"
      "s_branch LF71B_%=\n\t"
      "SPARE_%=:\n\t"
      "v_mov_b32_e32 v132, -1\n\t"
      "v_mov_b32_e32 v133, -1\n\t"
      "v_mov_b32_e32 v134, -1\n\t"
      "v_mov_b32_e32 v135, -1\n\t"
      "s_and_b32 s34, s25, 3\n\t"
      "s_lshl_b32 s34, s34, 15\n\t"
      "s_mul_i32 s35, s24, 917504\n\t"
      "s_add_u32 s34, s34, s35\n\t"
      "s_and_b32 s35, s2, 7\n\t"
      "s_lshl_b32 s35, s35, 9\n\t"
      "s_add_u32 s34, s34, s35\n\t"
      "v_and_b32_e32 v141, 0x1c0, v244\n\t"
      "v_lshlrev_b32_e32 v141, 6, v141\n\t"
      "v_and_b32_e32 v142, 0x30, v244\n\t"
      "v_lshl_add_u32 v141, v142, 3, v141\n\t"
      "v_add_u32_e32 v141, s34, v141\n\t"
      "s_mov_b32 m0, 64512\n\t"
      "s_lshr_b32 s34, s24, 1\n\t"
      "s_lshl_b32 s34, s34, 5\n\t"
      "s_add_u32 s34, s34, s25\n\t"
      "s_and_b32 s35, s24, 1\n\t"
      "s_lshl1_add_u32 s34, s34, s35\n\t"
      "s_lshl_b32 s34, s34, 10\n\t"
      "s_add_u32 s34, s34, 8388608\n\t"
      "s_add_u32 s40, s20, s34\n\t"
      "s_addc_u32 s41, s21, 0\n\t"
      "global_store_dwordx4 v244, v[132:135], s[40:41] sc1\n\t"
      "s_add_u32 s40, s40, 262144\n\t"
      "s_addc_u32 s41, s41, 0\n\t"
      "global_store_dwordx4 v244, v[132:135], s[40:41] sc1\n\t"
      "s_add_u32 s40, s40, 262144\n\t"
      "s_addc_u32 s41, s41, 0\n\t"
      "global_store_dwordx4 v244, v[132:135], s[40:41] sc1\n\t"
      "s_add_u32 s40, s40, 262144\n\t"
      "s_addc_u32 s41, s41, 0\n\t"
      "global_store_dwordx4 v244, v[132:135], s[40:41] sc1\n\t"
      "s_add_u32 s40, s40, 262144\n\t"
      "s_addc_u32 s41, s41, 0\n\t"
      "global_store_dwordx4 v244, v[132:135], s[40:41] sc1\n\t"
      "s_add_u32 s40, s40, 262144\n\t"
      "s_addc_u32 s41, s41, 0\n\t"
      "global_store_dwordx4 v244, v[132:135], s[40:41] sc1\n\t"
      "s_add_u32 s40, s40, 262144\n\t"
      "s_addc_u32 s41, s41, 0\n\t"
      "global_store_dwordx4 v244, v[132:135], s[40:41] sc1\n\t"
      "s_add_u32 s40, s40, 262144\n\t"
      "s_addc_u32 s41, s41, 0\n\t"
      "s_lshl_b32 s34, s25, 7\n\t"
      "s_add_u32 s34, s34, 16384\n\t"
      "v_mov_b32_e32 v131, s34\n\t"
      "s_waitcnt vmcnt(0)\n\t"
      "s_barrier\n\t"
      "s_cmp_lg_u32 s24, 0\n\t"
      "s_cbranch_scc1 SP0_%=\n\t"
      "s_mov_b64 exec, 1\n\t"
      "global_store_dwordx2 v131, v[128:129], s[60:61] sc1\n\t"
      "s_mov_b64 exec, -1\n\t"
      "SP0_%=:\n\t"
      "s_and_b32 s34, s25, 3\n\t"
      "s_lshl3_add_u32 s34, s34, s24\n\t"
      "s_cmp_lg_u32 s34, 19\n\t"
      "s_cbranch_scc1 SQ0_%=\n\t"
      "v_and_b32_e32 v136, 0x1f0, v244\n\t"
      "v_lshlrev_b32_e32 v131, 6, v136\n\t"
      "v_lshlrev_b32_e32 v136, 3, v136\n\t"
      "v_add_u32_e32 v136, 16384, v136\n\t"
      "s_and_b32 s34, s2, 7\n\t"
      "s_lshl_b32 s34, s34, 7\n\t"
      "s_add_u32 s34, s34, 32768\n\t"
      "v_add_u32_e32 v131, s34, v131\n\t"
      "s_cmp_lg_u32 s24, 3\n\t"
      "s_cbranch_scc1 SK15_%=\n\t"
      "global_load_dwordx2 v[138:139], v136, s[60:61] sc1\n\t"
      "SK15_%=:\n\t"
      "s_cmp_lg_u32 s24, 3\n\t"
      "s_cbranch_scc1 LF70B_%=\n\t"
      "s_waitcnt vmcnt(0)\n\t"
      "v_cmp_ne_u64_e32 vcc, s[22:23], v[138:139]\n\t"
      "s_cbranch_vccnz LF70R_%=\n\t"
      "LF70B_%=:\n\t"
      "global_store_dwordx2 v131, v[128:129], s[60:61] sc1\n\t"
      "SQ0_%=:\n\t"
      "s_barrier\n\t"
      "s_lshl3_add_u32 s34, s25, s24\n\t"
      "s_mul_i32 s34, s34, 7168\n\t"
      "s_add_u32 s34, s34, 10227712\n\t"
      "s_add_u32 s40, s20, s34\n\t"
      "s_addc_u32 s41, s21, 0\n\t"
      "global_store_dwordx4 v244, v[132:135], s[40:41] offset:-4096 sc1\n\t"
      "global_store_dwordx4 v244, v[132:135], s[40:41] offset:-3072 sc1\n\t"
      "global_store_dwordx4 v244, v[132:135], s[40:41] offset:-2048 sc1\n\t"
      "global_store_dwordx4 v244, v[132:135], s[40:41] offset:-1024 sc1\n\t"
      "global_store_dwordx4 v244, v[132:135], s[40:41] sc1\n\t"
      "global_store_dwordx4 v244, v[132:135], s[40:41] offset:1024 sc1\n\t"
      "global_store_dwordx4 v244, v[132:135], s[40:41] offset:2048 sc1\n\t"
      "s_lshl_b32 s34, s25, 7\n\t"
      "s_add_u32 s34, s34, 24576\n\t"
      "v_mov_b32_e32 v131, s34\n\t"
      "s_waitcnt vmcnt(0)\n\t"
      "s_barrier\n\t"
      "s_cmp_lg_u32 s24, 0\n\t"
      "s_cbranch_scc1 SP1_%=\n\t"
      "s_mov_b64 exec, 1\n\t"
      "global_store_dwordx2 v131, v[128:129], s[60:61] sc1\n\t"
      "s_mov_b64 exec, -1\n\t"
      "SP1_%=:\n\t"
      "s_and_b32 s34, s25, 3\n\t"
      "s_lshl3_add_u32 s34, s34, s24\n\t"
      "s_cmp_lg_u32 s34, 27\n\t"
      "s_cbranch_scc1 SQ1_%=\n\t"
      "v_and_b32_e32 v136, 0x1f0, v244\n\t"
      "v_lshlrev_b32_e32 v131, 6, v136\n\t"
      "v_lshlrev_b32_e32 v136, 3, v136\n\t"
      "v_add_u32_e32 v136, 24576, v136\n\t"
      "s_and_b32 s34, s2, 7\n\t"
      "s_lshl_b32 s34, s34, 7\n\t"
      "s_add_u32 s34, s34, 65536\n\t"
      "v_add_u32_e32 v131, s34, v131\n\t"
      "s_cmp_lg_u32 s24, 3\n\t"
      "s_cbranch_scc1 SK16_%=\n\t"
      "global_load_dwordx2 v[138:139], v136, s[60:61] sc1\n\t"
      "SK16_%=:\n\t"
      "s_cmp_lg_u32 s24, 3\n\t"
      "s_cbranch_scc1 LF71B_%=\n\t"
      "s_waitcnt vmcnt(0)\n\t"
      "v_cmp_ne_u64_e32 vcc, s[22:23], v[138:139]\n\t"
      "s_cbranch_vccnz LF71R_%=\n\t"
      "LF71B_%=:\n\t"
      "global_store_dwordx2 v131, v[128:129], s[60:61] sc1\n\t"
      "SQ1_%=:\n\t"
      "s_barrier\n\t"
      "s_and_b32 s35, s25, 3\n\t"
      "s_lshl2_add_u32 s31, s26, s35\n\t"
      "s_and_b32 s35, s2, 7\n\t"
      "s_mul_i32 s34, s35, 14\n\t"
      "s_lshl4_add_u32 s35, s35, -14\n\t"
      "s_cmp_lt_u32 s31, 14\n\t"
      "s_cselect_b32 s34, s34, s35\n\t"
      "s_cselect_b32 s36, 1, 0\n\t"
      "s_or_b32 s27, s27, s36\n\t"
      "s_add_u32 s34, s34, s31\n\t"
      "s_lshr_b32 s29, s34, 2\n\t"
      "s_and_b32 s32, s34, 3\n\t"
      "s_mul_i32 s30, s32, 7\n\t"
      "v_and_b32_e32 v199, 60, v192\n\t"
      "v_and_b32_e32 v200, 24, v192\n\t"
      "v_lshlrev_b32_e32 v202, 3, v196\n\t"
      "v_lshl_or_b32 v202, v200, 2, v202\n\t"
      "v_lshl_or_b32 v202, v195, 7, v202\n\t"
      "v_lshl_or_b32 v202, v193, 10, v202\n\t"
      "v_add_u32_e32 v224, s39, v202\n\t"
      "v_xor_b32_e32 v225, 32, v224\n\t"
      "v_xor_b32_e32 v226, 64, v224\n\t"
      "v_xor_b32_e32 v227, 96, v224\n\t"
      "s_lshl_b32 s34, s24, 12\n\t"
      "s_add_u32 s34, s34, 98304\n\t"
      "v_add_u32_e32 v245, s34, v244\n\t"
      "v_lshlrev_b32_e32 v199, 2, v199\n\t"
      "s_lshl_b32 s34, s24, 8\n\t"
      "v_lshl_add_u32 v247, v192, 2, s34\n\t"
      "v_and_b32_e32 v200, 2, v192\n\t"
      "v_mul_u32_u24_e32 v202, 576, v200\n\t"
      "v_xor_b32_e32 v199, v202, v199\n\t"
      "v_and_b32_e32 v202, 1, v192\n\t"
      "v_lshl_or_b32 v199, v202, 3, v199\n\t"
      "s_and_b32 s35, s34, 0x400\n\t"
      "s_add_u32 s34, s34, s35\n\t"
      "s_add_u32 s34, s34, 98304\n\t"
      "v_add_u32_e32 v246, s34, v199\n\t"
      "v_lshlrev_b32_e32 v199, 2, v202\n\t"
      "v_lshl_or_b32 v199, v200, 4, v199\n\t"
      "s_min_u32 s34, s29, 27\n\t"
      "s_lshl_b32 s34, s34, 7\n\t"
      "s_lshr_b32 s35, s24, 2\n\t"
      "s_lshl_b32 s35, s35, 6\n\t"
      "s_add_u32 s34, s34, s35\n\t"
      "s_lshr_b32 s35, s39, 9\n\t"
      "s_add_u32 s34, s34, s35\n\t"
      "v_add_u32_e32 v248, s34, v199\n\t"
      "s_add_u32 s48, s20, 8388608\n\t"
      "s_addc_u32 s49, s21, 0\n\t"
      "s_cmp_ge_u32 s31, 30\n\t"
      "s_cbranch_scc1 BYE_%=\n\t"
      "s_setprio 0\n\t"
      "s_waitcnt vmcnt(0)\n\t"
      "s_barrier\n\t"
      "s_branch G4_%=\n\t"
      "BYE_%=:\n\t"
      "s_endpgm\n\t"
      "LA30R_%=:\n\t"
      "s_mov_b32 s80, 0\n\t"
      "LA30L_%=:\n\t"
      "s_add_u32 s80, s80, 1\n\t"
      "s_cmp_lt_u32 s80, 262144\n\t"
      "s_cbranch_scc0 LA30X_%=\n\t"
      "s_waitcnt vmcnt(0)\n\t"
      "s_sleep 1\n\t"
      "global_load_dwordx4 v[128:131], v250, s[64:65] offset:-4096 sc1\n\t"
      "global_load_dwordx4 v[132:135], v250, s[64:65] offset:-3072 sc1\n\t"
      "global_load_dwordx4 v[136:139], v250, s[64:65] offset:-2048 sc1\n\t"
      "global_load_dwordx4 v[140:143], v250, s[64:65] offset:-1024 sc1\n\t"
      "global_load_dwordx4 v[144:147], v250, s[64:65] sc1\n\t"
      "global_load_dwordx4 v[148:151], v250, s[64:65] offset:1024 sc1\n\t"
      "global_load_dwordx4 v[152:155], v250, s[64:65] offset:2048 sc1\n\t"
      "global_load_dwordx4 v[156:159], v250, s[64:65] offset:3072 sc1\n\t"
      "s_waitcnt vmcnt(0)\n\t"
      "v_max3_u32 v251, v128, v129, v130\n\t"
      "v_max3_u32 v251, v251, v131, v132\n\t"
      "v_max3_u32 v251, v251, v133, v134\n\t"
      "v_max3_u32 v251, v251, v135, v136\n\t"
      "v_max3_u32 v251, v251, v137, v138\n\t"
      "v_max3_u32 v251, v251, v139, v140\n\t"
      "v_max3_u32 v251, v251, v141, v142\n\t"
      "v_max3_u32 v251, v251, v143, v144\n\t"
      "v_max3_u32 v251, v251, v145, v146\n\t"
      "v_max3_u32 v251, v251, v147, v148\n\t"
      "v_max3_u32 v251, v251, v149, v150\n\t"
      "v_max3_u32 v251, v251, v151, v152\n\t"
      "v_max3_u32 v251, v251, v153, v154\n\t"
      "v_max3_u32 v251, v251, v155, v156\n\t"
      "v_max3_u32 v251, v251, v157, v158\n\t"
      "v_max_u32_e32 v251, v251, v159\n\t"
      "v_cmp_eq_u32_e32 vcc, -1, v251\n\t"
      "s_cbranch_vccnz LA30L_%=\n\t"
      "LA30X_%=:\n\t"
      "s_branch LA30B_%=\n\t"
      "LF6R_%=:\n\t"
      "s_mov_b32 s80, 0\n\t"
      "LF6L_%=:\n\t"
      "s_add_u32 s80, s80, 1\n\t"
      "s_cmp_lt_u32 s80, 262144\n\t"
      "s_cbranch_scc0 LF6X_%=\n\t"
      "s_sleep 2\n\t"
      "global_load_dwordx2 v[228:229], v230, s[60:61] sc1\n\t"
      "s_waitcnt vmcnt(0)\n\t"
      "v_cmp_ne_u64_e32 vcc, s[22:23], v[228:229]\n\t"
      "s_cbranch_vccnz LF6L_%=\n\t"
      "LF6X_%=:\n\t"
      "s_branch LF6B_%=\n\t"
      "LA31R_%=:\n\t"
      "s_mov_b32 s80, 0\n\t"
      "LA31L_%=:\n\t"
      "s_add_u32 s80, s80, 1\n\t"
      "s_cmp_lt_u32 s80, 262144\n\t"
      "s_cbranch_scc0 LA31X_%=\n\t"
      "s_waitcnt vmcnt(0)\n\t"
      "s_sleep 1\n\t"
      "global_load_dwordx4 v[160:163], v250, s[66:67] offset:-4096 sc1\n\t"
      "global_load_dwordx4 v[164:167], v250, s[66:67] offset:-3072 sc1\n\t"
      "global_load_dwordx4 v[168:171], v250, s[66:67] offset:-2048 sc1\n\t"
      "global_load_dwordx4 v[172:175], v250, s[66:67] offset:-1024 sc1\n\t"
      "global_load_dwordx4 v[176:179], v250, s[66:67] sc1\n\t"
      "global_load_dwordx4 v[180:183], v250, s[66:67] offset:1024 sc1\n\t"
      "global_load_dwordx4 v[184:187], v250, s[66:67] offset:2048 sc1\n\t"
      "global_load_dwordx4 v[188:191], v250, s[66:67] offset:3072 sc1\n\t"
      "s_waitcnt vmcnt(0)\n\t"
      "v_max3_u32 v251, v160, v161, v162\n\t"
      "v_max3_u32 v251, v251, v163, v164\n\t"
      "v_max3_u32 v251, v251, v165, v166\n\t"
      "v_max3_u32 v251, v251, v167, v168\n\t"
      "v_max3_u32 v251, v251, v169, v170\n\t"
      "v_max3_u32 v251, v251, v171, v172\n\t"
      "v_max3_u32 v251, v251, v173, v174\n\t"
      "v_max3_u32 v251, v251, v175, v176\n\t"
      "v_max3_u32 v251, v251, v177, v178\n\t"
      "v_max3_u32 v251, v251, v179, v180\n\t"
      "v_max3_u32 v251, v251, v181, v182\n\t"
      "v_max3_u32 v251, v251, v183, v184\n\t"
      "v_max3_u32 v251, v251, v185, v186\n\t"
      "v_max3_u32 v251, v251, v187, v188\n\t"
      "v_max3_u32 v251, v251, v189, v190\n\t"
      "v_max_u32_e32 v251, v251, v191\n\t"
      "v_cmp_eq_u32_e32 vcc, -1, v251\n\t"
      "s_cbranch_vccnz LA31L_%=\n\t"
      "LA31X_%=:\n\t"
      "v_mfma_f32_16x16x32_bf16 v[224:227], v[0:3], v[160:163], 0\n\t"
      "v_mfma_f32_16x16x32_bf16 v[224:227], v[16:19], v[164:167], v[224:227]\n\t"
      "v_mfma_f32_16x16x32_bf16 v[224:227], v[32:35], v[168:171], v[224:227]\n\t"
      "v_mfma_f32_16x16x32_bf16 v[224:227], v[48:51], v[172:175], v[224:227]\n\t"
      "v_mfma_f32_16x16x32_bf16 v[224:227], v[64:67], v[176:179], v[224:227]\n\t"
      "v_mfma_f32_16x16x32_bf16 v[224:227], v[80:83], v[180:183], v[224:227]\n\t"
      "v_mfma_f32_16x16x32_bf16 v[224:227], v[96:99], v[184:187], v[224:227]\n\t"
      "v_mfma_f32_16x16x32_bf16 v[224:227], v[112:115], v[188:191], v[224:227]\n\t"
      "v_mfma_f32_16x16x32_bf16 v[208:211], v[4:7], v[160:163], 0\n\t"
      "v_mfma_f32_16x16x32_bf16 v[208:211], v[20:23], v[164:167], v[208:211]\n\t"
      "v_mfma_f32_16x16x32_bf16 v[208:211], v[36:39], v[168:171], v[208:211]\n\t"
      "s_nop 7\n\t"
      "s_branch LA31B_%=\n\t"
      "LA32R_%=:\n\t"
      "s_mov_b32 s80, 0\n\t"
      "LA32L_%=:\n\t"
      "s_add_u32 s80, s80, 1\n\t"
      "s_cmp_lt_u32 s80, 262144\n\t"
      "s_cbranch_scc0 LA32X_%=\n\t"
      "s_waitcnt vmcnt(0)\n\t"
      "s_sleep 1\n\t"
      "global_load_dwordx4 v[192:195], v250, s[68:69] offset:-4096 sc1\n\t"
      "global_load_dwordx4 v[196:199], v250, s[68:69] offset:-3072 sc1\n\t"
      "global_load_dwordx4 v[200:203], v250, s[68:69] offset:-2048 sc1\n\t"
      "global_load_dwordx4 v[204:207], v250, s[68:69] offset:-1024 sc1\n\t"
      "global_load_dwordx4 v[128:131], v250, s[68:69] sc1\n\t"
      "global_load_dwordx4 v[132:135], v250, s[68:69] offset:1024 sc1\n\t"
      "global_load_dwordx4 v[136:139], v250, s[68:69] offset:2048 sc1\n\t"
      "global_load_dwordx4 v[140:143], v250, s[68:69] offset:3072 sc1\n\t"
      "s_waitcnt vmcnt(0)\n\t"
      "v_max3_u32 v251, v192, v193, v194\n\t"
      "v_max3_u32 v251, v251, v195, v196\n\t"
      "v_max3_u32 v251, v251, v197, v198\n\t"
      "v_max3_u32 v251, v251, v199, v200\n\t"
      "v_max3_u32 v251, v251, v201, v202\n\t"
      "v_max3_u32 v251, v251, v203, v204\n\t"
      "v_max3_u32 v251, v251, v205, v206\n\t"
      "v_max3_u32 v251, v251, v207, v128\n\t"
      "v_max3_u32 v251, v251, v129, v130\n\t"
      "v_max3_u32 v251, v251, v131, v132\n\t"
      "v_max3_u32 v251, v251, v133, v134\n\t"
      "v_max3_u32 v251, v251, v135, v136\n\t"
      "v_max3_u32 v251, v251, v137, v138\n\t"
      "v_max3_u32 v251, v251, v139, v140\n\t"
      "v_max3_u32 v251, v251, v141, v142\n\t"
      "v_max_u32_e32 v251, v251, v143\n\t"
      "v_cmp_eq_u32_e32 vcc, -1, v251\n\t"
      "s_cbranch_vccnz LA32L_%=\n\t"
      "LA32X_%=:\n\t"
      "v_mfma_f32_16x16x32_bf16 v[220:223], v[0:3], v[192:195], 0\n\t"
      "v_mfma_f32_16x16x32_bf16 v[220:223], v[16:19], v[196:199], v[220:223]\n\t"
      "v_mfma_f32_16x16x32_bf16 v[220:223], v[32:35], v[200:203], v[220:223]\n\t"
      "v_mfma_f32_16x16x32_bf16 v[220:223], v[48:51], v[204:207], v[220:223]\n\t"
      "v_mfma_f32_16x16x32_bf16 v[220:223], v[64:67], v[128:131], v[220:223]\n\t"
      "v_mfma_f32_16x16x32_bf16 v[220:223], v[80:83], v[132:135], v[220:223]\n\t"
      "v_mfma_f32_16x16x32_bf16 v[220:223], v[96:99], v[136:139], v[220:223]\n\t"
      "v_mfma_f32_16x16x32_bf16 v[220:223], v[112:115], v[140:143], v[220:223]\n\t"
      "v_mfma_f32_16x16x32_bf16 v[224:227], v[4:7], v[192:195], 0\n\t"
      "v_mfma_f32_16x16x32_bf16 v[224:227], v[20:23], v[196:199], v[224:227]\n\t"
      "v_mfma_f32_16x16x32_bf16 v[224:227], v[36:39], v[200:203], v[224:227]\n\t"
      "s_nop 7\n\t"
      "s_branch LA32B_%=\n\t"
      "LA33R_%=:\n\t"
      "s_mov_b32 s80, 0\n\t"
      "LA33L_%=:\n\t"
      "s_add_u32 s80, s80, 1\n\t"
      "s_cmp_lt_u32 s80, 262144\n\t"
      "s_cbranch_scc0 LA33X_%=\n\t"
      "s_waitcnt vmcnt(0)\n\t"
      "s_sleep 1\n\t"
      "global_load_dwordx4 v[144:147], v250, s[70:71] offset:-4096 sc1\n\t"
      "global_load_dwordx4 v[148:151], v250, s[70:71] offset:-3072 sc1\n\t"
      "global_load_dwordx4 v[152:155], v250, s[70:71] offset:-2048 sc1\n\t"
      "global_load_dwordx4 v[156:159], v250, s[70:71] offset:-1024 sc1\n\t"
      "global_load_dwordx4 v[160:163], v250, s[70:71] sc1\n\t"
      "global_load_dwordx4 v[164:167], v250, s[70:71] offset:1024 sc1\n\t"
      "global_load_dwordx4 v[168:171], v250, s[70:71] offset:2048 sc1\n\t"
      "global_load_dwordx4 v[172:175], v250, s[70:71] offset:3072 sc1\n\t"
      "s_waitcnt vmcnt(0)\n\t"
      "v_max3_u32 v251, v144, v145, v146\n\t"
      "v_max3_u32 v251, v251, v147, v148\n\t"
      "v_max3_u32 v251, v251, v149, v150\n\t"
      "v_max3_u32 v251, v251, v151, v152\n\t"
      "v_max3_u32 v251, v251, v153, v154\n\t"
      "v_max3_u32 v251, v251, v155, v156\n\t"
      "v_max3_u32 v251, v251, v157, v158\n\t"
      "v_max3_u32 v251, v251, v159, v160\n\t"
      "v_max3_u32 v251, v251, v161, v162\n\t"
      "v_max3_u32 v251, v251, v163, v164\n\t"
      "v_max3_u32 v251, v251, v165, v166\n\t"
      "v_max3_u32 v251, v251, v167, v168\n\t"
      "v_max3_u32 v251, v251, v169, v170\n\t"
      "v_max3_u32 v251, v251, v171, v172\n\t"
      "v_max3_u32 v251, v251, v173, v174\n\t"
      "v_max_u32_e32 v251, v251, v175\n\t"
      "v_cmp_eq_u32_e32 vcc, -1, v251\n\t"
      "s_cbranch_vccnz LA33L_%=\n\t"
      "LA33X_%=:\n\t"
      "v_mfma_f32_16x16x32_bf16 v[216:219], v[0:3], v[144:147], 0\n\t"
      "v_mfma_f32_16x16x32_bf16 v[216:219], v[16:19], v[148:151], v[216:219]\n\t"
      "v_mfma_f32_16x16x32_bf16 v[216:219], v[32:35], v[152:155], v[216:219]\n\t"
      "v_mfma_f32_16x16x32_bf16 v[216:219], v[48:51], v[156:159], v[216:219]\n\t"
      "v_mfma_f32_16x16x32_bf16 v[216:219], v[64:67], v[160:163], v[216:219]\n\t"
      "v_mfma_f32_16x16x32_bf16 v[216:219], v[80:83], v[164:167], v[216:219]\n\t"
      "v_mfma_f32_16x16x32_bf16 v[216:219], v[96:99], v[168:171], v[216:219]\n\t"
      "v_mfma_f32_16x16x32_bf16 v[216:219], v[112:115], v[172:175], v[216:219]\n\t"
      "v_mfma_f32_16x16x32_bf16 v[220:223], v[4:7], v[144:147], 0\n\t"
      "v_mfma_f32_16x16x32_bf16 v[220:223], v[20:23], v[148:151], v[220:223]\n\t"
      "v_mfma_f32_16x16x32_bf16 v[220:223], v[36:39], v[152:155], v[220:223]\n\t"
      "s_nop 7\n\t"
      "s_branch LA33B_%=\n\t"
      "LA34R_%=:\n\t"
      "s_mov_b32 s80, 0\n\t"
      "LA34L_%=:\n\t"
      "s_add_u32 s80, s80, 1\n\t"
      "s_cmp_lt_u32 s80, 262144\n\t"
      "s_cbranch_scc0 LA34X_%=\n\t"
      "s_waitcnt vmcnt(0)\n\t"
      "s_sleep 1\n\t"
      "global_load_dwordx4 v[176:179], v250, s[72:73] offset:-4096 sc1\n\t"
      "global_load_dwordx4 v[180:183], v250, s[72:73] offset:-3072 sc1\n\t"
      "global_load_dwordx4 v[184:187], v250, s[72:73] offset:-2048 sc1\n\t"
      "global_load_dwordx4 v[188:191], v250, s[72:73] offset:-1024 sc1\n\t"
      "global_load_dwordx4 v[192:195], v250, s[72:73] sc1\n\t"
      "global_load_dwordx4 v[196:199], v250, s[72:73] offset:1024 sc1\n\t"
      "global_load_dwordx4 v[200:203], v250, s[72:73] offset:2048 sc1\n\t"
      "global_load_dwordx4 v[204:207], v250, s[72:73] offset:3072 sc1\n\t"
      "s_waitcnt vmcnt(0)\n\t"
      "v_max3_u32 v251, v176, v177, v178\n\t"
      "v_max3_u32 v251, v251, v179, v180\n\t"
      "v_max3_u32 v251, v251, v181, v182\n\t"
      "v_max3_u32 v251, v251, v183, v184\n\t"
      "v_max3_u32 v251, v251, v185, v186\n\t"
      "v_max3_u32 v251, v251, v187, v188\n\t"
      "v_max3_u32 v251, v251, v189, v190\n\t"
      "v_max3_u32 v251, v251, v191, v192\n\t"
      "v_max3_u32 v251, v251, v193, v194\n\t"
      "v_max3_u32 v251, v251, v195, v196\n\t"
      "v_max3_u32 v251, v251, v197, v198\n\t"
      "v_max3_u32 v251, v251, v199, v200\n\t"
      "v_max3_u32 v251, v251, v201, v202\n\t"
      "v_max3_u32 v251, v251, v203, v204\n\t"
      "v_max3_u32 v251, v251, v205, v206\n\t"
      "v_max_u32_e32 v251, v251, v207\n\t"
      "v_cmp_eq_u32_e32 vcc, -1, v251\n\t"
      "s_cbranch_vccnz LA34L_%=\n\t"
      "LA34X_%=:\n\t"
      "v_mfma_f32_16x16x32_bf16 v[212:215], v[0:3], v[176:179], 0\n\t"
      "v_mfma_f32_16x16x32_bf16 v[212:215], v[16:19], v[180:183], v[212:215]\n\t"
      "v_mfma_f32_16x16x32_bf16 v[212:215], v[32:35], v[184:187], v[212:215]\n\t"
      "v_mfma_f32_16x16x32_bf16 v[212:215], v[48:51], v[188:191], v[212:215]\n\t"
      "v_mfma_f32_16x16x32_bf16 v[212:215], v[64:67], v[192:195], v[212:215]\n\t"
      "v_mfma_f32_16x16x32_bf16 v[212:215], v[80:83], v[196:199], v[212:215]\n\t"
      "v_mfma_f32_16x16x32_bf16 v[212:215], v[96:99], v[200:203], v[212:215]\n\t"
      "v_mfma_f32_16x16x32_bf16 v[212:215], v[112:115], v[204:207], v[212:215]\n\t"
      "v_mfma_f32_16x16x32_bf16 v[216:219], v[4:7], v[176:179], 0\n\t"
      "v_mfma_f32_16x16x32_bf16 v[216:219], v[20:23], v[180:183], v[216:219]\n\t"
      "v_mfma_f32_16x16x32_bf16 v[216:219], v[36:39], v[184:187], v[216:219]\n\t"
      "s_nop 7\n\t"
      "s_branch LA34B_%=\n\t"
      "LA35R_%=:\n\t"
      "s_mov_b32 s80, 0\n\t"
      "LA35L_%=:\n\t"
      "s_add_u32 s80, s80, 1\n\t"
      "s_cmp_lt_u32 s80, 262144\n\t"
      "s_cbranch_scc0 LA35X_%=\n\t"
      "s_waitcnt vmcnt(0)\n\t"
      "s_sleep 1\n\t"
      "global_load_dwordx4 v[128:131], v250, s[74:75] offset:-4096 sc1\n\t"
      "global_load_dwordx4 v[132:135], v250, s[74:75] offset:-3072 sc1\n\t"
      "global_load_dwordx4 v[136:139], v250, s[74:75] offset:-2048 sc1\n\t"
      "global_load_dwordx4 v[140:143], v250, s[74:75] offset:-1024 sc1\n\t"
      "global_load_dwordx4 v[144:147], v250, s[74:75] sc1\n\t"
      "global_load_dwordx4 v[148:151], v250, s[74:75] offset:1024 sc1\n\t"
      "global_load_dwordx4 v[152:155], v250, s[74:75] offset:2048 sc1\n\t"
      "global_load_dwordx4 v[156:159], v250, s[74:75] offset:3072 sc1\n\t"
      "s_waitcnt vmcnt(0)\n\t"
      "v_max3_u32 v251, v128, v129, v130\n\t"
      "v_max3_u32 v251, v251, v131, v132\n\t"
      "v_max3_u32 v251, v251, v133, v134\n\t"
      "v_max3_u32 v251, v251, v135, v136\n\t"
      "v_max3_u32 v251, v251, v137, v138\n\t"
      "v_max3_u32 v251, v251, v139, v140\n\t"
      "v_max3_u32 v251, v251, v141, v142\n\t"
      "v_max3_u32 v251, v251, v143, v144\n\t"
      "v_max3_u32 v251, v251, v145, v146\n\t"
      "v_max3_u32 v251, v251, v147, v148\n\t"
      "v_max3_u32 v251, v251, v149, v150\n\t"
      "v_max3_u32 v251, v251, v151, v152\n\t"
      "v_max3_u32 v251, v251, v153, v154\n\t"
      "v_max3_u32 v251, v251, v155, v156\n\t"
      "v_max3_u32 v251, v251, v157, v158\n\t"
      "v_max_u32_e32 v251, v251, v159\n\t"
      "v_cmp_eq_u32_e32 vcc, -1, v251\n\t"
      "s_cbranch_vccnz LA35L_%=\n\t"
      "LA35X_%=:\n\t"
      "v_mfma_f32_16x16x32_bf16 v[208:211], v[0:3], v[128:131], 0\n\t"
      "v_mfma_f32_16x16x32_bf16 v[208:211], v[16:19], v[132:135], v[208:211]\n\t"
      "v_mfma_f32_16x16x32_bf16 v[208:211], v[32:35], v[136:139], v[208:211]\n\t"
      "v_mfma_f32_16x16x32_bf16 v[208:211], v[48:51], v[140:143], v[208:211]\n\t"
      "v_mfma_f32_16x16x32_bf16 v[208:211], v[64:67], v[144:147], v[208:211]\n\t"
      "v_mfma_f32_16x16x32_bf16 v[208:211], v[80:83], v[148:151], v[208:211]\n\t"
      "v_mfma_f32_16x16x32_bf16 v[208:211], v[96:99], v[152:155], v[208:211]\n\t"
      "v_mfma_f32_16x16x32_bf16 v[208:211], v[112:115], v[156:159], v[208:211]\n\t"
      "v_mfma_f32_16x16x32_bf16 v[212:215], v[4:7], v[128:131], 0\n\t"
      "v_mfma_f32_16x16x32_bf16 v[212:215], v[20:23], v[132:135], v[212:215]\n\t"
      "v_mfma_f32_16x16x32_bf16 v[212:215], v[36:39], v[136:139], v[212:215]\n\t"
      "s_nop 7\n\t"
      "s_branch LA35B_%=\n\t"
      "LA36R_%=:\n\t"
      "s_mov_b32 s80, 0\n\t"
      "LA36L_%=:\n\t"
      "s_add_u32 s80, s80, 1\n\t"
      "s_cmp_lt_u32 s80, 262144\n\t"
      "s_cbranch_scc0 LA36X_%=\n\t"
      "s_waitcnt vmcnt(0)\n\t"
      "s_sleep 1\n\t"
      "global_load_dwordx4 v[160:163], v250, s[76:77] offset:-4096 sc1\n\t"
      "global_load_dwordx4 v[164:167], v250, s[76:77] offset:-3072 sc1\n\t"
      "global_load_dwordx4 v[168:171], v250, s[76:77] offset:-2048 sc1\n\t"
      "global_load_dwordx4 v[172:175], v250, s[76:77] offset:-1024 sc1\n\t"
      "global_load_dwordx4 v[176:179], v250, s[76:77] sc1\n\t"
      "global_load_dwordx4 v[180:183], v250, s[76:77] offset:1024 sc1\n\t"
      "global_load_dwordx4 v[184:187], v250, s[76:77] offset:2048 sc1\n\t"
      "global_load_dwordx4 v[188:191], v250, s[76:77] offset:3072 sc1\n\t"
      "s_waitcnt vmcnt(0)\n\t"
      "v_max3_u32 v251, v160, v161, v162\n\t"
      "v_max3_u32 v251, v251, v163, v164\n\t"
      "v_max3_u32 v251, v251, v165, v166\n\t"
      "v_max3_u32 v251, v251, v167, v168\n\t"
      "v_max3_u32 v251, v251, v169, v170\n\t"
      "v_max3_u32 v251, v251, v171, v172\n\t"
      "v_max3_u32 v251, v251, v173, v174\n\t"
      "v_max3_u32 v251, v251, v175, v176\n\t"
      "v_max3_u32 v251, v251, v177, v178\n\t"
      "v_max3_u32 v251, v251, v179, v180\n\t"
      "v_max3_u32 v251, v251, v181, v182\n\t"
      "v_max3_u32 v251, v251, v183, v184\n\t"
      "v_max3_u32 v251, v251, v185, v186\n\t"
      "v_max3_u32 v251, v251, v187, v188\n\t"
      "v_max3_u32 v251, v251, v189, v190\n\t"
      "v_max_u32_e32 v251, v251, v191\n\t"
      "v_cmp_eq_u32_e32 vcc, -1, v251\n\t"
      "s_cbranch_vccnz LA36L_%=\n\t"
      "LA36X_%=:\n\t"
      "v_mfma_f32_16x16x32_bf16 v[224:227], v[0:3], v[160:163], 0\n\t"
      "v_mfma_f32_16x16x32_bf16 v[224:227], v[16:19], v[164:167], v[224:227]\n\t"
      "v_mfma_f32_16x16x32_bf16 v[224:227], v[32:35], v[168:171], v[224:227]\n\t"
      "v_mfma_f32_16x16x32_bf16 v[224:227], v[48:51], v[172:175], v[224:227]\n\t"
      "v_mfma_f32_16x16x32_bf16 v[224:227], v[64:67], v[176:179], v[224:227]\n\t"
      "v_mfma_f32_16x16x32_bf16 v[224:227], v[80:83], v[180:183], v[224:227]\n\t"
      "v_mfma_f32_16x16x32_bf16 v[224:227], v[96:99], v[184:187], v[224:227]\n\t"
      "v_mfma_f32_16x16x32_bf16 v[224:227], v[112:115], v[188:191], v[224:227]\n\t"
      "v_mfma_f32_16x16x32_bf16 v[208:211], v[4:7], v[160:163], 0\n\t"
      "v_mfma_f32_16x16x32_bf16 v[208:211], v[20:23], v[164:167], v[208:211]\n\t"
      "v_mfma_f32_16x16x32_bf16 v[208:211], v[36:39], v[168:171], v[208:211]\n\t"
      "s_nop 7\n\t"
      "s_branch LA36B_%=\n\t"
      "LF5R_%=:\n\t"
      "s_mov_b32 s80, 0\n\t"
      "LF5L_%=:\n\t"
      "s_add_u32 s80, s80, 1\n\t"
      "s_cmp_lt_u32 s80, 262144\n\t"
      "s_cbranch_scc0 LF5X_%=\n\t"
      "s_sleep 2\n\t"
      "global_load_dwordx2 v[138:139], v143, s[60:61] sc1\n\t"
      "s_waitcnt vmcnt(0)\n\t"
      "v_cmp_ne_u64_e32 vcc, s[22:23], v[138:139]\n\t"
      "s_cbranch_vccnz LF5L_%=\n\t"
      "LF5X_%=:\n\t"
      "s_branch LF5B_%=\n\t"
      "PGR_%=:\n\t"
      "s_mov_b32 s80, 0\n\t"
      "PGL_%=:\n\t"
      "s_add_u32 s80, s80, 1\n\t"
      "s_cmp_lt_u32 s80, 262144\n\t"
      "s_cbranch_scc0 PGX_%=\n\t"
      "s_sleep 2\n\t"
      "global_load_dword v251, v253, s[52:53] offset:252 sc1\n\t"
      "s_waitcnt vmcnt(0)\n\t"
      "v_cmp_eq_u32_e32 vcc, -1, v251\n\t"
      "s_cbranch_vccnz PGL_%=\n\t"
      "PGX_%=:\n\t"
      "s_sleep 5\n\t"
      "s_branch PGB_%=\n\t"
      "LF8R_%=:\n\t"
      "s_mov_b32 s80, 0\n\t"
      "LF8L_%=:\n\t"
      "s_add_u32 s80, s80, 1\n\t"
      "s_cmp_lt_u32 s80, 262144\n\t"
      "s_cbranch_scc0 LF8X_%=\n\t"
      "s_sleep 2\n\t"
      "global_load_dwordx2 v[240:241], v242, s[60:61] sc1\n\t"
      "s_waitcnt vmcnt(0)\n\t"
      "v_cmp_ne_u64_e32 vcc, s[22:23], v[240:241]\n\t"
      "s_cbranch_vccnz LF8L_%=\n\t"
      "LF8X_%=:\n\t"
      "s_branch LF8B_%=\n\t"
      "LA40R_%=:\n\t"
      "s_mov_b32 s80, 0\n\t"
      "LA40L_%=:\n\t"
      "s_add_u32 s80, s80, 1\n\t"
      "s_cmp_lt_u32 s80, 262144\n\t"
      "s_cbranch_scc0 LA40X_%=\n\t"
      "s_waitcnt vmcnt(0)\n\t"
      "PQ0R_%=:\n\t"
      "PQ0L_%=:\n\t"
      "s_add_u32 s80, s80, 1\n\t"
      "s_cmp_lt_u32 s80, 262144\n\t"
      "s_cbranch_scc0 PQ0X_%=\n\t"
      "s_sleep 1\n\t"
      "global_load_dword v251, v252, s[64:65] offset:252 sc1\n\t"
      "s_waitcnt vmcnt(0)\n\t"
      "v_cmp_eq_u32_e32 vcc, -1, v251\n\t"
      "s_cbranch_vccnz PQ0L_%=\n\t"
      "PQ0X_%=:\n\t"
      "s_add_u32 s58, s64, s40\n\t"
      "s_addc_u32 s59, s65, 0\n\t"
      "global_load_dwordx4 v[128:131], v244, s[58:59] sc1\n\t"
      "s_add_u32 s58, s64, s41\n\t"
      "s_addc_u32 s59, s65, 0\n\t"
      "global_load_dwordx4 v[132:135], v244, s[58:59] sc1\n\t"
      "s_add_u32 s58, s64, s42\n\t"
      "s_addc_u32 s59, s65, 0\n\t"
      "global_load_dwordx4 v[136:139], v244, s[58:59] sc1\n\t"
      "s_add_u32 s58, s64, s43\n\t"
      "s_addc_u32 s59, s65, 0\n\t"
      "global_load_dwordx4 v[140:143], v244, s[58:59] sc1\n\t"
      "s_add_u32 s58, s64, s44\n\t"
      "s_addc_u32 s59, s65, 0\n\t"
      "global_load_dwordx4 v[144:147], v244, s[58:59] sc1\n\t"
      "s_add_u32 s58, s64, s45\n\t"
      "s_addc_u32 s59, s65, 0\n\t"
      "global_load_dwordx4 v[148:151], v244, s[58:59] sc1\n\t"
      "s_add_u32 s58, s64, s46\n\t"
      "s_addc_u32 s59, s65, 0\n\t"
      "global_load_dwordx4 v[152:155], v244, s[58:59] sc1\n\t"
      "s_waitcnt vmcnt(0)\n\t"
      "v_max3_u32 v251, v128, v129, v130\n\t"
      "v_max3_u32 v251, v251, v131, v132\n\t"
      "v_max3_u32 v251, v251, v133, v134\n\t"
      "v_max3_u32 v251, v251, v135, v136\n\t"
      "v_max3_u32 v251, v251, v137, v138\n\t"
      "v_max3_u32 v251, v251, v139, v140\n\t"
      "v_max3_u32 v251, v251, v141, v142\n\t"
      "v_max3_u32 v251, v251, v143, v144\n\t"
      "v_max3_u32 v251, v251, v145, v146\n\t"
      "v_max3_u32 v251, v251, v147, v148\n\t"
      "v_max3_u32 v251, v251, v149, v150\n\t"
      "v_max3_u32 v251, v251, v151, v152\n\t"
      "v_max3_u32 v251, v251, v153, v154\n\t"
      "v_max_u32_e32 v251, v251, v155\n\t"
      "v_cmp_eq_u32_e32 vcc, -1, v251\n\t"
      "s_cbranch_vccnz LA40L_%=\n\t"
      "LA40X_%=:\n\t"
      "v_mfma_f32_16x16x32_bf16 v[196:199], v[128:131], v[0:3], 0\n\t"
      "v_mfma_f32_16x16x32_bf16 v[200:203], v[128:131], v[4:7], 0\n\t"
      "v_mfma_f32_16x16x32_bf16 v[204:207], v[128:131], v[8:11], 0\n\t"
      "v_mfma_f32_16x16x32_bf16 v[208:211], v[128:131], v[12:15], 0\n\t"
      "v_mfma_f32_16x16x32_bf16 v[196:199], v[132:135], v[16:19], v[196:199]\n\t"
      "v_mfma_f32_16x16x32_bf16 v[200:203], v[132:135], v[20:23], v[200:203]\n\t"
      "v_mfma_f32_16x16x32_bf16 v[204:207], v[132:135], v[24:27], v[204:207]\n\t"
      "v_mfma_f32_16x16x32_bf16 v[208:211], v[132:135], v[28:31], v[208:211]\n\t"
      "v_mfma_f32_16x16x32_bf16 v[196:199], v[136:139], v[32:35], v[196:199]\n\t"
      "v_mfma_f32_16x16x32_bf16 v[200:203], v[136:139], v[36:39], v[200:203]\n\t"
      "v_mfma_f32_16x16x32_bf16 v[204:207], v[136:139], v[40:43], v[204:207]\n\t"
      "v_mfma_f32_16x16x32_bf16 v[208:211], v[136:139], v[44:47], v[208:211]\n\t"
      "v_mfma_f32_16x16x32_bf16 v[196:199], v[140:143], v[48:51], v[196:199]\n\t"
      "v_mfma_f32_16x16x32_bf16 v[200:203], v[140:143], v[52:55], v[200:203]\n\t"
      "v_mfma_f32_16x16x32_bf16 v[204:207], v[140:143], v[56:59], v[204:207]\n\t"
      "v_mfma_f32_16x16x32_bf16 v[208:211], v[140:143], v[60:63], v[208:211]\n\t"
      "v_mfma_f32_16x16x32_bf16 v[196:199], v[144:147], v[64:67], v[196:199]\n\t"
      "v_mfma_f32_16x16x32_bf16 v[200:203], v[144:147], v[68:71], v[200:203]\n\t"
      "v_mfma_f32_16x16x32_bf16 v[204:207], v[144:147], v[72:75], v[204:207]\n\t"
      "v_mfma_f32_16x16x32_bf16 v[208:211], v[144:147], v[76:79], v[208:211]\n\t"
      "v_mfma_f32_16x16x32_bf16 v[196:199], v[148:151], v[80:83], v[196:199]\n\t"
      "v_mfma_f32_16x16x32_bf16 v[200:203], v[148:151], v[84:87], v[200:203]\n\t"
      "v_mfma_f32_16x16x32_bf16 v[204:207], v[148:151], v[88:91], v[204:207]\n\t"
      "v_mfma_f32_16x16x32_bf16 v[208:211], v[148:151], v[92:95], v[208:211]\n\t"
      "v_mfma_f32_16x16x32_bf16 v[196:199], v[152:155], v[96:99], v[196:199]\n\t"
      "v_mfma_f32_16x16x32_bf16 v[200:203], v[152:155], v[100:103], v[200:203]\n\t"
      "v_mfma_f32_16x16x32_bf16 v[204:207], v[152:155], v[104:107], v[204:207]\n\t"
      "v_mfma_f32_16x16x32_bf16 v[208:211], v[152:155], v[108:111], v[208:211]\n\t"
      "s_nop 7\n\t"
      "s_branch LA40B_%=\n\t"
      "LA41R_%=:\n\t"
      "s_mov_b32 s80, 0\n\t"
      "LA41L_%=:\n\t"
      "s_add_u32 s80, s80, 1\n\t"
      "s_cmp_lt_u32 s80, 262144\n\t"
      "s_cbranch_scc0 LA41X_%=\n\t"
      "s_waitcnt vmcnt(0)\n\t"
      "PQ1R_%=:\n\t"
      "PQ1L_%=:\n\t"
      "s_add_u32 s80, s80, 1\n\t"
      "s_cmp_lt_u32 s80, 262144\n\t"
      "s_cbranch_scc0 PQ1X_%=\n\t"
      "s_sleep 1\n\t"
      "global_load_dword v251, v252, s[66:67] offset:252 sc1\n\t"
      "s_waitcnt vmcnt(0)\n\t"
      "v_cmp_eq_u32_e32 vcc, -1, v251\n\t"
      "s_cbranch_vccnz PQ1L_%=\n\t"
      "PQ1X_%=:\n\t"
      "s_add_u32 s58, s66, s40\n\t"
      "s_addc_u32 s59, s67, 0\n\t"
      "global_load_dwordx4 v[156:159], v244, s[58:59] sc1\n\t"
      "s_add_u32 s58, s66, s41\n\t"
      "s_addc_u32 s59, s67, 0\n\t"
      "global_load_dwordx4 v[160:163], v244, s[58:59] sc1\n\t"
      "s_add_u32 s58, s66, s42\n\t"
      "s_addc_u32 s59, s67, 0\n\t"
      "global_load_dwordx4 v[164:167], v244, s[58:59] sc1\n\t"
      "s_add_u32 s58, s66, s43\n\t"
      "s_addc_u32 s59, s67, 0\n\t"
      "global_load_dwordx4 v[168:171], v244, s[58:59] sc1\n\t"
      "s_add_u32 s58, s66, s44\n\t"
      "s_addc_u32 s59, s67, 0\n\t"
      "global_load_dwordx4 v[172:175], v244, s[58:59] sc1\n\t"
      "s_add_u32 s58, s66, s45\n\t"
      "s_addc_u32 s59, s67, 0\n\t"
      "global_load_dwordx4 v[176:179], v244, s[58:59] sc1\n\t"
      "s_add_u32 s58, s66, s46\n\t"
      "s_addc_u32 s59, s67, 0\n\t"
      "global_load_dwordx4 v[180:183], v244, s[58:59] sc1\n\t"
      "s_waitcnt vmcnt(0)\n\t"
      "v_max3_u32 v251, v156, v157, v158\n\t"
      "v_max3_u32 v251, v251, v159, v160\n\t"
      "v_max3_u32 v251, v251, v161, v162\n\t"
      "v_max3_u32 v251, v251, v163, v164\n\t"
      "v_max3_u32 v251, v251, v165, v166\n\t"
      "v_max3_u32 v251, v251, v167, v168\n\t"
      "v_max3_u32 v251, v251, v169, v170\n\t"
      "v_max3_u32 v251, v251, v171, v172\n\t"
      "v_max3_u32 v251, v251, v173, v174\n\t"
      "v_max3_u32 v251, v251, v175, v176\n\t"
      "v_max3_u32 v251, v251, v177, v178\n\t"
      "v_max3_u32 v251, v251, v179, v180\n\t"
      "v_max3_u32 v251, v251, v181, v182\n\t"
      "v_max_u32_e32 v251, v251, v183\n\t"
      "v_cmp_eq_u32_e32 vcc, -1, v251\n\t"
      "s_cbranch_vccnz LA41L_%=\n\t"
      "LA41X_%=:\n\t"
      "v_mfma_f32_16x16x32_bf16 v[212:215], v[156:159], v[0:3], 0\n\t"
      "v_mfma_f32_16x16x32_bf16 v[216:219], v[156:159], v[4:7], 0\n\t"
      "v_mfma_f32_16x16x32_bf16 v[220:223], v[156:159], v[8:11], 0\n\t"
      "v_mfma_f32_16x16x32_bf16 v[224:227], v[156:159], v[12:15], 0\n\t"
      "v_mfma_f32_16x16x32_bf16 v[212:215], v[160:163], v[16:19], v[212:215]\n\t"
      "v_mfma_f32_16x16x32_bf16 v[216:219], v[160:163], v[20:23], v[216:219]\n\t"
      "v_mfma_f32_16x16x32_bf16 v[220:223], v[160:163], v[24:27], v[220:223]\n\t"
      "v_mfma_f32_16x16x32_bf16 v[224:227], v[160:163], v[28:31], v[224:227]\n\t"
      "v_mfma_f32_16x16x32_bf16 v[212:215], v[164:167], v[32:35], v[212:215]\n\t"
      "v_mfma_f32_16x16x32_bf16 v[216:219], v[164:167], v[36:39], v[216:219]\n\t"
      "v_mfma_f32_16x16x32_bf16 v[220:223], v[164:167], v[40:43], v[220:223]\n\t"
      "v_mfma_f32_16x16x32_bf16 v[224:227], v[164:167], v[44:47], v[224:227]\n\t"
      "v_mfma_f32_16x16x32_bf16 v[212:215], v[168:171], v[48:51], v[212:215]\n\t"
      "v_mfma_f32_16x16x32_bf16 v[216:219], v[168:171], v[52:55], v[216:219]\n\t"
      "v_mfma_f32_16x16x32_bf16 v[220:223], v[168:171], v[56:59], v[220:223]\n\t"
      "v_mfma_f32_16x16x32_bf16 v[224:227], v[168:171], v[60:63], v[224:227]\n\t"
      "v_mfma_f32_16x16x32_bf16 v[212:215], v[172:175], v[64:67], v[212:215]\n\t"
      "v_mfma_f32_16x16x32_bf16 v[216:219], v[172:175], v[68:71], v[216:219]\n\t"
      "v_mfma_f32_16x16x32_bf16 v[220:223], v[172:175], v[72:75], v[220:223]\n\t"
      "v_mfma_f32_16x16x32_bf16 v[224:227], v[172:175], v[76:79], v[224:227]\n\t"
      "v_mfma_f32_16x16x32_bf16 v[212:215], v[176:179], v[80:83], v[212:215]\n\t"
      "v_mfma_f32_16x16x32_bf16 v[216:219], v[176:179], v[84:87], v[216:219]\n\t"
      "v_mfma_f32_16x16x32_bf16 v[220:223], v[176:179], v[88:91], v[220:223]\n\t"
      "v_mfma_f32_16x16x32_bf16 v[224:227], v[176:179], v[92:95], v[224:227]\n\t"
      "v_mfma_f32_16x16x32_bf16 v[212:215], v[180:183], v[96:99], v[212:215]\n\t"
      "v_mfma_f32_16x16x32_bf16 v[216:219], v[180:183], v[100:103], v[216:219]\n\t"
      "v_mfma_f32_16x16x32_bf16 v[220:223], v[180:183], v[104:107], v[220:223]\n\t"
      "v_mfma_f32_16x16x32_bf16 v[224:227], v[180:183], v[108:111], v[224:227]\n\t"
      "s_nop 7\n\t"
      "s_branch LA41B_%=\n\t"
      "LA42R_%=:\n\t"
      "s_mov_b32 s80, 0\n\t"
      "LA42L_%=:\n\t"
      "s_add_u32 s80, s80, 1\n\t"
      "s_cmp_lt_u32 s80, 262144\n\t"
      "s_cbranch_scc0 LA42X_%=\n\t"
      "s_waitcnt vmcnt(0)\n\t"
      "PQ2R_%=:\n\t"
      "PQ2L_%=:\n\t"
      "s_add_u32 s80, s80, 1\n\t"
      "s_cmp_lt_u32 s80, 262144\n\t"
      "s_cbranch_scc0 PQ2X_%=\n\t"
      "s_sleep 1\n\t"
      "global_load_dword v251, v252, s[68:69] offset:252 sc1\n\t"
      "s_waitcnt vmcnt(0)\n\t"
      "v_cmp_eq_u32_e32 vcc, -1, v251\n\t"
      "s_cbranch_vccnz PQ2L_%=\n\t"
      "PQ2X_%=:\n\t"
      "s_add_u32 s58, s68, s40\n\t"
      "s_addc_u32 s59, s69, 0\n\t"
      "global_load_dwordx4 v[184:187], v244, s[58:59] sc1\n\t"
      "s_add_u32 s58, s68, s41\n\t"
      "s_addc_u32 s59, s69, 0\n\t"
      "global_load_dwordx4 v[188:191], v244, s[58:59] sc1\n\t"
      "s_add_u32 s58, s68, s42\n\t"
      "s_addc_u32 s59, s69, 0\n\t"
      "global_load_dwordx4 v[192:195], v244, s[58:59] sc1\n\t"
      "s_add_u32 s58, s68, s43\n\t"
      "s_addc_u32 s59, s69, 0\n\t"
      "global_load_dwordx4 v[128:131], v244, s[58:59] sc1\n\t"
      "s_add_u32 s58, s68, s44\n\t"
      "s_addc_u32 s59, s69, 0\n\t"
      "global_load_dwordx4 v[132:135], v244, s[58:59] sc1\n\t"
      "s_add_u32 s58, s68, s45\n\t"
      "s_addc_u32 s59, s69, 0\n\t"
      "global_load_dwordx4 v[136:139], v244, s[58:59] sc1\n\t"
      "s_add_u32 s58, s68, s46\n\t"
      "s_addc_u32 s59, s69, 0\n\t"
      "global_load_dwordx4 v[140:143], v244, s[58:59] sc1\n\t"
      "s_waitcnt vmcnt(0)\n\t"
      "v_max3_u32 v251, v184, v185, v186\n\t"
      "v_max3_u32 v251, v251, v187, v188\n\t"
      "v_max3_u32 v251, v251, v189, v190\n\t"
      "v_max3_u32 v251, v251, v191, v192\n\t"
      "v_max3_u32 v251, v251, v193, v194\n\t"
      "v_max3_u32 v251, v251, v195, v128\n\t"
      "v_max3_u32 v251, v251, v129, v130\n\t"
      "v_max3_u32 v251, v251, v131, v132\n\t"
      "v_max3_u32 v251, v251, v133, v134\n\t"
      "v_max3_u32 v251, v251, v135, v136\n\t"
      "v_max3_u32 v251, v251, v137, v138\n\t"
      "v_max3_u32 v251, v251, v139, v140\n\t"
      "v_max3_u32 v251, v251, v141, v142\n\t"
      "v_max_u32_e32 v251, v251, v143\n\t"
      "v_cmp_eq_u32_e32 vcc, -1, v251\n\t"
      "s_cbranch_vccnz LA42L_%=\n\t"
      "LA42X_%=:\n\t"
      "v_mfma_f32_16x16x32_bf16 v[196:199], v[184:187], v[0:3], 0\n\t"
      "v_mfma_f32_16x16x32_bf16 v[200:203], v[184:187], v[4:7], 0\n\t"
      "v_mfma_f32_16x16x32_bf16 v[204:207], v[184:187], v[8:11], 0\n\t"
      "v_mfma_f32_16x16x32_bf16 v[208:211], v[184:187], v[12:15], 0\n\t"
      "v_mfma_f32_16x16x32_bf16 v[196:199], v[188:191], v[16:19], v[196:199]\n\t"
      "v_mfma_f32_16x16x32_bf16 v[200:203], v[188:191], v[20:23], v[200:203]\n\t"
      "v_mfma_f32_16x16x32_bf16 v[204:207], v[188:191], v[24:27], v[204:207]\n\t"
      "v_mfma_f32_16x16x32_bf16 v[208:211], v[188:191], v[28:31], v[208:211]\n\t"
      "v_mfma_f32_16x16x32_bf16 v[196:199], v[192:195], v[32:35], v[196:199]\n\t"
      "v_mfma_f32_16x16x32_bf16 v[200:203], v[192:195], v[36:39], v[200:203]\n\t"
      "v_mfma_f32_16x16x32_bf16 v[204:207], v[192:195], v[40:43], v[204:207]\n\t"
      "v_mfma_f32_16x16x32_bf16 v[208:211], v[192:195], v[44:47], v[208:211]\n\t"
      "v_mfma_f32_16x16x32_bf16 v[196:199], v[128:131], v[48:51], v[196:199]\n\t"
      "v_mfma_f32_16x16x32_bf16 v[200:203], v[128:131], v[52:55], v[200:203]\n\t"
      "v_mfma_f32_16x16x32_bf16 v[204:207], v[128:131], v[56:59], v[204:207]\n\t"
      "v_mfma_f32_16x16x32_bf16 v[208:211], v[128:131], v[60:63], v[208:211]\n\t"
      "v_mfma_f32_16x16x32_bf16 v[196:199], v[132:135], v[64:67], v[196:199]\n\t"
      "v_mfma_f32_16x16x32_bf16 v[200:203], v[132:135], v[68:71], v[200:203]\n\t"
      "v_mfma_f32_16x16x32_bf16 v[204:207], v[132:135], v[72:75], v[204:207]\n\t"
      "v_mfma_f32_16x16x32_bf16 v[208:211], v[132:135], v[76:79], v[208:211]\n\t"
      "v_mfma_f32_16x16x32_bf16 v[196:199], v[136:139], v[80:83], v[196:199]\n\t"
      "v_mfma_f32_16x16x32_bf16 v[200:203], v[136:139], v[84:87], v[200:203]\n\t"
      "v_mfma_f32_16x16x32_bf16 v[204:207], v[136:139], v[88:91], v[204:207]\n\t"
      "v_mfma_f32_16x16x32_bf16 v[208:211], v[136:139], v[92:95], v[208:211]\n\t"
      "v_mfma_f32_16x16x32_bf16 v[196:199], v[140:143], v[96:99], v[196:199]\n\t"
      "v_mfma_f32_16x16x32_bf16 v[200:203], v[140:143], v[100:103], v[200:203]\n\t"
      "v_mfma_f32_16x16x32_bf16 v[204:207], v[140:143], v[104:107], v[204:207]\n\t"
      "v_mfma_f32_16x16x32_bf16 v[208:211], v[140:143], v[108:111], v[208:211]\n\t"
      "s_nop 7\n\t"
      "s_branch LA42B_%=\n\t"
      "LA43R_%=:\n\t"
      "s_mov_b32 s80, 0\n\t"
      "LA43L_%=:\n\t"
      "s_add_u32 s80, s80, 1\n\t"
      "s_cmp_lt_u32 s80, 262144\n\t"
      "s_cbranch_scc0 LA43X_%=\n\t"
      "s_waitcnt vmcnt(0)\n\t"
      "PQ3R_%=:\n\t"
      "PQ3L_%=:\n\t"
      "s_add_u32 s80, s80, 1\n\t"
      "s_cmp_lt_u32 s80, 262144\n\t"
      "s_cbranch_scc0 PQ3X_%=\n\t"
      "s_sleep 1\n\t"
      "global_load_dword v251, v252, s[70:71] offset:252 sc1\n\t"
      "s_waitcnt vmcnt(0)\n\t"
      "v_cmp_eq_u32_e32 vcc, -1, v251\n\t"
      "s_cbranch_vccnz PQ3L_%=\n\t"
      "PQ3X_%=:\n\t"
      "s_add_u32 s58, s70, s40\n\t"
      "s_addc_u32 s59, s71, 0\n\t"
      "global_load_dwordx4 v[144:147], v244, s[58:59] sc1\n\t"
      "s_add_u32 s58, s70, s41\n\t"
      "s_addc_u32 s59, s71, 0\n\t"
      "global_load_dwordx4 v[148:151], v244, s[58:59] sc1\n\t"
      "s_add_u32 s58, s70, s42\n\t"
      "s_addc_u32 s59, s71, 0\n\t"
      "global_load_dwordx4 v[152:155], v244, s[58:59] sc1\n\t"
      "s_add_u32 s58, s70, s43\n\t"
      "s_addc_u32 s59, s71, 0\n\t"
      "global_load_dwordx4 v[156:159], v244, s[58:59] sc1\n\t"
      "s_add_u32 s58, s70, s44\n\t"
      "s_addc_u32 s59, s71, 0\n\t"
      "global_load_dwordx4 v[160:163], v244, s[58:59] sc1\n\t"
      "s_add_u32 s58, s70, s45\n\t"
      "s_addc_u32 s59, s71, 0\n\t"
      "global_load_dwordx4 v[164:167], v244, s[58:59] sc1\n\t"
      "s_add_u32 s58, s70, s46\n\t"
      "s_addc_u32 s59, s71, 0\n\t"
      "global_load_dwordx4 v[168:171], v244, s[58:59] sc1\n\t"
      "s_waitcnt vmcnt(0)\n\t"
      "v_max3_u32 v251, v144, v145, v146\n\t"
      "v_max3_u32 v251, v251, v147, v148\n\t"
      "v_max3_u32 v251, v251, v149, v150\n\t"
      "v_max3_u32 v251, v251, v151, v152\n\t"
      "v_max3_u32 v251, v251, v153, v154\n\t"
      "v_max3_u32 v251, v251, v155, v156\n\t"
      "v_max3_u32 v251, v251, v157, v158\n\t"
      "v_max3_u32 v251, v251, v159, v160\n\t"
      "v_max3_u32 v251, v251, v161, v162\n\t"
      "v_max3_u32 v251, v251, v163, v164\n\t"
      "v_max3_u32 v251, v251, v165, v166\n\t"
      "v_max3_u32 v251, v251, v167, v168\n\t"
      "v_max3_u32 v251, v251, v169, v170\n\t"
      "v_max_u32_e32 v251, v251, v171\n\t"
      "v_cmp_eq_u32_e32 vcc, -1, v251\n\t"
      "s_cbranch_vccnz LA43L_%=\n\t"
      "LA43X_%=:\n\t"
      "v_mfma_f32_16x16x32_bf16 v[212:215], v[144:147], v[0:3], 0\n\t"
      "v_mfma_f32_16x16x32_bf16 v[216:219], v[144:147], v[4:7], 0\n\t"
      "v_mfma_f32_16x16x32_bf16 v[220:223], v[144:147], v[8:11], 0\n\t"
      "v_mfma_f32_16x16x32_bf16 v[224:227], v[144:147], v[12:15], 0\n\t"
      "v_mfma_f32_16x16x32_bf16 v[212:215], v[148:151], v[16:19], v[212:215]\n\t"
      "v_mfma_f32_16x16x32_bf16 v[216:219], v[148:151], v[20:23], v[216:219]\n\t"
      "v_mfma_f32_16x16x32_bf16 v[220:223], v[148:151], v[24:27], v[220:223]\n\t"
      "v_mfma_f32_16x16x32_bf16 v[224:227], v[148:151], v[28:31], v[224:227]\n\t"
      "v_mfma_f32_16x16x32_bf16 v[212:215], v[152:155], v[32:35], v[212:215]\n\t"
      "v_mfma_f32_16x16x32_bf16 v[216:219], v[152:155], v[36:39], v[216:219]\n\t"
      "v_mfma_f32_16x16x32_bf16 v[220:223], v[152:155], v[40:43], v[220:223]\n\t"
      "v_mfma_f32_16x16x32_bf16 v[224:227], v[152:155], v[44:47], v[224:227]\n\t"
      "v_mfma_f32_16x16x32_bf16 v[212:215], v[156:159], v[48:51], v[212:215]\n\t"
      "v_mfma_f32_16x16x32_bf16 v[216:219], v[156:159], v[52:55], v[216:219]\n\t"
      "v_mfma_f32_16x16x32_bf16 v[220:223], v[156:159], v[56:59], v[220:223]\n\t"
      "v_mfma_f32_16x16x32_bf16 v[224:227], v[156:159], v[60:63], v[224:227]\n\t"
      "v_mfma_f32_16x16x32_bf16 v[212:215], v[160:163], v[64:67], v[212:215]\n\t"
      "v_mfma_f32_16x16x32_bf16 v[216:219], v[160:163], v[68:71], v[216:219]\n\t"
      "v_mfma_f32_16x16x32_bf16 v[220:223], v[160:163], v[72:75], v[220:223]\n\t"
      "v_mfma_f32_16x16x32_bf16 v[224:227], v[160:163], v[76:79], v[224:227]\n\t"
      "v_mfma_f32_16x16x32_bf16 v[212:215], v[164:167], v[80:83], v[212:215]\n\t"
      "v_mfma_f32_16x16x32_bf16 v[216:219], v[164:167], v[84:87], v[216:219]\n\t"
      "v_mfma_f32_16x16x32_bf16 v[220:223], v[164:167], v[88:91], v[220:223]\n\t"
      "v_mfma_f32_16x16x32_bf16 v[224:227], v[164:167], v[92:95], v[224:227]\n\t"
      "v_mfma_f32_16x16x32_bf16 v[212:215], v[168:171], v[96:99], v[212:215]\n\t"
      "v_mfma_f32_16x16x32_bf16 v[216:219], v[168:171], v[100:103], v[216:219]\n\t"
      "v_mfma_f32_16x16x32_bf16 v[220:223], v[168:171], v[104:107], v[220:223]\n\t"
      "v_mfma_f32_16x16x32_bf16 v[224:227], v[168:171], v[108:111], v[224:227]\n\t"
      "s_nop 7\n\t"
      "s_branch LA43B_%=\n\t"
      "LA44R_%=:\n\t"
      "s_mov_b32 s80, 0\n\t"
      "LA44L_%=:\n\t"
      "s_add_u32 s80, s80, 1\n\t"
      "s_cmp_lt_u32 s80, 262144\n\t"
      "s_cbranch_scc0 LA44X_%=\n\t"
      "s_waitcnt vmcnt(0)\n\t"
      "PQ4R_%=:\n\t"
      "PQ4L_%=:\n\t"
      "s_add_u32 s80, s80, 1\n\t"
      "s_cmp_lt_u32 s80, 262144\n\t"
      "s_cbranch_scc0 PQ4X_%=\n\t"
      "s_sleep 1\n\t"
      "global_load_dword v251, v252, s[72:73] offset:252 sc1\n\t"
      "s_waitcnt vmcnt(0)\n\t"
      "v_cmp_eq_u32_e32 vcc, -1, v251\n\t"
      "s_cbranch_vccnz PQ4L_%=\n\t"
      "PQ4X_%=:\n\t"
      "s_add_u32 s58, s72, s40\n\t"
      "s_addc_u32 s59, s73, 0\n\t"
      "global_load_dwordx4 v[172:175], v244, s[58:59] sc1\n\t"
      "s_add_u32 s58, s72, s41\n\t"
      "s_addc_u32 s59, s73, 0\n\t"
      "global_load_dwordx4 v[176:179], v244, s[58:59] sc1\n\t"
      "s_add_u32 s58, s72, s42\n\t"
      "s_addc_u32 s59, s73, 0\n\t"
      "global_load_dwordx4 v[180:183], v244, s[58:59] sc1\n\t"
      "s_add_u32 s58, s72, s43\n\t"
      "s_addc_u32 s59, s73, 0\n\t"
      "global_load_dwordx4 v[184:187], v244, s[58:59] sc1\n\t"
      "s_add_u32 s58, s72, s44\n\t"
      "s_addc_u32 s59, s73, 0\n\t"
      "global_load_dwordx4 v[188:191], v244, s[58:59] sc1\n\t"
      "s_add_u32 s58, s72, s45\n\t"
      "s_addc_u32 s59, s73, 0\n\t"
      "global_load_dwordx4 v[192:195], v244, s[58:59] sc1\n\t"
      "s_add_u32 s58, s72, s46\n\t"
      "s_addc_u32 s59, s73, 0\n\t"
      "global_load_dwordx4 v[128:131], v244, s[58:59] sc1\n\t"
      "s_waitcnt vmcnt(0)\n\t"
      "v_max3_u32 v251, v172, v173, v174\n\t"
      "v_max3_u32 v251, v251, v175, v176\n\t"
      "v_max3_u32 v251, v251, v177, v178\n\t"
      "v_max3_u32 v251, v251, v179, v180\n\t"
      "v_max3_u32 v251, v251, v181, v182\n\t"
      "v_max3_u32 v251, v251, v183, v184\n\t"
      "v_max3_u32 v251, v251, v185, v186\n\t"
      "v_max3_u32 v251, v251, v187, v188\n\t"
      "v_max3_u32 v251, v251, v189, v190\n\t"
      "v_max3_u32 v251, v251, v191, v192\n\t"
      "v_max3_u32 v251, v251, v193, v194\n\t"
      "v_max3_u32 v251, v251, v195, v128\n\t"
      "v_max3_u32 v251, v251, v129, v130\n\t"
      "v_max_u32_e32 v251, v251, v131\n\t"
      "v_cmp_eq_u32_e32 vcc, -1, v251\n\t"
      "s_cbranch_vccnz LA44L_%=\n\t"
      "LA44X_%=:\n\t"
      "v_mfma_f32_16x16x32_bf16 v[196:199], v[172:175], v[0:3], 0\n\t"
      "v_mfma_f32_16x16x32_bf16 v[200:203], v[172:175], v[4:7], 0\n\t"
      "v_mfma_f32_16x16x32_bf16 v[204:207], v[172:175], v[8:11], 0\n\t"
      "v_mfma_f32_16x16x32_bf16 v[208:211], v[172:175], v[12:15], 0\n\t"
      "v_mfma_f32_16x16x32_bf16 v[196:199], v[176:179], v[16:19], v[196:199]\n\t"
      "v_mfma_f32_16x16x32_bf16 v[200:203], v[176:179], v[20:23], v[200:203]\n\t"
      "v_mfma_f32_16x16x32_bf16 v[204:207], v[176:179], v[24:27], v[204:207]\n\t"
      "v_mfma_f32_16x16x32_bf16 v[208:211], v[176:179], v[28:31], v[208:211]\n\t"
      "v_mfma_f32_16x16x32_bf16 v[196:199], v[180:183], v[32:35], v[196:199]\n\t"
      "v_mfma_f32_16x16x32_bf16 v[200:203], v[180:183], v[36:39], v[200:203]\n\t"
      "v_mfma_f32_16x16x32_bf16 v[204:207], v[180:183], v[40:43], v[204:207]\n\t"
      "v_mfma_f32_16x16x32_bf16 v[208:211], v[180:183], v[44:47], v[208:211]\n\t"
      "v_mfma_f32_16x16x32_bf16 v[196:199], v[184:187], v[48:51], v[196:199]\n\t"
      "v_mfma_f32_16x16x32_bf16 v[200:203], v[184:187], v[52:55], v[200:203]\n\t"
      "v_mfma_f32_16x16x32_bf16 v[204:207], v[184:187], v[56:59], v[204:207]\n\t"
      "v_mfma_f32_16x16x32_bf16 v[208:211], v[184:187], v[60:63], v[208:211]\n\t"
      "v_mfma_f32_16x16x32_bf16 v[196:199], v[188:191], v[64:67], v[196:199]\n\t"
      "v_mfma_f32_16x16x32_bf16 v[200:203], v[188:191], v[68:71], v[200:203]\n\t"
      "v_mfma_f32_16x16x32_bf16 v[204:207], v[188:191], v[72:75], v[204:207]\n\t"
      "v_mfma_f32_16x16x32_bf16 v[208:211], v[188:191], v[76:79], v[208:211]\n\t"
      "v_mfma_f32_16x16x32_bf16 v[196:199], v[192:195], v[80:83], v[196:199]\n\t"
      "v_mfma_f32_16x16x32_bf16 v[200:203], v[192:195], v[84:87], v[200:203]\n\t"
      "v_mfma_f32_16x16x32_bf16 v[204:207], v[192:195], v[88:91], v[204:207]\n\t"
      "v_mfma_f32_16x16x32_bf16 v[208:211], v[192:195], v[92:95], v[208:211]\n\t"
      "v_mfma_f32_16x16x32_bf16 v[196:199], v[128:131], v[96:99], v[196:199]\n\t"
      "v_mfma_f32_16x16x32_bf16 v[200:203], v[128:131], v[100:103], v[200:203]\n\t"
      "v_mfma_f32_16x16x32_bf16 v[204:207], v[128:131], v[104:107], v[204:207]\n\t"
      "v_mfma_f32_16x16x32_bf16 v[208:211], v[128:131], v[108:111], v[208:211]\n\t"
      "s_nop 7\n\t"
      "s_branch LA44B_%=\n\t"
      "LA45R_%=:\n\t"
      "s_mov_b32 s80, 0\n\t"
      "LA45L_%=:\n\t"
      "s_add_u32 s80, s80, 1\n\t"
      "s_cmp_lt_u32 s80, 262144\n\t"
      "s_cbranch_scc0 LA45X_%=\n\t"
      "s_waitcnt vmcnt(0)\n\t"
      "PQ5R_%=:\n\t"
      "PQ5L_%=:\n\t"
      "s_add_u32 s80, s80, 1\n\t"
      "s_cmp_lt_u32 s80, 262144\n\t"
      "s_cbranch_scc0 PQ5X_%=\n\t"
      "s_sleep 1\n\t"
      "global_load_dword v251, v252, s[74:75] offset:252 sc1\n\t"
      "s_waitcnt vmcnt(0)\n\t"
      "v_cmp_eq_u32_e32 vcc, -1, v251\n\t"
      "s_cbranch_vccnz PQ5L_%=\n\t"
      "PQ5X_%=:\n\t"
      "s_add_u32 s58, s74, s40\n\t"
      "s_addc_u32 s59, s75, 0\n\t"
      "global_load_dwordx4 v[132:135], v244, s[58:59] sc1\n\t"
      "s_add_u32 s58, s74, s41\n\t"
      "s_addc_u32 s59, s75, 0\n\t"
      "global_load_dwordx4 v[136:139], v244, s[58:59] sc1\n\t"
      "s_add_u32 s58, s74, s42\n\t"
      "s_addc_u32 s59, s75, 0\n\t"
      "global_load_dwordx4 v[140:143], v244, s[58:59] sc1\n\t"
      "s_add_u32 s58, s74, s43\n\t"
      "s_addc_u32 s59, s75, 0\n\t"
      "global_load_dwordx4 v[144:147], v244, s[58:59] sc1\n\t"
      "s_add_u32 s58, s74, s44\n\t"
      "s_addc_u32 s59, s75, 0\n\t"
      "global_load_dwordx4 v[148:151], v244, s[58:59] sc1\n\t"
      "s_add_u32 s58, s74, s45\n\t"
      "s_addc_u32 s59, s75, 0\n\t"
      "global_load_dwordx4 v[152:155], v244, s[58:59] sc1\n\t"
      "s_add_u32 s58, s74, s46\n\t"
      "s_addc_u32 s59, s75, 0\n\t"
      "global_load_dwordx4 v[156:159], v244, s[58:59] sc1\n\t"
      "s_waitcnt vmcnt(0)\n\t"
      "v_max3_u32 v251, v132, v133, v134\n\t"
      "v_max3_u32 v251, v251, v135, v136\n\t"
      "v_max3_u32 v251, v251, v137, v138\n\t"
      "v_max3_u32 v251, v251, v139, v140\n\t"
      "v_max3_u32 v251, v251, v141, v142\n\t"
      "v_max3_u32 v251, v251, v143, v144\n\t"
      "v_max3_u32 v251, v251, v145, v146\n\t"
      "v_max3_u32 v251, v251, v147, v148\n\t"
      "v_max3_u32 v251, v251, v149, v150\n\t"
      "v_max3_u32 v251, v251, v151, v152\n\t"
      "v_max3_u32 v251, v251, v153, v154\n\t"
      "v_max3_u32 v251, v251, v155, v156\n\t"
      "v_max3_u32 v251, v251, v157, v158\n\t"
      "v_max_u32_e32 v251, v251, v159\n\t"
      "v_cmp_eq_u32_e32 vcc, -1, v251\n\t"
      "s_cbranch_vccnz LA45L_%=\n\t"
      "LA45X_%=:\n\t"
      "v_mfma_f32_16x16x32_bf16 v[212:215], v[132:135], v[0:3], 0\n\t"
      "v_mfma_f32_16x16x32_bf16 v[216:219], v[132:135], v[4:7], 0\n\t"
      "v_mfma_f32_16x16x32_bf16 v[220:223], v[132:135], v[8:11], 0\n\t"
      "v_mfma_f32_16x16x32_bf16 v[224:227], v[132:135], v[12:15], 0\n\t"
      "v_mfma_f32_16x16x32_bf16 v[212:215], v[136:139], v[16:19], v[212:215]\n\t"
      "v_mfma_f32_16x16x32_bf16 v[216:219], v[136:139], v[20:23], v[216:219]\n\t"
      "v_mfma_f32_16x16x32_bf16 v[220:223], v[136:139], v[24:27], v[220:223]\n\t"
      "v_mfma_f32_16x16x32_bf16 v[224:227], v[136:139], v[28:31], v[224:227]\n\t"
      "v_mfma_f32_16x16x32_bf16 v[212:215], v[140:143], v[32:35], v[212:215]\n\t"
      "v_mfma_f32_16x16x32_bf16 v[216:219], v[140:143], v[36:39], v[216:219]\n\t"
      "v_mfma_f32_16x16x32_bf16 v[220:223], v[140:143], v[40:43], v[220:223]\n\t"
      "v_mfma_f32_16x16x32_bf16 v[224:227], v[140:143], v[44:47], v[224:227]\n\t"
      "v_mfma_f32_16x16x32_bf16 v[212:215], v[144:147], v[48:51], v[212:215]\n\t"
      "v_mfma_f32_16x16x32_bf16 v[216:219], v[144:147], v[52:55], v[216:219]\n\t"
      "v_mfma_f32_16x16x32_bf16 v[220:223], v[144:147], v[56:59], v[220:223]\n\t"
      "v_mfma_f32_16x16x32_bf16 v[224:227], v[144:147], v[60:63], v[224:227]\n\t"
      "v_mfma_f32_16x16x32_bf16 v[212:215], v[148:151], v[64:67], v[212:215]\n\t"
      "v_mfma_f32_16x16x32_bf16 v[216:219], v[148:151], v[68:71], v[216:219]\n\t"
      "v_mfma_f32_16x16x32_bf16 v[220:223], v[148:151], v[72:75], v[220:223]\n\t"
      "v_mfma_f32_16x16x32_bf16 v[224:227], v[148:151], v[76:79], v[224:227]\n\t"
      "v_mfma_f32_16x16x32_bf16 v[212:215], v[152:155], v[80:83], v[212:215]\n\t"
      "v_mfma_f32_16x16x32_bf16 v[216:219], v[152:155], v[84:87], v[216:219]\n\t"
      "v_mfma_f32_16x16x32_bf16 v[220:223], v[152:155], v[88:91], v[220:223]\n\t"
      "v_mfma_f32_16x16x32_bf16 v[224:227], v[152:155], v[92:95], v[224:227]\n\t"
      "v_mfma_f32_16x16x32_bf16 v[212:215], v[156:159], v[96:99], v[212:215]\n\t"
      "v_mfma_f32_16x16x32_bf16 v[216:219], v[156:159], v[100:103], v[216:219]\n\t"
      "v_mfma_f32_16x16x32_bf16 v[220:223], v[156:159], v[104:107], v[220:223]\n\t"
      "v_mfma_f32_16x16x32_bf16 v[224:227], v[156:159], v[108:111], v[224:227]\n\t"
      "s_nop 7\n\t"
      "s_branch LA45B_%=\n\t"
      "LA46R_%=:\n\t"
      "s_mov_b32 s80, 0\n\t"
      "LA46L_%=:\n\t"
      "s_add_u32 s80, s80, 1\n\t"
      "s_cmp_lt_u32 s80, 262144\n\t"
      "s_cbranch_scc0 LA46X_%=\n\t"
      "s_waitcnt vmcnt(0)\n\t"
      "PQ6R_%=:\n\t"
      "PQ6L_%=:\n\t"
      "s_add_u32 s80, s80, 1\n\t"
      "s_cmp_lt_u32 s80, 262144\n\t"
      "s_cbranch_scc0 PQ6X_%=\n\t"
      "s_sleep 1\n\t"
      "global_load_dword v251, v252, s[76:77] offset:252 sc1\n\t"
      "s_waitcnt vmcnt(0)\n\t"
      "v_cmp_eq_u32_e32 vcc, -1, v251\n\t"
      "s_cbranch_vccnz PQ6L_%=\n\t"
      "PQ6X_%=:\n\t"
      "s_add_u32 s58, s76, s40\n\t"
      "s_addc_u32 s59, s77, 0\n\t"
      "global_load_dwordx4 v[160:163], v244, s[58:59] sc1\n\t"
      "s_add_u32 s58, s76, s41\n\t"
      "s_addc_u32 s59, s77, 0\n\t"
      "global_load_dwordx4 v[164:167], v244, s[58:59] sc1\n\t"
      "s_add_u32 s58, s76, s42\n\t"
      "s_addc_u32 s59, s77, 0\n\t"
      "global_load_dwordx4 v[168:171], v244, s[58:59] sc1\n\t"
      "s_add_u32 s58, s76, s43\n\t"
      "s_addc_u32 s59, s77, 0\n\t"
      "global_load_dwordx4 v[172:175], v244, s[58:59] sc1\n\t"
      "s_add_u32 s58, s76, s44\n\t"
      "s_addc_u32 s59, s77, 0\n\t"
      "global_load_dwordx4 v[176:179], v244, s[58:59] sc1\n\t"
      "s_add_u32 s58, s76, s45\n\t"
      "s_addc_u32 s59, s77, 0\n\t"
      "global_load_dwordx4 v[180:183], v244, s[58:59] sc1\n\t"
      "s_add_u32 s58, s76, s46\n\t"
      "s_addc_u32 s59, s77, 0\n\t"
      "global_load_dwordx4 v[184:187], v244, s[58:59] sc1\n\t"
      "s_waitcnt vmcnt(0)\n\t"
      "v_max3_u32 v251, v160, v161, v162\n\t"
      "v_max3_u32 v251, v251, v163, v164\n\t"
      "v_max3_u32 v251, v251, v165, v166\n\t"
      "v_max3_u32 v251, v251, v167, v168\n\t"
      "v_max3_u32 v251, v251, v169, v170\n\t"
      "v_max3_u32 v251, v251, v171, v172\n\t"
      "v_max3_u32 v251, v251, v173, v174\n\t"
      "v_max3_u32 v251, v251, v175, v176\n\t"
      "v_max3_u32 v251, v251, v177, v178\n\t"
      "v_max3_u32 v251, v251, v179, v180\n\t"
      "v_max3_u32 v251, v251, v181, v182\n\t"
      "v_max3_u32 v251, v251, v183, v184\n\t"
      "v_max3_u32 v251, v251, v185, v186\n\t"
      "v_max_u32_e32 v251, v251, v187\n\t"
      "v_cmp_eq_u32_e32 vcc, -1, v251\n\t"
      "s_cbranch_vccnz LA46L_%=\n\t"
      "LA46X_%=:\n\t"
      "v_mfma_f32_16x16x32_bf16 v[196:199], v[160:163], v[0:3], 0\n\t"
      "v_mfma_f32_16x16x32_bf16 v[200:203], v[160:163], v[4:7], 0\n\t"
      "v_mfma_f32_16x16x32_bf16 v[204:207], v[160:163], v[8:11], 0\n\t"
      "v_mfma_f32_16x16x32_bf16 v[208:211], v[160:163], v[12:15], 0\n\t"
      "v_mfma_f32_16x16x32_bf16 v[196:199], v[164:167], v[16:19], v[196:199]\n\t"
      "v_mfma_f32_16x16x32_bf16 v[200:203], v[164:167], v[20:23], v[200:203]\n\t"
      "v_mfma_f32_16x16x32_bf16 v[204:207], v[164:167], v[24:27], v[204:207]\n\t"
      "v_mfma_f32_16x16x32_bf16 v[208:211], v[164:167], v[28:31], v[208:211]\n\t"
      "v_mfma_f32_16x16x32_bf16 v[196:199], v[168:171], v[32:35], v[196:199]\n\t"
      "v_mfma_f32_16x16x32_bf16 v[200:203], v[168:171], v[36:39], v[200:203]\n\t"
      "v_mfma_f32_16x16x32_bf16 v[204:207], v[168:171], v[40:43], v[204:207]\n\t"
      "v_mfma_f32_16x16x32_bf16 v[208:211], v[168:171], v[44:47], v[208:211]\n\t"
      "v_mfma_f32_16x16x32_bf16 v[196:199], v[172:175], v[48:51], v[196:199]\n\t"
      "v_mfma_f32_16x16x32_bf16 v[200:203], v[172:175], v[52:55], v[200:203]\n\t"
      "v_mfma_f32_16x16x32_bf16 v[204:207], v[172:175], v[56:59], v[204:207]\n\t"
      "v_mfma_f32_16x16x32_bf16 v[208:211], v[172:175], v[60:63], v[208:211]\n\t"
      "v_mfma_f32_16x16x32_bf16 v[196:199], v[176:179], v[64:67], v[196:199]\n\t"
      "v_mfma_f32_16x16x32_bf16 v[200:203], v[176:179], v[68:71], v[200:203]\n\t"
      "v_mfma_f32_16x16x32_bf16 v[204:207], v[176:179], v[72:75], v[204:207]\n\t"
      "v_mfma_f32_16x16x32_bf16 v[208:211], v[176:179], v[76:79], v[208:211]\n\t"
      "v_mfma_f32_16x16x32_bf16 v[196:199], v[180:183], v[80:83], v[196:199]\n\t"
      "v_mfma_f32_16x16x32_bf16 v[200:203], v[180:183], v[84:87], v[200:203]\n\t"
      "v_mfma_f32_16x16x32_bf16 v[204:207], v[180:183], v[88:91], v[204:207]\n\t"
      "v_mfma_f32_16x16x32_bf16 v[208:211], v[180:183], v[92:95], v[208:211]\n\t"
      "v_mfma_f32_16x16x32_bf16 v[196:199], v[184:187], v[96:99], v[196:199]\n\t"
      "v_mfma_f32_16x16x32_bf16 v[200:203], v[184:187], v[100:103], v[200:203]\n\t"
      "v_mfma_f32_16x16x32_bf16 v[204:207], v[184:187], v[104:107], v[204:207]\n\t"
      "v_mfma_f32_16x16x32_bf16 v[208:211], v[184:187], v[108:111], v[208:211]\n\t"
      "s_nop 7\n\t"
      "s_branch LA46B_%=\n\t"
      : "+{s[0:1]}"(ka), "+{v0}"(tid)
      : "{s2}"(blockIdx.x)
      : "memory", "vcc", "scc", "m0",
        "v1", "v2", "v3", "v4", "v5", "v6", "v7", "v8", "v9", "v10", "v11", "v12", "v13", "v14", "v15", "v16", "v17",
        "v18", "v19", "v20", "v21", "v22", "v23", "v24", "v25", "v26", "v27", "v28", "v29", "v30", "v31", "v32", "v33",
        "v34", "v35", "v36", "v37", "v38", "v39", "v40", "v41", "v42", "v43", "v44", "v45", "v46", "v47", "v48", "v49",
        "v50", "v51", "v52", "v53", "v54", "v55", "v56", "v57", "v58", "v59", "v60", "v61", "v62", "v63", "v64", "v65",
        "v66", "v67", "v68", "v69", "v70", "v71", "v72", "v73", "v74", "v75", "v76", "v77", "v78", "v79", "v80", "v81",
        "v82", "v83", "v84", "v85", "v86", "v87", "v88", "v89", "v90", "v91", "v92", "v93", "v94", "v95", "v96", "v97",
        "v98", "v99", "v100", "v101", "v102", "v103", "v104", "v105", "v106", "v107", "v108", "v109", "v110", "v111",
        "v112", "v113", "v114", "v115", "v116", "v117", "v118", "v119", "v120", "v121", "v122", "v123", "v124", "v125",
        "v126", "v127", "v128", "v129", "v130", "v131", "v132", "v133", "v134", "v135", "v136", "v137", "v138", "v139",
        "v140", "v141", "v142", "v143", "v144", "v145", "v146", "v147", "v148", "v149", "v150", "v151", "v152", "v153",
        "v154", "v155", "v156", "v157", "v158", "v159", "v160", "v161", "v162", "v163", "v164", "v165", "v166", "v167",
        "v168", "v169", "v170", "v171", "v172", "v173", "v174", "v175", "v176", "v177", "v178", "v179", "v180", "v181",
        "v182", "v183", "v184", "v185", "v186", "v187", "v188", "v189", "v190", "v191", "v192", "v193", "v194", "v195",
        "v196", "v197", "v198", "v199", "v200", "v201", "v202", "v203", "v204", "v205", "v206", "v207", "v208", "v209",
        "v210", "v211", "v212", "v213", "v214", "v215", "v216", "v217", "v218", "v219", "v220", "v221", "v222", "v223",
        "v224", "v225", "v226", "v227", "v228", "v229", "v230", "v231", "v232", "v233", "v234", "v235", "v236", "v237",
        "v238", "v239", "v240", "v241", "v242", "v243", "v244", "v245", "v246", "v247", "v248", "v249", "v250", "v251",
        "v252", "v253", "v254", "v255", "s3", "s4", "s5", "s6", "s7", "s8", "s9", "s10", "s11", "s12", "s13", "s14",
        "s15", "s16", "s17", "s18", "s19", "s20", "s21", "s22", "s23", "s24", "s25", "s26", "s27", "s28", "s29", "s30",
        "s31", "s32", "s33", "s34", "s35", "s36", "s37", "s38", "s39", "s40", "s41", "s42", "s43", "s44", "s45", "s46",
        "s47", "s48", "s49", "s50", "s51", "s52", "s53", "s54", "s55", "s56", "s57", "s58", "s59", "s60", "s61", "s62",
        "s63", "s64", "s65", "s66", "s67", "s68", "s69", "s70", "s71", "s72", "s73", "s74", "s75", "s76", "s77", "s78",
        "s79", "s80", "s81", "s82", "s83"
  );
#endif
}

extern "C" void kernel_launch(void* const* d_in, const int* in_sizes, int n_in, void* d_out, int out_size,
                              void* d_ws, size_t ws_size, hipStream_t stream) {
  (void)hipFuncSetAttribute((const void*)fused_block, hipFuncAttributeMaxDynamicSharedMemorySize, 163840);
  fused_block<<<256, 512, 163840, stream>>>((const u16*)d_in[0], (const u16*)d_in[1], (const u16*)d_in[2],
                                            (const u16*)d_in[4], (const u16*)d_in[5], (const u16*)d_in[6],
                                            (const u16*)d_in[7], (u16*)d_out, (char*)d_ws);
}
```

```cpp
#include <hip/hip_runtime.h>
#include <stdint.h>
typedef unsigned short u16;
__global__ __launch_bounds__(512, 2) void fused_block(const u16* x, const u16* wqkv, const u16* wo, const u16* w1, const u16* b1,
                                                   const u16* w2, const u16* b2, u16* out, char* ws) {
#if defined(__HIP_DEVICE_COMPILE__)
  unsigned long long ka = (unsigned long long)__builtin_amdgcn_kernarg_segment_ptr();
  unsigned tid = threadIdx.x;
  asm volatile(
      "s_load_dwordx16 s[4:19], s[0:1], 0x0\n\t"
      "s_load_dwordx2 s[20:21], s[0:1], 0x40\n\t"
      "v_lshrrev_b32_e32 v199, 6, v0\n\t"
      "v_and_b32_e32 v192, 63, v0\n\t"
      "v_readfirstlane_b32 s24, v199\n\t"
      "s_lshr_b32 s36, s2, 3\n\t"
      "s_and_b32 s35, s36, 28\n\t"
      "s_lshr_b32 s26, 0x63752041, s35\n\t"
      "s_and_b32 s26, s26, 7\n\t"
      "s_and_b32 s35, s36, 3\n\t"
      "s_and_b32 s34, s2, 7\n\t"
      "s_lshl2_add_u32 s25, s34, s35\n\t"
      "s_and_b32 s34, s25, 30\n\t"
      "s_lshl_b32 s34, s34, 6\n\t"
      "s_and_b32 s35, s25, 1\n\t"
      "s_add_u32 s38, s34, s35\n\t"
      "v_lshrrev_b32_e32 v197, 3, v192\n\t"
      "v_and_b32_e32 v198, 7, v192\n\t"
      "s_min_u32 s28, s26, 6\n\t"
      "s_lshl4_add_u32 s34, s24, s38\n\t"
      "s_mul_i32 s34, s34, 896\n\t"
      "s_movk_i32 s35, 1792\n\t"
      "v_lshl_add_u32 v199, v198, 4, s34\n\t"
      "v_mad_u32_u24 v199, v197, s35, v199\n\t"
      "s_lshl_b32 s34, s28, 7\n\t"
      "s_cmp_lg_u32 s24, 0\n\t"
      "s_cbranch_scc1 NTC_%=\n\t"
      "s_getpc_b64 s[58:59]\n\t"
      "s_lshr_b32 s37, s2, 3\n\t"
      "v_min_u32_e32 v200, 9, v192\n\t"
      "v_lshl_add_u32 v200, v200, 5, s37\n\t"
      "s_mov_b32 m0, 64512\n\t"
      "v_min_u32_e32 v200, 289, v200\n\t"
      "v_lshlrev_b32_e32 v200, 7, v200\n\t"
      "global_load_lds_dword v200, s[58:59]\n\t"
      "NTC_%=:\n\t"
      "v_add_u32_e32 v120, s34, v199\n\t"
      "s_add_u32 s35, s34, 128\n\t"
      "s_sub_u32 s36, s35, 896\n\t"
      "s_min_u32 s35, s35, s36\n\t"
      "v_add_u32_e32 v121, s35, v199\n\t"
      "s_waitcnt lgkmcnt(0)\n\t"
      "global_load_dwordx4 v[204:207], v120, s[4:5]\n\t"
      "s_add_u32 s35, s34, 256\n\t"
      "s_sub_u32 s36, s35, 896\n\t"
      "s_min_u32 s35, s35, s36\n\t"
      "v_add_u32_e32 v122, s35, v199\n\t"
      "global_load_dwordx4 v[4:7], v121, s[4:5]\n\t"
      "s_add_u32 s35, s34, 384\n\t"
      "s_sub_u32 s36, s35, 896\n\t"
      "s_min_u32 s35, s35, s36\n\t"
      "v_add_u32_e32 v123, s35, v199\n\t"
      "global_load_dwordx4 v[8:11], v122, s[4:5]\n\t"
      "s_add_u32 s35, s34, 512\n\t"
      "s_sub_u32 s36, s35, 896\n\t"
      "s_min_u32 s35, s35, s36\n\t"
      "v_add_u32_e32 v124, s35, v199\n\t"
      "global_load_dwordx4 v[12:15], v123, s[4:5]\n\t"
      "s_add_u32 s35, s34, 640\n\t"
      "s_sub_u32 s36, s35, 896\n\t"
      "s_min_u32 s35, s35, s36\n\t"
      "v_add_u32_e32 v125, s35, v199\n\t"
      "s_add_u32 s35, s34, 768\n\t"
      "s_sub_u32 s36, s35, 896\n\t"
      "s_min_u32 s35, s35, s36\n\t"
      "v_add_u32_e32 v126, s35, v199\n\t"
      "s_cmp_lt_u32 s25, 2\n\t"
      "s_cselect_b32 s34, 0, 16\n\t"
      "s_sub_u32 s34, s38, s34\n\t"
      "s_lshl1_add_u32 s34, s24, s34\n\t"
      "s_mul_i32 s34, s34, 896\n\t"
      "s_and_b32 s33, s25, 3\n\t"
      "s_lshl_b32 s33, s33, 1\n\t"
      "v_min_u32_e32 v200, 55, v192\n\t"
      "s_lshl_b32 s35, s33, 3\n\t"
      "v_add_u32_e32 v201, s35, v200\n\t"
      "v_subrev_u32_e32 v202, 56, v201\n\t"
      "v_min_u32_e32 v201, v201, v202\n\t"
      "v_lshl_add_u32 v127, v201, 4, s34\n\t"
      "s_and_b32 s39, s24, 3\n\t"
      "s_lshl_b32 s39, s39, 12\n\t"
      "s_cmp_lt_u32 s24, 4\n\t"
      "s_cselect_b32 s27, 4, 0\n\t"
      "v_lshrrev_b32_e32 v193, 4, v192\n\t"
      "v_and_b32_e32 v194, 15, v192\n\t"
      "v_bfe_u32 v195, v192, 2, 2\n\t"
      "v_and_b32_e32 v196, 3, v192\n\t"
      "v_lshlrev_b32_e32 v244, 4, v192\n\t"
      "s_lshr_b32 s34, s24, 1\n\t"
      "s_mul_i32 s34, s34, 14336\n\t"
      "s_and_b32 s35, s24, 1\n\t"
      "s_lshl_b32 s35, s35, 7\n\t"
      "s_add_u32 s34, s34, s35\n\t"
      "v_xor_b32_e32 v199, v197, v198\n\t"
      "v_lshl_add_u32 v199, v199, 4, s34\n\t"
      "v_lshl_add_u32 v199, v198, 8, v199\n\t"
      "s_sub_u32 s35, s28, s33\n\t"
      "s_cselect_b32 s36, 7, 0\n\t"
      "s_add_u32 s35, s35, s36\n\t"
      "s_lshl_b32 s35, s35, 11\n\t"
      "s_add_u32 s34, s35, 0\n\t"
      "s_sub_u32 s36, s34, 14336\n\t"
      "s_min_u32 s34, s34, s36\n\t"
      "v_add_u32_e32 v112, s34, v199\n\t"
      "s_add_u32 s34, s35, 2048\n\t"
      "s_sub_u32 s36, s34, 14336\n\t"
      "s_min_u32 s34, s34, s36\n\t"
      "v_add_u32_e32 v113, s34, v199\n\t"
      "s_add_u32 s34, s35, 4096\n\t"
      "s_sub_u32 s36, s34, 14336\n\t"
      "s_min_u32 s34, s34, s36\n\t"
      "v_add_u32_e32 v114, s34, v199\n\t"
      "s_add_u32 s34, s35, 6144\n\t"
      "s_sub_u32 s36, s34, 14336\n\t"
      "s_min_u32 s34, s34, s36\n\t"
      "v_add_u32_e32 v115, s34, v199\n\t"
      "s_add_u32 s34, s35, 8192\n\t"
      "s_sub_u32 s36, s34, 14336\n\t"
      "s_min_u32 s34, s34, s36\n\t"
      "v_add_u32_e32 v116, s34, v199\n\t"
      "s_add_u32 s34, s35, 10240\n\t"
      "s_sub_u32 s36, s34, 14336\n\t"
      "s_min_u32 s34, s34, s36\n\t"
      "v_add_u32_e32 v117, s34, v199\n\t"
      "s_add_u32 s34, s35, 12288\n\t"
      "s_sub_u32 s36, s34, 14336\n\t"
      "s_min_u32 s34, s34, s36\n\t"
      "v_add_u32_e32 v118, s34, v199\n\t"
      "v_min_u32_e32 v200, 55, v192\n\t"
      "v_lshrrev_b32_e32 v201, 2, v200\n\t"
      "v_lshlrev_b32_e32 v119, 10, v201\n\t"
      "v_cmp_lt_u32_e32 vcc, 6, v201\n\t"
      "v_mov_b32_e32 v202, 57344\n\t"
      "v_mov_b32_e32 v201, 50688\n\t"
      "v_cndmask_b32_e32 v202, v202, v201, vcc\n\t"
      "v_and_b32_e32 v201, 3, v200\n\t"
      "v_lshl_add_u32 v202, v201, 7, v202\n\t"
      "v_and_b32_e32 v200, 7, v200\n\t"
      "v_xor_b32_e32 v200, s24, v200\n\t"
      "v_lshl_add_u32 v202, v200, 4, v202\n\t"
      "v_add_u32_e32 v119, v119, v202\n\t"
      "s_bitcmp1_b32 s27, 2\n\t"
      "s_cbranch_scc1 LSET_%=\n\t"
      "v_xor_b32_e32 v199, v194, v193\n\t"
      "v_lshlrev_b32_e32 v199, 4, v199\n\t"
      "v_lshl_or_b32 v200, v193, 8, v199\n\t"
      "s_and_b32 s34, s24, 1\n\t"
      "s_mul_i32 s35, s34, 28672\n\t"
      "v_add_u32_e32 v160, s35, v200\n\t"
      "v_xor_b32_e32 v161, 64, v160\n\t"
      "v_and_b32_e32 v199, 0x70, v199\n\t"
      "v_lshl_or_b32 v199, v193, 7, v199\n\t"
      "v_add_u32_e32 v199, 57344, v199\n\t"
      "v_add_u32_e32 v200, 14336, v200\n\t"
      "s_cmp_eq_u32 s34, 1\n\t"
      "s_cselect_b64 vcc, -1, 0\n\t"
      "s_cselect_b32 s35, 0, 6656\n\t"
      "v_cndmask_b32_e32 v162, v199, v200, vcc\n\t"
      "v_xor_b32_e32 v163, 64, v162\n\t"
      "v_subrev_u32_e32 v164, s35, v162\n\t"
      "v_subrev_u32_e32 v165, s35, v163\n\t"
      "s_bfe_u32 s35, s24, 65537\n\t"
      "s_mul_i32 s35, s35, 7\n\t"
      "v_and_b32_e32 v201, 24, v192\n\t"
      "v_lshlrev_b32_e32 v201, 2, v201\n\t"
      "v_lshlrev_b32_e32 v202, 7, v195\n\t"
      "v_lshl_or_b32 v202, v193, 12, v202\n\t"
      "v_add_u32_e32 v202, 65536, v202\n\t"
      "v_add_lshl_u32 v199, v196, s35, 3\n\t"
      "v_xor_b32_e32 v199, v199, v201\n\t"
      "v_add_u32_e32 v166, v199, v202\n\t"
      "v_min_u32_e32 v199, 2, v196\n\t"
      "s_add_u32 s35, s35, 4\n\t"
      "v_add_lshl_u32 v199, v199, s35, 3\n\t"
      "v_xor_b32_e32 v199, v199, v201\n\t"
      "v_add_u32_e32 v167, v199, v202\n\t"
      "v_add_u32_e32 v168, 0x10000, v166\n\t"
      "v_add_u32_e32 v169, 0x10000, v167\n\t"
      "v_lshlrev_b32_e32 v199, 11, v193\n\t"
      "v_sub_u32_e32 v170, v166, v199\n\t"
      "v_sub_u32_e32 v171, v167, v199\n\t"
      "s_branch NORM_%=\n\t"
      "LSET_%=:\n\t"
      "s_add_u32 s60, s20, 16777216\n\t"
      "s_addc_u32 s61, s21, 0\n\t"
      "s_load_dwordx2 s[22:23], s[60:61], 0x0\n\t"
      "s_lshr_b32 s34, s39, 9\n\t"
      "v_add_u32_e32 v199, s34, v197\n\t"
      "s_and_b32 s34, s24, 1\n\t"
      "s_lshl_b32 s34, s34, 2\n\t"
      "v_and_b32_e32 v200, 2, v197\n\t"
      "v_or_b32_e32 v200, s34, v200\n\t"
      "v_xor_b32_e32 v200, v200, v198\n\t"
      "v_min_u32_e32 v200, 6, v200\n\t"
      "v_lshlrev_b32_e32 v200, 4, v200\n\t"
      "s_movk_i32 s34, 2688\n\t"
      "v_mad_u32_u24 v149, v199, s34, v200\n\t"
      "v_add_u32_e32 v148, 128, v149\n\t"
      "v_add_u32_e32 v150, 84096, v149\n\t"
      "v_add_u32_e32 v151, 83968, v149\n\t"
      "v_add_u32_e32 v152, 85120, v149\n\t"
      "s_sub_u32 s6, s6, 128\n\t"
      "s_subb_u32 s7, s7, 0\n\t"
      "s_mul_i32 s37, s26, 112\n\t"
      "s_mov_b32 s81, s33\n\t"
      "s_mov_b32 s82, s33\n\t"
      "s_and_b32 s35, s25, 3\n\t"
      "s_lshl2_add_u32 s31, s26, s35\n\t"
      "s_and_b32 s35, s2, 7\n\t"
      "s_mul_i32 s34, s35, 14\n\t"
      "s_lshl4_add_u32 s35, s35, -14\n\t"
      "s_cmp_lt_u32 s31, 14\n\t"
      "s_cselect_b32 s34, s34, s35\n\t"
      "s_cselect_b32 s36, 1, 0\n\t"
      "s_or_b32 s27, s27, s36\n\t"
      "s_add_u32 s34, s34, s31\n\t"
      "s_lshr_b32 s29, s34, 2\n\t"
      "s_and_b32 s32, s34, 3\n\t"
      "s_mul_i32 s30, s32, 7\n\t"
      "v_and_b32_e32 v199, 60, v192\n\t"
      "v_and_b32_e32 v200, 24, v192\n\t"
      "v_lshlrev_b32_e32 v202, 3, v196\n\t"
      "v_lshl_or_b32 v202, v200, 2, v202\n\t"
      "v_lshl_or_b32 v202, v195, 7, v202\n\t"
      "v_lshl_or_b32 v202, v193, 10, v202\n\t"
      "v_add_u32_e32 v224, s39, v202\n\t"
      "v_xor_b32_e32 v225, 32, v224\n\t"
      "v_xor_b32_e32 v226, 64, v224\n\t"
      "v_xor_b32_e32 v227, 96, v224\n\t"
      "s_lshl_b32 s34, s24, 12\n\t"
      "s_add_u32 s34, s34, 98304\n\t"
      "v_add_u32_e32 v245, s34, v244\n\t"
      "v_lshlrev_b32_e32 v199, 2, v199\n\t"
      "s_lshl_b32 s34, s24, 8\n\t"
      "v_lshl_add_u32 v247, v192, 2, s34\n\t"
      "v_and_b32_e32 v200, 2, v192\n\t"
      "v_mul_u32_u24_e32 v202, 576, v200\n\t"
      "v_xor_b32_e32 v199, v202, v199\n\t"
      "v_and_b32_e32 v202, 1, v192\n\t"
      "v_lshl_or_b32 v199, v202, 3, v199\n\t"
      "s_and_b32 s35, s34, 0x400\n\t"
      "s_add_u32 s34, s34, s35\n\t"
      "s_add_u32 s34, s34, 98304\n\t"
      "v_add_u32_e32 v246, s34, v199\n\t"
      "v_lshlrev_b32_e32 v199, 2, v202\n\t"
      "v_lshl_or_b32 v199, v200, 4, v199\n\t"
      "s_min_u32 s34, s29, 27\n\t"
      "s_lshl_b32 s34, s34, 7\n\t"
      "s_lshr_b32 s35, s24, 2\n\t"
      "s_lshl_b32 s35, s35, 6\n\t"
      "s_add_u32 s34, s34, s35\n\t"
      "s_lshr_b32 s35, s39, 9\n\t"
      "s_add_u32 s34, s34, s35\n\t"
      "v_add_u32_e32 v248, s34, v199\n\t"
      "s_add_u32 s48, s20, 8388608\n\t"
      "s_addc_u32 s49, s21, 0\n\t"
      "v_and_b32_e32 v199, 2, v197\n\t"
      "v_xor_b32_e32 v199, v199, v198\n\t"
      "v_lshlrev_b32_e32 v199, 4, v199\n\t"
      "s_movk_i32 s34, 7168\n\t"
      "v_mad_u32_u24 v144, v197, s34, v199\n\t"
      "v_xor_b32_e32 v199, 64, v144\n\t"
      "v_add_u32_e32 v145, 56320, v199\n\t"
      "v_add_u32_e32 v146, 112640, v144\n\t"
      "v_add_u32_e32 v147, 168960, v199\n\t"
      "v_and_b32_e32 v199, 6, v197\n\t"
      "v_xor_b32_e32 v199, v199, v198\n\t"
      "v_lshlrev_b32_e32 v199, 4, v199\n\t"
      "s_movk_i32 s35, 896\n\t"
      "v_mad_u32_u24 v154, v197, s35, v199\n\t"
      "v_add_u32_e32 v155, 6144, v154\n\t"
      "v_add_u32_e32 v156, 12288, v154\n\t"
      "v_add_u32_e32 v157, 18432, v154\n\t"
      "s_mul_i32 s34, s24, 917504\n\t"
      "s_lshl_b32 s35, s29, 7\n\t"
      "s_add_u32 s34, s34, s35\n\t"
      "s_add_u32 s52, s10, s34\n\t"
      "s_addc_u32 s53, s11, 0\n\t"
      "s_lshl_b32 s34, s28, 7\n\t"
      "s_add_u32 s56, s8, s34\n\t"
      "s_addc_u32 s57, s9, 0\n\t"
      "s_mov_b32 s34, 0\n\t"
      "s_and_b32 s35, s34, 1\n\t"
      "s_and_b32 s34, s34, 2\n\t"
      "s_lshl_b32 s34, s34, 6\n\t"
      "s_add_u32 s34, s34, s35\n\t"
      "s_mul_i32 s76, s34, 3584\n\t"
      "s_mov_b32 s34, 1\n\t"
      "s_and_b32 s35, s34, 1\n\t"
      "s_and_b32 s34, s34, 2\n\t"
      "s_lshl_b32 s34, s34, 6\n\t"
      "s_add_u32 s34, s34, s35\n\t"
      "s_mul_i32 s77, s34, 3584\n\t"
      "s_mov_b32 s34, 2\n\t"
      "s_and_b32 s35, s34, 1\n\t"
      "s_and_b32 s34, s34, 2\n\t"
      "s_lshl_b32 s34, s34, 6\n\t"
      "s_add_u32 s34, s34, s35\n\t"
      "s_mul_i32 s78, s34, 3584\n\t"
      "s_mov_b32 s34, 3\n\t"
      "s_and_b32 s35, s34, 1\n\t"
      "s_and_b32 s34, s34, 2\n\t"
      "s_lshl_b32 s34, s34, 6\n\t"
      "s_add_u32 s34, s34, s35\n\t"
      "s_mul_i32 s79, s34, 3584\n\t"
      "NORM_%=:\n\t"
      "global_load_dwordx4 v[16:19], v124, s[4:5]\n\t"
      "global_load_dwordx4 v[20:23], v125, s[4:5]\n\t"
      "global_load_dwordx4 v[24:27], v126, s[4:5]\n\t"
      "global_load_dwordx4 v[28:31], v127, s[4:5]\n\t"
      "v_mov_b32_e32 v199, 0x3f803f80\n\t"
      "s_waitcnt vmcnt(7)\n\t"
      "v_dot2_f32_bf16 v88, v204, v199, 0\n\t"
      "v_dot2_f32_bf16 v92, v204, v204, 0\n\t"
      "v_lshlrev_b32_e32 v32, 16, v204\n\t"
      "v_and_b32_e32 v33, 0xffff0000, v204\n\t"
      "v_dot2_f32_bf16 v89, v205, v199, 0\n\t"
      "v_dot2_f32_bf16 v93, v205, v205, 0\n\t"
      "v_lshlrev_b32_e32 v34, 16, v205\n\t"
      "v_and_b32_e32 v35, 0xffff0000, v205\n\t"
      "v_dot2_f32_bf16 v90, v206, v199, 0\n\t"
      "v_dot2_f32_bf16 v94, v206, v206, 0\n\t"
      "v_lshlrev_b32_e32 v36, 16, v206\n\t"
      "v_and_b32_e32 v37, 0xffff0000, v206\n\t"
      "v_dot2_f32_bf16 v91, v207, v199, 0\n\t"
      "v_dot2_f32_bf16 v95, v207, v207, 0\n\t"
      "v_lshlrev_b32_e32 v38, 16, v207\n\t"
      "v_and_b32_e32 v39, 0xffff0000, v207\n\t"
      "s_waitcnt vmcnt(6)\n\t"
      "v_dot2_f32_bf16 v88, v4, v199, v88\n\t"
      "v_dot2_f32_bf16 v92, v4, v4, v92\n\t"
      "v_lshlrev_b32_e32 v40, 16, v4\n\t"
      "v_and_b32_e32 v41, 0xffff0000, v4\n\t"
      "v_dot2_f32_bf16 v89, v5, v199, v89\n\t"
      "v_dot2_f32_bf16 v93, v5, v5, v93\n\t"
      "v_lshlrev_b32_e32 v42, 16, v5\n\t"
      "v_and_b32_e32 v43, 0xffff0000, v5\n\t"
      "v_dot2_f32_bf16 v90, v6, v199, v90\n\t"
      "v_dot2_f32_bf16 v94, v6, v6, v94\n\t"
      "v_lshlrev_b32_e32 v44, 16, v6\n\t"
      "v_and_b32_e32 v45, 0xffff0000, v6\n\t"
      "v_dot2_f32_bf16 v91, v7, v199, v91\n\t"
      "v_dot2_f32_bf16 v95, v7, v7, v95\n\t"
      "v_lshlrev_b32_e32 v46, 16, v7\n\t"
      "v_and_b32_e32 v47, 0xffff0000, v7\n\t"
      "s_waitcnt vmcnt(5)\n\t"
      "v_dot2_f32_bf16 v88, v8, v199, v88\n\t"
      "v_dot2_f32_bf16 v92, v8, v8, v92\n\t"
      "v_lshlrev_b32_e32 v48, 16, v8\n\t"
      "v_and_b32_e32 v49, 0xffff0000, v8\n\t"
      "v_dot2_f32_bf16 v89, v9, v199, v89\n\t"
      "v_dot2_f32_bf16 v93, v9, v9, v93\n\t"
      "v_lshlrev_b32_e32 v50, 16, v9\n\t"
      "v_and_b32_e32 v51, 0xffff0000, v9\n\t"
      "v_dot2_f32_bf16 v90, v10, v199, v90\n\t"
      "v_dot2_f32_bf16 v94, v10, v10, v94\n\t"
      "v_lshlrev_b32_e32 v52, 16, v10\n\t"
      "v_and_b32_e32 v53, 0xffff0000, v10\n\t"
      "v_dot2_f32_bf16 v91, v11, v199, v91\n\t"
      "v_dot2_f32_bf16 v95, v11, v11, v95\n\t"
      "v_lshlrev_b32_e32 v54, 16, v11\n\t"
      "v_and_b32_e32 v55, 0xffff0000, v11\n\t"
      "s_waitcnt vmcnt(4)\n\t"
      "v_dot2_f32_bf16 v88, v12, v199, v88\n\t"
      "v_dot2_f32_bf16 v92, v12, v12, v92\n\t"
      "v_lshlrev_b32_e32 v56, 16, v12\n\t"
      "v_and_b32_e32 v57, 0xffff0000, v12\n\t"
      "v_dot2_f32_bf16 v89, v13, v199, v89\n\t"
      "v_dot2_f32_bf16 v93, v13, v13, v93\n\t"
      "v_lshlrev_b32_e32 v58, 16, v13\n\t"
      "v_and_b32_e32 v59, 0xffff0000, v13\n\t"
      "v_dot2_f32_bf16 v90, v14, v199, v90\n\t"
      "v_dot2_f32_bf16 v94, v14, v14, v94\n\t"
      "v_lshlrev_b32_e32 v60, 16, v14\n\t"
      "v_and_b32_e32 v61, 0xffff0000, v14\n\t"
      "v_dot2_f32_bf16 v91, v15, v199, v91\n\t"
      "v_dot2_f32_bf16 v95, v15, v15, v95\n\t"
      "v_lshlrev_b32_e32 v62, 16, v15\n\t"
      "v_and_b32_e32 v63, 0xffff0000, v15\n\t"
      "s_waitcnt vmcnt(3)\n\t"
      "v_dot2_f32_bf16 v88, v16, v199, v88\n\t"
      "v_dot2_f32_bf16 v92, v16, v16, v92\n\t"
      "v_lshlrev_b32_e32 v64, 16, v16\n\t"
      "v_and_b32_e32 v65, 0xffff0000, v16\n\t"
      "v_dot2_f32_bf16 v89, v17, v199, v89\n\t"
      "v_dot2_f32_bf16 v93, v17, v17, v93\n\t"
      "v_lshlrev_b32_e32 v66, 16, v17\n\t"
      "v_and_b32_e32 v67, 0xffff0000, v17\n\t"
      "v_dot2_f32_bf16 v90, v18, v199, v90\n\t"
      "v_dot2_f32_bf16 v94, v18, v18, v94\n\t"
      "v_lshlrev_b32_e32 v68, 16, v18\n\t"
      "v_and_b32_e32 v69, 0xffff0000, v18\n\t"
      "v_dot2_f32_bf16 v91, v19, v199, v91\n\t"
      "v_dot2_f32_bf16 v95, v19, v19, v95\n\t"
      "v_lshlrev_b32_e32 v70, 16, v19\n\t"
      "v_and_b32_e32 v71, 0xffff0000, v19\n\t"
      "s_waitcnt vmcnt(2)\n\t"
      "v_dot2_f32_bf16 v88, v20, v199, v88\n\t"
      "v_dot2_f32_bf16 v92, v20, v20, v92\n\t"
      "v_lshlrev_b32_e32 v72, 16, v20\n\t"
      "v_and_b32_e32 v73, 0xffff0000, v20\n\t"
      "v_dot2_f32_bf16 v89, v21, v199, v89\n\t"
      "v_dot2_f32_bf16 v93, v21, v21, v93\n\t"
      "v_lshlrev_b32_e32 v74, 16, v21\n\t"
      "v_and_b32_e32 v75, 0xffff0000, v21\n\t"
      "v_dot2_f32_bf16 v90, v22, v199, v90\n\t"
      "v_dot2_f32_bf16 v94, v22, v22, v94\n\t"
      "v_lshlrev_b32_e32 v76, 16, v22\n\t"
      "v_and_b32_e32 v77, 0xffff0000, v22\n\t"
      "v_dot2_f32_bf16 v91, v23, v199, v91\n\t"
      "v_dot2_f32_bf16 v95, v23, v23, v95\n\t"
      "v_lshlrev_b32_e32 v78, 16, v23\n\t"
      "v_and_b32_e32 v79, 0xffff0000, v23\n\t"
      "s_waitcnt vmcnt(1)\n\t"
      "v_dot2_f32_bf16 v88, v24, v199, v88\n\t"
      "v_dot2_f32_bf16 v92, v24, v24, v92\n\t"
      "v_lshlrev_b32_e32 v80, 16, v24\n\t"
      "v_and_b32_e32 v81, 0xffff0000, v24\n\t"
      "v_dot2_f32_bf16 v89, v25, v199, v89\n\t"
      "v_dot2_f32_bf16 v93, v25, v25, v93\n\t"
      "v_lshlrev_b32_e32 v82, 16, v25\n\t"
      "v_and_b32_e32 v83, 0xffff0000, v25\n\t"
      "v_dot2_f32_bf16 v90, v26, v199, v90\n\t"
      "v_dot2_f32_bf16 v94, v26, v26, v94\n\t"
      "v_lshlrev_b32_e32 v84, 16, v26\n\t"
      "v_and_b32_e32 v85, 0xffff0000, v26\n\t"
      "v_dot2_f32_bf16 v91, v27, v199, v91\n\t"
      "v_dot2_f32_bf16 v95, v27, v27, v95\n\t"
      "v_lshlrev_b32_e32 v86, 16, v27\n\t"
      "v_and_b32_e32 v87, 0xffff0000, v27\n\t"
      "s_waitcnt vmcnt(0)\n\t"
      "s_bitcmp0_b32 s27, 2\n\t"
      "s_cbranch_scc1 SK1_%=\n\t"
      "s_mul_i32 s72, s81, 172032\n\t"
      "s_add_u32 s72, s72, s37\n\t"
      "s_add_u32 s40, s6, s72\n\t"
      "s_addc_u32 s41, s7, 0\n\t"
      "s_add_u32 s81, s81, 1\n\t"
      "s_cmp_eq_u32 s81, 7\n\t"
      "s_cselect_b32 s81, 0, s81\n\t"
      "s_add_u32 m0, s39, 131072\n\t"
      "s_nop 0\n\t"
      "global_load_lds_dwordx4 v148, s[40:41]\n\t"
      "global_load_lds_dwordx4 v149, s[40:41] offset:1024\n\t"
      "global_load_lds_dwordx4 v150, s[40:41] offset:2048\n\t"
      "global_load_lds_dwordx4 v151, s[40:41] offset:3072\n\t"
      "SK1_%=:\n\t"
      "v_dot2_f32_bf16 v96, v28, v199, 0\n\t"
      "v_dot2_f32_bf16 v97, v28, v28, 0\n\t"
      "v_dot2_f32_bf16 v96, v29, v199, v96\n\t"
      "v_dot2_f32_bf16 v97, v29, v29, v97\n\t"
      "v_dot2_f32_bf16 v96, v30, v199, v96\n\t"
      "v_dot2_f32_bf16 v97, v30, v30, v97\n\t"
      "v_dot2_f32_bf16 v96, v31, v199, v96\n\t"
      "v_dot2_f32_bf16 v97, v31, v31, v97\n\t"
      "v_cmp_gt_u32_e32 vcc, 56, v192\n\t"
      "v_pk_add_f32 v[88:89], v[88:89], v[90:91]\n\t"
      "v_pk_add_f32 v[92:93], v[92:93], v[94:95]\n\t"
      "v_cndmask_b32_e32 v96, 0, v96, vcc\n\t"
      "v_cndmask_b32_e32 v97, 0, v97, vcc\n\t"
      "v_add_f32_e32 v88, v88, v89\n\t"
      "v_add_f32_e32 v92, v92, v93\n\t"
      "v_add_f32_dpp v96, v96, v96 quad_perm:[1,0,3,2] row_mask:0xf bank_mask:0xf\n\t"
      "v_add_f32_dpp v97, v97, v97 quad_perm:[1,0,3,2] row_mask:0xf bank_mask:0xf\n\t"
      "v_add_f32_dpp v88, v88, v88 quad_perm:[1,0,3,2] row_mask:0xf bank_mask:0xf\n\t"
      "v_add_f32_dpp v92, v92, v92 quad_perm:[1,0,3,2] row_mask:0xf bank_mask:0xf\n\t"
      "v_add_f32_dpp v96, v96, v96 quad_perm:[2,3,0,1] row_mask:0xf bank_mask:0xf\n\t"
      "v_add_f32_dpp v97, v97, v97 quad_perm:[2,3,0,1] row_mask:0xf bank_mask:0xf\n\t"
      "v_add_f32_dpp v88, v88, v88 quad_perm:[2,3,0,1] row_mask:0xf bank_mask:0xf\n\t"
      "v_add_f32_dpp v92, v92, v92 quad_perm:[2,3,0,1] row_mask:0xf bank_mask:0xf\n\t"
      "v_add_f32_dpp v96, v96, v96 row_half_mirror row_mask:0xf bank_mask:0xf\n\t"
      "v_add_f32_dpp v97, v97, v97 row_half_mirror row_mask:0xf bank_mask:0xf\n\t"
      "v_add_f32_dpp v88, v88, v88 row_half_mirror row_mask:0xf bank_mask:0xf\n\t"
      "v_add_f32_dpp v92, v92, v92 row_half_mirror row_mask:0xf bank_mask:0xf\n\t"
      "v_mul_f32_e32 v88, 0xbb124925, v88\n\t"
      "v_add_f32_dpp v96, v96, v96 row_mirror row_mask:0xf bank_mask:0xf\n\t"
      "v_mul_f32_e32 v92, 0x3b124925, v92\n\t"
      "s_bitcmp0_b32 s27, 2\n\t"
      "s_cbranch_scc1 SK2_%=\n\t"
      "s_mul_i32 s72, s81, 172032\n\t"
      "s_add_u32 s72, s72, s37\n\t"
      "s_add_u32 s40, s6, s72\n\t"
      "s_addc_u32 s41, s7, 0\n\t"
      "s_add_u32 s81, s81, 1\n\t"
      "s_cmp_eq_u32 s81, 7\n\t"
      "s_cselect_b32 s81, 0, s81\n\t"
      "s_add_u32 m0, s39, 147456\n\t"
      "s_nop 0\n\t"
      "global_load_lds_dwordx4 v148, s[40:41]\n\t"
      "global_load_lds_dwordx4 v149, s[40:41] offset:1024\n\t"
      "SK2_%=:\n\t"
      "v_add_f32_dpp v97, v97, v97 row_mirror row_mask:0xf bank_mask:0xf\n\t"
      "v_fma_f32 v92, -v88, v88, v92\n\t"
      "s_nop 0\n\t"
      "v_permlane16_swap_b32_e32 v96, v97\n\t"
      "v_max_f32_e32 v92, 0, v92\n\t"
      "v_add_f32_e32 v96, v96, v97\n\t"
      "v_add_f32_e32 v92, 0x3727c5ac, v92\n\t"
      "v_mov_b32_e32 v97, v96\n\t"
      "v_rsq_f32_e32 v92, v92\n\t"
      "s_nop 0\n\t"
      "v_permlane32_swap_b32_e32 v96, v97\n\t"
      "v_add_f32_e32 v96, v96, v97\n\t"
      "v_mul_f32_e32 v88, v88, v92\n\t"
      "v_mov_b32_e32 v97, v96\n\t"
      "v_pk_fma_f32 v[32:33], v[32:33], v[92:93], v[88:89] op_sel_hi:[1,0,0]\n\t"
      "v_pk_fma_f32 v[34:35], v[34:35], v[92:93], v[88:89] op_sel_hi:[1,0,0]\n\t"
      "v_permlane16_swap_b32_e32 v96, v97\n\t"
      "v_pk_fma_f32 v[36:37], v[36:37], v[92:93], v[88:89] op_sel_hi:[1,0,0]\n\t"
      "v_pk_fma_f32 v[38:39], v[38:39], v[92:93], v[88:89] op_sel_hi:[1,0,0]\n\t"
      "v_mul_f32_e32 v96, 0xbb124925, v96\n\t"
      "v_cvt_pk_bf16_f32 v0, v32, v33\n\t"
      "v_cvt_pk_bf16_f32 v1, v34, v35\n\t"
      "v_cvt_pk_bf16_f32 v2, v36, v37\n\t"
      "v_cvt_pk_bf16_f32 v3, v38, v39\n\t"
      "ds_write_b128 v112, v[0:3]\n\t"
      "v_pk_fma_f32 v[40:41], v[40:41], v[92:93], v[88:89] op_sel_hi:[1,0,0]\n\t"
      "v_pk_fma_f32 v[42:43], v[42:43], v[92:93], v[88:89] op_sel_hi:[1,0,0]\n\t"
      "v_mul_f32_e32 v97, 0x3b124925, v97\n\t"
      "v_pk_fma_f32 v[44:45], v[44:45], v[92:93], v[88:89] op_sel_hi:[1,0,0]\n\t"
      "v_pk_fma_f32 v[46:47], v[46:47], v[92:93], v[88:89] op_sel_hi:[1,0,0]\n\t"
      "v_fma_f32 v97, -v96, v96, v97\n\t"
      "v_cvt_pk_bf16_f32 v4, v40, v41\n\t"
      "s_bitcmp0_b32 s27, 2\n\t"
      "s_cbranch_scc1 SK3_%=\n\t"
      "global_load_lds_dwordx4 v150, s[40:41] offset:2048\n\t"
      "global_load_lds_dwordx4 v151, s[40:41] offset:3072\n\t"
      "SK3_%=:\n\t"
      "v_cvt_pk_bf16_f32 v5, v42, v43\n\t"
      "v_cvt_pk_bf16_f32 v6, v44, v45\n\t"
      "v_cvt_pk_bf16_f32 v7, v46, v47\n\t"
      "ds_write_b128 v113, v[4:7]\n\t"
      "v_pk_fma_f32 v[48:49], v[48:49], v[92:93], v[88:89] op_sel_hi:[1,0,0]\n\t"
      "v_pk_fma_f32 v[50:51], v[50:51], v[92:93], v[88:89] op_sel_hi:[1,0,0]\n\t"
      "v_max_f32_e32 v97, 0, v97\n\t"
      "v_pk_fma_f32 v[52:53], v[52:53], v[92:93], v[88:89] op_sel_hi:[1,0,0]\n\t"
      "v_pk_fma_f32 v[54:55], v[54:55], v[92:93], v[88:89] op_sel_hi:[1,0,0]\n\t"
      "v_add_f32_e32 v97, 0x3727c5ac, v97\n\t"
      "v_cvt_pk_bf16_f32 v8, v48, v49\n\t"
      "v_cvt_pk_bf16_f32 v9, v50, v51\n\t"
      "v_cvt_pk_bf16_f32 v10, v52, v53\n\t"
      "v_cvt_pk_bf16_f32 v11, v54, v55\n\t"
      "ds_write_b128 v114, v[8:11]\n\t"
      "v_pk_fma_f32 v[56:57], v[56:57], v[92:93], v[88:89] op_sel_hi:[1,0,0]\n\t"
      "v_pk_fma_f32 v[58:59], v[58:59], v[92:93], v[88:89] op_sel_hi:[1,0,0]\n\t"
      "v_rsq_f32_e32 v97, v97\n\t"
      "v_pk_fma_f32 v[60:61], v[60:61], v[92:93], v[88:89] op_sel_hi:[1,0,0]\n\t"
      "v_pk_fma_f32 v[62:63], v[62:63], v[92:93], v[88:89] op_sel_hi:[1,0,0]\n\t"
      "v_cvt_pk_bf16_f32 v12, v56, v57\n\t"
      "v_cvt_pk_bf16_f32 v13, v58, v59\n\t"
      "v_cvt_pk_bf16_f32 v14, v60, v61\n\t"
      "v_cvt_pk_bf16_f32 v15, v62, v63\n\t"
      "ds_write_b128 v115, v[12:15]\n\t"
      "v_pk_fma_f32 v[64:65], v[64:65], v[92:93], v[88:89] op_sel_hi:[1,0,0]\n\t"
      "v_pk_fma_f32 v[66:67], v[66:67], v[92:93], v[88:89] op_sel_hi:[1,0,0]\n\t"
      "v_mul_f32_e32 v96, v96, v97\n\t"
      "v_pk_fma_f32 v[68:69], v[68:69], v[92:93], v[88:89] op_sel_hi:[1,0,0]\n\t"
      "v_pk_fma_f32 v[70:71], v[70:71], v[92:93], v[88:89] op_sel_hi:[1,0,0]\n\t"
      "s_bitcmp0_b32 s27, 2\n\t"
      "s_cbranch_scc1 SK4_%=\n\t"
      "s_mul_i32 s72, s82, 172032\n\t"
      "s_add_u32 s72, s72, s37\n\t"
      "s_add_u32 s72, s72, 1792\n\t"
      "s_add_u32 s40, s6, s72\n\t"
      "s_addc_u32 s41, s7, 0\n\t"
      "s_add_u32 s82, s82, 1\n\t"
      "s_cmp_eq_u32 s82, 7\n\t"
      "s_cselect_b32 s82, 0, s82\n\t"
      "s_add_u32 m0, s39, 65536\n\t"
      "s_nop 0\n\t"
      "global_load_lds_dwordx4 v148, s[40:41]\n\t"
      "global_load_lds_dwordx4 v152, s[40:41] offset:1024\n\t"
      "SK4_%=:\n\t"
      "v_cvt_pk_bf16_f32 v16, v64, v65\n\t"
      "v_cvt_pk_bf16_f32 v17, v66, v67\n\t"
      "v_cvt_pk_bf16_f32 v18, v68, v69\n\t"
      "v_cvt_pk_bf16_f32 v19, v70, v71\n\t"
      "ds_write_b128 v116, v[16:19]\n\t"
      "v_pk_fma_f32 v[72:73], v[72:73], v[92:93], v[88:89] op_sel_hi:[1,0,0]\n\t"
      "v_pk_fma_f32 v[74:75], v[74:75], v[92:93], v[88:89] op_sel_hi:[1,0,0]\n\t"
      "v_pk_fma_f32 v[76:77], v[76:77], v[92:93], v[88:89] op_sel_hi:[1,0,0]\n\t"
      "v_pk_fma_f32 v[78:79], v[78:79], v[92:93], v[88:89] op_sel_hi:[1,0,0]\n\t"
      "v_cvt_pk_bf16_f32 v20, v72, v73\n\t"
      "v_cvt_pk_bf16_f32 v21, v74, v75\n\t"
      "v_cvt_pk_bf16_f32 v22, v76, v77\n\t"
      "v_cvt_pk_bf16_f32 v23, v78, v79\n\t"
      "ds_write_b128 v117, v[20:23]\n\t"
      "v_pk_fma_f32 v[80:81], v[80:81], v[92:93], v[88:89] op_sel_hi:[1,0,0]\n\t"
      "v_pk_fma_f32 v[82:83], v[82:83], v[92:93], v[88:89] op_sel_hi:[1,0,0]\n\t"
      "v_pk_fma_f32 v[84:85], v[84:85], v[92:93], v[88:89] op_sel_hi:[1,0,0]\n\t"
      "v_pk_fma_f32 v[86:87], v[86:87], v[92:93], v[88:89] op_sel_hi:[1,0,0]\n\t"
      "v_cvt_pk_bf16_f32 v24, v80, v81\n\t"
      "v_cvt_pk_bf16_f32 v25, v82, v83\n\t"
      "v_cvt_pk_bf16_f32 v26, v84, v85\n\t"
      "v_cvt_pk_bf16_f32 v27, v86, v87\n\t"
      "ds_write_b128 v118, v[24:27]\n\t"
      "v_lshlrev_b32_e32 v32, 16, v28\n\t"
      "v_and_b32_e32 v33, 0xffff0000, v28\n\t"
      "v_lshlrev_b32_e32 v34, 16, v29\n\t"
      "v_and_b32_e32 v35, 0xffff0000, v29\n\t"
      "v_lshlrev_b32_e32 v36, 16, v30\n\t"
      "v_and_b32_e32 v37, 0xffff0000, v30\n\t"
      "v_lshlrev_b32_e32 v38, 16, v31\n\t"
      "s_bitcmp0_b32 s27, 2\n\t"
      "s_cbranch_scc1 SK5_%=\n\t"
      "s_mul_i32 s72, s82, 172032\n\t"
      "s_add_u32 s72, s72, s37\n\t"
      "s_add_u32 s72, s72, 1792\n\t"
      "s_add_u32 s40, s6, s72\n\t"
      "s_addc_u32 s41, s7, 0\n\t"
      "s_add_u32 s82, s82, 1\n\t"
      "s_cmp_eq_u32 s82, 7\n\t"
      "s_cselect_b32 s82, 0, s82\n\t"
      "s_add_u32 m0, s39, 67584\n\t"
      "s_nop 0\n\t"
      "global_load_lds_dwordx4 v148, s[40:41]\n\t"
      "global_load_lds_dwordx4 v152, s[40:41] offset:1024\n\t"
      "SK5_%=:\n\t"
      "v_and_b32_e32 v39, 0xffff0000, v31\n\t"
      "v_pk_fma_f32 v[32:33], v[32:33], v[96:97], v[96:97] op_sel:[0,1,0] op_sel_hi:[1,1,0]\n\t"
      "v_pk_fma_f32 v[34:35], v[34:35], v[96:97], v[96:97] op_sel:[0,1,0] op_sel_hi:[1,1,0]\n\t"
      "v_pk_fma_f32 v[36:37], v[36:37], v[96:97], v[96:97] op_sel:[0,1,0] op_sel_hi:[1,1,0]\n\t"
      "v_pk_fma_f32 v[38:39], v[38:39], v[96:97], v[96:97] op_sel:[0,1,0] op_sel_hi:[1,1,0]\n\t"
      "v_cvt_pk_bf16_f32 v28, v32, v33\n\t"
      "v_cvt_pk_bf16_f32 v29, v34, v35\n\t"
      "v_cvt_pk_bf16_f32 v30, v36, v37\n\t"
      "v_cvt_pk_bf16_f32 v31, v38, v39\n\t"
      "ds_write_b128 v119, v[28:31]\n\t"
      "s_waitcnt lgkmcnt(0)\n\t"
      "s_bitcmp1_b32 s27, 2\n\t"
      "s_cbranch_scc1 LPATH_%=\n\t"
      "s_setprio 2\n\t"
      "s_barrier\n\t"
      "ds_read_b64_tr_b16 v[76:77], v168\n\t"
      "ds_read_b64_tr_b16 v[78:79], v168 offset:512\n\t"
      "ds_read_b128 v[68:71], v160\n\t"
      "ds_read_b128 v[72:75], v160 offset:14336\n\t"
      "ds_read_b64_tr_b16 v[80:81], v169\n\t"
      "ds_read_b64_tr_b16 v[82:83], v169 offset:512\n\t"
      "ds_read_b64_tr_b16 v[84:85], v168 offset:1024\n\t"
      "ds_read_b64_tr_b16 v[86:87], v168 offset:1536\n\t"
      "ds_read_b128 v[64:67], v162\n\t"
      "ds_read_b64_tr_b16 v[88:89], v169 offset:1024\n\t"
      "ds_read_b64_tr_b16 v[90:91], v169 offset:1536\n\t"
      "ds_read_b64_tr_b16 v[104:105], v168 offset:2048\n\t"
      "ds_read_b64_tr_b16 v[106:107], v168 offset:2560\n\t"
      "ds_read_b128 v[96:99], v161 offset:1024\n\t"
      "ds_read_b128 v[100:103], v161 offset:15360\n\t"
      "ds_read_b64_tr_b16 v[108:109], v169 offset:2048\n\t"
      "ds_read_b64_tr_b16 v[110:111], v169 offset:2560\n\t"
      "ds_read_b64_tr_b16 v[112:113], v168 offset:3072\n\t"
      "ds_read_b64_tr_b16 v[114:115], v168 offset:3584\n\t"
      "ds_read_b128 v[92:95], v163 offset:1024\n\t"
      "ds_read_b64_tr_b16 v[116:117], v169 offset:3072\n\t"
      "ds_read_b64_tr_b16 v[118:119], v169 offset:3584\n\t"
      "s_waitcnt vmcnt(0)\n\t"
      "s_barrier\n\t"
      "s_waitcnt lgkmcnt(14)\n\t"
      "v_mfma_f32_16x16x32_bf16 v[0:3], v[76:79], v[68:71], 0\n\t"
      "ds_read_b64_tr_b16 v[132:133], v168 offset:16384\n\t"
      "ds_read_b64_tr_b16 v[134:135], v168 offset:16896\n\t"
      "v_mfma_f32_16x16x32_bf16 v[8:11], v[76:79], v[72:75], 0\n\t"
      "ds_read_b128 v[124:127], v160 offset:2048\n\t"
      "v_mfma_f32_16x16x32_bf16 v[4:7], v[80:83], v[68:71], 0\n\t"
      "ds_read_b128 v[128:131], v160 offset:16384\n\t"
      "v_mfma_f32_16x16x32_bf16 v[12:15], v[80:83], v[72:75], 0\n\t"
      "ds_read_b64_tr_b16 v[136:137], v169 offset:16384\n\t"
      "v_mfma_f32_16x16x32_bf16 v[24:27], v[84:87], v[68:71], 0\n\t"
      "ds_read_b64_tr_b16 v[138:139], v169 offset:16896\n\t"
      "v_mfma_f32_16x16x32_bf16 v[32:35], v[84:87], v[72:75], 0\n\t"
      "ds_read_b64_tr_b16 v[140:141], v168 offset:17408\n\t"
      "s_waitcnt lgkmcnt(14)\n\t"
      "v_mfma_f32_16x16x32_bf16 v[16:19], v[84:87], v[64:67], 0\n\t"
      "ds_read_b64_tr_b16 v[142:143], v168 offset:17920\n\t"
      "v_mfma_f32_16x16x32_bf16 v[28:31], v[88:91], v[68:71], 0\n\t"
      "ds_read_b128 v[120:123], v162 offset:2048\n\t"
      "v_mfma_f32_16x16x32_bf16 v[36:39], v[88:91], v[72:75], 0\n\t"
      "ds_read_b64_tr_b16 v[144:145], v169 offset:17408\n\t"
      "v_mfma_f32_16x16x32_bf16 v[20:23], v[88:91], v[64:67], 0\n\t"
      "ds_read_b64_tr_b16 v[146:147], v169 offset:17920\n\t"
      "s_waitcnt lgkmcnt(11)\n\t"
      "v_mfma_f32_16x16x32_bf16 v[0:3], v[104:107], v[96:99], v[0:3]\n\t"
      "ds_read_b64_tr_b16 v[76:77], v168 offset:18432\n\t"
      "ds_read_b64_tr_b16 v[78:79], v168 offset:18944\n\t"
      "v_mfma_f32_16x16x32_bf16 v[8:11], v[104:107], v[100:103], v[8:11]\n\t"
      "ds_read_b128 v[68:71], v161 offset:3072\n\t"
      "v_mfma_f32_16x16x32_bf16 v[4:7], v[108:111], v[96:99], v[4:7]\n\t"
      "ds_read_b128 v[72:75], v161 offset:17408\n\t"
      "v_mfma_f32_16x16x32_bf16 v[12:15], v[108:111], v[100:103], v[12:15]\n\t"
      "ds_read_b64_tr_b16 v[80:81], v169 offset:18432\n\t"
      "v_mfma_f32_16x16x32_bf16 v[24:27], v[112:115], v[96:99], v[24:27]\n\t"
      "ds_read_b64_tr_b16 v[82:83], v169 offset:18944\n\t"
      "v_mfma_f32_16x16x32_bf16 v[32:35], v[112:115], v[100:103], v[32:35]\n\t"
      "ds_read_b64_tr_b16 v[84:85], v168 offset:19456\n\t"
      "v_mfma_f32_16x16x32_bf16 v[16:19], v[112:115], v[92:95], v[16:19]\n\t"
      "ds_read_b64_tr_b16 v[86:87], v168 offset:19968\n\t"
      "v_mfma_f32_16x16x32_bf16 v[28:31], v[116:119], v[96:99], v[28:31]\n\t"
      "ds_read_b128 v[64:67], v163 offset:3072\n\t"
      "v_mfma_f32_16x16x32_bf16 v[36:39], v[116:119], v[100:103], v[36:39]\n\t"
      "ds_read_b64_tr_b16 v[88:89], v169 offset:19456\n\t"
      "v_mfma_f32_16x16x32_bf16 v[20:23], v[116:119], v[92:95], v[20:23]\n\t"
      "ds_read_b64_tr_b16 v[90:91], v169 offset:19968\n\t"
      "s_barrier\n\t"
      "s_waitcnt lgkmcnt(11)\n\t"
      "v_mfma_f32_16x16x32_bf16 v[0:3], v[132:135], v[124:127], v[0:3]\n\t"
      "ds_read_b64_tr_b16 v[104:105], v166\n\t"
      "v_lshlrev_b32_e32 v199, 2, v193\n\t"
      "v_mfma_f32_16x16x32_bf16 v[8:11], v[132:135], v[128:131], v[8:11]\n\t"
      "ds_read_b64_tr_b16 v[106:107], v166 offset:512\n\t"
      "v_sub_u32_e32 v199, v194, v199\n\t"
      "v_mfma_f32_16x16x32_bf16 v[4:7], v[136:139], v[124:127], v[4:7]\n\t"
      "ds_read_b128 v[96:99], v160\n\t"
      "v_mov_b32_e32 v200, 0xff800000\n\t"
      "v_mfma_f32_16x16x32_bf16 v[12:15], v[136:139], v[128:131], v[12:15]\n\t"
      "ds_read_b128 v[100:103], v160 offset:14336\n\t"
      "s_cmp_lt_u32 s25, 2\n\t"
      "v_mfma_f32_16x16x32_bf16 v[24:27], v[140:143], v[124:127], v[24:27]\n\t"
      "ds_read_b128 v[92:95], v162\n\t"
      "s_cselect_b32 s35, 0xff800000, 0\n\t"
      "v_mfma_f32_16x16x32_bf16 v[32:35], v[140:143], v[128:131], v[32:35]\n\t"
      "ds_read_b64_tr_b16 v[108:109], v167\n\t"
      "s_bitcmp0_b32 s24, 0\n\t"
      "v_mfma_f32_16x16x32_bf16 v[16:19], v[140:143], v[120:123], v[16:19]\n\t"
      "ds_read_b64_tr_b16 v[110:111], v167 offset:512\n\t"
      "s_cselect_b32 s35, s35, 0\n\t"
      "v_mfma_f32_16x16x32_bf16 v[28:31], v[144:147], v[124:127], v[28:31]\n\t"
      "v_cmp_ge_i32_e32 vcc, -9, v199\n\t"
      "v_mfma_f32_16x16x32_bf16 v[36:39], v[144:147], v[128:131], v[36:39]\n\t"
      "v_subrev_u32_e32 v201, 0, v199\n\t"
      "v_mfma_f32_16x16x32_bf16 v[20:23], v[144:147], v[120:123], v[20:23]\n\t"
      "v_cmp_gt_u32_e64 s[64:65], 8, v201\n\t"
      "s_waitcnt lgkmcnt(7)\n\t"
      "v_mfma_f32_16x16x32_bf16 v[0:3], v[76:79], v[68:71], v[0:3]\n\t"
      "ds_read_b64_tr_b16 v[132:133], v166 offset:1024\n\t"
      "v_cndmask_b32_e64 v172, v200, 0, vcc\n\t"
      "v_mfma_f32_16x16x32_bf16 v[8:11], v[76:79], v[72:75], v[8:11]\n\t"
      "ds_read_b64_tr_b16 v[134:135], v166 offset:1536\n\t"
      "v_cndmask_b32_e64 v176, v200, 0, s[64:65]\n\t"
      "v_mfma_f32_16x16x32_bf16 v[4:7], v[80:83], v[68:71], v[4:7]\n\t"
      "ds_read_b128 v[124:127], v161 offset:1024\n\t"
      "v_or_b32_e32 v180, s35, v172\n\t"
      "v_mfma_f32_16x16x32_bf16 v[12:15], v[80:83], v[72:75], v[12:15]\n\t"
      "ds_read_b128 v[128:131], v161 offset:15360\n\t"
      "v_cmp_ge_i32_e32 vcc, -8, v199\n\t"
      "v_mfma_f32_16x16x32_bf16 v[24:27], v[84:87], v[68:71], v[24:27]\n\t"
      "ds_read_b128 v[120:123], v163 offset:1024\n\t"
      "v_subrev_u32_e32 v201, 1, v199\n\t"
      "v_mfma_f32_16x16x32_bf16 v[32:35], v[84:87], v[72:75], v[32:35]\n\t"
      "ds_read_b64_tr_b16 v[136:137], v167 offset:1024\n\t"
      "v_cmp_gt_u32_e64 s[64:65], 8, v201\n\t"
      "v_mfma_f32_16x16x32_bf16 v[16:19], v[84:87], v[64:67], v[16:19]\n\t"
      "ds_read_b64_tr_b16 v[138:139], v167 offset:1536\n\t"
      "v_cndmask_b32_e64 v173, v200, 0, vcc\n\t"
      "v_mfma_f32_16x16x32_bf16 v[28:31], v[88:91], v[68:71], v[28:31]\n\t"
      "v_cndmask_b32_e64 v177, v200, 0, s[64:65]\n\t"
      "v_mfma_f32_16x16x32_bf16 v[36:39], v[88:91], v[72:75], v[36:39]\n\t"
      "v_or_b32_e32 v181, s35, v173\n\t"
      "v_mfma_f32_16x16x32_bf16 v[20:23], v[88:91], v[64:67], v[20:23]\n\t"
      "v_cmp_ge_i32_e32 vcc, -7, v199\n\t"
      "s_waitcnt lgkmcnt(7)\n\t"
      "v_mfma_f32_16x16x32_bf16 v[48:51], v[96:99], v[104:107], 0\n\t"
      "ds_read_b64_tr_b16 v[76:77], v166 offset:2048\n\t"
      "ds_read_b64_tr_b16 v[78:79], v166 offset:2560\n\t"
      "v_subrev_u32_e32 v201, 2, v199\n\t"
      "v_mfma_f32_16x16x32_bf16 v[56:59], v[100:103], v[104:107], 0\n\t"
      "ds_read_b128 v[68:71], v160 offset:2048\n\t"
      "v_cmp_gt_u32_e64 s[64:65], 8, v201\n\t"
      "v_mfma_f32_16x16x32_bf16 v[40:43], v[92:95], v[104:107], 0\n\t"
      "ds_read_b128 v[72:75], v160 offset:16384\n\t"
      "v_cndmask_b32_e64 v174, v200, 0, vcc\n\t"
      "v_mfma_f32_16x16x32_bf16 v[52:55], v[96:99], v[108:111], 0\n\t"
      "ds_read_b128 v[64:67], v162 offset:2048\n\t"
      "v_cndmask_b32_e64 v178, v200, 0, s[64:65]\n\t"
      "v_mfma_f32_16x16x32_bf16 v[60:63], v[100:103], v[108:111], 0\n\t"
      "ds_read_b64_tr_b16 v[80:81], v167 offset:2048\n\t"
      "v_or_b32_e32 v182, s35, v174\n\t"
      "v_mfma_f32_16x16x32_bf16 v[44:47], v[92:95], v[108:111], 0\n\t"
      "ds_read_b64_tr_b16 v[82:83], v167 offset:2560\n\t"
      "v_cmp_ge_i32_e32 vcc, -6, v199\n\t"
      "s_waitcnt lgkmcnt(7)\n\t"
      "v_mfma_f32_16x16x32_bf16 v[48:51], v[124:127], v[132:135], v[48:51]\n\t"
      "ds_read_b64_tr_b16 v[104:105], v166 offset:3072\n\t"
      "ds_read_b64_tr_b16 v[106:107], v166 offset:3584\n\t"
      "v_subrev_u32_e32 v201, 3, v199\n\t"
      "v_mfma_f32_16x16x32_bf16 v[56:59], v[128:131], v[132:135], v[56:59]\n\t"
      "ds_read_b128 v[96:99], v161 offset:3072\n\t"
      "v_cmp_gt_u32_e64 s[64:65], 8, v201\n\t"
      "v_mfma_f32_16x16x32_bf16 v[40:43], v[120:123], v[132:135], v[40:43]\n\t"
      "ds_read_b128 v[100:103], v161 offset:17408\n\t"
      "v_cndmask_b32_e64 v175, v200, 0, vcc\n\t"
      "v_mfma_f32_16x16x32_bf16 v[52:55], v[124:127], v[136:139], v[52:55]\n\t"
      "ds_read_b128 v[92:95], v163 offset:3072\n\t"
      "v_cndmask_b32_e64 v179, v200, 0, s[64:65]\n\t"
      "v_mfma_f32_16x16x32_bf16 v[60:63], v[128:131], v[136:139], v[60:63]\n\t"
      "ds_read_b64_tr_b16 v[108:109], v167 offset:3072\n\t"
      "v_or_b32_e32 v183, s35, v175\n\t"
      "v_mfma_f32_16x16x32_bf16 v[44:47], v[120:123], v[136:139], v[44:47]\n\t"
      "ds_read_b64_tr_b16 v[110:111], v167 offset:3584\n\t"
      "v_lshrrev_b32_e32 v201, 31, v172\n\t"
      "s_barrier\n\t"
      "s_waitcnt lgkmcnt(7)\n\t"
      "v_mfma_f32_16x16x32_bf16 v[48:51], v[68:71], v[76:79], v[48:51]\n\t"
      "ds_read_b64_tr_b16 v[132:133], v166 offset:16384\n\t"
      "ds_read_b64_tr_b16 v[134:135], v166 offset:16896\n\t"
      "ds_read_b128 v[124:127], v160 offset:4096\n\t"
      "ds_read_b128 v[128:131], v160 offset:18432\n\t"
      "ds_read_b64_tr_b16 v[136:137], v167 offset:16384\n\t"
      "ds_read_b64_tr_b16 v[138:139], v167 offset:16896\n\t"
      "v_lshrrev_b32_e32 v202, 31, v173\n\t"
      "v_mfma_f32_16x16x32_bf16 v[56:59], v[72:75], v[76:79], v[56:59]\n\t"
      "ds_read_b64_tr_b16 v[140:141], v166 offset:17408\n\t"
      "v_lshl_or_b32 v201, v202, 16, v201\n\t"
      "v_mfma_f32_16x16x32_bf16 v[40:43], v[64:67], v[76:79], v[40:43]\n\t"
      "ds_read_b64_tr_b16 v[142:143], v166 offset:17920\n\t"
      "v_mul_u32_u24_e32 v184, 0xffff, v201\n\t"
      "v_mfma_f32_16x16x32_bf16 v[52:55], v[68:71], v[80:83], v[52:55]\n\t"
      "ds_read_b128 v[120:123], v162 offset:4096\n\t"
      "v_lshrrev_b32_e32 v201, 31, v174\n\t"
      "v_mfma_f32_16x16x32_bf16 v[60:63], v[72:75], v[80:83], v[60:63]\n\t"
      "ds_read_b64_tr_b16 v[144:145], v167 offset:17408\n\t"
      "v_lshrrev_b32_e32 v202, 31, v175\n\t"
      "v_mfma_f32_16x16x32_bf16 v[44:47], v[64:67], v[80:83], v[44:47]\n\t"
      "ds_read_b64_tr_b16 v[146:147], v167 offset:17920\n\t"
      "v_lshl_or_b32 v201, v202, 16, v201\n\t"
      "s_waitcnt lgkmcnt(11)\n\t"
      "v_mfma_f32_16x16x32_bf16 v[48:51], v[96:99], v[104:107], v[48:51]\n\t"
      "ds_read_b64_tr_b16 v[76:77], v166 offset:18432\n\t"
      "ds_read_b64_tr_b16 v[78:79], v166 offset:18944\n\t"
      "ds_read_b128 v[68:71], v161 offset:5120\n\t"
      "ds_read_b128 v[72:75], v161 offset:19456\n\t"
      "ds_read_b64_tr_b16 v[80:81], v167 offset:18432\n\t"
      "ds_read_b64_tr_b16 v[82:83], v167 offset:18944\n\t"
      "v_mul_u32_u24_e32 v185, 0xffff, v201\n\t"
      "v_mfma_f32_16x16x32_bf16 v[56:59], v[100:103], v[104:107], v[56:59]\n\t"
      "ds_read_b64_tr_b16 v[84:85], v166 offset:19456\n\t"
      "s_mov_b32 s68, 0x0\n\t"
      "v_mfma_f32_16x16x32_bf16 v[40:43], v[92:95], v[104:107], v[40:43]\n\t"
      "ds_read_b64_tr_b16 v[86:87], v166 offset:19968\n\t"
      "s_mov_b32 s69, 0xffff0000\n\t"
      "v_mfma_f32_16x16x32_bf16 v[52:55], v[96:99], v[108:111], v[52:55]\n\t"
      "ds_read_b128 v[64:67], v163 offset:5120\n\t"
      "s_mov_b32 s70, 0xffff\n\t"
      "v_mfma_f32_16x16x32_bf16 v[60:63], v[100:103], v[108:111], v[60:63]\n\t"
      "ds_read_b64_tr_b16 v[88:89], v167 offset:19456\n\t"
      "s_mov_b32 s71, 0x0\n\t"
      "v_mfma_f32_16x16x32_bf16 v[44:47], v[92:95], v[108:111], v[44:47]\n\t"
      "ds_read_b64_tr_b16 v[90:91], v167 offset:19968\n\t"
      "v_lshlrev_b32_e32 v231, 3, v192\n\t"
      "s_barrier\n\t"
      "s_waitcnt lgkmcnt(11)\n\t"
      "v_mfma_f32_16x16x32_bf16 v[0:3], v[132:135], v[124:127], v[0:3]\n\t"
      "ds_read_b64_tr_b16 v[104:105], v166 offset:32768\n\t"
      "ds_read_b64_tr_b16 v[106:107], v166 offset:33280\n\t"
      "s_lshl_b32 s34, s25, 12\n\t"
      "v_mfma_f32_16x16x32_bf16 v[8:11], v[132:135], v[128:131], v[8:11]\n\t"
      "ds_read_b128 v[96:99], v160 offset:6144\n\t"
      "s_lshr_b32 s35, s39, 2\n\t"
      "v_mfma_f32_16x16x32_bf16 v[4:7], v[136:139], v[124:127], v[4:7]\n\t"
      "ds_read_b128 v[100:103], v160 offset:20480\n\t"
      "s_add_u32 s34, s34, s35\n\t"
      "v_mfma_f32_16x16x32_bf16 v[12:15], v[136:139], v[128:131], v[12:15]\n\t"
      "ds_read_b64_tr_b16 v[108:109], v167 offset:32768\n\t"
      "s_add_u32 s34, s34, 12582912\n\t"
      "v_mfma_f32_16x16x32_bf16 v[24:27], v[140:143], v[124:127], v[24:27]\n\t"
      "ds_read_b64_tr_b16 v[110:111], v167 offset:33280\n\t"
      "s_add_u32 s46, s20, s34\n\t"
      "v_mfma_f32_16x16x32_bf16 v[32:35], v[140:143], v[128:131], v[32:35]\n\t"
      "ds_read_b64_tr_b16 v[112:113], v166 offset:33792\n\t"
      "s_addc_u32 s47, s21, 0\n\t"
      "v_mfma_f32_16x16x32_bf16 v[16:19], v[140:143], v[120:123], v[16:19]\n\t"
      "ds_read_b64_tr_b16 v[114:115], v166 offset:34304\n\t"
      "s_lshl_b32 s34, s28, 7\n\t"
      "v_mfma_f32_16x16x32_bf16 v[28:31], v[144:147], v[124:127], v[28:31]\n\t"
      "ds_read_b128 v[92:95], v162 offset:6144\n\t"
      "s_add_u32 s58, s46, s34\n\t"
      "v_mfma_f32_16x16x32_bf16 v[36:39], v[144:147], v[128:131], v[36:39]\n\t"
      "ds_read_b64_tr_b16 v[116:117], v167 offset:33792\n\t"
      "s_addc_u32 s59, s47, 0\n\t"
      "v_mfma_f32_16x16x32_bf16 v[20:23], v[144:147], v[120:123], v[20:23]\n\t"
      "ds_read_b64_tr_b16 v[118:119], v167 offset:34304\n\t"
      "s_and_b32 s34, s24, 1\n\t"
      "s_waitcnt lgkmcnt(11)\n\t"
      "v_mfma_f32_16x16x32_bf16 v[0:3], v[76:79], v[68:71], v[0:3]\n\t"
      "ds_read_b64_tr_b16 v[132:133], v166 offset:34816\n\t"
      "ds_read_b64_tr_b16 v[134:135], v166 offset:35328\n\t"
      "s_lshl1_add_u32 s34, s25, s34\n\t"
      "v_mfma_f32_16x16x32_bf16 v[8:11], v[76:79], v[72:75], v[8:11]\n\t"
      "ds_read_b128 v[124:127], v161 offset:7168\n\t"
      "s_lshl_b32 s34, s34, 5\n\t"
      "v_mfma_f32_16x16x32_bf16 v[4:7], v[80:83], v[68:71], v[4:7]\n\t"
      "ds_read_b128 v[128:131], v161 offset:21504\n\t"
      "s_lshl1_add_u32 s34, s26, s34\n\t"
      "v_mfma_f32_16x16x32_bf16 v[12:15], v[80:83], v[72:75], v[12:15]\n\t"
      "ds_read_b64_tr_b16 v[136:137], v167 offset:34816\n\t"
      "s_bfe_u32 s35, s24, 65537\n\t"
      "v_mfma_f32_16x16x32_bf16 v[24:27], v[84:87], v[68:71], v[24:27]\n\t"
      "ds_read_b64_tr_b16 v[138:139], v167 offset:35328\n\t"
      "s_add_u32 s34, s34, s35\n\t"
      "v_mfma_f32_16x16x32_bf16 v[32:35], v[84:87], v[72:75], v[32:35]\n\t"
      "ds_read_b64_tr_b16 v[140:141], v166 offset:35840\n\t"
      "s_lshl_b32 s34, s34, 10\n\t"
      "v_mfma_f32_16x16x32_bf16 v[16:19], v[84:87], v[64:67], v[16:19]\n\t"
      "ds_read_b64_tr_b16 v[142:143], v166 offset:36352\n\t"
      "s_add_u32 s42, s20, s34\n\t"
      "v_mfma_f32_16x16x32_bf16 v[28:31], v[88:91], v[68:71], v[28:31]\n\t"
      "ds_read_b128 v[120:123], v165 offset:7168\n\t"
      "s_addc_u32 s43, s21, 0\n\t"
      "v_mfma_f32_16x16x32_bf16 v[36:39], v[88:91], v[72:75], v[36:39]\n\t"
      "ds_read_b64_tr_b16 v[144:145], v167 offset:35840\n\t"
      "s_add_u32 s44, s42, 16384\n\t"
      "v_mfma_f32_16x16x32_bf16 v[20:23], v[88:91], v[64:67], v[20:23]\n\t"
      "ds_read_b64_tr_b16 v[146:147], v167 offset:36352\n\t"
      "s_addc_u32 s45, s43, 0\n\t"
      "s_barrier\n\t"
      "s_waitcnt lgkmcnt(11)\n\t"
      "v_mfma_f32_16x16x32_bf16 v[0:3], v[104:107], v[96:99], v[0:3]\n\t"
      "ds_read_b64_tr_b16 v[76:77], v166 offset:49152\n\t"
      "ds_read_b64_tr_b16 v[78:79], v166 offset:49664\n\t"
      "v_and_b32_e32 v199, 60, v192\n\t"
      "v_mfma_f32_16x16x32_bf16 v[8:11], v[104:107], v[100:103], v[8:11]\n\t"
      "ds_read_b128 v[68:71], v160 offset:8192\n\t"
      "v_and_b32_e32 v200, 24, v192\n\t"
      "v_mfma_f32_16x16x32_bf16 v[4:7], v[108:111], v[96:99], v[4:7]\n\t"
      "ds_read_b128 v[72:75], v160 offset:22528\n\t"
      "v_lshlrev_b32_e32 v202, 3, v196\n\t"
      "v_mfma_f32_16x16x32_bf16 v[12:15], v[108:111], v[100:103], v[12:15]\n\t"
      "ds_read_b64_tr_b16 v[80:81], v167 offset:49152\n\t"
      "v_lshl_or_b32 v202, v200, 2, v202\n\t"
      "v_mfma_f32_16x16x32_bf16 v[24:27], v[112:115], v[96:99], v[24:27]\n\t"
      "ds_read_b64_tr_b16 v[82:83], v167 offset:49664\n\t"
      "v_lshl_or_b32 v201, v199, 5, v202\n\t"
      "v_mfma_f32_16x16x32_bf16 v[32:35], v[112:115], v[100:103], v[32:35]\n\t"
      "ds_read_b64_tr_b16 v[84:85], v166 offset:50176\n\t"
      "v_add_u32_e32 v216, 106496, v201\n\t"
      "v_mfma_f32_16x16x32_bf16 v[16:19], v[112:115], v[92:95], v[16:19]\n\t"
      "ds_read_b64_tr_b16 v[86:87], v166 offset:50688\n\t"
      "v_mov_b32_e32 v199, 2048\n\t"
      "v_mfma_f32_16x16x32_bf16 v[28:31], v[116:119], v[96:99], v[28:31]\n\t"
      "ds_read_b128 v[64:67], v164 offset:8192\n\t"
      "v_cndmask_b32_e64 v199, v199, 0, s[68:69]\n\t"
      "v_mfma_f32_16x16x32_bf16 v[36:39], v[116:119], v[100:103], v[36:39]\n\t"
      "ds_read_b64_tr_b16 v[88:89], v167 offset:50176\n\t"
      "v_add_u32_e32 v220, v216, v199\n\t"
      "v_mfma_f32_16x16x32_bf16 v[20:23], v[116:119], v[92:95], v[20:23]\n\t"
      "ds_read_b64_tr_b16 v[90:91], v167 offset:50688\n\t"
      "v_xor_b32_e32 v217, 32, v216\n\t"
      "s_waitcnt lgkmcnt(11)\n\t"
      "v_mfma_f32_16x16x32_bf16 v[0:3], v[132:135], v[124:127], v[0:3]\n\t"
      "ds_read_b64_tr_b16 v[104:105], v166 offset:51200\n\t"
      "ds_read_b64_tr_b16 v[106:107], v166 offset:51712\n\t"
      "v_xor_b32_e32 v218, 64, v216\n\t"
      "v_mfma_f32_16x16x32_bf16 v[8:11], v[132:135], v[128:131], v[8:11]\n\t"
      "ds_read_b128 v[96:99], v161 offset:9216\n\t"
      "v_xor_b32_e32 v219, 96, v216\n\t"
      "v_mfma_f32_16x16x32_bf16 v[4:7], v[136:139], v[124:127], v[4:7]\n\t"
      "ds_read_b128 v[100:103], v161 offset:23552\n\t"
      "v_xor_b32_e32 v221, 32, v220\n\t"
      "v_mfma_f32_16x16x32_bf16 v[12:15], v[136:139], v[128:131], v[12:15]\n\t"
      "ds_read_b64_tr_b16 v[108:109], v167 offset:51200\n\t"
      "v_xor_b32_e32 v222, 64, v220\n\t"
      "v_mfma_f32_16x16x32_bf16 v[24:27], v[140:143], v[124:127], v[24:27]\n\t"
      "ds_read_b64_tr_b16 v[110:111], v167 offset:51712\n\t"
      "v_xor_b32_e32 v223, 96, v220\n\t"
      "v_mfma_f32_16x16x32_bf16 v[32:35], v[140:143], v[128:131], v[32:35]\n\t"
      "ds_read_b64_tr_b16 v[112:113], v166 offset:52224\n\t"
      "s_lshr_b32 s34, s39, 1\n\t"
      "v_mfma_f32_16x16x32_bf16 v[16:19], v[140:143], v[120:123], v[16:19]\n\t"
      "ds_read_b64_tr_b16 v[114:115], v166 offset:52736\n\t"
      "s_add_u32 s34, s34, 98304\n\t"
      "v_mfma_f32_16x16x32_bf16 v[28:31], v[144:147], v[124:127], v[28:31]\n\t"
      "ds_read_b128 v[92:95], v165 offset:9216\n\t"
      "v_lshrrev_b32_e32 v199, 1, v193\n\t"
      "v_mfma_f32_16x16x32_bf16 v[36:39], v[144:147], v[128:131], v[36:39]\n\t"
      "ds_read_b64_tr_b16 v[116:117], v167 offset:52224\n\t"
      "v_xor_b32_e32 v199, v199, v198\n\t"
      "v_mfma_f32_16x16x32_bf16 v[20:23], v[144:147], v[120:123], v[20:23]\n\t"
      "ds_read_b64_tr_b16 v[118:119], v167 offset:52736\n\t"
      "v_lshlrev_b32_e32 v199, 4, v199\n\t"
      "s_barrier\n\t"
      "s_waitcnt lgkmcnt(11)\n\t"
      "v_mfma_f32_16x16x32_bf16 v[0:3], v[76:79], v[68:71], v[0:3]\n\t"
      "ds_read_b64_tr_b16 v[132:133], v168\n\t"
      "ds_read_b64_tr_b16 v[134:135], v168 offset:512\n\t"
      "v_lshl_or_b32 v199, v194, 7, v199\n\t"
      "v_mfma_f32_16x16x32_bf16 v[8:11], v[76:79], v[72:75], v[8:11]\n\t"
      "ds_read_b128 v[124:127], v160 offset:10240\n\t"
      "v_and_b32_e32 v200, 1, v193\n\t"
      "v_mfma_f32_16x16x32_bf16 v[4:7], v[80:83], v[68:71], v[4:7]\n\t"
      "ds_read_b128 v[128:131], v160 offset:24576\n\t"
      "v_lshl_or_b32 v199, v200, 3, v199\n\t"
      "v_mfma_f32_16x16x32_bf16 v[12:15], v[80:83], v[72:75], v[12:15]\n\t"
      "ds_read_b64_tr_b16 v[136:137], v169\n\t"
      "v_add_u32_e32 v228, s34, v199\n\t"
      "v_mfma_f32_16x16x32_bf16 v[24:27], v[84:87], v[68:71], v[24:27]\n\t"
      "ds_read_b64_tr_b16 v[138:139], v169 offset:512\n\t"
      "v_mfma_f32_16x16x32_bf16 v[32:35], v[84:87], v[72:75], v[32:35]\n\t"
      "ds_read_b64_tr_b16 v[140:141], v168 offset:1024\n\t"
      "v_mfma_f32_16x16x32_bf16 v[16:19], v[84:87], v[64:67], v[16:19]\n\t"
      "ds_read_b64_tr_b16 v[142:143], v168 offset:1536\n\t"
      "v_mfma_f32_16x16x32_bf16 v[28:31], v[88:91], v[68:71], v[28:31]\n\t"
      "ds_read_b128 v[120:123], v164 offset:10240\n\t"
      "v_mfma_f32_16x16x32_bf16 v[36:39], v[88:91], v[72:75], v[36:39]\n\t"
      "ds_read_b64_tr_b16 v[144:145], v169 offset:1024\n\t"
      "v_mfma_f32_16x16x32_bf16 v[20:23], v[88:91], v[64:67], v[20:23]\n\t"
      "ds_read_b64_tr_b16 v[146:147], v169 offset:1536\n\t"
      "s_waitcnt lgkmcnt(11)\n\t"
      "v_mfma_f32_16x16x32_bf16 v[0:3], v[104:107], v[96:99], v[0:3]\n\t"
      "ds_read_b64_tr_b16 v[76:77], v168 offset:2048\n\t"
      "ds_read_b64_tr_b16 v[78:79], v168 offset:2560\n\t"
      "v_mfma_f32_16x16x32_bf16 v[8:11], v[104:107], v[100:103], v[8:11]\n\t"
      "ds_read_b128 v[68:71], v161 offset:11264\n\t"
      "v_mfma_f32_16x16x32_bf16 v[4:7], v[108:111], v[96:99], v[4:7]\n\t"
      "ds_read_b128 v[72:75], v161 offset:25600\n\t"
      "v_mfma_f32_16x16x32_bf16 v[12:15], v[108:111], v[100:103], v[12:15]\n\t"
      "ds_read_b64_tr_b16 v[80:81], v169 offset:2048\n\t"
      "v_mfma_f32_16x16x32_bf16 v[24:27], v[112:115], v[96:99], v[24:27]\n\t"
      "ds_read_b64_tr_b16 v[82:83], v169 offset:2560\n\t"
      "v_mfma_f32_16x16x32_bf16 v[32:35], v[112:115], v[100:103], v[32:35]\n\t"
      "ds_read_b64_tr_b16 v[84:85], v168 offset:3072\n\t"
      "v_mfma_f32_16x16x32_bf16 v[16:19], v[112:115], v[92:95], v[16:19]\n\t"
      "ds_read_b64_tr_b16 v[86:87], v168 offset:3584\n\t"
      "v_mfma_f32_16x16x32_bf16 v[28:31], v[116:119], v[96:99], v[28:31]\n\t"
      "ds_read_b128 v[64:67], v165 offset:11264\n\t"
      "v_mfma_f32_16x16x32_bf16 v[36:39], v[116:119], v[100:103], v[36:39]\n\t"
      "ds_read_b64_tr_b16 v[88:89], v169 offset:3072\n\t"
      "v_mfma_f32_16x16x32_bf16 v[20:23], v[116:119], v[92:95], v[20:23]\n\t"
      "ds_read_b64_tr_b16 v[90:91], v169 offset:3584\n\t"
      "s_barrier\n\t"
      "s_waitcnt lgkmcnt(11)\n\t"
      "v_mfma_f32_16x16x32_bf16 v[0:3], v[132:135], v[124:127], v[0:3]\n\t"
      "ds_read_b64_tr_b16 v[104:105], v168 offset:16384\n\t"
      "ds_read_b64_tr_b16 v[106:107], v168 offset:16896\n\t"
      "v_mfma_f32_16x16x32_bf16 v[8:11], v[132:135], v[128:131], v[8:11]\n\t"
      "ds_read_b128 v[96:99], v160 offset:12288\n\t"
      "v_mfma_f32_16x16x32_bf16 v[4:7], v[136:139], v[124:127], v[4:7]\n\t"
      "ds_read_b128 v[100:103], v160 offset:26624\n\t"
      "v_mfma_f32_16x16x32_bf16 v[12:15], v[136:139], v[128:131], v[12:15]\n\t"
      "ds_read_b64_tr_b16 v[108:109], v169 offset:16384\n\t"
      "v_mfma_f32_16x16x32_bf16 v[24:27], v[140:143], v[124:127], v[24:27]\n\t"
      "ds_read_b64_tr_b16 v[110:111], v169 offset:16896\n\t"
      "v_mfma_f32_16x16x32_bf16 v[32:35], v[140:143], v[128:131], v[32:35]\n\t"
      "ds_read_b64_tr_b16 v[112:113], v168 offset:17408\n\t"
      "v_mfma_f32_16x16x32_bf16 v[16:19], v[140:143], v[120:123], v[16:19]\n\t"
      "ds_read_b64_tr_b16 v[114:115], v168 offset:17920\n\t"
      "v_mfma_f32_16x16x32_bf16 v[28:31], v[144:147], v[124:127], v[28:31]\n\t"
      "ds_read_b128 v[92:95], v164 offset:12288\n\t"
      "v_mfma_f32_16x16x32_bf16 v[36:39], v[144:147], v[128:131], v[36:39]\n\t"
      "ds_read_b64_tr_b16 v[116:117], v169 offset:17408\n\t"
      "v_mfma_f32_16x16x32_bf16 v[20:23], v[144:147], v[120:123], v[20:23]\n\t"
      "ds_read_b64_tr_b16 v[118:119], v169 offset:17920\n\t"
      "s_waitcnt lgkmcnt(11)\n\t"
      "v_mfma_f32_16x16x32_bf16 v[0:3], v[76:79], v[68:71], v[0:3]\n\t"
      "ds_read_b64_tr_b16 v[132:133], v168 offset:18432\n\t"
      "ds_read_b64_tr_b16 v[134:135], v168 offset:18944\n\t"
      "v_mfma_f32_16x16x32_bf16 v[8:11], v[76:79], v[72:75], v[8:11]\n\t"
      "ds_read_b128 v[124:127], v161 offset:13312\n\t"
      "v_mfma_f32_16x16x32_bf16 v[4:7], v[80:83], v[68:71], v[4:7]\n\t"
      "ds_read_b128 v[128:131], v161 offset:27648\n\t"
      "v_mfma_f32_16x16x32_bf16 v[12:15], v[80:83], v[72:75], v[12:15]\n\t"
      "ds_read_b64_tr_b16 v[136:137], v169 offset:18432\n\t"
      "v_mfma_f32_16x16x32_bf16 v[24:27], v[84:87], v[68:71], v[24:27]\n\t"
      "ds_read_b64_tr_b16 v[138:139], v169 offset:18944\n\t"
      "v_mfma_f32_16x16x32_bf16 v[32:35], v[84:87], v[72:75], v[32:35]\n\t"
      "ds_read_b64_tr_b16 v[140:141], v168 offset:19456\n\t"
      "v_mfma_f32_16x16x32_bf16 v[16:19], v[84:87], v[64:67], v[16:19]\n\t"
      "ds_read_b64_tr_b16 v[142:143], v168 offset:19968\n\t"
      "v_mfma_f32_16x16x32_bf16 v[28:31], v[88:91], v[68:71], v[28:31]\n\t"
      "ds_read_b128 v[120:123], v165 offset:13312\n\t"
      "v_mfma_f32_16x16x32_bf16 v[36:39], v[88:91], v[72:75], v[36:39]\n\t"
      "ds_read_b64_tr_b16 v[144:145], v169 offset:19456\n\t"
      "v_mfma_f32_16x16x32_bf16 v[20:23], v[88:91], v[64:67], v[20:23]\n\t"
      "ds_read_b64_tr_b16 v[146:147], v169 offset:19968\n\t"
      "s_barrier\n\t"
      "s_waitcnt lgkmcnt(11)\n\t"
      "v_mfma_f32_16x16x32_bf16 v[0:3], v[104:107], v[96:99], v[0:3]\n\t"
      "ds_read_b64_tr_b16 v[76:77], v166\n\t"
      "v_mfma_f32_16x16x32_bf16 v[8:11], v[104:107], v[100:103], v[8:11]\n\t"
      "ds_read_b64_tr_b16 v[78:79], v166 offset:512\n\t"
      "v_mfma_f32_16x16x32_bf16 v[4:7], v[108:111], v[96:99], v[4:7]\n\t"
      "ds_read_b128 v[68:71], v160 offset:4096\n\t"
      "v_mfma_f32_16x16x32_bf16 v[12:15], v[108:111], v[100:103], v[12:15]\n\t"
      "ds_read_b128 v[72:75], v160 offset:18432\n\t"
      "v_mfma_f32_16x16x32_bf16 v[24:27], v[112:115], v[96:99], v[24:27]\n\t"
      "ds_read_b128 v[64:67], v162 offset:4096\n\t"
      "v_mfma_f32_16x16x32_bf16 v[32:35], v[112:115], v[100:103], v[32:35]\n\t"
      "ds_read_b64_tr_b16 v[80:81], v167\n\t"
      "v_mfma_f32_16x16x32_bf16 v[16:19], v[112:115], v[92:95], v[16:19]\n\t"
      "ds_read_b64_tr_b16 v[82:83], v167 offset:512\n\t"
      "v_mfma_f32_16x16x32_bf16 v[28:31], v[116:119], v[96:99], v[28:31]\n\t"
      "v_mfma_f32_16x16x32_bf16 v[36:39], v[116:119], v[100:103], v[36:39]\n\t"
      "v_mfma_f32_16x16x32_bf16 v[20:23], v[116:119], v[92:95], v[20:23]\n\t"
      "s_waitcnt lgkmcnt(7)\n\t"
      "v_mfma_f32_16x16x32_bf16 v[0:3], v[132:135], v[124:127], v[0:3]\n\t"
      "ds_read_b64_tr_b16 v[104:105], v166 offset:1024\n\t"
      "v_mfma_f32_16x16x32_bf16 v[8:11], v[132:135], v[128:131], v[8:11]\n\t"
      "ds_read_b64_tr_b16 v[106:107], v166 offset:1536\n\t"
      "v_mfma_f32_16x16x32_bf16 v[4:7], v[136:139], v[124:127], v[4:7]\n\t"
      "ds_read_b128 v[96:99], v161 offset:5120\n\t"
      "v_mfma_f32_16x16x32_bf16 v[12:15], v[136:139], v[128:131], v[12:15]\n\t"
      "ds_read_b128 v[100:103], v161 offset:19456\n\t"
      "v_mfma_f32_16x16x32_bf16 v[24:27], v[140:143], v[124:127], v[24:27]\n\t"
      "ds_read_b128 v[92:95], v163 offset:5120\n\t"
      "v_mfma_f32_16x16x32_bf16 v[32:35], v[140:143], v[128:131], v[32:35]\n\t"
      "ds_read_b64_tr_b16 v[108:109], v167 offset:1024\n\t"
      "v_mfma_f32_16x16x32_bf16 v[16:19], v[140:143], v[120:123], v[16:19]\n\t"
      "ds_read_b64_tr_b16 v[110:111], v167 offset:1536\n\t"
      "v_mfma_f32_16x16x32_bf16 v[28:31], v[144:147], v[124:127], v[28:31]\n\t"
      "v_mfma_f32_16x16x32_bf16 v[36:39], v[144:147], v[128:131], v[36:39]\n\t"
      "v_mfma_f32_16x16x32_bf16 v[20:23], v[144:147], v[120:123], v[20:23]\n\t"
      "s_waitcnt lgkmcnt(7)\n\t"
      "v_mfma_f32_16x16x32_bf16 v[48:51], v[68:71], v[76:79], v[48:51]\n\t"
      "ds_read_b64_tr_b16 v[132:133], v166 offset:2048\n\t"
      "ds_read_b64_tr_b16 v[134:135], v166 offset:2560\n\t"
      "v_mfma_f32_16x16x32_bf16 v[56:59], v[72:75], v[76:79], v[56:59]\n\t"
      "ds_read_b128 v[124:127], v160 offset:6144\n\t"
      "v_mfma_f32_16x16x32_bf16 v[40:43], v[64:67], v[76:79], v[40:43]\n\t"
      "ds_read_b128 v[128:131], v160 offset:20480\n\t"
      "v_cvt_pk_bf16_f32 v84, v0, v1\n\t"
      "v_cvt_pk_bf16_f32 v85, v2, v3\n\t"
      "v_cvt_pk_bf16_f32 v86, v4, v5\n\t"
      "v_cvt_pk_bf16_f32 v87, v6, v7\n\t"
      "v_mfma_f32_16x16x32_bf16 v[52:55], v[68:71], v[80:83], v[52:55]\n\t"
      "ds_read_b128 v[120:123], v162 offset:6144\n\t"
      "v_cvt_pk_bf16_f32 v88, v8, v9\n\t"
      "v_cvt_pk_bf16_f32 v89, v10, v11\n\t"
      "v_cvt_pk_bf16_f32 v90, v12, v13\n\t"
      "v_cvt_pk_bf16_f32 v91, v14, v15\n\t"
      "v_mfma_f32_16x16x32_bf16 v[60:63], v[72:75], v[80:83], v[60:63]\n\t"
      "ds_read_b64_tr_b16 v[136:137], v167 offset:2048\n\t"
      "v_cvt_pk_bf16_f32 v152, v24, v25\n\t"
      "v_cvt_pk_bf16_f32 v153, v26, v27\n\t"
      "v_cvt_pk_bf16_f32 v154, v28, v29\n\t"
      "v_cvt_pk_bf16_f32 v155, v30, v31\n\t"
      "v_mfma_f32_16x16x32_bf16 v[44:47], v[64:67], v[80:83], v[44:47]\n\t"
      "ds_read_b64_tr_b16 v[138:139], v167 offset:2560\n\t"
      "v_cndmask_b32_e64 v86, v86, 0, s[68:69]\n\t"
      "v_cndmask_b32_e64 v87, v87, 0, s[68:69]\n\t"
      "v_cvt_pk_bf16_f32 v156, v32, v33\n\t"
      "v_cvt_pk_bf16_f32 v157, v34, v35\n\t"
      "s_waitcnt lgkmcnt(7)\n\t"
      "v_mfma_f32_16x16x32_bf16 v[48:51], v[96:99], v[104:107], v[48:51]\n\t"
      "ds_read_b64_tr_b16 v[76:77], v166 offset:3072\n\t"
      "ds_read_b64_tr_b16 v[78:79], v166 offset:3584\n\t"
      "v_cvt_pk_bf16_f32 v158, v36, v37\n\t"
      "v_cvt_pk_bf16_f32 v159, v38, v39\n\t"
      "v_cndmask_b32_e64 v90, v90, 0, s[68:69]\n\t"
      "v_cndmask_b32_e64 v91, v91, 0, s[68:69]\n\t"
      "v_mfma_f32_16x16x32_bf16 v[56:59], v[100:103], v[104:107], v[56:59]\n\t"
      "ds_read_b128 v[68:71], v161 offset:7168\n\t"
      "v_cvt_pk_bf16_f32 v148, v16, v17\n\t"
      "v_cvt_pk_bf16_f32 v149, v18, v19\n\t"
      "v_cvt_pk_bf16_f32 v150, v20, v21\n\t"
      "v_cvt_pk_bf16_f32 v151, v22, v23\n\t"
      "v_mfma_f32_16x16x32_bf16 v[40:43], v[92:95], v[104:107], v[40:43]\n\t"
      "ds_read_b128 v[72:75], v161 offset:21504\n\t"
      "v_mov_b32_e32 v34, 0x3e8b97f6\n\t"
      "v_mfma_f32_16x16x32_bf16 v[28:31], v[156:159], v[88:91], v[176:179]\n\t"
      "v_mfma_f32_16x16x32_bf16 v[24:27], v[152:155], v[88:91], v[172:175]\n\t"
      "v_mfma_f32_16x16x32_bf16 v[20:23], v[152:155], v[84:87], v[176:179]\n\t"
      "v_mfma_f32_16x16x32_bf16 v[52:55], v[96:99], v[108:111], v[52:55]\n\t"
      "ds_read_b128 v[64:67], v165 offset:7168\n\t"
      "v_mfma_f32_16x16x32_bf16 v[16:19], v[148:151], v[84:87], v[180:183]\n\t"
      "v_mfma_f32_16x16x32_bf16 v[60:63], v[100:103], v[108:111], v[60:63]\n\t"
      "ds_read_b64_tr_b16 v[80:81], v167 offset:3072\n\t"
      "s_nop 1\n\t"
      "v_max_f32_e32 v24, v24, v28\n\t"
      "v_mfma_f32_16x16x32_bf16 v[44:47], v[92:95], v[108:111], v[44:47]\n\t"
      "ds_read_b64_tr_b16 v[82:83], v167 offset:3584\n\t"
      "v_max_f32_e32 v25, v25, v29\n\t"
      "v_max_f32_e32 v26, v26, v30\n\t"
      "v_max_f32_e32 v27, v27, v31\n\t"
      "v_max_f32_e32 v16, v16, v20\n\t"
      "s_barrier\n\t"
      "s_waitcnt lgkmcnt(7)\n\t"
      "v_mfma_f32_16x16x32_bf16 v[48:51], v[124:127], v[132:135], v[48:51]\n\t"
      "ds_read_b64_tr_b16 v[104:105], v166 offset:16384\n\t"
      "ds_read_b64_tr_b16 v[106:107], v166 offset:16896\n\t"
      "v_max_f32_e32 v17, v17, v21\n\t"
      "v_max_f32_e32 v18, v18, v22\n\t"
      "v_max_f32_e32 v19, v19, v23\n\t"
      "v_max3_f32 v33, v24, v25, v26\n\t"
      "v_mfma_f32_16x16x32_bf16 v[56:59], v[128:131], v[132:135], v[56:59]\n\t"
      "ds_read_b128 v[96:99], v160 offset:8192\n\t"
      "v_max3_f32 v32, v16, v17, v18\n\t"
      "v_max_f32_e32 v33, v33, v27\n\t"
      "v_max_f32_e32 v32, v32, v19\n\t"
      "v_mfma_f32_16x16x32_bf16 v[40:43], v[120:123], v[132:135], v[40:43]\n\t"
      "ds_read_b128 v[100:103], v160 offset:22528\n\t"
      "v_permlane16_swap_b32_e32 v32, v33\n\t"
      "v_max_f32_e32 v32, v32, v33\n\t"
      "v_mov_b32_e32 v33, v32\n\t"
      "v_mfma_f32_16x16x32_bf16 v[52:55], v[124:127], v[136:139], v[52:55]\n\t"
      "ds_read_b128 v[92:95], v164 offset:8192\n\t"
      "v_permlane32_swap_b32_e32 v32, v33\n\t"
      "v_max_f32_e32 v32, v32, v33\n\t"
      "v_mov_b32_e32 v33, v32\n\t"
      "v_mfma_f32_16x16x32_bf16 v[60:63], v[128:131], v[136:139], v[60:63]\n\t"
      "ds_read_b64_tr_b16 v[108:109], v167 offset:16384\n\t"
      "v_permlane16_swap_b32_e32 v32, v33\n\t"
      "v_mul_f32_e64 v33, -v33, v34\n\t"
      "v_mul_f32_e64 v32, -v32, v34\n\t"
      "v_fma_f32 v24, v24, v34, v33\n\t"
      "v_mfma_f32_16x16x32_bf16 v[44:47], v[120:123], v[136:139], v[44:47]\n\t"
      "ds_read_b64_tr_b16 v[110:111], v167 offset:16896\n\t"
      "v_fma_f32 v25, v25, v34, v33\n\t"
      "v_fma_f32 v26, v26, v34, v33\n\t"
      "v_fma_f32 v27, v27, v34, v33\n\t"
      "v_fma_f32 v16, v16, v34, v32\n\t"
      "s_waitcnt lgkmcnt(7)\n\t"
      "v_mfma_f32_16x16x32_bf16 v[48:51], v[68:71], v[76:79], v[48:51]\n\t"
      "ds_read_b64_tr_b16 v[132:133], v166 offset:17408\n\t"
      "ds_read_b64_tr_b16 v[134:135], v166 offset:17920\n\t"
      "v_fma_f32 v17, v17, v34, v32\n\t"
      "v_fma_f32 v18, v18, v34, v32\n\t"
      "v_fma_f32 v19, v19, v34, v32\n\t"
      "v_exp_f32_e32 v24, v24\n\t"
      "v_mfma_f32_16x16x32_bf16 v[56:59], v[72:75], v[76:79], v[56:59]\n\t"
      "ds_read_b128 v[124:127], v161 offset:9216\n\t"
      "v_exp_f32_e32 v25, v25\n\t"
      "v_exp_f32_e32 v26, v26\n\t"
      "v_mfma_f32_16x16x32_bf16 v[40:43], v[64:67], v[76:79], v[40:43]\n\t"
      "ds_read_b128 v[128:131], v161 offset:23552\n\t"
      "v_exp_f32_e32 v27, v27\n\t"
      "v_exp_f32_e32 v16, v16\n\t"
      "v_mfma_f32_16x16x32_bf16 v[52:55], v[68:71], v[80:83], v[52:55]\n\t"
      "ds_read_b128 v[120:123], v165 offset:9216\n\t"
      "v_exp_f32_e32 v17, v17\n\t"
      "v_exp_f32_e32 v18, v18\n\t"
      "v_mfma_f32_16x16x32_bf16 v[60:63], v[72:75], v[80:83], v[60:63]\n\t"
      "ds_read_b64_tr_b16 v[136:137], v167 offset:17408\n\t"
      "v_exp_f32_e32 v19, v19\n\t"
      "v_add_f32_e32 v33, v24, v25\n\t"
      "v_mfma_f32_16x16x32_bf16 v[44:47], v[64:67], v[80:83], v[44:47]\n\t"
      "ds_read_b64_tr_b16 v[138:139], v167 offset:17920\n\t"
      "v_add_f32_e32 v28, v26, v27\n\t"
      "v_add_f32_e32 v32, v16, v17\n\t"
      "v_add_f32_e32 v20, v18, v19\n\t"
      "v_add_f32_e32 v33, v33, v28\n\t"
      "s_waitcnt lgkmcnt(7)\n\t"
      "v_mfma_f32_16x16x32_bf16 v[48:51], v[96:99], v[104:107], v[48:51]\n\t"
      "ds_read_b64_tr_b16 v[76:77], v166 offset:18432\n\t"
      "ds_read_b64_tr_b16 v[78:79], v166 offset:18944\n\t"
      "v_add_f32_e32 v32, v32, v20\n\t"
      "s_nop 1\n\t"
      "v_permlane16_swap_b32_e32 v32, v33\n\t"
      "v_add_f32_e32 v32, v32, v33\n\t"
      "v_mfma_f32_16x16x32_bf16 v[56:59], v[100:103], v[104:107], v[56:59]\n\t"
      "ds_read_b128 v[68:71], v160 offset:10240\n\t"
      "v_mov_b32_e32 v33, v32\n\t"
      "s_nop 1\n\t"
      "v_permlane32_swap_b32_e32 v32, v33\n\t"
      "v_add_f32_e32 v32, v32, v33\n\t"
      "v_mfma_f32_16x16x32_bf16 v[40:43], v[92:95], v[104:107], v[40:43]\n\t"
      "ds_read_b128 v[72:75], v160 offset:24576\n\t"
      "v_mov_b32_e32 v33, v32\n\t"
      "s_nop 1\n\t"
      "v_permlane16_swap_b32_e32 v32, v33\n\t"
      "v_rcp_f32_e32 v33, v33\n\t"
      "v_mfma_f32_16x16x32_bf16 v[52:55], v[96:99], v[108:111], v[52:55]\n\t"
      "ds_read_b128 v[64:67], v164 offset:10240\n\t"
      "v_rcp_f32_e32 v32, v32\n\t"
      "v_mul_f32_e32 v24, v24, v33\n\t"
      "v_mfma_f32_16x16x32_bf16 v[60:63], v[100:103], v[108:111], v[60:63]\n\t"
      "ds_read_b64_tr_b16 v[80:81], v167 offset:18432\n\t"
      "v_mul_f32_e32 v25, v25, v33\n\t"
      "v_mul_f32_e32 v26, v26, v33\n\t"
      "v_mul_f32_e32 v27, v27, v33\n\t"
      "v_mul_f32_e32 v16, v16, v32\n\t"
      "v_mfma_f32_16x16x32_bf16 v[44:47], v[92:95], v[108:111], v[44:47]\n\t"
      "ds_read_b64_tr_b16 v[82:83], v167 offset:18944\n\t"
      "v_mul_f32_e32 v17, v17, v32\n\t"
      "v_mul_f32_e32 v18, v18, v32\n\t"
      "v_mul_f32_e32 v19, v19, v32\n\t"
      "v_cvt_pk_bf16_f32 v4, v24, v25\n\t"
      "s_waitcnt lgkmcnt(7)\n\t"
      "v_mfma_f32_16x16x32_bf16 v[48:51], v[124:127], v[132:135], v[48:51]\n\t"
      "ds_read_b64_tr_b16 v[104:105], v166 offset:19456\n\t"
      "ds_read_b64_tr_b16 v[106:107], v166 offset:19968\n\t"
      "v_cvt_pk_bf16_f32 v5, v26, v27\n\t"
      "v_cvt_pk_bf16_f32 v0, v16, v17\n\t"
      "v_cvt_pk_bf16_f32 v1, v18, v19\n\t"
      "v_and_b32_e32 v6, v184, v4\n\t"
      "v_mfma_f32_16x16x32_bf16 v[56:59], v[128:131], v[132:135], v[56:59]\n\t"
      "ds_read_b128 v[96:99], v161 offset:11264\n\t"
      "v_and_b32_e32 v7, v185, v5\n\t"
      "v_and_b32_e32 v2, v184, v0\n\t"
      "v_and_b32_e32 v3, v185, v1\n\t"
      "v_xor_b32_e32 v4, v4, v6\n\t"
      "v_mfma_f32_16x16x32_bf16 v[40:43], v[120:123], v[132:135], v[40:43]\n\t"
      "ds_read_b128 v[100:103], v161 offset:25600\n\t"
      "v_xor_b32_e32 v5, v5, v7\n\t"
      "v_xor_b32_e32 v0, v0, v2\n\t"
      "v_xor_b32_e32 v1, v1, v3\n\t"
      "v_mfma_f32_16x16x32_bf16 v[52:55], v[124:127], v[136:139], v[52:55]\n\t"
      "ds_read_b128 v[92:95], v165 offset:11264\n\t"
      "v_mfma_f32_16x16x32_bf16 v[60:63], v[128:131], v[136:139], v[60:63]\n\t"
      "ds_read_b64_tr_b16 v[108:109], v167 offset:19456\n\t"
      "v_mfma_f32_16x16x32_bf16 v[44:47], v[120:123], v[136:139], v[44:47]\n\t"
      "ds_read_b64_tr_b16 v[110:111], v167 offset:19968\n\t"
      "s_barrier\n\t"
      "s_waitcnt lgkmcnt(7)\n\t"
      "v_mfma_f32_16x16x32_bf16 v[48:51], v[68:71], v[76:79], v[48:51]\n\t"
      "ds_read_b64_tr_b16 v[132:133], v170 offset:32768\n\t"
      "ds_read_b64_tr_b16 v[134:135], v170 offset:33280\n\t"
      "v_mfma_f32_16x16x32_bf16 v[56:59], v[72:75], v[76:79], v[56:59]\n\t"
      "ds_read_b128 v[124:127], v160 offset:12288\n\t"
      "v_mfma_f32_16x16x32_bf16 v[40:43], v[64:67], v[76:79], v[40:43]\n\t"
      "ds_read_b128 v[128:131], v160 offset:26624\n\t"
      "v_mfma_f32_16x16x32_bf16 v[52:55], v[68:71], v[80:83], v[52:55]\n\t"
      "ds_read_b128 v[120:123], v164 offset:12288\n\t"
      "v_mfma_f32_16x16x32_bf16 v[60:63], v[72:75], v[80:83], v[60:63]\n\t"
      "ds_read_b64_tr_b16 v[136:137], v171 offset:32768\n\t"
      "v_mfma_f32_16x16x32_bf16 v[44:47], v[64:67], v[80:83], v[44:47]\n\t"
      "ds_read_b64_tr_b16 v[138:139], v171 offset:33280\n\t"
      "s_waitcnt lgkmcnt(7)\n\t"
      "v_mfma_f32_16x16x32_bf16 v[48:51], v[96:99], v[104:107], v[48:51]\n\t"
      "ds_read_b64_tr_b16 v[76:77], v170 offset:33792\n\t"
      "ds_read_b64_tr_b16 v[78:79], v170 offset:34304\n\t"
      "v_mfma_f32_16x16x32_bf16 v[56:59], v[100:103], v[104:107], v[56:59]\n\t"
      "ds_read_b128 v[68:71], v161 offset:13312\n\t"
      "v_mfma_f32_16x16x32_bf16 v[40:43], v[92:95], v[104:107], v[40:43]\n\t"
      "ds_read_b128 v[72:75], v161 offset:27648\n\t"
      "v_mfma_f32_16x16x32_bf16 v[52:55], v[96:99], v[108:111], v[52:55]\n\t"
      "ds_read_b128 v[64:67], v165 offset:13312\n\t"
      "v_mfma_f32_16x16x32_bf16 v[60:63], v[100:103], v[108:111], v[60:63]\n\t"
      "ds_read_b64_tr_b16 v[80:81], v171 offset:33792\n\t"
      "v_mfma_f32_16x16x32_bf16 v[44:47], v[92:95], v[108:111], v[44:47]\n\t"
      "ds_read_b64_tr_b16 v[82:83], v171 offset:34304\n\t"
      "s_waitcnt lgkmcnt(7)\n\t"
      "v_mfma_f32_16x16x32_bf16 v[48:51], v[124:127], v[132:135], v[48:51]\n\t"
      "v_mfma_f32_16x16x32_bf16 v[56:59], v[128:131], v[132:135], v[56:59]\n\t"
      "v_mfma_f32_16x16x32_bf16 v[40:43], v[120:123], v[132:135], v[40:43]\n\t"
      "v_mfma_f32_16x16x32_bf16 v[52:55], v[124:127], v[136:139], v[52:55]\n\t"
      "v_mfma_f32_16x16x32_bf16 v[60:63], v[128:131], v[136:139], v[60:63]\n\t"
      "v_mfma_f32_16x16x32_bf16 v[44:47], v[120:123], v[136:139], v[44:47]\n\t"
      "s_waitcnt lgkmcnt(0)\n\t"
      "v_mfma_f32_16x16x32_bf16 v[48:51], v[68:71], v[76:79], v[48:51]\n\t"
      "v_mfma_f32_16x16x32_bf16 v[56:59], v[72:75], v[76:79], v[56:59]\n\t"
      "v_mfma_f32_16x16x32_bf16 v[40:43], v[64:67], v[76:79], v[40:43]\n\t"
      "v_mfma_f32_16x16x32_bf16 v[52:55], v[68:71], v[80:83], v[52:55]\n\t"
      "v_mfma_f32_16x16x32_bf16 v[60:63], v[72:75], v[80:83], v[60:63]\n\t"
      "v_mfma_f32_16x16x32_bf16 v[44:47], v[64:67], v[80:83], v[44:47]\n\t"
      "s_nop 2\n\t"
      "v_cvt_pk_bf16_f32 v66, v48, v49\n\t"
      "v_cvt_pk_bf16_f32 v67, v50, v51\n\t"
      "v_cvt_pk_bf16_f32 v68, v56, v57\n\t"
      "v_cvt_pk_bf16_f32 v69, v58, v59\n\t"
      "v_cvt_pk_bf16_f32 v64, v40, v41\n\t"
      "v_cvt_pk_bf16_f32 v65, v42, v43\n\t"
      "v_mfma_f32_16x16x32_bf16 v[88:91], v[66:69], v[4:7], 0\n\t"
      "s_nop 0\n\t"
      "v_mfma_f32_16x16x32_bf16 v[80:83], v[64:67], v[0:3], 0\n\t"
      "v_cvt_pk_bf16_f32 v72, v52, v53\n\t"
      "v_cvt_pk_bf16_f32 v73, v54, v55\n\t"
      "v_cvt_pk_bf16_f32 v74, v60, v61\n\t"
      "v_cvt_pk_bf16_f32 v75, v62, v63\n\t"
      "v_cvt_pk_bf16_f32 v70, v44, v45\n\t"
      "v_cvt_pk_bf16_f32 v71, v46, v47\n\t"
      "v_mfma_f32_16x16x32_bf16 v[92:95], v[72:75], v[4:7], 0\n\t"
      "s_nop 0\n\t"
      "v_mfma_f32_16x16x32_bf16 v[84:87], v[70:73], v[0:3], 0\n\t"
      "v_cvt_pk_bf16_f32 v152, v88, v89\n\t"
      "v_cvt_pk_bf16_f32 v153, v90, v91\n\t"
      "v_min_u32_e32 v152, -2, v152\n\t"
      "v_min_u32_e32 v153, -2, v153\n\t"
      "v_cvt_pk_bf16_f32 v148, v80, v81\n\t"
      "v_cvt_pk_bf16_f32 v149, v82, v83\n\t"
      "v_min_u32_e32 v148, -2, v148\n\t"
      "v_min_u32_e32 v149, -2, v149\n\t"
      "v_cvt_pk_bf16_f32 v154, v92, v93\n\t"
      "v_cvt_pk_bf16_f32 v155, v94, v95\n\t"
      "v_cndmask_b32_e64 v154, v154, 0, s[68:69]\n\t"
      "v_min_u32_e32 v154, -2, v154\n\t"
      "v_cndmask_b32_e64 v155, v155, 0, s[68:69]\n\t"
      "v_min_u32_e32 v155, -2, v155\n\t"
      "global_store_dwordx4 v244, v[152:155], s[44:45] sc1\n\t"
      "v_cvt_pk_bf16_f32 v150, v84, v85\n\t"
      "v_cvt_pk_bf16_f32 v151, v86, v87\n\t"
      "v_cndmask_b32_e64 v150, v150, 0, s[68:69]\n\t"
      "v_min_u32_e32 v150, -2, v150\n\t"
      "v_cndmask_b32_e64 v151, v151, 0, s[68:69]\n\t"
      "v_min_u32_e32 v151, -2, v151\n\t"
      "global_store_dwordx4 v244, v[148:151], s[42:43] sc1\n\t"
      "s_barrier\n\t"
      "s_cmp_eq_u32 s26, 7\n\t"
      "s_cbranch_scc1 SPARE_%=\n\t"
      "s_lshl_b32 s34, s25, 16\n\t"
      "s_lshl2_add_u32 s34, s39, s34\n\t"
      "s_add_u32 s34, s34, 4096\n\t"
      "s_add_u32 s42, s20, s34\n\t"
      "s_addc_u32 s43, s21, 0\n\t"
      "s_add_u32 s44, s42, 8192\n\t"
      "s_addc_u32 s45, s43, 0\n\t"
      "s_lshl_b32 s72, s24, 10\n\t"
      "s_add_u32 s72, s72, 98304\n\t"
      "v_xor_b32_e32 v203, v197, v198\n\t"
      "v_lshl_add_u32 v203, v203, 4, s72\n\t"
      "v_lshl_add_u32 v203, v197, 7, v203\n\t"
      "ds_write_b128 v203, v[204:207]\n\t"
      "s_and_b32 s35, s25, 3\n\t"
      "s_lshl2_add_u32 s31, s26, s35\n\t"
      "s_and_b32 s35, s2, 7\n\t"
      "s_mul_i32 s34, s35, 14\n\t"
      "s_lshl4_add_u32 s35, s35, -14\n\t"
      "s_cmp_lt_u32 s31, 14\n\t"
      "s_cselect_b32 s34, s34, s35\n\t"
      "s_cselect_b32 s36, 1, 0\n\t"
      "s_or_b32 s27, s27, s36\n\t"
      "s_add_u32 s34, s34, s31\n\t"
      "s_lshr_b32 s29, s34, 2\n\t"
      "s_and_b32 s32, s34, 3\n\t"
      "s_mul_i32 s30, s32, 7\n\t"
      "v_and_b32_e32 v199, 60, v192\n\t"
      "v_and_b32_e32 v200, 24, v192\n\t"
      "v_lshlrev_b32_e32 v202, 3, v196\n\t"
      "v_lshl_or_b32 v202, v200, 2, v202\n\t"
      "v_lshl_or_b32 v202, v195, 7, v202\n\t"
      "v_lshl_or_b32 v202, v193, 10, v202\n\t"
      "v_add_u32_e32 v224, s39, v202\n\t"
      "v_xor_b32_e32 v225, 32, v224\n\t"
      "v_xor_b32_e32 v226, 64, v224\n\t"
      "v_xor_b32_e32 v227, 96, v224\n\t"
      "s_lshl_b32 s34, s24, 12\n\t"
      "s_add_u32 s34, s34, 98304\n\t"
      "v_add_u32_e32 v245, s34, v244\n\t"
      "v_lshlrev_b32_e32 v199, 2, v199\n\t"
      "s_lshl_b32 s34, s24, 8\n\t"
      "v_lshl_add_u32 v247, v192, 2, s34\n\t"
      "v_and_b32_e32 v200, 2, v192\n\t"
      "v_mul_u32_u24_e32 v202, 576, v200\n\t"
      "v_xor_b32_e32 v199, v202, v199\n\t"
      "v_and_b32_e32 v202, 1, v192\n\t"
      "v_lshl_or_b32 v199, v202, 3, v199\n\t"
      "s_and_b32 s35, s34, 0x400\n\t"
      "s_add_u32 s34, s34, s35\n\t"
      "s_add_u32 s34, s34, 98304\n\t"
      "v_add_u32_e32 v246, s34, v199\n\t"
      "v_lshlrev_b32_e32 v199, 2, v202\n\t"
      "v_lshl_or_b32 v199, v200, 4, v199\n\t"
      "s_min_u32 s34, s29, 27\n\t"
      "s_lshl_b32 s34, s34, 7\n\t"
      "s_lshr_b32 s35, s24, 2\n\t"
      "s_lshl_b32 s35, s35, 6\n\t"
      "s_add_u32 s34, s34, s35\n\t"
      "s_lshr_b32 s35, s39, 9\n\t"
      "s_add_u32 s34, s34, s35\n\t"
      "v_add_u32_e32 v248, s34, v199\n\t"
      "s_add_u32 s48, s20, 8388608\n\t"
      "s_addc_u32 s49, s21, 0\n\t"
      "v_and_b32_e32 v199, 16, v192\n\t"
      "v_lshlrev_b32_e32 v199, 1, v199\n\t"
      "v_and_b32_e32 v200, 32, v192\n\t"
      "s_lshl_b32 s34, s28, 18\n\t"
      "s_lshl_b32 s35, s25, 11\n\t"
      "s_add_u32 s34, s34, s35\n\t"
      "s_lshr_b32 s35, s39, 3\n\t"
      "s_add_u32 s34, s34, s35\n\t"
      "v_and_b32_e32 v201, 0x1f0, v244\n\t"
      "v_lshl_or_b32 v201, v200, 11, v201\n\t"
      "s_waitcnt lgkmcnt(0)\n\t"
      "s_barrier\n\t"
      "v_xor_b32_e32 v210, 32, v228\n\t"
      "v_xor_b32_e32 v212, 64, v228\n\t"
      "v_xor_b32_e32 v214, 96, v228\n\t"
      "ds_read_b64 v[208:209], v228\n\t"
      "ds_read_b64 v[210:211], v210\n\t"
      "ds_read_b64 v[212:213], v212\n\t"
      "ds_read_b64 v[214:215], v214\n\t"
      "ds_read_b64_tr_b16 v[0:1], v216 offset:7168\n\t"
      "ds_read_b64_tr_b16 v[2:3], v220 offset:7168\n\t"
      "ds_read_b64_tr_b16 v[4:5], v217 offset:7168\n\t"
      "ds_read_b64_tr_b16 v[6:7], v221 offset:7168\n\t"
      "ds_read_b64_tr_b16 v[8:9], v218 offset:7168\n\t"
      "ds_read_b64_tr_b16 v[10:11], v222 offset:7168\n\t"
      "ds_read_b64_tr_b16 v[12:13], v219 offset:7168\n\t"
      "ds_read_b64_tr_b16 v[14:15], v223 offset:7168\n\t"
      "ds_read_b64_tr_b16 v[16:17], v218 offset:10752\n\t"
      "ds_read_b64_tr_b16 v[18:19], v222 offset:10752\n\t"
      "ds_read_b64_tr_b16 v[20:21], v219 offset:10752\n\t"
      "ds_read_b64_tr_b16 v[22:23], v223 offset:10752\n\t"
      "ds_read_b64_tr_b16 v[24:25], v216 offset:10752\n\t"
      "ds_read_b64_tr_b16 v[26:27], v220 offset:10752\n\t"
      "ds_read_b64_tr_b16 v[28:29], v217 offset:10752\n\t"
      "ds_read_b64_tr_b16 v[30:31], v221 offset:10752\n\t"
      "ds_read_b64_tr_b16 v[32:33], v216 offset:14336\n\t"
      "ds_read_b64_tr_b16 v[34:35], v220 offset:14336\n\t"
      "ds_read_b64_tr_b16 v[36:37], v217 offset:14336\n\t"
      "ds_read_b64_tr_b16 v[38:39], v221 offset:14336\n\t"
      "ds_read_b64_tr_b16 v[40:41], v218 offset:14336\n\t"
      "ds_read_b64_tr_b16 v[42:43], v222 offset:14336\n\t"
      "ds_read_b64_tr_b16 v[44:45], v219 offset:14336\n\t"
      "ds_read_b64_tr_b16 v[46:47], v223 offset:14336\n\t"
      "ds_read_b64_tr_b16 v[48:49], v218 offset:17920\n\t"
      "ds_read_b64_tr_b16 v[50:51], v222 offset:17920\n\t"
      "ds_read_b64_tr_b16 v[52:53], v219 offset:17920\n\t"
      "ds_read_b64_tr_b16 v[54:55], v223 offset:17920\n\t"
      "ds_read_b64_tr_b16 v[56:57], v216 offset:17920\n\t"
      "ds_read_b64_tr_b16 v[58:59], v220 offset:17920\n\t"
      "ds_read_b64_tr_b16 v[60:61], v217 offset:17920\n\t"
      "ds_read_b64_tr_b16 v[62:63], v221 offset:17920\n\t"
      "ds_read_b64_tr_b16 v[64:65], v216 offset:21504\n\t"
      "ds_read_b64_tr_b16 v[66:67], v220 offset:21504\n\t"
      "ds_read_b64_tr_b16 v[68:69], v217 offset:21504\n\t"
      "ds_read_b64_tr_b16 v[70:71], v221 offset:21504\n\t"
      "ds_read_b64_tr_b16 v[72:73], v218 offset:21504\n\t"
      "ds_read_b64_tr_b16 v[74:75], v222 offset:21504\n\t"
      "ds_read_b64_tr_b16 v[76:77], v219 offset:21504\n\t"
      "ds_read_b64_tr_b16 v[78:79], v223 offset:21504\n\t"
      "ds_read_b64_tr_b16 v[80:81], v218 offset:25088\n\t"
      "ds_read_b64_tr_b16 v[82:83], v222 offset:25088\n\t"
      "ds_read_b64_tr_b16 v[84:85], v219 offset:25088\n\t"
      "ds_read_b64_tr_b16 v[86:87], v223 offset:25088\n\t"
      "ds_read_b64_tr_b16 v[88:89], v216 offset:25088\n\t"
      "ds_read_b64_tr_b16 v[90:91], v220 offset:25088\n\t"
      "ds_read_b64_tr_b16 v[92:93], v217 offset:25088\n\t"
      "ds_read_b64_tr_b16 v[94:95], v221 offset:25088\n\t"
      "ds_read_b64_tr_b16 v[96:97], v216 offset:28672\n\t"
      "ds_read_b64_tr_b16 v[98:99], v220 offset:28672\n\t"
      "ds_read_b64_tr_b16 v[100:101], v217 offset:28672\n\t"
      "ds_read_b64_tr_b16 v[102:103], v221 offset:28672\n\t"
      "ds_read_b64_tr_b16 v[104:105], v218 offset:28672\n\t"
      "ds_read_b64_tr_b16 v[106:107], v222 offset:28672\n\t"
      "ds_read_b64_tr_b16 v[108:109], v219 offset:28672\n\t"
      "ds_read_b64_tr_b16 v[110:111], v223 offset:28672\n\t"
      "ds_read_b64_tr_b16 v[112:113], v218 offset:32256\n\t"
      "ds_read_b64_tr_b16 v[114:115], v222 offset:32256\n\t"
      "ds_read_b64_tr_b16 v[116:117], v219 offset:32256\n\t"
      "ds_read_b64_tr_b16 v[118:119], v223 offset:32256\n\t"
      "ds_read_b64_tr_b16 v[120:121], v216 offset:32256\n\t"
      "ds_read_b64_tr_b16 v[122:123], v220 offset:32256\n\t"
      "ds_read_b64_tr_b16 v[124:125], v217 offset:32256\n\t"
      "ds_read_b64_tr_b16 v[126:127], v221 offset:32256\n\t"
      "s_sleep 2\n\t"
      "global_load_dwordx4 v[128:131], v244, s[42:43] offset:-2048 sc1\n\t"
      "global_load_dwordx4 v[132:135], v244, s[42:43] offset:-1024 sc1\n\t"
      "global_load_dwordx4 v[136:139], v244, s[42:43] sc1\n\t"
      "global_load_dwordx4 v[140:143], v244, s[42:43] offset:1024 sc1\n\t"
      "global_load_dwordx4 v[144:147], v244, s[42:43] offset:2048 sc1\n\t"
      "global_load_dwordx4 v[148:151], v244, s[42:43] offset:3072 sc1\n\t"
      "global_load_dwordx4 v[152:155], v244, s[44:45] offset:-4096 sc1\n\t"
      "global_load_dwordx4 v[156:159], v244, s[44:45] offset:-3072 sc1\n\t"
      "global_load_dwordx4 v[160:163], v244, s[44:45] offset:-2048 sc1\n\t"
      "global_load_dwordx4 v[164:167], v244, s[44:45] offset:-1024 sc1\n\t"
      "v_add_u32_e32 v229, s34, v201\n\t"
      "v_add_u32_e32 v230, 131072, v229\n\t"
      "s_and_b32 s34, s25, 30\n\t"
      "s_lshr_b32 s35, s39, 13\n\t"
      "s_add_u32 s34, s34, s35\n\t"
      "s_mul_i32 s34, s34, 7\n\t"
      "s_add_u32 s34, s34, s28\n\t"
      "s_lshl_b32 s34, s34, 3\n\t"
      "s_and_b32 s35, s25, 1\n\t"
      "s_add_u32 s34, s34, s35\n\t"
      "s_lshl_b32 s34, s34, 10\n\t"
      "s_and_b32 s35, s24, 1\n\t"
      "s_lshl_b32 s35, s35, 9\n\t"
      "s_add_u32 s34, s34, s35\n\t"
      "s_add_u32 s34, s34, 6293504\n\t"
      "v_lshrrev_b32_e32 v201, 1, v200\n\t"
      "v_lshl_or_b32 v201, v199, 6, v201\n\t"
      "v_lshl_or_b32 v201, v194, 5, v201\n\t"
      "v_add_u32_e32 v241, s34, v201\n\t"
      "s_lshl_b32 s34, s24, 12\n\t"
      "s_add_u32 s34, s34, 131072\n\t"
      "v_xor_b32_e32 v201, v193, v194\n\t"
      "v_lshlrev_b32_e32 v201, 3, v201\n\t"
      "v_lshl_or_b32 v201, v194, 7, v201\n\t"
      "v_add_u32_e32 v233, s34, v201\n\t"
      "v_lshrrev_b32_e32 v199, 2, v199\n\t"
      "v_or_b32_e32 v199, v199, v195\n\t"
      "v_lshrrev_b32_e32 v200, 3, v200\n\t"
      "v_or_b32_e32 v200, v196, v200\n\t"
      "v_xor_b32_e32 v200, v200, v199\n\t"
      "v_lshlrev_b32_e32 v200, 3, v200\n\t"
      "v_lshl_or_b32 v200, v199, 7, v200\n\t"
      "v_add_u32_e32 v237, s34, v200\n\t"
      "v_xor_b32_e32 v234, 32, v233\n\t"
      "v_xor_b32_e32 v238, 32, v237\n\t"
      "v_xor_b32_e32 v235, 64, v233\n\t"
      "v_xor_b32_e32 v239, 64, v237\n\t"
      "v_xor_b32_e32 v236, 96, v233\n\t"
      "v_xor_b32_e32 v240, 96, v237\n\t"
      "s_waitcnt lgkmcnt(14)\n\t"
      "v_lshlrev_b32_e32 v192, 16, v208\n\t"
      "v_and_b32_e32 v193, 0xffff0000, v208\n\t"
      "v_lshlrev_b32_e32 v194, 16, v209\n\t"
      "v_and_b32_e32 v195, 0xffff0000, v209\n\t"
      "v_lshlrev_b32_e32 v196, 16, v210\n\t"
      "v_and_b32_e32 v197, 0xffff0000, v210\n\t"
      "v_lshlrev_b32_e32 v198, 16, v211\n\t"
      "v_and_b32_e32 v199, 0xffff0000, v211\n\t"
      "v_lshlrev_b32_e32 v200, 16, v212\n\t"
      "v_and_b32_e32 v201, 0xffff0000, v212\n\t"
      "v_lshlrev_b32_e32 v202, 16, v213\n\t"
      "v_and_b32_e32 v203, 0xffff0000, v213\n\t"
      "v_lshlrev_b32_e32 v204, 16, v214\n\t"
      "v_and_b32_e32 v205, 0xffff0000, v214\n\t"
      "v_lshlrev_b32_e32 v206, 16, v215\n\t"
      "v_and_b32_e32 v207, 0xffff0000, v215\n\t"
      "global_load_dwordx4 v[168:171], v244, s[44:45] sc1\n\t"
      "global_load_dwordx4 v[172:175], v244, s[44:45] offset:1024 sc1\n\t"
      "global_load_dwordx4 v[176:179], v244, s[44:45] offset:2048 sc1\n\t"
      "global_load_dwordx4 v[180:183], v244, s[44:45] offset:3072 sc1\n\t"
      "global_load_dwordx4 v[184:187], v244, s[42:43] offset:-4096 sc1\n\t"
      "global_load_dwordx4 v[188:191], v244, s[42:43] offset:-3072 sc1\n\t"
      "global_load_dword v249, v248, s[12:13]\n\t"
      "s_barrier\n\t"
      "s_waitcnt lgkmcnt(0)\n\t"
      "LGM_%=:\n\t"
      "s_waitcnt vmcnt(15)\n\t"
      "v_max_u32_e32 v232, v128, v132\n\t"
      "v_cmp_eq_u32_e32 vcc, -1, v232\n\t"
      "s_cbranch_vccnz LO0R_%=\n\t"
      "LO0B_%=:\n\t"
      "v_mfma_f32_16x16x32_bf16 v[192:195], v[0:3], v[128:131], v[192:195]\n\t"
      "ds_read_b64_tr_b16 v[0:1], v216 offset:35840\n\t"
      "ds_read_b64_tr_b16 v[2:3], v220 offset:35840\n\t"
      "v_mfma_f32_16x16x32_bf16 v[196:199], v[4:7], v[128:131], v[196:199]\n\t"
      "ds_read_b64_tr_b16 v[4:5], v217 offset:35840\n\t"
      "v_mfma_f32_16x16x32_bf16 v[200:203], v[8:11], v[128:131], v[200:203]\n\t"
      "ds_read_b64_tr_b16 v[6:7], v221 offset:35840\n\t"
      "ds_read_b64_tr_b16 v[8:9], v218 offset:35840\n\t"
      "v_mfma_f32_16x16x32_bf16 v[204:207], v[12:15], v[128:131], v[204:207]\n\t"
      "ds_read_b64_tr_b16 v[10:11], v222 offset:35840\n\t"
      "v_mfma_f32_16x16x32_bf16 v[192:195], v[16:19], v[132:135], v[192:195]\n\t"
      "ds_read_b64_tr_b16 v[12:13], v219 offset:35840\n\t"
      "ds_read_b64_tr_b16 v[14:15], v223 offset:35840\n\t"
      "v_mfma_f32_16x16x32_bf16 v[196:199], v[20:23], v[132:135], v[196:199]\n\t"
      "ds_read_b64_tr_b16 v[16:17], v218 offset:39424\n\t"
      "v_mfma_f32_16x16x32_bf16 v[200:203], v[24:27], v[132:135], v[200:203]\n\t"
      "ds_read_b64_tr_b16 v[18:19], v222 offset:39424\n\t"
      "ds_read_b64_tr_b16 v[20:21], v219 offset:39424\n\t"
      "v_mfma_f32_16x16x32_bf16 v[204:207], v[28:31], v[132:135], v[204:207]\n\t"
      "ds_read_b64_tr_b16 v[22:23], v223 offset:39424\n\t"
      "s_waitcnt vmcnt(13)\n\t"
      "v_max_u32_e32 v232, v136, v140\n\t"
      "v_cmp_eq_u32_e32 vcc, -1, v232\n\t"
      "s_cbranch_vccnz LO2R_%=\n\t"
      "LO2B_%=:\n\t"
      "v_mfma_f32_16x16x32_bf16 v[192:195], v[32:35], v[136:139], v[192:195]\n\t"
      "ds_read_b64_tr_b16 v[24:25], v216 offset:39424\n\t"
      "ds_read_b64_tr_b16 v[26:27], v220 offset:39424\n\t"
      "v_mfma_f32_16x16x32_bf16 v[196:199], v[36:39], v[136:139], v[196:199]\n\t"
      "ds_read_b64_tr_b16 v[28:29], v217 offset:39424\n\t"
      "v_mfma_f32_16x16x32_bf16 v[200:203], v[40:43], v[136:139], v[200:203]\n\t"
      "ds_read_b64_tr_b16 v[30:31], v221 offset:39424\n\t"
      "ds_read_b64_tr_b16 v[32:33], v216 offset:43008\n\t"
      "v_mfma_f32_16x16x32_bf16 v[204:207], v[44:47], v[136:139], v[204:207]\n\t"
      "ds_read_b64_tr_b16 v[34:35], v220 offset:43008\n\t"
      "v_mfma_f32_16x16x32_bf16 v[192:195], v[48:51], v[140:143], v[192:195]\n\t"
      "ds_read_b64_tr_b16 v[36:37], v217 offset:43008\n\t"
      "ds_read_b64_tr_b16 v[38:39], v221 offset:43008\n\t"
      "v_mfma_f32_16x16x32_bf16 v[196:199], v[52:55], v[140:143], v[196:199]\n\t"
      "ds_read_b64_tr_b16 v[40:41], v218 offset:43008\n\t"
      "v_mfma_f32_16x16x32_bf16 v[200:203], v[56:59], v[140:143], v[200:203]\n\t"
      "ds_read_b64_tr_b16 v[42:43], v222 offset:43008\n\t"
      "ds_read_b64_tr_b16 v[44:45], v219 offset:43008\n\t"
      "v_mfma_f32_16x16x32_bf16 v[204:207], v[60:63], v[140:143], v[204:207]\n\t"
      "ds_read_b64_tr_b16 v[46:47], v223 offset:43008\n\t"
      "s_waitcnt vmcnt(11)\n\t"
      "v_max_u32_e32 v232, v144, v148\n\t"
      "v_cmp_eq_u32_e32 vcc, -1, v232\n\t"
      "s_cbranch_vccnz LO4R_%=\n\t"
      "LO4B_%=:\n\t"
      "v_mfma_f32_16x16x32_bf16 v[192:195], v[64:67], v[144:147], v[192:195]\n\t"
      "ds_read_b64_tr_b16 v[48:49], v218 offset:46592\n\t"
      "ds_read_b64_tr_b16 v[50:51], v222 offset:46592\n\t"
      "v_mfma_f32_16x16x32_bf16 v[196:199], v[68:71], v[144:147], v[196:199]\n\t"
      "ds_read_b64_tr_b16 v[52:53], v219 offset:46592\n\t"
      "v_mfma_f32_16x16x32_bf16 v[200:203], v[72:75], v[144:147], v[200:203]\n\t"
      "ds_read_b64_tr_b16 v[54:55], v223 offset:46592\n\t"
      "ds_read_b64_tr_b16 v[56:57], v216 offset:46592\n\t"
      "v_mfma_f32_16x16x32_bf16 v[204:207], v[76:79], v[144:147], v[204:207]\n\t"
      "ds_read_b64_tr_b16 v[58:59], v220 offset:46592\n\t"
      "v_mfma_f32_16x16x32_bf16 v[192:195], v[80:83], v[148:151], v[192:195]\n\t"
      "ds_read_b64_tr_b16 v[60:61], v217 offset:46592\n\t"
      "ds_read_b64_tr_b16 v[62:63], v221 offset:46592\n\t"
      "v_mfma_f32_16x16x32_bf16 v[196:199], v[84:87], v[148:151], v[196:199]\n\t"
      "ds_read_b64_tr_b16 v[64:65], v216 offset:50176\n\t"
      "v_mfma_f32_16x16x32_bf16 v[200:203], v[88:91], v[148:151], v[200:203]\n\t"
      "ds_read_b64_tr_b16 v[66:67], v220 offset:50176\n\t"
      "ds_read_b64_tr_b16 v[68:69], v217 offset:50176\n\t"
      "v_mfma_f32_16x16x32_bf16 v[204:207], v[92:95], v[148:151], v[204:207]\n\t"
      "ds_read_b64_tr_b16 v[70:71], v221 offset:50176\n\t"
      "s_waitcnt vmcnt(9)\n\t"
      "v_max_u32_e32 v232, v152, v156\n\t"
      "v_cmp_eq_u32_e32 vcc, -1, v232\n\t"
      "s_cbranch_vccnz LO6R_%=\n\t"
      "LO6B_%=:\n\t"
      "v_mfma_f32_16x16x32_bf16 v[192:195], v[96:99], v[152:155], v[192:195]\n\t"
      "ds_read_b64_tr_b16 v[72:73], v218 offset:50176\n\t"
      "ds_read_b64_tr_b16 v[74:75], v222 offset:50176\n\t"
      "v_mfma_f32_16x16x32_bf16 v[196:199], v[100:103], v[152:155], v[196:199]\n\t"
      "ds_read_b64_tr_b16 v[76:77], v219 offset:50176\n\t"
      "v_mfma_f32_16x16x32_bf16 v[200:203], v[104:107], v[152:155], v[200:203]\n\t"
      "ds_read_b64_tr_b16 v[78:79], v223 offset:50176\n\t"
      "ds_read_b64_tr_b16 v[80:81], v218 offset:53760\n\t"
      "v_mfma_f32_16x16x32_bf16 v[204:207], v[108:111], v[152:155], v[204:207]\n\t"
      "ds_read_b64_tr_b16 v[82:83], v222 offset:53760\n\t"
      "v_mfma_f32_16x16x32_bf16 v[192:195], v[112:115], v[156:159], v[192:195]\n\t"
      "ds_read_b64_tr_b16 v[84:85], v219 offset:53760\n\t"
      "ds_read_b64_tr_b16 v[86:87], v223 offset:53760\n\t"
      "v_mfma_f32_16x16x32_bf16 v[196:199], v[116:119], v[156:159], v[196:199]\n\t"
      "ds_read_b64_tr_b16 v[88:89], v216 offset:53760\n\t"
      "v_mfma_f32_16x16x32_bf16 v[200:203], v[120:123], v[156:159], v[200:203]\n\t"
      "ds_read_b64_tr_b16 v[90:91], v220 offset:53760\n\t"
      "ds_read_b64_tr_b16 v[92:93], v217 offset:53760\n\t"
      "v_mfma_f32_16x16x32_bf16 v[204:207], v[124:127], v[156:159], v[204:207]\n\t"
      "ds_read_b64_tr_b16 v[94:95], v221 offset:53760\n\t"
      "s_waitcnt vmcnt(7)\n\t"
      "v_max_u32_e32 v232, v160, v164\n\t"
      "v_cmp_eq_u32_e32 vcc, -1, v232\n\t"
      "s_cbranch_vccnz LO8R_%=\n\t"
      "LO8B_%=:\n\t"
      "s_waitcnt lgkmcnt(14)\n\t"
      "v_mfma_f32_16x16x32_bf16 v[192:195], v[0:3], v[160:163], v[192:195]\n\t"
      "ds_read_b64_tr_b16 v[96:97], v216\n\t"
      "ds_read_b64_tr_b16 v[98:99], v220\n\t"
      "v_mfma_f32_16x16x32_bf16 v[196:199], v[4:7], v[160:163], v[196:199]\n\t"
      "ds_read_b64_tr_b16 v[100:101], v217\n\t"
      "v_mfma_f32_16x16x32_bf16 v[200:203], v[8:11], v[160:163], v[200:203]\n\t"
      "ds_read_b64_tr_b16 v[102:103], v221\n\t"
      "ds_read_b64_tr_b16 v[104:105], v218\n\t"
      "v_mfma_f32_16x16x32_bf16 v[204:207], v[12:15], v[160:163], v[204:207]\n\t"
      "ds_read_b64_tr_b16 v[106:107], v222\n\t"
      "v_mfma_f32_16x16x32_bf16 v[192:195], v[16:19], v[164:167], v[192:195]\n\t"
      "ds_read_b64_tr_b16 v[108:109], v219\n\t"
      "ds_read_b64_tr_b16 v[110:111], v223\n\t"
      "v_mfma_f32_16x16x32_bf16 v[196:199], v[20:23], v[164:167], v[196:199]\n\t"
      "ds_read_b64_tr_b16 v[112:113], v218 offset:3584\n\t"
      "v_mfma_f32_16x16x32_bf16 v[200:203], v[24:27], v[164:167], v[200:203]\n\t"
      "ds_read_b64_tr_b16 v[114:115], v222 offset:3584\n\t"
      "ds_read_b64_tr_b16 v[116:117], v219 offset:3584\n\t"
      "v_mfma_f32_16x16x32_bf16 v[204:207], v[28:31], v[164:167], v[204:207]\n\t"
      "ds_read_b64_tr_b16 v[118:119], v223 offset:3584\n\t"
      "s_waitcnt vmcnt(5)\n\t"
      "v_max_u32_e32 v232, v168, v172\n\t"
      "v_cmp_eq_u32_e32 vcc, -1, v232\n\t"
      "s_cbranch_vccnz LO10R_%=\n\t"
      "LO10B_%=:\n\t"
      "v_mfma_f32_16x16x32_bf16 v[192:195], v[32:35], v[168:171], v[192:195]\n\t"
      "ds_read_b64_tr_b16 v[120:121], v216 offset:3584\n\t"
      "ds_read_b64_tr_b16 v[122:123], v220 offset:3584\n\t"
      "v_mfma_f32_16x16x32_bf16 v[196:199], v[36:39], v[168:171], v[196:199]\n\t"
      "ds_read_b64_tr_b16 v[124:125], v217 offset:3584\n\t"
      "v_mfma_f32_16x16x32_bf16 v[200:203], v[40:43], v[168:171], v[200:203]\n\t"
      "ds_read_b64_tr_b16 v[126:127], v221 offset:3584\n\t"
      "v_mfma_f32_16x16x32_bf16 v[204:207], v[44:47], v[168:171], v[204:207]\n\t"
      "v_mfma_f32_16x16x32_bf16 v[192:195], v[48:51], v[172:175], v[192:195]\n\t"
      "v_mfma_f32_16x16x32_bf16 v[196:199], v[52:55], v[172:175], v[196:199]\n\t"
      "v_mfma_f32_16x16x32_bf16 v[200:203], v[56:59], v[172:175], v[200:203]\n\t"
      "v_mfma_f32_16x16x32_bf16 v[204:207], v[60:63], v[172:175], v[204:207]\n\t"
      "s_waitcnt vmcnt(3)\n\t"
      "v_max_u32_e32 v232, v176, v180\n\t"
      "v_cmp_eq_u32_e32 vcc, -1, v232\n\t"
      "s_cbranch_vccnz LO12R_%=\n\t"
      "LO12B_%=:\n\t"
      "s_waitcnt lgkmcnt(14)\n\t"
      "v_mfma_f32_16x16x32_bf16 v[192:195], v[64:67], v[176:179], v[192:195]\n\t"
      "v_mfma_f32_16x16x32_bf16 v[196:199], v[68:71], v[176:179], v[196:199]\n\t"
      "v_mfma_f32_16x16x32_bf16 v[200:203], v[72:75], v[176:179], v[200:203]\n\t"
      "v_mfma_f32_16x16x32_bf16 v[204:207], v[76:79], v[176:179], v[204:207]\n\t"
      "v_mfma_f32_16x16x32_bf16 v[192:195], v[80:83], v[180:183], v[192:195]\n\t"
      "v_mfma_f32_16x16x32_bf16 v[196:199], v[84:87], v[180:183], v[196:199]\n\t"
      "v_mfma_f32_16x16x32_bf16 v[200:203], v[88:91], v[180:183], v[200:203]\n\t"
      "v_mfma_f32_16x16x32_bf16 v[204:207], v[92:95], v[180:183], v[204:207]\n\t"
      "s_waitcnt vmcnt(1)\n\t"
      "v_max_u32_e32 v232, v184, v188\n\t"
      "v_cmp_eq_u32_e32 vcc, -1, v232\n\t"
      "s_cbranch_vccnz LO14R_%=\n\t"
      "LO14B_%=:\n\t"
      "s_waitcnt lgkmcnt(8)\n\t"
      "v_mfma_f32_16x16x32_bf16 v[192:195], v[96:99], v[184:187], v[192:195]\n\t"
      "v_mfma_f32_16x16x32_bf16 v[196:199], v[100:103], v[184:187], v[196:199]\n\t"
      "v_mfma_f32_16x16x32_bf16 v[200:203], v[104:107], v[184:187], v[200:203]\n\t"
      "v_mfma_f32_16x16x32_bf16 v[204:207], v[108:111], v[184:187], v[204:207]\n\t"
      "s_waitcnt lgkmcnt(0)\n\t"
      "v_mfma_f32_16x16x32_bf16 v[192:195], v[112:115], v[188:191], v[192:195]\n\t"
      "v_mfma_f32_16x16x32_bf16 v[196:199], v[116:119], v[188:191], v[196:199]\n\t"
      "v_mfma_f32_16x16x32_bf16 v[200:203], v[120:123], v[188:191], v[200:203]\n\t"
      "v_mfma_f32_16x16x32_bf16 v[204:207], v[124:127], v[188:191], v[204:207]\n\t"
      "s_nop 4\n\t"
      "v_cmp_u_f32_e32 vcc, v192, v192\n\t"
      "s_cbranch_vccnz LGX_%=\n\t"
      "LGB_%=:\n\t"
      "v_mov_b32_e32 v148, 0x3f803f80\n\t"
      "v_cvt_pk_bf16_f32 v208, v192, v193\n\t"
      "v_cvt_pk_bf16_f32 v209, v194, v195\n\t"
      "v_cvt_pk_bf16_f32 v210, v196, v197\n\t"
      "v_cvt_pk_bf16_f32 v211, v198, v199\n\t"
      "v_cvt_pk_bf16_f32 v212, v200, v201\n\t"
      "v_cvt_pk_bf16_f32 v213, v202, v203\n\t"
      "v_cvt_pk_bf16_f32 v214, v204, v205\n\t"
      "v_cvt_pk_bf16_f32 v215, v206, v207\n\t"
      "v_dot2_f32_bf16 v144, v208, v148, 0\n\t"
      "v_dot2_f32_bf16 v145, v208, v208, 0\n\t"
      "v_dot2_f32_bf16 v146, v209, v148, 0\n\t"
      "v_dot2_f32_bf16 v147, v209, v209, 0\n\t"
      "v_dot2_f32_bf16 v144, v210, v148, v144\n\t"
      "v_dot2_f32_bf16 v145, v210, v210, v145\n\t"
      "v_dot2_f32_bf16 v146, v211, v148, v146\n\t"
      "v_dot2_f32_bf16 v147, v211, v211, v147\n\t"
      "v_dot2_f32_bf16 v144, v212, v148, v144\n\t"
      "v_dot2_f32_bf16 v145, v212, v212, v145\n\t"
      "v_dot2_f32_bf16 v146, v213, v148, v146\n\t"
      "v_dot2_f32_bf16 v147, v213, v213, v147\n\t"
      "v_dot2_f32_bf16 v144, v214, v148, v144\n\t"
      "v_dot2_f32_bf16 v145, v214, v214, v145\n\t"
      "v_dot2_f32_bf16 v146, v215, v148, v146\n\t"
      "v_dot2_f32_bf16 v147, v215, v215, v147\n\t"
      "v_lshlrev_b32_e32 v128, 16, v208\n\t"
      "v_and_b32_e32 v129, 0xffff0000, v208\n\t"
      "v_lshlrev_b32_e32 v130, 16, v209\n\t"
      "v_pk_add_f32 v[144:145], v[144:145], v[146:147]\n\t"
      "v_and_b32_e32 v131, 0xffff0000, v209\n\t"
      "v_lshlrev_b32_e32 v132, 16, v210\n\t"
      "v_permlane16_swap_b32_e32 v144, v145\n\t"
      "v_add_f32_e32 v144, v144, v145\n\t"
      "v_mov_b32_e32 v145, v144\n\t"
      "v_and_b32_e32 v133, 0xffff0000, v210\n\t"
      "v_lshlrev_b32_e32 v134, 16, v211\n\t"
      "v_permlane32_swap_b32_e32 v144, v145\n\t"
      "v_add_f32_e32 v144, v144, v145\n\t"
      "v_mov_b32_e32 v145, v144\n\t"
      "v_and_b32_e32 v135, 0xffff0000, v211\n\t"
      "v_lshlrev_b32_e32 v136, 16, v212\n\t"
      "v_permlane16_swap_b32_e32 v144, v145\n\t"
      "s_mov_b64 exec, s[70:71]\n\t"
      "v_min_u32_e32 v144, -2, v144\n\t"
      "v_min_u32_e32 v145, -2, v145\n\t"
      "global_store_dwordx2 v231, v[144:145], s[58:59] sc1\n\t"
      "s_mov_b64 exec, -1\n\t"
      "s_barrier\n\t"
      "v_and_b32_e32 v137, 0xffff0000, v212\n\t"
      "v_lshlrev_b32_e32 v138, 16, v213\n\t"
      "v_and_b32_e32 v139, 0xffff0000, v213\n\t"
      "v_lshlrev_b32_e32 v140, 16, v214\n\t"
      "v_and_b32_e32 v141, 0xffff0000, v214\n\t"
      "v_lshlrev_b32_e32 v142, 16, v215\n\t"
      "v_and_b32_e32 v143, 0xffff0000, v215\n\t"
      "v_permlane16_swap_b32_e32 v208, v210\n\t"
      "v_permlane16_swap_b32_e32 v209, v211\n\t"
      "v_permlane16_swap_b32_e32 v212, v214\n\t"
      "v_permlane16_swap_b32_e32 v213, v215\n\t"
      "s_bitcmp0_b32 s27, 0\n\t"
      "s_cbranch_scc1 NTK0_%=\n\t"
      "ds_read_b64_tr_b16 v[0:1], v224 offset:16384\n\t"
      "ds_read_b64_tr_b16 v[2:3], v224 offset:16896\n\t"
      "ds_read_b64_tr_b16 v[4:5], v225 offset:16384\n\t"
      "ds_read_b64_tr_b16 v[6:7], v225 offset:16896\n\t"
      "ds_read_b64_tr_b16 v[8:9], v226 offset:16384\n\t"
      "ds_read_b64_tr_b16 v[10:11], v226 offset:16896\n\t"
      "ds_read_b64_tr_b16 v[12:13], v227 offset:16384\n\t"
      "ds_read_b64_tr_b16 v[14:15], v227 offset:16896\n\t"
      "ds_read_b64_tr_b16 v[16:17], v224 offset:49152\n\t"
      "ds_read_b64_tr_b16 v[18:19], v224 offset:49664\n\t"
      "ds_read_b64_tr_b16 v[20:21], v225 offset:49152\n\t"
      "ds_read_b64_tr_b16 v[22:23], v225 offset:49664\n\t"
      "ds_read_b64_tr_b16 v[24:25], v226 offset:49152\n\t"
      "ds_read_b64_tr_b16 v[26:27], v226 offset:49664\n\t"
      "ds_read_b64_tr_b16 v[28:29], v227 offset:49152\n\t"
      "ds_read_b64_tr_b16 v[30:31], v227 offset:49664\n\t"
      "ds_read_b64_tr_b16 v[32:33], v224\n\t"
      "ds_read_b64_tr_b16 v[34:35], v224 offset:512\n\t"
      "ds_read_b64_tr_b16 v[36:37], v225\n\t"
      "ds_read_b64_tr_b16 v[38:39], v225 offset:512\n\t"
      "ds_read_b64_tr_b16 v[40:41], v226\n\t"
      "ds_read_b64_tr_b16 v[42:43], v226 offset:512\n\t"
      "ds_read_b64_tr_b16 v[44:45], v227\n\t"
      "ds_read_b64_tr_b16 v[46:47], v227 offset:512\n\t"
      "ds_read_b64_tr_b16 v[48:49], v224 offset:32768\n\t"
      "ds_read_b64_tr_b16 v[50:51], v224 offset:33280\n\t"
      "ds_read_b64_tr_b16 v[52:53], v225 offset:32768\n\t"
      "ds_read_b64_tr_b16 v[54:55], v225 offset:33280\n\t"
      "ds_read_b64_tr_b16 v[56:57], v226 offset:32768\n\t"
      "ds_read_b64_tr_b16 v[58:59], v226 offset:33280\n\t"
      "ds_read_b64_tr_b16 v[60:61], v227 offset:32768\n\t"
      "ds_read_b64_tr_b16 v[62:63], v227 offset:33280\n\t"
      "NTK0_%=:\n\t"
      "s_waitcnt lgkmcnt(0)\n\t"
      "s_barrier\n\t"
      "global_load_dwordx2 v[148:149], v231, s[46:47] sc1\n\t"
      "global_load_dwordx2 v[150:151], v231, s[46:47] offset:384 sc1\n\t"
      "s_waitcnt vmcnt(0)\n\t"
      "v_max3_u32 v232, v148, v149, v150\n\t"
      "v_max_u32_e32 v232, v232, v151\n\t"
      "v_cmp_eq_u32_e32 vcc, -1, v232\n\t"
      "s_cbranch_vccnz LSR_%=\n\t"
      "LSB_%=:\n\t"
      "v_cndmask_b32_e64 v150, v150, 0, s[70:71]\n\t"
      "v_cndmask_b32_e64 v151, v151, 0, s[70:71]\n\t"
      "v_pk_add_f32 v[144:145], v[148:149], v[150:151]\n\t"
      "s_nop 1\n\t"
      "v_permlane16_swap_b32_e32 v144, v145\n\t"
      "v_add_f32_e32 v144, v144, v145\n\t"
      "v_mov_b32_e32 v145, v144\n\t"
      "s_nop 1\n\t"
      "v_permlane32_swap_b32_e32 v144, v145\n\t"
      "v_add_f32_e32 v144, v144, v145\n\t"
      "v_mov_b32_e32 v145, v144\n\t"
      "s_nop 1\n\t"
      "v_permlane16_swap_b32_e32 v144, v145\n\t"
      "v_mul_f32_e32 v144, 0xbb124925, v144\n\t"
      "v_mul_f32_e32 v145, 0x3b124925, v145\n\t"
      "v_fma_f32 v145, -v144, v144, v145\n\t"
      "v_max_f32_e32 v145, 0, v145\n\t"
      "v_add_f32_e32 v146, 0x3727c5ac, v145\n\t"
      "v_fmac_f32_e32 v145, 0x3727c5ac, v146\n\t"
      "v_rsq_f32_e32 v145, v145\n\t"
      "s_nop 0\n\t"
      "v_mul_f32_e32 v144, v144, v145\n\t"
      "v_pk_fma_f32 v[128:129], v[128:129], v[144:145], v[144:145] op_sel:[0,1,0] op_sel_hi:[1,1,0]\n\t"
      "v_pk_fma_f32 v[130:131], v[130:131], v[144:145], v[144:145] op_sel:[0,1,0] op_sel_hi:[1,1,0]\n\t"
      "v_cvt_pk_bf16_f32 v152, v128, v129\n\t"
      "v_cvt_pk_bf16_f32 v153, v130, v131\n\t"
      "ds_write_b64 v233, v[152:153]\n\t"
      "v_pk_fma_f32 v[132:133], v[132:133], v[144:145], v[144:145] op_sel:[0,1,0] op_sel_hi:[1,1,0]\n\t"
      "v_pk_fma_f32 v[134:135], v[134:135], v[144:145], v[144:145] op_sel:[0,1,0] op_sel_hi:[1,1,0]\n\t"
      "v_cvt_pk_bf16_f32 v154, v132, v133\n\t"
      "v_cvt_pk_bf16_f32 v155, v134, v135\n\t"
      "ds_write_b64 v234, v[154:155]\n\t"
      "ds_read_b64_tr_b16 v[160:161], v237\n\t"
      "ds_read_b64_tr_b16 v[162:163], v238 offset:512\n\t"
      "v_pk_fma_f32 v[136:137], v[136:137], v[144:145], v[144:145] op_sel:[0,1,0] op_sel_hi:[1,1,0]\n\t"
      "v_pk_fma_f32 v[138:139], v[138:139], v[144:145], v[144:145] op_sel:[0,1,0] op_sel_hi:[1,1,0]\n\t"
      "v_cvt_pk_bf16_f32 v156, v136, v137\n\t"
      "v_cvt_pk_bf16_f32 v157, v138, v139\n\t"
      "ds_write_b64 v235, v[156:157]\n\t"
      "v_pk_fma_f32 v[140:141], v[140:141], v[144:145], v[144:145] op_sel:[0,1,0] op_sel_hi:[1,1,0]\n\t"
      "v_pk_fma_f32 v[142:143], v[142:143], v[144:145], v[144:145] op_sel:[0,1,0] op_sel_hi:[1,1,0]\n\t"
      "v_cvt_pk_bf16_f32 v158, v140, v141\n\t"
      "v_cvt_pk_bf16_f32 v159, v142, v143\n\t"
      "ds_write_b64 v236, v[158:159]\n\t"
      "ds_read_b64_tr_b16 v[164:165], v239\n\t"
      "ds_read_b64_tr_b16 v[166:167], v240 offset:512\n\t"
      "s_waitcnt lgkmcnt(4)\n\t"
      "v_min_u32_e32 v160, -2, v160\n\t"
      "v_min_u32_e32 v161, -2, v161\n\t"
      "v_min_u32_e32 v162, -2, v162\n\t"
      "v_min_u32_e32 v163, -2, v163\n\t"
      "global_store_dwordx4 v229, v[160:163], s[48:49] sc1\n\t"
      "s_waitcnt lgkmcnt(0)\n\t"
      "v_min_u32_e32 v164, -2, v164\n\t"
      "v_min_u32_e32 v165, -2, v165\n\t"
      "v_min_u32_e32 v166, -2, v166\n\t"
      "v_min_u32_e32 v167, -2, v167\n\t"
      "global_store_dwordx4 v230, v[164:167], s[48:49] sc1\n\t"
      "s_barrier\n\t"
      "s_bitcmp0_b32 s27, 0\n\t"
      "s_cbranch_scc1 G4C_%=\n\t"
      "s_mul_i32 s34, s32, 196608\n\t"
      "s_mul_i32 s35, s24, 8192\n\t"
      "s_add_u32 s34, s34, s35\n\t"
      "s_add_u32 s34, s34, 1048576\n\t"
      "s_add_u32 s64, s48, s34\n\t"
      "s_addc_u32 s65, s49, 0\n\t"
      "v_add_u32_e32 v250, 4096, v244\n\t"
      "global_load_dwordx4 v[128:131], v250, s[64:65] offset:-4096 sc1\n\t"
      "global_load_dwordx4 v[132:135], v250, s[64:65] offset:-3072 sc1\n\t"
      "global_load_dwordx4 v[136:139], v250, s[64:65] offset:-2048 sc1\n\t"
      "global_load_dwordx4 v[140:143], v250, s[64:65] offset:-1024 sc1\n\t"
      "global_load_dwordx4 v[144:147], v250, s[64:65] sc1\n\t"
      "global_load_dwordx4 v[148:151], v250, s[64:65] offset:1024 sc1\n\t"
      "global_load_dwordx4 v[152:155], v250, s[64:65] offset:2048 sc1\n\t"
      "global_load_dwordx4 v[156:159], v250, s[64:65] offset:3072 sc1\n\t"
      "global_store_dwordx4 v241, v[208:211], s[20:21] offset:-2048 sc1\n\t"
      "global_store_dwordx4 v241, v[212:215], s[20:21] offset:2048 sc1\n\t"
      "s_setprio 0\n\t"
      "ds_read_b64_tr_b16 v[64:65], v224 offset:16384\n\t"
      "ds_read_b64_tr_b16 v[66:67], v224 offset:16896\n\t"
      "ds_read_b64_tr_b16 v[68:69], v225 offset:16384\n\t"
      "ds_read_b64_tr_b16 v[70:71], v225 offset:16896\n\t"
      "ds_read_b64_tr_b16 v[72:73], v226 offset:16384\n\t"
      "ds_read_b64_tr_b16 v[74:75], v226 offset:16896\n\t"
      "ds_read_b64_tr_b16 v[76:77], v227 offset:16384\n\t"
      "ds_read_b64_tr_b16 v[78:79], v227 offset:16896\n\t"
      "ds_read_b64_tr_b16 v[80:81], v224 offset:49152\n\t"
      "ds_read_b64_tr_b16 v[82:83], v224 offset:49664\n\t"
      "ds_read_b64_tr_b16 v[84:85], v225 offset:49152\n\t"
      "ds_read_b64_tr_b16 v[86:87], v225 offset:49664\n\t"
      "ds_read_b64_tr_b16 v[88:89], v226 offset:49152\n\t"
      "ds_read_b64_tr_b16 v[90:91], v226 offset:49664\n\t"
      "ds_read_b64_tr_b16 v[92:93], v227 offset:49152\n\t"
      "ds_read_b64_tr_b16 v[94:95], v227 offset:49664\n\t"
      "ds_read_b64_tr_b16 v[96:97], v224\n\t"
      "ds_read_b64_tr_b16 v[98:99], v224 offset:512\n\t"
      "ds_read_b64_tr_b16 v[100:101], v225\n\t"
      "ds_read_b64_tr_b16 v[102:103], v225 offset:512\n\t"
      "ds_read_b64_tr_b16 v[104:105], v226\n\t"
      "ds_read_b64_tr_b16 v[106:107], v226 offset:512\n\t"
      "ds_read_b64_tr_b16 v[108:109], v227\n\t"
      "ds_read_b64_tr_b16 v[110:111], v227 offset:512\n\t"
      "ds_read_b64_tr_b16 v[112:113], v224 offset:32768\n\t"
      "ds_read_b64_tr_b16 v[114:115], v224 offset:33280\n\t"
      "ds_read_b64_tr_b16 v[116:117], v225 offset:32768\n\t"
      "ds_read_b64_tr_b16 v[118:119], v225 offset:33280\n\t"
      "ds_read_b64_tr_b16 v[120:121], v226 offset:32768\n\t"
      "ds_read_b64_tr_b16 v[122:123], v226 offset:33280\n\t"
      "ds_read_b64_tr_b16 v[124:125], v227 offset:32768\n\t"
      "ds_read_b64_tr_b16 v[126:127], v227 offset:33280\n\t"
      "s_waitcnt vmcnt(2) lgkmcnt(0)\n\t"
      "GST_%=:\n\t"
      "s_add_u32 s66, s64, 65536\n\t"
      "s_addc_u32 s67, s65, 0\n\t"
      "s_add_u32 s68, s66, 65536\n\t"
      "s_addc_u32 s69, s67, 0\n\t"
      "s_lshl_b32 s34, s32, 18\n\t"
      "s_lshl_b32 s35, s24, 13\n\t"
      "s_add_u32 s34, s34, s35\n\t"
      "s_add_u32 s70, s48, s34\n\t"
      "s_addc_u32 s71, s49, 0\n\t"
      "s_add_u32 s72, s70, 65536\n\t"
      "s_addc_u32 s73, s71, 0\n\t"
      "s_add_u32 s74, s72, 65536\n\t"
      "s_addc_u32 s75, s73, 0\n\t"
      "s_add_u32 s76, s74, 65536\n\t"
      "s_addc_u32 s77, s75, 0\n\t"
      "s_mul_i32 s34, s30, 28\n\t"
      "s_add_u32 s34, s34, s29\n\t"
      "s_lshl_b32 s34, s34, 11\n\t"
      "s_add_u32 s34, s34, 10223616\n\t"
      "s_add_u32 s62, s20, s34\n\t"
      "s_addc_u32 s63, s21, 0\n\t"
      "v_lshlrev_b32_e32 v248, 16, v249\n\t"
      "v_and_b32_e32 v249, 0xffff0000, v249\n\t"
      "v_mov_b32_e32 v252, 0xbdd2d3e8\n\t"
      "v_xor_b32_e32 v253, 128, v245\n\t"
      "v_max3_u32 v251, v128, v129, v130\n\t"
      "v_max3_u32 v251, v251, v131, v132\n\t"
      "v_max3_u32 v251, v251, v133, v134\n\t"
      "v_max3_u32 v251, v251, v135, v136\n\t"
      "v_max3_u32 v251, v251, v137, v138\n\t"
      "v_max3_u32 v251, v251, v139, v140\n\t"
      "v_max3_u32 v251, v251, v141, v142\n\t"
      "v_max3_u32 v251, v251, v143, v144\n\t"
      "v_max3_u32 v251, v251, v145, v146\n\t"
      "v_max3_u32 v251, v251, v147, v148\n\t"
      "v_max3_u32 v251, v251, v149, v150\n\t"
      "v_max3_u32 v251, v251, v151, v152\n\t"
      "v_max3_u32 v251, v251, v153, v154\n\t"
      "v_max3_u32 v251, v251, v155, v156\n\t"
      "v_max3_u32 v251, v251, v157, v158\n\t"
      "v_max_u32_e32 v251, v251, v159\n\t"
      "v_cmp_eq_u32_e32 vcc, -1, v251\n\t"
      "s_cbranch_vccnz LA30R_%=\n\t"
      "LA30B_%=:\n\t"
      "v_mfma_f32_16x16x32_bf16 v[208:211], v[0:3], v[128:131], 0\n\t"
      "global_load_dwordx4 v[160:163], v250, s[66:67] offset:-4096 sc1\n\t"
      "v_mfma_f32_16x16x32_bf16 v[208:211], v[16:19], v[132:135], v[208:211]\n\t"
      "v_mfma_f32_16x16x32_bf16 v[208:211], v[32:35], v[136:139], v[208:211]\n\t"
      "global_load_dwordx4 v[164:167], v250, s[66:67] offset:-3072 sc1\n\t"
      "v_mfma_f32_16x16x32_bf16 v[208:211], v[48:51], v[140:143], v[208:211]\n\t"
      "v_mfma_f32_16x16x32_bf16 v[208:211], v[64:67], v[144:147], v[208:211]\n\t"
      "global_load_dwordx4 v[168:171], v250, s[66:67] offset:-2048 sc1\n\t"
      "v_mfma_f32_16x16x32_bf16 v[208:211], v[80:83], v[148:151], v[208:211]\n\t"
      "s_lshl_b32 s34, s2, 7\n\t"
      "s_add_u32 s34, s34, 65536\n\t"
      "v_mov_b32_e32 v230, s34\n\t"
      "s_cmp_lg_u32 s24, 3\n\t"
      "s_cbranch_scc1 SK17_%=\n\t"
      "global_load_dwordx2 v[228:229], v230, s[60:61] sc1\n\t"
      "SK17_%=:\n\t"
      "v_mfma_f32_16x16x32_bf16 v[208:211], v[96:99], v[152:155], v[208:211]\n\t"
      "global_load_dwordx4 v[172:175], v250, s[66:67] offset:-1024 sc1\n\t"
      "v_mfma_f32_16x16x32_bf16 v[208:211], v[112:115], v[156:159], v[208:211]\n\t"
      "v_mfma_f32_16x16x32_bf16 v[212:215], v[4:7], v[128:131], 0\n\t"
      "global_load_dwordx4 v[176:179], v250, s[66:67] sc1\n\t"
      "v_mfma_f32_16x16x32_bf16 v[212:215], v[20:23], v[132:135], v[212:215]\n\t"
      "v_mfma_f32_16x16x32_bf16 v[212:215], v[36:39], v[136:139], v[212:215]\n\t"
      "global_load_dwordx4 v[180:183], v250, s[66:67] offset:1024 sc1\n\t"
      "v_mfma_f32_16x16x32_bf16 v[212:215], v[52:55], v[140:143], v[212:215]\n\t"
      "v_mfma_f32_16x16x32_bf16 v[212:215], v[68:71], v[144:147], v[212:215]\n\t"
      "global_load_dwordx4 v[184:187], v250, s[66:67] offset:2048 sc1\n\t"
      "v_mfma_f32_16x16x32_bf16 v[212:215], v[84:87], v[148:151], v[212:215]\n\t"
      "ds_write_b128 v245, v[208:211]\n\t"
      "v_mfma_f32_16x16x32_bf16 v[212:215], v[100:103], v[152:155], v[212:215]\n\t"
      "global_load_dwordx4 v[188:191], v250, s[66:67] offset:3072 sc1\n\t"
      "v_mfma_f32_16x16x32_bf16 v[212:215], v[116:119], v[156:159], v[212:215]\n\t"
      "v_mfma_f32_16x16x32_bf16 v[216:219], v[8:11], v[128:131], 0\n\t"
      "global_load_dwordx4 v[192:195], v250, s[68:69] offset:-4096 sc1\n\t"
      "v_mfma_f32_16x16x32_bf16 v[216:219], v[24:27], v[132:135], v[216:219]\n\t"
      "v_mfma_f32_16x16x32_bf16 v[216:219], v[40:43], v[136:139], v[216:219]\n\t"
      "global_load_dwordx4 v[196:199], v250, s[68:69] offset:-3072 sc1\n\t"
      "v_mfma_f32_16x16x32_bf16 v[216:219], v[56:59], v[140:143], v[216:219]\n\t"
      "v_mfma_f32_16x16x32_bf16 v[216:219], v[72:75], v[144:147], v[216:219]\n\t"
      "global_load_dwordx4 v[200:203], v250, s[68:69] offset:-2048 sc1\n\t"
      "v_mfma_f32_16x16x32_bf16 v[216:219], v[88:91], v[148:151], v[216:219]\n\t"
      "ds_write_b128 v253, v[212:215] offset:1024\n\t"
      "v_mfma_f32_16x16x32_bf16 v[216:219], v[104:107], v[152:155], v[216:219]\n\t"
      "global_load_dwordx4 v[204:207], v250, s[68:69] offset:-1024 sc1\n\t"
      "v_mfma_f32_16x16x32_bf16 v[216:219], v[120:123], v[156:159], v[216:219]\n\t"
      "v_mfma_f32_16x16x32_bf16 v[220:223], v[12:15], v[128:131], 0\n\t"
      "global_load_dwordx4 v[128:131], v250, s[68:69] sc1\n\t"
      "v_mfma_f32_16x16x32_bf16 v[220:223], v[28:31], v[132:135], v[220:223]\n\t"
      "v_mfma_f32_16x16x32_bf16 v[220:223], v[44:47], v[136:139], v[220:223]\n\t"
      "v_mfma_f32_16x16x32_bf16 v[220:223], v[60:63], v[140:143], v[220:223]\n\t"
      "v_mfma_f32_16x16x32_bf16 v[220:223], v[76:79], v[144:147], v[220:223]\n\t"
      "global_load_dwordx4 v[132:135], v250, s[68:69] offset:1024 sc1\n\t"
      "v_mfma_f32_16x16x32_bf16 v[220:223], v[92:95], v[148:151], v[220:223]\n\t"
      "ds_write_b128 v245, v[216:219] offset:2048\n\t"
      "v_mfma_f32_16x16x32_bf16 v[220:223], v[108:111], v[152:155], v[220:223]\n\t"
      "v_mfma_f32_16x16x32_bf16 v[220:223], v[124:127], v[156:159], v[220:223]\n\t"
      "s_waitcnt vmcnt(10)\n\t"
      "v_mfma_f32_16x16x32_bf16 v[224:227], v[0:3], v[160:163], 0\n\t"
      "global_load_dwordx4 v[136:139], v250, s[68:69] offset:2048 sc1\n\t"
      "v_mfma_f32_16x16x32_bf16 v[224:227], v[16:19], v[164:167], v[224:227]\n\t"
      "s_nop 3\n\t"
      "ds_write_b128 v253, v[220:223] offset:3072\n\t"
      "v_mfma_f32_16x16x32_bf16 v[224:227], v[32:35], v[168:171], v[224:227]\n\t"
      "v_mfma_f32_16x16x32_bf16 v[224:227], v[48:51], v[172:175], v[224:227]\n\t"
      "s_waitcnt vmcnt(7)\n\t"
      "v_mfma_f32_16x16x32_bf16 v[224:227], v[64:67], v[176:179], v[224:227]\n\t"
      "global_load_dwordx4 v[140:143], v250, s[68:69] offset:3072 sc1\n\t"
      "s_cmp_lg_u32 s24, 3\n\t"
      "s_cbranch_scc1 LF6B_%=\n\t"
      "v_cmp_ne_u64_e32 vcc, s[22:23], v[228:229]\n\t"
      "s_cbranch_vccnz LF6R_%=\n\t"
      "LF6B_%=:\n\t"
      "s_waitcnt lgkmcnt(0)\n\t"
      "s_barrier\n\t"
      "s_cmp_lg_u32 s24, 3\n\t"
      "s_cbranch_scc1 HF3_%=\n\t"
      "s_lshl3_add_u32 s34, s25, s26\n\t"
      "s_lshl_b32 s34, s34, 7\n\t"
      "s_add_u32 s34, s34, 98304\n\t"
      "v_mov_b32_e32 v238, s34\n\t"
      "v_mov_b32_e32 v236, s22\n\t"
      "v_mov_b32_e32 v237, s23\n\t"
      "s_mov_b64 exec, 1\n\t"
      "global_store_dwordx2 v238, v[236:237], s[60:61] sc1\n\t"
      "s_mov_b64 exec, -1\n\t"
      "HF3_%=:\n\t"
      "ds_read_b64 v[228:229], v246\n\t"
      "ds_read_b64 v[230:231], v246 offset:4096\n\t"
      "s_setprio 1\n\t"
      "v_mfma_f32_16x16x32_bf16 v[224:227], v[80:83], v[180:183], v[224:227]\n\t"
      "ds_read_b64 v[232:233], v246 offset:8192\n\t"
      "ds_read_b64 v[234:235], v246 offset:12288\n\t"
      "v_mfma_f32_16x16x32_bf16 v[224:227], v[96:99], v[184:187], v[224:227]\n\t"
      "ds_read_b64 v[236:237], v246 offset:16384\n\t"
      "ds_read_b64 v[238:239], v246 offset:20480\n\t"
      "v_mfma_f32_16x16x32_bf16 v[224:227], v[112:115], v[188:191], v[224:227]\n\t"
      "ds_read_b64 v[240:241], v246 offset:24576\n\t"
      "ds_read_b64 v[242:243], v246 offset:28672\n\t"
      "v_mfma_f32_16x16x32_bf16 v[208:211], v[4:7], v[160:163], 0\n\t"
      "global_load_dwordx4 v[144:147], v250, s[70:71] offset:-4096 sc1\n\t"
      "v_mfma_f32_16x16x32_bf16 v[208:211], v[20:23], v[164:167], v[208:211]\n\t"
      "s_waitcnt lgkmcnt(4)\n\t"
      "v_add_f32_e32 v228, v228, v230\n\t"
      "v_add_f32_e32 v232, v232, v234\n\t"
      "v_mfma_f32_16x16x32_bf16 v[208:211], v[36:39], v[168:171], v[208:211]\n\t"
      "v_add_f32_e32 v229, v229, v231\n\t"
      "v_add_f32_e32 v233, v233, v235\n\t"
      "v_add_f32_e32 v228, v228, v232\n\t"
      "v_cmp_u_f32_e32 vcc, v224, v224\n\t"
      "s_cbranch_vccnz LA31R_%=\n\t"
      "LA31B_%=:\n\t"
      "v_mfma_f32_16x16x32_bf16 v[208:211], v[52:55], v[172:175], v[208:211]\n\t"
      "v_add_f32_e32 v229, v229, v233\n\t"
      "s_waitcnt lgkmcnt(0)\n\t"
      "v_add_f32_e32 v236, v236, v238\n\t"
      "v_mfma_f32_16x16x32_bf16 v[208:211], v[68:71], v[176:179], v[208:211]\n\t"
      "v_add_f32_e32 v240, v240, v242\n\t"
      "v_add_f32_e32 v237, v237, v239\n\t"
      "v_add_f32_e32 v241, v241, v243\n\t"
      "global_load_dwordx4 v[148:151], v250, s[70:71] offset:-3072 sc1\n\t"
      "v_mfma_f32_16x16x32_bf16 v[208:211], v[84:87], v[180:183], v[208:211]\n\t"
      "v_add_f32_e32 v228, v228, v248\n\t"
      "v_add_f32_e32 v236, v236, v240\n\t"
      "ds_write_b128 v245, v[224:227] offset:32768\n\t"
      "v_mfma_f32_16x16x32_bf16 v[208:211], v[100:103], v[184:187], v[208:211]\n\t"
      "v_add_f32_e32 v229, v229, v249\n\t"
      "v_add_f32_e32 v237, v237, v241\n\t"
      "v_add_f32_e32 v228, v228, v236\n\t"
      "v_mfma_f32_16x16x32_bf16 v[208:211], v[116:119], v[188:191], v[208:211]\n\t"
      "v_add_f32_e32 v229, v229, v237\n\t"
      "v_mul_f32_e32 v230, v228, v228\n\t"
      "v_mul_f32_e32 v231, v229, v229\n\t"
      "v_mfma_f32_16x16x32_bf16 v[212:215], v[8:11], v[160:163], 0\n\t"
      "v_fmaak_f32 v230, v252, v230, 0xc0135761\n\t"
      "v_fmaak_f32 v231, v252, v231, 0xc0135761\n\t"
      "v_mul_f32_e32 v230, v228, v230\n\t"
      "global_load_dwordx4 v[152:155], v250, s[70:71] offset:-2048 sc1\n\t"
      "v_mfma_f32_16x16x32_bf16 v[212:215], v[24:27], v[164:167], v[212:215]\n\t"
      "v_mul_f32_e32 v231, v229, v231\n\t"
      "v_exp_f32_e32 v230, v230\n\t"
      "v_exp_f32_e32 v231, v231\n\t"
      "v_mfma_f32_16x16x32_bf16 v[212:215], v[40:43], v[168:171], v[212:215]\n\t"
      "v_add_f32_e32 v230, 1.0, v230\n\t"
      "v_add_f32_e32 v231, 1.0, v231\n\t"
      "v_mfma_f32_16x16x32_bf16 v[212:215], v[56:59], v[172:175], v[212:215]\n\t"
      "v_rcp_f32_e32 v230, v230\n\t"
      "v_rcp_f32_e32 v231, v231\n\t"
      "v_mul_f32_e32 v228, v228, v230\n\t"
      "v_mfma_f32_16x16x32_bf16 v[212:215], v[72:75], v[176:179], v[212:215]\n\t"
      "v_mul_f32_e32 v229, v229, v231\n\t"
      "v_cvt_pk_bf16_f32 v230, v228, v229\n\t"
      "v_min_u32_e32 v254, -2, v230\n\t"
      "global_load_dwordx4 v[156:159], v250, s[70:71] offset:-1024 sc1\n\t"
      "v_mfma_f32_16x16x32_bf16 v[212:215], v[88:91], v[180:183], v[212:215]\n\t"
      "global_store_dword v247, v254, s[62:63] sc1\n\t"
      "s_add_u32 s62, s62, 57344\n\t"
      "s_addc_u32 s63, s63, 0\n\t"
      "s_setprio 0\n\t"
      "ds_write_b128 v253, v[208:211] offset:33792\n\t"
      "v_mfma_f32_16x16x32_bf16 v[212:215], v[104:107], v[184:187], v[212:215]\n\t"
      "v_mfma_f32_16x16x32_bf16 v[212:215], v[120:123], v[188:191], v[212:215]\n\t"
      "v_mfma_f32_16x16x32_bf16 v[216:219], v[12:15], v[160:163], 0\n\t"
      "global_load_dwordx4 v[160:163], v250, s[70:71] sc1\n\t"
      "v_mfma_f32_16x16x32_bf16 v[216:219], v[28:31], v[164:167], v[216:219]\n\t"
      "v_mfma_f32_16x16x32_bf16 v[216:219], v[44:47], v[168:171], v[216:219]\n\t"
      "v_mfma_f32_16x16x32_bf16 v[216:219], v[60:63], v[172:175], v[216:219]\n\t"
      "v_mfma_f32_16x16x32_bf16 v[216:219], v[76:79], v[176:179], v[216:219]\n\t"
      "global_load_dwordx4 v[164:167], v250, s[70:71] offset:1024 sc1\n\t"
      "v_mfma_f32_16x16x32_bf16 v[216:219], v[92:95], v[180:183], v[216:219]\n\t"
      "ds_write_b128 v245, v[212:215] offset:34816\n\t"
      "v_mfma_f32_16x16x32_bf16 v[216:219], v[108:111], v[184:187], v[216:219]\n\t"
      "v_mfma_f32_16x16x32_bf16 v[216:219], v[124:127], v[188:191], v[216:219]\n\t"
      "s_waitcnt vmcnt(11)\n\t"
      "v_mfma_f32_16x16x32_bf16 v[220:223], v[0:3], v[192:195], 0\n\t"
      "global_load_dwordx4 v[168:171], v250, s[70:71] offset:2048 sc1\n\t"
      "v_mfma_f32_16x16x32_bf16 v[220:223], v[16:19], v[196:199], v[220:223]\n\t"
      "s_nop 3\n\t"
      "ds_write_b128 v253, v[216:219] offset:35840\n\t"
      "v_mfma_f32_16x16x32_bf16 v[220:223], v[32:35], v[200:203], v[220:223]\n\t"
      "v_mfma_f32_16x16x32_bf16 v[220:223], v[48:51], v[204:207], v[220:223]\n\t"
      "s_waitcnt vmcnt(8)\n\t"
      "v_mfma_f32_16x16x32_bf16 v[220:223], v[64:67], v[128:131], v[220:223]\n\t"
      "global_load_dwordx4 v[172:175], v250, s[70:71] offset:3072 sc1\n\t"
      "s_waitcnt lgkmcnt(0)\n\t"
      "s_barrier\n\t"
      "ds_read_b64 v[228:229], v246 offset:32768\n\t"
      "ds_read_b64 v[230:231], v246 offset:36864\n\t"
      "s_setprio 1\n\t"
      "v_mfma_f32_16x16x32_bf16 v[220:223], v[80:83], v[132:135], v[220:223]\n\t"
      "ds_read_b64 v[232:233], v246 offset:40960\n\t"
      "ds_read_b64 v[234:235], v246 offset:45056\n\t"
      "v_mfma_f32_16x16x32_bf16 v[220:223], v[96:99], v[136:139], v[220:223]\n\t"
      "ds_read_b64 v[236:237], v246 offset:49152\n\t"
      "ds_read_b64 v[238:239], v246 offset:53248\n\t"
      "v_mfma_f32_16x16x32_bf16 v[220:223], v[112:115], v[140:143], v[220:223]\n\t"
      "ds_read_b64 v[240:241], v246 offset:57344\n\t"
      "ds_read_b64 v[242:243], v246 offset:61440\n\t"
      "v_mfma_f32_16x16x32_bf16 v[224:227], v[4:7], v[192:195], 0\n\t"
      "global_load_dwordx4 v[176:179], v250, s[72:73] offset:-4096 sc1\n\t"
      "v_mfma_f32_16x16x32_bf16 v[224:227], v[20:23], v[196:199], v[224:227]\n\t"
      "s_waitcnt lgkmcnt(4)\n\t"
      "v_add_f32_e32 v228, v228, v230\n\t"
      "v_add_f32_e32 v232, v232, v234\n\t"
      "v_mfma_f32_16x16x32_bf16 v[224:227], v[36:39], v[200:203], v[224:227]\n\t"
      "v_add_f32_e32 v229, v229, v231\n\t"
      "v_add_f32_e32 v233, v233, v235\n\t"
      "v_add_f32_e32 v228, v228, v232\n\t"
      "v_cmp_u_f32_e32 vcc, v220, v220\n\t"
      "s_cbranch_vccnz LA32R_%=\n\t"
      "LA32B_%=:\n\t"
      "v_mfma_f32_16x16x32_bf16 v[224:227], v[52:55], v[204:207], v[224:227]\n\t"
      "v_add_f32_e32 v229, v229, v233\n\t"
      "s_waitcnt lgkmcnt(0)\n\t"
      "v_add_f32_e32 v236, v236, v238\n\t"
      "v_mfma_f32_16x16x32_bf16 v[224:227], v[68:71], v[128:131], v[224:227]\n\t"
      "v_add_f32_e32 v240, v240, v242\n\t"
      "v_add_f32_e32 v237, v237, v239\n\t"
      "v_add_f32_e32 v241, v241, v243\n\t"
      "global_load_dwordx4 v[180:183], v250, s[72:73] offset:-3072 sc1\n\t"
      "v_mfma_f32_16x16x32_bf16 v[224:227], v[84:87], v[132:135], v[224:227]\n\t"
      "v_add_f32_e32 v228, v228, v248\n\t"
      "v_add_f32_e32 v236, v236, v240\n\t"
      "ds_write_b128 v245, v[220:223]\n\t"
      "v_mfma_f32_16x16x32_bf16 v[224:227], v[100:103], v[136:139], v[224:227]\n\t"
      "v_add_f32_e32 v229, v229, v249\n\t"
      "v_add_f32_e32 v237, v237, v241\n\t"
      "v_add_f32_e32 v228, v228, v236\n\t"
      "v_mfma_f32_16x16x32_bf16 v[224:227], v[116:119], v[140:143], v[224:227]\n\t"
      "v_add_f32_e32 v229, v229, v237\n\t"
      "v_mul_f32_e32 v230, v228, v228\n\t"
      "v_mul_f32_e32 v231, v229, v229\n\t"
      "v_mfma_f32_16x16x32_bf16 v[208:211], v[8:11], v[192:195], 0\n\t"
      "v_fmaak_f32 v230, v252, v230, 0xc0135761\n\t"
      "v_fmaak_f32 v231, v252, v231, 0xc0135761\n\t"
      "v_mul_f32_e32 v230, v228, v230\n\t"
      "global_load_dwordx4 v[184:187], v250, s[72:73] offset:-2048 sc1\n\t"
      "v_mfma_f32_16x16x32_bf16 v[208:211], v[24:27], v[196:199], v[208:211]\n\t"
      "v_mul_f32_e32 v231, v229, v231\n\t"
      "v_exp_f32_e32 v230, v230\n\t"
      "v_exp_f32_e32 v231, v231\n\t"
      "v_mfma_f32_16x16x32_bf16 v[208:211], v[40:43], v[200:203], v[208:211]\n\t"
      "v_add_f32_e32 v230, 1.0, v230\n\t"
      "v_add_f32_e32 v231, 1.0, v231\n\t"
      "v_mfma_f32_16x16x32_bf16 v[208:211], v[56:59], v[204:207], v[208:211]\n\t"
      "v_rcp_f32_e32 v230, v230\n\t"
      "v_rcp_f32_e32 v231, v231\n\t"
      "v_mul_f32_e32 v228, v228, v230\n\t"
      "v_mfma_f32_16x16x32_bf16 v[208:211], v[72:75], v[128:131], v[208:211]\n\t"
      "v_mul_f32_e32 v229, v229, v231\n\t"
      "v_cvt_pk_bf16_f32 v230, v228, v229\n\t"
      "v_min_u32_e32 v254, -2, v230\n\t"
      "global_load_dwordx4 v[188:191], v250, s[72:73] offset:-1024 sc1\n\t"
      "v_mfma_f32_16x16x32_bf16 v[208:211], v[88:91], v[132:135], v[208:211]\n\t"
      "global_store_dword v247, v254, s[62:63] sc1\n\t"
      "s_add_u32 s62, s62, 57344\n\t"
      "s_addc_u32 s63, s63, 0\n\t"
      "s_setprio 0\n\t"
      "ds_write_b128 v253, v[224:227] offset:1024\n\t"
      "v_mfma_f32_16x16x32_bf16 v[208:211], v[104:107], v[136:139], v[208:211]\n\t"
      "v_mfma_f32_16x16x32_bf16 v[208:211], v[120:123], v[140:143], v[208:211]\n\t"
      "v_mfma_f32_16x16x32_bf16 v[212:215], v[12:15], v[192:195], 0\n\t"
      "global_load_dwordx4 v[192:195], v250, s[72:73] sc1\n\t"
      "v_mfma_f32_16x16x32_bf16 v[212:215], v[28:31], v[196:199], v[212:215]\n\t"
      "v_mfma_f32_16x16x32_bf16 v[212:215], v[44:47], v[200:203], v[212:215]\n\t"
      "v_mfma_f32_16x16x32_bf16 v[212:215], v[60:63], v[204:207], v[212:215]\n\t"
      "v_mfma_f32_16x16x32_bf16 v[212:215], v[76:79], v[128:131], v[212:215]\n\t"
      "global_load_dwordx4 v[196:199], v250, s[72:73] offset:1024 sc1\n\t"
      "v_mfma_f32_16x16x32_bf16 v[212:215], v[92:95], v[132:135], v[212:215]\n\t"
      "ds_write_b128 v245, v[208:211] offset:2048\n\t"
      "v_mfma_f32_16x16x32_bf16 v[212:215], v[108:111], v[136:139], v[212:215]\n\t"
      "v_mfma_f32_16x16x32_bf16 v[212:215], v[124:127], v[140:143], v[212:215]\n\t"
      "s_waitcnt vmcnt(12)\n\t"
      "v_mfma_f32_16x16x32_bf16 v[216:219], v[0:3], v[144:147], 0\n\t"
      "global_load_dwordx4 v[200:203], v250, s[72:73] offset:2048 sc1\n\t"
      "v_mfma_f32_16x16x32_bf16 v[216:219], v[16:19], v[148:151], v[216:219]\n\t"
      "s_nop 3\n\t"
      "ds_write_b128 v253, v[212:215] offset:3072\n\t"
      "v_mfma_f32_16x16x32_bf16 v[216:219], v[32:35], v[152:155], v[216:219]\n\t"
      "v_mfma_f32_16x16x32_bf16 v[216:219], v[48:51], v[156:159], v[216:219]\n\t"
      "s_waitcnt vmcnt(8)\n\t"
      "v_mfma_f32_16x16x32_bf16 v[216:219], v[64:67], v[160:163], v[216:219]\n\t"
      "global_load_dwordx4 v[204:207], v250, s[72:73] offset:3072 sc1\n\t"
      "s_waitcnt lgkmcnt(0)\n\t"
      "s_barrier\n\t"
      "ds_read_b64 v[228:229], v246\n\t"
      "ds_read_b64 v[230:231], v246 offset:4096\n\t"
      "s_setprio 1\n\t"
      "v_mfma_f32_16x16x32_bf16 v[216:219], v[80:83], v[164:167], v[216:219]\n\t"
      "ds_read_b64 v[232:233], v246 offset:8192\n\t"
      "ds_read_b64 v[234:235], v246 offset:12288\n\t"
      "v_mfma_f32_16x16x32_bf16 v[216:219], v[96:99], v[168:171], v[216:219]\n\t"
      "ds_read_b64 v[236:237], v246 offset:16384\n\t"
      "ds_read_b64 v[238:239], v246 offset:20480\n\t"
      "v_mfma_f32_16x16x32_bf16 v[216:219], v[112:115], v[172:175], v[216:219]\n\t"
      "ds_read_b64 v[240:241], v246 offset:24576\n\t"
      "ds_read_b64 v[242:243], v246 offset:28672\n\t"
      "v_mfma_f32_16x16x32_bf16 v[220:223], v[4:7], v[144:147], 0\n\t"
      "global_load_dwordx4 v[128:131], v250, s[74:75] offset:-4096 sc1\n\t"
      "v_mfma_f32_16x16x32_bf16 v[220:223], v[20:23], v[148:151], v[220:223]\n\t"
      "s_waitcnt lgkmcnt(4)\n\t"
      "v_add_f32_e32 v228, v228, v230\n\t"
      "v_add_f32_e32 v232, v232, v234\n\t"
      "v_mfma_f32_16x16x32_bf16 v[220:223], v[36:39], v[152:155], v[220:223]\n\t"
      "v_add_f32_e32 v229, v229, v231\n\t"
      "v_add_f32_e32 v233, v233, v235\n\t"
      "v_add_f32_e32 v228, v228, v232\n\t"
      "v_cmp_u_f32_e32 vcc, v216, v216\n\t"
      "s_cbranch_vccnz LA33R_%=\n\t"
      "LA33B_%=:\n\t"
      "v_mfma_f32_16x16x32_bf16 v[220:223], v[52:55], v[156:159], v[220:223]\n\t"
      "v_add_f32_e32 v229, v229, v233\n\t"
      "s_waitcnt lgkmcnt(0)\n\t"
      "v_add_f32_e32 v236, v236, v238\n\t"
      "v_mfma_f32_16x16x32_bf16 v[220:223], v[68:71], v[160:163], v[220:223]\n\t"
      "v_add_f32_e32 v240, v240, v242\n\t"
      "v_add_f32_e32 v237, v237, v239\n\t"
      "v_add_f32_e32 v241, v241, v243\n\t"
      "global_load_dwordx4 v[132:135], v250, s[74:75] offset:-3072 sc1\n\t"
      "v_mfma_f32_16x16x32_bf16 v[220:223], v[84:87], v[164:167], v[220:223]\n\t"
      "v_add_f32_e32 v228, v228, v248\n\t"
      "v_add_f32_e32 v236, v236, v240\n\t"
      "ds_write_b128 v245, v[216:219] offset:32768\n\t"
      "v_mfma_f32_16x16x32_bf16 v[220:223], v[100:103], v[168:171], v[220:223]\n\t"
      "v_add_f32_e32 v229, v229, v249\n\t"
      "v_add_f32_e32 v237, v237, v241\n\t"
      "v_add_f32_e32 v228, v228, v236\n\t"
      "v_mfma_f32_16x16x32_bf16 v[220:223], v[116:119], v[172:175], v[220:223]\n\t"
      "v_add_f32_e32 v229, v229, v237\n\t"
      "v_mul_f32_e32 v230, v228, v228\n\t"
      "v_mul_f32_e32 v231, v229, v229\n\t"
      "v_mfma_f32_16x16x32_bf16 v[224:227], v[8:11], v[144:147], 0\n\t"
      "v_fmaak_f32 v230, v252, v230, 0xc0135761\n\t"
      "v_fmaak_f32 v231, v252, v231, 0xc0135761\n\t"
      "v_mul_f32_e32 v230, v228, v230\n\t"
      "global_load_dwordx4 v[136:139], v250, s[74:75] offset:-2048 sc1\n\t"
      "v_mfma_f32_16x16x32_bf16 v[224:227], v[24:27], v[148:151], v[224:227]\n\t"
      "v_mul_f32_e32 v231, v229, v231\n\t"
      "v_exp_f32_e32 v230, v230\n\t"
      "v_exp_f32_e32 v231, v231\n\t"
      "v_mfma_f32_16x16x32_bf16 v[224:227], v[40:43], v[152:155], v[224:227]\n\t"
      "v_add_f32_e32 v230, 1.0, v230\n\t"
      "v_add_f32_e32 v231, 1.0, v231\n\t"
      "v_mfma_f32_16x16x32_bf16 v[224:227], v[56:59], v[156:159], v[224:227]\n\t"
      "v_rcp_f32_e32 v230, v230\n\t"
      "v_rcp_f32_e32 v231, v231\n\t"
      "v_mul_f32_e32 v228, v228, v230\n\t"
      "v_mfma_f32_16x16x32_bf16 v[224:227], v[72:75], v[160:163], v[224:227]\n\t"
      "v_mul_f32_e32 v229, v229, v231\n\t"
      "v_cvt_pk_bf16_f32 v230, v228, v229\n\t"
      "v_min_u32_e32 v254, -2, v230\n\t"
      "global_load_dwordx4 v[140:143], v250, s[74:75] offset:-1024 sc1\n\t"
      "v_mfma_f32_16x16x32_bf16 v[224:227], v[88:91], v[164:167], v[224:227]\n\t"
      "global_store_dword v247, v254, s[62:63] sc1\n\t"
      "s_add_u32 s62, s62, 57344\n\t"
      "s_addc_u32 s63, s63, 0\n\t"
      "s_setprio 0\n\t"
      "ds_write_b128 v253, v[220:223] offset:33792\n\t"
      "v_mfma_f32_16x16x32_bf16 v[224:227], v[104:107], v[168:171], v[224:227]\n\t"
      "v_mfma_f32_16x16x32_bf16 v[224:227], v[120:123], v[172:175], v[224:227]\n\t"
      "v_mfma_f32_16x16x32_bf16 v[208:211], v[12:15], v[144:147], 0\n\t"
      "global_load_dwordx4 v[144:147], v250, s[74:75] sc1\n\t"
      "v_mfma_f32_16x16x32_bf16 v[208:211], v[28:31], v[148:151], v[208:211]\n\t"
      "v_mfma_f32_16x16x32_bf16 v[208:211], v[44:47], v[152:155], v[208:211]\n\t"
      "v_mfma_f32_16x16x32_bf16 v[208:211], v[60:63], v[156:159], v[208:211]\n\t"
      "v_mfma_f32_16x16x32_bf16 v[208:211], v[76:79], v[160:163], v[208:211]\n\t"
      "global_load_dwordx4 v[148:151], v250, s[74:75] offset:1024 sc1\n\t"
      "v_mfma_f32_16x16x32_bf16 v[208:211], v[92:95], v[164:167], v[208:211]\n\t"
      "ds_write_b128 v245, v[224:227] offset:34816\n\t"
      "v_mfma_f32_16x16x32_bf16 v[208:211], v[108:111], v[168:171], v[208:211]\n\t"
      "v_mfma_f32_16x16x32_bf16 v[208:211], v[124:127], v[172:175], v[208:211]\n\t"
      "s_waitcnt vmcnt(12)\n\t"
      "v_mfma_f32_16x16x32_bf16 v[212:215], v[0:3], v[176:179], 0\n\t"
      "global_load_dwordx4 v[152:155], v250, s[74:75] offset:2048 sc1\n\t"
      "v_mfma_f32_16x16x32_bf16 v[212:215], v[16:19], v[180:183], v[212:215]\n\t"
      "s_nop 3\n\t"
      "ds_write_b128 v253, v[208:211] offset:35840\n\t"
      "v_mfma_f32_16x16x32_bf16 v[212:215], v[32:35], v[184:187], v[212:215]\n\t"
      "v_mfma_f32_16x16x32_bf16 v[212:215], v[48:51], v[188:191], v[212:215]\n\t"
      "s_waitcnt vmcnt(8)\n\t"
      "v_mfma_f32_16x16x32_bf16 v[212:215], v[64:67], v[192:195], v[212:215]\n\t"
      "global_load_dwordx4 v[156:159], v250, s[74:75] offset:3072 sc1\n\t"
      "s_waitcnt lgkmcnt(0)\n\t"
      "s_barrier\n\t"
      "ds_read_b64 v[228:229], v246 offset:32768\n\t"
      "ds_read_b64 v[230:231], v246 offset:36864\n\t"
      "s_setprio 1\n\t"
      "v_mfma_f32_16x16x32_bf16 v[212:215], v[80:83], v[196:199], v[212:215]\n\t"
      "ds_read_b64 v[232:233], v246 offset:40960\n\t"
      "ds_read_b64 v[234:235], v246 offset:45056\n\t"
      "v_mfma_f32_16x16x32_bf16 v[212:215], v[96:99], v[200:203], v[212:215]\n\t"
      "ds_read_b64 v[236:237], v246 offset:49152\n\t"
      "ds_read_b64 v[238:239], v246 offset:53248\n\t"
      "v_mfma_f32_16x16x32_bf16 v[212:215], v[112:115], v[204:207], v[212:215]\n\t"
      "ds_read_b64 v[240:241], v246 offset:57344\n\t"
      "ds_read_b64 v[242:243], v246 offset:61440\n\t"
      "v_mfma_f32_16x16x32_bf16 v[216:219], v[4:7], v[176:179], 0\n\t"
      "global_load_dwordx4 v[160:163], v250, s[76:77] offset:-4096 sc1\n\t"
      "v_mfma_f32_16x16x32_bf16 v[216:219], v[20:23], v[180:183], v[216:219]\n\t"
      "s_waitcnt lgkmcnt(4)\n\t"
      "v_add_f32_e32 v228, v228, v230\n\t"
      "v_add_f32_e32 v232, v232, v234\n\t"
      "v_mfma_f32_16x16x32_bf16 v[216:219], v[36:39], v[184:187], v[216:219]\n\t"
      "v_add_f32_e32 v229, v229, v231\n\t"
      "v_add_f32_e32 v233, v233, v235\n\t"
      "v_add_f32_e32 v228, v228, v232\n\t"
      "v_cmp_u_f32_e32 vcc, v212, v212\n\t"
      "s_cbranch_vccnz LA34R_%=\n\t"
      "LA34B_%=:\n\t"
      "v_mfma_f32_16x16x32_bf16 v[216:219], v[52:55], v[188:191], v[216:219]\n\t"
      "v_add_f32_e32 v229, v229, v233\n\t"
      "s_waitcnt lgkmcnt(0)\n\t"
      "v_add_f32_e32 v236, v236, v238\n\t"
      "v_mfma_f32_16x16x32_bf16 v[216:219], v[68:71], v[192:195], v[216:219]\n\t"
      "v_add_f32_e32 v240, v240, v242\n\t"
      "v_add_f32_e32 v237, v237, v239\n\t"
      "v_add_f32_e32 v241, v241, v243\n\t"
      "global_load_dwordx4 v[164:167], v250, s[76:77] offset:-3072 sc1\n\t"
      "v_mfma_f32_16x16x32_bf16 v[216:219], v[84:87], v[196:199], v[216:219]\n\t"
      "v_add_f32_e32 v228, v228, v248\n\t"
      "v_add_f32_e32 v236, v236, v240\n\t"
      "ds_write_b128 v245, v[212:215]\n\t"
      "v_mfma_f32_16x16x32_bf16 v[216:219], v[100:103], v[200:203], v[216:219]\n\t"
      "v_add_f32_e32 v229, v229, v249\n\t"
      "v_add_f32_e32 v237, v237, v241\n\t"
      "v_add_f32_e32 v228, v228, v236\n\t"
      "v_mfma_f32_16x16x32_bf16 v[216:219], v[116:119], v[204:207], v[216:219]\n\t"
      "v_add_f32_e32 v229, v229, v237\n\t"
      "v_mul_f32_e32 v230, v228, v228\n\t"
      "v_mul_f32_e32 v231, v229, v229\n\t"
      "v_mfma_f32_16x16x32_bf16 v[220:223], v[8:11], v[176:179], 0\n\t"
      "v_fmaak_f32 v230, v252, v230, 0xc0135761\n\t"
      "v_fmaak_f32 v231, v252, v231, 0xc0135761\n\t"
      "v_mul_f32_e32 v230, v228, v230\n\t"
      "global_load_dwordx4 v[168:171], v250, s[76:77] offset:-2048 sc1\n\t"
      "v_mfma_f32_16x16x32_bf16 v[220:223], v[24:27], v[180:183], v[220:223]\n\t"
      "v_mul_f32_e32 v231, v229, v231\n\t"
      "v_exp_f32_e32 v230, v230\n\t"
      "v_exp_f32_e32 v231, v231\n\t"
      "v_mfma_f32_16x16x32_bf16 v[220:223], v[40:43], v[184:187], v[220:223]\n\t"
      "v_add_f32_e32 v230, 1.0, v230\n\t"
      "v_add_f32_e32 v231, 1.0, v231\n\t"
      "v_mfma_f32_16x16x32_bf16 v[220:223], v[56:59], v[188:191], v[220:223]\n\t"
      "v_rcp_f32_e32 v230, v230\n\t"
      "v_rcp_f32_e32 v231, v231\n\t"
      "v_mul_f32_e32 v228, v228, v230\n\t"
      "v_mfma_f32_16x16x32_bf16 v[220:223], v[72:75], v[192:195], v[220:223]\n\t"
      "v_mul_f32_e32 v229, v229, v231\n\t"
      "v_cvt_pk_bf16_f32 v230, v228, v229\n\t"
      "v_min_u32_e32 v254, -2, v230\n\t"
      "global_load_dwordx4 v[172:175], v250, s[76:77] offset:-1024 sc1\n\t"
      "v_mfma_f32_16x16x32_bf16 v[220:223], v[88:91], v[196:199], v[220:223]\n\t"
      "global_store_dword v247, v254, s[62:63] sc1\n\t"
      "s_add_u32 s62, s62, 57344\n\t"
      "s_addc_u32 s63, s63, 0\n\t"
      "s_setprio 0\n\t"
      "ds_write_b128 v253, v[216:219] offset:1024\n\t"
      "v_mfma_f32_16x16x32_bf16 v[220:223], v[104:107], v[200:203], v[220:223]\n\t"
      "v_mfma_f32_16x16x32_bf16 v[220:223], v[120:123], v[204:207], v[220:223]\n\t"
      "v_mfma_f32_16x16x32_bf16 v[224:227], v[12:15], v[176:179], 0\n\t"
      "global_load_dwordx4 v[176:179], v250, s[76:77] sc1\n\t"
      "v_mfma_f32_16x16x32_bf16 v[224:227], v[28:31], v[180:183], v[224:227]\n\t"
      "v_mfma_f32_16x16x32_bf16 v[224:227], v[44:47], v[184:187], v[224:227]\n\t"
      "v_mfma_f32_16x16x32_bf16 v[224:227], v[60:63], v[188:191], v[224:227]\n\t"
      "v_mfma_f32_16x16x32_bf16 v[224:227], v[76:79], v[192:195], v[224:227]\n\t"
      "global_load_dwordx4 v[180:183], v250, s[76:77] offset:1024 sc1\n\t"
      "v_mfma_f32_16x16x32_bf16 v[224:227], v[92:95], v[196:199], v[224:227]\n\t"
      "ds_write_b128 v245, v[220:223] offset:2048\n\t"
      "v_mfma_f32_16x16x32_bf16 v[224:227], v[108:111], v[200:203], v[224:227]\n\t"
      "v_mfma_f32_16x16x32_bf16 v[224:227], v[124:127], v[204:207], v[224:227]\n\t"
      "s_waitcnt vmcnt(12)\n\t"
      "v_mfma_f32_16x16x32_bf16 v[208:211], v[0:3], v[128:131], 0\n\t"
      "global_load_dwordx4 v[184:187], v250, s[76:77] offset:2048 sc1\n\t"
      "v_mfma_f32_16x16x32_bf16 v[208:211], v[16:19], v[132:135], v[208:211]\n\t"
      "s_nop 3\n\t"
      "ds_write_b128 v253, v[224:227] offset:3072\n\t"
      "v_mfma_f32_16x16x32_bf16 v[208:211], v[32:35], v[136:139], v[208:211]\n\t"
      "v_mfma_f32_16x16x32_bf16 v[208:211], v[48:51], v[140:143], v[208:211]\n\t"
      "s_waitcnt vmcnt(8)\n\t"
      "v_mfma_f32_16x16x32_bf16 v[208:211], v[64:67], v[144:147], v[208:211]\n\t"
      "global_load_dwordx4 v[188:191], v250, s[76:77] offset:3072 sc1\n\t"
      "s_waitcnt lgkmcnt(0)\n\t"
      "s_barrier\n\t"
      "ds_read_b64 v[228:229], v246\n\t"
      "ds_read_b64 v[230:231], v246 offset:4096\n\t"
      "s_setprio 1\n\t"
      "v_mfma_f32_16x16x32_bf16 v[208:211], v[80:83], v[148:151], v[208:211]\n\t"
      "ds_read_b64 v[232:233], v246 offset:8192\n\t"
      "ds_read_b64 v[234:235], v246 offset:12288\n\t"
      "v_mfma_f32_16x16x32_bf16 v[208:211], v[96:99], v[152:155], v[208:211]\n\t"
      "ds_read_b64 v[236:237], v246 offset:16384\n\t"
      "ds_read_b64 v[238:239], v246 offset:20480\n\t"
      "v_mfma_f32_16x16x32_bf16 v[208:211], v[112:115], v[156:159], v[208:211]\n\t"
      "ds_read_b64 v[240:241], v246 offset:24576\n\t"
      "ds_read_b64 v[242:243], v246 offset:28672\n\t"
      "v_mfma_f32_16x16x32_bf16 v[212:215], v[4:7], v[128:131], 0\n\t"
      "v_mfma_f32_16x16x32_bf16 v[212:215], v[20:23], v[132:135], v[212:215]\n\t"
      "s_waitcnt lgkmcnt(4)\n\t"
      "v_add_f32_e32 v228, v228, v230\n\t"
      "v_add_f32_e32 v232, v232, v234\n\t"
      "v_mfma_f32_16x16x32_bf16 v[212:215], v[36:39], v[136:139], v[212:215]\n\t"
      "v_add_f32_e32 v229, v229, v231\n\t"
      "v_add_f32_e32 v233, v233, v235\n\t"
      "v_add_f32_e32 v228, v228, v232\n\t"
      "v_cmp_u_f32_e32 vcc, v208, v208\n\t"
      "s_cbranch_vccnz LA35R_%=\n\t"
      "LA35B_%=:\n\t"
      "v_mfma_f32_16x16x32_bf16 v[212:215], v[52:55], v[140:143], v[212:215]\n\t"
      "v_add_f32_e32 v229, v229, v233\n\t"
      "s_waitcnt lgkmcnt(0)\n\t"
      "v_add_f32_e32 v236, v236, v238\n\t"
      "v_mfma_f32_16x16x32_bf16 v[212:215], v[68:71], v[144:147], v[212:215]\n\t"
      "v_add_f32_e32 v240, v240, v242\n\t"
      "v_add_f32_e32 v237, v237, v239\n\t"
      "v_add_f32_e32 v241, v241, v243\n\t"
      "v_mfma_f32_16x16x32_bf16 v[212:215], v[84:87], v[148:151], v[212:215]\n\t"
      "v_add_f32_e32 v228, v228, v248\n\t"
      "v_add_f32_e32 v236, v236, v240\n\t"
      "ds_write_b128 v245, v[208:211] offset:32768\n\t"
      "v_mfma_f32_16x16x32_bf16 v[212:215], v[100:103], v[152:155], v[212:215]\n\t"
      "v_add_f32_e32 v229, v229, v249\n\t"
      "v_add_f32_e32 v237, v237, v241\n\t"
      "v_add_f32_e32 v228, v228, v236\n\t"
      "v_mfma_f32_16x16x32_bf16 v[212:215], v[116:119], v[156:159], v[212:215]\n\t"
      "v_add_f32_e32 v229, v229, v237\n\t"
      "v_mul_f32_e32 v230, v228, v228\n\t"
      "v_mul_f32_e32 v231, v229, v229\n\t"
      "v_mfma_f32_16x16x32_bf16 v[216:219], v[8:11], v[128:131], 0\n\t"
      "v_fmaak_f32 v230, v252, v230, 0xc0135761\n\t"
      "v_fmaak_f32 v231, v252, v231, 0xc0135761\n\t"
      "v_mul_f32_e32 v230, v228, v230\n\t"
      "v_mfma_f32_16x16x32_bf16 v[216:219], v[24:27], v[132:135], v[216:219]\n\t"
      "v_mul_f32_e32 v231, v229, v231\n\t"
      "v_exp_f32_e32 v230, v230\n\t"
      "v_exp_f32_e32 v231, v231\n\t"
      "v_mfma_f32_16x16x32_bf16 v[216:219], v[40:43], v[136:139], v[216:219]\n\t"
      "v_add_f32_e32 v230, 1.0, v230\n\t"
      "v_add_f32_e32 v231, 1.0, v231\n\t"
      "v_mfma_f32_16x16x32_bf16 v[216:219], v[56:59], v[140:143], v[216:219]\n\t"
      "v_rcp_f32_e32 v230, v230\n\t"
      "v_rcp_f32_e32 v231, v231\n\t"
      "v_mul_f32_e32 v228, v228, v230\n\t"
      "v_mfma_f32_16x16x32_bf16 v[216:219], v[72:75], v[144:147], v[216:219]\n\t"
      "v_mul_f32_e32 v229, v229, v231\n\t"
      "v_cvt_pk_bf16_f32 v230, v228, v229\n\t"
      "v_min_u32_e32 v254, -2, v230\n\t"
      "v_mfma_f32_16x16x32_bf16 v[216:219], v[88:91], v[148:151], v[216:219]\n\t"
      "global_store_dword v247, v254, s[62:63] sc1\n\t"
      "s_add_u32 s62, s62, 57344\n\t"
      "s_addc_u32 s63, s63, 0\n\t"
      "s_setprio 0\n\t"
      "ds_write_b128 v253, v[212:215] offset:33792\n\t"
      "v_mfma_f32_16x16x32_bf16 v[216:219], v[104:107], v[152:155], v[216:219]\n\t"
      "v_mfma_f32_16x16x32_bf16 v[216:219], v[120:123], v[156:159], v[216:219]\n\t"
      "v_mfma_f32_16x16x32_bf16 v[220:223], v[12:15], v[128:131], 0\n\t"
      "v_mfma_f32_16x16x32_bf16 v[220:223], v[28:31], v[132:135], v[220:223]\n\t"
      "v_mfma_f32_16x16x32_bf16 v[220:223], v[44:47], v[136:139], v[220:223]\n\t"
      "v_mfma_f32_16x16x32_bf16 v[220:223], v[60:63], v[140:143], v[220:223]\n\t"
      "v_mfma_f32_16x16x32_bf16 v[220:223], v[76:79], v[144:147], v[220:223]\n\t"
      "v_mfma_f32_16x16x32_bf16 v[220:223], v[92:95], v[148:151], v[220:223]\n\t"
      "s_nop 1\n\t"
      "ds_write_b128 v245, v[216:219] offset:34816\n\t"
      "v_mfma_f32_16x16x32_bf16 v[220:223], v[108:111], v[152:155], v[220:223]\n\t"
      "v_mfma_f32_16x16x32_bf16 v[220:223], v[124:127], v[156:159], v[220:223]\n\t"
      "s_waitcnt vmcnt(6)\n\t"
      "v_mfma_f32_16x16x32_bf16 v[224:227], v[0:3], v[160:163], 0\n\t"
      "v_mfma_f32_16x16x32_bf16 v[224:227], v[16:19], v[164:167], v[224:227]\n\t"
      "s_nop 4\n\t"
      "ds_write_b128 v253, v[220:223] offset:35840\n\t"
      "v_mfma_f32_16x16x32_bf16 v[224:227], v[32:35], v[168:171], v[224:227]\n\t"
      "v_mfma_f32_16x16x32_bf16 v[224:227], v[48:51], v[172:175], v[224:227]\n\t"
      "s_waitcnt vmcnt(1)\n\t"
      "v_mfma_f32_16x16x32_bf16 v[224:227], v[64:67], v[176:179], v[224:227]\n\t"
      "s_waitcnt lgkmcnt(0)\n\t"
      "s_barrier\n\t"
      "ds_read_b64 v[228:229], v246 offset:32768\n\t"
      "ds_read_b64 v[230:231], v246 offset:36864\n\t"
      "s_setprio 1\n\t"
      "v_mfma_f32_16x16x32_bf16 v[224:227], v[80:83], v[180:183], v[224:227]\n\t"
      "ds_read_b64 v[232:233], v246 offset:40960\n\t"
      "ds_read_b64 v[234:235], v246 offset:45056\n\t"
      "v_mfma_f32_16x16x32_bf16 v[224:227], v[96:99], v[184:187], v[224:227]\n\t"
      "ds_read_b64 v[236:237], v246 offset:49152\n\t"
      "ds_read_b64 v[238:239], v246 offset:53248\n\t"
      "v_mfma_f32_16x16x32_bf16 v[224:227], v[112:115], v[188:191], v[224:227]\n\t"
      "ds_read_b64 v[240:241], v246 offset:57344\n\t"
      "ds_read_b64 v[242:243], v246 offset:61440\n\t"
      "v_mfma_f32_16x16x32_bf16 v[208:211], v[4:7], v[160:163], 0\n\t"
      "v_mfma_f32_16x16x32_bf16 v[208:211], v[20:23], v[164:167], v[208:211]\n\t"
      "s_waitcnt lgkmcnt(4)\n\t"
      "v_add_f32_e32 v228, v228, v230\n\t"
      "v_add_f32_e32 v232, v232, v234\n\t"
      "v_mfma_f32_16x16x32_bf16 v[208:211], v[36:39], v[168:171], v[208:211]\n\t"
      "v_add_f32_e32 v229, v229, v231\n\t"
      "v_add_f32_e32 v233, v233, v235\n\t"
      "v_add_f32_e32 v228, v228, v232\n\t"
      "v_cmp_u_f32_e32 vcc, v224, v224\n\t"
      "s_cbranch_vccnz LA36R_%=\n\t"
      "LA36B_%=:\n\t"
      "v_mfma_f32_16x16x32_bf16 v[208:211], v[52:55], v[172:175], v[208:211]\n\t"
      "v_add_f32_e32 v229, v229, v233\n\t"
      "s_waitcnt lgkmcnt(0)\n\t"
      "v_add_f32_e32 v236, v236, v238\n\t"
      "v_mfma_f32_16x16x32_bf16 v[208:211], v[68:71], v[176:179], v[208:211]\n\t"
      "v_add_f32_e32 v240, v240, v242\n\t"
      "v_add_f32_e32 v237, v237, v239\n\t"
      "v_add_f32_e32 v241, v241, v243\n\t"
      "v_mfma_f32_16x16x32_bf16 v[208:211], v[84:87], v[180:183], v[208:211]\n\t"
      "v_add_f32_e32 v228, v228, v248\n\t"
      "v_add_f32_e32 v236, v236, v240\n\t"
      "ds_write_b128 v245, v[224:227]\n\t"
      "v_mfma_f32_16x16x32_bf16 v[208:211], v[100:103], v[184:187], v[208:211]\n\t"
      "v_add_f32_e32 v229, v229, v249\n\t"
      "v_add_f32_e32 v237, v237, v241\n\t"
      "v_add_f32_e32 v228, v228, v236\n\t"
      "v_mfma_f32_16x16x32_bf16 v[208:211], v[116:119], v[188:191], v[208:211]\n\t"
      "v_add_f32_e32 v229, v229, v237\n\t"
      "v_mul_f32_e32 v230, v228, v228\n\t"
      "v_mul_f32_e32 v231, v229, v229\n\t"
      "v_mfma_f32_16x16x32_bf16 v[212:215], v[8:11], v[160:163], 0\n\t"
      "v_fmaak_f32 v230, v252, v230, 0xc0135761\n\t"
      "v_fmaak_f32 v231, v252, v231, 0xc0135761\n\t"
      "v_mul_f32_e32 v230, v228, v230\n\t"
      "v_mfma_f32_16x16x32_bf16 v[212:215], v[24:27], v[164:167], v[212:215]\n\t"
      "v_mul_f32_e32 v231, v229, v231\n\t"
      "v_exp_f32_e32 v230, v230\n\t"
      "v_exp_f32_e32 v231, v231\n\t"
      "v_mfma_f32_16x16x32_bf16 v[212:215], v[40:43], v[168:171], v[212:215]\n\t"
      "v_add_f32_e32 v230, 1.0, v230\n\t"
      "v_add_f32_e32 v231, 1.0, v231\n\t"
      "v_mfma_f32_16x16x32_bf16 v[212:215], v[56:59], v[172:175], v[212:215]\n\t"
      "v_rcp_f32_e32 v230, v230\n\t"
      "v_rcp_f32_e32 v231, v231\n\t"
      "v_mul_f32_e32 v228, v228, v230\n\t"
      "v_mfma_f32_16x16x32_bf16 v[212:215], v[72:75], v[176:179], v[212:215]\n\t"
      "v_mul_f32_e32 v229, v229, v231\n\t"
      "v_cvt_pk_bf16_f32 v230, v228, v229\n\t"
      "v_min_u32_e32 v254, -2, v230\n\t"
      "v_mfma_f32_16x16x32_bf16 v[212:215], v[88:91], v[180:183], v[212:215]\n\t"
      "global_store_dword v247, v254, s[62:63] sc1\n\t"
      "s_add_u32 s62, s62, 57344\n\t"
      "s_addc_u32 s63, s63, 0\n\t"
      "s_setprio 0\n\t"
      "ds_write_b128 v253, v[208:211] offset:1024\n\t"
      "v_mfma_f32_16x16x32_bf16 v[212:215], v[104:107], v[184:187], v[212:215]\n\t"
      "v_mfma_f32_16x16x32_bf16 v[212:215], v[120:123], v[188:191], v[212:215]\n\t"
      "v_mfma_f32_16x16x32_bf16 v[216:219], v[12:15], v[160:163], 0\n\t"
      "v_mfma_f32_16x16x32_bf16 v[216:219], v[28:31], v[164:167], v[216:219]\n\t"
      "v_mfma_f32_16x16x32_bf16 v[216:219], v[44:47], v[168:171], v[216:219]\n\t"
      "v_mfma_f32_16x16x32_bf16 v[216:219], v[60:63], v[172:175], v[216:219]\n\t"
      "v_mfma_f32_16x16x32_bf16 v[216:219], v[76:79], v[176:179], v[216:219]\n\t"
      "v_mfma_f32_16x16x32_bf16 v[216:219], v[92:95], v[180:183], v[216:219]\n\t"
      "s_nop 1\n\t"
      "ds_write_b128 v245, v[212:215] offset:2048\n\t"
      "v_mfma_f32_16x16x32_bf16 v[216:219], v[108:111], v[184:187], v[216:219]\n\t"
      "v_mfma_f32_16x16x32_bf16 v[216:219], v[124:127], v[188:191], v[216:219]\n\t"
      "s_nop 7\n\t"
      "ds_write_b128 v253, v[216:219] offset:3072\n\t"
      "s_waitcnt lgkmcnt(0)\n\t"
      "s_barrier\n\t"
      "ds_read_b64 v[228:229], v246\n\t"
      "ds_read_b64 v[230:231], v246 offset:4096\n\t"
      "ds_read_b64 v[232:233], v246 offset:8192\n\t"
      "ds_read_b64 v[234:235], v246 offset:12288\n\t"
      "ds_read_b64 v[236:237], v246 offset:16384\n\t"
      "ds_read_b64 v[238:239], v246 offset:20480\n\t"
      "ds_read_b64 v[240:241], v246 offset:24576\n\t"
      "ds_read_b64 v[242:243], v246 offset:28672\n\t"
      "s_waitcnt lgkmcnt(4)\n\t"
      "v_add_f32_e32 v228, v228, v230\n\t"
      "v_add_f32_e32 v232, v232, v234\n\t"
      "v_add_f32_e32 v229, v229, v231\n\t"
      "v_add_f32_e32 v233, v233, v235\n\t"
      "v_add_f32_e32 v228, v228, v232\n\t"
      "v_add_f32_e32 v229, v229, v233\n\t"
      "s_waitcnt lgkmcnt(0)\n\t"
      "v_add_f32_e32 v236, v236, v238\n\t"
      "v_add_f32_e32 v240, v240, v242\n\t"
      "v_add_f32_e32 v237, v237, v239\n\t"
      "v_add_f32_e32 v241, v241, v243\n\t"
      "v_add_f32_e32 v228, v228, v248\n\t"
      "v_add_f32_e32 v236, v236, v240\n\t"
      "v_add_f32_e32 v229, v229, v249\n\t"
      "v_add_f32_e32 v237, v237, v241\n\t"
      "v_add_f32_e32 v228, v228, v236\n\t"
      "v_add_f32_e32 v229, v229, v237\n\t"
      "v_mul_f32_e32 v230, v228, v228\n\t"
      "v_mul_f32_e32 v231, v229, v229\n\t"
      "v_fmaak_f32 v230, v252, v230, 0xc0135761\n\t"
      "v_fmaak_f32 v231, v252, v231, 0xc0135761\n\t"
      "v_mul_f32_e32 v230, v228, v230\n\t"
      "v_mul_f32_e32 v231, v229, v231\n\t"
      "v_exp_f32_e32 v230, v230\n\t"
      "v_exp_f32_e32 v231, v231\n\t"
      "v_add_f32_e32 v230, 1.0, v230\n\t"
      "v_add_f32_e32 v231, 1.0, v231\n\t"
      "v_rcp_f32_e32 v230, v230\n\t"
      "v_rcp_f32_e32 v231, v231\n\t"
      "v_mul_f32_e32 v228, v228, v230\n\t"
      "v_mul_f32_e32 v229, v229, v231\n\t"
      "v_cvt_pk_bf16_f32 v230, v228, v229\n\t"
      "v_min_u32_e32 v254, -2, v230\n\t"
      "global_store_dword v247, v254, s[62:63] sc1\n\t"
      "s_add_u32 s62, s62, 57344\n\t"
      "s_addc_u32 s63, s63, 0\n\t"
      "s_or_b32 s34, s2, s24\n\t"
      "s_cbranch_scc1 EPB_%=\n\t"
      "v_mov_b32_e32 v228, s22\n\t"
      "v_mov_b32_e32 v229, s23\n\t"
      "v_mov_b32_e32 v230, 0\n\t"
      "s_mov_b64 exec, 1\n\t"
      "global_store_dwordx2 v230, v[228:229], s[60:61] sc1\n\t"
      "s_mov_b64 exec, -1\n\t"
      "EPB_%=:\n\t"
      "s_endpgm\n\t"
      "LPATH_%=:\n\t"
      "s_add_u32 s22, s22, 1\n\t"
      "s_addc_u32 s23, s23, 0\n\t"
      "s_add_u32 s72, s26, 1\n\t"
      "s_and_b32 s72, s72, 6\n\t"
      "s_cselect_b32 s72, 0, 2\n\t"
      "s_or_b32 s27, s27, s72\n\t"
      "s_mul_i32 s72, s81, 172032\n\t"
      "s_add_u32 s72, s72, s37\n\t"
      "s_add_u32 s40, s6, s72\n\t"
      "s_addc_u32 s41, s7, 0\n\t"
      "s_add_u32 s81, s81, 1\n\t"
      "s_cmp_eq_u32 s81, 7\n\t"
      "s_cselect_b32 s81, 0, s81\n\t"
      "s_add_u32 m0, s39, 81920\n\t"
      "s_nop 0\n\t"
      "global_load_lds_dwordx4 v148, s[40:41]\n\t"
      "global_load_lds_dwordx4 v149, s[40:41] offset:1024\n\t"
      "global_load_lds_dwordx4 v150, s[40:41] offset:2048\n\t"
      "global_load_lds_dwordx4 v151, s[40:41] offset:3072\n\t"
      "s_mul_i32 s72, s81, 172032\n\t"
      "s_add_u32 s72, s72, s37\n\t"
      "s_add_u32 s40, s6, s72\n\t"
      "s_addc_u32 s41, s7, 0\n\t"
      "s_add_u32 s81, s81, 1\n\t"
      "s_cmp_eq_u32 s81, 7\n\t"
      "s_cselect_b32 s81, 0, s81\n\t"
      "s_add_u32 m0, s39, 98304\n\t"
      "s_nop 0\n\t"
      "global_load_lds_dwordx4 v148, s[40:41]\n\t"
      "global_load_lds_dwordx4 v149, s[40:41] offset:1024\n\t"
      "s_waitcnt vmcnt(14)\n\t"
      "s_barrier\n\t"
      "v_add_u32_e32 v172, 0x10000, v224\n\t"
      "v_add_u32_e32 v173, 0x10000, v225\n\t"
      "v_add_u32_e32 v174, 0x10000, v226\n\t"
      "v_add_u32_e32 v175, 0x10000, v227\n\t"
      "s_lshl_b32 s34, s25, 8\n\t"
      "s_add_u32 s34, s34, 4096\n\t"
      "v_and_b32_e32 v136, 1, v192\n\t"
      "v_lshl_add_u32 v136, v136, 7, s34\n\t"
      "s_lshl_b32 s34, s25, 7\n\t"
      "s_add_u32 s34, s34, 16384\n\t"
      "v_mov_b32_e32 v137, s34\n\t"
      "s_lshl_b32 s34, s2, 7\n\t"
      "s_add_u32 s34, s34, 32768\n\t"
      "v_mov_b32_e32 v143, s34\n\t"
      "s_bitcmp0_b32 s27, 1\n\t"
      "s_cbranch_scc1 SK6_%=\n\t"
      "s_lshr_b32 s73, s26, 2\n\t"
      "s_lshl1_add_u32 s73, s25, s73\n\t"
      "s_lshl_b32 s72, s73, 7\n\t"
      "s_add_u32 s72, s72, 4096\n\t"
      "v_mov_b32_e32 v130, s72\n\t"
      "s_lshl2_add_u32 s75, s73, s24\n\t"
      "v_mov_b32_e32 v128, s22\n\t"
      "v_mov_b32_e32 v129, s23\n\t"
      "v_lshrrev_b32_e32 v140, 1, v244\n\t"
      "s_lshl_b32 s72, s75, 9\n\t"
      "s_add_u32 s72, s72, 12582912\n\t"
      "v_mov_b32_e32 v132, -1\n\t"
      "v_mov_b32_e32 v133, -1\n\t"
      "v_mov_b32_e32 v134, -1\n\t"
      "v_mov_b32_e32 v135, -1\n\t"
      "s_add_u32 s42, s20, s72\n\t"
      "s_addc_u32 s43, s21, 0\n\t"
      "global_store_dwordx2 v140, v[132:133], s[42:43] sc1\n\t"
      "s_lshl_b32 s72, s75, 13\n\t"
      "s_add_u32 s72, s72, 4096\n\t"
      "s_add_u32 s42, s20, s72\n\t"
      "s_addc_u32 s43, s21, 0\n\t"
      "global_store_dwordx4 v244, v[132:135], s[42:43] offset:-4096 sc1\n\t"
      "global_store_dwordx4 v244, v[132:135], s[42:43] offset:-3072 sc1\n\t"
      "global_store_dwordx4 v244, v[132:135], s[42:43] offset:-2048 sc1\n\t"
      "SK6_%=:\n\t"
      "s_waitcnt vmcnt(10)\n\t"
      "s_barrier\n\t"
      "global_load_lds_dwordx4 v150, s[40:41] offset:2048\n\t"
      "global_load_lds_dwordx4 v151, s[40:41] offset:3072\n\t"
      "s_bitcmp0_b32 s27, 1\n\t"
      "s_cbranch_scc1 SK7_%=\n\t"
      "global_store_dwordx4 v244, v[132:135], s[42:43] offset:-1024 sc1\n\t"
      "global_store_dwordx4 v244, v[132:135], s[42:43] sc1\n\t"
      "global_store_dwordx4 v244, v[132:135], s[42:43] offset:1024 sc1\n\t"
      "SK7_%=:\n\t"
      "s_waitcnt vmcnt(8)\n\t"
      "s_barrier\n\t"
      "s_mul_i32 s72, s81, 172032\n\t"
      "s_add_u32 s72, s72, s37\n\t"
      "s_add_u32 s40, s6, s72\n\t"
      "s_addc_u32 s41, s7, 0\n\t"
      "s_add_u32 s81, s81, 1\n\t"
      "s_cmp_eq_u32 s81, 7\n\t"
      "s_cselect_b32 s81, 0, s81\n\t"
      "s_add_u32 m0, s39, 114688\n\t"
      "s_nop 0\n\t"
      "global_load_lds_dwordx4 v148, s[40:41]\n\t"
      "global_load_lds_dwordx4 v149, s[40:41] offset:1024\n\t"
      "global_load_lds_dwordx4 v150, s[40:41] offset:2048\n\t"
      "global_load_lds_dwordx4 v151, s[40:41] offset:3072\n\t"
      "s_bitcmp0_b32 s27, 1\n\t"
      "s_cbranch_scc1 SK8_%=\n\t"
      "global_store_dwordx4 v244, v[132:135], s[42:43] offset:2048 sc1\n\t"
      "global_store_dwordx4 v244, v[132:135], s[42:43] offset:3072 sc1\n\t"
      "SK8_%=:\n\t"
      "s_waitcnt vmcnt(8)\n\t"
      "s_barrier\n\t"
      "s_mul_i32 s72, s81, 172032\n\t"
      "s_add_u32 s72, s72, s37\n\t"
      "s_add_u32 s40, s6, s72\n\t"
      "s_addc_u32 s41, s7, 0\n\t"
      "s_add_u32 s81, s81, 1\n\t"
      "s_cmp_eq_u32 s81, 7\n\t"
      "s_cselect_b32 s81, 0, s81\n\t"
      "s_add_u32 m0, s39, 131072\n\t"
      "s_nop 0\n\t"
      "global_load_lds_dwordx4 v148, s[40:41]\n\t"
      "global_load_lds_dwordx4 v149, s[40:41] offset:1024\n\t"
      "global_load_lds_dwordx4 v150, s[40:41] offset:2048\n\t"
      "global_load_lds_dwordx4 v151, s[40:41] offset:3072\n\t"
      "s_waitcnt vmcnt(8)\n\t"
      "s_bitcmp0_b32 s27, 1\n\t"
      "s_cbranch_scc1 SK9_%=\n\t"
      "s_waitcnt vmcnt(4)\n\t"
      "SK9_%=:\n\t"
      "s_barrier\n\t"
      "s_mul_i32 s72, s81, 172032\n\t"
      "s_add_u32 s72, s72, s37\n\t"
      "s_add_u32 s40, s6, s72\n\t"
      "s_addc_u32 s41, s7, 0\n\t"
      "s_add_u32 s81, s81, 1\n\t"
      "s_cmp_eq_u32 s81, 7\n\t"
      "s_cselect_b32 s81, 0, s81\n\t"
      "s_add_u32 m0, s39, 147456\n\t"
      "s_nop 0\n\t"
      "global_load_lds_dwordx4 v148, s[40:41]\n\t"
      "global_load_lds_dwordx4 v149, s[40:41] offset:1024\n\t"
      "global_load_lds_dwordx4 v150, s[40:41] offset:2048\n\t"
      "global_load_lds_dwordx4 v151, s[40:41] offset:3072\n\t"
      "s_bitcmp0_b32 s27, 1\n\t"
      "s_cbranch_scc1 SK10_%=\n\t"
      "s_cmp_eq_u32 s24, 0\n\t"
      "s_cselect_b64 exec, 1, 0\n\t"
      "global_store_dwordx2 v130, v[128:129], s[60:61] sc1\n\t"
      "s_mov_b64 exec, -1\n\t"
      "s_mov_b32 m0, 64512\n\t"
      "s_mul_i32 s72, s75, 13\n\t"
      "v_min_u32_e32 v141, 12, v192\n\t"
      "v_add_u32_e32 v141, s72, v141\n\t"
      "v_min_u32_e32 v141, 3135, v141\n\t"
      "v_lshlrev_b32_e32 v141, 7, v141\n\t"
      "global_load_lds_dword v141, s[8:9]\n\t"
      "SK10_%=:\n\t"
      "s_waitcnt vmcnt(8)\n\t"
      "s_barrier\n\t"
      "s_mul_i32 s72, s82, 172032\n\t"
      "s_add_u32 s72, s72, s37\n\t"
      "s_add_u32 s72, s72, 1792\n\t"
      "s_add_u32 s40, s6, s72\n\t"
      "s_addc_u32 s41, s7, 0\n\t"
      "s_add_u32 s82, s82, 1\n\t"
      "s_cmp_eq_u32 s82, 7\n\t"
      "s_cselect_b32 s82, 0, s82\n\t"
      "s_add_u32 m0, s39, 65536\n\t"
      "s_nop 0\n\t"
      "global_load_lds_dwordx4 v148, s[40:41]\n\t"
      "global_load_lds_dwordx4 v152, s[40:41] offset:1024\n\t"
      "s_mul_i32 s72, s82, 172032\n\t"
      "s_add_u32 s72, s72, s37\n\t"
      "s_add_u32 s72, s72, 1792\n\t"
      "s_add_u32 s40, s6, s72\n\t"
      "s_addc_u32 s41, s7, 0\n\t"
      "s_add_u32 s82, s82, 1\n\t"
      "s_cmp_eq_u32 s82, 7\n\t"
      "s_cselect_b32 s82, 0, s82\n\t"
      "s_add_u32 m0, s39, 67584\n\t"
      "s_nop 0\n\t"
      "global_load_lds_dwordx4 v148, s[40:41]\n\t"
      "global_load_lds_dwordx4 v152, s[40:41] offset:1024\n\t"
      "s_waitcnt vmcnt(8)\n\t"
      "s_barrier\n\t"
      "s_mul_i32 s72, s82, 172032\n\t"
      "s_add_u32 s72, s72, s37\n\t"
      "s_add_u32 s72, s72, 1792\n\t"
      "s_add_u32 s40, s6, s72\n\t"
      "s_addc_u32 s41, s7, 0\n\t"
      "s_add_u32 s82, s82, 1\n\t"
      "s_cmp_eq_u32 s82, 7\n\t"
      "s_cselect_b32 s82, 0, s82\n\t"
      "s_add_u32 m0, s39, 81920\n\t"
      "s_nop 0\n\t"
      "global_load_lds_dwordx4 v148, s[40:41]\n\t"
      "global_load_lds_dwordx4 v152, s[40:41] offset:1024\n\t"
      "s_mul_i32 s72, s82, 172032\n\t"
      "s_add_u32 s72, s72, s37\n\t"
      "s_add_u32 s72, s72, 1792\n\t"
      "s_add_u32 s40, s6, s72\n\t"
      "s_addc_u32 s41, s7, 0\n\t"
      "s_add_u32 s82, s82, 1\n\t"
      "s_cmp_eq_u32 s82, 7\n\t"
      "s_cselect_b32 s82, 0, s82\n\t"
      "s_add_u32 m0, s39, 83968\n\t"
      "s_nop 0\n\t"
      "global_load_lds_dwordx4 v148, s[40:41]\n\t"
      "global_load_lds_dwordx4 v152, s[40:41] offset:1024\n\t"
      "s_waitcnt vmcnt(8)\n\t"
      "s_barrier\n\t"
      "s_cmp_lg_u32 s24, 3\n\t"
      "s_cbranch_scc1 SK11_%=\n\t"
      "global_load_dwordx2 v[138:139], v136, s[60:61] sc1\n\t"
      "SK11_%=:\n\t"
      "s_mul_i32 s72, s82, 172032\n\t"
      "s_add_u32 s72, s72, s37\n\t"
      "s_add_u32 s72, s72, 1792\n\t"
      "s_add_u32 s40, s6, s72\n\t"
      "s_addc_u32 s41, s7, 0\n\t"
      "s_add_u32 s82, s82, 1\n\t"
      "s_cmp_eq_u32 s82, 7\n\t"
      "s_cselect_b32 s82, 0, s82\n\t"
      "s_lshr_b32 s72, s39, 1\n\t"
      "s_add_u32 m0, s72, 98304\n\t"
      "s_nop 0\n\t"
      "global_load_lds_dwordx4 v148, s[40:41]\n\t"
      "global_load_lds_dwordx4 v152, s[40:41] offset:1024\n\t"
      "global_load_dword v249, v248, s[12:13]\n\t"
      "s_lshl2_add_u32 s72, s31, s24\n\t"
      "s_and_b32 s73, s72, 0x70\n\t"
      "s_add_u32 s72, s72, s73\n\t"
      "s_mov_b32 m0, 64512\n\t"
      "s_mul_i32 s72, s72, 28672\n\t"
      "s_and_b32 s73, s2, 7\n\t"
      "s_mul_i32 s73, s73, 448\n\t"
      "s_and_b32 s73, s73, 0xffffff80\n\t"
      "s_add_u32 s72, s72, s73\n\t"
      "v_bfe_u32 v141, v192, 2, 3\n\t"
      "s_cmp_ge_u32 s31, 28\n\t"
      "s_cselect_b32 s73, 32, 0\n\t"
      "v_and_b32_e32 v142, s73, v192\n\t"
      "v_lshl_or_b32 v141, v142, 2, v141\n\t"
      "v_mul_u32_u24_e32 v141, 3584, v141\n\t"
      "v_lshl_add_u32 v141, v196, 7, v141\n\t"
      "v_add_u32_e32 v141, s72, v141\n\t"
      "global_load_lds_dword v141, s[10:11]\n\t"
      "s_waitcnt vmcnt(8)\n\t"
      "s_barrier\n\t"
      "s_mul_i32 s72, s24, 14336\n\t"
      "s_add_u32 s40, s56, s72\n\t"
      "s_addc_u32 s41, s57, 0\n\t"
      "s_lshr_b32 s72, s39, 1\n\t"
      "s_add_u32 m0, s72, 106496\n\t"
      "s_nop 0\n\t"
      "global_load_lds_dwordx4 v154, s[40:41]\n\t"
      "global_load_lds_dwordx4 v155, s[40:41] offset:1024\n\t"
      "s_waitcnt vmcnt(6)\n\t"
      "s_barrier\n\t"
      "s_mul_i32 s72, s24, 28672\n\t"
      "s_add_u32 s72, s72, 57344\n\t"
      "s_add_u32 s40, s56, s72\n\t"
      "s_addc_u32 s41, s57, 0\n\t"
      "s_lshr_b32 s72, s39, 0\n\t"
      "s_add_u32 m0, s72, 114688\n\t"
      "s_nop 0\n\t"
      "global_load_lds_dwordx4 v154, s[40:41]\n\t"
      "global_load_lds_dwordx4 v155, s[40:41] offset:1024\n\t"
      "global_load_lds_dwordx4 v156, s[40:41] offset:2048\n\t"
      "global_load_lds_dwordx4 v157, s[40:41] offset:3072\n\t"
      "s_waitcnt vmcnt(8)\n\t"
      "s_cmp_lg_u32 s24, 3\n\t"
      "s_cbranch_scc1 LF1B_%=\n\t"
      "v_cmp_ne_u64_e32 vcc, s[22:23], v[138:139]\n\t"
      "s_cbranch_vccnz LF1R_%=\n\t"
      "LF1B_%=:\n\t"
      "s_barrier\n\t"
      "s_mul_i32 s72, s24, 28672\n\t"
      "s_add_u32 s72, s72, 172032\n\t"
      "s_add_u32 s40, s56, s72\n\t"
      "s_addc_u32 s41, s57, 0\n\t"
      "s_lshr_b32 s72, s39, 0\n\t"
      "s_add_u32 m0, s72, 131072\n\t"
      "s_nop 0\n\t"
      "global_load_lds_dwordx4 v154, s[40:41]\n\t"
      "global_load_lds_dwordx4 v155, s[40:41] offset:1024\n\t"
      "global_load_lds_dwordx4 v156, s[40:41] offset:2048\n\t"
      "global_load_lds_dwordx4 v157, s[40:41] offset:3072\n\t"
      "s_mul_i32 s72, s24, 28672\n\t"
      "s_add_u32 s72, s72, 286720\n\t"
      "s_add_u32 s40, s56, s72\n\t"
      "s_addc_u32 s41, s57, 0\n\t"
      "s_lshr_b32 s72, s39, 0\n\t"
      "s_add_u32 m0, s72, 147456\n\t"
      "s_nop 0\n\t"
      "global_load_lds_dwordx4 v154, s[40:41]\n\t"
      "global_load_lds_dwordx4 v155, s[40:41] offset:1024\n\t"
      "global_load_lds_dwordx4 v156, s[40:41] offset:2048\n\t"
      "global_load_lds_dwordx4 v157, s[40:41] offset:3072\n\t"
      "s_barrier\n\t"
      "s_cmp_eq_u32 s26, 7\n\t"
      "s_cbranch_scc1 SPARE_%=\n\t"
      "s_lshl_b32 s72, s24, 10\n\t"
      "s_add_u32 s72, s72, 98304\n\t"
      "v_xor_b32_e32 v203, v197, v198\n\t"
      "v_lshl_add_u32 v203, v203, 4, s72\n\t"
      "v_lshl_add_u32 v203, v197, 7, v203\n\t"
      "ds_write_b128 v203, v[204:207]\n\t"
      "s_waitcnt lgkmcnt(0)\n\t"
      "s_bitcmp0_b32 s27, 0\n\t"
      "s_cbranch_scc1 L4_%=\n\t"
      "s_add_u32 s72, s76, 0\n\t"
      "s_add_u32 s40, s52, s72\n\t"
      "s_addc_u32 s41, s53, 0\n\t"
      "s_add_u32 m0, s39, 65536\n\t"
      "s_nop 0\n\t"
      "global_load_lds_dwordx4 v144, s[40:41]\n\t"
      "global_load_lds_dwordx4 v145, s[40:41] offset:1024\n\t"
      "global_load_lds_dwordx4 v146, s[40:41] offset:2048\n\t"
      "global_load_lds_dwordx4 v147, s[40:41] offset:3072\n\t"
      "s_add_u32 s72, s76, 229376\n\t"
      "s_add_u32 s40, s52, s72\n\t"
      "s_addc_u32 s41, s53, 0\n\t"
      "s_add_u32 m0, s39, 81920\n\t"
      "s_nop 0\n\t"
      "global_load_lds_dwordx4 v144, s[40:41]\n\t"
      "global_load_lds_dwordx4 v145, s[40:41] offset:1024\n\t"
      "global_load_lds_dwordx4 v146, s[40:41] offset:2048\n\t"
      "global_load_lds_dwordx4 v147, s[40:41] offset:3072\n\t"
      "s_waitcnt vmcnt(8)\n\t"
      "s_barrier\n\t"
      "s_barrier\n\t"
      "s_add_u32 s72, s76, 3670016\n\t"
      "s_add_u32 s40, s52, s72\n\t"
      "s_addc_u32 s41, s53, 0\n\t"
      "s_add_u32 m0, s39, 16384\n\t"
      "s_nop 0\n\t"
      "global_load_lds_dwordx4 v144, s[40:41]\n\t"
      "global_load_lds_dwordx4 v145, s[40:41] offset:1024\n\t"
      "global_load_lds_dwordx4 v146, s[40:41] offset:2048\n\t"
      "global_load_lds_dwordx4 v147, s[40:41] offset:3072\n\t"
      "s_add_u32 s72, s76, 3899392\n\t"
      "s_add_u32 s40, s52, s72\n\t"
      "s_addc_u32 s41, s53, 0\n\t"
      "s_add_u32 m0, s39, 49152\n\t"
      "s_nop 0\n\t"
      "global_load_lds_dwordx4 v144, s[40:41]\n\t"
      "global_load_lds_dwordx4 v145, s[40:41] offset:1024\n\t"
      "global_load_lds_dwordx4 v146, s[40:41] offset:2048\n\t"
      "global_load_lds_dwordx4 v147, s[40:41] offset:3072\n\t"
      "s_waitcnt vmcnt(12)\n\t"
      "ds_read_b64_tr_b16 v[0:1], v172\n\t"
      "ds_read_b64_tr_b16 v[2:3], v172 offset:512\n\t"
      "ds_read_b64_tr_b16 v[4:5], v173\n\t"
      "ds_read_b64_tr_b16 v[6:7], v173 offset:512\n\t"
      "ds_read_b64_tr_b16 v[8:9], v174\n\t"
      "ds_read_b64_tr_b16 v[10:11], v174 offset:512\n\t"
      "ds_read_b64_tr_b16 v[12:13], v175\n\t"
      "ds_read_b64_tr_b16 v[14:15], v175 offset:512\n\t"
      "s_waitcnt lgkmcnt(0)\n\t"
      "s_add_u32 s72, s77, 0\n\t"
      "s_add_u32 s40, s52, s72\n\t"
      "s_addc_u32 s41, s53, 0\n\t"
      "s_add_u32 m0, s39, 65536\n\t"
      "s_nop 0\n\t"
      "global_load_lds_dwordx4 v144, s[40:41]\n\t"
      "global_load_lds_dwordx4 v145, s[40:41] offset:1024\n\t"
      "global_load_lds_dwordx4 v146, s[40:41] offset:2048\n\t"
      "global_load_lds_dwordx4 v147, s[40:41] offset:3072\n\t"
      "s_waitcnt vmcnt(12)\n\t"
      "ds_read_b64_tr_b16 v[16:17], v172 offset:16384\n\t"
      "ds_read_b64_tr_b16 v[18:19], v172 offset:16896\n\t"
      "ds_read_b64_tr_b16 v[20:21], v173 offset:16384\n\t"
      "ds_read_b64_tr_b16 v[22:23], v173 offset:16896\n\t"
      "ds_read_b64_tr_b16 v[24:25], v174 offset:16384\n\t"
      "ds_read_b64_tr_b16 v[26:27], v174 offset:16896\n\t"
      "ds_read_b64_tr_b16 v[28:29], v175 offset:16384\n\t"
      "ds_read_b64_tr_b16 v[30:31], v175 offset:16896\n\t"
      "s_waitcnt lgkmcnt(0)\n\t"
      "s_add_u32 s72, s77, 229376\n\t"
      "s_add_u32 s40, s52, s72\n\t"
      "s_addc_u32 s41, s53, 0\n\t"
      "s_add_u32 m0, s39, 81920\n\t"
      "s_nop 0\n\t"
      "global_load_lds_dwordx4 v144, s[40:41]\n\t"
      "global_load_lds_dwordx4 v145, s[40:41] offset:1024\n\t"
      "global_load_lds_dwordx4 v146, s[40:41] offset:2048\n\t"
      "global_load_lds_dwordx4 v147, s[40:41] offset:3072\n\t"
      "s_add_u32 s72, s77, 3670016\n\t"
      "s_add_u32 s40, s52, s72\n\t"
      "s_addc_u32 s41, s53, 0\n\t"
      "s_add_u32 m0, s39, 0\n\t"
      "s_nop 0\n\t"
      "global_load_lds_dwordx4 v144, s[40:41]\n\t"
      "global_load_lds_dwordx4 v145, s[40:41] offset:1024\n\t"
      "global_load_lds_dwordx4 v146, s[40:41] offset:2048\n\t"
      "global_load_lds_dwordx4 v147, s[40:41] offset:3072\n\t"
      "s_add_u32 s72, s77, 3899392\n\t"
      "s_add_u32 s40, s52, s72\n\t"
      "s_addc_u32 s41, s53, 0\n\t"
      "s_add_u32 m0, s39, 32768\n\t"
      "s_nop 0\n\t"
      "global_load_lds_dwordx4 v144, s[40:41]\n\t"
      "global_load_lds_dwordx4 v145, s[40:41] offset:1024\n\t"
      "global_load_lds_dwordx4 v146, s[40:41] offset:2048\n\t"
      "global_load_lds_dwordx4 v147, s[40:41] offset:3072\n\t"
      "s_waitcnt vmcnt(12)\n\t"
      "ds_read_b64_tr_b16 v[32:33], v172\n\t"
      "ds_read_b64_tr_b16 v[34:35], v172 offset:512\n\t"
      "ds_read_b64_tr_b16 v[36:37], v173\n\t"
      "ds_read_b64_tr_b16 v[38:39], v173 offset:512\n\t"
      "ds_read_b64_tr_b16 v[40:41], v174\n\t"
      "ds_read_b64_tr_b16 v[42:43], v174 offset:512\n\t"
      "ds_read_b64_tr_b16 v[44:45], v175\n\t"
      "ds_read_b64_tr_b16 v[46:47], v175 offset:512\n\t"
      "s_waitcnt lgkmcnt(0)\n\t"
      "s_add_u32 s72, s78, 0\n\t"
      "s_add_u32 s40, s52, s72\n\t"
      "s_addc_u32 s41, s53, 0\n\t"
      "s_add_u32 m0, s39, 65536\n\t"
      "s_nop 0\n\t"
      "global_load_lds_dwordx4 v144, s[40:41]\n\t"
      "global_load_lds_dwordx4 v145, s[40:41] offset:1024\n\t"
      "global_load_lds_dwordx4 v146, s[40:41] offset:2048\n\t"
      "global_load_lds_dwordx4 v147, s[40:41] offset:3072\n\t"
      "s_waitcnt vmcnt(12)\n\t"
      "ds_read_b64_tr_b16 v[48:49], v172 offset:16384\n\t"
      "ds_read_b64_tr_b16 v[50:51], v172 offset:16896\n\t"
      "ds_read_b64_tr_b16 v[52:53], v173 offset:16384\n\t"
      "ds_read_b64_tr_b16 v[54:55], v173 offset:16896\n\t"
      "ds_read_b64_tr_b16 v[56:57], v174 offset:16384\n\t"
      "ds_read_b64_tr_b16 v[58:59], v174 offset:16896\n\t"
      "ds_read_b64_tr_b16 v[60:61], v175 offset:16384\n\t"
      "ds_read_b64_tr_b16 v[62:63], v175 offset:16896\n\t"
      "s_waitcnt lgkmcnt(0)\n\t"
      "s_add_u32 s72, s78, 229376\n\t"
      "s_add_u32 s40, s52, s72\n\t"
      "s_addc_u32 s41, s53, 0\n\t"
      "s_add_u32 m0, s39, 81920\n\t"
      "s_nop 0\n\t"
      "global_load_lds_dwordx4 v144, s[40:41]\n\t"
      "global_load_lds_dwordx4 v145, s[40:41] offset:1024\n\t"
      "global_load_lds_dwordx4 v146, s[40:41] offset:2048\n\t"
      "global_load_lds_dwordx4 v147, s[40:41] offset:3072\n\t"
      "s_waitcnt vmcnt(8)\n\t"
      "s_barrier\n\t"
      "s_cmp_lg_u32 s24, 3\n\t"
      "s_cbranch_scc1 SK13_%=\n\t"
      "global_load_dwordx2 v[138:139], v137, s[60:61] sc1\n\t"
      "SK13_%=:\n\t"
      "s_add_u32 s72, s79, 0\n\t"
      "s_add_u32 s40, s52, s72\n\t"
      "s_addc_u32 s41, s53, 0\n\t"
      "s_add_u32 m0, s39, 98304\n\t"
      "s_nop 0\n\t"
      "global_load_lds_dwordx4 v144, s[40:41]\n\t"
      "global_load_lds_dwordx4 v145, s[40:41] offset:1024\n\t"
      "global_load_lds_dwordx4 v146, s[40:41] offset:2048\n\t"
      "global_load_lds_dwordx4 v147, s[40:41] offset:3072\n\t"
      "s_add_u32 s72, s79, 229376\n\t"
      "s_add_u32 s40, s52, s72\n\t"
      "s_addc_u32 s41, s53, 0\n\t"
      "s_add_u32 m0, s39, 114688\n\t"
      "s_nop 0\n\t"
      "global_load_lds_dwordx4 v144, s[40:41]\n\t"
      "global_load_lds_dwordx4 v145, s[40:41] offset:1024\n\t"
      "global_load_lds_dwordx4 v146, s[40:41] offset:2048\n\t"
      "global_load_lds_dwordx4 v147, s[40:41] offset:3072\n\t"
      "s_cmp_lg_u32 s24, 3\n\t"
      "s_cbranch_scc1 LF2B_%=\n\t"
      "s_waitcnt vmcnt(8)\n\t"
      "v_cmp_ne_u64_e32 vcc, s[22:23], v[138:139]\n\t"
      "s_cbranch_vccnz LF2R_%=\n\t"
      "LF2B_%=:\n\t"
      "s_barrier\n\t"
      "s_add_u32 s72, s78, 3670016\n\t"
      "s_add_u32 s40, s52, s72\n\t"
      "s_addc_u32 s41, s53, 0\n\t"
      "s_add_u32 m0, s39, 16384\n\t"
      "s_nop 0\n\t"
      "global_load_lds_dwordx4 v144, s[40:41]\n\t"
      "global_load_lds_dwordx4 v145, s[40:41] offset:1024\n\t"
      "global_load_lds_dwordx4 v146, s[40:41] offset:2048\n\t"
      "global_load_lds_dwordx4 v147, s[40:41] offset:3072\n\t"
      "s_add_u32 s72, s78, 3899392\n\t"
      "s_add_u32 s40, s52, s72\n\t"
      "s_addc_u32 s41, s53, 0\n\t"
      "s_add_u32 m0, s39, 49152\n\t"
      "s_nop 0\n\t"
      "global_load_lds_dwordx4 v144, s[40:41]\n\t"
      "global_load_lds_dwordx4 v145, s[40:41] offset:1024\n\t"
      "global_load_lds_dwordx4 v146, s[40:41] offset:2048\n\t"
      "global_load_lds_dwordx4 v147, s[40:41] offset:3072\n\t"
      "s_add_u32 s72, s79, 3670016\n\t"
      "s_add_u32 s40, s52, s72\n\t"
      "s_addc_u32 s41, s53, 0\n\t"
      "s_add_u32 m0, s39, 0\n\t"
      "s_nop 0\n\t"
      "global_load_lds_dwordx4 v144, s[40:41]\n\t"
      "global_load_lds_dwordx4 v145, s[40:41] offset:1024\n\t"
      "global_load_lds_dwordx4 v146, s[40:41] offset:2048\n\t"
      "global_load_lds_dwordx4 v147, s[40:41] offset:3072\n\t"
      "s_add_u32 s72, s79, 3899392\n\t"
      "s_add_u32 s40, s52, s72\n\t"
      "s_addc_u32 s41, s53, 0\n\t"
      "s_add_u32 m0, s39, 32768\n\t"
      "s_nop 0\n\t"
      "global_load_lds_dwordx4 v144, s[40:41]\n\t"
      "global_load_lds_dwordx4 v145, s[40:41] offset:1024\n\t"
      "global_load_lds_dwordx4 v146, s[40:41] offset:2048\n\t"
      "global_load_lds_dwordx4 v147, s[40:41] offset:3072\n\t"
      "s_cmp_lg_u32 s24, 3\n\t"
      "s_cbranch_scc1 SK14_%=\n\t"
      "global_load_dwordx2 v[138:139], v143, s[60:61] sc1\n\t"
      "SK14_%=:\n\t"
      "s_waitcnt vmcnt(28)\n\t"
      "ds_read_b64_tr_b16 v[64:65], v172\n\t"
      "ds_read_b64_tr_b16 v[66:67], v172 offset:512\n\t"
      "ds_read_b64_tr_b16 v[68:69], v173\n\t"
      "ds_read_b64_tr_b16 v[70:71], v173 offset:512\n\t"
      "ds_read_b64_tr_b16 v[72:73], v174\n\t"
      "ds_read_b64_tr_b16 v[74:75], v174 offset:512\n\t"
      "ds_read_b64_tr_b16 v[76:77], v175\n\t"
      "ds_read_b64_tr_b16 v[78:79], v175 offset:512\n\t"
      "s_waitcnt vmcnt(24)\n\t"
      "ds_read_b64_tr_b16 v[80:81], v172 offset:16384\n\t"
      "ds_read_b64_tr_b16 v[82:83], v172 offset:16896\n\t"
      "ds_read_b64_tr_b16 v[84:85], v173 offset:16384\n\t"
      "ds_read_b64_tr_b16 v[86:87], v173 offset:16896\n\t"
      "ds_read_b64_tr_b16 v[88:89], v174 offset:16384\n\t"
      "ds_read_b64_tr_b16 v[90:91], v174 offset:16896\n\t"
      "ds_read_b64_tr_b16 v[92:93], v175 offset:16384\n\t"
      "ds_read_b64_tr_b16 v[94:95], v175 offset:16896\n\t"
      "s_waitcnt vmcnt(20)\n\t"
      "ds_read_b64_tr_b16 v[96:97], v172 offset:32768\n\t"
      "ds_read_b64_tr_b16 v[98:99], v172 offset:33280\n\t"
      "ds_read_b64_tr_b16 v[100:101], v173 offset:32768\n\t"
      "ds_read_b64_tr_b16 v[102:103], v173 offset:33280\n\t"
      "ds_read_b64_tr_b16 v[104:105], v174 offset:32768\n\t"
      "ds_read_b64_tr_b16 v[106:107], v174 offset:33280\n\t"
      "ds_read_b64_tr_b16 v[108:109], v175 offset:32768\n\t"
      "ds_read_b64_tr_b16 v[110:111], v175 offset:33280\n\t"
      "s_waitcnt vmcnt(16)\n\t"
      "ds_read_b64_tr_b16 v[112:113], v172 offset:49152\n\t"
      "ds_read_b64_tr_b16 v[114:115], v172 offset:49664\n\t"
      "ds_read_b64_tr_b16 v[116:117], v173 offset:49152\n\t"
      "ds_read_b64_tr_b16 v[118:119], v173 offset:49664\n\t"
      "ds_read_b64_tr_b16 v[120:121], v174 offset:49152\n\t"
      "ds_read_b64_tr_b16 v[122:123], v174 offset:49664\n\t"
      "ds_read_b64_tr_b16 v[124:125], v175 offset:49152\n\t"
      "ds_read_b64_tr_b16 v[126:127], v175 offset:49664\n\t"
      "s_waitcnt vmcnt(0)\n\t"
      "s_cmp_lg_u32 s24, 3\n\t"
      "s_cbranch_scc1 LF3B_%=\n\t"
      "s_waitcnt vmcnt(0)\n\t"
      "v_cmp_ne_u64_e32 vcc, s[22:23], v[138:139]\n\t"
      "s_cbranch_vccnz LF3R_%=\n\t"
      "LF3B_%=:\n\t"
      "s_waitcnt lgkmcnt(0)\n\t"
      "s_barrier\n\t"
      "s_mul_i32 s34, s32, 196608\n\t"
      "s_mul_i32 s35, s24, 8192\n\t"
      "s_add_u32 s34, s34, s35\n\t"
      "s_add_u32 s34, s34, 1048576\n\t"
      "s_add_u32 s64, s48, s34\n\t"
      "s_addc_u32 s65, s49, 0\n\t"
      "v_add_u32_e32 v250, 4096, v244\n\t"
      "global_load_dwordx4 v[128:131], v250, s[64:65] offset:-4096 sc1\n\t"
      "global_load_dwordx4 v[132:135], v250, s[64:65] offset:-3072 sc1\n\t"
      "global_load_dwordx4 v[136:139], v250, s[64:65] offset:-2048 sc1\n\t"
      "global_load_dwordx4 v[140:143], v250, s[64:65] offset:-1024 sc1\n\t"
      "global_load_dwordx4 v[144:147], v250, s[64:65] sc1\n\t"
      "global_load_dwordx4 v[148:151], v250, s[64:65] offset:1024 sc1\n\t"
      "global_load_dwordx4 v[152:155], v250, s[64:65] offset:2048 sc1\n\t"
      "global_load_dwordx4 v[156:159], v250, s[64:65] offset:3072 sc1\n\t"
      "s_waitcnt vmcnt(0) lgkmcnt(0)\n\t"
      "s_branch GST_%=\n\t"
      "G4C_%=:\n\t"
      "global_store_dwordx4 v241, v[208:211], s[20:21] offset:-2048 sc1\n\t"
      "global_store_dwordx4 v241, v[212:215], s[20:21] offset:2048 sc1\n\t"
      "s_setprio 0\n\t"
      "G4_%=:\n\t"
      "s_mul_i32 s34, s32, 401408\n\t"
      "s_mul_i32 s35, s24, 7168\n\t"
      "s_add_u32 s34, s34, s35\n\t"
      "s_add_u32 s34, s34, 1835008\n\t"
      "s_add_u32 s64, s48, s34\n\t"
      "s_addc_u32 s65, s49, 0\n\t"
      "s_and_b32 s34, s30, 3\n\t"
      "s_lshl_b32 s34, s34, 11\n\t"
      "s_add_u32 s40, s34, 0\n\t"
      "s_sub_u32 s35, s40, 7168\n\t"
      "s_min_u32 s40, s40, s35\n\t"
      "s_add_u32 s41, s34, 1024\n\t"
      "s_sub_u32 s35, s41, 7168\n\t"
      "s_min_u32 s41, s41, s35\n\t"
      "s_add_u32 s42, s34, 2048\n\t"
      "s_sub_u32 s35, s42, 7168\n\t"
      "s_min_u32 s42, s42, s35\n\t"
      "s_add_u32 s43, s34, 3072\n\t"
      "s_sub_u32 s35, s43, 7168\n\t"
      "s_min_u32 s43, s43, s35\n\t"
      "s_add_u32 s44, s34, 4096\n\t"
      "s_sub_u32 s35, s44, 7168\n\t"
      "s_min_u32 s44, s44, s35\n\t"
      "s_add_u32 s45, s34, 5120\n\t"
      "s_sub_u32 s35, s45, 7168\n\t"
      "s_min_u32 s45, s45, s35\n\t"
      "s_add_u32 s46, s34, 6144\n\t"
      "s_sub_u32 s35, s46, 7168\n\t"
      "s_min_u32 s46, s46, s35\n\t"
      "s_add_u32 s66, s64, 57344\n\t"
      "s_addc_u32 s67, s65, 0\n\t"
      "s_add_u32 s68, s66, 57344\n\t"
      "s_addc_u32 s69, s67, 0\n\t"
      "s_add_u32 s70, s68, 57344\n\t"
      "s_addc_u32 s71, s69, 0\n\t"
      "s_add_u32 s72, s70, 57344\n\t"
      "s_addc_u32 s73, s71, 0\n\t"
      "s_add_u32 s74, s72, 57344\n\t"
      "s_addc_u32 s75, s73, 0\n\t"
      "s_add_u32 s76, s74, 57344\n\t"
      "s_addc_u32 s77, s75, 0\n\t"
      "v_lshrrev_b32_e32 v240, 7, v244\n\t"
      "v_and_b32_e32 v241, 0x70, v244\n\t"
      "v_and_b32_e32 v242, 6, v240\n\t"
      "v_lshlrev_b32_e32 v242, 4, v242\n\t"
      "v_xor_b32_e32 v242, v242, v241\n\t"
      "v_lshl_or_b32 v228, v240, 12, v242\n\t"
      "s_mul_i32 s34, s24, 917504\n\t"
      "s_lshl_b32 s35, s29, 7\n\t"
      "s_add_u32 s34, s34, s35\n\t"
      "s_lshl_b32 s39, s24, 12\n\t"
      "s_add_u32 s54, s14, s34\n\t"
      "s_addc_u32 s55, s15, 0\n\t"
      "v_add_u32_e32 v229, 31744, v228\n\t"
      "v_add_u32_e32 v230, 63488, v228\n\t"
      "v_add_u32_e32 v231, 95232, v228\n\t"
      "s_lshl_b32 s37, s40, 7\n\t"
      "s_add_u32 s58, s54, s37\n\t"
      "s_addc_u32 s59, s55, 0\n\t"
      "s_add_u32 m0, s39, 0\n\t"
      "s_nop 0\n\t"
      "global_load_lds_dwordx4 v228, s[58:59]\n\t"
      "global_load_lds_dwordx4 v229, s[58:59] offset:1024\n\t"
      "global_load_lds_dwordx4 v230, s[58:59] offset:2048\n\t"
      "global_load_lds_dwordx4 v231, s[58:59] offset:3072\n\t"
      "s_lshl_b32 s37, s41, 7\n\t"
      "s_add_u32 s58, s54, s37\n\t"
      "s_addc_u32 s59, s55, 0\n\t"
      "s_add_u32 m0, s39, 32768\n\t"
      "s_nop 0\n\t"
      "global_load_lds_dwordx4 v228, s[58:59]\n\t"
      "global_load_lds_dwordx4 v229, s[58:59] offset:1024\n\t"
      "global_load_lds_dwordx4 v230, s[58:59] offset:2048\n\t"
      "global_load_lds_dwordx4 v231, s[58:59] offset:3072\n\t"
      "s_lshr_b32 s34, s24, 1\n\t"
      "s_lshl2_add_u32 s35, s29, s34\n\t"
      "s_and_b32 s36, s24, 1\n\t"
      "s_lshl4_add_u32 s35, s35, s36\n\t"
      "v_lshl_add_u32 v242, v240, 1, s35\n\t"
      "v_lshlrev_b32_e32 v248, 1, v242\n\t"
      "global_load_ushort v249, v248, s[16:17]\n\t"
      "s_movk_i32 s35, 896\n\t"
      "v_and_b32_e32 v247, 64, v244\n\t"
      "v_lshrrev_b32_e32 v247, 2, v247\n\t"
      "v_mad_u32_u24 v247, v242, s35, v247\n\t"
      "v_and_b32_e32 v242, 0x30, v244\n\t"
      "v_lshl_add_u32 v247, v242, 1, v247\n\t"
      "s_mov_b32 s82, 0x77777777\n\t"
      "s_mov_b32 s83, 0x77777777\n\t"
      "s_lshl_b32 s35, s32, 7\n\t"
      "s_add_u32 s62, s18, s35\n\t"
      "s_addc_u32 s63, s19, 0\n\t"
      "s_mul_i32 s35, s32, 96\n\t"
      "s_add_u32 s35, s35, 512\n\t"
      "s_add_u32 s18, s18, s35\n\t"
      "s_addc_u32 s19, s19, 0\n\t"
      "s_mul_i32 s35, s29, 28\n\t"
      "s_lshl2_add_u32 s35, s32, s35\n\t"
      "s_lshl1_add_u32 s35, s35, s36\n\t"
      "s_lshl2_add_u32 s35, s35, s34\n\t"
      "s_lshl_b32 s35, s35, 8\n\t"
      "s_add_u32 s35, s35, 6295552\n\t"
      "s_add_u32 s50, s20, s35\n\t"
      "s_addc_u32 s51, s21, 0\n\t"
      "v_lshrrev_b32_e32 v248, 2, v244\n\t"
      "v_bfe_u32 v242, v244, 8, 1\n\t"
      "v_lshlrev_b32_e32 v242, 4, v242\n\t"
      "v_xor_b32_e32 v245, v244, v242\n\t"
      "v_bfe_u32 v242, v244, 9, 1\n\t"
      "v_lshlrev_b32_e32 v242, 7, v242\n\t"
      "v_xor_b32_e32 v245, v245, v242\n\t"
      "s_add_u32 s35, s39, 98304\n\t"
      "v_add_u32_e32 v245, s35, v245\n\t"
      "v_lshl_or_b32 v246, v240, 1, s36\n\t"
      "v_bfe_u32 v242, v244, 5, 1\n\t"
      "v_xor_b32_e32 v246, v246, v242\n\t"
      "v_bfe_u32 v242, v244, 6, 1\n\t"
      "v_lshlrev_b32_e32 v242, 3, v242\n\t"
      "v_xor_b32_e32 v246, v246, v242\n\t"
      "v_lshlrev_b32_e32 v246, 4, v246\n\t"
      "v_and_b32_e32 v242, 0x60, v244\n\t"
      "v_lshl_or_b32 v246, v242, 3, v246\n\t"
      "v_bfe_u32 v242, v244, 4, 1\n\t"
      "v_lshl_or_b32 v246, v242, 3, v246\n\t"
      "s_lshl_b32 s34, s34, 10\n\t"
      "s_add_u32 s34, s34, 98304\n\t"
      "v_add_u32_e32 v246, s34, v246\n\t"
      "v_and_b32_e32 v240, 0x3c0, v244\n\t"
      "v_lshlrev_b32_e32 v240, 1, v240\n\t"
      "v_and_b32_e32 v241, 0x180, v244\n\t"
      "v_lshrrev_b32_e32 v241, 2, v241\n\t"
      "v_and_b32_e32 v242, 0x30, v244\n\t"
      "v_lshrrev_b32_e32 v242, 1, v242\n\t"
      "v_or3_b32 v232, v240, v241, v242\n\t"
      "v_add_u32_e32 v232, s39, v232\n\t"
      "v_min_u32_e32 v252, 432, v244\n\t"
      "v_lshlrev_b32_e32 v252, 4, v252\n\t"
      "v_xor_b32_e32 v233, 32, v232\n\t"
      "v_xor_b32_e32 v234, 64, v232\n\t"
      "v_xor_b32_e32 v235, 96, v232\n\t"
      "v_xor_b32_e32 v236, 65536, v232\n\t"
      "v_xor_b32_e32 v237, 65568, v232\n\t"
      "v_xor_b32_e32 v238, 65600, v232\n\t"
      "v_xor_b32_e32 v239, 65632, v232\n\t"
      "v_min_u32_e32 v240, 48, v244\n\t"
      "v_mul_u32_u24_e32 v240, 7, v240\n\t"
      "s_lshl_b32 s34, s29, 4\n\t"
      "v_add_u32_e32 v240, s34, v240\n\t"
      "v_subrev_u32_e32 v241, 448, v240\n\t"
      "v_min_u32_e32 v240, v240, v241\n\t"
      "v_subrev_u32_e32 v241, 448, v240\n\t"
      "v_min_u32_e32 v240, v240, v241\n\t"
      "s_and_b32 s34, s29, 7\n\t"
      "s_lshl_b32 s34, s34, 8\n\t"
      "v_lshl_add_u32 v253, v240, 7, s34\n\t"
      "s_sub_u32 s52, s64, 0\n\t"
      "s_subb_u32 s53, s65, 0\n\t"
      "v_add_u32_e32 v143, 32768, v143\n\t"
      "s_barrier\n\t"
      "s_lshl_b32 s37, s42, 7\n\t"
      "s_add_u32 s58, s54, s37\n\t"
      "s_addc_u32 s59, s55, 0\n\t"
      "s_add_u32 m0, s39, 65536\n\t"
      "s_nop 0\n\t"
      "global_load_lds_dwordx4 v228, s[58:59]\n\t"
      "global_load_lds_dwordx4 v229, s[58:59] offset:1024\n\t"
      "global_load_lds_dwordx4 v230, s[58:59] offset:2048\n\t"
      "global_load_lds_dwordx4 v231, s[58:59] offset:3072\n\t"
      "s_lshl_b32 s37, s43, 7\n\t"
      "s_add_u32 s58, s54, s37\n\t"
      "s_addc_u32 s59, s55, 0\n\t"
      "s_add_u32 m0, s39, 98304\n\t"
      "s_nop 0\n\t"
      "global_load_lds_dwordx4 v228, s[58:59]\n\t"
      "global_load_lds_dwordx4 v229, s[58:59] offset:1024\n\t"
      "global_load_lds_dwordx4 v230, s[58:59] offset:2048\n\t"
      "global_load_lds_dwordx4 v231, s[58:59] offset:3072\n\t"
      "s_waitcnt vmcnt(13)\n\t"
      "ds_read_b64_tr_b16 v[0:1], v232\n\t"
      "ds_read_b64_tr_b16 v[2:3], v232 offset:2048\n\t"
      "ds_read_b64_tr_b16 v[4:5], v233\n\t"
      "ds_read_b64_tr_b16 v[6:7], v233 offset:2048\n\t"
      "ds_read_b64_tr_b16 v[8:9], v234\n\t"
      "ds_read_b64_tr_b16 v[10:11], v234 offset:2048\n\t"
      "ds_read_b64_tr_b16 v[12:13], v235\n\t"
      "ds_read_b64_tr_b16 v[14:15], v235 offset:2048\n\t"
      "s_waitcnt lgkmcnt(0)\n\t"
      "s_lshl_b32 s37, s44, 7\n\t"
      "s_add_u32 s58, s54, s37\n\t"
      "s_addc_u32 s59, s55, 0\n\t"
      "s_add_u32 m0, s39, 0\n\t"
      "s_nop 0\n\t"
      "global_load_lds_dwordx4 v228, s[58:59]\n\t"
      "global_load_lds_dwordx4 v229, s[58:59] offset:1024\n\t"
      "global_load_lds_dwordx4 v230, s[58:59] offset:2048\n\t"
      "global_load_lds_dwordx4 v231, s[58:59] offset:3072\n\t"
      "s_waitcnt vmcnt(13)\n\t"
      "ds_read_b64_tr_b16 v[16:17], v232 offset:32768\n\t"
      "ds_read_b64_tr_b16 v[18:19], v232 offset:34816\n\t"
      "ds_read_b64_tr_b16 v[20:21], v233 offset:32768\n\t"
      "ds_read_b64_tr_b16 v[22:23], v233 offset:34816\n\t"
      "ds_read_b64_tr_b16 v[24:25], v234 offset:32768\n\t"
      "ds_read_b64_tr_b16 v[26:27], v234 offset:34816\n\t"
      "ds_read_b64_tr_b16 v[28:29], v235 offset:32768\n\t"
      "ds_read_b64_tr_b16 v[30:31], v235 offset:34816\n\t"
      "s_cmp_lg_u32 s24, 3\n\t"
      "s_cbranch_scc1 SK18_%=\n\t"
      "global_load_dwordx2 v[138:139], v143, s[60:61] sc1\n\t"
      "SK18_%=:\n\t"
      "s_waitcnt lgkmcnt(0)\n\t"
      "s_lshl_b32 s37, s45, 7\n\t"
      "s_add_u32 s58, s54, s37\n\t"
      "s_addc_u32 s59, s55, 0\n\t"
      "s_add_u32 m0, s39, 32768\n\t"
      "s_nop 0\n\t"
      "global_load_lds_dwordx4 v228, s[58:59]\n\t"
      "global_load_lds_dwordx4 v229, s[58:59] offset:1024\n\t"
      "global_load_lds_dwordx4 v230, s[58:59] offset:2048\n\t"
      "global_load_lds_dwordx4 v231, s[58:59] offset:3072\n\t"
      "s_waitcnt vmcnt(12)\n\t"
      "ds_read_b64_tr_b16 v[32:33], v236\n\t"
      "ds_read_b64_tr_b16 v[34:35], v236 offset:2048\n\t"
      "ds_read_b64_tr_b16 v[36:37], v237\n\t"
      "ds_read_b64_tr_b16 v[38:39], v237 offset:2048\n\t"
      "ds_read_b64_tr_b16 v[40:41], v238\n\t"
      "ds_read_b64_tr_b16 v[42:43], v238 offset:2048\n\t"
      "ds_read_b64_tr_b16 v[44:45], v239\n\t"
      "ds_read_b64_tr_b16 v[46:47], v239 offset:2048\n\t"
      "s_waitcnt lgkmcnt(0)\n\t"
      "s_lshl_b32 s37, s46, 7\n\t"
      "s_add_u32 s58, s54, s37\n\t"
      "s_addc_u32 s59, s55, 0\n\t"
      "s_add_u32 m0, s39, 65536\n\t"
      "s_nop 0\n\t"
      "global_load_lds_dwordx4 v228, s[58:59]\n\t"
      "global_load_lds_dwordx4 v229, s[58:59] offset:1024\n\t"
      "global_load_lds_dwordx4 v230, s[58:59] offset:2048\n\t"
      "global_load_lds_dwordx4 v231, s[58:59] offset:3072\n\t"
      "s_waitcnt vmcnt(12)\n\t"
      "ds_read_b64_tr_b16 v[48:49], v236 offset:32768\n\t"
      "ds_read_b64_tr_b16 v[50:51], v236 offset:34816\n\t"
      "ds_read_b64_tr_b16 v[52:53], v237 offset:32768\n\t"
      "ds_read_b64_tr_b16 v[54:55], v237 offset:34816\n\t"
      "ds_read_b64_tr_b16 v[56:57], v238 offset:32768\n\t"
      "ds_read_b64_tr_b16 v[58:59], v238 offset:34816\n\t"
      "ds_read_b64_tr_b16 v[60:61], v239 offset:32768\n\t"
      "ds_read_b64_tr_b16 v[62:63], v239 offset:34816\n\t"
      "s_waitcnt vmcnt(8)\n\t"
      "ds_read_b64_tr_b16 v[64:65], v232\n\t"
      "ds_read_b64_tr_b16 v[66:67], v232 offset:2048\n\t"
      "ds_read_b64_tr_b16 v[68:69], v233\n\t"
      "ds_read_b64_tr_b16 v[70:71], v233 offset:2048\n\t"
      "ds_read_b64_tr_b16 v[72:73], v234\n\t"
      "ds_read_b64_tr_b16 v[74:75], v234 offset:2048\n\t"
      "ds_read_b64_tr_b16 v[76:77], v235\n\t"
      "ds_read_b64_tr_b16 v[78:79], v235 offset:2048\n\t"
      "s_cmp_lg_u32 s24, 3\n\t"
      "s_cbranch_scc1 LF5B_%=\n\t"
      "s_waitcnt vmcnt(8)\n\t"
      "v_cmp_ne_u64_e32 vcc, s[22:23], v[138:139]\n\t"
      "s_cbranch_vccnz LF5R_%=\n\t"
      "LF5B_%=:\n\t"
      "s_barrier\n\t"
      "s_cmp_lg_u32 s24, 3\n\t"
      "s_cbranch_scc1 HF4_%=\n\t"
      "s_lshl3_add_u32 s34, s25, s26\n\t"
      "s_lshl_b32 s34, s34, 7\n\t"
      "s_add_u32 s34, s34, 98304\n\t"
      "v_mov_b32_e32 v146, s34\n\t"
      "v_mov_b32_e32 v144, s22\n\t"
      "v_mov_b32_e32 v145, s23\n\t"
      "s_mov_b64 exec, 1\n\t"
      "global_store_dwordx2 v146, v[144:145], s[60:61] sc1\n\t"
      "s_mov_b64 exec, -1\n\t"
      "HF4_%=:\n\t"
      "s_cmp_lg_u32 s24, 0\n\t"
      "s_cbranch_scc1 PG0_%=\n\t"
      "global_load_dword v251, v253, s[52:53] offset:252 sc1\n\t"
      "PG0_%=:\n\t"
      "s_waitcnt vmcnt(4)\n\t"
      "ds_read_b64_tr_b16 v[80:81], v232 offset:32768\n\t"
      "ds_read_b64_tr_b16 v[82:83], v232 offset:34816\n\t"
      "ds_read_b64_tr_b16 v[84:85], v233 offset:32768\n\t"
      "ds_read_b64_tr_b16 v[86:87], v233 offset:34816\n\t"
      "ds_read_b64_tr_b16 v[88:89], v234 offset:32768\n\t"
      "ds_read_b64_tr_b16 v[90:91], v234 offset:34816\n\t"
      "ds_read_b64_tr_b16 v[92:93], v235 offset:32768\n\t"
      "ds_read_b64_tr_b16 v[94:95], v235 offset:34816\n\t"
      "s_waitcnt vmcnt(0)\n\t"
      "ds_read_b64_tr_b16 v[96:97], v236\n\t"
      "ds_read_b64_tr_b16 v[98:99], v236 offset:2048\n\t"
      "ds_read_b64_tr_b16 v[100:101], v237\n\t"
      "ds_read_b64_tr_b16 v[102:103], v237 offset:2048\n\t"
      "ds_read_b64_tr_b16 v[104:105], v238\n\t"
      "ds_read_b64_tr_b16 v[106:107], v238 offset:2048\n\t"
      "ds_read_b64_tr_b16 v[108:109], v239\n\t"
      "ds_read_b64_tr_b16 v[110:111], v239 offset:2048\n\t"
      "v_lshlrev_b32_e32 v249, 16, v249\n\t"
      "s_and_b32 s34, s29, 30\n\t"
      "s_lshl_b32 s34, s34, 10\n\t"
      "s_add_u32 s34, s34, 98304\n\t"
      "v_bfe_u32 v143, v244, 5, 2\n\t"
      "v_add_u32_e32 v143, 3, v143\n\t"
      "v_writelane_b32 v143, s32, 0\n\t"
      "v_writelane_b32 v143, s32, 1\n\t"
      "v_lshl_add_u32 v143, v143, 7, s34\n\t"
      "v_and_b32_e32 v142, 16, v244\n\t"
      "v_lshl_add_u32 v143, v142, 6, v143\n\t"
      "s_waitcnt vmcnt(0) lgkmcnt(0)\n\t"
      "s_cmp_lg_u32 s24, 0\n\t"
      "s_cbranch_scc1 PGB_%=\n\t"
      "v_cmp_eq_u32_e32 vcc, -1, v251\n\t"
      "s_cbranch_vccnz PGR_%=\n\t"
      "PGB_%=:\n\t"
      "s_barrier\n\t"
      "s_cmp_lg_u32 s24, 3\n\t"
      "s_cbranch_scc1 SK19_%=\n\t"
      "global_load_dwordx2 v[240:241], v143, s[60:61] sc1\n\t"
      "SK19_%=:\n\t"
      "v_mov_b32_e32 v242, v143\n\t"
      "s_add_u32 s58, s64, s40\n\t"
      "s_addc_u32 s59, s65, 0\n\t"
      "global_load_dwordx4 v[128:131], v244, s[58:59] sc1\n\t"
      "s_add_u32 s58, s64, s41\n\t"
      "s_addc_u32 s59, s65, 0\n\t"
      "global_load_dwordx4 v[132:135], v244, s[58:59] sc1\n\t"
      "s_add_u32 s58, s64, s42\n\t"
      "s_addc_u32 s59, s65, 0\n\t"
      "global_load_dwordx4 v[136:139], v244, s[58:59] sc1\n\t"
      "s_add_u32 s58, s64, s43\n\t"
      "s_addc_u32 s59, s65, 0\n\t"
      "global_load_dwordx4 v[140:143], v244, s[58:59] sc1\n\t"
      "s_add_u32 s58, s64, s44\n\t"
      "s_addc_u32 s59, s65, 0\n\t"
      "global_load_dwordx4 v[144:147], v244, s[58:59] sc1\n\t"
      "s_add_u32 s58, s64, s45\n\t"
      "s_addc_u32 s59, s65, 0\n\t"
      "global_load_dwordx4 v[148:151], v244, s[58:59] sc1\n\t"
      "s_add_u32 s58, s64, s46\n\t"
      "s_addc_u32 s59, s65, 0\n\t"
      "global_load_dwordx4 v[152:155], v244, s[58:59] sc1\n\t"
      "v_mov_b32_e32 v254, v249\n\t"
      "s_waitcnt vmcnt(6)\n\t"
      "v_mfma_f32_16x16x32_bf16 v[196:199], v[128:131], v[0:3], 0\n\t"
      "v_mfma_f32_16x16x32_bf16 v[200:203], v[128:131], v[4:7], 0\n\t"
      "v_mfma_f32_16x16x32_bf16 v[204:207], v[128:131], v[8:11], 0\n\t"
      "v_mfma_f32_16x16x32_bf16 v[208:211], v[128:131], v[12:15], 0\n\t"
      "s_waitcnt vmcnt(5)\n\t"
      "v_mfma_f32_16x16x32_bf16 v[196:199], v[132:135], v[16:19], v[196:199]\n\t"
      "v_mfma_f32_16x16x32_bf16 v[200:203], v[132:135], v[20:23], v[200:203]\n\t"
      "v_mfma_f32_16x16x32_bf16 v[204:207], v[132:135], v[24:27], v[204:207]\n\t"
      "s_cmp_lg_u32 s24, 3\n\t"
      "s_cbranch_scc1 LF8B_%=\n\t"
      "v_cmp_ne_u64_e32 vcc, s[22:23], v[240:241]\n\t"
      "s_cbranch_vccnz LF8R_%=\n\t"
      "LF8B_%=:\n\t"
      "s_barrier\n\t"
      "v_mfma_f32_16x16x32_bf16 v[208:211], v[132:135], v[28:31], v[208:211]\n\t"
      "global_load_dword v115, v248, s[50:51] offset:-4096 sc1\n\t"
      "global_load_dword v116, v248, s[50:51] offset:-2048 sc1\n\t"
      "s_waitcnt vmcnt(6)\n\t"
      "v_mfma_f32_16x16x32_bf16 v[196:199], v[136:139], v[32:35], v[196:199]\n\t"
      "global_load_dword v117, v248, s[50:51] sc1\n\t"
      "global_load_dword v118, v248, s[50:51] offset:2048 sc1\n\t"
      "v_mfma_f32_16x16x32_bf16 v[200:203], v[136:139], v[36:39], v[200:203]\n\t"
      "s_sub_u32 s34, 16, s32\n\t"
      "s_lshl_b32 s34, s34, 11\n\t"
      "s_add_u32 s50, s50, s34\n\t"
      "s_addc_u32 s51, s51, 0\n\t"
      "global_load_dword v112, v248, s[50:51] offset:-4096 sc1\n\t"
      "global_load_dword v113, v248, s[50:51] offset:-2048 sc1\n\t"
      "v_mfma_f32_16x16x32_bf16 v[204:207], v[136:139], v[40:43], v[204:207]\n\t"
      "global_load_dword v114, v248, s[50:51] sc1\n\t"
      "s_add_u32 s58, s66, s40\n\t"
      "s_addc_u32 s59, s67, 0\n\t"
      "global_load_dwordx4 v[156:159], v244, s[58:59] sc1\n\t"
      "v_mfma_f32_16x16x32_bf16 v[208:211], v[136:139], v[44:47], v[208:211]\n\t"
      "s_waitcnt vmcnt(11)\n\t"
      "v_mfma_f32_16x16x32_bf16 v[196:199], v[140:143], v[48:51], v[196:199]\n\t"
      "v_mfma_f32_16x16x32_bf16 v[200:203], v[140:143], v[52:55], v[200:203]\n\t"
      "v_mfma_f32_16x16x32_bf16 v[204:207], v[140:143], v[56:59], v[204:207]\n\t"
      "s_add_u32 s58, s66, s41\n\t"
      "s_addc_u32 s59, s67, 0\n\t"
      "global_load_dwordx4 v[160:163], v244, s[58:59] sc1\n\t"
      "v_mfma_f32_16x16x32_bf16 v[208:211], v[140:143], v[60:63], v[208:211]\n\t"
      "s_waitcnt vmcnt(11)\n\t"
      "v_mfma_f32_16x16x32_bf16 v[196:199], v[144:147], v[64:67], v[196:199]\n\t"
      "v_mfma_f32_16x16x32_bf16 v[200:203], v[144:147], v[68:71], v[200:203]\n\t"
      "v_mfma_f32_16x16x32_bf16 v[204:207], v[144:147], v[72:75], v[204:207]\n\t"
      "s_add_u32 s58, s66, s42\n\t"
      "s_addc_u32 s59, s67, 0\n\t"
      "global_load_dwordx4 v[164:167], v244, s[58:59] sc1\n\t"
      "v_mfma_f32_16x16x32_bf16 v[208:211], v[144:147], v[76:79], v[208:211]\n\t"
      "s_waitcnt vmcnt(11)\n\t"
      "v_mfma_f32_16x16x32_bf16 v[196:199], v[148:151], v[80:83], v[196:199]\n\t"
      "v_mfma_f32_16x16x32_bf16 v[200:203], v[148:151], v[84:87], v[200:203]\n\t"
      "v_mfma_f32_16x16x32_bf16 v[204:207], v[148:151], v[88:91], v[204:207]\n\t"
      "s_add_u32 s58, s66, s43\n\t"
      "s_addc_u32 s59, s67, 0\n\t"
      "global_load_dwordx4 v[168:171], v244, s[58:59] sc1\n\t"
      "v_mfma_f32_16x16x32_bf16 v[208:211], v[148:151], v[92:95], v[208:211]\n\t"
      "s_waitcnt vmcnt(11)\n\t"
      "v_mfma_f32_16x16x32_bf16 v[196:199], v[152:155], v[96:99], v[196:199]\n\t"
      "v_mfma_f32_16x16x32_bf16 v[200:203], v[152:155], v[100:103], v[200:203]\n\t"
      "v_mfma_f32_16x16x32_bf16 v[204:207], v[152:155], v[104:107], v[204:207]\n\t"
      "s_add_u32 s58, s66, s44\n\t"
      "s_addc_u32 s59, s67, 0\n\t"
      "global_load_dwordx4 v[172:175], v244, s[58:59] sc1\n\t"
      "v_mfma_f32_16x16x32_bf16 v[208:211], v[152:155], v[108:111], v[208:211]\n\t"
      "s_nop 1\n\t"
      "v_add_f32_e32 v251, v196, v197\n\t"
      "v_add_f32_e32 v251, v251, v198\n\t"
      "v_cmp_u_f32_e32 vcc, v251, v199\n\t"
      "s_cbranch_vccnz LA40R_%=\n\t"
      "LA40B_%=:\n\t"
      "ds_write_b128 v245, v[196:199]\n\t"
      "s_waitcnt vmcnt(4)\n\t"
      "v_mfma_f32_16x16x32_bf16 v[212:215], v[156:159], v[0:3], 0\n\t"
      "ds_write_b128 v245, v[200:203] offset:1024\n\t"
      "v_mfma_f32_16x16x32_bf16 v[216:219], v[156:159], v[4:7], 0\n\t"
      "ds_write_b128 v245, v[204:207] offset:2048\n\t"
      "v_mfma_f32_16x16x32_bf16 v[220:223], v[156:159], v[8:11], 0\n\t"
      "ds_write_b128 v245, v[208:211] offset:3072\n\t"
      "s_add_u32 s58, s66, s45\n\t"
      "s_addc_u32 s59, s67, 0\n\t"
      "global_load_dwordx4 v[176:179], v244, s[58:59] sc1\n\t"
      "v_mfma_f32_16x16x32_bf16 v[224:227], v[156:159], v[12:15], 0\n\t"
      "s_waitcnt vmcnt(4)\n\t"
      "v_mfma_f32_16x16x32_bf16 v[212:215], v[160:163], v[16:19], v[212:215]\n\t"
      "v_mfma_f32_16x16x32_bf16 v[216:219], v[160:163], v[20:23], v[216:219]\n\t"
      "v_mfma_f32_16x16x32_bf16 v[220:223], v[160:163], v[24:27], v[220:223]\n\t"
      "s_waitcnt lgkmcnt(0)\n\t"
      "s_barrier\n\t"
      "ds_read_b64 v[228:229], v246\n\t"
      "ds_read_b64 v[230:231], v246 offset:4096\n\t"
      "s_add_u32 s58, s66, s46\n\t"
      "s_addc_u32 s59, s67, 0\n\t"
      "global_load_dwordx4 v[180:183], v244, s[58:59] sc1\n\t"
      "v_mfma_f32_16x16x32_bf16 v[224:227], v[160:163], v[28:31], v[224:227]\n\t"
      "ds_read_b64 v[232:233], v246 offset:8192\n\t"
      "ds_read_b64 v[234:235], v246 offset:12288\n\t"
      "s_waitcnt vmcnt(4)\n\t"
      "v_mfma_f32_16x16x32_bf16 v[212:215], v[164:167], v[32:35], v[212:215]\n\t"
      "ds_read_b64 v[236:237], v246 offset:16384\n\t"
      "ds_read_b64 v[238:239], v246 offset:20480\n\t"
      "v_mfma_f32_16x16x32_bf16 v[216:219], v[164:167], v[36:39], v[216:219]\n\t"
      "ds_read_b64 v[240:241], v246 offset:24576\n\t"
      "ds_read_b64 v[242:243], v246 offset:28672\n\t"
      "v_lshlrev_b32_e32 v248, 16, v112\n\t"
      "s_add_u32 s58, s68, s40\n\t"
      "s_addc_u32 s59, s69, 0\n\t"
      "global_load_dwordx4 v[184:187], v244, s[58:59] sc1\n\t"
      "v_mfma_f32_16x16x32_bf16 v[220:223], v[164:167], v[40:43], v[220:223]\n\t"
      "v_and_b32_e32 v249, 0xffff0000, v112\n\t"
      "v_mfma_f32_16x16x32_bf16 v[224:227], v[164:167], v[44:47], v[224:227]\n\t"
      "v_add_f32_e32 v248, v254, v248\n\t"
      "v_add_f32_e32 v249, v254, v249\n\t"
      "s_waitcnt vmcnt(4)\n\t"
      "v_mfma_f32_16x16x32_bf16 v[212:215], v[168:171], v[48:51], v[212:215]\n\t"
      "s_waitcnt lgkmcnt(4)\n\t"
      "v_mfma_f32_16x16x32_bf16 v[216:219], v[168:171], v[52:55], v[216:219]\n\t"
      "v_add_f32_e32 v228, v228, v230\n\t"
      "v_add_f32_e32 v232, v232, v234\n\t"
      "s_add_u32 s58, s68, s41\n\t"
      "s_addc_u32 s59, s69, 0\n\t"
      "global_load_dwordx4 v[188:191], v244, s[58:59] sc1\n\t"
      "v_mfma_f32_16x16x32_bf16 v[220:223], v[168:171], v[56:59], v[220:223]\n\t"
      "v_add_f32_e32 v229, v229, v231\n\t"
      "v_mfma_f32_16x16x32_bf16 v[224:227], v[168:171], v[60:63], v[224:227]\n\t"
      "v_add_f32_e32 v233, v233, v235\n\t"
      "v_add_f32_e32 v228, v228, v232\n\t"
      "s_waitcnt vmcnt(4)\n\t"
      "v_mfma_f32_16x16x32_bf16 v[212:215], v[172:175], v[64:67], v[212:215]\n\t"
      "v_add_f32_e32 v229, v229, v233\n\t"
      "v_mfma_f32_16x16x32_bf16 v[216:219], v[172:175], v[68:71], v[216:219]\n\t"
      "s_waitcnt lgkmcnt(0)\n\t"
      "v_add_f32_e32 v236, v236, v238\n\t"
      "s_add_u32 s58, s68, s42\n\t"
      "s_addc_u32 s59, s69, 0\n\t"
      "global_load_dwordx4 v[192:195], v244, s[58:59] sc1\n\t"
      "v_mfma_f32_16x16x32_bf16 v[220:223], v[172:175], v[72:75], v[220:223]\n\t"
      "v_add_f32_e32 v240, v240, v242\n\t"
      "v_mfma_f32_16x16x32_bf16 v[224:227], v[172:175], v[76:79], v[224:227]\n\t"
      "v_add_f32_e32 v237, v237, v239\n\t"
      "v_add_f32_e32 v241, v241, v243\n\t"
      "s_waitcnt vmcnt(4)\n\t"
      "v_mfma_f32_16x16x32_bf16 v[212:215], v[176:179], v[80:83], v[212:215]\n\t"
      "v_add_f32_e32 v228, v228, v248\n\t"
      "v_mfma_f32_16x16x32_bf16 v[216:219], v[176:179], v[84:87], v[216:219]\n\t"
      "v_add_f32_e32 v236, v236, v240\n\t"
      "v_add_f32_e32 v229, v229, v249\n\t"
      "s_add_u32 s58, s68, s43\n\t"
      "s_addc_u32 s59, s69, 0\n\t"
      "global_load_dwordx4 v[128:131], v244, s[58:59] sc1\n\t"
      "v_mfma_f32_16x16x32_bf16 v[220:223], v[176:179], v[88:91], v[220:223]\n\t"
      "v_add_f32_e32 v237, v237, v241\n\t"
      "v_mfma_f32_16x16x32_bf16 v[224:227], v[176:179], v[92:95], v[224:227]\n\t"
      "v_add_f32_e32 v228, v228, v236\n\t"
      "v_add_f32_e32 v229, v229, v237\n\t"
      "s_waitcnt vmcnt(4)\n\t"
      "v_mfma_f32_16x16x32_bf16 v[212:215], v[180:183], v[96:99], v[212:215]\n\t"
      "v_cvt_pk_bf16_f32 v232, v228, v229\n\t"
      "v_mfma_f32_16x16x32_bf16 v[216:219], v[180:183], v[100:103], v[216:219]\n\t"
      "s_mov_b32 vcc_lo, 0xeeeeeeee\n\t"
      "s_mov_b32 vcc_hi, 0xeeeeeeee\n\t"
      "v_cndmask_b32_dpp v120, v232, v120, vcc quad_perm:[0,0,0,0] row_mask:0xf bank_mask:0xf\n\t"
      "v_cndmask_b32_dpp v121, v232, v121, vcc quad_perm:[1,1,1,1] row_mask:0xf bank_mask:0xf\n\t"
      "v_cndmask_b32_dpp v122, v232, v122, vcc quad_perm:[2,2,2,2] row_mask:0xf bank_mask:0xf\n\t"
      "v_cndmask_b32_dpp v123, v232, v123, vcc quad_perm:[3,3,3,3] row_mask:0xf bank_mask:0xf\n\t"
      "s_add_u32 s58, s68, s44\n\t"
      "s_addc_u32 s59, s69, 0\n\t"
      "global_load_dwordx4 v[132:135], v244, s[58:59] sc1\n\t"
      "v_mfma_f32_16x16x32_bf16 v[220:223], v[180:183], v[104:107], v[220:223]\n\t"
      "v_mfma_f32_16x16x32_bf16 v[224:227], v[180:183], v[108:111], v[224:227]\n\t"
      "v_add_f32_e32 v251, v212, v213\n\t"
      "v_add_f32_e32 v251, v251, v214\n\t"
      "v_cmp_u_f32_e32 vcc, v251, v215\n\t"
      "s_cbranch_vccnz LA41R_%=\n\t"
      "LA41B_%=:\n\t"
      "ds_write_b128 v245, v[212:215] offset:32768\n\t"
      "s_waitcnt vmcnt(4)\n\t"
      "v_mfma_f32_16x16x32_bf16 v[196:199], v[184:187], v[0:3], 0\n\t"
      "ds_write_b128 v245, v[216:219] offset:33792\n\t"
      "v_mfma_f32_16x16x32_bf16 v[200:203], v[184:187], v[4:7], 0\n\t"
      "ds_write_b128 v245, v[220:223] offset:34816\n\t"
      "s_add_u32 s58, s68, s45\n\t"
      "s_addc_u32 s59, s69, 0\n\t"
      "global_load_dwordx4 v[136:139], v244, s[58:59] sc1\n\t"
      "v_mfma_f32_16x16x32_bf16 v[204:207], v[184:187], v[8:11], 0\n\t"
      "ds_write_b128 v245, v[224:227] offset:35840\n\t"
      "v_mfma_f32_16x16x32_bf16 v[208:211], v[184:187], v[12:15], 0\n\t"
      "s_waitcnt vmcnt(4)\n\t"
      "v_mfma_f32_16x16x32_bf16 v[196:199], v[188:191], v[16:19], v[196:199]\n\t"
      "v_mfma_f32_16x16x32_bf16 v[200:203], v[188:191], v[20:23], v[200:203]\n\t"
      "s_add_u32 s58, s68, s46\n\t"
      "s_addc_u32 s59, s69, 0\n\t"
      "global_load_dwordx4 v[140:143], v244, s[58:59] sc1\n\t"
      "v_mfma_f32_16x16x32_bf16 v[204:207], v[188:191], v[24:27], v[204:207]\n\t"
      "s_waitcnt lgkmcnt(0)\n\t"
      "s_barrier\n\t"
      "ds_read_b64 v[228:229], v246 offset:32768\n\t"
      "ds_read_b64 v[230:231], v246 offset:36864\n\t"
      "v_mfma_f32_16x16x32_bf16 v[208:211], v[188:191], v[28:31], v[208:211]\n\t"
      "ds_read_b64 v[232:233], v246 offset:40960\n\t"
      "ds_read_b64 v[234:235], v246 offset:45056\n\t"
      "s_waitcnt vmcnt(4)\n\t"
      "v_mfma_f32_16x16x32_bf16 v[196:199], v[192:195], v[32:35], v[196:199]\n\t"
      "ds_read_b64 v[236:237], v246 offset:49152\n\t"
      "ds_read_b64 v[238:239], v246 offset:53248\n\t"
      "v_mfma_f32_16x16x32_bf16 v[200:203], v[192:195], v[36:39], v[200:203]\n\t"
      "ds_read_b64 v[240:241], v246 offset:57344\n\t"
      "ds_read_b64 v[242:243], v246 offset:61440\n\t"
      "v_lshlrev_b32_e32 v248, 16, v113\n\t"
      "s_add_u32 s58, s70, s40\n\t"
      "s_addc_u32 s59, s71, 0\n\t"
      "global_load_dwordx4 v[144:147], v244, s[58:59] sc1\n\t"
      "v_mfma_f32_16x16x32_bf16 v[204:207], v[192:195], v[40:43], v[204:207]\n\t"
      "v_and_b32_e32 v249, 0xffff0000, v113\n\t"
      "v_mfma_f32_16x16x32_bf16 v[208:211], v[192:195], v[44:47], v[208:211]\n\t"
      "v_add_f32_e32 v248, v254, v248\n\t"
      "v_add_f32_e32 v249, v254, v249\n\t"
      "s_waitcnt vmcnt(4)\n\t"
      "v_mfma_f32_16x16x32_bf16 v[196:199], v[128:131], v[48:51], v[196:199]\n\t"
      "s_waitcnt lgkmcnt(4)\n\t"
      "v_mfma_f32_16x16x32_bf16 v[200:203], v[128:131], v[52:55], v[200:203]\n\t"
      "v_add_f32_e32 v228, v228, v230\n\t"
      "v_add_f32_e32 v232, v232, v234\n\t"
      "s_add_u32 s58, s70, s41\n\t"
      "s_addc_u32 s59, s71, 0\n\t"
      "global_load_dwordx4 v[148:151], v244, s[58:59] sc1\n\t"
      "v_mfma_f32_16x16x32_bf16 v[204:207], v[128:131], v[56:59], v[204:207]\n\t"
      "v_add_f32_e32 v229, v229, v231\n\t"
      "v_mfma_f32_16x16x32_bf16 v[208:211], v[128:131], v[60:63], v[208:211]\n\t"
      "v_add_f32_e32 v233, v233, v235\n\t"
      "v_add_f32_e32 v228, v228, v232\n\t"
      "s_waitcnt vmcnt(4)\n\t"
      "v_mfma_f32_16x16x32_bf16 v[196:199], v[132:135], v[64:67], v[196:199]\n\t"
      "v_add_f32_e32 v229, v229, v233\n\t"
      "v_mfma_f32_16x16x32_bf16 v[200:203], v[132:135], v[68:71], v[200:203]\n\t"
      "s_waitcnt lgkmcnt(0)\n\t"
      "v_add_f32_e32 v236, v236, v238\n\t"
      "s_add_u32 s58, s70, s42\n\t"
      "s_addc_u32 s59, s71, 0\n\t"
      "global_load_dwordx4 v[152:155], v244, s[58:59] sc1\n\t"
      "v_mfma_f32_16x16x32_bf16 v[204:207], v[132:135], v[72:75], v[204:207]\n\t"
      "v_add_f32_e32 v240, v240, v242\n\t"
      "v_mfma_f32_16x16x32_bf16 v[208:211], v[132:135], v[76:79], v[208:211]\n\t"
      "v_add_f32_e32 v237, v237, v239\n\t"
      "v_add_f32_e32 v241, v241, v243\n\t"
      "s_waitcnt vmcnt(4)\n\t"
      "v_mfma_f32_16x16x32_bf16 v[196:199], v[136:139], v[80:83], v[196:199]\n\t"
      "v_add_f32_e32 v228, v228, v248\n\t"
      "v_mfma_f32_16x16x32_bf16 v[200:203], v[136:139], v[84:87], v[200:203]\n\t"
      "v_add_f32_e32 v236, v236, v240\n\t"
      "v_add_f32_e32 v229, v229, v249\n\t"
      "s_add_u32 s58, s70, s43\n\t"
      "s_addc_u32 s59, s71, 0\n\t"
      "global_load_dwordx4 v[156:159], v244, s[58:59] sc1\n\t"
      "v_mfma_f32_16x16x32_bf16 v[204:207], v[136:139], v[88:91], v[204:207]\n\t"
      "v_add_f32_e32 v237, v237, v241\n\t"
      "v_mfma_f32_16x16x32_bf16 v[208:211], v[136:139], v[92:95], v[208:211]\n\t"
      "v_add_f32_e32 v228, v228, v236\n\t"
      "v_add_f32_e32 v229, v229, v237\n\t"
      "s_waitcnt vmcnt(4)\n\t"
      "v_mfma_f32_16x16x32_bf16 v[196:199], v[140:143], v[96:99], v[196:199]\n\t"
      "v_cvt_pk_bf16_f32 v232, v228, v229\n\t"
      "v_mfma_f32_16x16x32_bf16 v[200:203], v[140:143], v[100:103], v[200:203]\n\t"
      "s_mov_b32 vcc_lo, 0xdddddddd\n\t"
      "s_mov_b32 vcc_hi, 0xdddddddd\n\t"
      "v_cndmask_b32_dpp v120, v232, v120, vcc quad_perm:[0,0,0,0] row_mask:0xf bank_mask:0xf\n\t"
      "v_cndmask_b32_dpp v121, v232, v121, vcc quad_perm:[1,1,1,1] row_mask:0xf bank_mask:0xf\n\t"
      "v_cndmask_b32_dpp v122, v232, v122, vcc quad_perm:[2,2,2,2] row_mask:0xf bank_mask:0xf\n\t"
      "v_cndmask_b32_dpp v123, v232, v123, vcc quad_perm:[3,3,3,3] row_mask:0xf bank_mask:0xf\n\t"
      "s_add_u32 s58, s70, s44\n\t"
      "s_addc_u32 s59, s71, 0\n\t"
      "global_load_dwordx4 v[160:163], v244, s[58:59] sc1\n\t"
      "v_mfma_f32_16x16x32_bf16 v[204:207], v[140:143], v[104:107], v[204:207]\n\t"
      "v_mfma_f32_16x16x32_bf16 v[208:211], v[140:143], v[108:111], v[208:211]\n\t"
      "v_add_f32_e32 v251, v196, v197\n\t"
      "v_add_f32_e32 v251, v251, v198\n\t"
      "v_cmp_u_f32_e32 vcc, v251, v199\n\t"
      "s_cbranch_vccnz LA42R_%=\n\t"
      "LA42B_%=:\n\t"
      "ds_write_b128 v245, v[196:199]\n\t"
      "s_waitcnt vmcnt(4)\n\t"
      "v_mfma_f32_16x16x32_bf16 v[212:215], v[144:147], v[0:3], 0\n\t"
      "ds_write_b128 v245, v[200:203] offset:1024\n\t"
      "v_mfma_f32_16x16x32_bf16 v[216:219], v[144:147], v[4:7], 0\n\t"
      "ds_write_b128 v245, v[204:207] offset:2048\n\t"
      "s_add_u32 s58, s70, s45\n\t"
      "s_addc_u32 s59, s71, 0\n\t"
      "global_load_dwordx4 v[164:167], v244, s[58:59] sc1\n\t"
      "v_mfma_f32_16x16x32_bf16 v[220:223], v[144:147], v[8:11], 0\n\t"
      "ds_write_b128 v245, v[208:211] offset:3072\n\t"
      "v_mfma_f32_16x16x32_bf16 v[224:227], v[144:147], v[12:15], 0\n\t"
      "s_waitcnt vmcnt(4)\n\t"
      "v_mfma_f32_16x16x32_bf16 v[212:215], v[148:151], v[16:19], v[212:215]\n\t"
      "v_mfma_f32_16x16x32_bf16 v[216:219], v[148:151], v[20:23], v[216:219]\n\t"
      "s_add_u32 s58, s70, s46\n\t"
      "s_addc_u32 s59, s71, 0\n\t"
      "global_load_dwordx4 v[168:171], v244, s[58:59] sc1\n\t"
      "v_mfma_f32_16x16x32_bf16 v[220:223], v[148:151], v[24:27], v[220:223]\n\t"
      "s_waitcnt lgkmcnt(0)\n\t"
      "s_barrier\n\t"
      "ds_read_b64 v[228:229], v246\n\t"
      "ds_read_b64 v[230:231], v246 offset:4096\n\t"
      "v_mfma_f32_16x16x32_bf16 v[224:227], v[148:151], v[28:31], v[224:227]\n\t"
      "ds_read_b64 v[232:233], v246 offset:8192\n\t"
      "ds_read_b64 v[234:235], v246 offset:12288\n\t"
      "s_waitcnt vmcnt(4)\n\t"
      "v_mfma_f32_16x16x32_bf16 v[212:215], v[152:155], v[32:35], v[212:215]\n\t"
      "ds_read_b64 v[236:237], v246 offset:16384\n\t"
      "ds_read_b64 v[238:239], v246 offset:20480\n\t"
      "v_mfma_f32_16x16x32_bf16 v[216:219], v[152:155], v[36:39], v[216:219]\n\t"
      "ds_read_b64 v[240:241], v246 offset:24576\n\t"
      "ds_read_b64 v[242:243], v246 offset:28672\n\t"
      "v_lshlrev_b32_e32 v248, 16, v114\n\t"
      "s_add_u32 s58, s72, s40\n\t"
      "s_addc_u32 s59, s73, 0\n\t"
      "global_load_dwordx4 v[172:175], v244, s[58:59] sc1\n\t"
      "v_mfma_f32_16x16x32_bf16 v[220:223], v[152:155], v[40:43], v[220:223]\n\t"
      "v_and_b32_e32 v249, 0xffff0000, v114\n\t"
      "v_mfma_f32_16x16x32_bf16 v[224:227], v[152:155], v[44:47], v[224:227]\n\t"
      "v_add_f32_e32 v248, v254, v248\n\t"
      "v_add_f32_e32 v249, v254, v249\n\t"
      "s_waitcnt vmcnt(4)\n\t"
      "v_mfma_f32_16x16x32_bf16 v[212:215], v[156:159], v[48:51], v[212:215]\n\t"
      "s_waitcnt lgkmcnt(4)\n\t"
      "v_mfma_f32_16x16x32_bf16 v[216:219], v[156:159], v[52:55], v[216:219]\n\t"
      "v_add_f32_e32 v228, v228, v230\n\t"
      "v_add_f32_e32 v232, v232, v234\n\t"
      "s_add_u32 s58, s72, s41\n\t"
      "s_addc_u32 s59, s73, 0\n\t"
      "global_load_dwordx4 v[176:179], v244, s[58:59] sc1\n\t"
      "v_mfma_f32_16x16x32_bf16 v[220:223], v[156:159], v[56:59], v[220:223]\n\t"
      "v_add_f32_e32 v229, v229, v231\n\t"
      "v_mfma_f32_16x16x32_bf16 v[224:227], v[156:159], v[60:63], v[224:227]\n\t"
      "v_add_f32_e32 v233, v233, v235\n\t"
      "v_add_f32_e32 v228, v228, v232\n\t"
      "s_waitcnt vmcnt(4)\n\t"
      "v_mfma_f32_16x16x32_bf16 v[212:215], v[160:163], v[64:67], v[212:215]\n\t"
      "v_add_f32_e32 v229, v229, v233\n\t"
      "v_mfma_f32_16x16x32_bf16 v[216:219], v[160:163], v[68:71], v[216:219]\n\t"
      "s_waitcnt lgkmcnt(0)\n\t"
      "v_add_f32_e32 v236, v236, v238\n\t"
      "s_add_u32 s58, s72, s42\n\t"
      "s_addc_u32 s59, s73, 0\n\t"
      "global_load_dwordx4 v[180:183], v244, s[58:59] sc1\n\t"
      "v_mfma_f32_16x16x32_bf16 v[220:223], v[160:163], v[72:75], v[220:223]\n\t"
      "v_add_f32_e32 v240, v240, v242\n\t"
      "v_mfma_f32_16x16x32_bf16 v[224:227], v[160:163], v[76:79], v[224:227]\n\t"
      "v_add_f32_e32 v237, v237, v239\n\t"
      "v_add_f32_e32 v241, v241, v243\n\t"
      "s_waitcnt vmcnt(4)\n\t"
      "v_mfma_f32_16x16x32_bf16 v[212:215], v[164:167], v[80:83], v[212:215]\n\t"
      "v_add_f32_e32 v228, v228, v248\n\t"
      "v_mfma_f32_16x16x32_bf16 v[216:219], v[164:167], v[84:87], v[216:219]\n\t"
      "v_add_f32_e32 v236, v236, v240\n\t"
      "v_add_f32_e32 v229, v229, v249\n\t"
      "s_add_u32 s58, s72, s43\n\t"
      "s_addc_u32 s59, s73, 0\n\t"
      "global_load_dwordx4 v[184:187], v244, s[58:59] sc1\n\t"
      "v_mfma_f32_16x16x32_bf16 v[220:223], v[164:167], v[88:91], v[220:223]\n\t"
      "v_add_f32_e32 v237, v237, v241\n\t"
      "v_mfma_f32_16x16x32_bf16 v[224:227], v[164:167], v[92:95], v[224:227]\n\t"
      "v_add_f32_e32 v228, v228, v236\n\t"
      "v_add_f32_e32 v229, v229, v237\n\t"
      "s_waitcnt vmcnt(4)\n\t"
      "v_mfma_f32_16x16x32_bf16 v[212:215], v[168:171], v[96:99], v[212:215]\n\t"
      "v_cvt_pk_bf16_f32 v232, v228, v229\n\t"
      "v_mfma_f32_16x16x32_bf16 v[216:219], v[168:171], v[100:103], v[216:219]\n\t"
      "s_mov_b32 vcc_lo, 0xbbbbbbbb\n\t"
      "s_mov_b32 vcc_hi, 0xbbbbbbbb\n\t"
      "v_cndmask_b32_dpp v120, v232, v120, vcc quad_perm:[0,0,0,0] row_mask:0xf bank_mask:0xf\n\t"
      "v_cndmask_b32_dpp v121, v232, v121, vcc quad_perm:[1,1,1,1] row_mask:0xf bank_mask:0xf\n\t"
      "v_cndmask_b32_dpp v122, v232, v122, vcc quad_perm:[2,2,2,2] row_mask:0xf bank_mask:0xf\n\t"
      "v_cndmask_b32_dpp v123, v232, v123, vcc quad_perm:[3,3,3,3] row_mask:0xf bank_mask:0xf\n\t"
      "s_add_u32 s58, s72, s44\n\t"
      "s_addc_u32 s59, s73, 0\n\t"
      "global_load_dwordx4 v[188:191], v244, s[58:59] sc1\n\t"
      "v_mfma_f32_16x16x32_bf16 v[220:223], v[168:171], v[104:107], v[220:223]\n\t"
      "v_mfma_f32_16x16x32_bf16 v[224:227], v[168:171], v[108:111], v[224:227]\n\t"
      "v_add_f32_e32 v251, v212, v213\n\t"
      "v_add_f32_e32 v251, v251, v214\n\t"
      "v_cmp_u_f32_e32 vcc, v251, v215\n\t"
      "s_cbranch_vccnz LA43R_%=\n\t"
      "LA43B_%=:\n\t"
      "ds_write_b128 v245, v[212:215] offset:32768\n\t"
      "s_waitcnt vmcnt(4)\n\t"
      "v_mfma_f32_16x16x32_bf16 v[196:199], v[172:175], v[0:3], 0\n\t"
      "ds_write_b128 v245, v[216:219] offset:33792\n\t"
      "v_mfma_f32_16x16x32_bf16 v[200:203], v[172:175], v[4:7], 0\n\t"
      "ds_write_b128 v245, v[220:223] offset:34816\n\t"
      "s_add_u32 s58, s72, s45\n\t"
      "s_addc_u32 s59, s73, 0\n\t"
      "global_load_dwordx4 v[192:195], v244, s[58:59] sc1\n\t"
      "v_mfma_f32_16x16x32_bf16 v[204:207], v[172:175], v[8:11], 0\n\t"
      "ds_write_b128 v245, v[224:227] offset:35840\n\t"
      "v_mfma_f32_16x16x32_bf16 v[208:211], v[172:175], v[12:15], 0\n\t"
      "s_waitcnt vmcnt(4)\n\t"
      "v_mfma_f32_16x16x32_bf16 v[196:199], v[176:179], v[16:19], v[196:199]\n\t"
      "v_mfma_f32_16x16x32_bf16 v[200:203], v[176:179], v[20:23], v[200:203]\n\t"
      "s_add_u32 s58, s72, s46\n\t"
      "s_addc_u32 s59, s73, 0\n\t"
      "global_load_dwordx4 v[128:131], v244, s[58:59] sc1\n\t"
      "v_mfma_f32_16x16x32_bf16 v[204:207], v[176:179], v[24:27], v[204:207]\n\t"
      "s_waitcnt lgkmcnt(0)\n\t"
      "s_barrier\n\t"
      "ds_read_b64 v[228:229], v246 offset:32768\n\t"
      "ds_read_b64 v[230:231], v246 offset:36864\n\t"
      "v_mfma_f32_16x16x32_bf16 v[208:211], v[176:179], v[28:31], v[208:211]\n\t"
      "ds_read_b64 v[232:233], v246 offset:40960\n\t"
      "ds_read_b64 v[234:235], v246 offset:45056\n\t"
      "s_waitcnt vmcnt(4)\n\t"
      "v_mfma_f32_16x16x32_bf16 v[196:199], v[180:183], v[32:35], v[196:199]\n\t"
      "ds_read_b64 v[236:237], v246 offset:49152\n\t"
      "ds_read_b64 v[238:239], v246 offset:53248\n\t"
      "v_mfma_f32_16x16x32_bf16 v[200:203], v[180:183], v[36:39], v[200:203]\n\t"
      "ds_read_b64 v[240:241], v246 offset:57344\n\t"
      "ds_read_b64 v[242:243], v246 offset:61440\n\t"
      "v_lshlrev_b32_e32 v248, 16, v115\n\t"
      "s_add_u32 s58, s74, s40\n\t"
      "s_addc_u32 s59, s75, 0\n\t"
      "global_load_dwordx4 v[132:135], v244, s[58:59] sc1\n\t"
      "v_mfma_f32_16x16x32_bf16 v[204:207], v[180:183], v[40:43], v[204:207]\n\t"
      "v_and_b32_e32 v249, 0xffff0000, v115\n\t"
      "v_mfma_f32_16x16x32_bf16 v[208:211], v[180:183], v[44:47], v[208:211]\n\t"
      "v_add_f32_e32 v248, v254, v248\n\t"
      "v_add_f32_e32 v249, v254, v249\n\t"
      "s_waitcnt vmcnt(4)\n\t"
      "v_mfma_f32_16x16x32_bf16 v[196:199], v[184:187], v[48:51], v[196:199]\n\t"
      "s_waitcnt lgkmcnt(4)\n\t"
      "v_mfma_f32_16x16x32_bf16 v[200:203], v[184:187], v[52:55], v[200:203]\n\t"
      "v_add_f32_e32 v228, v228, v230\n\t"
      "v_add_f32_e32 v232, v232, v234\n\t"
      "s_add_u32 s58, s74, s41\n\t"
      "s_addc_u32 s59, s75, 0\n\t"
      "global_load_dwordx4 v[136:139], v244, s[58:59] sc1\n\t"
      "v_mfma_f32_16x16x32_bf16 v[204:207], v[184:187], v[56:59], v[204:207]\n\t"
      "v_add_f32_e32 v229, v229, v231\n\t"
      "v_mfma_f32_16x16x32_bf16 v[208:211], v[184:187], v[60:63], v[208:211]\n\t"
      "v_add_f32_e32 v233, v233, v235\n\t"
      "v_add_f32_e32 v228, v228, v232\n\t"
      "s_waitcnt vmcnt(4)\n\t"
      "v_mfma_f32_16x16x32_bf16 v[196:199], v[188:191], v[64:67], v[196:199]\n\t"
      "v_add_f32_e32 v229, v229, v233\n\t"
      "v_mfma_f32_16x16x32_bf16 v[200:203], v[188:191], v[68:71], v[200:203]\n\t"
      "s_waitcnt lgkmcnt(0)\n\t"
      "v_add_f32_e32 v236, v236, v238\n\t"
      "s_add_u32 s58, s74, s42\n\t"
      "s_addc_u32 s59, s75, 0\n\t"
      "global_load_dwordx4 v[140:143], v244, s[58:59] sc1\n\t"
      "v_mfma_f32_16x16x32_bf16 v[204:207], v[188:191], v[72:75], v[204:207]\n\t"
      "v_add_f32_e32 v240, v240, v242\n\t"
      "v_mfma_f32_16x16x32_bf16 v[208:211], v[188:191], v[76:79], v[208:211]\n\t"
      "v_add_f32_e32 v237, v237, v239\n\t"
      "v_add_f32_e32 v241, v241, v243\n\t"
      "s_waitcnt vmcnt(4)\n\t"
      "v_mfma_f32_16x16x32_bf16 v[196:199], v[192:195], v[80:83], v[196:199]\n\t"
      "v_add_f32_e32 v228, v228, v248\n\t"
      "v_mfma_f32_16x16x32_bf16 v[200:203], v[192:195], v[84:87], v[200:203]\n\t"
      "v_add_f32_e32 v236, v236, v240\n\t"
      "v_add_f32_e32 v229, v229, v249\n\t"
      "s_add_u32 s58, s74, s43\n\t"
      "s_addc_u32 s59, s75, 0\n\t"
      "global_load_dwordx4 v[144:147], v244, s[58:59] sc1\n\t"
      "v_mfma_f32_16x16x32_bf16 v[204:207], v[192:195], v[88:91], v[204:207]\n\t"
      "v_add_f32_e32 v237, v237, v241\n\t"
      "v_mfma_f32_16x16x32_bf16 v[208:211], v[192:195], v[92:95], v[208:211]\n\t"
      "v_add_f32_e32 v228, v228, v236\n\t"
      "v_add_f32_e32 v229, v229, v237\n\t"
      "s_waitcnt vmcnt(4)\n\t"
      "v_mfma_f32_16x16x32_bf16 v[196:199], v[128:131], v[96:99], v[196:199]\n\t"
      "v_cvt_pk_bf16_f32 v232, v228, v229\n\t"
      "v_mfma_f32_16x16x32_bf16 v[200:203], v[128:131], v[100:103], v[200:203]\n\t"
      "s_mov_b32 vcc_lo, 0xeeeeeeee\n\t"
      "s_mov_b32 vcc_hi, 0xeeeeeeee\n\t"
      "v_cndmask_b32_dpp v124, v232, v124, vcc quad_perm:[0,0,0,0] row_mask:0xf bank_mask:0xf\n\t"
      "v_cndmask_b32_dpp v125, v232, v125, vcc quad_perm:[1,1,1,1] row_mask:0xf bank_mask:0xf\n\t"
      "v_cndmask_b32_dpp v126, v232, v126, vcc quad_perm:[2,2,2,2] row_mask:0xf bank_mask:0xf\n\t"
      "v_cndmask_b32_dpp v127, v232, v127, vcc quad_perm:[3,3,3,3] row_mask:0xf bank_mask:0xf\n\t"
      "s_add_u32 s58, s74, s44\n\t"
      "s_addc_u32 s59, s75, 0\n\t"
      "global_load_dwordx4 v[148:151], v244, s[58:59] sc1\n\t"
      "v_mfma_f32_16x16x32_bf16 v[204:207], v[128:131], v[104:107], v[204:207]\n\t"
      "v_mfma_f32_16x16x32_bf16 v[208:211], v[128:131], v[108:111], v[208:211]\n\t"
      "v_add_f32_e32 v251, v196, v197\n\t"
      "v_add_f32_e32 v251, v251, v198\n\t"
      "v_cmp_u_f32_e32 vcc, v251, v199\n\t"
      "s_cbranch_vccnz LA44R_%=\n\t"
      "LA44B_%=:\n\t"
      "ds_write_b128 v245, v[196:199]\n\t"
      "s_waitcnt vmcnt(4)\n\t"
      "v_mfma_f32_16x16x32_bf16 v[212:215], v[132:135], v[0:3], 0\n\t"
      "ds_write_b128 v245, v[200:203] offset:1024\n\t"
      "v_mfma_f32_16x16x32_bf16 v[216:219], v[132:135], v[4:7], 0\n\t"
      "ds_write_b128 v245, v[204:207] offset:2048\n\t"
      "s_add_u32 s58, s74, s45\n\t"
      "s_addc_u32 s59, s75, 0\n\t"
      "global_load_dwordx4 v[152:155], v244, s[58:59] sc1\n\t"
      "v_mfma_f32_16x16x32_bf16 v[220:223], v[132:135], v[8:11], 0\n\t"
      "ds_write_b128 v245, v[208:211] offset:3072\n\t"
      "v_mfma_f32_16x16x32_bf16 v[224:227], v[132:135], v[12:15], 0\n\t"
      "s_waitcnt vmcnt(4)\n\t"
      "v_mfma_f32_16x16x32_bf16 v[212:215], v[136:139], v[16:19], v[212:215]\n\t"
      "v_mfma_f32_16x16x32_bf16 v[216:219], v[136:139], v[20:23], v[216:219]\n\t"
      "s_add_u32 s58, s74, s46\n\t"
      "s_addc_u32 s59, s75, 0\n\t"
      "global_load_dwordx4 v[156:159], v244, s[58:59] sc1\n\t"
      "v_mfma_f32_16x16x32_bf16 v[220:223], v[136:139], v[24:27], v[220:223]\n\t"
      "s_waitcnt lgkmcnt(0)\n\t"
      "s_barrier\n\t"
      "ds_read_b64 v[228:229], v246\n\t"
      "ds_read_b64 v[230:231], v246 offset:4096\n\t"
      "v_mfma_f32_16x16x32_bf16 v[224:227], v[136:139], v[28:31], v[224:227]\n\t"
      "ds_read_b64 v[232:233], v246 offset:8192\n\t"
      "ds_read_b64 v[234:235], v246 offset:12288\n\t"
      "s_waitcnt vmcnt(4)\n\t"
      "v_mfma_f32_16x16x32_bf16 v[212:215], v[140:143], v[32:35], v[212:215]\n\t"
      "ds_read_b64 v[236:237], v246 offset:16384\n\t"
      "ds_read_b64 v[238:239], v246 offset:20480\n\t"
      "s_add_u32 s58, s76, s40\n\t"
      "s_addc_u32 s59, s77, 0\n\t"
      "global_load_dwordx4 v[160:163], v244, s[58:59] sc1\n\t"
      "v_mfma_f32_16x16x32_bf16 v[216:219], v[140:143], v[36:39], v[216:219]\n\t"
      "ds_read_b64 v[240:241], v246 offset:24576\n\t"
      "ds_read_b64 v[242:243], v246 offset:28672\n\t"
      "v_lshlrev_b32_e32 v248, 16, v116\n\t"
      "v_mfma_f32_16x16x32_bf16 v[220:223], v[140:143], v[40:43], v[220:223]\n\t"
      "v_and_b32_e32 v249, 0xffff0000, v116\n\t"
      "v_mfma_f32_16x16x32_bf16 v[224:227], v[140:143], v[44:47], v[224:227]\n\t"
      "v_add_f32_e32 v248, v254, v248\n\t"
      "v_add_f32_e32 v249, v254, v249\n\t"
      "s_waitcnt vmcnt(4)\n\t"
      "v_mfma_f32_16x16x32_bf16 v[212:215], v[144:147], v[48:51], v[212:215]\n\t"
      "s_waitcnt lgkmcnt(4)\n\t"
      "s_add_u32 s58, s76, s41\n\t"
      "s_addc_u32 s59, s77, 0\n\t"
      "global_load_dwordx4 v[164:167], v244, s[58:59] sc1\n\t"
      "v_mfma_f32_16x16x32_bf16 v[216:219], v[144:147], v[52:55], v[216:219]\n\t"
      "v_add_f32_e32 v228, v228, v230\n\t"
      "v_add_f32_e32 v232, v232, v234\n\t"
      "v_mfma_f32_16x16x32_bf16 v[220:223], v[144:147], v[56:59], v[220:223]\n\t"
      "v_add_f32_e32 v229, v229, v231\n\t"
      "v_mfma_f32_16x16x32_bf16 v[224:227], v[144:147], v[60:63], v[224:227]\n\t"
      "v_add_f32_e32 v233, v233, v235\n\t"
      "v_add_f32_e32 v228, v228, v232\n\t"
      "s_waitcnt vmcnt(4)\n\t"
      "v_mfma_f32_16x16x32_bf16 v[212:215], v[148:151], v[64:67], v[212:215]\n\t"
      "v_add_f32_e32 v229, v229, v233\n\t"
      "s_add_u32 s58, s76, s42\n\t"
      "s_addc_u32 s59, s77, 0\n\t"
      "global_load_dwordx4 v[168:171], v244, s[58:59] sc1\n\t"
      "v_mfma_f32_16x16x32_bf16 v[216:219], v[148:151], v[68:71], v[216:219]\n\t"
      "s_waitcnt lgkmcnt(0)\n\t"
      "v_add_f32_e32 v236, v236, v238\n\t"
      "v_mfma_f32_16x16x32_bf16 v[220:223], v[148:151], v[72:75], v[220:223]\n\t"
      "v_add_f32_e32 v240, v240, v242\n\t"
      "v_mfma_f32_16x16x32_bf16 v[224:227], v[148:151], v[76:79], v[224:227]\n\t"
      "v_add_f32_e32 v237, v237, v239\n\t"
      "v_add_f32_e32 v241, v241, v243\n\t"
      "s_waitcnt vmcnt(4)\n\t"
      "v_mfma_f32_16x16x32_bf16 v[212:215], v[152:155], v[80:83], v[212:215]\n\t"
      "v_add_f32_e32 v228, v228, v248\n\t"
      "s_add_u32 s58, s76, s43\n\t"
      "s_addc_u32 s59, s77, 0\n\t"
      "global_load_dwordx4 v[172:175], v244, s[58:59] sc1\n\t"
      "v_mfma_f32_16x16x32_bf16 v[216:219], v[152:155], v[84:87], v[216:219]\n\t"
      "v_add_f32_e32 v236, v236, v240\n\t"
      "v_add_f32_e32 v229, v229, v249\n\t"
      "v_mfma_f32_16x16x32_bf16 v[220:223], v[152:155], v[88:91], v[220:223]\n\t"
      "v_add_f32_e32 v237, v237, v241\n\t"
      "v_mfma_f32_16x16x32_bf16 v[224:227], v[152:155], v[92:95], v[224:227]\n\t"
      "v_add_f32_e32 v228, v228, v236\n\t"
      "v_add_f32_e32 v229, v229, v237\n\t"
      "s_waitcnt vmcnt(4)\n\t"
      "v_mfma_f32_16x16x32_bf16 v[212:215], v[156:159], v[96:99], v[212:215]\n\t"
      "v_cvt_pk_bf16_f32 v232, v228, v229\n\t"
      "s_add_u32 s58, s76, s44\n\t"
      "s_addc_u32 s59, s77, 0\n\t"
      "global_load_dwordx4 v[176:179], v244, s[58:59] sc1\n\t"
      "v_mfma_f32_16x16x32_bf16 v[216:219], v[156:159], v[100:103], v[216:219]\n\t"
      "s_mov_b32 vcc_lo, 0xdddddddd\n\t"
      "s_mov_b32 vcc_hi, 0xdddddddd\n\t"
      "v_cndmask_b32_dpp v124, v232, v124, vcc quad_perm:[0,0,0,0] row_mask:0xf bank_mask:0xf\n\t"
      "v_cndmask_b32_dpp v125, v232, v125, vcc quad_perm:[1,1,1,1] row_mask:0xf bank_mask:0xf\n\t"
      "v_cndmask_b32_dpp v126, v232, v126, vcc quad_perm:[2,2,2,2] row_mask:0xf bank_mask:0xf\n\t"
      "v_cndmask_b32_dpp v127, v232, v127, vcc quad_perm:[3,3,3,3] row_mask:0xf bank_mask:0xf\n\t"
      "v_mfma_f32_16x16x32_bf16 v[220:223], v[156:159], v[104:107], v[220:223]\n\t"
      "v_mfma_f32_16x16x32_bf16 v[224:227], v[156:159], v[108:111], v[224:227]\n\t"
      "v_add_f32_e32 v251, v212, v213\n\t"
      "v_add_f32_e32 v251, v251, v214\n\t"
      "v_cmp_u_f32_e32 vcc, v251, v215\n\t"
      "s_cbranch_vccnz LA45R_%=\n\t"
      "LA45B_%=:\n\t"
      "ds_write_b128 v245, v[212:215] offset:32768\n\t"
      "s_waitcnt vmcnt(4)\n\t"
      "v_mfma_f32_16x16x32_bf16 v[196:199], v[160:163], v[0:3], 0\n\t"
      "ds_write_b128 v245, v[216:219] offset:33792\n\t"
      "s_add_u32 s58, s76, s45\n\t"
      "s_addc_u32 s59, s77, 0\n\t"
      "global_load_dwordx4 v[180:183], v244, s[58:59] sc1\n\t"
      "v_mfma_f32_16x16x32_bf16 v[200:203], v[160:163], v[4:7], 0\n\t"
      "ds_write_b128 v245, v[220:223] offset:34816\n\t"
      "v_mfma_f32_16x16x32_bf16 v[204:207], v[160:163], v[8:11], 0\n\t"
      "ds_write_b128 v245, v[224:227] offset:35840\n\t"
      "v_mfma_f32_16x16x32_bf16 v[208:211], v[160:163], v[12:15], 0\n\t"
      "s_waitcnt vmcnt(4)\n\t"
      "v_mfma_f32_16x16x32_bf16 v[196:199], v[164:167], v[16:19], v[196:199]\n\t"
      "s_add_u32 s58, s76, s46\n\t"
      "s_addc_u32 s59, s77, 0\n\t"
      "global_load_dwordx4 v[184:187], v244, s[58:59] sc1\n\t"
      "v_mfma_f32_16x16x32_bf16 v[200:203], v[164:167], v[20:23], v[200:203]\n\t"
      "v_mfma_f32_16x16x32_bf16 v[204:207], v[164:167], v[24:27], v[204:207]\n\t"
      "s_waitcnt lgkmcnt(0)\n\t"
      "s_barrier\n\t"
      "ds_read_b64 v[228:229], v246 offset:32768\n\t"
      "ds_read_b64 v[230:231], v246 offset:36864\n\t"
      "v_mfma_f32_16x16x32_bf16 v[208:211], v[164:167], v[28:31], v[208:211]\n\t"
      "ds_read_b64 v[232:233], v246 offset:40960\n\t"
      "ds_read_b64 v[234:235], v246 offset:45056\n\t"
      "s_waitcnt vmcnt(4)\n\t"
      "v_mfma_f32_16x16x32_bf16 v[196:199], v[168:171], v[32:35], v[196:199]\n\t"
      "ds_read_b64 v[236:237], v246 offset:49152\n\t"
      "ds_read_b64 v[238:239], v246 offset:53248\n\t"
      "v_mfma_f32_16x16x32_bf16 v[200:203], v[168:171], v[36:39], v[200:203]\n\t"
      "ds_read_b64 v[240:241], v246 offset:57344\n\t"
      "ds_read_b64 v[242:243], v246 offset:61440\n\t"
      "v_lshlrev_b32_e32 v248, 16, v117\n\t"
      "v_and_b32_e32 v249, 0xffff0000, v117\n\t"
      "v_mfma_f32_16x16x32_bf16 v[204:207], v[168:171], v[40:43], v[204:207]\n\t"
      "v_add_f32_e32 v248, v254, v248\n\t"
      "v_add_f32_e32 v249, v254, v249\n\t"
      "v_mfma_f32_16x16x32_bf16 v[208:211], v[168:171], v[44:47], v[208:211]\n\t"
      "s_waitcnt lgkmcnt(4)\n\t"
      "v_add_f32_e32 v228, v228, v230\n\t"
      "v_add_f32_e32 v232, v232, v234\n\t"
      "s_waitcnt vmcnt(3)\n\t"
      "v_mfma_f32_16x16x32_bf16 v[196:199], v[172:175], v[48:51], v[196:199]\n\t"
      "v_add_f32_e32 v229, v229, v231\n\t"
      "v_add_f32_e32 v233, v233, v235\n\t"
      "v_mfma_f32_16x16x32_bf16 v[200:203], v[172:175], v[52:55], v[200:203]\n\t"
      "v_add_f32_e32 v228, v228, v232\n\t"
      "v_add_f32_e32 v229, v229, v233\n\t"
      "s_waitcnt lgkmcnt(0)\n\t"
      "v_mfma_f32_16x16x32_bf16 v[204:207], v[172:175], v[56:59], v[204:207]\n\t"
      "v_add_f32_e32 v236, v236, v238\n\t"
      "v_add_f32_e32 v240, v240, v242\n\t"
      "v_mfma_f32_16x16x32_bf16 v[208:211], v[172:175], v[60:63], v[208:211]\n\t"
      "v_add_f32_e32 v237, v237, v239\n\t"
      "v_add_f32_e32 v241, v241, v243\n\t"
      "v_add_f32_e32 v228, v228, v248\n\t"
      "s_waitcnt vmcnt(2)\n\t"
      "v_mfma_f32_16x16x32_bf16 v[196:199], v[176:179], v[64:67], v[196:199]\n\t"
      "v_add_f32_e32 v236, v236, v240\n\t"
      "v_add_f32_e32 v229, v229, v249\n\t"
      "v_mfma_f32_16x16x32_bf16 v[200:203], v[176:179], v[68:71], v[200:203]\n\t"
      "v_add_f32_e32 v237, v237, v241\n\t"
      "v_add_f32_e32 v228, v228, v236\n\t"
      "v_add_f32_e32 v229, v229, v237\n\t"
      "v_mfma_f32_16x16x32_bf16 v[204:207], v[176:179], v[72:75], v[204:207]\n\t"
      "v_cvt_pk_bf16_f32 v232, v228, v229\n\t"
      "s_mov_b32 vcc_lo, 0xbbbbbbbb\n\t"
      "s_mov_b32 vcc_hi, 0xbbbbbbbb\n\t"
      "v_cndmask_b32_dpp v124, v232, v124, vcc quad_perm:[0,0,0,0] row_mask:0xf bank_mask:0xf\n\t"
      "v_cndmask_b32_dpp v125, v232, v125, vcc quad_perm:[1,1,1,1] row_mask:0xf bank_mask:0xf\n\t"
      "v_cndmask_b32_dpp v126, v232, v126, vcc quad_perm:[2,2,2,2] row_mask:0xf bank_mask:0xf\n\t"
      "v_cndmask_b32_dpp v127, v232, v127, vcc quad_perm:[3,3,3,3] row_mask:0xf bank_mask:0xf\n\t"
      "v_mfma_f32_16x16x32_bf16 v[208:211], v[176:179], v[76:79], v[208:211]\n\t"
      "s_waitcnt vmcnt(1)\n\t"
      "v_mfma_f32_16x16x32_bf16 v[196:199], v[180:183], v[80:83], v[196:199]\n\t"
      "v_mfma_f32_16x16x32_bf16 v[200:203], v[180:183], v[84:87], v[200:203]\n\t"
      "v_mfma_f32_16x16x32_bf16 v[204:207], v[180:183], v[88:91], v[204:207]\n\t"
      "v_mfma_f32_16x16x32_bf16 v[208:211], v[180:183], v[92:95], v[208:211]\n\t"
      "s_waitcnt vmcnt(0)\n\t"
      "v_mfma_f32_16x16x32_bf16 v[196:199], v[184:187], v[96:99], v[196:199]\n\t"
      "v_mfma_f32_16x16x32_bf16 v[200:203], v[184:187], v[100:103], v[200:203]\n\t"
      "v_mfma_f32_16x16x32_bf16 v[204:207], v[184:187], v[104:107], v[204:207]\n\t"
      "v_mfma_f32_16x16x32_bf16 v[208:211], v[184:187], v[108:111], v[208:211]\n\t"
      "s_nop 4\n\t"
      "v_add_f32_e32 v251, v196, v197\n\t"
      "v_add_f32_e32 v251, v251, v198\n\t"
      "v_cmp_u_f32_e32 vcc, v251, v199\n\t"
      "s_cbranch_vccnz LA46R_%=\n\t"
      "LA46B_%=:\n\t"
      "ds_write_b128 v245, v[196:199]\n\t"
      "s_mov_b64 exec, s[82:83]\n\t"
      "global_store_dwordx4 v247, v[120:123], s[18:19] sc1\n\t"
      "s_mov_b64 exec, -1\n\t"
      "ds_write_b128 v245, v[200:203] offset:1024\n\t"
      "ds_write_b128 v245, v[204:207] offset:2048\n\t"
      "ds_write_b128 v245, v[208:211] offset:3072\n\t"
      "s_waitcnt lgkmcnt(0)\n\t"
      "s_barrier\n\t"
      "ds_read_b64 v[228:229], v246\n\t"
      "ds_read_b64 v[230:231], v246 offset:4096\n\t"
      "ds_read_b64 v[232:233], v246 offset:8192\n\t"
      "ds_read_b64 v[234:235], v246 offset:12288\n\t"
      "ds_read_b64 v[236:237], v246 offset:16384\n\t"
      "ds_read_b64 v[238:239], v246 offset:20480\n\t"
      "ds_read_b64 v[240:241], v246 offset:24576\n\t"
      "ds_read_b64 v[242:243], v246 offset:28672\n\t"
      "v_lshlrev_b32_e32 v248, 16, v118\n\t"
      "v_and_b32_e32 v249, 0xffff0000, v118\n\t"
      "v_add_f32_e32 v248, v254, v248\n\t"
      "v_add_f32_e32 v249, v254, v249\n\t"
      "s_waitcnt lgkmcnt(4)\n\t"
      "v_add_f32_e32 v228, v228, v230\n\t"
      "v_add_f32_e32 v232, v232, v234\n\t"
      "v_add_f32_e32 v229, v229, v231\n\t"
      "v_add_f32_e32 v233, v233, v235\n\t"
      "v_add_f32_e32 v228, v228, v232\n\t"
      "v_add_f32_e32 v229, v229, v233\n\t"
      "s_waitcnt lgkmcnt(0)\n\t"
      "v_add_f32_e32 v236, v236, v238\n\t"
      "v_add_f32_e32 v240, v240, v242\n\t"
      "v_add_f32_e32 v237, v237, v239\n\t"
      "v_add_f32_e32 v241, v241, v243\n\t"
      "v_add_f32_e32 v228, v228, v248\n\t"
      "v_add_f32_e32 v236, v236, v240\n\t"
      "v_add_f32_e32 v229, v229, v249\n\t"
      "v_add_f32_e32 v237, v237, v241\n\t"
      "v_add_f32_e32 v228, v228, v236\n\t"
      "v_add_f32_e32 v229, v229, v237\n\t"
      "v_cvt_pk_bf16_f32 v232, v228, v229\n\t"
      "s_mov_b32 vcc_lo, 0x77777777\n\t"
      "s_mov_b32 vcc_hi, 0x77777777\n\t"
      "v_cndmask_b32_dpp v124, v232, v124, vcc quad_perm:[0,0,0,0] row_mask:0xf bank_mask:0xf\n\t"
      "v_cndmask_b32_dpp v125, v232, v125, vcc quad_perm:[1,1,1,1] row_mask:0xf bank_mask:0xf\n\t"
      "v_cndmask_b32_dpp v126, v232, v126, vcc quad_perm:[2,2,2,2] row_mask:0xf bank_mask:0xf\n\t"
      "v_cndmask_b32_dpp v127, v232, v127, vcc quad_perm:[3,3,3,3] row_mask:0xf bank_mask:0xf\n\t"
      "global_store_dwordx4 v247, v[124:127], s[62:63] sc1\n\t"
      "s_endpgm\n\t"
      "LO0R_%=:\n\t"
      "s_mov_b32 s80, 0\n\t"
      "LO0L_%=:\n\t"
      "s_add_u32 s80, s80, 1\n\t"
      "s_cmp_lt_u32 s80, 262144\n\t"
      "s_cbranch_scc0 LO0X_%=\n\t"
      "s_waitcnt vmcnt(0)\n\t"
      "s_sleep 1\n\t"
      "global_load_dwordx4 v[128:131], v244, s[42:43] offset:-2048 sc1\n\t"
      "global_load_dwordx4 v[132:135], v244, s[42:43] offset:-1024 sc1\n\t"
      "global_load_dwordx4 v[136:139], v244, s[42:43] sc1\n\t"
      "global_load_dwordx4 v[140:143], v244, s[42:43] offset:1024 sc1\n\t"
      "global_load_dwordx4 v[144:147], v244, s[42:43] offset:2048 sc1\n\t"
      "global_load_dwordx4 v[148:151], v244, s[42:43] offset:3072 sc1\n\t"
      "global_load_dwordx4 v[152:155], v244, s[44:45] offset:-4096 sc1\n\t"
      "global_load_dwordx4 v[156:159], v244, s[44:45] offset:-3072 sc1\n\t"
      "global_load_dwordx4 v[160:163], v244, s[44:45] offset:-2048 sc1\n\t"
      "global_load_dwordx4 v[164:167], v244, s[44:45] offset:-1024 sc1\n\t"
      "global_load_dwordx4 v[168:171], v244, s[44:45] sc1\n\t"
      "global_load_dwordx4 v[172:175], v244, s[44:45] offset:1024 sc1\n\t"
      "global_load_dwordx4 v[176:179], v244, s[44:45] offset:2048 sc1\n\t"
      "global_load_dwordx4 v[180:183], v244, s[44:45] offset:3072 sc1\n\t"
      "global_load_dwordx4 v[184:187], v244, s[42:43] offset:-4096 sc1\n\t"
      "global_load_dwordx4 v[188:191], v244, s[42:43] offset:-3072 sc1\n\t"
      "s_waitcnt vmcnt(0)\n\t"
      "v_max3_u32 v232, v128, v129, v130\n\t"
      "v_max3_u32 v232, v232, v131, v132\n\t"
      "v_max3_u32 v232, v232, v133, v134\n\t"
      "v_max_u32_e32 v232, v232, v135\n\t"
      "v_cmp_eq_u32_e32 vcc, -1, v232\n\t"
      "s_cbranch_vccnz LO0L_%=\n\t"
      "LO0X_%=:\n\t"
      "s_branch LO0B_%=\n\t"
      "LO2R_%=:\n\t"
      "s_mov_b32 s80, 0\n\t"
      "LO2L_%=:\n\t"
      "s_add_u32 s80, s80, 1\n\t"
      "s_cmp_lt_u32 s80, 262144\n\t"
      "s_cbranch_scc0 LO2X_%=\n\t"
      "s_waitcnt vmcnt(0)\n\t"
      "s_sleep 1\n\t"
      "global_load_dwordx4 v[136:139], v244, s[42:43] sc1\n\t"
      "global_load_dwordx4 v[140:143], v244, s[42:43] offset:1024 sc1\n\t"
      "global_load_dwordx4 v[144:147], v244, s[42:43] offset:2048 sc1\n\t"
      "global_load_dwordx4 v[148:151], v244, s[42:43] offset:3072 sc1\n\t"
      "global_load_dwordx4 v[152:155], v244, s[44:45] offset:-4096 sc1\n\t"
      "global_load_dwordx4 v[156:159], v244, s[44:45] offset:-3072 sc1\n\t"
      "global_load_dwordx4 v[160:163], v244, s[44:45] offset:-2048 sc1\n\t"
      "global_load_dwordx4 v[164:167], v244, s[44:45] offset:-1024 sc1\n\t"
      "global_load_dwordx4 v[168:171], v244, s[44:45] sc1\n\t"
      "global_load_dwordx4 v[172:175], v244, s[44:45] offset:1024 sc1\n\t"
      "global_load_dwordx4 v[176:179], v244, s[44:45] offset:2048 sc1\n\t"
      "global_load_dwordx4 v[180:183], v244, s[44:45] offset:3072 sc1\n\t"
      "global_load_dwordx4 v[184:187], v244, s[42:43] offset:-4096 sc1\n\t"
      "global_load_dwordx4 v[188:191], v244, s[42:43] offset:-3072 sc1\n\t"
      "s_waitcnt vmcnt(0)\n\t"
      "v_max3_u32 v232, v136, v137, v138\n\t"
      "v_max3_u32 v232, v232, v139, v140\n\t"
      "v_max3_u32 v232, v232, v141, v142\n\t"
      "v_max_u32_e32 v232, v232, v143\n\t"
      "v_cmp_eq_u32_e32 vcc, -1, v232\n\t"
      "s_cbranch_vccnz LO2L_%=\n\t"
      "LO2X_%=:\n\t"
      "s_branch LO2B_%=\n\t"
      "LO4R_%=:\n\t"
      "s_mov_b32 s80, 0\n\t"
      "LO4L_%=:\n\t"
      "s_add_u32 s80, s80, 1\n\t"
      "s_cmp_lt_u32 s80, 262144\n\t"
      "s_cbranch_scc0 LO4X_%=\n\t"
      "s_waitcnt vmcnt(0)\n\t"
      "s_sleep 1\n\t"
      "global_load_dwordx4 v[144:147], v244, s[42:43] offset:2048 sc1\n\t"
      "global_load_dwordx4 v[148:151], v244, s[42:43] offset:3072 sc1\n\t"
      "global_load_dwordx4 v[152:155], v244, s[44:45] offset:-4096 sc1\n\t"
      "global_load_dwordx4 v[156:159], v244, s[44:45] offset:-3072 sc1\n\t"
      "global_load_dwordx4 v[160:163], v244, s[44:45] offset:-2048 sc1\n\t"
      "global_load_dwordx4 v[164:167], v244, s[44:45] offset:-1024 sc1\n\t"
      "global_load_dwordx4 v[168:171], v244, s[44:45] sc1\n\t"
      "global_load_dwordx4 v[172:175], v244, s[44:45] offset:1024 sc1\n\t"
      "global_load_dwordx4 v[176:179], v244, s[44:45] offset:2048 sc1\n\t"
      "global_load_dwordx4 v[180:183], v244, s[44:45] offset:3072 sc1\n\t"
      "global_load_dwordx4 v[184:187], v244, s[42:43] offset:-4096 sc1\n\t"
      "global_load_dwordx4 v[188:191], v244, s[42:43] offset:-3072 sc1\n\t"
      "s_waitcnt vmcnt(0)\n\t"
      "v_max3_u32 v232, v144, v145, v146\n\t"
      "v_max3_u32 v232, v232, v147, v148\n\t"
      "v_max3_u32 v232, v232, v149, v150\n\t"
      "v_max_u32_e32 v232, v232, v151\n\t"
      "v_cmp_eq_u32_e32 vcc, -1, v232\n\t"
      "s_cbranch_vccnz LO4L_%=\n\t"
      "LO4X_%=:\n\t"
      "s_branch LO4B_%=\n\t"
      "LO6R_%=:\n\t"
      "s_mov_b32 s80, 0\n\t"
      "LO6L_%=:\n\t"
      "s_add_u32 s80, s80, 1\n\t"
      "s_cmp_lt_u32 s80, 262144\n\t"
      "s_cbranch_scc0 LO6X_%=\n\t"
      "s_waitcnt vmcnt(0)\n\t"
      "s_sleep 1\n\t"
      "global_load_dwordx4 v[152:155], v244, s[44:45] offset:-4096 sc1\n\t"
      "global_load_dwordx4 v[156:159], v244, s[44:45] offset:-3072 sc1\n\t"
      "global_load_dwordx4 v[160:163], v244, s[44:45] offset:-2048 sc1\n\t"
      "global_load_dwordx4 v[164:167], v244, s[44:45] offset:-1024 sc1\n\t"
      "global_load_dwordx4 v[168:171], v244, s[44:45] sc1\n\t"
      "global_load_dwordx4 v[172:175], v244, s[44:45] offset:1024 sc1\n\t"
      "global_load_dwordx4 v[176:179], v244, s[44:45] offset:2048 sc1\n\t"
      "global_load_dwordx4 v[180:183], v244, s[44:45] offset:3072 sc1\n\t"
      "global_load_dwordx4 v[184:187], v244, s[42:43] offset:-4096 sc1\n\t"
      "global_load_dwordx4 v[188:191], v244, s[42:43] offset:-3072 sc1\n\t"
      "s_waitcnt vmcnt(0)\n\t"
      "v_max3_u32 v232, v152, v153, v154\n\t"
      "v_max3_u32 v232, v232, v155, v156\n\t"
      "v_max3_u32 v232, v232, v157, v158\n\t"
      "v_max_u32_e32 v232, v232, v159\n\t"
      "v_cmp_eq_u32_e32 vcc, -1, v232\n\t"
      "s_cbranch_vccnz LO6L_%=\n\t"
      "LO6X_%=:\n\t"
      "s_branch LO6B_%=\n\t"
      "LO8R_%=:\n\t"
      "s_mov_b32 s80, 0\n\t"
      "LO8L_%=:\n\t"
      "s_add_u32 s80, s80, 1\n\t"
      "s_cmp_lt_u32 s80, 262144\n\t"
      "s_cbranch_scc0 LO8X_%=\n\t"
      "s_waitcnt vmcnt(0)\n\t"
      "s_sleep 1\n\t"
      "global_load_dwordx4 v[160:163], v244, s[44:45] offset:-2048 sc1\n\t"
      "global_load_dwordx4 v[164:167], v244, s[44:45] offset:-1024 sc1\n\t"
      "global_load_dwordx4 v[168:171], v244, s[44:45] sc1\n\t"
      "global_load_dwordx4 v[172:175], v244, s[44:45] offset:1024 sc1\n\t"
      "global_load_dwordx4 v[176:179], v244, s[44:45] offset:2048 sc1\n\t"
      "global_load_dwordx4 v[180:183], v244, s[44:45] offset:3072 sc1\n\t"
      "global_load_dwordx4 v[184:187], v244, s[42:43] offset:-4096 sc1\n\t"
      "global_load_dwordx4 v[188:191], v244, s[42:43] offset:-3072 sc1\n\t"
      "s_waitcnt vmcnt(0)\n\t"
      "v_max3_u32 v232, v160, v161, v162\n\t"
      "v_max3_u32 v232, v232, v163, v164\n\t"
      "v_max3_u32 v232, v232, v165, v166\n\t"
      "v_max_u32_e32 v232, v232, v167\n\t"
      "v_cmp_eq_u32_e32 vcc, -1, v232\n\t"
      "s_cbranch_vccnz LO8L_%=\n\t"
      "LO8X_%=:\n\t"
      "s_branch LO8B_%=\n\t"
      "LO10R_%=:\n\t"
      "s_mov_b32 s80, 0\n\t"
      "LO10L_%=:\n\t"
      "s_add_u32 s80, s80, 1\n\t"
      "s_cmp_lt_u32 s80, 262144\n\t"
      "s_cbranch_scc0 LO10X_%=\n\t"
      "s_waitcnt vmcnt(0)\n\t"
      "s_sleep 1\n\t"
      "global_load_dwordx4 v[168:171], v244, s[44:45] sc1\n\t"
      "global_load_dwordx4 v[172:175], v244, s[44:45] offset:1024 sc1\n\t"
      "global_load_dwordx4 v[176:179], v244, s[44:45] offset:2048 sc1\n\t"
      "global_load_dwordx4 v[180:183], v244, s[44:45] offset:3072 sc1\n\t"
      "global_load_dwordx4 v[184:187], v244, s[42:43] offset:-4096 sc1\n\t"
      "global_load_dwordx4 v[188:191], v244, s[42:43] offset:-3072 sc1\n\t"
      "s_waitcnt vmcnt(0)\n\t"
      "v_max3_u32 v232, v168, v169, v170\n\t"
      "v_max3_u32 v232, v232, v171, v172\n\t"
      "v_max3_u32 v232, v232, v173, v174\n\t"
      "v_max_u32_e32 v232, v232, v175\n\t"
      "v_cmp_eq_u32_e32 vcc, -1, v232\n\t"
      "s_cbranch_vccnz LO10L_%=\n\t"
      "LO10X_%=:\n\t"
      "s_branch LO10B_%=\n\t"
      "LO12R_%=:\n\t"
      "s_mov_b32 s80, 0\n\t"
      "LO12L_%=:\n\t"
      "s_add_u32 s80, s80, 1\n\t"
      "s_cmp_lt_u32 s80, 262144\n\t"
      "s_cbranch_scc0 LO12X_%=\n\t"
      "s_waitcnt vmcnt(0)\n\t"
      "s_sleep 1\n\t"
      "global_load_dwordx4 v[176:179], v244, s[44:45] offset:2048 sc1\n\t"
      "global_load_dwordx4 v[180:183], v244, s[44:45] offset:3072 sc1\n\t"
      "global_load_dwordx4 v[184:187], v244, s[42:43] offset:-4096 sc1\n\t"
      "global_load_dwordx4 v[188:191], v244, s[42:43] offset:-3072 sc1\n\t"
      "s_waitcnt vmcnt(0)\n\t"
      "v_max3_u32 v232, v176, v177, v178\n\t"
      "v_max3_u32 v232, v232, v179, v180\n\t"
      "v_max3_u32 v232, v232, v181, v182\n\t"
      "v_max_u32_e32 v232, v232, v183\n\t"
      "v_cmp_eq_u32_e32 vcc, -1, v232\n\t"
      "s_cbranch_vccnz LO12L_%=\n\t"
      "LO12X_%=:\n\t"
      "s_branch LO12B_%=\n\t"
      "LO14R_%=:\n\t"
      "s_mov_b32 s80, 0\n\t"
      "LO14L_%=:\n\t"
      "s_add_u32 s80, s80, 1\n\t"
      "s_cmp_lt_u32 s80, 262144\n\t"
      "s_cbranch_scc0 LO14X_%=\n\t"
      "s_waitcnt vmcnt(0)\n\t"
      "s_sleep 1\n\t"
      "global_load_dwordx4 v[184:187], v244, s[42:43] offset:-4096 sc1\n\t"
      "global_load_dwordx4 v[188:191], v244, s[42:43] offset:-3072 sc1\n\t"
      "s_waitcnt vmcnt(0)\n\t"
      "v_max3_u32 v232, v184, v185, v186\n\t"
      "v_max3_u32 v232, v232, v187, v188\n\t"
      "v_max3_u32 v232, v232, v189, v190\n\t"
      "v_max_u32_e32 v232, v232, v191\n\t"
      "v_cmp_eq_u32_e32 vcc, -1, v232\n\t"
      "s_cbranch_vccnz LO14L_%=\n\t"
      "LO14X_%=:\n\t"
      "s_branch LO14B_%=\n\t"
      "LGX_%=:\n\t"
      "v_max3_u32 v232, v128, v129, v130\n\t"
      "v_max3_u32 v232, v232, v131, v132\n\t"
      "v_max3_u32 v232, v232, v133, v134\n\t"
      "v_max3_u32 v232, v232, v135, v136\n\t"
      "v_max3_u32 v232, v232, v137, v138\n\t"
      "v_max3_u32 v232, v232, v139, v140\n\t"
      "v_max3_u32 v232, v232, v141, v142\n\t"
      "v_max3_u32 v232, v232, v143, v144\n\t"
      "v_max3_u32 v232, v232, v145, v146\n\t"
      "v_max3_u32 v232, v232, v147, v148\n\t"
      "v_max3_u32 v232, v232, v149, v150\n\t"
      "v_max3_u32 v232, v232, v151, v152\n\t"
      "v_max3_u32 v232, v232, v153, v154\n\t"
      "v_max3_u32 v232, v232, v155, v156\n\t"
      "v_max3_u32 v232, v232, v157, v158\n\t"
      "v_max3_u32 v232, v232, v159, v160\n\t"
      "v_max3_u32 v232, v232, v161, v162\n\t"
      "v_max3_u32 v232, v232, v163, v164\n\t"
      "v_max3_u32 v232, v232, v165, v166\n\t"
      "v_max3_u32 v232, v232, v167, v168\n\t"
      "v_max3_u32 v232, v232, v169, v170\n\t"
      "v_max3_u32 v232, v232, v171, v172\n\t"
      "v_max3_u32 v232, v232, v173, v174\n\t"
      "v_max3_u32 v232, v232, v175, v176\n\t"
      "v_max3_u32 v232, v232, v177, v178\n\t"
      "v_max3_u32 v232, v232, v179, v180\n\t"
      "v_max3_u32 v232, v232, v181, v182\n\t"
      "v_max3_u32 v232, v232, v183, v184\n\t"
      "v_max3_u32 v232, v232, v185, v186\n\t"
      "v_max3_u32 v232, v232, v187, v188\n\t"
      "v_max3_u32 v232, v232, v189, v190\n\t"
      "v_max_u32_e32 v232, v232, v191\n\t"
      "v_cmp_eq_u32_e32 vcc, -1, v232\n\t"
      "s_cbranch_vccz LGB_%=\n\t"
      "LGYR_%=:\n\t"
      "s_mov_b32 s80, 0\n\t"
      "LGYL_%=:\n\t"
      "s_add_u32 s80, s80, 1\n\t"
      "s_cmp_lt_u32 s80, 262144\n\t"
      "s_cbranch_scc0 LGYX_%=\n\t"
      "s_sleep 1\n\t"
      "global_load_dwordx4 v[128:131], v244, s[42:43] offset:-2048 sc1\n\t"
      "global_load_dwordx4 v[132:135], v244, s[42:43] offset:-1024 sc1\n\t"
      "global_load_dwordx4 v[136:139], v244, s[42:43] sc1\n\t"
      "global_load_dwordx4 v[140:143], v244, s[42:43] offset:1024 sc1\n\t"
      "global_load_dwordx4 v[144:147], v244, s[42:43] offset:2048 sc1\n\t"
      "global_load_dwordx4 v[148:151], v244, s[42:43] offset:3072 sc1\n\t"
      "global_load_dwordx4 v[152:155], v244, s[44:45] offset:-4096 sc1\n\t"
      "global_load_dwordx4 v[156:159], v244, s[44:45] offset:-3072 sc1\n\t"
      "global_load_dwordx4 v[160:163], v244, s[44:45] offset:-2048 sc1\n\t"
      "global_load_dwordx4 v[164:167], v244, s[44:45] offset:-1024 sc1\n\t"
      "global_load_dwordx4 v[168:171], v244, s[44:45] sc1\n\t"
      "global_load_dwordx4 v[172:175], v244, s[44:45] offset:1024 sc1\n\t"
      "global_load_dwordx4 v[176:179], v244, s[44:45] offset:2048 sc1\n\t"
      "global_load_dwordx4 v[180:183], v244, s[44:45] offset:3072 sc1\n\t"
      "global_load_dwordx4 v[184:187], v244, s[42:43] offset:-4096 sc1\n\t"
      "global_load_dwordx4 v[188:191], v244, s[42:43] offset:-3072 sc1\n\t"
      "s_waitcnt vmcnt(0)\n\t"
      "v_max3_u32 v232, v128, v129, v130\n\t"
      "v_max3_u32 v232, v232, v131, v132\n\t"
      "v_max3_u32 v232, v232, v133, v134\n\t"
      "v_max3_u32 v232, v232, v135, v136\n\t"
      "v_max3_u32 v232, v232, v137, v138\n\t"
      "v_max3_u32 v232, v232, v139, v140\n\t"
      "v_max3_u32 v232, v232, v141, v142\n\t"
      "v_max3_u32 v232, v232, v143, v144\n\t"
      "v_max3_u32 v232, v232, v145, v146\n\t"
      "v_max3_u32 v232, v232, v147, v148\n\t"
      "v_max3_u32 v232, v232, v149, v150\n\t"
      "v_max3_u32 v232, v232, v151, v152\n\t"
      "v_max3_u32 v232, v232, v153, v154\n\t"
      "v_max3_u32 v232, v232, v155, v156\n\t"
      "v_max3_u32 v232, v232, v157, v158\n\t"
      "v_max3_u32 v232, v232, v159, v160\n\t"
      "v_max3_u32 v232, v232, v161, v162\n\t"
      "v_max3_u32 v232, v232, v163, v164\n\t"
      "v_max3_u32 v232, v232, v165, v166\n\t"
      "v_max3_u32 v232, v232, v167, v168\n\t"
      "v_max3_u32 v232, v232, v169, v170\n\t"
      "v_max3_u32 v232, v232, v171, v172\n\t"
      "v_max3_u32 v232, v232, v173, v174\n\t"
      "v_max3_u32 v232, v232, v175, v176\n\t"
      "v_max3_u32 v232, v232, v177, v178\n\t"
      "v_max3_u32 v232, v232, v179, v180\n\t"
      "v_max3_u32 v232, v232, v181, v182\n\t"
      "v_max3_u32 v232, v232, v183, v184\n\t"
      "v_max3_u32 v232, v232, v185, v186\n\t"
      "v_max3_u32 v232, v232, v187, v188\n\t"
      "v_max3_u32 v232, v232, v189, v190\n\t"
      "v_max_u32_e32 v232, v232, v191\n\t"
      "v_cmp_eq_u32_e32 vcc, -1, v232\n\t"
      "s_cbranch_vccnz LGYL_%=\n\t"
      "LGYX_%=:\n\t"
      "s_cbranch_vccnz LGB_%=\n\t"
      "s_waitcnt lgkmcnt(0)\n\t"
      "ds_read_b64_tr_b16 v[0:1], v216 offset:7168\n\t"
      "ds_read_b64_tr_b16 v[2:3], v220 offset:7168\n\t"
      "ds_read_b64_tr_b16 v[4:5], v217 offset:7168\n\t"
      "ds_read_b64_tr_b16 v[6:7], v221 offset:7168\n\t"
      "ds_read_b64_tr_b16 v[8:9], v218 offset:7168\n\t"
      "ds_read_b64_tr_b16 v[10:11], v222 offset:7168\n\t"
      "ds_read_b64_tr_b16 v[12:13], v219 offset:7168\n\t"
      "ds_read_b64_tr_b16 v[14:15], v223 offset:7168\n\t"
      "ds_read_b64_tr_b16 v[16:17], v218 offset:10752\n\t"
      "ds_read_b64_tr_b16 v[18:19], v222 offset:10752\n\t"
      "ds_read_b64_tr_b16 v[20:21], v219 offset:10752\n\t"
      "ds_read_b64_tr_b16 v[22:23], v223 offset:10752\n\t"
      "ds_read_b64_tr_b16 v[24:25], v216 offset:10752\n\t"
      "ds_read_b64_tr_b16 v[26:27], v220 offset:10752\n\t"
      "ds_read_b64_tr_b16 v[28:29], v217 offset:10752\n\t"
      "ds_read_b64_tr_b16 v[30:31], v221 offset:10752\n\t"
      "ds_read_b64_tr_b16 v[32:33], v216 offset:14336\n\t"
      "ds_read_b64_tr_b16 v[34:35], v220 offset:14336\n\t"
      "ds_read_b64_tr_b16 v[36:37], v217 offset:14336\n\t"
      "ds_read_b64_tr_b16 v[38:39], v221 offset:14336\n\t"
      "ds_read_b64_tr_b16 v[40:41], v218 offset:14336\n\t"
      "ds_read_b64_tr_b16 v[42:43], v222 offset:14336\n\t"
      "ds_read_b64_tr_b16 v[44:45], v219 offset:14336\n\t"
      "ds_read_b64_tr_b16 v[46:47], v223 offset:14336\n\t"
      "ds_read_b64_tr_b16 v[48:49], v218 offset:17920\n\t"
      "ds_read_b64_tr_b16 v[50:51], v222 offset:17920\n\t"
      "ds_read_b64_tr_b16 v[52:53], v219 offset:17920\n\t"
      "ds_read_b64_tr_b16 v[54:55], v223 offset:17920\n\t"
      "ds_read_b64_tr_b16 v[56:57], v216 offset:17920\n\t"
      "ds_read_b64_tr_b16 v[58:59], v220 offset:17920\n\t"
      "ds_read_b64_tr_b16 v[60:61], v217 offset:17920\n\t"
      "ds_read_b64_tr_b16 v[62:63], v221 offset:17920\n\t"
      "ds_read_b64_tr_b16 v[64:65], v216 offset:21504\n\t"
      "ds_read_b64_tr_b16 v[66:67], v220 offset:21504\n\t"
      "ds_read_b64_tr_b16 v[68:69], v217 offset:21504\n\t"
      "ds_read_b64_tr_b16 v[70:71], v221 offset:21504\n\t"
      "ds_read_b64_tr_b16 v[72:73], v218 offset:21504\n\t"
      "ds_read_b64_tr_b16 v[74:75], v222 offset:21504\n\t"
      "ds_read_b64_tr_b16 v[76:77], v219 offset:21504\n\t"
      "ds_read_b64_tr_b16 v[78:79], v223 offset:21504\n\t"
      "ds_read_b64_tr_b16 v[80:81], v218 offset:25088\n\t"
      "ds_read_b64_tr_b16 v[82:83], v222 offset:25088\n\t"
      "ds_read_b64_tr_b16 v[84:85], v219 offset:25088\n\t"
      "ds_read_b64_tr_b16 v[86:87], v223 offset:25088\n\t"
      "ds_read_b64_tr_b16 v[88:89], v216 offset:25088\n\t"
      "ds_read_b64_tr_b16 v[90:91], v220 offset:25088\n\t"
      "ds_read_b64_tr_b16 v[92:93], v217 offset:25088\n\t"
      "ds_read_b64_tr_b16 v[94:95], v221 offset:25088\n\t"
      "ds_read_b64_tr_b16 v[96:97], v216 offset:28672\n\t"
      "ds_read_b64_tr_b16 v[98:99], v220 offset:28672\n\t"
      "ds_read_b64_tr_b16 v[100:101], v217 offset:28672\n\t"
      "ds_read_b64_tr_b16 v[102:103], v221 offset:28672\n\t"
      "ds_read_b64_tr_b16 v[104:105], v218 offset:28672\n\t"
      "ds_read_b64_tr_b16 v[106:107], v222 offset:28672\n\t"
      "ds_read_b64_tr_b16 v[108:109], v219 offset:28672\n\t"
      "ds_read_b64_tr_b16 v[110:111], v223 offset:28672\n\t"
      "ds_read_b64_tr_b16 v[112:113], v218 offset:32256\n\t"
      "ds_read_b64_tr_b16 v[114:115], v222 offset:32256\n\t"
      "ds_read_b64_tr_b16 v[116:117], v219 offset:32256\n\t"
      "ds_read_b64_tr_b16 v[118:119], v223 offset:32256\n\t"
      "ds_read_b64_tr_b16 v[120:121], v216 offset:32256\n\t"
      "ds_read_b64_tr_b16 v[122:123], v220 offset:32256\n\t"
      "ds_read_b64_tr_b16 v[124:125], v217 offset:32256\n\t"
      "ds_read_b64_tr_b16 v[126:127], v221 offset:32256\n\t"
      "v_lshlrev_b32_e32 v192, 16, v208\n\t"
      "v_and_b32_e32 v193, 0xffff0000, v208\n\t"
      "v_lshlrev_b32_e32 v194, 16, v209\n\t"
      "v_and_b32_e32 v195, 0xffff0000, v209\n\t"
      "v_lshlrev_b32_e32 v196, 16, v210\n\t"
      "v_and_b32_e32 v197, 0xffff0000, v210\n\t"
      "v_lshlrev_b32_e32 v198, 16, v211\n\t"
      "v_and_b32_e32 v199, 0xffff0000, v211\n\t"
      "v_lshlrev_b32_e32 v200, 16, v212\n\t"
      "v_and_b32_e32 v201, 0xffff0000, v212\n\t"
      "v_lshlrev_b32_e32 v202, 16, v213\n\t"
      "v_and_b32_e32 v203, 0xffff0000, v213\n\t"
      "v_lshlrev_b32_e32 v204, 16, v214\n\t"
      "v_and_b32_e32 v205, 0xffff0000, v214\n\t"
      "v_lshlrev_b32_e32 v206, 16, v215\n\t"
      "v_and_b32_e32 v207, 0xffff0000, v215\n\t"
      "s_waitcnt lgkmcnt(0)\n\t"
      "s_branch LGM_%=\n\t"
      "LSR_%=:\n\t"
      "s_mov_b32 s80, 0\n\t"
      "LSL_%=:\n\t"
      "s_add_u32 s80, s80, 1\n\t"
      "s_cmp_lt_u32 s80, 262144\n\t"
      "s_cbranch_scc0 LSX_%=\n\t"
      "s_sleep 1\n\t"
      "global_load_dwordx2 v[148:149], v231, s[46:47] sc1\n\t"
      "global_load_dwordx2 v[150:151], v231, s[46:47] offset:384 sc1\n\t"
      "s_waitcnt vmcnt(0)\n\t"
      "v_max3_u32 v232, v148, v149, v150\n\t"
      "v_max_u32_e32 v232, v232, v151\n\t"
      "v_cmp_eq_u32_e32 vcc, -1, v232\n\t"
      "s_cbranch_vccnz LSL_%=\n\t"
      "LSX_%=:\n\t"
      "s_branch LSB_%=\n\t"
      "LF1R_%=:\n\t"
      "s_mov_b32 s80, 0\n\t"
      "LF1L_%=:\n\t"
      "s_add_u32 s80, s80, 1\n\t"
      "s_cmp_lt_u32 s80, 262144\n\t"
      "s_cbranch_scc0 LF1X_%=\n\t"
      "s_sleep 2\n\t"
      "global_load_dwordx2 v[138:139], v136, s[60:61] sc1\n\t"
      "s_waitcnt vmcnt(0)\n\t"
      "v_cmp_ne_u64_e32 vcc, s[22:23], v[138:139]\n\t"
      "s_cbranch_vccnz LF1L_%=\n\t"
      "LF1X_%=:\n\t"
      "s_branch LF1B_%=\n\t"
      "LF4R_%=:\n\t"
      "s_mov_b32 s80, 0\n\t"
      "LF4L_%=:\n\t"
      "s_add_u32 s80, s80, 1\n\t"
      "s_cmp_lt_u32 s80, 262144\n\t"
      "s_cbranch_scc0 LF4X_%=\n\t"
      "s_sleep 2\n\t"
      "global_load_dwordx2 v[138:139], v137, s[60:61] sc1\n\t"
      "s_waitcnt vmcnt(0)\n\t"
      "v_cmp_ne_u64_e32 vcc, s[22:23], v[138:139]\n\t"
      "s_cbranch_vccnz LF4L_%=\n\t"
      "LF4X_%=:\n\t"
      "s_branch LF4B_%=\n\t"
      "L4_%=:\n\t"
      "s_lshl2_add_u32 s72, s31, s24\n\t"
      "s_sub_u32 s72, s72, 56\n\t"
      "s_mov_b32 m0, 64512\n\t"
      "s_lshl1_add_u32 s72, s72, 0\n\t"
      "s_mul_i32 s72, s72, 65536\n\t"
      "s_and_b32 s73, s2, 7\n\t"
      "s_mul_i32 s73, s73, 512\n\t"
      "s_and_b32 s73, s73, 0xffffff80\n\t"
      "s_add_u32 s72, s72, s73\n\t"
      "v_bfe_u32 v141, v192, 2, 3\n\t"
      "v_mul_u32_u24_e32 v141, 4096, v141\n\t"
      "v_lshl_add_u32 v141, v196, 7, v141\n\t"
      "v_add_u32_e32 v141, s72, v141\n\t"
      "global_load_lds_dword v141, s[14:15]\n\t"
      "s_waitcnt vmcnt(1)\n\t"
      "s_barrier\n\t"
      "s_barrier\n\t"
      "s_lshl2_add_u32 s72, s31, s24\n\t"
      "s_sub_u32 s72, s72, 56\n\t"
      "s_mov_b32 m0, 64512\n\t"
      "s_cmp_ge_u32 s72, 64\n\t"
      "s_cselect_b32 s73, 8, 0\n\t"
      "s_sub_u32 s73, s72, s73\n\t"
      "s_and_b32 s73, s73, 0x38\n\t"
      "s_add_u32 s72, s72, s73\n\t"
      "s_add_u32 s72, s72, 8\n\t"
      "s_mul_i32 s72, s72, 57344\n\t"
      "s_and_b32 s73, s2, 7\n\t"
      "s_mul_i32 s73, s73, 448\n\t"
      "s_and_b32 s73, s73, 0xffffff80\n\t"
      "s_add_u32 s72, s72, s73\n\t"
      "v_bfe_u32 v141, v192, 2, 4\n\t"
      "v_mul_u32_u24_e32 v141, 3584, v141\n\t"
      "v_lshl_add_u32 v141, v196, 7, v141\n\t"
      "v_add_u32_e32 v141, s72, v141\n\t"
      "global_load_lds_dword v141, s[10:11]\n\t"
      "s_barrier\n\t"
      "s_cmp_lg_u32 s24, 3\n\t"
      "s_cbranch_scc1 SK12_%=\n\t"
      "global_load_dwordx2 v[138:139], v137, s[60:61] sc1\n\t"
      "SK12_%=:\n\t"
      "s_cmp_lg_u32 s24, 3\n\t"
      "s_cbranch_scc1 LF4B_%=\n\t"
      "s_waitcnt vmcnt(0)\n\t"
      "v_cmp_ne_u64_e32 vcc, s[22:23], v[138:139]\n\t"
      "s_cbranch_vccnz LF4R_%=\n\t"
      "LF4B_%=:\n\t"
      "s_barrier\n\t"
      "s_waitcnt vmcnt(0)\n\t"
      "s_barrier\n\t"
      "s_branch G4_%=\n\t"
      "LF2R_%=:\n\t"
      "s_mov_b32 s80, 0\n\t"
      "LF2L_%=:\n\t"
      "s_add_u32 s80, s80, 1\n\t"
      "s_cmp_lt_u32 s80, 262144\n\t"
      "s_cbranch_scc0 LF2X_%=\n\t"
      "s_sleep 2\n\t"
      "global_load_dwordx2 v[138:139], v137, s[60:61] sc1\n\t"
      "s_waitcnt vmcnt(0)\n\t"
      "v_cmp_ne_u64_e32 vcc, s[22:23], v[138:139]\n\t"
      "s_cbranch_vccnz LF2L_%=\n\t"
      "LF2X_%=:\n\t"
      "s_branch LF2B_%=\n\t"
      "LF3R_%=:\n\t"
      "s_mov_b32 s80, 0\n\t"
      "LF3L_%=:\n\t"
      "s_add_u32 s80, s80, 1\n\t"
      "s_cmp_lt_u32 s80, 262144\n\t"
      "s_cbranch_scc0 LF3X_%=\n\t"
      "s_sleep 2\n\t"
      "global_load_dwordx2 v[138:139], v143, s[60:61] sc1\n\t"
      "s_waitcnt vmcnt(0)\n\t"
      "v_cmp_ne_u64_e32 vcc, s[22:23], v[138:139]\n\t"
      "s_cbranch_vccnz LF3L_%=\n\t"
      "LF3X_%=:\n\t"
      "s_branch LF3B_%=\n\t"
      "LF70R_%=:\n\t"
      "s_mov_b32 s80, 0\n\t"
      "LF70L_%=:\n\t"
      "s_add_u32 s80, s80, 1\n\t"
      "s_cmp_lt_u32 s80, 262144\n\t"
      "s_cbranch_scc0 LF70X_%=\n\t"
      "s_sleep 4\n\t"
      "global_load_dwordx2 v[138:139], v136, s[60:61] sc1\n\t"
      "s_waitcnt vmcnt(0)\n\t"
      "v_cmp_ne_u64_e32 vcc, s[22:23], v[138:139]\n\t"
      "s_cbranch_vccnz LF70L_%=\n\t"
      "LF70X_%=:\n\t"
      "s_branch LF70B_%=\n\t"
      "LF71R_%=:\n\t"
      "s_mov_b32 s80, 0\n\t"
      "LF71L_%=:\n\t"
      "s_add_u32 s80, s80, 1\n\t"
      "s_cmp_lt_u32 s80, 262144\n\t"
      "s_cbranch_scc0 LF71X_%=\n\t"
      "s_sleep 4\n\t"
      "global_load_dwordx2 v[138:139], v136, s[60:61] sc1\n\t"
      "s_waitcnt vmcnt(0)\n\t"
      "v_cmp_ne_u64_e32 vcc, s[22:23], v[138:139]\n\t"
      "s_cbranch_vccnz LF71L_%=\n\t"
      "LF71X_%=:\n\t"
      "s_branch LF71B_%=\n\t"
      "SPARE_%=:\n\t"
      "v_mov_b32_e32 v132, -1\n\t"
      "v_mov_b32_e32 v133, -1\n\t"
      "v_mov_b32_e32 v134, -1\n\t"
      "v_mov_b32_e32 v135, -1\n\t"
      "s_and_b32 s34, s25, 3\n\t"
      "s_lshl_b32 s34, s34, 15\n\t"
      "s_mul_i32 s35, s24, 917504\n\t"
      "s_add_u32 s34, s34, s35\n\t"
      "s_and_b32 s35, s2, 7\n\t"
      "s_lshl_b32 s35, s35, 9\n\t"
      "s_add_u32 s34, s34, s35\n\t"
      "v_and_b32_e32 v141, 0x1c0, v244\n\t"
      "v_lshlrev_b32_e32 v141, 6, v141\n\t"
      "v_and_b32_e32 v142, 0x30, v244\n\t"
      "v_lshl_add_u32 v141, v142, 3, v141\n\t"
      "v_add_u32_e32 v141, s34, v141\n\t"
      "s_mov_b32 m0, 64512\n\t"
      "s_lshr_b32 s34, s24, 1\n\t"
      "s_lshl_b32 s34, s34, 5\n\t"
      "s_add_u32 s34, s34, s25\n\t"
      "s_and_b32 s35, s24, 1\n\t"
      "s_lshl1_add_u32 s34, s34, s35\n\t"
      "s_lshl_b32 s34, s34, 10\n\t"
      "s_add_u32 s34, s34, 8388608\n\t"
      "s_add_u32 s40, s20, s34\n\t"
      "s_addc_u32 s41, s21, 0\n\t"
      "global_store_dwordx4 v244, v[132:135], s[40:41] sc1\n\t"
      "s_add_u32 s40, s40, 262144\n\t"
      "s_addc_u32 s41, s41, 0\n\t"
      "global_store_dwordx4 v244, v[132:135], s[40:41] sc1\n\t"
      "s_add_u32 s40, s40, 262144\n\t"
      "s_addc_u32 s41, s41, 0\n\t"
      "global_store_dwordx4 v244, v[132:135], s[40:41] sc1\n\t"
      "s_add_u32 s40, s40, 262144\n\t"
      "s_addc_u32 s41, s41, 0\n\t"
      "global_store_dwordx4 v244, v[132:135], s[40:41] sc1\n\t"
      "s_add_u32 s40, s40, 262144\n\t"
      "s_addc_u32 s41, s41, 0\n\t"
      "global_store_dwordx4 v244, v[132:135], s[40:41] sc1\n\t"
      "s_add_u32 s40, s40, 262144\n\t"
      "s_addc_u32 s41, s41, 0\n\t"
      "global_store_dwordx4 v244, v[132:135], s[40:41] sc1\n\t"
      "s_add_u32 s40, s40, 262144\n\t"
      "s_addc_u32 s41, s41, 0\n\t"
      "global_store_dwordx4 v244, v[132:135], s[40:41] sc1\n\t"
      "s_add_u32 s40, s40, 262144\n\t"
      "s_addc_u32 s41, s41, 0\n\t"
      "s_lshl_b32 s34, s25, 7\n\t"
      "s_add_u32 s34, s34, 16384\n\t"
      "v_mov_b32_e32 v131, s34\n\t"
      "s_waitcnt vmcnt(0)\n\t"
      "s_barrier\n\t"
      "s_cmp_lg_u32 s24, 0\n\t"
      "s_cbranch_scc1 SP0_%=\n\t"
      "s_mov_b64 exec, 1\n\t"
      "global_store_dwordx2 v131, v[128:129], s[60:61] sc1\n\t"
      "s_mov_b64 exec, -1\n\t"
      "SP0_%=:\n\t"
      "s_and_b32 s34, s25, 3\n\t"
      "s_lshl3_add_u32 s34, s34, s24\n\t"
      "s_cmp_lg_u32 s34, 19\n\t"
      "s_cbranch_scc1 SQ0_%=\n\t"
      "v_and_b32_e32 v136, 0x1f0, v244\n\t"
      "v_lshlrev_b32_e32 v131, 6, v136\n\t"
      "v_lshlrev_b32_e32 v136, 3, v136\n\t"
      "v_add_u32_e32 v136, 16384, v136\n\t"
      "s_and_b32 s34, s2, 7\n\t"
      "s_lshl_b32 s34, s34, 7\n\t"
      "s_add_u32 s34, s34, 32768\n\t"
      "v_add_u32_e32 v131, s34, v131\n\t"
      "s_cmp_lg_u32 s24, 3\n\t"
      "s_cbranch_scc1 SK15_%=\n\t"
      "global_load_dwordx2 v[138:139], v136, s[60:61] sc1\n\t"
      "SK15_%=:\n\t"
      "s_cmp_lg_u32 s24, 3\n\t"
      "s_cbranch_scc1 LF70B_%=\n\t"
      "s_waitcnt vmcnt(0)\n\t"
      "v_cmp_ne_u64_e32 vcc, s[22:23], v[138:139]\n\t"
      "s_cbranch_vccnz LF70R_%=\n\t"
      "LF70B_%=:\n\t"
      "global_store_dwordx2 v131, v[128:129], s[60:61] sc1\n\t"
      "SQ0_%=:\n\t"
      "s_barrier\n\t"
      "s_lshl3_add_u32 s34, s25, s24\n\t"
      "s_mul_i32 s34, s34, 7168\n\t"
      "s_add_u32 s34, s34, 10227712\n\t"
      "s_add_u32 s40, s20, s34\n\t"
      "s_addc_u32 s41, s21, 0\n\t"
      "global_store_dwordx4 v244, v[132:135], s[40:41] offset:-4096 sc1\n\t"
      "global_store_dwordx4 v244, v[132:135], s[40:41] offset:-3072 sc1\n\t"
      "global_store_dwordx4 v244, v[132:135], s[40:41] offset:-2048 sc1\n\t"
      "global_store_dwordx4 v244, v[132:135], s[40:41] offset:-1024 sc1\n\t"
      "global_store_dwordx4 v244, v[132:135], s[40:41] sc1\n\t"
      "global_store_dwordx4 v244, v[132:135], s[40:41] offset:1024 sc1\n\t"
      "global_store_dwordx4 v244, v[132:135], s[40:41] offset:2048 sc1\n\t"
      "s_lshl_b32 s34, s25, 7\n\t"
      "s_add_u32 s34, s34, 24576\n\t"
      "v_mov_b32_e32 v131, s34\n\t"
      "s_waitcnt vmcnt(0)\n\t"
      "s_barrier\n\t"
      "s_cmp_lg_u32 s24, 0\n\t"
      "s_cbranch_scc1 SP1_%=\n\t"
      "s_mov_b64 exec, 1\n\t"
      "global_store_dwordx2 v131, v[128:129], s[60:61] sc1\n\t"
      "s_mov_b64 exec, -1\n\t"
      "SP1_%=:\n\t"
      "s_and_b32 s34, s25, 3\n\t"
      "s_lshl3_add_u32 s34, s34, s24\n\t"
      "s_cmp_lg_u32 s34, 27\n\t"
      "s_cbranch_scc1 SQ1_%=\n\t"
      "v_and_b32_e32 v136, 0x1f0, v244\n\t"
      "v_lshlrev_b32_e32 v131, 6, v136\n\t"
      "v_lshlrev_b32_e32 v136, 3, v136\n\t"
      "v_add_u32_e32 v136, 24576, v136\n\t"
      "s_and_b32 s34, s2, 7\n\t"
      "s_lshl_b32 s34, s34, 7\n\t"
      "s_add_u32 s34, s34, 65536\n\t"
      "v_add_u32_e32 v131, s34, v131\n\t"
      "s_cmp_lg_u32 s24, 3\n\t"
      "s_cbranch_scc1 SK16_%=\n\t"
      "global_load_dwordx2 v[138:139], v136, s[60:61] sc1\n\t"
      "SK16_%=:\n\t"
      "s_cmp_lg_u32 s24, 3\n\t"
      "s_cbranch_scc1 LF71B_%=\n\t"
      "s_waitcnt vmcnt(0)\n\t"
      "v_cmp_ne_u64_e32 vcc, s[22:23], v[138:139]\n\t"
      "s_cbranch_vccnz LF71R_%=\n\t"
      "LF71B_%=:\n\t"
      "global_store_dwordx2 v131, v[128:129], s[60:61] sc1\n\t"
      "SQ1_%=:\n\t"
      "s_barrier\n\t"
      "s_and_b32 s35, s25, 3\n\t"
      "s_lshl2_add_u32 s31, s26, s35\n\t"
      "s_and_b32 s35, s2, 7\n\t"
      "s_mul_i32 s34, s35, 14\n\t"
      "s_lshl4_add_u32 s35, s35, -14\n\t"
      "s_cmp_lt_u32 s31, 14\n\t"
      "s_cselect_b32 s34, s34, s35\n\t"
      "s_cselect_b32 s36, 1, 0\n\t"
      "s_or_b32 s27, s27, s36\n\t"
      "s_add_u32 s34, s34, s31\n\t"
      "s_lshr_b32 s29, s34, 2\n\t"
      "s_and_b32 s32, s34, 3\n\t"
      "s_mul_i32 s30, s32, 7\n\t"
      "v_and_b32_e32 v199, 60, v192\n\t"
      "v_and_b32_e32 v200, 24, v192\n\t"
      "v_lshlrev_b32_e32 v202, 3, v196\n\t"
      "v_lshl_or_b32 v202, v200, 2, v202\n\t"
      "v_lshl_or_b32 v202, v195, 7, v202\n\t"
      "v_lshl_or_b32 v202, v193, 10, v202\n\t"
      "v_add_u32_e32 v224, s39, v202\n\t"
      "v_xor_b32_e32 v225, 32, v224\n\t"
      "v_xor_b32_e32 v226, 64, v224\n\t"
      "v_xor_b32_e32 v227, 96, v224\n\t"
      "s_lshl_b32 s34, s24, 12\n\t"
      "s_add_u32 s34, s34, 98304\n\t"
      "v_add_u32_e32 v245, s34, v244\n\t"
      "v_lshlrev_b32_e32 v199, 2, v199\n\t"
      "s_lshl_b32 s34, s24, 8\n\t"
      "v_lshl_add_u32 v247, v192, 2, s34\n\t"
      "v_and_b32_e32 v200, 2, v192\n\t"
      "v_mul_u32_u24_e32 v202, 576, v200\n\t"
      "v_xor_b32_e32 v199, v202, v199\n\t"
      "v_and_b32_e32 v202, 1, v192\n\t"
      "v_lshl_or_b32 v199, v202, 3, v199\n\t"
      "s_and_b32 s35, s34, 0x400\n\t"
      "s_add_u32 s34, s34, s35\n\t"
      "s_add_u32 s34, s34, 98304\n\t"
      "v_add_u32_e32 v246, s34, v199\n\t"
      "v_lshlrev_b32_e32 v199, 2, v202\n\t"
      "v_lshl_or_b32 v199, v200, 4, v199\n\t"
      "s_min_u32 s34, s29, 27\n\t"
      "s_lshl_b32 s34, s34, 7\n\t"
      "s_lshr_b32 s35, s24, 2\n\t"
      "s_lshl_b32 s35, s35, 6\n\t"
      "s_add_u32 s34, s34, s35\n\t"
      "s_lshr_b32 s35, s39, 9\n\t"
      "s_add_u32 s34, s34, s35\n\t"
      "v_add_u32_e32 v248, s34, v199\n\t"
      "s_add_u32 s48, s20, 8388608\n\t"
      "s_addc_u32 s49, s21, 0\n\t"
      "s_cmp_ge_u32 s31, 30\n\t"
      "s_cbranch_scc1 BYE_%=\n\t"
      "s_setprio 0\n\t"
      "s_waitcnt vmcnt(0)\n\t"
      "s_barrier\n\t"
      "s_branch G4_%=\n\t"
      "BYE_%=:\n\t"
      "s_endpgm\n\t"
      "LA30R_%=:\n\t"
      "s_mov_b32 s80, 0\n\t"
      "LA30L_%=:\n\t"
      "s_add_u32 s80, s80, 1\n\t"
      "s_cmp_lt_u32 s80, 262144\n\t"
      "s_cbranch_scc0 LA30X_%=\n\t"
      "s_waitcnt vmcnt(0)\n\t"
      "s_sleep 1\n\t"
      "global_load_dwordx4 v[128:131], v250, s[64:65] offset:-4096 sc1\n\t"
      "global_load_dwordx4 v[132:135], v250, s[64:65] offset:-3072 sc1\n\t"
      "global_load_dwordx4 v[136:139], v250, s[64:65] offset:-2048 sc1\n\t"
      "global_load_dwordx4 v[140:143], v250, s[64:65] offset:-1024 sc1\n\t"
      "global_load_dwordx4 v[144:147], v250, s[64:65] sc1\n\t"
      "global_load_dwordx4 v[148:151], v250, s[64:65] offset:1024 sc1\n\t"
      "global_load_dwordx4 v[152:155], v250, s[64:65] offset:2048 sc1\n\t"
      "global_load_dwordx4 v[156:159], v250, s[64:65] offset:3072 sc1\n\t"
      "s_waitcnt vmcnt(0)\n\t"
      "v_max3_u32 v251, v128, v129, v130\n\t"
      "v_max3_u32 v251, v251, v131, v132\n\t"
      "v_max3_u32 v251, v251, v133, v134\n\t"
      "v_max3_u32 v251, v251, v135, v136\n\t"
      "v_max3_u32 v251, v251, v137, v138\n\t"
      "v_max3_u32 v251, v251, v139, v140\n\t"
      "v_max3_u32 v251, v251, v141, v142\n\t"
      "v_max3_u32 v251, v251, v143, v144\n\t"
      "v_max3_u32 v251, v251, v145, v146\n\t"
      "v_max3_u32 v251, v251, v147, v148\n\t"
      "v_max3_u32 v251, v251, v149, v150\n\t"
      "v_max3_u32 v251, v251, v151, v152\n\t"
      "v_max3_u32 v251, v251, v153, v154\n\t"
      "v_max3_u32 v251, v251, v155, v156\n\t"
      "v_max3_u32 v251, v251, v157, v158\n\t"
      "v_max_u32_e32 v251, v251, v159\n\t"
      "v_cmp_eq_u32_e32 vcc, -1, v251\n\t"
      "s_cbranch_vccnz LA30L_%=\n\t"
      "LA30X_%=:\n\t"
      "s_branch LA30B_%=\n\t"
      "LF6R_%=:\n\t"
      "s_mov_b32 s80, 0\n\t"
      "LF6L_%=:\n\t"
      "s_add_u32 s80, s80, 1\n\t"
      "s_cmp_lt_u32 s80, 262144\n\t"
      "s_cbranch_scc0 LF6X_%=\n\t"
      "s_sleep 2\n\t"
      "global_load_dwordx2 v[228:229], v230, s[60:61] sc1\n\t"
      "s_waitcnt vmcnt(0)\n\t"
      "v_cmp_ne_u64_e32 vcc, s[22:23], v[228:229]\n\t"
      "s_cbranch_vccnz LF6L_%=\n\t"
      "LF6X_%=:\n\t"
      "s_branch LF6B_%=\n\t"
      "LA31R_%=:\n\t"
      "s_mov_b32 s80, 0\n\t"
      "LA31L_%=:\n\t"
      "s_add_u32 s80, s80, 1\n\t"
      "s_cmp_lt_u32 s80, 262144\n\t"
      "s_cbranch_scc0 LA31X_%=\n\t"
      "s_waitcnt vmcnt(0)\n\t"
      "s_sleep 1\n\t"
      "global_load_dwordx4 v[160:163], v250, s[66:67] offset:-4096 sc1\n\t"
      "global_load_dwordx4 v[164:167], v250, s[66:67] offset:-3072 sc1\n\t"
      "global_load_dwordx4 v[168:171], v250, s[66:67] offset:-2048 sc1\n\t"
      "global_load_dwordx4 v[172:175], v250, s[66:67] offset:-1024 sc1\n\t"
      "global_load_dwordx4 v[176:179], v250, s[66:67] sc1\n\t"
      "global_load_dwordx4 v[180:183], v250, s[66:67] offset:1024 sc1\n\t"
      "global_load_dwordx4 v[184:187], v250, s[66:67] offset:2048 sc1\n\t"
      "global_load_dwordx4 v[188:191], v250, s[66:67] offset:3072 sc1\n\t"
      "s_waitcnt vmcnt(0)\n\t"
      "v_max3_u32 v251, v160, v161, v162\n\t"
      "v_max3_u32 v251, v251, v163, v164\n\t"
      "v_max3_u32 v251, v251, v165, v166\n\t"
      "v_max3_u32 v251, v251, v167, v168\n\t"
      "v_max3_u32 v251, v251, v169, v170\n\t"
      "v_max3_u32 v251, v251, v171, v172\n\t"
      "v_max3_u32 v251, v251, v173, v174\n\t"
      "v_max3_u32 v251, v251, v175, v176\n\t"
      "v_max3_u32 v251, v251, v177, v178\n\t"
      "v_max3_u32 v251, v251, v179, v180\n\t"
      "v_max3_u32 v251, v251, v181, v182\n\t"
      "v_max3_u32 v251, v251, v183, v184\n\t"
      "v_max3_u32 v251, v251, v185, v186\n\t"
      "v_max3_u32 v251, v251, v187, v188\n\t"
      "v_max3_u32 v251, v251, v189, v190\n\t"
      "v_max_u32_e32 v251, v251, v191\n\t"
      "v_cmp_eq_u32_e32 vcc, -1, v251\n\t"
      "s_cbranch_vccnz LA31L_%=\n\t"
      "LA31X_%=:\n\t"
      "v_mfma_f32_16x16x32_bf16 v[224:227], v[0:3], v[160:163], 0\n\t"
      "v_mfma_f32_16x16x32_bf16 v[224:227], v[16:19], v[164:167], v[224:227]\n\t"
      "v_mfma_f32_16x16x32_bf16 v[224:227], v[32:35], v[168:171], v[224:227]\n\t"
      "v_mfma_f32_16x16x32_bf16 v[224:227], v[48:51], v[172:175], v[224:227]\n\t"
      "v_mfma_f32_16x16x32_bf16 v[224:227], v[64:67], v[176:179], v[224:227]\n\t"
      "v_mfma_f32_16x16x32_bf16 v[224:227], v[80:83], v[180:183], v[224:227]\n\t"
      "v_mfma_f32_16x16x32_bf16 v[224:227], v[96:99], v[184:187], v[224:227]\n\t"
      "v_mfma_f32_16x16x32_bf16 v[224:227], v[112:115], v[188:191], v[224:227]\n\t"
      "v_mfma_f32_16x16x32_bf16 v[208:211], v[4:7], v[160:163], 0\n\t"
      "v_mfma_f32_16x16x32_bf16 v[208:211], v[20:23], v[164:167], v[208:211]\n\t"
      "v_mfma_f32_16x16x32_bf16 v[208:211], v[36:39], v[168:171], v[208:211]\n\t"
      "s_nop 7\n\t"
      "s_branch LA31B_%=\n\t"
      "LA32R_%=:\n\t"
      "s_mov_b32 s80, 0\n\t"
      "LA32L_%=:\n\t"
      "s_add_u32 s80, s80, 1\n\t"
      "s_cmp_lt_u32 s80, 262144\n\t"
      "s_cbranch_scc0 LA32X_%=\n\t"
      "s_waitcnt vmcnt(0)\n\t"
      "s_sleep 1\n\t"
      "global_load_dwordx4 v[192:195], v250, s[68:69] offset:-4096 sc1\n\t"
      "global_load_dwordx4 v[196:199], v250, s[68:69] offset:-3072 sc1\n\t"
      "global_load_dwordx4 v[200:203], v250, s[68:69] offset:-2048 sc1\n\t"
      "global_load_dwordx4 v[204:207], v250, s[68:69] offset:-1024 sc1\n\t"
      "global_load_dwordx4 v[128:131], v250, s[68:69] sc1\n\t"
      "global_load_dwordx4 v[132:135], v250, s[68:69] offset:1024 sc1\n\t"
      "global_load_dwordx4 v[136:139], v250, s[68:69] offset:2048 sc1\n\t"
      "global_load_dwordx4 v[140:143], v250, s[68:69] offset:3072 sc1\n\t"
      "s_waitcnt vmcnt(0)\n\t"
      "v_max3_u32 v251, v192, v193, v194\n\t"
      "v_max3_u32 v251, v251, v195, v196\n\t"
      "v_max3_u32 v251, v251, v197, v198\n\t"
      "v_max3_u32 v251, v251, v199, v200\n\t"
      "v_max3_u32 v251, v251, v201, v202\n\t"
      "v_max3_u32 v251, v251, v203, v204\n\t"
      "v_max3_u32 v251, v251, v205, v206\n\t"
      "v_max3_u32 v251, v251, v207, v128\n\t"
      "v_max3_u32 v251, v251, v129, v130\n\t"
      "v_max3_u32 v251, v251, v131, v132\n\t"
      "v_max3_u32 v251, v251, v133, v134\n\t"
      "v_max3_u32 v251, v251, v135, v136\n\t"
      "v_max3_u32 v251, v251, v137, v138\n\t"
      "v_max3_u32 v251, v251, v139, v140\n\t"
      "v_max3_u32 v251, v251, v141, v142\n\t"
      "v_max_u32_e32 v251, v251, v143\n\t"
      "v_cmp_eq_u32_e32 vcc, -1, v251\n\t"
      "s_cbranch_vccnz LA32L_%=\n\t"
      "LA32X_%=:\n\t"
      "v_mfma_f32_16x16x32_bf16 v[220:223], v[0:3], v[192:195], 0\n\t"
      "v_mfma_f32_16x16x32_bf16 v[220:223], v[16:19], v[196:199], v[220:223]\n\t"
      "v_mfma_f32_16x16x32_bf16 v[220:223], v[32:35], v[200:203], v[220:223]\n\t"
      "v_mfma_f32_16x16x32_bf16 v[220:223], v[48:51], v[204:207], v[220:223]\n\t"
      "v_mfma_f32_16x16x32_bf16 v[220:223], v[64:67], v[128:131], v[220:223]\n\t"
      "v_mfma_f32_16x16x32_bf16 v[220:223], v[80:83], v[132:135], v[220:223]\n\t"
      "v_mfma_f32_16x16x32_bf16 v[220:223], v[96:99], v[136:139], v[220:223]\n\t"
      "v_mfma_f32_16x16x32_bf16 v[220:223], v[112:115], v[140:143], v[220:223]\n\t"
      "v_mfma_f32_16x16x32_bf16 v[224:227], v[4:7], v[192:195], 0\n\t"
      "v_mfma_f32_16x16x32_bf16 v[224:227], v[20:23], v[196:199], v[224:227]\n\t"
      "v_mfma_f32_16x16x32_bf16 v[224:227], v[36:39], v[200:203], v[224:227]\n\t"
      "s_nop 7\n\t"
      "s_branch LA32B_%=\n\t"
      "LA33R_%=:\n\t"
      "s_mov_b32 s80, 0\n\t"
      "LA33L_%=:\n\t"
      "s_add_u32 s80, s80, 1\n\t"
      "s_cmp_lt_u32 s80, 262144\n\t"
      "s_cbranch_scc0 LA33X_%=\n\t"
      "s_waitcnt vmcnt(0)\n\t"
      "s_sleep 1\n\t"
      "global_load_dwordx4 v[144:147], v250, s[70:71] offset:-4096 sc1\n\t"
      "global_load_dwordx4 v[148:151], v250, s[70:71] offset:-3072 sc1\n\t"
      "global_load_dwordx4 v[152:155], v250, s[70:71] offset:-2048 sc1\n\t"
      "global_load_dwordx4 v[156:159], v250, s[70:71] offset:-1024 sc1\n\t"
      "global_load_dwordx4 v[160:163], v250, s[70:71] sc1\n\t"
      "global_load_dwordx4 v[164:167], v250, s[70:71] offset:1024 sc1\n\t"
      "global_load_dwordx4 v[168:171], v250, s[70:71] offset:2048 sc1\n\t"
      "global_load_dwordx4 v[172:175], v250, s[70:71] offset:3072 sc1\n\t"
      "s_waitcnt vmcnt(0)\n\t"
      "v_max3_u32 v251, v144, v145, v146\n\t"
      "v_max3_u32 v251, v251, v147, v148\n\t"
      "v_max3_u32 v251, v251, v149, v150\n\t"
      "v_max3_u32 v251, v251, v151, v152\n\t"
      "v_max3_u32 v251, v251, v153, v154\n\t"
      "v_max3_u32 v251, v251, v155, v156\n\t"
      "v_max3_u32 v251, v251, v157, v158\n\t"
      "v_max3_u32 v251, v251, v159, v160\n\t"
      "v_max3_u32 v251, v251, v161, v162\n\t"
      "v_max3_u32 v251, v251, v163, v164\n\t"
      "v_max3_u32 v251, v251, v165, v166\n\t"
      "v_max3_u32 v251, v251, v167, v168\n\t"
      "v_max3_u32 v251, v251, v169, v170\n\t"
      "v_max3_u32 v251, v251, v171, v172\n\t"
      "v_max3_u32 v251, v251, v173, v174\n\t"
      "v_max_u32_e32 v251, v251, v175\n\t"
      "v_cmp_eq_u32_e32 vcc, -1, v251\n\t"
      "s_cbranch_vccnz LA33L_%=\n\t"
      "LA33X_%=:\n\t"
      "v_mfma_f32_16x16x32_bf16 v[216:219], v[0:3], v[144:147], 0\n\t"
      "v_mfma_f32_16x16x32_bf16 v[216:219], v[16:19], v[148:151], v[216:219]\n\t"
      "v_mfma_f32_16x16x32_bf16 v[216:219], v[32:35], v[152:155], v[216:219]\n\t"
      "v_mfma_f32_16x16x32_bf16 v[216:219], v[48:51], v[156:159], v[216:219]\n\t"
      "v_mfma_f32_16x16x32_bf16 v[216:219], v[64:67], v[160:163], v[216:219]\n\t"
      "v_mfma_f32_16x16x32_bf16 v[216:219], v[80:83], v[164:167], v[216:219]\n\t"
      "v_mfma_f32_16x16x32_bf16 v[216:219], v[96:99], v[168:171], v[216:219]\n\t"
      "v_mfma_f32_16x16x32_bf16 v[216:219], v[112:115], v[172:175], v[216:219]\n\t"
      "v_mfma_f32_16x16x32_bf16 v[220:223], v[4:7], v[144:147], 0\n\t"
      "v_mfma_f32_16x16x32_bf16 v[220:223], v[20:23], v[148:151], v[220:223]\n\t"
      "v_mfma_f32_16x16x32_bf16 v[220:223], v[36:39], v[152:155], v[220:223]\n\t"
      "s_nop 7\n\t"
      "s_branch LA33B_%=\n\t"
      "LA34R_%=:\n\t"
      "s_mov_b32 s80, 0\n\t"
      "LA34L_%=:\n\t"
      "s_add_u32 s80, s80, 1\n\t"
      "s_cmp_lt_u32 s80, 262144\n\t"
      "s_cbranch_scc0 LA34X_%=\n\t"
      "s_waitcnt vmcnt(0)\n\t"
      "s_sleep 1\n\t"
      "global_load_dwordx4 v[176:179], v250, s[72:73] offset:-4096 sc1\n\t"
      "global_load_dwordx4 v[180:183], v250, s[72:73] offset:-3072 sc1\n\t"
      "global_load_dwordx4 v[184:187], v250, s[72:73] offset:-2048 sc1\n\t"
      "global_load_dwordx4 v[188:191], v250, s[72:73] offset:-1024 sc1\n\t"
      "global_load_dwordx4 v[192:195], v250, s[72:73] sc1\n\t"
      "global_load_dwordx4 v[196:199], v250, s[72:73] offset:1024 sc1\n\t"
      "global_load_dwordx4 v[200:203], v250, s[72:73] offset:2048 sc1\n\t"
      "global_load_dwordx4 v[204:207], v250, s[72:73] offset:3072 sc1\n\t"
      "s_waitcnt vmcnt(0)\n\t"
      "v_max3_u32 v251, v176, v177, v178\n\t"
      "v_max3_u32 v251, v251, v179, v180\n\t"
      "v_max3_u32 v251, v251, v181, v182\n\t"
      "v_max3_u32 v251, v251, v183, v184\n\t"
      "v_max3_u32 v251, v251, v185, v186\n\t"
      "v_max3_u32 v251, v251, v187, v188\n\t"
      "v_max3_u32 v251, v251, v189, v190\n\t"
      "v_max3_u32 v251, v251, v191, v192\n\t"
      "v_max3_u32 v251, v251, v193, v194\n\t"
      "v_max3_u32 v251, v251, v195, v196\n\t"
      "v_max3_u32 v251, v251, v197, v198\n\t"
      "v_max3_u32 v251, v251, v199, v200\n\t"
      "v_max3_u32 v251, v251, v201, v202\n\t"
      "v_max3_u32 v251, v251, v203, v204\n\t"
      "v_max3_u32 v251, v251, v205, v206\n\t"
      "v_max_u32_e32 v251, v251, v207\n\t"
      "v_cmp_eq_u32_e32 vcc, -1, v251\n\t"
      "s_cbranch_vccnz LA34L_%=\n\t"
      "LA34X_%=:\n\t"
      "v_mfma_f32_16x16x32_bf16 v[212:215], v[0:3], v[176:179], 0\n\t"
      "v_mfma_f32_16x16x32_bf16 v[212:215], v[16:19], v[180:183], v[212:215]\n\t"
      "v_mfma_f32_16x16x32_bf16 v[212:215], v[32:35], v[184:187], v[212:215]\n\t"
      "v_mfma_f32_16x16x32_bf16 v[212:215], v[48:51], v[188:191], v[212:215]\n\t"
      "v_mfma_f32_16x16x32_bf16 v[212:215], v[64:67], v[192:195], v[212:215]\n\t"
      "v_mfma_f32_16x16x32_bf16 v[212:215], v[80:83], v[196:199], v[212:215]\n\t"
      "v_mfma_f32_16x16x32_bf16 v[212:215], v[96:99], v[200:203], v[212:215]\n\t"
      "v_mfma_f32_16x16x32_bf16 v[212:215], v[112:115], v[204:207], v[212:215]\n\t"
      "v_mfma_f32_16x16x32_bf16 v[216:219], v[4:7], v[176:179], 0\n\t"
      "v_mfma_f32_16x16x32_bf16 v[216:219], v[20:23], v[180:183], v[216:219]\n\t"
      "v_mfma_f32_16x16x32_bf16 v[216:219], v[36:39], v[184:187], v[216:219]\n\t"
      "s_nop 7\n\t"
      "s_branch LA34B_%=\n\t"
      "LA35R_%=:\n\t"
      "s_mov_b32 s80, 0\n\t"
      "LA35L_%=:\n\t"
      "s_add_u32 s80, s80, 1\n\t"
      "s_cmp_lt_u32 s80, 262144\n\t"
      "s_cbranch_scc0 LA35X_%=\n\t"
      "s_waitcnt vmcnt(0)\n\t"
      "s_sleep 1\n\t"
      "global_load_dwordx4 v[128:131], v250, s[74:75] offset:-4096 sc1\n\t"
      "global_load_dwordx4 v[132:135], v250, s[74:75] offset:-3072 sc1\n\t"
      "global_load_dwordx4 v[136:139], v250, s[74:75] offset:-2048 sc1\n\t"
      "global_load_dwordx4 v[140:143], v250, s[74:75] offset:-1024 sc1\n\t"
      "global_load_dwordx4 v[144:147], v250, s[74:75] sc1\n\t"
      "global_load_dwordx4 v[148:151], v250, s[74:75] offset:1024 sc1\n\t"
      "global_load_dwordx4 v[152:155], v250, s[74:75] offset:2048 sc1\n\t"
      "global_load_dwordx4 v[156:159], v250, s[74:75] offset:3072 sc1\n\t"
      "s_waitcnt vmcnt(0)\n\t"
      "v_max3_u32 v251, v128, v129, v130\n\t"
      "v_max3_u32 v251, v251, v131, v132\n\t"
      "v_max3_u32 v251, v251, v133, v134\n\t"
      "v_max3_u32 v251, v251, v135, v136\n\t"
      "v_max3_u32 v251, v251, v137, v138\n\t"
      "v_max3_u32 v251, v251, v139, v140\n\t"
      "v_max3_u32 v251, v251, v141, v142\n\t"
      "v_max3_u32 v251, v251, v143, v144\n\t"
      "v_max3_u32 v251, v251, v145, v146\n\t"
      "v_max3_u32 v251, v251, v147, v148\n\t"
      "v_max3_u32 v251, v251, v149, v150\n\t"
      "v_max3_u32 v251, v251, v151, v152\n\t"
      "v_max3_u32 v251, v251, v153, v154\n\t"
      "v_max3_u32 v251, v251, v155, v156\n\t"
      "v_max3_u32 v251, v251, v157, v158\n\t"
      "v_max_u32_e32 v251, v251, v159\n\t"
      "v_cmp_eq_u32_e32 vcc, -1, v251\n\t"
      "s_cbranch_vccnz LA35L_%=\n\t"
      "LA35X_%=:\n\t"
      "v_mfma_f32_16x16x32_bf16 v[208:211], v[0:3], v[128:131], 0\n\t"
      "v_mfma_f32_16x16x32_bf16 v[208:211], v[16:19], v[132:135], v[208:211]\n\t"
      "v_mfma_f32_16x16x32_bf16 v[208:211], v[32:35], v[136:139], v[208:211]\n\t"
      "v_mfma_f32_16x16x32_bf16 v[208:211], v[48:51], v[140:143], v[208:211]\n\t"
      "v_mfma_f32_16x16x32_bf16 v[208:211], v[64:67], v[144:147], v[208:211]\n\t"
      "v_mfma_f32_16x16x32_bf16 v[208:211], v[80:83], v[148:151], v[208:211]\n\t"
      "v_mfma_f32_16x16x32_bf16 v[208:211], v[96:99], v[152:155], v[208:211]\n\t"
      "v_mfma_f32_16x16x32_bf16 v[208:211], v[112:115], v[156:159], v[208:211]\n\t"
      "v_mfma_f32_16x16x32_bf16 v[212:215], v[4:7], v[128:131], 0\n\t"
      "v_mfma_f32_16x16x32_bf16 v[212:215], v[20:23], v[132:135], v[212:215]\n\t"
      "v_mfma_f32_16x16x32_bf16 v[212:215], v[36:39], v[136:139], v[212:215]\n\t"
      "s_nop 7\n\t"
      "s_branch LA35B_%=\n\t"
      "LA36R_%=:\n\t"
      "s_mov_b32 s80, 0\n\t"
      "LA36L_%=:\n\t"
      "s_add_u32 s80, s80, 1\n\t"
      "s_cmp_lt_u32 s80, 262144\n\t"
      "s_cbranch_scc0 LA36X_%=\n\t"
      "s_waitcnt vmcnt(0)\n\t"
      "s_sleep 1\n\t"
      "global_load_dwordx4 v[160:163], v250, s[76:77] offset:-4096 sc1\n\t"
      "global_load_dwordx4 v[164:167], v250, s[76:77] offset:-3072 sc1\n\t"
      "global_load_dwordx4 v[168:171], v250, s[76:77] offset:-2048 sc1\n\t"
      "global_load_dwordx4 v[172:175], v250, s[76:77] offset:-1024 sc1\n\t"
      "global_load_dwordx4 v[176:179], v250, s[76:77] sc1\n\t"
      "global_load_dwordx4 v[180:183], v250, s[76:77] offset:1024 sc1\n\t"
      "global_load_dwordx4 v[184:187], v250, s[76:77] offset:2048 sc1\n\t"
      "global_load_dwordx4 v[188:191], v250, s[76:77] offset:3072 sc1\n\t"
      "s_waitcnt vmcnt(0)\n\t"
      "v_max3_u32 v251, v160, v161, v162\n\t"
      "v_max3_u32 v251, v251, v163, v164\n\t"
      "v_max3_u32 v251, v251, v165, v166\n\t"
      "v_max3_u32 v251, v251, v167, v168\n\t"
      "v_max3_u32 v251, v251, v169, v170\n\t"
      "v_max3_u32 v251, v251, v171, v172\n\t"
      "v_max3_u32 v251, v251, v173, v174\n\t"
      "v_max3_u32 v251, v251, v175, v176\n\t"
      "v_max3_u32 v251, v251, v177, v178\n\t"
      "v_max3_u32 v251, v251, v179, v180\n\t"
      "v_max3_u32 v251, v251, v181, v182\n\t"
      "v_max3_u32 v251, v251, v183, v184\n\t"
      "v_max3_u32 v251, v251, v185, v186\n\t"
      "v_max3_u32 v251, v251, v187, v188\n\t"
      "v_max3_u32 v251, v251, v189, v190\n\t"
      "v_max_u32_e32 v251, v251, v191\n\t"
      "v_cmp_eq_u32_e32 vcc, -1, v251\n\t"
      "s_cbranch_vccnz LA36L_%=\n\t"
      "LA36X_%=:\n\t"
      "v_mfma_f32_16x16x32_bf16 v[224:227], v[0:3], v[160:163], 0\n\t"
      "v_mfma_f32_16x16x32_bf16 v[224:227], v[16:19], v[164:167], v[224:227]\n\t"
      "v_mfma_f32_16x16x32_bf16 v[224:227], v[32:35], v[168:171], v[224:227]\n\t"
      "v_mfma_f32_16x16x32_bf16 v[224:227], v[48:51], v[172:175], v[224:227]\n\t"
      "v_mfma_f32_16x16x32_bf16 v[224:227], v[64:67], v[176:179], v[224:227]\n\t"
      "v_mfma_f32_16x16x32_bf16 v[224:227], v[80:83], v[180:183], v[224:227]\n\t"
      "v_mfma_f32_16x16x32_bf16 v[224:227], v[96:99], v[184:187], v[224:227]\n\t"
      "v_mfma_f32_16x16x32_bf16 v[224:227], v[112:115], v[188:191], v[224:227]\n\t"
      "v_mfma_f32_16x16x32_bf16 v[208:211], v[4:7], v[160:163], 0\n\t"
      "v_mfma_f32_16x16x32_bf16 v[208:211], v[20:23], v[164:167], v[208:211]\n\t"
      "v_mfma_f32_16x16x32_bf16 v[208:211], v[36:39], v[168:171], v[208:211]\n\t"
      "s_nop 7\n\t"
      "s_branch LA36B_%=\n\t"
      "LF5R_%=:\n\t"
      "s_mov_b32 s80, 0\n\t"
      "LF5L_%=:\n\t"
      "s_add_u32 s80, s80, 1\n\t"
      "s_cmp_lt_u32 s80, 262144\n\t"
      "s_cbranch_scc0 LF5X_%=\n\t"
      "s_sleep 2\n\t"
      "global_load_dwordx2 v[138:139], v143, s[60:61] sc1\n\t"
      "s_waitcnt vmcnt(0)\n\t"
      "v_cmp_ne_u64_e32 vcc, s[22:23], v[138:139]\n\t"
      "s_cbranch_vccnz LF5L_%=\n\t"
      "LF5X_%=:\n\t"
      "s_branch LF5B_%=\n\t"
      "PGR_%=:\n\t"
      "s_mov_b32 s80, 0\n\t"
      "PGL_%=:\n\t"
      "s_add_u32 s80, s80, 1\n\t"
      "s_cmp_lt_u32 s80, 262144\n\t"
      "s_cbranch_scc0 PGX_%=\n\t"
      "s_sleep 2\n\t"
      "global_load_dword v251, v253, s[52:53] offset:252 sc1\n\t"
      "s_waitcnt vmcnt(0)\n\t"
      "v_cmp_eq_u32_e32 vcc, -1, v251\n\t"
      "s_cbranch_vccnz PGL_%=\n\t"
      "PGX_%=:\n\t"
      "s_sleep 5\n\t"
      "s_branch PGB_%=\n\t"
      "LF8R_%=:\n\t"
      "s_mov_b32 s80, 0\n\t"
      "LF8L_%=:\n\t"
      "s_add_u32 s80, s80, 1\n\t"
      "s_cmp_lt_u32 s80, 262144\n\t"
      "s_cbranch_scc0 LF8X_%=\n\t"
      "s_sleep 2\n\t"
      "global_load_dwordx2 v[240:241], v242, s[60:61] sc1\n\t"
      "s_waitcnt vmcnt(0)\n\t"
      "v_cmp_ne_u64_e32 vcc, s[22:23], v[240:241]\n\t"
      "s_cbranch_vccnz LF8L_%=\n\t"
      "LF8X_%=:\n\t"
      "s_branch LF8B_%=\n\t"
      "LA40R_%=:\n\t"
      "s_mov_b32 s80, 0\n\t"
      "LA40L_%=:\n\t"
      "s_add_u32 s80, s80, 1\n\t"
      "s_cmp_lt_u32 s80, 262144\n\t"
      "s_cbranch_scc0 LA40X_%=\n\t"
      "s_waitcnt vmcnt(0)\n\t"
      "PQ0R_%=:\n\t"
      "PQ0L_%=:\n\t"
      "s_add_u32 s80, s80, 1\n\t"
      "s_cmp_lt_u32 s80, 262144\n\t"
      "s_cbranch_scc0 PQ0X_%=\n\t"
      "s_sleep 1\n\t"
      "global_load_dword v251, v252, s[64:65] offset:252 sc1\n\t"
      "s_waitcnt vmcnt(0)\n\t"
      "v_cmp_eq_u32_e32 vcc, -1, v251\n\t"
      "s_cbranch_vccnz PQ0L_%=\n\t"
      "PQ0X_%=:\n\t"
      "s_add_u32 s58, s64, s40\n\t"
      "s_addc_u32 s59, s65, 0\n\t"
      "global_load_dwordx4 v[128:131], v244, s[58:59] sc1\n\t"
      "s_add_u32 s58, s64, s41\n\t"
      "s_addc_u32 s59, s65, 0\n\t"
      "global_load_dwordx4 v[132:135], v244, s[58:59] sc1\n\t"
      "s_add_u32 s58, s64, s42\n\t"
      "s_addc_u32 s59, s65, 0\n\t"
      "global_load_dwordx4 v[136:139], v244, s[58:59] sc1\n\t"
      "s_add_u32 s58, s64, s43\n\t"
      "s_addc_u32 s59, s65, 0\n\t"
      "global_load_dwordx4 v[140:143], v244, s[58:59] sc1\n\t"
      "s_add_u32 s58, s64, s44\n\t"
      "s_addc_u32 s59, s65, 0\n\t"
      "global_load_dwordx4 v[144:147], v244, s[58:59] sc1\n\t"
      "s_add_u32 s58, s64, s45\n\t"
      "s_addc_u32 s59, s65, 0\n\t"
      "global_load_dwordx4 v[148:151], v244, s[58:59] sc1\n\t"
      "s_add_u32 s58, s64, s46\n\t"
      "s_addc_u32 s59, s65, 0\n\t"
      "global_load_dwordx4 v[152:155], v244, s[58:59] sc1\n\t"
      "s_waitcnt vmcnt(0)\n\t"
      "v_max3_u32 v251, v128, v129, v130\n\t"
      "v_max3_u32 v251, v251, v131, v132\n\t"
      "v_max3_u32 v251, v251, v133, v134\n\t"
      "v_max3_u32 v251, v251, v135, v136\n\t"
      "v_max3_u32 v251, v251, v137, v138\n\t"
      "v_max3_u32 v251, v251, v139, v140\n\t"
      "v_max3_u32 v251, v251, v141, v142\n\t"
      "v_max3_u32 v251, v251, v143, v144\n\t"
      "v_max3_u32 v251, v251, v145, v146\n\t"
      "v_max3_u32 v251, v251, v147, v148\n\t"
      "v_max3_u32 v251, v251, v149, v150\n\t"
      "v_max3_u32 v251, v251, v151, v152\n\t"
      "v_max3_u32 v251, v251, v153, v154\n\t"
      "v_max_u32_e32 v251, v251, v155\n\t"
      "v_cmp_eq_u32_e32 vcc, -1, v251\n\t"
      "s_cbranch_vccnz LA40L_%=\n\t"
      "LA40X_%=:\n\t"
      "v_mfma_f32_16x16x32_bf16 v[196:199], v[128:131], v[0:3], 0\n\t"
      "v_mfma_f32_16x16x32_bf16 v[200:203], v[128:131], v[4:7], 0\n\t"
      "v_mfma_f32_16x16x32_bf16 v[204:207], v[128:131], v[8:11], 0\n\t"
      "v_mfma_f32_16x16x32_bf16 v[208:211], v[128:131], v[12:15], 0\n\t"
      "v_mfma_f32_16x16x32_bf16 v[196:199], v[132:135], v[16:19], v[196:199]\n\t"
      "v_mfma_f32_16x16x32_bf16 v[200:203], v[132:135], v[20:23], v[200:203]\n\t"
      "v_mfma_f32_16x16x32_bf16 v[204:207], v[132:135], v[24:27], v[204:207]\n\t"
      "v_mfma_f32_16x16x32_bf16 v[208:211], v[132:135], v[28:31], v[208:211]\n\t"
      "v_mfma_f32_16x16x32_bf16 v[196:199], v[136:139], v[32:35], v[196:199]\n\t"
      "v_mfma_f32_16x16x32_bf16 v[200:203], v[136:139], v[36:39], v[200:203]\n\t"
      "v_mfma_f32_16x16x32_bf16 v[204:207], v[136:139], v[40:43], v[204:207]\n\t"
      "v_mfma_f32_16x16x32_bf16 v[208:211], v[136:139], v[44:47], v[208:211]\n\t"
      "v_mfma_f32_16x16x32_bf16 v[196:199], v[140:143], v[48:51], v[196:199]\n\t"
      "v_mfma_f32_16x16x32_bf16 v[200:203], v[140:143], v[52:55], v[200:203]\n\t"
      "v_mfma_f32_16x16x32_bf16 v[204:207], v[140:143], v[56:59], v[204:207]\n\t"
      "v_mfma_f32_16x16x32_bf16 v[208:211], v[140:143], v[60:63], v[208:211]\n\t"
      "v_mfma_f32_16x16x32_bf16 v[196:199], v[144:147], v[64:67], v[196:199]\n\t"
      "v_mfma_f32_16x16x32_bf16 v[200:203], v[144:147], v[68:71], v[200:203]\n\t"
      "v_mfma_f32_16x16x32_bf16 v[204:207], v[144:147], v[72:75], v[204:207]\n\t"
      "v_mfma_f32_16x16x32_bf16 v[208:211], v[144:147], v[76:79], v[208:211]\n\t"
      "v_mfma_f32_16x16x32_bf16 v[196:199], v[148:151], v[80:83], v[196:199]\n\t"
      "v_mfma_f32_16x16x32_bf16 v[200:203], v[148:151], v[84:87], v[200:203]\n\t"
      "v_mfma_f32_16x16x32_bf16 v[204:207], v[148:151], v[88:91], v[204:207]\n\t"
      "v_mfma_f32_16x16x32_bf16 v[208:211], v[148:151], v[92:95], v[208:211]\n\t"
      "v_mfma_f32_16x16x32_bf16 v[196:199], v[152:155], v[96:99], v[196:199]\n\t"
      "v_mfma_f32_16x16x32_bf16 v[200:203], v[152:155], v[100:103], v[200:203]\n\t"
      "v_mfma_f32_16x16x32_bf16 v[204:207], v[152:155], v[104:107], v[204:207]\n\t"
      "v_mfma_f32_16x16x32_bf16 v[208:211], v[152:155], v[108:111], v[208:211]\n\t"
      "s_nop 7\n\t"
      "s_branch LA40B_%=\n\t"
      "LA41R_%=:\n\t"
      "s_mov_b32 s80, 0\n\t"
      "LA41L_%=:\n\t"
      "s_add_u32 s80, s80, 1\n\t"
      "s_cmp_lt_u32 s80, 262144\n\t"
      "s_cbranch_scc0 LA41X_%=\n\t"
      "s_waitcnt vmcnt(0)\n\t"
      "PQ1R_%=:\n\t"
      "PQ1L_%=:\n\t"
      "s_add_u32 s80, s80, 1\n\t"
      "s_cmp_lt_u32 s80, 262144\n\t"
      "s_cbranch_scc0 PQ1X_%=\n\t"
      "s_sleep 1\n\t"
      "global_load_dword v251, v252, s[66:67] offset:252 sc1\n\t"
      "s_waitcnt vmcnt(0)\n\t"
      "v_cmp_eq_u32_e32 vcc, -1, v251\n\t"
      "s_cbranch_vccnz PQ1L_%=\n\t"
      "PQ1X_%=:\n\t"
      "s_add_u32 s58, s66, s40\n\t"
      "s_addc_u32 s59, s67, 0\n\t"
      "global_load_dwordx4 v[156:159], v244, s[58:59] sc1\n\t"
      "s_add_u32 s58, s66, s41\n\t"
      "s_addc_u32 s59, s67, 0\n\t"
      "global_load_dwordx4 v[160:163], v244, s[58:59] sc1\n\t"
      "s_add_u32 s58, s66, s42\n\t"
      "s_addc_u32 s59, s67, 0\n\t"
      "global_load_dwordx4 v[164:167], v244, s[58:59] sc1\n\t"
      "s_add_u32 s58, s66, s43\n\t"
      "s_addc_u32 s59, s67, 0\n\t"
      "global_load_dwordx4 v[168:171], v244, s[58:59] sc1\n\t"
      "s_add_u32 s58, s66, s44\n\t"
      "s_addc_u32 s59, s67, 0\n\t"
      "global_load_dwordx4 v[172:175], v244, s[58:59] sc1\n\t"
      "s_add_u32 s58, s66, s45\n\t"
      "s_addc_u32 s59, s67, 0\n\t"
      "global_load_dwordx4 v[176:179], v244, s[58:59] sc1\n\t"
      "s_add_u32 s58, s66, s46\n\t"
      "s_addc_u32 s59, s67, 0\n\t"
      "global_load_dwordx4 v[180:183], v244, s[58:59] sc1\n\t"
      "s_waitcnt vmcnt(0)\n\t"
      "v_max3_u32 v251, v156, v157, v158\n\t"
      "v_max3_u32 v251, v251, v159, v160\n\t"
      "v_max3_u32 v251, v251, v161, v162\n\t"
      "v_max3_u32 v251, v251, v163, v164\n\t"
      "v_max3_u32 v251, v251, v165, v166\n\t"
      "v_max3_u32 v251, v251, v167, v168\n\t"
      "v_max3_u32 v251, v251, v169, v170\n\t"
      "v_max3_u32 v251, v251, v171, v172\n\t"
      "v_max3_u32 v251, v251, v173, v174\n\t"
      "v_max3_u32 v251, v251, v175, v176\n\t"
      "v_max3_u32 v251, v251, v177, v178\n\t"
      "v_max3_u32 v251, v251, v179, v180\n\t"
      "v_max3_u32 v251, v251, v181, v182\n\t"
      "v_max_u32_e32 v251, v251, v183\n\t"
      "v_cmp_eq_u32_e32 vcc, -1, v251\n\t"
      "s_cbranch_vccnz LA41L_%=\n\t"
      "LA41X_%=:\n\t"
      "v_mfma_f32_16x16x32_bf16 v[212:215], v[156:159], v[0:3], 0\n\t"
      "v_mfma_f32_16x16x32_bf16 v[216:219], v[156:159], v[4:7], 0\n\t"
      "v_mfma_f32_16x16x32_bf16 v[220:223], v[156:159], v[8:11], 0\n\t"
      "v_mfma_f32_16x16x32_bf16 v[224:227], v[156:159], v[12:15], 0\n\t"
      "v_mfma_f32_16x16x32_bf16 v[212:215], v[160:163], v[16:19], v[212:215]\n\t"
      "v_mfma_f32_16x16x32_bf16 v[216:219], v[160:163], v[20:23], v[216:219]\n\t"
      "v_mfma_f32_16x16x32_bf16 v[220:223], v[160:163], v[24:27], v[220:223]\n\t"
      "v_mfma_f32_16x16x32_bf16 v[224:227], v[160:163], v[28:31], v[224:227]\n\t"
      "v_mfma_f32_16x16x32_bf16 v[212:215], v[164:167], v[32:35], v[212:215]\n\t"
      "v_mfma_f32_16x16x32_bf16 v[216:219], v[164:167], v[36:39], v[216:219]\n\t"
      "v_mfma_f32_16x16x32_bf16 v[220:223], v[164:167], v[40:43], v[220:223]\n\t"
      "v_mfma_f32_16x16x32_bf16 v[224:227], v[164:167], v[44:47], v[224:227]\n\t"
      "v_mfma_f32_16x16x32_bf16 v[212:215], v[168:171], v[48:51], v[212:215]\n\t"
      "v_mfma_f32_16x16x32_bf16 v[216:219], v[168:171], v[52:55], v[216:219]\n\t"
      "v_mfma_f32_16x16x32_bf16 v[220:223], v[168:171], v[56:59], v[220:223]\n\t"
      "v_mfma_f32_16x16x32_bf16 v[224:227], v[168:171], v[60:63], v[224:227]\n\t"
      "v_mfma_f32_16x16x32_bf16 v[212:215], v[172:175], v[64:67], v[212:215]\n\t"
      "v_mfma_f32_16x16x32_bf16 v[216:219], v[172:175], v[68:71], v[216:219]\n\t"
      "v_mfma_f32_16x16x32_bf16 v[220:223], v[172:175], v[72:75], v[220:223]\n\t"
      "v_mfma_f32_16x16x32_bf16 v[224:227], v[172:175], v[76:79], v[224:227]\n\t"
      "v_mfma_f32_16x16x32_bf16 v[212:215], v[176:179], v[80:83], v[212:215]\n\t"
      "v_mfma_f32_16x16x32_bf16 v[216:219], v[176:179], v[84:87], v[216:219]\n\t"
      "v_mfma_f32_16x16x32_bf16 v[220:223], v[176:179], v[88:91], v[220:223]\n\t"
      "v_mfma_f32_16x16x32_bf16 v[224:227], v[176:179], v[92:95], v[224:227]\n\t"
      "v_mfma_f32_16x16x32_bf16 v[212:215], v[180:183], v[96:99], v[212:215]\n\t"
      "v_mfma_f32_16x16x32_bf16 v[216:219], v[180:183], v[100:103], v[216:219]\n\t"
      "v_mfma_f32_16x16x32_bf16 v[220:223], v[180:183], v[104:107], v[220:223]\n\t"
      "v_mfma_f32_16x16x32_bf16 v[224:227], v[180:183], v[108:111], v[224:227]\n\t"
      "s_nop 7\n\t"
      "s_branch LA41B_%=\n\t"
      "LA42R_%=:\n\t"
      "s_mov_b32 s80, 0\n\t"
      "LA42L_%=:\n\t"
      "s_add_u32 s80, s80, 1\n\t"
      "s_cmp_lt_u32 s80, 262144\n\t"
      "s_cbranch_scc0 LA42X_%=\n\t"
      "s_waitcnt vmcnt(0)\n\t"
      "PQ2R_%=:\n\t"
      "PQ2L_%=:\n\t"
      "s_add_u32 s80, s80, 1\n\t"
      "s_cmp_lt_u32 s80, 262144\n\t"
      "s_cbranch_scc0 PQ2X_%=\n\t"
      "s_sleep 1\n\t"
      "global_load_dword v251, v252, s[68:69] offset:252 sc1\n\t"
      "s_waitcnt vmcnt(0)\n\t"
      "v_cmp_eq_u32_e32 vcc, -1, v251\n\t"
      "s_cbranch_vccnz PQ2L_%=\n\t"
      "PQ2X_%=:\n\t"
      "s_add_u32 s58, s68, s40\n\t"
      "s_addc_u32 s59, s69, 0\n\t"
      "global_load_dwordx4 v[184:187], v244, s[58:59] sc1\n\t"
      "s_add_u32 s58, s68, s41\n\t"
      "s_addc_u32 s59, s69, 0\n\t"
      "global_load_dwordx4 v[188:191], v244, s[58:59] sc1\n\t"
      "s_add_u32 s58, s68, s42\n\t"
      "s_addc_u32 s59, s69, 0\n\t"
      "global_load_dwordx4 v[192:195], v244, s[58:59] sc1\n\t"
      "s_add_u32 s58, s68, s43\n\t"
      "s_addc_u32 s59, s69, 0\n\t"
      "global_load_dwordx4 v[128:131], v244, s[58:59] sc1\n\t"
      "s_add_u32 s58, s68, s44\n\t"
      "s_addc_u32 s59, s69, 0\n\t"
      "global_load_dwordx4 v[132:135], v244, s[58:59] sc1\n\t"
      "s_add_u32 s58, s68, s45\n\t"
      "s_addc_u32 s59, s69, 0\n\t"
      "global_load_dwordx4 v[136:139], v244, s[58:59] sc1\n\t"
      "s_add_u32 s58, s68, s46\n\t"
      "s_addc_u32 s59, s69, 0\n\t"
      "global_load_dwordx4 v[140:143], v244, s[58:59] sc1\n\t"
      "s_waitcnt vmcnt(0)\n\t"
      "v_max3_u32 v251, v184, v185, v186\n\t"
      "v_max3_u32 v251, v251, v187, v188\n\t"
      "v_max3_u32 v251, v251, v189, v190\n\t"
      "v_max3_u32 v251, v251, v191, v192\n\t"
      "v_max3_u32 v251, v251, v193, v194\n\t"
      "v_max3_u32 v251, v251, v195, v128\n\t"
      "v_max3_u32 v251, v251, v129, v130\n\t"
      "v_max3_u32 v251, v251, v131, v132\n\t"
      "v_max3_u32 v251, v251, v133, v134\n\t"
      "v_max3_u32 v251, v251, v135, v136\n\t"
      "v_max3_u32 v251, v251, v137, v138\n\t"
      "v_max3_u32 v251, v251, v139, v140\n\t"
      "v_max3_u32 v251, v251, v141, v142\n\t"
      "v_max_u32_e32 v251, v251, v143\n\t"
      "v_cmp_eq_u32_e32 vcc, -1, v251\n\t"
      "s_cbranch_vccnz LA42L_%=\n\t"
      "LA42X_%=:\n\t"
      "v_mfma_f32_16x16x32_bf16 v[196:199], v[184:187], v[0:3], 0\n\t"
      "v_mfma_f32_16x16x32_bf16 v[200:203], v[184:187], v[4:7], 0\n\t"
      "v_mfma_f32_16x16x32_bf16 v[204:207], v[184:187], v[8:11], 0\n\t"
      "v_mfma_f32_16x16x32_bf16 v[208:211], v[184:187], v[12:15], 0\n\t"
      "v_mfma_f32_16x16x32_bf16 v[196:199], v[188:191], v[16:19], v[196:199]\n\t"
      "v_mfma_f32_16x16x32_bf16 v[200:203], v[188:191], v[20:23], v[200:203]\n\t"
      "v_mfma_f32_16x16x32_bf16 v[204:207], v[188:191], v[24:27], v[204:207]\n\t"
      "v_mfma_f32_16x16x32_bf16 v[208:211], v[188:191], v[28:31], v[208:211]\n\t"
      "v_mfma_f32_16x16x32_bf16 v[196:199], v[192:195], v[32:35], v[196:199]\n\t"
      "v_mfma_f32_16x16x32_bf16 v[200:203], v[192:195], v[36:39], v[200:203]\n\t"
      "v_mfma_f32_16x16x32_bf16 v[204:207], v[192:195], v[40:43], v[204:207]\n\t"
      "v_mfma_f32_16x16x32_bf16 v[208:211], v[192:195], v[44:47], v[208:211]\n\t"
      "v_mfma_f32_16x16x32_bf16 v[196:199], v[128:131], v[48:51], v[196:199]\n\t"
      "v_mfma_f32_16x16x32_bf16 v[200:203], v[128:131], v[52:55], v[200:203]\n\t"
      "v_mfma_f32_16x16x32_bf16 v[204:207], v[128:131], v[56:59], v[204:207]\n\t"
      "v_mfma_f32_16x16x32_bf16 v[208:211], v[128:131], v[60:63], v[208:211]\n\t"
      "v_mfma_f32_16x16x32_bf16 v[196:199], v[132:135], v[64:67], v[196:199]\n\t"
      "v_mfma_f32_16x16x32_bf16 v[200:203], v[132:135], v[68:71], v[200:203]\n\t"
      "v_mfma_f32_16x16x32_bf16 v[204:207], v[132:135], v[72:75], v[204:207]\n\t"
      "v_mfma_f32_16x16x32_bf16 v[208:211], v[132:135], v[76:79], v[208:211]\n\t"
      "v_mfma_f32_16x16x32_bf16 v[196:199], v[136:139], v[80:83], v[196:199]\n\t"
      "v_mfma_f32_16x16x32_bf16 v[200:203], v[136:139], v[84:87], v[200:203]\n\t"
      "v_mfma_f32_16x16x32_bf16 v[204:207], v[136:139], v[88:91], v[204:207]\n\t"
      "v_mfma_f32_16x16x32_bf16 v[208:211], v[136:139], v[92:95], v[208:211]\n\t"
      "v_mfma_f32_16x16x32_bf16 v[196:199], v[140:143], v[96:99], v[196:199]\n\t"
      "v_mfma_f32_16x16x32_bf16 v[200:203], v[140:143], v[100:103], v[200:203]\n\t"
      "v_mfma_f32_16x16x32_bf16 v[204:207], v[140:143], v[104:107], v[204:207]\n\t"
      "v_mfma_f32_16x16x32_bf16 v[208:211], v[140:143], v[108:111], v[208:211]\n\t"
      "s_nop 7\n\t"
      "s_branch LA42B_%=\n\t"
      "LA43R_%=:\n\t"
      "s_mov_b32 s80, 0\n\t"
      "LA43L_%=:\n\t"
      "s_add_u32 s80, s80, 1\n\t"
      "s_cmp_lt_u32 s80, 262144\n\t"
      "s_cbranch_scc0 LA43X_%=\n\t"
      "s_waitcnt vmcnt(0)\n\t"
      "PQ3R_%=:\n\t"
      "PQ3L_%=:\n\t"
      "s_add_u32 s80, s80, 1\n\t"
      "s_cmp_lt_u32 s80, 262144\n\t"
      "s_cbranch_scc0 PQ3X_%=\n\t"
      "s_sleep 1\n\t"
      "global_load_dword v251, v252, s[70:71] offset:252 sc1\n\t"
      "s_waitcnt vmcnt(0)\n\t"
      "v_cmp_eq_u32_e32 vcc, -1, v251\n\t"
      "s_cbranch_vccnz PQ3L_%=\n\t"
      "PQ3X_%=:\n\t"
      "s_add_u32 s58, s70, s40\n\t"
      "s_addc_u32 s59, s71, 0\n\t"
      "global_load_dwordx4 v[144:147], v244, s[58:59] sc1\n\t"
      "s_add_u32 s58, s70, s41\n\t"
      "s_addc_u32 s59, s71, 0\n\t"
      "global_load_dwordx4 v[148:151], v244, s[58:59] sc1\n\t"
      "s_add_u32 s58, s70, s42\n\t"
      "s_addc_u32 s59, s71, 0\n\t"
      "global_load_dwordx4 v[152:155], v244, s[58:59] sc1\n\t"
      "s_add_u32 s58, s70, s43\n\t"
      "s_addc_u32 s59, s71, 0\n\t"
      "global_load_dwordx4 v[156:159], v244, s[58:59] sc1\n\t"
      "s_add_u32 s58, s70, s44\n\t"
      "s_addc_u32 s59, s71, 0\n\t"
      "global_load_dwordx4 v[160:163], v244, s[58:59] sc1\n\t"
      "s_add_u32 s58, s70, s45\n\t"
      "s_addc_u32 s59, s71, 0\n\t"
      "global_load_dwordx4 v[164:167], v244, s[58:59] sc1\n\t"
      "s_add_u32 s58, s70, s46\n\t"
      "s_addc_u32 s59, s71, 0\n\t"
      "global_load_dwordx4 v[168:171], v244, s[58:59] sc1\n\t"
      "s_waitcnt vmcnt(0)\n\t"
      "v_max3_u32 v251, v144, v145, v146\n\t"
      "v_max3_u32 v251, v251, v147, v148\n\t"
      "v_max3_u32 v251, v251, v149, v150\n\t"
      "v_max3_u32 v251, v251, v151, v152\n\t"
      "v_max3_u32 v251, v251, v153, v154\n\t"
      "v_max3_u32 v251, v251, v155, v156\n\t"
      "v_max3_u32 v251, v251, v157, v158\n\t"
      "v_max3_u32 v251, v251, v159, v160\n\t"
      "v_max3_u32 v251, v251, v161, v162\n\t"
      "v_max3_u32 v251, v251, v163, v164\n\t"
      "v_max3_u32 v251, v251, v165, v166\n\t"
      "v_max3_u32 v251, v251, v167, v168\n\t"
      "v_max3_u32 v251, v251, v169, v170\n\t"
      "v_max_u32_e32 v251, v251, v171\n\t"
      "v_cmp_eq_u32_e32 vcc, -1, v251\n\t"
      "s_cbranch_vccnz LA43L_%=\n\t"
      "LA43X_%=:\n\t"
      "v_mfma_f32_16x16x32_bf16 v[212:215], v[144:147], v[0:3], 0\n\t"
      "v_mfma_f32_16x16x32_bf16 v[216:219], v[144:147], v[4:7], 0\n\t"
      "v_mfma_f32_16x16x32_bf16 v[220:223], v[144:147], v[8:11], 0\n\t"
      "v_mfma_f32_16x16x32_bf16 v[224:227], v[144:147], v[12:15], 0\n\t"
      "v_mfma_f32_16x16x32_bf16 v[212:215], v[148:151], v[16:19], v[212:215]\n\t"
      "v_mfma_f32_16x16x32_bf16 v[216:219], v[148:151], v[20:23], v[216:219]\n\t"
      "v_mfma_f32_16x16x32_bf16 v[220:223], v[148:151], v[24:27], v[220:223]\n\t"
      "v_mfma_f32_16x16x32_bf16 v[224:227], v[148:151], v[28:31], v[224:227]\n\t"
      "v_mfma_f32_16x16x32_bf16 v[212:215], v[152:155], v[32:35], v[212:215]\n\t"
      "v_mfma_f32_16x16x32_bf16 v[216:219], v[152:155], v[36:39], v[216:219]\n\t"
      "v_mfma_f32_16x16x32_bf16 v[220:223], v[152:155], v[40:43], v[220:223]\n\t"
      "v_mfma_f32_16x16x32_bf16 v[224:227], v[152:155], v[44:47], v[224:227]\n\t"
      "v_mfma_f32_16x16x32_bf16 v[212:215], v[156:159], v[48:51], v[212:215]\n\t"
      "v_mfma_f32_16x16x32_bf16 v[216:219], v[156:159], v[52:55], v[216:219]\n\t"
      "v_mfma_f32_16x16x32_bf16 v[220:223], v[156:159], v[56:59], v[220:223]\n\t"
      "v_mfma_f32_16x16x32_bf16 v[224:227], v[156:159], v[60:63], v[224:227]\n\t"
      "v_mfma_f32_16x16x32_bf16 v[212:215], v[160:163], v[64:67], v[212:215]\n\t"
      "v_mfma_f32_16x16x32_bf16 v[216:219], v[160:163], v[68:71], v[216:219]\n\t"
      "v_mfma_f32_16x16x32_bf16 v[220:223], v[160:163], v[72:75], v[220:223]\n\t"
      "v_mfma_f32_16x16x32_bf16 v[224:227], v[160:163], v[76:79], v[224:227]\n\t"
      "v_mfma_f32_16x16x32_bf16 v[212:215], v[164:167], v[80:83], v[212:215]\n\t"
      "v_mfma_f32_16x16x32_bf16 v[216:219], v[164:167], v[84:87], v[216:219]\n\t"
      "v_mfma_f32_16x16x32_bf16 v[220:223], v[164:167], v[88:91], v[220:223]\n\t"
      "v_mfma_f32_16x16x32_bf16 v[224:227], v[164:167], v[92:95], v[224:227]\n\t"
      "v_mfma_f32_16x16x32_bf16 v[212:215], v[168:171], v[96:99], v[212:215]\n\t"
      "v_mfma_f32_16x16x32_bf16 v[216:219], v[168:171], v[100:103], v[216:219]\n\t"
      "v_mfma_f32_16x16x32_bf16 v[220:223], v[168:171], v[104:107], v[220:223]\n\t"
      "v_mfma_f32_16x16x32_bf16 v[224:227], v[168:171], v[108:111], v[224:227]\n\t"
      "s_nop 7\n\t"
      "s_branch LA43B_%=\n\t"
      "LA44R_%=:\n\t"
      "s_mov_b32 s80, 0\n\t"
      "LA44L_%=:\n\t"
      "s_add_u32 s80, s80, 1\n\t"
      "s_cmp_lt_u32 s80, 262144\n\t"
      "s_cbranch_scc0 LA44X_%=\n\t"
      "s_waitcnt vmcnt(0)\n\t"
      "PQ4R_%=:\n\t"
      "PQ4L_%=:\n\t"
      "s_add_u32 s80, s80, 1\n\t"
      "s_cmp_lt_u32 s80, 262144\n\t"
      "s_cbranch_scc0 PQ4X_%=\n\t"
      "s_sleep 1\n\t"
      "global_load_dword v251, v252, s[72:73] offset:252 sc1\n\t"
      "s_waitcnt vmcnt(0)\n\t"
      "v_cmp_eq_u32_e32 vcc, -1, v251\n\t"
      "s_cbranch_vccnz PQ4L_%=\n\t"
      "PQ4X_%=:\n\t"
      "s_add_u32 s58, s72, s40\n\t"
      "s_addc_u32 s59, s73, 0\n\t"
      "global_load_dwordx4 v[172:175], v244, s[58:59] sc1\n\t"
      "s_add_u32 s58, s72, s41\n\t"
      "s_addc_u32 s59, s73, 0\n\t"
      "global_load_dwordx4 v[176:179], v244, s[58:59] sc1\n\t"
      "s_add_u32 s58, s72, s42\n\t"
      "s_addc_u32 s59, s73, 0\n\t"
      "global_load_dwordx4 v[180:183], v244, s[58:59] sc1\n\t"
      "s_add_u32 s58, s72, s43\n\t"
      "s_addc_u32 s59, s73, 0\n\t"
      "global_load_dwordx4 v[184:187], v244, s[58:59] sc1\n\t"
      "s_add_u32 s58, s72, s44\n\t"
      "s_addc_u32 s59, s73, 0\n\t"
      "global_load_dwordx4 v[188:191], v244, s[58:59] sc1\n\t"
      "s_add_u32 s58, s72, s45\n\t"
      "s_addc_u32 s59, s73, 0\n\t"
      "global_load_dwordx4 v[192:195], v244, s[58:59] sc1\n\t"
      "s_add_u32 s58, s72, s46\n\t"
      "s_addc_u32 s59, s73, 0\n\t"
      "global_load_dwordx4 v[128:131], v244, s[58:59] sc1\n\t"
      "s_waitcnt vmcnt(0)\n\t"
      "v_max3_u32 v251, v172, v173, v174\n\t"
      "v_max3_u32 v251, v251, v175, v176\n\t"
      "v_max3_u32 v251, v251, v177, v178\n\t"
      "v_max3_u32 v251, v251, v179, v180\n\t"
      "v_max3_u32 v251, v251, v181, v182\n\t"
      "v_max3_u32 v251, v251, v183, v184\n\t"
      "v_max3_u32 v251, v251, v185, v186\n\t"
      "v_max3_u32 v251, v251, v187, v188\n\t"
      "v_max3_u32 v251, v251, v189, v190\n\t"
      "v_max3_u32 v251, v251, v191, v192\n\t"
      "v_max3_u32 v251, v251, v193, v194\n\t"
      "v_max3_u32 v251, v251, v195, v128\n\t"
      "v_max3_u32 v251, v251, v129, v130\n\t"
      "v_max_u32_e32 v251, v251, v131\n\t"
      "v_cmp_eq_u32_e32 vcc, -1, v251\n\t"
      "s_cbranch_vccnz LA44L_%=\n\t"
      "LA44X_%=:\n\t"
      "v_mfma_f32_16x16x32_bf16 v[196:199], v[172:175], v[0:3], 0\n\t"
      "v_mfma_f32_16x16x32_bf16 v[200:203], v[172:175], v[4:7], 0\n\t"
      "v_mfma_f32_16x16x32_bf16 v[204:207], v[172:175], v[8:11], 0\n\t"
      "v_mfma_f32_16x16x32_bf16 v[208:211], v[172:175], v[12:15], 0\n\t"
      "v_mfma_f32_16x16x32_bf16 v[196:199], v[176:179], v[16:19], v[196:199]\n\t"
      "v_mfma_f32_16x16x32_bf16 v[200:203], v[176:179], v[20:23], v[200:203]\n\t"
      "v_mfma_f32_16x16x32_bf16 v[204:207], v[176:179], v[24:27], v[204:207]\n\t"
      "v_mfma_f32_16x16x32_bf16 v[208:211], v[176:179], v[28:31], v[208:211]\n\t"
      "v_mfma_f32_16x16x32_bf16 v[196:199], v[180:183], v[32:35], v[196:199]\n\t"
      "v_mfma_f32_16x16x32_bf16 v[200:203], v[180:183], v[36:39], v[200:203]\n\t"
      "v_mfma_f32_16x16x32_bf16 v[204:207], v[180:183], v[40:43], v[204:207]\n\t"
      "v_mfma_f32_16x16x32_bf16 v[208:211], v[180:183], v[44:47], v[208:211]\n\t"
      "v_mfma_f32_16x16x32_bf16 v[196:199], v[184:187], v[48:51], v[196:199]\n\t"
      "v_mfma_f32_16x16x32_bf16 v[200:203], v[184:187], v[52:55], v[200:203]\n\t"
      "v_mfma_f32_16x16x32_bf16 v[204:207], v[184:187], v[56:59], v[204:207]\n\t"
      "v_mfma_f32_16x16x32_bf16 v[208:211], v[184:187], v[60:63], v[208:211]\n\t"
      "v_mfma_f32_16x16x32_bf16 v[196:199], v[188:191], v[64:67], v[196:199]\n\t"
      "v_mfma_f32_16x16x32_bf16 v[200:203], v[188:191], v[68:71], v[200:203]\n\t"
      "v_mfma_f32_16x16x32_bf16 v[204:207], v[188:191], v[72:75], v[204:207]\n\t"
      "v_mfma_f32_16x16x32_bf16 v[208:211], v[188:191], v[76:79], v[208:211]\n\t"
      "v_mfma_f32_16x16x32_bf16 v[196:199], v[192:195], v[80:83], v[196:199]\n\t"
      "v_mfma_f32_16x16x32_bf16 v[200:203], v[192:195], v[84:87], v[200:203]\n\t"
      "v_mfma_f32_16x16x32_bf16 v[204:207], v[192:195], v[88:91], v[204:207]\n\t"
      "v_mfma_f32_16x16x32_bf16 v[208:211], v[192:195], v[92:95], v[208:211]\n\t"
      "v_mfma_f32_16x16x32_bf16 v[196:199], v[128:131], v[96:99], v[196:199]\n\t"
      "v_mfma_f32_16x16x32_bf16 v[200:203], v[128:131], v[100:103], v[200:203]\n\t"
      "v_mfma_f32_16x16x32_bf16 v[204:207], v[128:131], v[104:107], v[204:207]\n\t"
      "v_mfma_f32_16x16x32_bf16 v[208:211], v[128:131], v[108:111], v[208:211]\n\t"
      "s_nop 7\n\t"
      "s_branch LA44B_%=\n\t"
      "LA45R_%=:\n\t"
      "s_mov_b32 s80, 0\n\t"
      "LA45L_%=:\n\t"
      "s_add_u32 s80, s80, 1\n\t"
      "s_cmp_lt_u32 s80, 262144\n\t"
      "s_cbranch_scc0 LA45X_%=\n\t"
      "s_waitcnt vmcnt(0)\n\t"
      "PQ5R_%=:\n\t"
      "PQ5L_%=:\n\t"
      "s_add_u32 s80, s80, 1\n\t"
      "s_cmp_lt_u32 s80, 262144\n\t"
      "s_cbranch_scc0 PQ5X_%=\n\t"
      "s_sleep 1\n\t"
      "global_load_dword v251, v252, s[74:75] offset:252 sc1\n\t"
      "s_waitcnt vmcnt(0)\n\t"
      "v_cmp_eq_u32_e32 vcc, -1, v251\n\t"
      "s_cbranch_vccnz PQ5L_%=\n\t"
      "PQ5X_%=:\n\t"
      "s_add_u32 s58, s74, s40\n\t"
      "s_addc_u32 s59, s75, 0\n\t"
      "global_load_dwordx4 v[132:135], v244, s[58:59] sc1\n\t"
      "s_add_u32 s58, s74, s41\n\t"
      "s_addc_u32 s59, s75, 0\n\t"
      "global_load_dwordx4 v[136:139], v244, s[58:59] sc1\n\t"
      "s_add_u32 s58, s74, s42\n\t"
      "s_addc_u32 s59, s75, 0\n\t"
      "global_load_dwordx4 v[140:143], v244, s[58:59] sc1\n\t"
      "s_add_u32 s58, s74, s43\n\t"
      "s_addc_u32 s59, s75, 0\n\t"
      "global_load_dwordx4 v[144:147], v244, s[58:59] sc1\n\t"
      "s_add_u32 s58, s74, s44\n\t"
      "s_addc_u32 s59, s75, 0\n\t"
      "global_load_dwordx4 v[148:151], v244, s[58:59] sc1\n\t"
      "s_add_u32 s58, s74, s45\n\t"
      "s_addc_u32 s59, s75, 0\n\t"
      "global_load_dwordx4 v[152:155], v244, s[58:59] sc1\n\t"
      "s_add_u32 s58, s74, s46\n\t"
      "s_addc_u32 s59, s75, 0\n\t"
      "global_load_dwordx4 v[156:159], v244, s[58:59] sc1\n\t"
      "s_waitcnt vmcnt(0)\n\t"
      "v_max3_u32 v251, v132, v133, v134\n\t"
      "v_max3_u32 v251, v251, v135, v136\n\t"
      "v_max3_u32 v251, v251, v137, v138\n\t"
      "v_max3_u32 v251, v251, v139, v140\n\t"
      "v_max3_u32 v251, v251, v141, v142\n\t"
      "v_max3_u32 v251, v251, v143, v144\n\t"
      "v_max3_u32 v251, v251, v145, v146\n\t"
      "v_max3_u32 v251, v251, v147, v148\n\t"
      "v_max3_u32 v251, v251, v149, v150\n\t"
      "v_max3_u32 v251, v251, v151, v152\n\t"
      "v_max3_u32 v251, v251, v153, v154\n\t"
      "v_max3_u32 v251, v251, v155, v156\n\t"
      "v_max3_u32 v251, v251, v157, v158\n\t"
      "v_max_u32_e32 v251, v251, v159\n\t"
      "v_cmp_eq_u32_e32 vcc, -1, v251\n\t"
      "s_cbranch_vccnz LA45L_%=\n\t"
      "LA45X_%=:\n\t"
      "v_mfma_f32_16x16x32_bf16 v[212:215], v[132:135], v[0:3], 0\n\t"
      "v_mfma_f32_16x16x32_bf16 v[216:219], v[132:135], v[4:7], 0\n\t"
      "v_mfma_f32_16x16x32_bf16 v[220:223], v[132:135], v[8:11], 0\n\t"
      "v_mfma_f32_16x16x32_bf16 v[224:227], v[132:135], v[12:15], 0\n\t"
      "v_mfma_f32_16x16x32_bf16 v[212:215], v[136:139], v[16:19], v[212:215]\n\t"
      "v_mfma_f32_16x16x32_bf16 v[216:219], v[136:139], v[20:23], v[216:219]\n\t"
      "v_mfma_f32_16x16x32_bf16 v[220:223], v[136:139], v[24:27], v[220:223]\n\t"
      "v_mfma_f32_16x16x32_bf16 v[224:227], v[136:139], v[28:31], v[224:227]\n\t"
      "v_mfma_f32_16x16x32_bf16 v[212:215], v[140:143], v[32:35], v[212:215]\n\t"
      "v_mfma_f32_16x16x32_bf16 v[216:219], v[140:143], v[36:39], v[216:219]\n\t"
      "v_mfma_f32_16x16x32_bf16 v[220:223], v[140:143], v[40:43], v[220:223]\n\t"
      "v_mfma_f32_16x16x32_bf16 v[224:227], v[140:143], v[44:47], v[224:227]\n\t"
      "v_mfma_f32_16x16x32_bf16 v[212:215], v[144:147], v[48:51], v[212:215]\n\t"
      "v_mfma_f32_16x16x32_bf16 v[216:219], v[144:147], v[52:55], v[216:219]\n\t"
      "v_mfma_f32_16x16x32_bf16 v[220:223], v[144:147], v[56:59], v[220:223]\n\t"
      "v_mfma_f32_16x16x32_bf16 v[224:227], v[144:147], v[60:63], v[224:227]\n\t"
      "v_mfma_f32_16x16x32_bf16 v[212:215], v[148:151], v[64:67], v[212:215]\n\t"
      "v_mfma_f32_16x16x32_bf16 v[216:219], v[148:151], v[68:71], v[216:219]\n\t"
      "v_mfma_f32_16x16x32_bf16 v[220:223], v[148:151], v[72:75], v[220:223]\n\t"
      "v_mfma_f32_16x16x32_bf16 v[224:227], v[148:151], v[76:79], v[224:227]\n\t"
      "v_mfma_f32_16x16x32_bf16 v[212:215], v[152:155], v[80:83], v[212:215]\n\t"
      "v_mfma_f32_16x16x32_bf16 v[216:219], v[152:155], v[84:87], v[216:219]\n\t"
      "v_mfma_f32_16x16x32_bf16 v[220:223], v[152:155], v[88:91], v[220:223]\n\t"
      "v_mfma_f32_16x16x32_bf16 v[224:227], v[152:155], v[92:95], v[224:227]\n\t"
      "v_mfma_f32_16x16x32_bf16 v[212:215], v[156:159], v[96:99], v[212:215]\n\t"
      "v_mfma_f32_16x16x32_bf16 v[216:219], v[156:159], v[100:103], v[216:219]\n\t"
      "v_mfma_f32_16x16x32_bf16 v[220:223], v[156:159], v[104:107], v[220:223]\n\t"
      "v_mfma_f32_16x16x32_bf16 v[224:227], v[156:159], v[108:111], v[224:227]\n\t"
      "s_nop 7\n\t"
      "s_branch LA45B_%=\n\t"
      "LA46R_%=:\n\t"
      "s_mov_b32 s80, 0\n\t"
      "LA46L_%=:\n\t"
      "s_add_u32 s80, s80, 1\n\t"
      "s_cmp_lt_u32 s80, 262144\n\t"
      "s_cbranch_scc0 LA46X_%=\n\t"
      "s_waitcnt vmcnt(0)\n\t"
      "PQ6R_%=:\n\t"
      "PQ6L_%=:\n\t"
      "s_add_u32 s80, s80, 1\n\t"
      "s_cmp_lt_u32 s80, 262144\n\t"
      "s_cbranch_scc0 PQ6X_%=\n\t"
      "s_sleep 1\n\t"
      "global_load_dword v251, v252, s[76:77] offset:252 sc1\n\t"
      "s_waitcnt vmcnt(0)\n\t"
      "v_cmp_eq_u32_e32 vcc, -1, v251\n\t"
      "s_cbranch_vccnz PQ6L_%=\n\t"
      "PQ6X_%=:\n\t"
      "s_add_u32 s58, s76, s40\n\t"
      "s_addc_u32 s59, s77, 0\n\t"
      "global_load_dwordx4 v[160:163], v244, s[58:59] sc1\n\t"
      "s_add_u32 s58, s76, s41\n\t"
      "s_addc_u32 s59, s77, 0\n\t"
      "global_load_dwordx4 v[164:167], v244, s[58:59] sc1\n\t"
      "s_add_u32 s58, s76, s42\n\t"
      "s_addc_u32 s59, s77, 0\n\t"
      "global_load_dwordx4 v[168:171], v244, s[58:59] sc1\n\t"
      "s_add_u32 s58, s76, s43\n\t"
      "s_addc_u32 s59, s77, 0\n\t"
      "global_load_dwordx4 v[172:175], v244, s[58:59] sc1\n\t"
      "s_add_u32 s58, s76, s44\n\t"
      "s_addc_u32 s59, s77, 0\n\t"
      "global_load_dwordx4 v[176:179], v244, s[58:59] sc1\n\t"
      "s_add_u32 s58, s76, s45\n\t"
      "s_addc_u32 s59, s77, 0\n\t"
      "global_load_dwordx4 v[180:183], v244, s[58:59] sc1\n\t"
      "s_add_u32 s58, s76, s46\n\t"
      "s_addc_u32 s59, s77, 0\n\t"
      "global_load_dwordx4 v[184:187], v244, s[58:59] sc1\n\t"
      "s_waitcnt vmcnt(0)\n\t"
      "v_max3_u32 v251, v160, v161, v162\n\t"
      "v_max3_u32 v251, v251, v163, v164\n\t"
      "v_max3_u32 v251, v251, v165, v166\n\t"
      "v_max3_u32 v251, v251, v167, v168\n\t"
      "v_max3_u32 v251, v251, v169, v170\n\t"
      "v_max3_u32 v251, v251, v171, v172\n\t"
      "v_max3_u32 v251, v251, v173, v174\n\t"
      "v_max3_u32 v251, v251, v175, v176\n\t"
      "v_max3_u32 v251, v251, v177, v178\n\t"
      "v_max3_u32 v251, v251, v179, v180\n\t"
      "v_max3_u32 v251, v251, v181, v182\n\t"
      "v_max3_u32 v251, v251, v183, v184\n\t"
      "v_max3_u32 v251, v251, v185, v186\n\t"
      "v_max_u32_e32 v251, v251, v187\n\t"
      "v_cmp_eq_u32_e32 vcc, -1, v251\n\t"
      "s_cbranch_vccnz LA46L_%=\n\t"
      "LA46X_%=:\n\t"
      "v_mfma_f32_16x16x32_bf16 v[196:199], v[160:163], v[0:3], 0\n\t"
      "v_mfma_f32_16x16x32_bf16 v[200:203], v[160:163], v[4:7], 0\n\t"
      "v_mfma_f32_16x16x32_bf16 v[204:207], v[160:163], v[8:11], 0\n\t"
      "v_mfma_f32_16x16x32_bf16 v[208:211], v[160:163], v[12:15], 0\n\t"
      "v_mfma_f32_16x16x32_bf16 v[196:199], v[164:167], v[16:19], v[196:199]\n\t"
      "v_mfma_f32_16x16x32_bf16 v[200:203], v[164:167], v[20:23], v[200:203]\n\t"
      "v_mfma_f32_16x16x32_bf16 v[204:207], v[164:167], v[24:27], v[204:207]\n\t"
      "v_mfma_f32_16x16x32_bf16 v[208:211], v[164:167], v[28:31], v[208:211]\n\t"
      "v_mfma_f32_16x16x32_bf16 v[196:199], v[168:171], v[32:35], v[196:199]\n\t"
      "v_mfma_f32_16x16x32_bf16 v[200:203], v[168:171], v[36:39], v[200:203]\n\t"
      "v_mfma_f32_16x16x32_bf16 v[204:207], v[168:171], v[40:43], v[204:207]\n\t"
      "v_mfma_f32_16x16x32_bf16 v[208:211], v[168:171], v[44:47], v[208:211]\n\t"
      "v_mfma_f32_16x16x32_bf16 v[196:199], v[172:175], v[48:51], v[196:199]\n\t"
      "v_mfma_f32_16x16x32_bf16 v[200:203], v[172:175], v[52:55], v[200:203]\n\t"
      "v_mfma_f32_16x16x32_bf16 v[204:207], v[172:175], v[56:59], v[204:207]\n\t"
      "v_mfma_f32_16x16x32_bf16 v[208:211], v[172:175], v[60:63], v[208:211]\n\t"
      "v_mfma_f32_16x16x32_bf16 v[196:199], v[176:179], v[64:67], v[196:199]\n\t"
      "v_mfma_f32_16x16x32_bf16 v[200:203], v[176:179], v[68:71], v[200:203]\n\t"
      "v_mfma_f32_16x16x32_bf16 v[204:207], v[176:179], v[72:75], v[204:207]\n\t"
      "v_mfma_f32_16x16x32_bf16 v[208:211], v[176:179], v[76:79], v[208:211]\n\t"
      "v_mfma_f32_16x16x32_bf16 v[196:199], v[180:183], v[80:83], v[196:199]\n\t"
      "v_mfma_f32_16x16x32_bf16 v[200:203], v[180:183], v[84:87], v[200:203]\n\t"
      "v_mfma_f32_16x16x32_bf16 v[204:207], v[180:183], v[88:91], v[204:207]\n\t"
      "v_mfma_f32_16x16x32_bf16 v[208:211], v[180:183], v[92:95], v[208:211]\n\t"
      "v_mfma_f32_16x16x32_bf16 v[196:199], v[184:187], v[96:99], v[196:199]\n\t"
      "v_mfma_f32_16x16x32_bf16 v[200:203], v[184:187], v[100:103], v[200:203]\n\t"
      "v_mfma_f32_16x16x32_bf16 v[204:207], v[184:187], v[104:107], v[204:207]\n\t"
      "v_mfma_f32_16x16x32_bf16 v[208:211], v[184:187], v[108:111], v[208:211]\n\t"
      "s_nop 7\n\t"
      "s_branch LA46B_%=\n\t"
      : "+{s[0:1]}"(ka), "+{v0}"(tid)
      : "{s2}"(blockIdx.x)
      : "memory", "vcc", "scc", "m0",
        "v1", "v2", "v3", "v4", "v5", "v6", "v7", "v8", "v9", "v10", "v11", "v12", "v13", "v14", "v15", "v16", "v17",
        "v18", "v19", "v20", "v21", "v22", "v23", "v24", "v25", "v26", "v27", "v28", "v29", "v30", "v31", "v32", "v33",
        "v34", "v35", "v36", "v37", "v38", "v39", "v40", "v41", "v42", "v43", "v44", "v45", "v46", "v47", "v48", "v49",
        "v50", "v51", "v52", "v53", "v54", "v55", "v56", "v57", "v58", "v59", "v60", "v61", "v62", "v63", "v64", "v65",
        "v66", "v67", "v68", "v69", "v70", "v71", "v72", "v73", "v74", "v75", "v76", "v77", "v78", "v79", "v80", "v81",
        "v82", "v83", "v84", "v85", "v86", "v87", "v88", "v89", "v90", "v91", "v92", "v93", "v94", "v95", "v96", "v97",
        "v98", "v99", "v100", "v101", "v102", "v103", "v104", "v105", "v106", "v107", "v108", "v109", "v110", "v111",
        "v112", "v113", "v114", "v115", "v116", "v117", "v118", "v119", "v120", "v121", "v122", "v123", "v124", "v125",
        "v126", "v127", "v128", "v129", "v130", "v131", "v132", "v133", "v134", "v135", "v136", "v137", "v138", "v139",
        "v140", "v141", "v142", "v143", "v144", "v145", "v146", "v147", "v148", "v149", "v150", "v151", "v152", "v153",
        "v154", "v155", "v156", "v157", "v158", "v159", "v160", "v161", "v162", "v163", "v164", "v165", "v166", "v167",
        "v168", "v169", "v170", "v171", "v172", "v173", "v174", "v175", "v176", "v177", "v178", "v179", "v180", "v181",
        "v182", "v183", "v184", "v185", "v186", "v187", "v188", "v189", "v190", "v191", "v192", "v193", "v194", "v195",
        "v196", "v197", "v198", "v199", "v200", "v201", "v202", "v203", "v204", "v205", "v206", "v207", "v208", "v209",
        "v210", "v211", "v212", "v213", "v214", "v215", "v216", "v217", "v218", "v219", "v220", "v221", "v222", "v223",
        "v224", "v225", "v226", "v227", "v228", "v229", "v230", "v231", "v232", "v233", "v234", "v235", "v236", "v237",
        "v238", "v239", "v240", "v241", "v242", "v243", "v244", "v245", "v246", "v247", "v248", "v249", "v250", "v251",
        "v252", "v253", "v254", "v255", "s3", "s4", "s5", "s6", "s7", "s8", "s9", "s10", "s11", "s12", "s13", "s14",
        "s15", "s16", "s17", "s18", "s19", "s20", "s21", "s22", "s23", "s24", "s25", "s26", "s27", "s28", "s29", "s30",
        "s31", "s32", "s33", "s34", "s35", "s36", "s37", "s38", "s39", "s40", "s41", "s42", "s43", "s44", "s45", "s46",
        "s47", "s48", "s49", "s50", "s51", "s52", "s53", "s54", "s55", "s56", "s57", "s58", "s59", "s60", "s61", "s62",
        "s63", "s64", "s65", "s66", "s67", "s68", "s69", "s70", "s71", "s72", "s73", "s74", "s75", "s76", "s77", "s78",
        "s79", "s80", "s81", "s82", "s83"
  );
#endif
}

extern "C" void kernel_launch(void* const* d_in, const int* in_sizes, int n_in, void* d_out, int out_size,
                              void* d_ws, size_t ws_size, hipStream_t stream) {
  (void)hipFuncSetAttribute((const void*)fused_block, hipFuncAttributeMaxDynamicSharedMemorySize, 163840);
  fused_block<<<256, 512, 163840, stream>>>((const u16*)d_in[0], (const u16*)d_in[1], (const u16*)d_in[2],
                                            (const u16*)d_in[4], (const u16*)d_in[5], (const u16*)d_in[6],
                                            (const u16*)d_in[7], (u16*)d_out, (char*)d_ws);
}
```

```cpp
#include <hip/hip_runtime.h>
#include <stdint.h>
typedef unsigned short u16;
__global__ __launch_bounds__(512, 2) void fused_block(const u16* x, const u16* wqkv, const u16* wo, const u16* w1, const u16* b1,
                                                   const u16* w2, const u16* b2, u16* out, char* ws) {
#if defined(__HIP_DEVICE_COMPILE__)
  unsigned long long ka = (unsigned long long)__builtin_amdgcn_kernarg_segment_ptr();
  unsigned tid = threadIdx.x;
  asm volatile(
      "s_load_dwordx16 s[4:19], s[0:1], 0x0\n\t"
      "s_load_dwordx2 s[20:21], s[0:1], 0x40\n\t"
      "v_lshrrev_b32_e32 v199, 6, v0\n\t"
      "v_and_b32_e32 v192, 63, v0\n\t"
      "v_readfirstlane_b32 s24, v199\n\t"
      "s_lshr_b32 s36, s2, 3\n\t"
      "s_and_b32 s35, s36, 28\n\t"
      "s_lshr_b32 s26, 0x63752041, s35\n\t"
      "s_and_b32 s26, s26, 7\n\t"
      "s_and_b32 s35, s36, 3\n\t"
      "s_and_b32 s34, s2, 7\n\t"
      "s_lshl2_add_u32 s25, s34, s35\n\t"
      "s_and_b32 s34, s25, 30\n\t"
      "s_lshl_b32 s34, s34, 6\n\t"
      "s_and_b32 s35, s25, 1\n\t"
      "s_add_u32 s38, s34, s35\n\t"
      "v_lshrrev_b32_e32 v197, 3, v192\n\t"
      "v_and_b32_e32 v198, 7, v192\n\t"
      "s_min_u32 s28, s26, 6\n\t"
      "s_lshl4_add_u32 s34, s24, s38\n\t"
      "s_mul_i32 s34, s34, 896\n\t"
      "s_movk_i32 s35, 1792\n\t"
      "v_lshl_add_u32 v199, v198, 4, s34\n\t"
      "v_mad_u32_u24 v199, v197, s35, v199\n\t"
      "s_lshl_b32 s34, s28, 7\n\t"
      "s_cmp_lg_u32 s24, 0\n\t"
      "s_cbranch_scc1 NTC_%=\n\t"
      "s_getpc_b64 s[58:59]\n\t"
      "s_lshr_b32 s37, s2, 3\n\t"
      "v_min_u32_e32 v200, 9, v192\n\t"
      "v_lshl_add_u32 v200, v200, 5, s37\n\t"
      "s_mov_b32 m0, 64512\n\t"
      "v_min_u32_e32 v200, 288, v200\n\t"
      "v_lshlrev_b32_e32 v200, 7, v200\n\t"
      "global_load_lds_dword v200, s[58:59]\n\t"
      "NTC_%=:\n\t"
      "v_add_u32_e32 v120, s34, v199\n\t"
      "s_add_u32 s35, s34, 128\n\t"
      "s_sub_u32 s36, s35, 896\n\t"
      "s_min_u32 s35, s35, s36\n\t"
      "v_add_u32_e32 v121, s35, v199\n\t"
      "s_waitcnt lgkmcnt(0)\n\t"
      "global_load_dwordx4 v[204:207], v120, s[4:5]\n\t"
      "s_add_u32 s35, s34, 256\n\t"
      "s_sub_u32 s36, s35, 896\n\t"
      "s_min_u32 s35, s35, s36\n\t"
      "v_add_u32_e32 v122, s35, v199\n\t"
      "global_load_dwordx4 v[4:7], v121, s[4:5]\n\t"
      "s_add_u32 s35, s34, 384\n\t"
      "s_sub_u32 s36, s35, 896\n\t"
      "s_min_u32 s35, s35, s36\n\t"
      "v_add_u32_e32 v123, s35, v199\n\t"
      "global_load_dwordx4 v[8:11], v122, s[4:5]\n\t"
      "s_add_u32 s35, s34, 512\n\t"
      "s_sub_u32 s36, s35, 896\n\t"
      "s_min_u32 s35, s35, s36\n\t"
      "v_add_u32_e32 v124, s35, v199\n\t"
      "global_load_dwordx4 v[12:15], v123, s[4:5]\n\t"
      "s_add_u32 s35, s34, 640\n\t"
      "s_sub_u32 s36, s35, 896\n\t"
      "s_min_u32 s35, s35, s36\n\t"
      "v_add_u32_e32 v125, s35, v199\n\t"
      "s_add_u32 s35, s34, 768\n\t"
      "s_sub_u32 s36, s35, 896\n\t"
      "s_min_u32 s35, s35, s36\n\t"
      "v_add_u32_e32 v126, s35, v199\n\t"
      "s_cmp_lt_u32 s25, 2\n\t"
      "s_cselect_b32 s34, 0, 16\n\t"
      "s_sub_u32 s34, s38, s34\n\t"
      "s_lshl1_add_u32 s34, s24, s34\n\t"
      "s_mul_i32 s34, s34, 896\n\t"
      "s_and_b32 s33, s25, 3\n\t"
      "s_lshl_b32 s33, s33, 1\n\t"
      "v_min_u32_e32 v200, 55, v192\n\t"
      "s_lshl_b32 s35, s33, 3\n\t"
      "v_add_u32_e32 v201, s35, v200\n\t"
      "v_subrev_u32_e32 v202, 56, v201\n\t"
      "v_min_u32_e32 v201, v201, v202\n\t"
      "v_lshl_add_u32 v127, v201, 4, s34\n\t"
      "s_and_b32 s39, s24, 3\n\t"
      "s_lshl_b32 s39, s39, 12\n\t"
      "s_cmp_lt_u32 s24, 4\n\t"
      "s_cselect_b32 s27, 4, 0\n\t"
      "v_lshrrev_b32_e32 v193, 4, v192\n\t"
      "v_and_b32_e32 v194, 15, v192\n\t"
      "v_bfe_u32 v195, v192, 2, 2\n\t"
      "v_and_b32_e32 v196, 3, v192\n\t"
      "v_lshlrev_b32_e32 v244, 4, v192\n\t"
      "s_lshr_b32 s34, s24, 1\n\t"
      "s_mul_i32 s34, s34, 14336\n\t"
      "s_and_b32 s35, s24, 1\n\t"
      "s_lshl_b32 s35, s35, 7\n\t"
      "s_add_u32 s34, s34, s35\n\t"
      "v_xor_b32_e32 v199, v197, v198\n\t"
      "v_lshl_add_u32 v199, v199, 4, s34\n\t"
      "v_lshl_add_u32 v199, v198, 8, v199\n\t"
      "s_sub_u32 s35, s28, s33\n\t"
      "s_cselect_b32 s36, 7, 0\n\t"
      "s_add_u32 s35, s35, s36\n\t"
      "s_lshl_b32 s35, s35, 11\n\t"
      "s_add_u32 s34, s35, 0\n\t"
      "s_sub_u32 s36, s34, 14336\n\t"
      "s_min_u32 s34, s34, s36\n\t"
      "v_add_u32_e32 v112, s34, v199\n\t"
      "s_add_u32 s34, s35, 2048\n\t"
      "s_sub_u32 s36, s34, 14336\n\t"
      "s_min_u32 s34, s34, s36\n\t"
      "v_add_u32_e32 v113, s34, v199\n\t"
      "s_add_u32 s34, s35, 4096\n\t"
      "s_sub_u32 s36, s34, 14336\n\t"
      "s_min_u32 s34, s34, s36\n\t"
      "v_add_u32_e32 v114, s34, v199\n\t"
      "s_add_u32 s34, s35, 6144\n\t"
      "s_sub_u32 s36, s34, 14336\n\t"
      "s_min_u32 s34, s34, s36\n\t"
      "v_add_u32_e32 v115, s34, v199\n\t"
      "s_add_u32 s34, s35, 8192\n\t"
      "s_sub_u32 s36, s34, 14336\n\t"
      "s_min_u32 s34, s34, s36\n\t"
      "v_add_u32_e32 v116, s34, v199\n\t"
      "s_add_u32 s34, s35, 10240\n\t"
      "s_sub_u32 s36, s34, 14336\n\t"
      "s_min_u32 s34, s34, s36\n\t"
      "v_add_u32_e32 v117, s34, v199\n\t"
      "s_add_u32 s34, s35, 12288\n\t"
      "s_sub_u32 s36, s34, 14336\n\t"
      "s_min_u32 s34, s34, s36\n\t"
      "v_add_u32_e32 v118, s34, v199\n\t"
      "v_min_u32_e32 v200, 55, v192\n\t"
      "v_lshrrev_b32_e32 v201, 2, v200\n\t"
      "v_lshlrev_b32_e32 v119, 10, v201\n\t"
      "v_cmp_lt_u32_e32 vcc, 6, v201\n\t"
      "v_mov_b32_e32 v202, 57344\n\t"
      "v_mov_b32_e32 v201, 50688\n\t"
      "v_cndmask_b32_e32 v202, v202, v201, vcc\n\t"
      "v_and_b32_e32 v201, 3, v200\n\t"
      "v_lshl_add_u32 v202, v201, 7, v202\n\t"
      "v_and_b32_e32 v200, 7, v200\n\t"
      "v_xor_b32_e32 v200, s24, v200\n\t"
      "v_lshl_add_u32 v202, v200, 4, v202\n\t"
      "v_add_u32_e32 v119, v119, v202\n\t"
      "s_bitcmp1_b32 s27, 2\n\t"
      "s_cbranch_scc1 LSET_%=\n\t"
      "v_xor_b32_e32 v199, v194, v193\n\t"
      "v_lshlrev_b32_e32 v199, 4, v199\n\t"
      "v_lshl_or_b32 v200, v193, 8, v199\n\t"
      "s_and_b32 s34, s24, 1\n\t"
      "s_mul_i32 s35, s34, 28672\n\t"
      "v_add_u32_e32 v160, s35, v200\n\t"
      "v_xor_b32_e32 v161, 64, v160\n\t"
      "v_and_b32_e32 v199, 0x70, v199\n\t"
      "v_lshl_or_b32 v199, v193, 7, v199\n\t"
      "v_add_u32_e32 v199, 57344, v199\n\t"
      "v_add_u32_e32 v200, 14336, v200\n\t"
      "s_cmp_eq_u32 s34, 1\n\t"
      "s_cselect_b64 vcc, -1, 0\n\t"
      "s_cselect_b32 s35, 0, 6656\n\t"
      "v_cndmask_b32_e32 v162, v199, v200, vcc\n\t"
      "v_xor_b32_e32 v163, 64, v162\n\t"
      "v_subrev_u32_e32 v164, s35, v162\n\t"
      "v_subrev_u32_e32 v165, s35, v163\n\t"
      "s_bfe_u32 s35, s24, 65537\n\t"
      "s_mul_i32 s35, s35, 7\n\t"
      "v_and_b32_e32 v201, 24, v192\n\t"
      "v_lshlrev_b32_e32 v201, 2, v201\n\t"
      "v_lshlrev_b32_e32 v202, 7, v195\n\t"
      "v_lshl_or_b32 v202, v193, 12, v202\n\t"
      "v_add_u32_e32 v202, 65536, v202\n\t"
      "v_add_lshl_u32 v199, v196, s35, 3\n\t"
      "v_xor_b32_e32 v199, v199, v201\n\t"
      "v_add_u32_e32 v166, v199, v202\n\t"
      "v_min_u32_e32 v199, 2, v196\n\t"
      "s_add_u32 s35, s35, 4\n\t"
      "v_add_lshl_u32 v199, v199, s35, 3\n\t"
      "v_xor_b32_e32 v199, v199, v201\n\t"
      "v_add_u32_e32 v167, v199, v202\n\t"
      "v_add_u32_e32 v168, 0x10000, v166\n\t"
      "v_add_u32_e32 v169, 0x10000, v167\n\t"
      "v_lshlrev_b32_e32 v199, 11, v193\n\t"
      "v_sub_u32_e32 v170, v166, v199\n\t"
      "v_sub_u32_e32 v171, v167, v199\n\t"
      "s_branch NORM_%=\n\t"
      "LSET_%=:\n\t"
      "s_add_u32 s60, s20, 16777216\n\t"
      "s_addc_u32 s61, s21, 0\n\t"
      "s_load_dwordx2 s[22:23], s[60:61], 0x0\n\t"
      "s_lshr_b32 s34, s39, 9\n\t"
      "v_add_u32_e32 v199, s34, v197\n\t"
      "s_and_b32 s34, s24, 1\n\t"
      "s_lshl_b32 s34, s34, 2\n\t"
      "v_and_b32_e32 v200, 2, v197\n\t"
      "v_or_b32_e32 v200, s34, v200\n\t"
      "v_xor_b32_e32 v200, v200, v198\n\t"
      "v_min_u32_e32 v200, 6, v200\n\t"
      "v_lshlrev_b32_e32 v200, 4, v200\n\t"
      "s_movk_i32 s34, 2688\n\t"
      "v_mad_u32_u24 v149, v199, s34, v200\n\t"
      "v_add_u32_e32 v148, 128, v149\n\t"
      "v_add_u32_e32 v150, 84096, v149\n\t"
      "v_add_u32_e32 v151, 83968, v149\n\t"
      "v_add_u32_e32 v152, 85120, v149\n\t"
      "s_sub_u32 s6, s6, 128\n\t"
      "s_subb_u32 s7, s7, 0\n\t"
      "s_mul_i32 s37, s26, 112\n\t"
      "s_mov_b32 s81, s33\n\t"
      "s_mov_b32 s82, s33\n\t"
      "s_and_b32 s35, s25, 3\n\t"
      "s_lshl2_add_u32 s31, s26, s35\n\t"
      "s_and_b32 s35, s2, 7\n\t"
      "s_mul_i32 s34, s35, 14\n\t"
      "s_lshl4_add_u32 s35, s35, -14\n\t"
      "s_cmp_lt_u32 s31, 14\n\t"
      "s_cselect_b32 s34, s34, s35\n\t"
      "s_cselect_b32 s36, 1, 0\n\t"
      "s_or_b32 s27, s27, s36\n\t"
      "s_add_u32 s34, s34, s31\n\t"
      "s_lshr_b32 s29, s34, 2\n\t"
      "s_and_b32 s32, s34, 3\n\t"
      "s_mul_i32 s30, s32, 7\n\t"
      "v_and_b32_e32 v199, 60, v192\n\t"
      "v_and_b32_e32 v200, 24, v192\n\t"
      "v_lshlrev_b32_e32 v202, 3, v196\n\t"
      "v_lshl_or_b32 v202, v200, 2, v202\n\t"
      "v_lshl_or_b32 v202, v195, 7, v202\n\t"
      "v_lshl_or_b32 v202, v193, 10, v202\n\t"
      "v_add_u32_e32 v224, s39, v202\n\t"
      "v_xor_b32_e32 v225, 32, v224\n\t"
      "v_xor_b32_e32 v226, 64, v224\n\t"
      "v_xor_b32_e32 v227, 96, v224\n\t"
      "s_lshl_b32 s34, s24, 12\n\t"
      "s_add_u32 s34, s34, 98304\n\t"
      "v_add_u32_e32 v245, s34, v244\n\t"
      "v_lshlrev_b32_e32 v199, 2, v199\n\t"
      "s_lshl_b32 s34, s24, 8\n\t"
      "v_lshl_add_u32 v247, v192, 2, s34\n\t"
      "v_and_b32_e32 v200, 2, v192\n\t"
      "v_mul_u32_u24_e32 v202, 576, v200\n\t"
      "v_xor_b32_e32 v199, v202, v199\n\t"
      "v_and_b32_e32 v202, 1, v192\n\t"
      "v_lshl_or_b32 v199, v202, 3, v199\n\t"
      "s_and_b32 s35, s34, 0x400\n\t"
      "s_add_u32 s34, s34, s35\n\t"
      "s_add_u32 s34, s34, 98304\n\t"
      "v_add_u32_e32 v246, s34, v199\n\t"
      "v_lshlrev_b32_e32 v199, 2, v202\n\t"
      "v_lshl_or_b32 v199, v200, 4, v199\n\t"
      "s_min_u32 s34, s29, 27\n\t"
      "s_lshl_b32 s34, s34, 7\n\t"
      "s_lshr_b32 s35, s24, 2\n\t"
      "s_lshl_b32 s35, s35, 6\n\t"
      "s_add_u32 s34, s34, s35\n\t"
      "s_lshr_b32 s35, s39, 9\n\t"
      "s_add_u32 s34, s34, s35\n\t"
      "v_add_u32_e32 v248, s34, v199\n\t"
      "s_add_u32 s48, s20, 8388608\n\t"
      "s_addc_u32 s49, s21, 0\n\t"
      "v_and_b32_e32 v199, 2, v197\n\t"
      "v_xor_b32_e32 v199, v199, v198\n\t"
      "v_lshlrev_b32_e32 v199, 4, v199\n\t"
      "s_movk_i32 s34, 7168\n\t"
      "v_mad_u32_u24 v144, v197, s34, v199\n\t"
      "v_xor_b32_e32 v199, 64, v144\n\t"
      "v_add_u32_e32 v145, 56320, v199\n\t"
      "v_add_u32_e32 v146, 112640, v144\n\t"
      "v_add_u32_e32 v147, 168960, v199\n\t"
      "v_and_b32_e32 v199, 6, v197\n\t"
      "v_xor_b32_e32 v199, v199, v198\n\t"
      "v_lshlrev_b32_e32 v199, 4, v199\n\t"
      "s_movk_i32 s35, 896\n\t"
      "v_mad_u32_u24 v154, v197, s35, v199\n\t"
      "v_add_u32_e32 v155, 6144, v154\n\t"
      "v_add_u32_e32 v156, 12288, v154\n\t"
      "v_add_u32_e32 v157, 18432, v154\n\t"
      "s_mul_i32 s34, s24, 917504\n\t"
      "s_lshl_b32 s35, s29, 7\n\t"
      "s_add_u32 s34, s34, s35\n\t"
      "s_add_u32 s52, s10, s34\n\t"
      "s_addc_u32 s53, s11, 0\n\t"
      "s_lshl_b32 s34, s28, 7\n\t"
      "s_add_u32 s56, s8, s34\n\t"
      "s_addc_u32 s57, s9, 0\n\t"
      "s_mov_b32 s34, 0\n\t"
      "s_and_b32 s35, s34, 1\n\t"
      "s_and_b32 s34, s34, 2\n\t"
      "s_lshl_b32 s34, s34, 6\n\t"
      "s_add_u32 s34, s34, s35\n\t"
      "s_mul_i32 s76, s34, 3584\n\t"
      "s_mov_b32 s34, 1\n\t"
      "s_and_b32 s35, s34, 1\n\t"
      "s_and_b32 s34, s34, 2\n\t"
      "s_lshl_b32 s34, s34, 6\n\t"
      "s_add_u32 s34, s34, s35\n\t"
      "s_mul_i32 s77, s34, 3584\n\t"
      "s_mov_b32 s34, 2\n\t"
      "s_and_b32 s35, s34, 1\n\t"
      "s_and_b32 s34, s34, 2\n\t"
      "s_lshl_b32 s34, s34, 6\n\t"
      "s_add_u32 s34, s34, s35\n\t"
      "s_mul_i32 s78, s34, 3584\n\t"
      "s_mov_b32 s34, 3\n\t"
      "s_and_b32 s35, s34, 1\n\t"
      "s_and_b32 s34, s34, 2\n\t"
      "s_lshl_b32 s34, s34, 6\n\t"
      "s_add_u32 s34, s34, s35\n\t"
      "s_mul_i32 s79, s34, 3584\n\t"
      "NORM_%=:\n\t"
      "global_load_dwordx4 v[16:19], v124, s[4:5]\n\t"
      "global_load_dwordx4 v[20:23], v125, s[4:5]\n\t"
      "global_load_dwordx4 v[24:27], v126, s[4:5]\n\t"
      "global_load_dwordx4 v[28:31], v127, s[4:5]\n\t"
      "v_mov_b32_e32 v199, 0x3f803f80\n\t"
      "s_waitcnt vmcnt(7)\n\t"
      "v_dot2_f32_bf16 v88, v204, v199, 0\n\t"
      "v_dot2_f32_bf16 v92, v204, v204, 0\n\t"
      "v_lshlrev_b32_e32 v32, 16, v204\n\t"
      "v_and_b32_e32 v33, 0xffff0000, v204\n\t"
      "v_dot2_f32_bf16 v89, v205, v199, 0\n\t"
      "v_dot2_f32_bf16 v93, v205, v205, 0\n\t"
      "v_lshlrev_b32_e32 v34, 16, v205\n\t"
      "v_and_b32_e32 v35, 0xffff0000, v205\n\t"
      "v_dot2_f32_bf16 v90, v206, v199, 0\n\t"
      "v_dot2_f32_bf16 v94, v206, v206, 0\n\t"
      "v_lshlrev_b32_e32 v36, 16, v206\n\t"
      "v_and_b32_e32 v37, 0xffff0000, v206\n\t"
      "v_dot2_f32_bf16 v91, v207, v199, 0\n\t"
      "v_dot2_f32_bf16 v95, v207, v207, 0\n\t"
      "v_lshlrev_b32_e32 v38, 16, v207\n\t"
      "v_and_b32_e32 v39, 0xffff0000, v207\n\t"
      "s_waitcnt vmcnt(6)\n\t"
      "v_dot2_f32_bf16 v88, v4, v199, v88\n\t"
      "v_dot2_f32_bf16 v92, v4, v4, v92\n\t"
      "v_lshlrev_b32_e32 v40, 16, v4\n\t"
      "v_and_b32_e32 v41, 0xffff0000, v4\n\t"
      "v_dot2_f32_bf16 v89, v5, v199, v89\n\t"
      "v_dot2_f32_bf16 v93, v5, v5, v93\n\t"
      "v_lshlrev_b32_e32 v42, 16, v5\n\t"
      "v_and_b32_e32 v43, 0xffff0000, v5\n\t"
      "v_dot2_f32_bf16 v90, v6, v199, v90\n\t"
      "v_dot2_f32_bf16 v94, v6, v6, v94\n\t"
      "v_lshlrev_b32_e32 v44, 16, v6\n\t"
      "v_and_b32_e32 v45, 0xffff0000, v6\n\t"
      "v_dot2_f32_bf16 v91, v7, v199, v91\n\t"
      "v_dot2_f32_bf16 v95, v7, v7, v95\n\t"
      "v_lshlrev_b32_e32 v46, 16, v7\n\t"
      "v_and_b32_e32 v47, 0xffff0000, v7\n\t"
      "s_waitcnt vmcnt(5)\n\t"
      "v_dot2_f32_bf16 v88, v8, v199, v88\n\t"
      "v_dot2_f32_bf16 v92, v8, v8, v92\n\t"
      "v_lshlrev_b32_e32 v48, 16, v8\n\t"
      "v_and_b32_e32 v49, 0xffff0000, v8\n\t"
      "v_dot2_f32_bf16 v89, v9, v199, v89\n\t"
      "v_dot2_f32_bf16 v93, v9, v9, v93\n\t"
      "v_lshlrev_b32_e32 v50, 16, v9\n\t"
      "v_and_b32_e32 v51, 0xffff0000, v9\n\t"
      "v_dot2_f32_bf16 v90, v10, v199, v90\n\t"
      "v_dot2_f32_bf16 v94, v10, v10, v94\n\t"
      "v_lshlrev_b32_e32 v52, 16, v10\n\t"
      "v_and_b32_e32 v53, 0xffff0000, v10\n\t"
      "v_dot2_f32_bf16 v91, v11, v199, v91\n\t"
      "v_dot2_f32_bf16 v95, v11, v11, v95\n\t"
      "v_lshlrev_b32_e32 v54, 16, v11\n\t"
      "v_and_b32_e32 v55, 0xffff0000, v11\n\t"
      "s_waitcnt vmcnt(4)\n\t"
      "v_dot2_f32_bf16 v88, v12, v199, v88\n\t"
      "v_dot2_f32_bf16 v92, v12, v12, v92\n\t"
      "v_lshlrev_b32_e32 v56, 16, v12\n\t"
      "v_and_b32_e32 v57, 0xffff0000, v12\n\t"
      "v_dot2_f32_bf16 v89, v13, v199, v89\n\t"
      "v_dot2_f32_bf16 v93, v13, v13, v93\n\t"
      "v_lshlrev_b32_e32 v58, 16, v13\n\t"
      "v_and_b32_e32 v59, 0xffff0000, v13\n\t"
      "v_dot2_f32_bf16 v90, v14, v199, v90\n\t"
      "v_dot2_f32_bf16 v94, v14, v14, v94\n\t"
      "v_lshlrev_b32_e32 v60, 16, v14\n\t"
      "v_and_b32_e32 v61, 0xffff0000, v14\n\t"
      "v_dot2_f32_bf16 v91, v15, v199, v91\n\t"
      "v_dot2_f32_bf16 v95, v15, v15, v95\n\t"
      "v_lshlrev_b32_e32 v62, 16, v15\n\t"
      "v_and_b32_e32 v63, 0xffff0000, v15\n\t"
      "s_waitcnt vmcnt(3)\n\t"
      "v_dot2_f32_bf16 v88, v16, v199, v88\n\t"
      "v_dot2_f32_bf16 v92, v16, v16, v92\n\t"
      "v_lshlrev_b32_e32 v64, 16, v16\n\t"
      "v_and_b32_e32 v65, 0xffff0000, v16\n\t"
      "v_dot2_f32_bf16 v89, v17, v199, v89\n\t"
      "v_dot2_f32_bf16 v93, v17, v17, v93\n\t"
      "v_lshlrev_b32_e32 v66, 16, v17\n\t"
      "v_and_b32_e32 v67, 0xffff0000, v17\n\t"
      "v_dot2_f32_bf16 v90, v18, v199, v90\n\t"
      "v_dot2_f32_bf16 v94, v18, v18, v94\n\t"
      "v_lshlrev_b32_e32 v68, 16, v18\n\t"
      "v_and_b32_e32 v69, 0xffff0000, v18\n\t"
      "v_dot2_f32_bf16 v91, v19, v199, v91\n\t"
      "v_dot2_f32_bf16 v95, v19, v19, v95\n\t"
      "v_lshlrev_b32_e32 v70, 16, v19\n\t"
      "v_and_b32_e32 v71, 0xffff0000, v19\n\t"
      "s_waitcnt vmcnt(2)\n\t"
      "v_dot2_f32_bf16 v88, v20, v199, v88\n\t"
      "v_dot2_f32_bf16 v92, v20, v20, v92\n\t"
      "v_lshlrev_b32_e32 v72, 16, v20\n\t"
      "v_and_b32_e32 v73, 0xffff0000, v20\n\t"
      "v_dot2_f32_bf16 v89, v21, v199, v89\n\t"
      "v_dot2_f32_bf16 v93, v21, v21, v93\n\t"
      "v_lshlrev_b32_e32 v74, 16, v21\n\t"
      "v_and_b32_e32 v75, 0xffff0000, v21\n\t"
      "v_dot2_f32_bf16 v90, v22, v199, v90\n\t"
      "v_dot2_f32_bf16 v94, v22, v22, v94\n\t"
      "v_lshlrev_b32_e32 v76, 16, v22\n\t"
      "v_and_b32_e32 v77, 0xffff0000, v22\n\t"
      "v_dot2_f32_bf16 v91, v23, v199, v91\n\t"
      "v_dot2_f32_bf16 v95, v23, v23, v95\n\t"
      "v_lshlrev_b32_e32 v78, 16, v23\n\t"
      "v_and_b32_e32 v79, 0xffff0000, v23\n\t"
      "s_waitcnt vmcnt(1)\n\t"
      "v_dot2_f32_bf16 v88, v24, v199, v88\n\t"
      "v_dot2_f32_bf16 v92, v24, v24, v92\n\t"
      "v_lshlrev_b32_e32 v80, 16, v24\n\t"
      "v_and_b32_e32 v81, 0xffff0000, v24\n\t"
      "v_dot2_f32_bf16 v89, v25, v199, v89\n\t"
      "v_dot2_f32_bf16 v93, v25, v25, v93\n\t"
      "v_lshlrev_b32_e32 v82, 16, v25\n\t"
      "v_and_b32_e32 v83, 0xffff0000, v25\n\t"
      "v_dot2_f32_bf16 v90, v26, v199, v90\n\t"
      "v_dot2_f32_bf16 v94, v26, v26, v94\n\t"
      "v_lshlrev_b32_e32 v84, 16, v26\n\t"
      "v_and_b32_e32 v85, 0xffff0000, v26\n\t"
      "v_dot2_f32_bf16 v91, v27, v199, v91\n\t"
      "v_dot2_f32_bf16 v95, v27, v27, v95\n\t"
      "v_lshlrev_b32_e32 v86, 16, v27\n\t"
      "v_and_b32_e32 v87, 0xffff0000, v27\n\t"
      "s_waitcnt vmcnt(0)\n\t"
      "s_bitcmp0_b32 s27, 2\n\t"
      "s_cbranch_scc1 SK1_%=\n\t"
      "s_mul_i32 s72, s81, 172032\n\t"
      "s_add_u32 s72, s72, s37\n\t"
      "s_add_u32 s40, s6, s72\n\t"
      "s_addc_u32 s41, s7, 0\n\t"
      "s_add_u32 s81, s81, 1\n\t"
      "s_cmp_eq_u32 s81, 7\n\t"
      "s_cselect_b32 s81, 0, s81\n\t"
      "s_add_u32 m0, s39, 131072\n\t"
      "s_nop 0\n\t"
      "global_load_lds_dwordx4 v148, s[40:41]\n\t"
      "global_load_lds_dwordx4 v149, s[40:41] offset:1024\n\t"
      "global_load_lds_dwordx4 v150, s[40:41] offset:2048\n\t"
      "global_load_lds_dwordx4 v151, s[40:41] offset:3072\n\t"
      "SK1_%=:\n\t"
      "v_dot2_f32_bf16 v96, v28, v199, 0\n\t"
      "v_dot2_f32_bf16 v97, v28, v28, 0\n\t"
      "v_dot2_f32_bf16 v96, v29, v199, v96\n\t"
      "v_dot2_f32_bf16 v97, v29, v29, v97\n\t"
      "v_dot2_f32_bf16 v96, v30, v199, v96\n\t"
      "v_dot2_f32_bf16 v97, v30, v30, v97\n\t"
      "v_dot2_f32_bf16 v96, v31, v199, v96\n\t"
      "v_dot2_f32_bf16 v97, v31, v31, v97\n\t"
      "v_cmp_gt_u32_e32 vcc, 56, v192\n\t"
      "v_pk_add_f32 v[88:89], v[88:89], v[90:91]\n\t"
      "v_pk_add_f32 v[92:93], v[92:93], v[94:95]\n\t"
      "v_cndmask_b32_e32 v96, 0, v96, vcc\n\t"
      "v_cndmask_b32_e32 v97, 0, v97, vcc\n\t"
      "v_add_f32_e32 v88, v88, v89\n\t"
      "v_add_f32_e32 v92, v92, v93\n\t"
      "v_add_f32_dpp v96, v96, v96 quad_perm:[1,0,3,2] row_mask:0xf bank_mask:0xf\n\t"
      "v_add_f32_dpp v97, v97, v97 quad_perm:[1,0,3,2] row_mask:0xf bank_mask:0xf\n\t"
      "v_add_f32_dpp v88, v88, v88 quad_perm:[1,0,3,2] row_mask:0xf bank_mask:0xf\n\t"
      "v_add_f32_dpp v92, v92, v92 quad_perm:[1,0,3,2] row_mask:0xf bank_mask:0xf\n\t"
      "v_add_f32_dpp v96, v96, v96 quad_perm:[2,3,0,1] row_mask:0xf bank_mask:0xf\n\t"
      "v_add_f32_dpp v97, v97, v97 quad_perm:[2,3,0,1] row_mask:0xf bank_mask:0xf\n\t"
      "v_add_f32_dpp v88, v88, v88 quad_perm:[2,3,0,1] row_mask:0xf bank_mask:0xf\n\t"
      "v_add_f32_dpp v92, v92, v92 quad_perm:[2,3,0,1] row_mask:0xf bank_mask:0xf\n\t"
      "v_add_f32_dpp v96, v96, v96 row_half_mirror row_mask:0xf bank_mask:0xf\n\t"
      "v_add_f32_dpp v97, v97, v97 row_half_mirror row_mask:0xf bank_mask:0xf\n\t"
      "v_add_f32_dpp v88, v88, v88 row_half_mirror row_mask:0xf bank_mask:0xf\n\t"
      "v_add_f32_dpp v92, v92, v92 row_half_mirror row_mask:0xf bank_mask:0xf\n\t"
      "v_mul_f32_e32 v88, 0xbb124925, v88\n\t"
      "v_add_f32_dpp v96, v96, v96 row_mirror row_mask:0xf bank_mask:0xf\n\t"
      "v_mul_f32_e32 v92, 0x3b124925, v92\n\t"
      "s_bitcmp0_b32 s27, 2\n\t"
      "s_cbranch_scc1 SK2_%=\n\t"
      "s_mul_i32 s72, s81, 172032\n\t"
      "s_add_u32 s72, s72, s37\n\t"
      "s_add_u32 s40, s6, s72\n\t"
      "s_addc_u32 s41, s7, 0\n\t"
      "s_add_u32 s81, s81, 1\n\t"
      "s_cmp_eq_u32 s81, 7\n\t"
      "s_cselect_b32 s81, 0, s81\n\t"
      "s_add_u32 m0, s39, 147456\n\t"
      "s_nop 0\n\t"
      "global_load_lds_dwordx4 v148, s[40:41]\n\t"
      "global_load_lds_dwordx4 v149, s[40:41] offset:1024\n\t"
      "SK2_%=:\n\t"
      "v_add_f32_dpp v97, v97, v97 row_mirror row_mask:0xf bank_mask:0xf\n\t"
      "v_fma_f32 v92, -v88, v88, v92\n\t"
      "s_nop 0\n\t"
      "v_permlane16_swap_b32_e32 v96, v97\n\t"
      "v_max_f32_e32 v92, 0, v92\n\t"
      "v_add_f32_e32 v96, v96, v97\n\t"
      "v_add_f32_e32 v92, 0x3727c5ac, v92\n\t"
      "v_mov_b32_e32 v97, v96\n\t"
      "v_rsq_f32_e32 v92, v92\n\t"
      "s_nop 0\n\t"
      "v_permlane32_swap_b32_e32 v96, v97\n\t"
      "v_add_f32_e32 v96, v96, v97\n\t"
      "v_mul_f32_e32 v88, v88, v92\n\t"
      "v_mov_b32_e32 v97, v96\n\t"
      "v_pk_fma_f32 v[32:33], v[32:33], v[92:93], v[88:89] op_sel_hi:[1,0,0]\n\t"
      "v_pk_fma_f32 v[34:35], v[34:35], v[92:93], v[88:89] op_sel_hi:[1,0,0]\n\t"
      "v_permlane16_swap_b32_e32 v96, v97\n\t"
      "v_pk_fma_f32 v[36:37], v[36:37], v[92:93], v[88:89] op_sel_hi:[1,0,0]\n\t"
      "v_pk_fma_f32 v[38:39], v[38:39], v[92:93], v[88:89] op_sel_hi:[1,0,0]\n\t"
      "v_mul_f32_e32 v96, 0xbb124925, v96\n\t"
      "v_cvt_pk_bf16_f32 v0, v32, v33\n\t"
      "v_cvt_pk_bf16_f32 v1, v34, v35\n\t"
      "v_cvt_pk_bf16_f32 v2, v36, v37\n\t"
      "v_cvt_pk_bf16_f32 v3, v38, v39\n\t"
      "ds_write_b128 v112, v[0:3]\n\t"
      "v_pk_fma_f32 v[40:41], v[40:41], v[92:93], v[88:89] op_sel_hi:[1,0,0]\n\t"
      "v_pk_fma_f32 v[42:43], v[42:43], v[92:93], v[88:89] op_sel_hi:[1,0,0]\n\t"
      "v_mul_f32_e32 v97, 0x3b124925, v97\n\t"
      "v_pk_fma_f32 v[44:45], v[44:45], v[92:93], v[88:89] op_sel_hi:[1,0,0]\n\t"
      "v_pk_fma_f32 v[46:47], v[46:47], v[92:93], v[88:89] op_sel_hi:[1,0,0]\n\t"
      "v_fma_f32 v97, -v96, v96, v97\n\t"
      "v_cvt_pk_bf16_f32 v4, v40, v41\n\t"
      "s_bitcmp0_b32 s27, 2\n\t"
      "s_cbranch_scc1 SK3_%=\n\t"
      "global_load_lds_dwordx4 v150, s[40:41] offset:2048\n\t"
      "global_load_lds_dwordx4 v151, s[40:41] offset:3072\n\t"
      "SK3_%=:\n\t"
      "v_cvt_pk_bf16_f32 v5, v42, v43\n\t"
      "v_cvt_pk_bf16_f32 v6, v44, v45\n\t"
      "v_cvt_pk_bf16_f32 v7, v46, v47\n\t"
      "ds_write_b128 v113, v[4:7]\n\t"
      "v_pk_fma_f32 v[48:49], v[48:49], v[92:93], v[88:89] op_sel_hi:[1,0,0]\n\t"
      "v_pk_fma_f32 v[50:51], v[50:51], v[92:93], v[88:89] op_sel_hi:[1,0,0]\n\t"
      "v_max_f32_e32 v97, 0, v97\n\t"
      "v_pk_fma_f32 v[52:53], v[52:53], v[92:93], v[88:89] op_sel_hi:[1,0,0]\n\t"
      "v_pk_fma_f32 v[54:55], v[54:55], v[92:93], v[88:89] op_sel_hi:[1,0,0]\n\t"
      "v_add_f32_e32 v97, 0x3727c5ac, v97\n\t"
      "v_cvt_pk_bf16_f32 v8, v48, v49\n\t"
      "v_cvt_pk_bf16_f32 v9, v50, v51\n\t"
      "v_cvt_pk_bf16_f32 v10, v52, v53\n\t"
      "v_cvt_pk_bf16_f32 v11, v54, v55\n\t"
      "ds_write_b128 v114, v[8:11]\n\t"
      "v_pk_fma_f32 v[56:57], v[56:57], v[92:93], v[88:89] op_sel_hi:[1,0,0]\n\t"
      "v_pk_fma_f32 v[58:59], v[58:59], v[92:93], v[88:89] op_sel_hi:[1,0,0]\n\t"
      "v_rsq_f32_e32 v97, v97\n\t"
      "v_pk_fma_f32 v[60:61], v[60:61], v[92:93], v[88:89] op_sel_hi:[1,0,0]\n\t"
      "v_pk_fma_f32 v[62:63], v[62:63], v[92:93], v[88:89] op_sel_hi:[1,0,0]\n\t"
      "v_cvt_pk_bf16_f32 v12, v56, v57\n\t"
      "v_cvt_pk_bf16_f32 v13, v58, v59\n\t"
      "v_cvt_pk_bf16_f32 v14, v60, v61\n\t"
      "v_cvt_pk_bf16_f32 v15, v62, v63\n\t"
      "ds_write_b128 v115, v[12:15]\n\t"
      "v_pk_fma_f32 v[64:65], v[64:65], v[92:93], v[88:89] op_sel_hi:[1,0,0]\n\t"
      "v_pk_fma_f32 v[66:67], v[66:67], v[92:93], v[88:89] op_sel_hi:[1,0,0]\n\t"
      "v_mul_f32_e32 v96, v96, v97\n\t"
      "v_pk_fma_f32 v[68:69], v[68:69], v[92:93], v[88:89] op_sel_hi:[1,0,0]\n\t"
      "v_pk_fma_f32 v[70:71], v[70:71], v[92:93], v[88:89] op_sel_hi:[1,0,0]\n\t"
      "s_bitcmp0_b32 s27, 2\n\t"
      "s_cbranch_scc1 SK4_%=\n\t"
      "s_mul_i32 s72, s82, 172032\n\t"
      "s_add_u32 s72, s72, s37\n\t"
      "s_add_u32 s72, s72, 1792\n\t"
      "s_add_u32 s40, s6, s72\n\t"
      "s_addc_u32 s41, s7, 0\n\t"
      "s_add_u32 s82, s82, 1\n\t"
      "s_cmp_eq_u32 s82, 7\n\t"
      "s_cselect_b32 s82, 0, s82\n\t"
      "s_add_u32 m0, s39, 65536\n\t"
      "s_nop 0\n\t"
      "global_load_lds_dwordx4 v148, s[40:41]\n\t"
      "global_load_lds_dwordx4 v152, s[40:41] offset:1024\n\t"
      "SK4_%=:\n\t"
      "v_cvt_pk_bf16_f32 v16, v64, v65\n\t"
      "v_cvt_pk_bf16_f32 v17, v66, v67\n\t"
      "v_cvt_pk_bf16_f32 v18, v68, v69\n\t"
      "v_cvt_pk_bf16_f32 v19, v70, v71\n\t"
      "ds_write_b128 v116, v[16:19]\n\t"
      "v_pk_fma_f32 v[72:73], v[72:73], v[92:93], v[88:89] op_sel_hi:[1,0,0]\n\t"
      "v_pk_fma_f32 v[74:75], v[74:75], v[92:93], v[88:89] op_sel_hi:[1,0,0]\n\t"
      "v_pk_fma_f32 v[76:77], v[76:77], v[92:93], v[88:89] op_sel_hi:[1,0,0]\n\t"
      "v_pk_fma_f32 v[78:79], v[78:79], v[92:93], v[88:89] op_sel_hi:[1,0,0]\n\t"
      "v_cvt_pk_bf16_f32 v20, v72, v73\n\t"
      "v_cvt_pk_bf16_f32 v21, v74, v75\n\t"
      "v_cvt_pk_bf16_f32 v22, v76, v77\n\t"
      "v_cvt_pk_bf16_f32 v23, v78, v79\n\t"
      "ds_write_b128 v117, v[20:23]\n\t"
      "v_pk_fma_f32 v[80:81], v[80:81], v[92:93], v[88:89] op_sel_hi:[1,0,0]\n\t"
      "v_pk_fma_f32 v[82:83], v[82:83], v[92:93], v[88:89] op_sel_hi:[1,0,0]\n\t"
      "v_pk_fma_f32 v[84:85], v[84:85], v[92:93], v[88:89] op_sel_hi:[1,0,0]\n\t"
      "v_pk_fma_f32 v[86:87], v[86:87], v[92:93], v[88:89] op_sel_hi:[1,0,0]\n\t"
      "v_cvt_pk_bf16_f32 v24, v80, v81\n\t"
      "v_cvt_pk_bf16_f32 v25, v82, v83\n\t"
      "v_cvt_pk_bf16_f32 v26, v84, v85\n\t"
      "v_cvt_pk_bf16_f32 v27, v86, v87\n\t"
      "ds_write_b128 v118, v[24:27]\n\t"
      "v_lshlrev_b32_e32 v32, 16, v28\n\t"
      "v_and_b32_e32 v33, 0xffff0000, v28\n\t"
      "v_lshlrev_b32_e32 v34, 16, v29\n\t"
      "v_and_b32_e32 v35, 0xffff0000, v29\n\t"
      "v_lshlrev_b32_e32 v36, 16, v30\n\t"
      "v_and_b32_e32 v37, 0xffff0000, v30\n\t"
      "v_lshlrev_b32_e32 v38, 16, v31\n\t"
      "s_bitcmp0_b32 s27, 2\n\t"
      "s_cbranch_scc1 SK5_%=\n\t"
      "s_mul_i32 s72, s82, 172032\n\t"
      "s_add_u32 s72, s72, s37\n\t"
      "s_add_u32 s72, s72, 1792\n\t"
      "s_add_u32 s40, s6, s72\n\t"
      "s_addc_u32 s41, s7, 0\n\t"
      "s_add_u32 s82, s82, 1\n\t"
      "s_cmp_eq_u32 s82, 7\n\t"
      "s_cselect_b32 s82, 0, s82\n\t"
      "s_add_u32 m0, s39, 67584\n\t"
      "s_nop 0\n\t"
      "global_load_lds_dwordx4 v148, s[40:41]\n\t"
      "global_load_lds_dwordx4 v152, s[40:41] offset:1024\n\t"
      "SK5_%=:\n\t"
      "v_and_b32_e32 v39, 0xffff0000, v31\n\t"
      "v_pk_fma_f32 v[32:33], v[32:33], v[96:97], v[96:97] op_sel:[0,1,0] op_sel_hi:[1,1,0]\n\t"
      "v_pk_fma_f32 v[34:35], v[34:35], v[96:97], v[96:97] op_sel:[0,1,0] op_sel_hi:[1,1,0]\n\t"
      "v_pk_fma_f32 v[36:37], v[36:37], v[96:97], v[96:97] op_sel:[0,1,0] op_sel_hi:[1,1,0]\n\t"
      "v_pk_fma_f32 v[38:39], v[38:39], v[96:97], v[96:97] op_sel:[0,1,0] op_sel_hi:[1,1,0]\n\t"
      "v_cvt_pk_bf16_f32 v28, v32, v33\n\t"
      "v_cvt_pk_bf16_f32 v29, v34, v35\n\t"
      "v_cvt_pk_bf16_f32 v30, v36, v37\n\t"
      "v_cvt_pk_bf16_f32 v31, v38, v39\n\t"
      "ds_write_b128 v119, v[28:31]\n\t"
      "s_waitcnt lgkmcnt(0)\n\t"
      "s_bitcmp1_b32 s27, 2\n\t"
      "s_cbranch_scc1 LPATH_%=\n\t"
      "s_setprio 2\n\t"
      "s_barrier\n\t"
      "ds_read_b64_tr_b16 v[76:77], v168\n\t"
      "ds_read_b64_tr_b16 v[78:79], v168 offset:512\n\t"
      "ds_read_b128 v[68:71], v160\n\t"
      "ds_read_b128 v[72:75], v160 offset:14336\n\t"
      "ds_read_b64_tr_b16 v[80:81], v169\n\t"
      "ds_read_b64_tr_b16 v[82:83], v169 offset:512\n\t"
      "ds_read_b64_tr_b16 v[84:85], v168 offset:1024\n\t"
      "ds_read_b64_tr_b16 v[86:87], v168 offset:1536\n\t"
      "ds_read_b128 v[64:67], v162\n\t"
      "ds_read_b64_tr_b16 v[88:89], v169 offset:1024\n\t"
      "ds_read_b64_tr_b16 v[90:91], v169 offset:1536\n\t"
      "ds_read_b64_tr_b16 v[104:105], v168 offset:2048\n\t"
      "ds_read_b64_tr_b16 v[106:107], v168 offset:2560\n\t"
      "ds_read_b128 v[96:99], v161 offset:1024\n\t"
      "ds_read_b128 v[100:103], v161 offset:15360\n\t"
      "ds_read_b64_tr_b16 v[108:109], v169 offset:2048\n\t"
      "ds_read_b64_tr_b16 v[110:111], v169 offset:2560\n\t"
      "ds_read_b64_tr_b16 v[112:113], v168 offset:3072\n\t"
      "ds_read_b64_tr_b16 v[114:115], v168 offset:3584\n\t"
      "ds_read_b128 v[92:95], v163 offset:1024\n\t"
      "ds_read_b64_tr_b16 v[116:117], v169 offset:3072\n\t"
      "ds_read_b64_tr_b16 v[118:119], v169 offset:3584\n\t"
      "s_waitcnt vmcnt(0)\n\t"
      "s_barrier\n\t"
      "s_waitcnt lgkmcnt(14)\n\t"
      "v_mfma_f32_16x16x32_bf16 v[0:3], v[76:79], v[68:71], 0\n\t"
      "ds_read_b64_tr_b16 v[132:133], v168 offset:16384\n\t"
      "ds_read_b64_tr_b16 v[134:135], v168 offset:16896\n\t"
      "v_mfma_f32_16x16x32_bf16 v[8:11], v[76:79], v[72:75], 0\n\t"
      "ds_read_b128 v[124:127], v160 offset:2048\n\t"
      "v_mfma_f32_16x16x32_bf16 v[4:7], v[80:83], v[68:71], 0\n\t"
      "ds_read_b128 v[128:131], v160 offset:16384\n\t"
      "v_mfma_f32_16x16x32_bf16 v[12:15], v[80:83], v[72:75], 0\n\t"
      "ds_read_b64_tr_b16 v[136:137], v169 offset:16384\n\t"
      "v_mfma_f32_16x16x32_bf16 v[24:27], v[84:87], v[68:71], 0\n\t"
      "ds_read_b64_tr_b16 v[138:139], v169 offset:16896\n\t"
      "v_mfma_f32_16x16x32_bf16 v[32:35], v[84:87], v[72:75], 0\n\t"
      "ds_read_b64_tr_b16 v[140:141], v168 offset:17408\n\t"
      "s_waitcnt lgkmcnt(14)\n\t"
      "v_mfma_f32_16x16x32_bf16 v[16:19], v[84:87], v[64:67], 0\n\t"
      "ds_read_b64_tr_b16 v[142:143], v168 offset:17920\n\t"
      "v_mfma_f32_16x16x32_bf16 v[28:31], v[88:91], v[68:71], 0\n\t"
      "ds_read_b128 v[120:123], v162 offset:2048\n\t"
      "v_mfma_f32_16x16x32_bf16 v[36:39], v[88:91], v[72:75], 0\n\t"
      "ds_read_b64_tr_b16 v[144:145], v169 offset:17408\n\t"
      "v_mfma_f32_16x16x32_bf16 v[20:23], v[88:91], v[64:67], 0\n\t"
      "ds_read_b64_tr_b16 v[146:147], v169 offset:17920\n\t"
      "s_waitcnt lgkmcnt(11)\n\t"
      "v_mfma_f32_16x16x32_bf16 v[0:3], v[104:107], v[96:99], v[0:3]\n\t"
      "ds_read_b64_tr_b16 v[76:77], v168 offset:18432\n\t"
      "ds_read_b64_tr_b16 v[78:79], v168 offset:18944\n\t"
      "v_mfma_f32_16x16x32_bf16 v[8:11], v[104:107], v[100:103], v[8:11]\n\t"
      "ds_read_b128 v[68:71], v161 offset:3072\n\t"
      "v_mfma_f32_16x16x32_bf16 v[4:7], v[108:111], v[96:99], v[4:7]\n\t"
      "ds_read_b128 v[72:75], v161 offset:17408\n\t"
      "v_mfma_f32_16x16x32_bf16 v[12:15], v[108:111], v[100:103], v[12:15]\n\t"
      "ds_read_b64_tr_b16 v[80:81], v169 offset:18432\n\t"
      "v_mfma_f32_16x16x32_bf16 v[24:27], v[112:115], v[96:99], v[24:27]\n\t"
      "ds_read_b64_tr_b16 v[82:83], v169 offset:18944\n\t"
      "v_mfma_f32_16x16x32_bf16 v[32:35], v[112:115], v[100:103], v[32:35]\n\t"
      "ds_read_b64_tr_b16 v[84:85], v168 offset:19456\n\t"
      "v_mfma_f32_16x16x32_bf16 v[16:19], v[112:115], v[92:95], v[16:19]\n\t"
      "ds_read_b64_tr_b16 v[86:87], v168 offset:19968\n\t"
      "v_mfma_f32_16x16x32_bf16 v[28:31], v[116:119], v[96:99], v[28:31]\n\t"
      "ds_read_b128 v[64:67], v163 offset:3072\n\t"
      "v_mfma_f32_16x16x32_bf16 v[36:39], v[116:119], v[100:103], v[36:39]\n\t"
      "ds_read_b64_tr_b16 v[88:89], v169 offset:19456\n\t"
      "v_mfma_f32_16x16x32_bf16 v[20:23], v[116:119], v[92:95], v[20:23]\n\t"
      "ds_read_b64_tr_b16 v[90:91], v169 offset:19968\n\t"
      "s_barrier\n\t"
      "s_waitcnt lgkmcnt(11)\n\t"
      "v_mfma_f32_16x16x32_bf16 v[0:3], v[132:135], v[124:127], v[0:3]\n\t"
      "ds_read_b64_tr_b16 v[104:105], v166\n\t"
      "v_lshlrev_b32_e32 v199, 2, v193\n\t"
      "v_mfma_f32_16x16x32_bf16 v[8:11], v[132:135], v[128:131], v[8:11]\n\t"
      "ds_read_b64_tr_b16 v[106:107], v166 offset:512\n\t"
      "v_sub_u32_e32 v199, v194, v199\n\t"
      "v_mfma_f32_16x16x32_bf16 v[4:7], v[136:139], v[124:127], v[4:7]\n\t"
      "ds_read_b128 v[96:99], v160\n\t"
      "v_mov_b32_e32 v200, 0xff800000\n\t"
      "v_mfma_f32_16x16x32_bf16 v[12:15], v[136:139], v[128:131], v[12:15]\n\t"
      "ds_read_b128 v[100:103], v160 offset:14336\n\t"
      "s_cmp_lt_u32 s25, 2\n\t"
      "v_mfma_f32_16x16x32_bf16 v[24:27], v[140:143], v[124:127], v[24:27]\n\t"
      "ds_read_b128 v[92:95], v162\n\t"
      "s_cselect_b32 s35, 0xff800000, 0\n\t"
      "v_mfma_f32_16x16x32_bf16 v[32:35], v[140:143], v[128:131], v[32:35]\n\t"
      "ds_read_b64_tr_b16 v[108:109], v167\n\t"
      "s_bitcmp0_b32 s24, 0\n\t"
      "v_mfma_f32_16x16x32_bf16 v[16:19], v[140:143], v[120:123], v[16:19]\n\t"
      "ds_read_b64_tr_b16 v[110:111], v167 offset:512\n\t"
      "s_cselect_b32 s35, s35, 0\n\t"
      "v_mfma_f32_16x16x32_bf16 v[28:31], v[144:147], v[124:127], v[28:31]\n\t"
      "v_cmp_ge_i32_e32 vcc, -9, v199\n\t"
      "v_mfma_f32_16x16x32_bf16 v[36:39], v[144:147], v[128:131], v[36:39]\n\t"
      "v_subrev_u32_e32 v201, 0, v199\n\t"
      "v_mfma_f32_16x16x32_bf16 v[20:23], v[144:147], v[120:123], v[20:23]\n\t"
      "v_cmp_gt_u32_e64 s[64:65], 8, v201\n\t"
      "s_waitcnt lgkmcnt(7)\n\t"
      "v_mfma_f32_16x16x32_bf16 v[0:3], v[76:79], v[68:71], v[0:3]\n\t"
      "ds_read_b64_tr_b16 v[132:133], v166 offset:1024\n\t"
      "v_cndmask_b32_e64 v172, v200, 0, vcc\n\t"
      "v_mfma_f32_16x16x32_bf16 v[8:11], v[76:79], v[72:75], v[8:11]\n\t"
      "ds_read_b64_tr_b16 v[134:135], v166 offset:1536\n\t"
      "v_cndmask_b32_e64 v176, v200, 0, s[64:65]\n\t"
      "v_mfma_f32_16x16x32_bf16 v[4:7], v[80:83], v[68:71], v[4:7]\n\t"
      "ds_read_b128 v[124:127], v161 offset:1024\n\t"
      "v_or_b32_e32 v180, s35, v172\n\t"
      "v_mfma_f32_16x16x32_bf16 v[12:15], v[80:83], v[72:75], v[12:15]\n\t"
      "ds_read_b128 v[128:131], v161 offset:15360\n\t"
      "v_cmp_ge_i32_e32 vcc, -8, v199\n\t"
      "v_mfma_f32_16x16x32_bf16 v[24:27], v[84:87], v[68:71], v[24:27]\n\t"
      "ds_read_b128 v[120:123], v163 offset:1024\n\t"
      "v_subrev_u32_e32 v201, 1, v199\n\t"
      "v_mfma_f32_16x16x32_bf16 v[32:35], v[84:87], v[72:75], v[32:35]\n\t"
      "ds_read_b64_tr_b16 v[136:137], v167 offset:1024\n\t"
      "v_cmp_gt_u32_e64 s[64:65], 8, v201\n\t"
      "v_mfma_f32_16x16x32_bf16 v[16:19], v[84:87], v[64:67], v[16:19]\n\t"
      "ds_read_b64_tr_b16 v[138:139], v167 offset:1536\n\t"
      "v_cndmask_b32_e64 v173, v200, 0, vcc\n\t"
      "v_mfma_f32_16x16x32_bf16 v[28:31], v[88:91], v[68:71], v[28:31]\n\t"
      "v_cndmask_b32_e64 v177, v200, 0, s[64:65]\n\t"
      "v_mfma_f32_16x16x32_bf16 v[36:39], v[88:91], v[72:75], v[36:39]\n\t"
      "v_or_b32_e32 v181, s35, v173\n\t"
      "v_mfma_f32_16x16x32_bf16 v[20:23], v[88:91], v[64:67], v[20:23]\n\t"
      "v_cmp_ge_i32_e32 vcc, -7, v199\n\t"
      "s_waitcnt lgkmcnt(7)\n\t"
      "v_mfma_f32_16x16x32_bf16 v[48:51], v[96:99], v[104:107], 0\n\t"
      "ds_read_b64_tr_b16 v[76:77], v166 offset:2048\n\t"
      "ds_read_b64_tr_b16 v[78:79], v166 offset:2560\n\t"
      "v_subrev_u32_e32 v201, 2, v199\n\t"
      "v_mfma_f32_16x16x32_bf16 v[56:59], v[100:103], v[104:107], 0\n\t"
      "ds_read_b128 v[68:71], v160 offset:2048\n\t"
      "v_cmp_gt_u32_e64 s[64:65], 8, v201\n\t"
      "v_mfma_f32_16x16x32_bf16 v[40:43], v[92:95], v[104:107], 0\n\t"
      "ds_read_b128 v[72:75], v160 offset:16384\n\t"
      "v_cndmask_b32_e64 v174, v200, 0, vcc\n\t"
      "v_mfma_f32_16x16x32_bf16 v[52:55], v[96:99], v[108:111], 0\n\t"
      "ds_read_b128 v[64:67], v162 offset:2048\n\t"
      "v_cndmask_b32_e64 v178, v200, 0, s[64:65]\n\t"
      "v_mfma_f32_16x16x32_bf16 v[60:63], v[100:103], v[108:111], 0\n\t"
      "ds_read_b64_tr_b16 v[80:81], v167 offset:2048\n\t"
      "v_or_b32_e32 v182, s35, v174\n\t"
      "v_mfma_f32_16x16x32_bf16 v[44:47], v[92:95], v[108:111], 0\n\t"
      "ds_read_b64_tr_b16 v[82:83], v167 offset:2560\n\t"
      "v_cmp_ge_i32_e32 vcc, -6, v199\n\t"
      "s_waitcnt lgkmcnt(7)\n\t"
      "v_mfma_f32_16x16x32_bf16 v[48:51], v[124:127], v[132:135], v[48:51]\n\t"
      "ds_read_b64_tr_b16 v[104:105], v166 offset:3072\n\t"
      "ds_read_b64_tr_b16 v[106:107], v166 offset:3584\n\t"
      "v_subrev_u32_e32 v201, 3, v199\n\t"
      "v_mfma_f32_16x16x32_bf16 v[56:59], v[128:131], v[132:135], v[56:59]\n\t"
      "ds_read_b128 v[96:99], v161 offset:3072\n\t"
      "v_cmp_gt_u32_e64 s[64:65], 8, v201\n\t"
      "v_mfma_f32_16x16x32_bf16 v[40:43], v[120:123], v[132:135], v[40:43]\n\t"
      "ds_read_b128 v[100:103], v161 offset:17408\n\t"
      "v_cndmask_b32_e64 v175, v200, 0, vcc\n\t"
      "v_mfma_f32_16x16x32_bf16 v[52:55], v[124:127], v[136:139], v[52:55]\n\t"
      "ds_read_b128 v[92:95], v163 offset:3072\n\t"
      "v_cndmask_b32_e64 v179, v200, 0, s[64:65]\n\t"
      "v_mfma_f32_16x16x32_bf16 v[60:63], v[128:131], v[136:139], v[60:63]\n\t"
      "ds_read_b64_tr_b16 v[108:109], v167 offset:3072\n\t"
      "v_or_b32_e32 v183, s35, v175\n\t"
      "v_mfma_f32_16x16x32_bf16 v[44:47], v[120:123], v[136:139], v[44:47]\n\t"
      "ds_read_b64_tr_b16 v[110:111], v167 offset:3584\n\t"
      "v_lshrrev_b32_e32 v201, 31, v172\n\t"
      "s_barrier\n\t"
      "s_waitcnt lgkmcnt(7)\n\t"
      "v_mfma_f32_16x16x32_bf16 v[48:51], v[68:71], v[76:79], v[48:51]\n\t"
      "ds_read_b64_tr_b16 v[132:133], v166 offset:16384\n\t"
      "ds_read_b64_tr_b16 v[134:135], v166 offset:16896\n\t"
      "ds_read_b128 v[124:127], v160 offset:4096\n\t"
      "ds_read_b128 v[128:131], v160 offset:18432\n\t"
      "ds_read_b64_tr_b16 v[136:137], v167 offset:16384\n\t"
      "ds_read_b64_tr_b16 v[138:139], v167 offset:16896\n\t"
      "v_lshrrev_b32_e32 v202, 31, v173\n\t"
      "v_mfma_f32_16x16x32_bf16 v[56:59], v[72:75], v[76:79], v[56:59]\n\t"
      "ds_read_b64_tr_b16 v[140:141], v166 offset:17408\n\t"
      "v_lshl_or_b32 v201, v202, 16, v201\n\t"
      "v_mfma_f32_16x16x32_bf16 v[40:43], v[64:67], v[76:79], v[40:43]\n\t"
      "ds_read_b64_tr_b16 v[142:143], v166 offset:17920\n\t"
      "v_mul_u32_u24_e32 v184, 0xffff, v201\n\t"
      "v_mfma_f32_16x16x32_bf16 v[52:55], v[68:71], v[80:83], v[52:55]\n\t"
      "ds_read_b128 v[120:123], v162 offset:4096\n\t"
      "v_lshrrev_b32_e32 v201, 31, v174\n\t"
      "v_mfma_f32_16x16x32_bf16 v[60:63], v[72:75], v[80:83], v[60:63]\n\t"
      "ds_read_b64_tr_b16 v[144:145], v167 offset:17408\n\t"
      "v_lshrrev_b32_e32 v202, 31, v175\n\t"
      "v_mfma_f32_16x16x32_bf16 v[44:47], v[64:67], v[80:83], v[44:47]\n\t"
      "ds_read_b64_tr_b16 v[146:147], v167 offset:17920\n\t"
      "v_lshl_or_b32 v201, v202, 16, v201\n\t"
      "s_waitcnt lgkmcnt(11)\n\t"
      "v_mfma_f32_16x16x32_bf16 v[48:51], v[96:99], v[104:107], v[48:51]\n\t"
      "ds_read_b64_tr_b16 v[76:77], v166 offset:18432\n\t"
      "ds_read_b64_tr_b16 v[78:79], v166 offset:18944\n\t"
      "ds_read_b128 v[68:71], v161 offset:5120\n\t"
      "ds_read_b128 v[72:75], v161 offset:19456\n\t"
      "ds_read_b64_tr_b16 v[80:81], v167 offset:18432\n\t"
      "ds_read_b64_tr_b16 v[82:83], v167 offset:18944\n\t"
      "v_mul_u32_u24_e32 v185, 0xffff, v201\n\t"
      "v_mfma_f32_16x16x32_bf16 v[56:59], v[100:103], v[104:107], v[56:59]\n\t"
      "ds_read_b64_tr_b16 v[84:85], v166 offset:19456\n\t"
      "s_mov_b32 s68, 0x0\n\t"
      "v_mfma_f32_16x16x32_bf16 v[40:43], v[92:95], v[104:107], v[40:43]\n\t"
      "ds_read_b64_tr_b16 v[86:87], v166 offset:19968\n\t"
      "s_mov_b32 s69, 0xffff0000\n\t"
      "v_mfma_f32_16x16x32_bf16 v[52:55], v[96:99], v[108:111], v[52:55]\n\t"
      "ds_read_b128 v[64:67], v163 offset:5120\n\t"
      "s_mov_b32 s70, 0xffff\n\t"
      "v_mfma_f32_16x16x32_bf16 v[60:63], v[100:103], v[108:111], v[60:63]\n\t"
      "ds_read_b64_tr_b16 v[88:89], v167 offset:19456\n\t"
      "s_mov_b32 s71, 0x0\n\t"
      "v_mfma_f32_16x16x32_bf16 v[44:47], v[92:95], v[108:111], v[44:47]\n\t"
      "ds_read_b64_tr_b16 v[90:91], v167 offset:19968\n\t"
      "v_lshlrev_b32_e32 v231, 3, v192\n\t"
      "s_barrier\n\t"
      "s_waitcnt lgkmcnt(11)\n\t"
      "v_mfma_f32_16x16x32_bf16 v[0:3], v[132:135], v[124:127], v[0:3]\n\t"
      "ds_read_b64_tr_b16 v[104:105], v166 offset:32768\n\t"
      "ds_read_b64_tr_b16 v[106:107], v166 offset:33280\n\t"
      "s_lshl_b32 s34, s25, 12\n\t"
      "v_mfma_f32_16x16x32_bf16 v[8:11], v[132:135], v[128:131], v[8:11]\n\t"
      "ds_read_b128 v[96:99], v160 offset:6144\n\t"
      "s_lshr_b32 s35, s39, 2\n\t"
      "v_mfma_f32_16x16x32_bf16 v[4:7], v[136:139], v[124:127], v[4:7]\n\t"
      "ds_read_b128 v[100:103], v160 offset:20480\n\t"
      "s_add_u32 s34, s34, s35\n\t"
      "v_mfma_f32_16x16x32_bf16 v[12:15], v[136:139], v[128:131], v[12:15]\n\t"
      "ds_read_b64_tr_b16 v[108:109], v167 offset:32768\n\t"
      "s_add_u32 s34, s34, 12582912\n\t"
      "v_mfma_f32_16x16x32_bf16 v[24:27], v[140:143], v[124:127], v[24:27]\n\t"
      "ds_read_b64_tr_b16 v[110:111], v167 offset:33280\n\t"
      "s_add_u32 s46, s20, s34\n\t"
      "v_mfma_f32_16x16x32_bf16 v[32:35], v[140:143], v[128:131], v[32:35]\n\t"
      "ds_read_b64_tr_b16 v[112:113], v166 offset:33792\n\t"
      "s_addc_u32 s47, s21, 0\n\t"
      "v_mfma_f32_16x16x32_bf16 v[16:19], v[140:143], v[120:123], v[16:19]\n\t"
      "ds_read_b64_tr_b16 v[114:115], v166 offset:34304\n\t"
      "s_lshl_b32 s34, s28, 7\n\t"
      "v_mfma_f32_16x16x32_bf16 v[28:31], v[144:147], v[124:127], v[28:31]\n\t"
      "ds_read_b128 v[92:95], v162 offset:6144\n\t"
      "s_add_u32 s58, s46, s34\n\t"
      "v_mfma_f32_16x16x32_bf16 v[36:39], v[144:147], v[128:131], v[36:39]\n\t"
      "ds_read_b64_tr_b16 v[116:117], v167 offset:33792\n\t"
      "s_addc_u32 s59, s47, 0\n\t"
      "v_mfma_f32_16x16x32_bf16 v[20:23], v[144:147], v[120:123], v[20:23]\n\t"
      "ds_read_b64_tr_b16 v[118:119], v167 offset:34304\n\t"
      "s_and_b32 s34, s24, 1\n\t"
      "s_waitcnt lgkmcnt(11)\n\t"
      "v_mfma_f32_16x16x32_bf16 v[0:3], v[76:79], v[68:71], v[0:3]\n\t"
      "ds_read_b64_tr_b16 v[132:133], v166 offset:34816\n\t"
      "ds_read_b64_tr_b16 v[134:135], v166 offset:35328\n\t"
      "s_lshl1_add_u32 s34, s25, s34\n\t"
      "v_mfma_f32_16x16x32_bf16 v[8:11], v[76:79], v[72:75], v[8:11]\n\t"
      "ds_read_b128 v[124:127], v161 offset:7168\n\t"
      "s_lshl_b32 s34, s34, 5\n\t"
      "v_mfma_f32_16x16x32_bf16 v[4:7], v[80:83], v[68:71], v[4:7]\n\t"
      "ds_read_b128 v[128:131], v161 offset:21504\n\t"
      "s_lshl1_add_u32 s34, s26, s34\n\t"
      "v_mfma_f32_16x16x32_bf16 v[12:15], v[80:83], v[72:75], v[12:15]\n\t"
      "ds_read_b64_tr_b16 v[136:137], v167 offset:34816\n\t"
      "s_bfe_u32 s35, s24, 65537\n\t"
      "v_mfma_f32_16x16x32_bf16 v[24:27], v[84:87], v[68:71], v[24:27]\n\t"
      "ds_read_b64_tr_b16 v[138:139], v167 offset:35328\n\t"
      "s_add_u32 s34, s34, s35\n\t"
      "v_mfma_f32_16x16x32_bf16 v[32:35], v[84:87], v[72:75], v[32:35]\n\t"
      "ds_read_b64_tr_b16 v[140:141], v166 offset:35840\n\t"
      "s_lshl_b32 s34, s34, 10\n\t"
      "v_mfma_f32_16x16x32_bf16 v[16:19], v[84:87], v[64:67], v[16:19]\n\t"
      "ds_read_b64_tr_b16 v[142:143], v166 offset:36352\n\t"
      "s_add_u32 s42, s20, s34\n\t"
      "v_mfma_f32_16x16x32_bf16 v[28:31], v[88:91], v[68:71], v[28:31]\n\t"
      "ds_read_b128 v[120:123], v165 offset:7168\n\t"
      "s_addc_u32 s43, s21, 0\n\t"
      "v_mfma_f32_16x16x32_bf16 v[36:39], v[88:91], v[72:75], v[36:39]\n\t"
      "ds_read_b64_tr_b16 v[144:145], v167 offset:35840\n\t"
      "s_add_u32 s44, s42, 16384\n\t"
      "v_mfma_f32_16x16x32_bf16 v[20:23], v[88:91], v[64:67], v[20:23]\n\t"
      "ds_read_b64_tr_b16 v[146:147], v167 offset:36352\n\t"
      "s_addc_u32 s45, s43, 0\n\t"
      "s_barrier\n\t"
      "s_waitcnt lgkmcnt(11)\n\t"
      "v_mfma_f32_16x16x32_bf16 v[0:3], v[104:107], v[96:99], v[0:3]\n\t"
      "ds_read_b64_tr_b16 v[76:77], v166 offset:49152\n\t"
      "ds_read_b64_tr_b16 v[78:79], v166 offset:49664\n\t"
      "v_and_b32_e32 v199, 60, v192\n\t"
      "v_mfma_f32_16x16x32_bf16 v[8:11], v[104:107], v[100:103], v[8:11]\n\t"
      "ds_read_b128 v[68:71], v160 offset:8192\n\t"
      "v_and_b32_e32 v200, 24, v192\n\t"
      "v_mfma_f32_16x16x32_bf16 v[4:7], v[108:111], v[96:99], v[4:7]\n\t"
      "ds_read_b128 v[72:75], v160 offset:22528\n\t"
      "v_lshlrev_b32_e32 v202, 3, v196\n\t"
      "v_mfma_f32_16x16x32_bf16 v[12:15], v[108:111], v[100:103], v[12:15]\n\t"
      "ds_read_b64_tr_b16 v[80:81], v167 offset:49152\n\t"
      "v_lshl_or_b32 v202, v200, 2, v202\n\t"
      "v_mfma_f32_16x16x32_bf16 v[24:27], v[112:115], v[96:99], v[24:27]\n\t"
      "ds_read_b64_tr_b16 v[82:83], v167 offset:49664\n\t"
      "v_lshl_or_b32 v201, v199, 5, v202\n\t"
      "v_mfma_f32_16x16x32_bf16 v[32:35], v[112:115], v[100:103], v[32:35]\n\t"
      "ds_read_b64_tr_b16 v[84:85], v166 offset:50176\n\t"
      "v_add_u32_e32 v216, 106496, v201\n\t"
      "v_mfma_f32_16x16x32_bf16 v[16:19], v[112:115], v[92:95], v[16:19]\n\t"
      "ds_read_b64_tr_b16 v[86:87], v166 offset:50688\n\t"
      "v_mov_b32_e32 v199, 2048\n\t"
      "v_mfma_f32_16x16x32_bf16 v[28:31], v[116:119], v[96:99], v[28:31]\n\t"
      "ds_read_b128 v[64:67], v164 offset:8192\n\t"
      "v_cndmask_b32_e64 v199, v199, 0, s[68:69]\n\t"
      "v_mfma_f32_16x16x32_bf16 v[36:39], v[116:119], v[100:103], v[36:39]\n\t"
      "ds_read_b64_tr_b16 v[88:89], v167 offset:50176\n\t"
      "v_add_u32_e32 v220, v216, v199\n\t"
      "v_mfma_f32_16x16x32_bf16 v[20:23], v[116:119], v[92:95], v[20:23]\n\t"
      "ds_read_b64_tr_b16 v[90:91], v167 offset:50688\n\t"
      "v_xor_b32_e32 v217, 32, v216\n\t"
      "s_waitcnt lgkmcnt(11)\n\t"
      "v_mfma_f32_16x16x32_bf16 v[0:3], v[132:135], v[124:127], v[0:3]\n\t"
      "ds_read_b64_tr_b16 v[104:105], v166 offset:51200\n\t"
      "ds_read_b64_tr_b16 v[106:107], v166 offset:51712\n\t"
      "v_xor_b32_e32 v218, 64, v216\n\t"
      "v_mfma_f32_16x16x32_bf16 v[8:11], v[132:135], v[128:131], v[8:11]\n\t"
      "ds_read_b128 v[96:99], v161 offset:9216\n\t"
      "v_xor_b32_e32 v219, 96, v216\n\t"
      "v_mfma_f32_16x16x32_bf16 v[4:7], v[136:139], v[124:127], v[4:7]\n\t"
      "ds_read_b128 v[100:103], v161 offset:23552\n\t"
      "v_xor_b32_e32 v221, 32, v220\n\t"
      "v_mfma_f32_16x16x32_bf16 v[12:15], v[136:139], v[128:131], v[12:15]\n\t"
      "ds_read_b64_tr_b16 v[108:109], v167 offset:51200\n\t"
      "v_xor_b32_e32 v222, 64, v220\n\t"
      "v_mfma_f32_16x16x32_bf16 v[24:27], v[140:143], v[124:127], v[24:27]\n\t"
      "ds_read_b64_tr_b16 v[110:111], v167 offset:51712\n\t"
      "v_xor_b32_e32 v223, 96, v220\n\t"
      "v_mfma_f32_16x16x32_bf16 v[32:35], v[140:143], v[128:131], v[32:35]\n\t"
      "ds_read_b64_tr_b16 v[112:113], v166 offset:52224\n\t"
      "s_lshr_b32 s34, s39, 1\n\t"
      "v_mfma_f32_16x16x32_bf16 v[16:19], v[140:143], v[120:123], v[16:19]\n\t"
      "ds_read_b64_tr_b16 v[114:115], v166 offset:52736\n\t"
      "s_add_u32 s34, s34, 98304\n\t"
      "v_mfma_f32_16x16x32_bf16 v[28:31], v[144:147], v[124:127], v[28:31]\n\t"
      "ds_read_b128 v[92:95], v165 offset:9216\n\t"
      "v_lshrrev_b32_e32 v199, 1, v193\n\t"
      "v_mfma_f32_16x16x32_bf16 v[36:39], v[144:147], v[128:131], v[36:39]\n\t"
      "ds_read_b64_tr_b16 v[116:117], v167 offset:52224\n\t"
      "v_xor_b32_e32 v199, v199, v198\n\t"
      "v_mfma_f32_16x16x32_bf16 v[20:23], v[144:147], v[120:123], v[20:23]\n\t"
      "ds_read_b64_tr_b16 v[118:119], v167 offset:52736\n\t"
      "v_lshlrev_b32_e32 v199, 4, v199\n\t"
      "s_barrier\n\t"
      "s_waitcnt lgkmcnt(11)\n\t"
      "v_mfma_f32_16x16x32_bf16 v[0:3], v[76:79], v[68:71], v[0:3]\n\t"
      "ds_read_b64_tr_b16 v[132:133], v168\n\t"
      "ds_read_b64_tr_b16 v[134:135], v168 offset:512\n\t"
      "v_lshl_or_b32 v199, v194, 7, v199\n\t"
      "v_mfma_f32_16x16x32_bf16 v[8:11], v[76:79], v[72:75], v[8:11]\n\t"
      "ds_read_b128 v[124:127], v160 offset:10240\n\t"
      "v_and_b32_e32 v200, 1, v193\n\t"
      "v_mfma_f32_16x16x32_bf16 v[4:7], v[80:83], v[68:71], v[4:7]\n\t"
      "ds_read_b128 v[128:131], v160 offset:24576\n\t"
      "v_lshl_or_b32 v199, v200, 3, v199\n\t"
      "v_mfma_f32_16x16x32_bf16 v[12:15], v[80:83], v[72:75], v[12:15]\n\t"
      "ds_read_b64_tr_b16 v[136:137], v169\n\t"
      "v_add_u32_e32 v228, s34, v199\n\t"
      "v_mfma_f32_16x16x32_bf16 v[24:27], v[84:87], v[68:71], v[24:27]\n\t"
      "ds_read_b64_tr_b16 v[138:139], v169 offset:512\n\t"
      "v_mfma_f32_16x16x32_bf16 v[32:35], v[84:87], v[72:75], v[32:35]\n\t"
      "ds_read_b64_tr_b16 v[140:141], v168 offset:1024\n\t"
      "v_mfma_f32_16x16x32_bf16 v[16:19], v[84:87], v[64:67], v[16:19]\n\t"
      "ds_read_b64_tr_b16 v[142:143], v168 offset:1536\n\t"
      "v_mfma_f32_16x16x32_bf16 v[28:31], v[88:91], v[68:71], v[28:31]\n\t"
      "ds_read_b128 v[120:123], v164 offset:10240\n\t"
      "v_mfma_f32_16x16x32_bf16 v[36:39], v[88:91], v[72:75], v[36:39]\n\t"
      "ds_read_b64_tr_b16 v[144:145], v169 offset:1024\n\t"
      "v_mfma_f32_16x16x32_bf16 v[20:23], v[88:91], v[64:67], v[20:23]\n\t"
      "ds_read_b64_tr_b16 v[146:147], v169 offset:1536\n\t"
      "s_waitcnt lgkmcnt(11)\n\t"
      "v_mfma_f32_16x16x32_bf16 v[0:3], v[104:107], v[96:99], v[0:3]\n\t"
      "ds_read_b64_tr_b16 v[76:77], v168 offset:2048\n\t"
      "ds_read_b64_tr_b16 v[78:79], v168 offset:2560\n\t"
      "v_mfma_f32_16x16x32_bf16 v[8:11], v[104:107], v[100:103], v[8:11]\n\t"
      "ds_read_b128 v[68:71], v161 offset:11264\n\t"
      "v_mfma_f32_16x16x32_bf16 v[4:7], v[108:111], v[96:99], v[4:7]\n\t"
      "ds_read_b128 v[72:75], v161 offset:25600\n\t"
      "v_mfma_f32_16x16x32_bf16 v[12:15], v[108:111], v[100:103], v[12:15]\n\t"
      "ds_read_b64_tr_b16 v[80:81], v169 offset:2048\n\t"
      "v_mfma_f32_16x16x32_bf16 v[24:27], v[112:115], v[96:99], v[24:27]\n\t"
      "ds_read_b64_tr_b16 v[82:83], v169 offset:2560\n\t"
      "v_mfma_f32_16x16x32_bf16 v[32:35], v[112:115], v[100:103], v[32:35]\n\t"
      "ds_read_b64_tr_b16 v[84:85], v168 offset:3072\n\t"
      "v_mfma_f32_16x16x32_bf16 v[16:19], v[112:115], v[92:95], v[16:19]\n\t"
      "ds_read_b64_tr_b16 v[86:87], v168 offset:3584\n\t"
      "v_mfma_f32_16x16x32_bf16 v[28:31], v[116:119], v[96:99], v[28:31]\n\t"
      "ds_read_b128 v[64:67], v165 offset:11264\n\t"
      "v_mfma_f32_16x16x32_bf16 v[36:39], v[116:119], v[100:103], v[36:39]\n\t"
      "ds_read_b64_tr_b16 v[88:89], v169 offset:3072\n\t"
      "v_mfma_f32_16x16x32_bf16 v[20:23], v[116:119], v[92:95], v[20:23]\n\t"
      "ds_read_b64_tr_b16 v[90:91], v169 offset:3584\n\t"
      "s_barrier\n\t"
      "s_waitcnt lgkmcnt(11)\n\t"
      "v_mfma_f32_16x16x32_bf16 v[0:3], v[132:135], v[124:127], v[0:3]\n\t"
      "ds_read_b64_tr_b16 v[104:105], v168 offset:16384\n\t"
      "ds_read_b64_tr_b16 v[106:107], v168 offset:16896\n\t"
      "v_mfma_f32_16x16x32_bf16 v[8:11], v[132:135], v[128:131], v[8:11]\n\t"
      "ds_read_b128 v[96:99], v160 offset:12288\n\t"
      "v_mfma_f32_16x16x32_bf16 v[4:7], v[136:139], v[124:127], v[4:7]\n\t"
      "ds_read_b128 v[100:103], v160 offset:26624\n\t"
      "v_mfma_f32_16x16x32_bf16 v[12:15], v[136:139], v[128:131], v[12:15]\n\t"
      "ds_read_b64_tr_b16 v[108:109], v169 offset:16384\n\t"
      "v_mfma_f32_16x16x32_bf16 v[24:27], v[140:143], v[124:127], v[24:27]\n\t"
      "ds_read_b64_tr_b16 v[110:111], v169 offset:16896\n\t"
      "v_mfma_f32_16x16x32_bf16 v[32:35], v[140:143], v[128:131], v[32:35]\n\t"
      "ds_read_b64_tr_b16 v[112:113], v168 offset:17408\n\t"
      "v_mfma_f32_16x16x32_bf16 v[16:19], v[140:143], v[120:123], v[16:19]\n\t"
      "ds_read_b64_tr_b16 v[114:115], v168 offset:17920\n\t"
      "v_mfma_f32_16x16x32_bf16 v[28:31], v[144:147], v[124:127], v[28:31]\n\t"
      "ds_read_b128 v[92:95], v164 offset:12288\n\t"
      "v_mfma_f32_16x16x32_bf16 v[36:39], v[144:147], v[128:131], v[36:39]\n\t"
      "ds_read_b64_tr_b16 v[116:117], v169 offset:17408\n\t"
      "v_mfma_f32_16x16x32_bf16 v[20:23], v[144:147], v[120:123], v[20:23]\n\t"
      "ds_read_b64_tr_b16 v[118:119], v169 offset:17920\n\t"
      "s_waitcnt lgkmcnt(11)\n\t"
      "v_mfma_f32_16x16x32_bf16 v[0:3], v[76:79], v[68:71], v[0:3]\n\t"
      "ds_read_b64_tr_b16 v[132:133], v168 offset:18432\n\t"
      "ds_read_b64_tr_b16 v[134:135], v168 offset:18944\n\t"
      "v_mfma_f32_16x16x32_bf16 v[8:11], v[76:79], v[72:75], v[8:11]\n\t"
      "ds_read_b128 v[124:127], v161 offset:13312\n\t"
      "v_mfma_f32_16x16x32_bf16 v[4:7], v[80:83], v[68:71], v[4:7]\n\t"
      "ds_read_b128 v[128:131], v161 offset:27648\n\t"
      "v_mfma_f32_16x16x32_bf16 v[12:15], v[80:83], v[72:75], v[12:15]\n\t"
      "ds_read_b64_tr_b16 v[136:137], v169 offset:18432\n\t"
      "v_mfma_f32_16x16x32_bf16 v[24:27], v[84:87], v[68:71], v[24:27]\n\t"
      "ds_read_b64_tr_b16 v[138:139], v169 offset:18944\n\t"
      "v_mfma_f32_16x16x32_bf16 v[32:35], v[84:87], v[72:75], v[32:35]\n\t"
      "ds_read_b64_tr_b16 v[140:141], v168 offset:19456\n\t"
      "v_mfma_f32_16x16x32_bf16 v[16:19], v[84:87], v[64:67], v[16:19]\n\t"
      "ds_read_b64_tr_b16 v[142:143], v168 offset:19968\n\t"
      "v_mfma_f32_16x16x32_bf16 v[28:31], v[88:91], v[68:71], v[28:31]\n\t"
      "ds_read_b128 v[120:123], v165 offset:13312\n\t"
      "v_mfma_f32_16x16x32_bf16 v[36:39], v[88:91], v[72:75], v[36:39]\n\t"
      "ds_read_b64_tr_b16 v[144:145], v169 offset:19456\n\t"
      "v_mfma_f32_16x16x32_bf16 v[20:23], v[88:91], v[64:67], v[20:23]\n\t"
      "ds_read_b64_tr_b16 v[146:147], v169 offset:19968\n\t"
      "s_barrier\n\t"
      "s_waitcnt lgkmcnt(11)\n\t"
      "v_mfma_f32_16x16x32_bf16 v[0:3], v[104:107], v[96:99], v[0:3]\n\t"
      "ds_read_b64_tr_b16 v[76:77], v166\n\t"
      "v_mfma_f32_16x16x32_bf16 v[8:11], v[104:107], v[100:103], v[8:11]\n\t"
      "ds_read_b64_tr_b16 v[78:79], v166 offset:512\n\t"
      "v_mfma_f32_16x16x32_bf16 v[4:7], v[108:111], v[96:99], v[4:7]\n\t"
      "ds_read_b128 v[68:71], v160 offset:4096\n\t"
      "v_mfma_f32_16x16x32_bf16 v[12:15], v[108:111], v[100:103], v[12:15]\n\t"
      "ds_read_b128 v[72:75], v160 offset:18432\n\t"
      "v_mfma_f32_16x16x32_bf16 v[24:27], v[112:115], v[96:99], v[24:27]\n\t"
      "ds_read_b128 v[64:67], v162 offset:4096\n\t"
      "v_mfma_f32_16x16x32_bf16 v[32:35], v[112:115], v[100:103], v[32:35]\n\t"
      "ds_read_b64_tr_b16 v[80:81], v167\n\t"
      "v_mfma_f32_16x16x32_bf16 v[16:19], v[112:115], v[92:95], v[16:19]\n\t"
      "ds_read_b64_tr_b16 v[82:83], v167 offset:512\n\t"
      "v_mfma_f32_16x16x32_bf16 v[28:31], v[116:119], v[96:99], v[28:31]\n\t"
      "v_mfma_f32_16x16x32_bf16 v[36:39], v[116:119], v[100:103], v[36:39]\n\t"
      "v_mfma_f32_16x16x32_bf16 v[20:23], v[116:119], v[92:95], v[20:23]\n\t"
      "s_waitcnt lgkmcnt(7)\n\t"
      "v_mfma_f32_16x16x32_bf16 v[0:3], v[132:135], v[124:127], v[0:3]\n\t"
      "ds_read_b64_tr_b16 v[104:105], v166 offset:1024\n\t"
      "v_mfma_f32_16x16x32_bf16 v[8:11], v[132:135], v[128:131], v[8:11]\n\t"
      "ds_read_b64_tr_b16 v[106:107], v166 offset:1536\n\t"
      "v_mfma_f32_16x16x32_bf16 v[4:7], v[136:139], v[124:127], v[4:7]\n\t"
      "ds_read_b128 v[96:99], v161 offset:5120\n\t"
      "v_mfma_f32_16x16x32_bf16 v[12:15], v[136:139], v[128:131], v[12:15]\n\t"
      "ds_read_b128 v[100:103], v161 offset:19456\n\t"
      "v_mfma_f32_16x16x32_bf16 v[24:27], v[140:143], v[124:127], v[24:27]\n\t"
      "ds_read_b128 v[92:95], v163 offset:5120\n\t"
      "v_mfma_f32_16x16x32_bf16 v[32:35], v[140:143], v[128:131], v[32:35]\n\t"
      "ds_read_b64_tr_b16 v[108:109], v167 offset:1024\n\t"
      "v_mfma_f32_16x16x32_bf16 v[16:19], v[140:143], v[120:123], v[16:19]\n\t"
      "ds_read_b64_tr_b16 v[110:111], v167 offset:1536\n\t"
      "v_mfma_f32_16x16x32_bf16 v[28:31], v[144:147], v[124:127], v[28:31]\n\t"
      "v_mfma_f32_16x16x32_bf16 v[36:39], v[144:147], v[128:131], v[36:39]\n\t"
      "v_mfma_f32_16x16x32_bf16 v[20:23], v[144:147], v[120:123], v[20:23]\n\t"
      "s_waitcnt lgkmcnt(7)\n\t"
      "v_mfma_f32_16x16x32_bf16 v[48:51], v[68:71], v[76:79], v[48:51]\n\t"
      "ds_read_b64_tr_b16 v[132:133], v166 offset:2048\n\t"
      "ds_read_b64_tr_b16 v[134:135], v166 offset:2560\n\t"
      "v_mfma_f32_16x16x32_bf16 v[56:59], v[72:75], v[76:79], v[56:59]\n\t"
      "ds_read_b128 v[124:127], v160 offset:6144\n\t"
      "v_mfma_f32_16x16x32_bf16 v[40:43], v[64:67], v[76:79], v[40:43]\n\t"
      "ds_read_b128 v[128:131], v160 offset:20480\n\t"
      "v_cvt_pk_bf16_f32 v84, v0, v1\n\t"
      "v_cvt_pk_bf16_f32 v85, v2, v3\n\t"
      "v_cvt_pk_bf16_f32 v86, v4, v5\n\t"
      "v_cvt_pk_bf16_f32 v87, v6, v7\n\t"
      "v_mfma_f32_16x16x32_bf16 v[52:55], v[68:71], v[80:83], v[52:55]\n\t"
      "ds_read_b128 v[120:123], v162 offset:6144\n\t"
      "v_cvt_pk_bf16_f32 v88, v8, v9\n\t"
      "v_cvt_pk_bf16_f32 v89, v10, v11\n\t"
      "v_cvt_pk_bf16_f32 v90, v12, v13\n\t"
      "v_cvt_pk_bf16_f32 v91, v14, v15\n\t"
      "v_mfma_f32_16x16x32_bf16 v[60:63], v[72:75], v[80:83], v[60:63]\n\t"
      "ds_read_b64_tr_b16 v[136:137], v167 offset:2048\n\t"
      "v_cvt_pk_bf16_f32 v152, v24, v25\n\t"
      "v_cvt_pk_bf16_f32 v153, v26, v27\n\t"
      "v_cvt_pk_bf16_f32 v154, v28, v29\n\t"
      "v_cvt_pk_bf16_f32 v155, v30, v31\n\t"
      "v_mfma_f32_16x16x32_bf16 v[44:47], v[64:67], v[80:83], v[44:47]\n\t"
      "ds_read_b64_tr_b16 v[138:139], v167 offset:2560\n\t"
      "v_cndmask_b32_e64 v86, v86, 0, s[68:69]\n\t"
      "v_cndmask_b32_e64 v87, v87, 0, s[68:69]\n\t"
      "v_cvt_pk_bf16_f32 v156, v32, v33\n\t"
      "v_cvt_pk_bf16_f32 v157, v34, v35\n\t"
      "s_waitcnt lgkmcnt(7)\n\t"
      "v_mfma_f32_16x16x32_bf16 v[48:51], v[96:99], v[104:107], v[48:51]\n\t"
      "ds_read_b64_tr_b16 v[76:77], v166 offset:3072\n\t"
      "ds_read_b64_tr_b16 v[78:79], v166 offset:3584\n\t"
      "v_cvt_pk_bf16_f32 v158, v36, v37\n\t"
      "v_cvt_pk_bf16_f32 v159, v38, v39\n\t"
      "v_cndmask_b32_e64 v90, v90, 0, s[68:69]\n\t"
      "v_cndmask_b32_e64 v91, v91, 0, s[68:69]\n\t"
      "v_mfma_f32_16x16x32_bf16 v[56:59], v[100:103], v[104:107], v[56:59]\n\t"
      "ds_read_b128 v[68:71], v161 offset:7168\n\t"
      "v_cvt_pk_bf16_f32 v148, v16, v17\n\t"
      "v_cvt_pk_bf16_f32 v149, v18, v19\n\t"
      "v_cvt_pk_bf16_f32 v150, v20, v21\n\t"
      "v_cvt_pk_bf16_f32 v151, v22, v23\n\t"
      "v_mfma_f32_16x16x32_bf16 v[40:43], v[92:95], v[104:107], v[40:43]\n\t"
      "ds_read_b128 v[72:75], v161 offset:21504\n\t"
      "v_mov_b32_e32 v34, 0x3e8b97f6\n\t"
      "v_mfma_f32_16x16x32_bf16 v[28:31], v[156:159], v[88:91], v[176:179]\n\t"
      "v_mfma_f32_16x16x32_bf16 v[24:27], v[152:155], v[88:91], v[172:175]\n\t"
      "v_mfma_f32_16x16x32_bf16 v[20:23], v[152:155], v[84:87], v[176:179]\n\t"
      "v_mfma_f32_16x16x32_bf16 v[52:55], v[96:99], v[108:111], v[52:55]\n\t"
      "ds_read_b128 v[64:67], v165 offset:7168\n\t"
      "v_mfma_f32_16x16x32_bf16 v[16:19], v[148:151], v[84:87], v[180:183]\n\t"
      "v_mfma_f32_16x16x32_bf16 v[60:63], v[100:103], v[108:111], v[60:63]\n\t"
      "ds_read_b64_tr_b16 v[80:81], v167 offset:3072\n\t"
      "s_nop 1\n\t"
      "v_max_f32_e32 v24, v24, v28\n\t"
      "v_mfma_f32_16x16x32_bf16 v[44:47], v[92:95], v[108:111], v[44:47]\n\t"
      "ds_read_b64_tr_b16 v[82:83], v167 offset:3584\n\t"
      "v_max_f32_e32 v25, v25, v29\n\t"
      "v_max_f32_e32 v26, v26, v30\n\t"
      "v_max_f32_e32 v27, v27, v31\n\t"
      "v_max_f32_e32 v16, v16, v20\n\t"
      "s_barrier\n\t"
      "s_waitcnt lgkmcnt(7)\n\t"
      "v_mfma_f32_16x16x32_bf16 v[48:51], v[124:127], v[132:135], v[48:51]\n\t"
      "ds_read_b64_tr_b16 v[104:105], v166 offset:16384\n\t"
      "ds_read_b64_tr_b16 v[106:107], v166 offset:16896\n\t"
      "v_max_f32_e32 v17, v17, v21\n\t"
      "v_max_f32_e32 v18, v18, v22\n\t"
      "v_max_f32_e32 v19, v19, v23\n\t"
      "v_max3_f32 v33, v24, v25, v26\n\t"
      "v_mfma_f32_16x16x32_bf16 v[56:59], v[128:131], v[132:135], v[56:59]\n\t"
      "ds_read_b128 v[96:99], v160 offset:8192\n\t"
      "v_max3_f32 v32, v16, v17, v18\n\t"
      "v_max_f32_e32 v33, v33, v27\n\t"
      "v_max_f32_e32 v32, v32, v19\n\t"
      "v_mfma_f32_16x16x32_bf16 v[40:43], v[120:123], v[132:135], v[40:43]\n\t"
      "ds_read_b128 v[100:103], v160 offset:22528\n\t"
      "v_permlane16_swap_b32_e32 v32, v33\n\t"
      "v_max_f32_e32 v32, v32, v33\n\t"
      "v_mov_b32_e32 v33, v32\n\t"
      "v_mfma_f32_16x16x32_bf16 v[52:55], v[124:127], v[136:139], v[52:55]\n\t"
      "ds_read_b128 v[92:95], v164 offset:8192\n\t"
      "v_permlane32_swap_b32_e32 v32, v33\n\t"
      "v_max_f32_e32 v32, v32, v33\n\t"
      "v_mov_b32_e32 v33, v32\n\t"
      "v_mfma_f32_16x16x32_bf16 v[60:63], v[128:131], v[136:139], v[60:63]\n\t"
      "ds_read_b64_tr_b16 v[108:109], v167 offset:16384\n\t"
      "v_permlane16_swap_b32_e32 v32, v33\n\t"
      "v_mul_f32_e64 v33, -v33, v34\n\t"
      "v_mul_f32_e64 v32, -v32, v34\n\t"
      "v_fma_f32 v24, v24, v34, v33\n\t"
      "v_mfma_f32_16x16x32_bf16 v[44:47], v[120:123], v[136:139], v[44:47]\n\t"
      "ds_read_b64_tr_b16 v[110:111], v167 offset:16896\n\t"
      "v_fma_f32 v25, v25, v34, v33\n\t"
      "v_fma_f32 v26, v26, v34, v33\n\t"
      "v_fma_f32 v27, v27, v34, v33\n\t"
      "v_fma_f32 v16, v16, v34, v32\n\t"
      "s_waitcnt lgkmcnt(7)\n\t"
      "v_mfma_f32_16x16x32_bf16 v[48:51], v[68:71], v[76:79], v[48:51]\n\t"
      "ds_read_b64_tr_b16 v[132:133], v166 offset:17408\n\t"
      "ds_read_b64_tr_b16 v[134:135], v166 offset:17920\n\t"
      "v_fma_f32 v17, v17, v34, v32\n\t"
      "v_fma_f32 v18, v18, v34, v32\n\t"
      "v_fma_f32 v19, v19, v34, v32\n\t"
      "v_exp_f32_e32 v24, v24\n\t"
      "v_mfma_f32_16x16x32_bf16 v[56:59], v[72:75], v[76:79], v[56:59]\n\t"
      "ds_read_b128 v[124:127], v161 offset:9216\n\t"
      "v_exp_f32_e32 v25, v25\n\t"
      "v_exp_f32_e32 v26, v26\n\t"
      "v_mfma_f32_16x16x32_bf16 v[40:43], v[64:67], v[76:79], v[40:43]\n\t"
      "ds_read_b128 v[128:131], v161 offset:23552\n\t"
      "v_exp_f32_e32 v27, v27\n\t"
      "v_exp_f32_e32 v16, v16\n\t"
      "v_mfma_f32_16x16x32_bf16 v[52:55], v[68:71], v[80:83], v[52:55]\n\t"
      "ds_read_b128 v[120:123], v165 offset:9216\n\t"
      "v_exp_f32_e32 v17, v17\n\t"
      "v_exp_f32_e32 v18, v18\n\t"
      "v_mfma_f32_16x16x32_bf16 v[60:63], v[72:75], v[80:83], v[60:63]\n\t"
      "ds_read_b64_tr_b16 v[136:137], v167 offset:17408\n\t"
      "v_exp_f32_e32 v19, v19\n\t"
      "v_add_f32_e32 v33, v24, v25\n\t"
      "v_mfma_f32_16x16x32_bf16 v[44:47], v[64:67], v[80:83], v[44:47]\n\t"
      "ds_read_b64_tr_b16 v[138:139], v167 offset:17920\n\t"
      "v_add_f32_e32 v28, v26, v27\n\t"
      "v_add_f32_e32 v32, v16, v17\n\t"
      "v_add_f32_e32 v20, v18, v19\n\t"
      "v_add_f32_e32 v33, v33, v28\n\t"
      "s_waitcnt lgkmcnt(7)\n\t"
      "v_mfma_f32_16x16x32_bf16 v[48:51], v[96:99], v[104:107], v[48:51]\n\t"
      "ds_read_b64_tr_b16 v[76:77], v166 offset:18432\n\t"
      "ds_read_b64_tr_b16 v[78:79], v166 offset:18944\n\t"
      "v_add_f32_e32 v32, v32, v20\n\t"
      "s_nop 1\n\t"
      "v_permlane16_swap_b32_e32 v32, v33\n\t"
      "v_add_f32_e32 v32, v32, v33\n\t"
      "v_mfma_f32_16x16x32_bf16 v[56:59], v[100:103], v[104:107], v[56:59]\n\t"
      "ds_read_b128 v[68:71], v160 offset:10240\n\t"
      "v_mov_b32_e32 v33, v32\n\t"
      "s_nop 1\n\t"
      "v_permlane32_swap_b32_e32 v32, v33\n\t"
      "v_add_f32_e32 v32, v32, v33\n\t"
      "v_mfma_f32_16x16x32_bf16 v[40:43], v[92:95], v[104:107], v[40:43]\n\t"
      "ds_read_b128 v[72:75], v160 offset:24576\n\t"
      "v_mov_b32_e32 v33, v32\n\t"
      "s_nop 1\n\t"
      "v_permlane16_swap_b32_e32 v32, v33\n\t"
      "v_rcp_f32_e32 v33, v33\n\t"
      "v_mfma_f32_16x16x32_bf16 v[52:55], v[96:99], v[108:111], v[52:55]\n\t"
      "ds_read_b128 v[64:67], v164 offset:10240\n\t"
      "v_rcp_f32_e32 v32, v32\n\t"
      "v_mul_f32_e32 v24, v24, v33\n\t"
      "v_mfma_f32_16x16x32_bf16 v[60:63], v[100:103], v[108:111], v[60:63]\n\t"
      "ds_read_b64_tr_b16 v[80:81], v167 offset:18432\n\t"
      "v_mul_f32_e32 v25, v25, v33\n\t"
      "v_mul_f32_e32 v26, v26, v33\n\t"
      "v_mul_f32_e32 v27, v27, v33\n\t"
      "v_mul_f32_e32 v16, v16, v32\n\t"
      "v_mfma_f32_16x16x32_bf16 v[44:47], v[92:95], v[108:111], v[44:47]\n\t"
      "ds_read_b64_tr_b16 v[82:83], v167 offset:18944\n\t"
      "v_mul_f32_e32 v17, v17, v32\n\t"
      "v_mul_f32_e32 v18, v18, v32\n\t"
      "v_mul_f32_e32 v19, v19, v32\n\t"
      "v_cvt_pk_bf16_f32 v4, v24, v25\n\t"
      "s_waitcnt lgkmcnt(7)\n\t"
      "v_mfma_f32_16x16x32_bf16 v[48:51], v[124:127], v[132:135], v[48:51]\n\t"
      "ds_read_b64_tr_b16 v[104:105], v166 offset:19456\n\t"
      "ds_read_b64_tr_b16 v[106:107], v166 offset:19968\n\t"
      "v_cvt_pk_bf16_f32 v5, v26, v27\n\t"
      "v_cvt_pk_bf16_f32 v0, v16, v17\n\t"
      "v_cvt_pk_bf16_f32 v1, v18, v19\n\t"
      "v_and_b32_e32 v6, v184, v4\n\t"
      "v_mfma_f32_16x16x32_bf16 v[56:59], v[128:131], v[132:135], v[56:59]\n\t"
      "ds_read_b128 v[96:99], v161 offset:11264\n\t"
      "v_and_b32_e32 v7, v185, v5\n\t"
      "v_and_b32_e32 v2, v184, v0\n\t"
      "v_and_b32_e32 v3, v185, v1\n\t"
      "v_xor_b32_e32 v4, v4, v6\n\t"
      "v_mfma_f32_16x16x32_bf16 v[40:43], v[120:123], v[132:135], v[40:43]\n\t"
      "ds_read_b128 v[100:103], v161 offset:25600\n\t"
      "v_xor_b32_e32 v5, v5, v7\n\t"
      "v_xor_b32_e32 v0, v0, v2\n\t"
      "v_xor_b32_e32 v1, v1, v3\n\t"
      "v_mfma_f32_16x16x32_bf16 v[52:55], v[124:127], v[136:139], v[52:55]\n\t"
      "ds_read_b128 v[92:95], v165 offset:11264\n\t"
      "v_mfma_f32_16x16x32_bf16 v[60:63], v[128:131], v[136:139], v[60:63]\n\t"
      "ds_read_b64_tr_b16 v[108:109], v167 offset:19456\n\t"
      "v_mfma_f32_16x16x32_bf16 v[44:47], v[120:123], v[136:139], v[44:47]\n\t"
      "ds_read_b64_tr_b16 v[110:111], v167 offset:19968\n\t"
      "s_barrier\n\t"
      "s_waitcnt lgkmcnt(7)\n\t"
      "v_mfma_f32_16x16x32_bf16 v[48:51], v[68:71], v[76:79], v[48:51]\n\t"
      "ds_read_b64_tr_b16 v[132:133], v170 offset:32768\n\t"
      "ds_read_b64_tr_b16 v[134:135], v170 offset:33280\n\t"
      "v_mfma_f32_16x16x32_bf16 v[56:59], v[72:75], v[76:79], v[56:59]\n\t"
      "ds_read_b128 v[124:127], v160 offset:12288\n\t"
      "v_mfma_f32_16x16x32_bf16 v[40:43], v[64:67], v[76:79], v[40:43]\n\t"
      "ds_read_b128 v[128:131], v160 offset:26624\n\t"
      "v_mfma_f32_16x16x32_bf16 v[52:55], v[68:71], v[80:83], v[52:55]\n\t"
      "ds_read_b128 v[120:123], v164 offset:12288\n\t"
      "v_mfma_f32_16x16x32_bf16 v[60:63], v[72:75], v[80:83], v[60:63]\n\t"
      "ds_read_b64_tr_b16 v[136:137], v171 offset:32768\n\t"
      "v_mfma_f32_16x16x32_bf16 v[44:47], v[64:67], v[80:83], v[44:47]\n\t"
      "ds_read_b64_tr_b16 v[138:139], v171 offset:33280\n\t"
      "s_waitcnt lgkmcnt(7)\n\t"
      "v_mfma_f32_16x16x32_bf16 v[48:51], v[96:99], v[104:107], v[48:51]\n\t"
      "ds_read_b64_tr_b16 v[76:77], v170 offset:33792\n\t"
      "ds_read_b64_tr_b16 v[78:79], v170 offset:34304\n\t"
      "v_mfma_f32_16x16x32_bf16 v[56:59], v[100:103], v[104:107], v[56:59]\n\t"
      "ds_read_b128 v[68:71], v161 offset:13312\n\t"
      "v_mfma_f32_16x16x32_bf16 v[40:43], v[92:95], v[104:107], v[40:43]\n\t"
      "ds_read_b128 v[72:75], v161 offset:27648\n\t"
      "v_mfma_f32_16x16x32_bf16 v[52:55], v[96:99], v[108:111], v[52:55]\n\t"
      "ds_read_b128 v[64:67], v165 offset:13312\n\t"
      "v_mfma_f32_16x16x32_bf16 v[60:63], v[100:103], v[108:111], v[60:63]\n\t"
      "ds_read_b64_tr_b16 v[80:81], v171 offset:33792\n\t"
      "v_mfma_f32_16x16x32_bf16 v[44:47], v[92:95], v[108:111], v[44:47]\n\t"
      "ds_read_b64_tr_b16 v[82:83], v171 offset:34304\n\t"
      "s_waitcnt lgkmcnt(7)\n\t"
      "v_mfma_f32_16x16x32_bf16 v[48:51], v[124:127], v[132:135], v[48:51]\n\t"
      "v_mfma_f32_16x16x32_bf16 v[56:59], v[128:131], v[132:135], v[56:59]\n\t"
      "v_mfma_f32_16x16x32_bf16 v[40:43], v[120:123], v[132:135], v[40:43]\n\t"
      "v_mfma_f32_16x16x32_bf16 v[52:55], v[124:127], v[136:139], v[52:55]\n\t"
      "v_mfma_f32_16x16x32_bf16 v[60:63], v[128:131], v[136:139], v[60:63]\n\t"
      "v_mfma_f32_16x16x32_bf16 v[44:47], v[120:123], v[136:139], v[44:47]\n\t"
      "s_waitcnt lgkmcnt(0)\n\t"
      "v_mfma_f32_16x16x32_bf16 v[48:51], v[68:71], v[76:79], v[48:51]\n\t"
      "v_mfma_f32_16x16x32_bf16 v[56:59], v[72:75], v[76:79], v[56:59]\n\t"
      "v_mfma_f32_16x16x32_bf16 v[40:43], v[64:67], v[76:79], v[40:43]\n\t"
      "v_mfma_f32_16x16x32_bf16 v[52:55], v[68:71], v[80:83], v[52:55]\n\t"
      "v_mfma_f32_16x16x32_bf16 v[60:63], v[72:75], v[80:83], v[60:63]\n\t"
      "v_mfma_f32_16x16x32_bf16 v[44:47], v[64:67], v[80:83], v[44:47]\n\t"
      "s_nop 2\n\t"
      "v_cvt_pk_bf16_f32 v66, v48, v49\n\t"
      "v_cvt_pk_bf16_f32 v67, v50, v51\n\t"
      "v_cvt_pk_bf16_f32 v68, v56, v57\n\t"
      "v_cvt_pk_bf16_f32 v69, v58, v59\n\t"
      "v_cvt_pk_bf16_f32 v64, v40, v41\n\t"
      "v_cvt_pk_bf16_f32 v65, v42, v43\n\t"
      "v_mfma_f32_16x16x32_bf16 v[88:91], v[66:69], v[4:7], 0\n\t"
      "s_nop 0\n\t"
      "v_mfma_f32_16x16x32_bf16 v[80:83], v[64:67], v[0:3], 0\n\t"
      "v_cvt_pk_bf16_f32 v72, v52, v53\n\t"
      "v_cvt_pk_bf16_f32 v73, v54, v55\n\t"
      "v_cvt_pk_bf16_f32 v74, v60, v61\n\t"
      "v_cvt_pk_bf16_f32 v75, v62, v63\n\t"
      "v_cvt_pk_bf16_f32 v70, v44, v45\n\t"
      "v_cvt_pk_bf16_f32 v71, v46, v47\n\t"
      "v_mfma_f32_16x16x32_bf16 v[92:95], v[72:75], v[4:7], 0\n\t"
      "s_nop 0\n\t"
      "v_mfma_f32_16x16x32_bf16 v[84:87], v[70:73], v[0:3], 0\n\t"
      "v_cvt_pk_bf16_f32 v152, v88, v89\n\t"
      "v_cvt_pk_bf16_f32 v153, v90, v91\n\t"
      "v_min_u32_e32 v152, -2, v152\n\t"
      "v_min_u32_e32 v153, -2, v153\n\t"
      "v_cvt_pk_bf16_f32 v148, v80, v81\n\t"
      "v_cvt_pk_bf16_f32 v149, v82, v83\n\t"
      "v_min_u32_e32 v148, -2, v148\n\t"
      "v_min_u32_e32 v149, -2, v149\n\t"
      "v_cvt_pk_bf16_f32 v154, v92, v93\n\t"
      "v_cvt_pk_bf16_f32 v155, v94, v95\n\t"
      "v_cndmask_b32_e64 v154, v154, 0, s[68:69]\n\t"
      "v_min_u32_e32 v154, -2, v154\n\t"
      "v_cndmask_b32_e64 v155, v155, 0, s[68:69]\n\t"
      "v_min_u32_e32 v155, -2, v155\n\t"
      "global_store_dwordx4 v244, v[152:155], s[44:45] sc1\n\t"
      "v_cvt_pk_bf16_f32 v150, v84, v85\n\t"
      "v_cvt_pk_bf16_f32 v151, v86, v87\n\t"
      "v_cndmask_b32_e64 v150, v150, 0, s[68:69]\n\t"
      "v_min_u32_e32 v150, -2, v150\n\t"
      "v_cndmask_b32_e64 v151, v151, 0, s[68:69]\n\t"
      "v_min_u32_e32 v151, -2, v151\n\t"
      "global_store_dwordx4 v244, v[148:151], s[42:43] sc1\n\t"
      "s_barrier\n\t"
      "s_cmp_eq_u32 s26, 7\n\t"
      "s_cbranch_scc1 SPARE_%=\n\t"
      "s_lshl_b32 s34, s25, 16\n\t"
      "s_lshl2_add_u32 s34, s39, s34\n\t"
      "s_add_u32 s34, s34, 4096\n\t"
      "s_add_u32 s42, s20, s34\n\t"
      "s_addc_u32 s43, s21, 0\n\t"
      "s_add_u32 s44, s42, 8192\n\t"
      "s_addc_u32 s45, s43, 0\n\t"
      "s_lshl_b32 s72, s24, 10\n\t"
      "s_add_u32 s72, s72, 98304\n\t"
      "v_xor_b32_e32 v203, v197, v198\n\t"
      "v_lshl_add_u32 v203, v203, 4, s72\n\t"
      "v_lshl_add_u32 v203, v197, 7, v203\n\t"
      "ds_write_b128 v203, v[204:207]\n\t"
      "s_and_b32 s35, s25, 3\n\t"
      "s_lshl2_add_u32 s31, s26, s35\n\t"
      "s_and_b32 s35, s2, 7\n\t"
      "s_mul_i32 s34, s35, 14\n\t"
      "s_lshl4_add_u32 s35, s35, -14\n\t"
      "s_cmp_lt_u32 s31, 14\n\t"
      "s_cselect_b32 s34, s34, s35\n\t"
      "s_cselect_b32 s36, 1, 0\n\t"
      "s_or_b32 s27, s27, s36\n\t"
      "s_add_u32 s34, s34, s31\n\t"
      "s_lshr_b32 s29, s34, 2\n\t"
      "s_and_b32 s32, s34, 3\n\t"
      "s_mul_i32 s30, s32, 7\n\t"
      "v_and_b32_e32 v199, 60, v192\n\t"
      "v_and_b32_e32 v200, 24, v192\n\t"
      "v_lshlrev_b32_e32 v202, 3, v196\n\t"
      "v_lshl_or_b32 v202, v200, 2, v202\n\t"
      "v_lshl_or_b32 v202, v195, 7, v202\n\t"
      "v_lshl_or_b32 v202, v193, 10, v202\n\t"
      "v_add_u32_e32 v224, s39, v202\n\t"
      "v_xor_b32_e32 v225, 32, v224\n\t"
      "v_xor_b32_e32 v226, 64, v224\n\t"
      "v_xor_b32_e32 v227, 96, v224\n\t"
      "s_lshl_b32 s34, s24, 12\n\t"
      "s_add_u32 s34, s34, 98304\n\t"
      "v_add_u32_e32 v245, s34, v244\n\t"
      "v_lshlrev_b32_e32 v199, 2, v199\n\t"
      "s_lshl_b32 s34, s24, 8\n\t"
      "v_lshl_add_u32 v247, v192, 2, s34\n\t"
      "v_and_b32_e32 v200, 2, v192\n\t"
      "v_mul_u32_u24_e32 v202, 576, v200\n\t"
      "v_xor_b32_e32 v199, v202, v199\n\t"
      "v_and_b32_e32 v202, 1, v192\n\t"
      "v_lshl_or_b32 v199, v202, 3, v199\n\t"
      "s_and_b32 s35, s34, 0x400\n\t"
      "s_add_u32 s34, s34, s35\n\t"
      "s_add_u32 s34, s34, 98304\n\t"
      "v_add_u32_e32 v246, s34, v199\n\t"
      "v_lshlrev_b32_e32 v199, 2, v202\n\t"
      "v_lshl_or_b32 v199, v200, 4, v199\n\t"
      "s_min_u32 s34, s29, 27\n\t"
      "s_lshl_b32 s34, s34, 7\n\t"
      "s_lshr_b32 s35, s24, 2\n\t"
      "s_lshl_b32 s35, s35, 6\n\t"
      "s_add_u32 s34, s34, s35\n\t"
      "s_lshr_b32 s35, s39, 9\n\t"
      "s_add_u32 s34, s34, s35\n\t"
      "v_add_u32_e32 v248, s34, v199\n\t"
      "s_add_u32 s48, s20, 8388608\n\t"
      "s_addc_u32 s49, s21, 0\n\t"
      "v_and_b32_e32 v199, 16, v192\n\t"
      "v_lshlrev_b32_e32 v199, 1, v199\n\t"
      "v_and_b32_e32 v200, 32, v192\n\t"
      "s_lshl_b32 s34, s28, 18\n\t"
      "s_lshl_b32 s35, s25, 11\n\t"
      "s_add_u32 s34, s34, s35\n\t"
      "s_lshr_b32 s35, s39, 3\n\t"
      "s_add_u32 s34, s34, s35\n\t"
      "v_and_b32_e32 v201, 0x1f0, v244\n\t"
      "v_lshl_or_b32 v201, v200, 11, v201\n\t"
      "s_waitcnt lgkmcnt(0)\n\t"
      "s_barrier\n\t"
      "v_xor_b32_e32 v210, 32, v228\n\t"
      "v_xor_b32_e32 v212, 64, v228\n\t"
      "v_xor_b32_e32 v214, 96, v228\n\t"
      "ds_read_b64 v[208:209], v228\n\t"
      "ds_read_b64 v[210:211], v210\n\t"
      "ds_read_b64 v[212:213], v212\n\t"
      "ds_read_b64 v[214:215], v214\n\t"
      "ds_read_b64_tr_b16 v[0:1], v216 offset:7168\n\t"
      "ds_read_b64_tr_b16 v[2:3], v220 offset:7168\n\t"
      "ds_read_b64_tr_b16 v[4:5], v217 offset:7168\n\t"
      "ds_read_b64_tr_b16 v[6:7], v221 offset:7168\n\t"
      "ds_read_b64_tr_b16 v[8:9], v218 offset:7168\n\t"
      "ds_read_b64_tr_b16 v[10:11], v222 offset:7168\n\t"
      "ds_read_b64_tr_b16 v[12:13], v219 offset:7168\n\t"
      "ds_read_b64_tr_b16 v[14:15], v223 offset:7168\n\t"
      "ds_read_b64_tr_b16 v[16:17], v218 offset:10752\n\t"
      "ds_read_b64_tr_b16 v[18:19], v222 offset:10752\n\t"
      "ds_read_b64_tr_b16 v[20:21], v219 offset:10752\n\t"
      "ds_read_b64_tr_b16 v[22:23], v223 offset:10752\n\t"
      "ds_read_b64_tr_b16 v[24:25], v216 offset:10752\n\t"
      "ds_read_b64_tr_b16 v[26:27], v220 offset:10752\n\t"
      "ds_read_b64_tr_b16 v[28:29], v217 offset:10752\n\t"
      "ds_read_b64_tr_b16 v[30:31], v221 offset:10752\n\t"
      "ds_read_b64_tr_b16 v[32:33], v216 offset:14336\n\t"
      "ds_read_b64_tr_b16 v[34:35], v220 offset:14336\n\t"
      "ds_read_b64_tr_b16 v[36:37], v217 offset:14336\n\t"
      "ds_read_b64_tr_b16 v[38:39], v221 offset:14336\n\t"
      "ds_read_b64_tr_b16 v[40:41], v218 offset:14336\n\t"
      "ds_read_b64_tr_b16 v[42:43], v222 offset:14336\n\t"
      "ds_read_b64_tr_b16 v[44:45], v219 offset:14336\n\t"
      "ds_read_b64_tr_b16 v[46:47], v223 offset:14336\n\t"
      "ds_read_b64_tr_b16 v[48:49], v218 offset:17920\n\t"
      "ds_read_b64_tr_b16 v[50:51], v222 offset:17920\n\t"
      "ds_read_b64_tr_b16 v[52:53], v219 offset:17920\n\t"
      "ds_read_b64_tr_b16 v[54:55], v223 offset:17920\n\t"
      "ds_read_b64_tr_b16 v[56:57], v216 offset:17920\n\t"
      "ds_read_b64_tr_b16 v[58:59], v220 offset:17920\n\t"
      "ds_read_b64_tr_b16 v[60:61], v217 offset:17920\n\t"
      "ds_read_b64_tr_b16 v[62:63], v221 offset:17920\n\t"
      "ds_read_b64_tr_b16 v[64:65], v216 offset:21504\n\t"
      "ds_read_b64_tr_b16 v[66:67], v220 offset:21504\n\t"
      "ds_read_b64_tr_b16 v[68:69], v217 offset:21504\n\t"
      "ds_read_b64_tr_b16 v[70:71], v221 offset:21504\n\t"
      "ds_read_b64_tr_b16 v[72:73], v218 offset:21504\n\t"
      "ds_read_b64_tr_b16 v[74:75], v222 offset:21504\n\t"
      "ds_read_b64_tr_b16 v[76:77], v219 offset:21504\n\t"
      "ds_read_b64_tr_b16 v[78:79], v223 offset:21504\n\t"
      "ds_read_b64_tr_b16 v[80:81], v218 offset:25088\n\t"
      "ds_read_b64_tr_b16 v[82:83], v222 offset:25088\n\t"
      "ds_read_b64_tr_b16 v[84:85], v219 offset:25088\n\t"
      "ds_read_b64_tr_b16 v[86:87], v223 offset:25088\n\t"
      "ds_read_b64_tr_b16 v[88:89], v216 offset:25088\n\t"
      "ds_read_b64_tr_b16 v[90:91], v220 offset:25088\n\t"
      "ds_read_b64_tr_b16 v[92:93], v217 offset:25088\n\t"
      "ds_read_b64_tr_b16 v[94:95], v221 offset:25088\n\t"
      "ds_read_b64_tr_b16 v[96:97], v216 offset:28672\n\t"
      "ds_read_b64_tr_b16 v[98:99], v220 offset:28672\n\t"
      "ds_read_b64_tr_b16 v[100:101], v217 offset:28672\n\t"
      "ds_read_b64_tr_b16 v[102:103], v221 offset:28672\n\t"
      "ds_read_b64_tr_b16 v[104:105], v218 offset:28672\n\t"
      "ds_read_b64_tr_b16 v[106:107], v222 offset:28672\n\t"
      "ds_read_b64_tr_b16 v[108:109], v219 offset:28672\n\t"
      "ds_read_b64_tr_b16 v[110:111], v223 offset:28672\n\t"
      "ds_read_b64_tr_b16 v[112:113], v218 offset:32256\n\t"
      "ds_read_b64_tr_b16 v[114:115], v222 offset:32256\n\t"
      "ds_read_b64_tr_b16 v[116:117], v219 offset:32256\n\t"
      "ds_read_b64_tr_b16 v[118:119], v223 offset:32256\n\t"
      "ds_read_b64_tr_b16 v[120:121], v216 offset:32256\n\t"
      "ds_read_b64_tr_b16 v[122:123], v220 offset:32256\n\t"
      "ds_read_b64_tr_b16 v[124:125], v217 offset:32256\n\t"
      "ds_read_b64_tr_b16 v[126:127], v221 offset:32256\n\t"
      "s_sleep 2\n\t"
      "global_load_dwordx4 v[128:131], v244, s[42:43] offset:-2048 sc1\n\t"
      "global_load_dwordx4 v[132:135], v244, s[42:43] offset:-1024 sc1\n\t"
      "global_load_dwordx4 v[136:139], v244, s[42:43] sc1\n\t"
      "global_load_dwordx4 v[140:143], v244, s[42:43] offset:1024 sc1\n\t"
      "global_load_dwordx4 v[144:147], v244, s[42:43] offset:2048 sc1\n\t"
      "global_load_dwordx4 v[148:151], v244, s[42:43] offset:3072 sc1\n\t"
      "global_load_dwordx4 v[152:155], v244, s[44:45] offset:-4096 sc1\n\t"
      "global_load_dwordx4 v[156:159], v244, s[44:45] offset:-3072 sc1\n\t"
      "global_load_dwordx4 v[160:163], v244, s[44:45] offset:-2048 sc1\n\t"
      "global_load_dwordx4 v[164:167], v244, s[44:45] offset:-1024 sc1\n\t"
      "v_add_u32_e32 v229, s34, v201\n\t"
      "v_add_u32_e32 v230, 131072, v229\n\t"
      "s_and_b32 s34, s25, 30\n\t"
      "s_lshr_b32 s35, s39, 13\n\t"
      "s_add_u32 s34, s34, s35\n\t"
      "s_mul_i32 s34, s34, 7\n\t"
      "s_add_u32 s34, s34, s28\n\t"
      "s_lshl_b32 s34, s34, 3\n\t"
      "s_and_b32 s35, s25, 1\n\t"
      "s_add_u32 s34, s34, s35\n\t"
      "s_lshl_b32 s34, s34, 10\n\t"
      "s_and_b32 s35, s24, 1\n\t"
      "s_lshl_b32 s35, s35, 9\n\t"
      "s_add_u32 s34, s34, s35\n\t"
      "s_add_u32 s34, s34, 6293504\n\t"
      "v_lshrrev_b32_e32 v201, 1, v200\n\t"
      "v_lshl_or_b32 v201, v199, 6, v201\n\t"
      "v_lshl_or_b32 v201, v194, 5, v201\n\t"
      "v_add_u32_e32 v241, s34, v201\n\t"
      "s_lshl_b32 s34, s24, 12\n\t"
      "s_add_u32 s34, s34, 131072\n\t"
      "v_xor_b32_e32 v201, v193, v194\n\t"
      "v_lshlrev_b32_e32 v201, 3, v201\n\t"
      "v_lshl_or_b32 v201, v194, 7, v201\n\t"
      "v_add_u32_e32 v233, s34, v201\n\t"
      "v_lshrrev_b32_e32 v199, 2, v199\n\t"
      "v_or_b32_e32 v199, v199, v195\n\t"
      "v_lshrrev_b32_e32 v200, 3, v200\n\t"
      "v_or_b32_e32 v200, v196, v200\n\t"
      "v_xor_b32_e32 v200, v200, v199\n\t"
      "v_lshlrev_b32_e32 v200, 3, v200\n\t"
      "v_lshl_or_b32 v200, v199, 7, v200\n\t"
      "v_add_u32_e32 v237, s34, v200\n\t"
      "v_xor_b32_e32 v234, 32, v233\n\t"
      "v_xor_b32_e32 v238, 32, v237\n\t"
      "v_xor_b32_e32 v235, 64, v233\n\t"
      "v_xor_b32_e32 v239, 64, v237\n\t"
      "v_xor_b32_e32 v236, 96, v233\n\t"
      "v_xor_b32_e32 v240, 96, v237\n\t"
      "s_waitcnt lgkmcnt(14)\n\t"
      "v_lshlrev_b32_e32 v192, 16, v208\n\t"
      "v_and_b32_e32 v193, 0xffff0000, v208\n\t"
      "v_lshlrev_b32_e32 v194, 16, v209\n\t"
      "v_and_b32_e32 v195, 0xffff0000, v209\n\t"
      "v_lshlrev_b32_e32 v196, 16, v210\n\t"
      "v_and_b32_e32 v197, 0xffff0000, v210\n\t"
      "v_lshlrev_b32_e32 v198, 16, v211\n\t"
      "v_and_b32_e32 v199, 0xffff0000, v211\n\t"
      "v_lshlrev_b32_e32 v200, 16, v212\n\t"
      "v_and_b32_e32 v201, 0xffff0000, v212\n\t"
      "v_lshlrev_b32_e32 v202, 16, v213\n\t"
      "v_and_b32_e32 v203, 0xffff0000, v213\n\t"
      "v_lshlrev_b32_e32 v204, 16, v214\n\t"
      "v_and_b32_e32 v205, 0xffff0000, v214\n\t"
      "v_lshlrev_b32_e32 v206, 16, v215\n\t"
      "v_and_b32_e32 v207, 0xffff0000, v215\n\t"
      "global_load_dwordx4 v[168:171], v244, s[44:45] sc1\n\t"
      "global_load_dwordx4 v[172:175], v244, s[44:45] offset:1024 sc1\n\t"
      "global_load_dwordx4 v[176:179], v244, s[44:45] offset:2048 sc1\n\t"
      "global_load_dwordx4 v[180:183], v244, s[44:45] offset:3072 sc1\n\t"
      "global_load_dwordx4 v[184:187], v244, s[42:43] offset:-4096 sc1\n\t"
      "global_load_dwordx4 v[188:191], v244, s[42:43] offset:-3072 sc1\n\t"
      "global_load_dword v249, v248, s[12:13]\n\t"
      "s_barrier\n\t"
      "s_waitcnt lgkmcnt(0)\n\t"
      "LGM_%=:\n\t"
      "s_waitcnt vmcnt(15)\n\t"
      "v_max_u32_e32 v232, v128, v132\n\t"
      "v_cmp_eq_u32_e32 vcc, -1, v232\n\t"
      "s_cbranch_vccnz LO0R_%=\n\t"
      "LO0B_%=:\n\t"
      "v_mfma_f32_16x16x32_bf16 v[192:195], v[0:3], v[128:131], v[192:195]\n\t"
      "ds_read_b64_tr_b16 v[0:1], v216 offset:35840\n\t"
      "ds_read_b64_tr_b16 v[2:3], v220 offset:35840\n\t"
      "v_mfma_f32_16x16x32_bf16 v[196:199], v[4:7], v[128:131], v[196:199]\n\t"
      "ds_read_b64_tr_b16 v[4:5], v217 offset:35840\n\t"
      "v_mfma_f32_16x16x32_bf16 v[200:203], v[8:11], v[128:131], v[200:203]\n\t"
      "ds_read_b64_tr_b16 v[6:7], v221 offset:35840\n\t"
      "ds_read_b64_tr_b16 v[8:9], v218 offset:35840\n\t"
      "v_mfma_f32_16x16x32_bf16 v[204:207], v[12:15], v[128:131], v[204:207]\n\t"
      "ds_read_b64_tr_b16 v[10:11], v222 offset:35840\n\t"
      "v_mfma_f32_16x16x32_bf16 v[192:195], v[16:19], v[132:135], v[192:195]\n\t"
      "ds_read_b64_tr_b16 v[12:13], v219 offset:35840\n\t"
      "ds_read_b64_tr_b16 v[14:15], v223 offset:35840\n\t"
      "v_mfma_f32_16x16x32_bf16 v[196:199], v[20:23], v[132:135], v[196:199]\n\t"
      "ds_read_b64_tr_b16 v[16:17], v218 offset:39424\n\t"
      "v_mfma_f32_16x16x32_bf16 v[200:203], v[24:27], v[132:135], v[200:203]\n\t"
      "ds_read_b64_tr_b16 v[18:19], v222 offset:39424\n\t"
      "ds_read_b64_tr_b16 v[20:21], v219 offset:39424\n\t"
      "v_mfma_f32_16x16x32_bf16 v[204:207], v[28:31], v[132:135], v[204:207]\n\t"
      "ds_read_b64_tr_b16 v[22:23], v223 offset:39424\n\t"
      "s_waitcnt vmcnt(13)\n\t"
      "v_max_u32_e32 v232, v136, v140\n\t"
      "v_cmp_eq_u32_e32 vcc, -1, v232\n\t"
      "s_cbranch_vccnz LO2R_%=\n\t"
      "LO2B_%=:\n\t"
      "v_mfma_f32_16x16x32_bf16 v[192:195], v[32:35], v[136:139], v[192:195]\n\t"
      "ds_read_b64_tr_b16 v[24:25], v216 offset:39424\n\t"
      "ds_read_b64_tr_b16 v[26:27], v220 offset:39424\n\t"
      "v_mfma_f32_16x16x32_bf16 v[196:199], v[36:39], v[136:139], v[196:199]\n\t"
      "ds_read_b64_tr_b16 v[28:29], v217 offset:39424\n\t"
      "v_mfma_f32_16x16x32_bf16 v[200:203], v[40:43], v[136:139], v[200:203]\n\t"
      "ds_read_b64_tr_b16 v[30:31], v221 offset:39424\n\t"
      "ds_read_b64_tr_b16 v[32:33], v216 offset:43008\n\t"
      "v_mfma_f32_16x16x32_bf16 v[204:207], v[44:47], v[136:139], v[204:207]\n\t"
      "ds_read_b64_tr_b16 v[34:35], v220 offset:43008\n\t"
      "v_mfma_f32_16x16x32_bf16 v[192:195], v[48:51], v[140:143], v[192:195]\n\t"
      "ds_read_b64_tr_b16 v[36:37], v217 offset:43008\n\t"
      "ds_read_b64_tr_b16 v[38:39], v221 offset:43008\n\t"
      "v_mfma_f32_16x16x32_bf16 v[196:199], v[52:55], v[140:143], v[196:199]\n\t"
      "ds_read_b64_tr_b16 v[40:41], v218 offset:43008\n\t"
      "v_mfma_f32_16x16x32_bf16 v[200:203], v[56:59], v[140:143], v[200:203]\n\t"
      "ds_read_b64_tr_b16 v[42:43], v222 offset:43008\n\t"
      "ds_read_b64_tr_b16 v[44:45], v219 offset:43008\n\t"
      "v_mfma_f32_16x16x32_bf16 v[204:207], v[60:63], v[140:143], v[204:207]\n\t"
      "ds_read_b64_tr_b16 v[46:47], v223 offset:43008\n\t"
      "s_waitcnt vmcnt(11)\n\t"
      "v_max_u32_e32 v232, v144, v148\n\t"
      "v_cmp_eq_u32_e32 vcc, -1, v232\n\t"
      "s_cbranch_vccnz LO4R_%=\n\t"
      "LO4B_%=:\n\t"
      "v_mfma_f32_16x16x32_bf16 v[192:195], v[64:67], v[144:147], v[192:195]\n\t"
      "ds_read_b64_tr_b16 v[48:49], v218 offset:46592\n\t"
      "ds_read_b64_tr_b16 v[50:51], v222 offset:46592\n\t"
      "v_mfma_f32_16x16x32_bf16 v[196:199], v[68:71], v[144:147], v[196:199]\n\t"
      "ds_read_b64_tr_b16 v[52:53], v219 offset:46592\n\t"
      "v_mfma_f32_16x16x32_bf16 v[200:203], v[72:75], v[144:147], v[200:203]\n\t"
      "ds_read_b64_tr_b16 v[54:55], v223 offset:46592\n\t"
      "ds_read_b64_tr_b16 v[56:57], v216 offset:46592\n\t"
      "v_mfma_f32_16x16x32_bf16 v[204:207], v[76:79], v[144:147], v[204:207]\n\t"
      "ds_read_b64_tr_b16 v[58:59], v220 offset:46592\n\t"
      "v_mfma_f32_16x16x32_bf16 v[192:195], v[80:83], v[148:151], v[192:195]\n\t"
      "ds_read_b64_tr_b16 v[60:61], v217 offset:46592\n\t"
      "ds_read_b64_tr_b16 v[62:63], v221 offset:46592\n\t"
      "v_mfma_f32_16x16x32_bf16 v[196:199], v[84:87], v[148:151], v[196:199]\n\t"
      "ds_read_b64_tr_b16 v[64:65], v216 offset:50176\n\t"
      "v_mfma_f32_16x16x32_bf16 v[200:203], v[88:91], v[148:151], v[200:203]\n\t"
      "ds_read_b64_tr_b16 v[66:67], v220 offset:50176\n\t"
      "ds_read_b64_tr_b16 v[68:69], v217 offset:50176\n\t"
      "v_mfma_f32_16x16x32_bf16 v[204:207], v[92:95], v[148:151], v[204:207]\n\t"
      "ds_read_b64_tr_b16 v[70:71], v221 offset:50176\n\t"
      "s_waitcnt vmcnt(9)\n\t"
      "v_max_u32_e32 v232, v152, v156\n\t"
      "v_cmp_eq_u32_e32 vcc, -1, v232\n\t"
      "s_cbranch_vccnz LO6R_%=\n\t"
      "LO6B_%=:\n\t"
      "v_mfma_f32_16x16x32_bf16 v[192:195], v[96:99], v[152:155], v[192:195]\n\t"
      "ds_read_b64_tr_b16 v[72:73], v218 offset:50176\n\t"
      "ds_read_b64_tr_b16 v[74:75], v222 offset:50176\n\t"
      "v_mfma_f32_16x16x32_bf16 v[196:199], v[100:103], v[152:155], v[196:199]\n\t"
      "ds_read_b64_tr_b16 v[76:77], v219 offset:50176\n\t"
      "v_mfma_f32_16x16x32_bf16 v[200:203], v[104:107], v[152:155], v[200:203]\n\t"
      "ds_read_b64_tr_b16 v[78:79], v223 offset:50176\n\t"
      "ds_read_b64_tr_b16 v[80:81], v218 offset:53760\n\t"
      "v_mfma_f32_16x16x32_bf16 v[204:207], v[108:111], v[152:155], v[204:207]\n\t"
      "ds_read_b64_tr_b16 v[82:83], v222 offset:53760\n\t"
      "v_mfma_f32_16x16x32_bf16 v[192:195], v[112:115], v[156:159], v[192:195]\n\t"
      "ds_read_b64_tr_b16 v[84:85], v219 offset:53760\n\t"
      "ds_read_b64_tr_b16 v[86:87], v223 offset:53760\n\t"
      "v_mfma_f32_16x16x32_bf16 v[196:199], v[116:119], v[156:159], v[196:199]\n\t"
      "ds_read_b64_tr_b16 v[88:89], v216 offset:53760\n\t"
      "v_mfma_f32_16x16x32_bf16 v[200:203], v[120:123], v[156:159], v[200:203]\n\t"
      "ds_read_b64_tr_b16 v[90:91], v220 offset:53760\n\t"
      "ds_read_b64_tr_b16 v[92:93], v217 offset:53760\n\t"
      "v_mfma_f32_16x16x32_bf16 v[204:207], v[124:127], v[156:159], v[204:207]\n\t"
      "ds_read_b64_tr_b16 v[94:95], v221 offset:53760\n\t"
      "s_waitcnt vmcnt(7)\n\t"
      "v_max_u32_e32 v232, v160, v164\n\t"
      "v_cmp_eq_u32_e32 vcc, -1, v232\n\t"
      "s_cbranch_vccnz LO8R_%=\n\t"
      "LO8B_%=:\n\t"
      "s_waitcnt lgkmcnt(14)\n\t"
      "v_mfma_f32_16x16x32_bf16 v[192:195], v[0:3], v[160:163], v[192:195]\n\t"
      "ds_read_b64_tr_b16 v[96:97], v216\n\t"
      "ds_read_b64_tr_b16 v[98:99], v220\n\t"
      "v_mfma_f32_16x16x32_bf16 v[196:199], v[4:7], v[160:163], v[196:199]\n\t"
      "ds_read_b64_tr_b16 v[100:101], v217\n\t"
      "v_mfma_f32_16x16x32_bf16 v[200:203], v[8:11], v[160:163], v[200:203]\n\t"
      "ds_read_b64_tr_b16 v[102:103], v221\n\t"
      "ds_read_b64_tr_b16 v[104:105], v218\n\t"
      "v_mfma_f32_16x16x32_bf16 v[204:207], v[12:15], v[160:163], v[204:207]\n\t"
      "ds_read_b64_tr_b16 v[106:107], v222\n\t"
      "v_mfma_f32_16x16x32_bf16 v[192:195], v[16:19], v[164:167], v[192:195]\n\t"
      "ds_read_b64_tr_b16 v[108:109], v219\n\t"
      "ds_read_b64_tr_b16 v[110:111], v223\n\t"
      "v_mfma_f32_16x16x32_bf16 v[196:199], v[20:23], v[164:167], v[196:199]\n\t"
      "ds_read_b64_tr_b16 v[112:113], v218 offset:3584\n\t"
      "v_mfma_f32_16x16x32_bf16 v[200:203], v[24:27], v[164:167], v[200:203]\n\t"
      "ds_read_b64_tr_b16 v[114:115], v222 offset:3584\n\t"
      "ds_read_b64_tr_b16 v[116:117], v219 offset:3584\n\t"
      "v_mfma_f32_16x16x32_bf16 v[204:207], v[28:31], v[164:167], v[204:207]\n\t"
      "ds_read_b64_tr_b16 v[118:119], v223 offset:3584\n\t"
      "s_waitcnt vmcnt(5)\n\t"
      "v_max_u32_e32 v232, v168, v172\n\t"
      "v_cmp_eq_u32_e32 vcc, -1, v232\n\t"
      "s_cbranch_vccnz LO10R_%=\n\t"
      "LO10B_%=:\n\t"
      "v_mfma_f32_16x16x32_bf16 v[192:195], v[32:35], v[168:171], v[192:195]\n\t"
      "ds_read_b64_tr_b16 v[120:121], v216 offset:3584\n\t"
      "ds_read_b64_tr_b16 v[122:123], v220 offset:3584\n\t"
      "v_mfma_f32_16x16x32_bf16 v[196:199], v[36:39], v[168:171], v[196:199]\n\t"
      "ds_read_b64_tr_b16 v[124:125], v217 offset:3584\n\t"
      "v_mfma_f32_16x16x32_bf16 v[200:203], v[40:43], v[168:171], v[200:203]\n\t"
      "ds_read_b64_tr_b16 v[126:127], v221 offset:3584\n\t"
      "v_mfma_f32_16x16x32_bf16 v[204:207], v[44:47], v[168:171], v[204:207]\n\t"
      "v_mfma_f32_16x16x32_bf16 v[192:195], v[48:51], v[172:175], v[192:195]\n\t"
      "v_mfma_f32_16x16x32_bf16 v[196:199], v[52:55], v[172:175], v[196:199]\n\t"
      "v_mfma_f32_16x16x32_bf16 v[200:203], v[56:59], v[172:175], v[200:203]\n\t"
      "v_mfma_f32_16x16x32_bf16 v[204:207], v[60:63], v[172:175], v[204:207]\n\t"
      "s_waitcnt vmcnt(3)\n\t"
      "v_max_u32_e32 v232, v176, v180\n\t"
      "v_cmp_eq_u32_e32 vcc, -1, v232\n\t"
      "s_cbranch_vccnz LO12R_%=\n\t"
      "LO12B_%=:\n\t"
      "s_waitcnt lgkmcnt(14)\n\t"
      "v_mfma_f32_16x16x32_bf16 v[192:195], v[64:67], v[176:179], v[192:195]\n\t"
      "v_mfma_f32_16x16x32_bf16 v[196:199], v[68:71], v[176:179], v[196:199]\n\t"
      "v_mfma_f32_16x16x32_bf16 v[200:203], v[72:75], v[176:179], v[200:203]\n\t"
      "v_mfma_f32_16x16x32_bf16 v[204:207], v[76:79], v[176:179], v[204:207]\n\t"
      "v_mfma_f32_16x16x32_bf16 v[192:195], v[80:83], v[180:183], v[192:195]\n\t"
      "v_mfma_f32_16x16x32_bf16 v[196:199], v[84:87], v[180:183], v[196:199]\n\t"
      "v_mfma_f32_16x16x32_bf16 v[200:203], v[88:91], v[180:183], v[200:203]\n\t"
      "v_mfma_f32_16x16x32_bf16 v[204:207], v[92:95], v[180:183], v[204:207]\n\t"
      "s_waitcnt vmcnt(1)\n\t"
      "v_max_u32_e32 v232, v184, v188\n\t"
      "v_cmp_eq_u32_e32 vcc, -1, v232\n\t"
      "s_cbranch_vccnz LO14R_%=\n\t"
      "LO14B_%=:\n\t"
      "s_waitcnt lgkmcnt(8)\n\t"
      "v_mfma_f32_16x16x32_bf16 v[192:195], v[96:99], v[184:187], v[192:195]\n\t"
      "v_mfma_f32_16x16x32_bf16 v[196:199], v[100:103], v[184:187], v[196:199]\n\t"
      "v_mfma_f32_16x16x32_bf16 v[200:203], v[104:107], v[184:187], v[200:203]\n\t"
      "v_mfma_f32_16x16x32_bf16 v[204:207], v[108:111], v[184:187], v[204:207]\n\t"
      "s_waitcnt lgkmcnt(0)\n\t"
      "v_mfma_f32_16x16x32_bf16 v[192:195], v[112:115], v[188:191], v[192:195]\n\t"
      "v_mfma_f32_16x16x32_bf16 v[196:199], v[116:119], v[188:191], v[196:199]\n\t"
      "v_mfma_f32_16x16x32_bf16 v[200:203], v[120:123], v[188:191], v[200:203]\n\t"
      "v_mfma_f32_16x16x32_bf16 v[204:207], v[124:127], v[188:191], v[204:207]\n\t"
      "s_nop 4\n\t"
      "v_cmp_u_f32_e32 vcc, v192, v192\n\t"
      "s_cbranch_vccnz LGX_%=\n\t"
      "LGB_%=:\n\t"
      "v_mov_b32_e32 v148, 0x3f803f80\n\t"
      "v_cvt_pk_bf16_f32 v208, v192, v193\n\t"
      "v_cvt_pk_bf16_f32 v209, v194, v195\n\t"
      "v_cvt_pk_bf16_f32 v210, v196, v197\n\t"
      "v_cvt_pk_bf16_f32 v211, v198, v199\n\t"
      "v_cvt_pk_bf16_f32 v212, v200, v201\n\t"
      "v_cvt_pk_bf16_f32 v213, v202, v203\n\t"
      "v_cvt_pk_bf16_f32 v214, v204, v205\n\t"
      "v_cvt_pk_bf16_f32 v215, v206, v207\n\t"
      "v_dot2_f32_bf16 v144, v208, v148, 0\n\t"
      "v_dot2_f32_bf16 v145, v208, v208, 0\n\t"
      "v_dot2_f32_bf16 v146, v209, v148, 0\n\t"
      "v_dot2_f32_bf16 v147, v209, v209, 0\n\t"
      "v_dot2_f32_bf16 v144, v210, v148, v144\n\t"
      "v_dot2_f32_bf16 v145, v210, v210, v145\n\t"
      "v_dot2_f32_bf16 v146, v211, v148, v146\n\t"
      "v_dot2_f32_bf16 v147, v211, v211, v147\n\t"
      "v_dot2_f32_bf16 v144, v212, v148, v144\n\t"
      "v_dot2_f32_bf16 v145, v212, v212, v145\n\t"
      "v_dot2_f32_bf16 v146, v213, v148, v146\n\t"
      "v_dot2_f32_bf16 v147, v213, v213, v147\n\t"
      "v_dot2_f32_bf16 v144, v214, v148, v144\n\t"
      "v_dot2_f32_bf16 v145, v214, v214, v145\n\t"
      "v_dot2_f32_bf16 v146, v215, v148, v146\n\t"
      "v_dot2_f32_bf16 v147, v215, v215, v147\n\t"
      "v_lshlrev_b32_e32 v128, 16, v208\n\t"
      "v_and_b32_e32 v129, 0xffff0000, v208\n\t"
      "v_lshlrev_b32_e32 v130, 16, v209\n\t"
      "v_pk_add_f32 v[144:145], v[144:145], v[146:147]\n\t"
      "v_and_b32_e32 v131, 0xffff0000, v209\n\t"
      "v_lshlrev_b32_e32 v132, 16, v210\n\t"
      "v_permlane16_swap_b32_e32 v144, v145\n\t"
      "v_add_f32_e32 v144, v144, v145\n\t"
      "v_mov_b32_e32 v145, v144\n\t"
      "v_and_b32_e32 v133, 0xffff0000, v210\n\t"
      "v_lshlrev_b32_e32 v134, 16, v211\n\t"
      "v_permlane32_swap_b32_e32 v144, v145\n\t"
      "v_add_f32_e32 v144, v144, v145\n\t"
      "v_mov_b32_e32 v145, v144\n\t"
      "v_and_b32_e32 v135, 0xffff0000, v211\n\t"
      "v_lshlrev_b32_e32 v136, 16, v212\n\t"
      "v_permlane16_swap_b32_e32 v144, v145\n\t"
      "s_mov_b64 exec, s[70:71]\n\t"
      "v_min_u32_e32 v144, -2, v144\n\t"
      "v_min_u32_e32 v145, -2, v145\n\t"
      "global_store_dwordx2 v231, v[144:145], s[58:59] sc1\n\t"
      "s_mov_b64 exec, -1\n\t"
      "s_barrier\n\t"
      "v_and_b32_e32 v137, 0xffff0000, v212\n\t"
      "v_lshlrev_b32_e32 v138, 16, v213\n\t"
      "v_and_b32_e32 v139, 0xffff0000, v213\n\t"
      "v_lshlrev_b32_e32 v140, 16, v214\n\t"
      "v_and_b32_e32 v141, 0xffff0000, v214\n\t"
      "v_lshlrev_b32_e32 v142, 16, v215\n\t"
      "v_and_b32_e32 v143, 0xffff0000, v215\n\t"
      "v_permlane16_swap_b32_e32 v208, v210\n\t"
      "v_permlane16_swap_b32_e32 v209, v211\n\t"
      "v_permlane16_swap_b32_e32 v212, v214\n\t"
      "v_permlane16_swap_b32_e32 v213, v215\n\t"
      "s_bitcmp0_b32 s27, 0\n\t"
      "s_cbranch_scc1 NTK0_%=\n\t"
      "ds_read_b64_tr_b16 v[0:1], v224 offset:16384\n\t"
      "ds_read_b64_tr_b16 v[2:3], v224 offset:16896\n\t"
      "ds_read_b64_tr_b16 v[4:5], v225 offset:16384\n\t"
      "ds_read_b64_tr_b16 v[6:7], v225 offset:16896\n\t"
      "ds_read_b64_tr_b16 v[8:9], v226 offset:16384\n\t"
      "ds_read_b64_tr_b16 v[10:11], v226 offset:16896\n\t"
      "ds_read_b64_tr_b16 v[12:13], v227 offset:16384\n\t"
      "ds_read_b64_tr_b16 v[14:15], v227 offset:16896\n\t"
      "ds_read_b64_tr_b16 v[16:17], v224 offset:49152\n\t"
      "ds_read_b64_tr_b16 v[18:19], v224 offset:49664\n\t"
      "ds_read_b64_tr_b16 v[20:21], v225 offset:49152\n\t"
      "ds_read_b64_tr_b16 v[22:23], v225 offset:49664\n\t"
      "ds_read_b64_tr_b16 v[24:25], v226 offset:49152\n\t"
      "ds_read_b64_tr_b16 v[26:27], v226 offset:49664\n\t"
      "ds_read_b64_tr_b16 v[28:29], v227 offset:49152\n\t"
      "ds_read_b64_tr_b16 v[30:31], v227 offset:49664\n\t"
      "ds_read_b64_tr_b16 v[32:33], v224\n\t"
      "ds_read_b64_tr_b16 v[34:35], v224 offset:512\n\t"
      "ds_read_b64_tr_b16 v[36:37], v225\n\t"
      "ds_read_b64_tr_b16 v[38:39], v225 offset:512\n\t"
      "ds_read_b64_tr_b16 v[40:41], v226\n\t"
      "ds_read_b64_tr_b16 v[42:43], v226 offset:512\n\t"
      "ds_read_b64_tr_b16 v[44:45], v227\n\t"
      "ds_read_b64_tr_b16 v[46:47], v227 offset:512\n\t"
      "ds_read_b64_tr_b16 v[48:49], v224 offset:32768\n\t"
      "ds_read_b64_tr_b16 v[50:51], v224 offset:33280\n\t"
      "ds_read_b64_tr_b16 v[52:53], v225 offset:32768\n\t"
      "ds_read_b64_tr_b16 v[54:55], v225 offset:33280\n\t"
      "ds_read_b64_tr_b16 v[56:57], v226 offset:32768\n\t"
      "ds_read_b64_tr_b16 v[58:59], v226 offset:33280\n\t"
      "ds_read_b64_tr_b16 v[60:61], v227 offset:32768\n\t"
      "ds_read_b64_tr_b16 v[62:63], v227 offset:33280\n\t"
      "NTK0_%=:\n\t"
      "s_waitcnt lgkmcnt(0)\n\t"
      "s_barrier\n\t"
      "global_load_dwordx2 v[148:149], v231, s[46:47] sc1\n\t"
      "global_load_dwordx2 v[150:151], v231, s[46:47] offset:384 sc1\n\t"
      "s_waitcnt vmcnt(0)\n\t"
      "v_max3_u32 v232, v148, v149, v150\n\t"
      "v_max_u32_e32 v232, v232, v151\n\t"
      "v_cmp_eq_u32_e32 vcc, -1, v232\n\t"
      "s_cbranch_vccnz LSR_%=\n\t"
      "LSB_%=:\n\t"
      "v_cndmask_b32_e64 v150, v150, 0, s[70:71]\n\t"
      "v_cndmask_b32_e64 v151, v151, 0, s[70:71]\n\t"
      "v_pk_add_f32 v[144:145], v[148:149], v[150:151]\n\t"
      "s_nop 1\n\t"
      "v_permlane16_swap_b32_e32 v144, v145\n\t"
      "v_add_f32_e32 v144, v144, v145\n\t"
      "v_mov_b32_e32 v145, v144\n\t"
      "s_nop 1\n\t"
      "v_permlane32_swap_b32_e32 v144, v145\n\t"
      "v_add_f32_e32 v144, v144, v145\n\t"
      "v_mov_b32_e32 v145, v144\n\t"
      "s_nop 1\n\t"
      "v_permlane16_swap_b32_e32 v144, v145\n\t"
      "v_mul_f32_e32 v144, 0xbb124925, v144\n\t"
      "v_mul_f32_e32 v145, 0x3b124925, v145\n\t"
      "v_fma_f32 v145, -v144, v144, v145\n\t"
      "v_max_f32_e32 v145, 0, v145\n\t"
      "v_add_f32_e32 v146, 0x3727c5ac, v145\n\t"
      "v_fmac_f32_e32 v145, 0x3727c5ac, v146\n\t"
      "v_rsq_f32_e32 v145, v145\n\t"
      "s_nop 0\n\t"
      "v_mul_f32_e32 v144, v144, v145\n\t"
      "v_pk_fma_f32 v[128:129], v[128:129], v[144:145], v[144:145] op_sel:[0,1,0] op_sel_hi:[1,1,0]\n\t"
      "v_pk_fma_f32 v[130:131], v[130:131], v[144:145], v[144:145] op_sel:[0,1,0] op_sel_hi:[1,1,0]\n\t"
      "v_cvt_pk_bf16_f32 v152, v128, v129\n\t"
      "v_cvt_pk_bf16_f32 v153, v130, v131\n\t"
      "ds_write_b64 v233, v[152:153]\n\t"
      "v_pk_fma_f32 v[132:133], v[132:133], v[144:145], v[144:145] op_sel:[0,1,0] op_sel_hi:[1,1,0]\n\t"
      "v_pk_fma_f32 v[134:135], v[134:135], v[144:145], v[144:145] op_sel:[0,1,0] op_sel_hi:[1,1,0]\n\t"
      "v_cvt_pk_bf16_f32 v154, v132, v133\n\t"
      "v_cvt_pk_bf16_f32 v155, v134, v135\n\t"
      "ds_write_b64 v234, v[154:155]\n\t"
      "ds_read_b64_tr_b16 v[160:161], v237\n\t"
      "ds_read_b64_tr_b16 v[162:163], v238 offset:512\n\t"
      "v_pk_fma_f32 v[136:137], v[136:137], v[144:145], v[144:145] op_sel:[0,1,0] op_sel_hi:[1,1,0]\n\t"
      "v_pk_fma_f32 v[138:139], v[138:139], v[144:145], v[144:145] op_sel:[0,1,0] op_sel_hi:[1,1,0]\n\t"
      "v_cvt_pk_bf16_f32 v156, v136, v137\n\t"
      "v_cvt_pk_bf16_f32 v157, v138, v139\n\t"
      "ds_write_b64 v235, v[156:157]\n\t"
      "v_pk_fma_f32 v[140:141], v[140:141], v[144:145], v[144:145] op_sel:[0,1,0] op_sel_hi:[1,1,0]\n\t"
      "v_pk_fma_f32 v[142:143], v[142:143], v[144:145], v[144:145] op_sel:[0,1,0] op_sel_hi:[1,1,0]\n\t"
      "v_cvt_pk_bf16_f32 v158, v140, v141\n\t"
      "v_cvt_pk_bf16_f32 v159, v142, v143\n\t"
      "ds_write_b64 v236, v[158:159]\n\t"
      "ds_read_b64_tr_b16 v[164:165], v239\n\t"
      "ds_read_b64_tr_b16 v[166:167], v240 offset:512\n\t"
      "s_waitcnt lgkmcnt(4)\n\t"
      "v_min_u32_e32 v160, -2, v160\n\t"
      "v_min_u32_e32 v161, -2, v161\n\t"
      "v_min_u32_e32 v162, -2, v162\n\t"
      "v_min_u32_e32 v163, -2, v163\n\t"
      "global_store_dwordx4 v229, v[160:163], s[48:49] sc1\n\t"
      "s_waitcnt lgkmcnt(0)\n\t"
      "v_min_u32_e32 v164, -2, v164\n\t"
      "v_min_u32_e32 v165, -2, v165\n\t"
      "v_min_u32_e32 v166, -2, v166\n\t"
      "v_min_u32_e32 v167, -2, v167\n\t"
      "global_store_dwordx4 v230, v[164:167], s[48:49] sc1\n\t"
      "s_barrier\n\t"
      "s_bitcmp0_b32 s27, 0\n\t"
      "s_cbranch_scc1 G4C_%=\n\t"
      "s_mul_i32 s34, s32, 196608\n\t"
      "s_mul_i32 s35, s24, 8192\n\t"
      "s_add_u32 s34, s34, s35\n\t"
      "s_add_u32 s34, s34, 1048576\n\t"
      "s_add_u32 s64, s48, s34\n\t"
      "s_addc_u32 s65, s49, 0\n\t"
      "v_add_u32_e32 v250, 4096, v244\n\t"
      "global_load_dwordx4 v[128:131], v250, s[64:65] offset:-4096 sc1\n\t"
      "global_load_dwordx4 v[132:135], v250, s[64:65] offset:-3072 sc1\n\t"
      "global_load_dwordx4 v[136:139], v250, s[64:65] offset:-2048 sc1\n\t"
      "global_load_dwordx4 v[140:143], v250, s[64:65] offset:-1024 sc1\n\t"
      "global_load_dwordx4 v[144:147], v250, s[64:65] sc1\n\t"
      "global_load_dwordx4 v[148:151], v250, s[64:65] offset:1024 sc1\n\t"
      "global_load_dwordx4 v[152:155], v250, s[64:65] offset:2048 sc1\n\t"
      "global_load_dwordx4 v[156:159], v250, s[64:65] offset:3072 sc1\n\t"
      "global_store_dwordx4 v241, v[208:211], s[20:21] offset:-2048 sc1\n\t"
      "global_store_dwordx4 v241, v[212:215], s[20:21] offset:2048 sc1\n\t"
      "s_setprio 0\n\t"
      "ds_read_b64_tr_b16 v[64:65], v224 offset:16384\n\t"
      "ds_read_b64_tr_b16 v[66:67], v224 offset:16896\n\t"
      "ds_read_b64_tr_b16 v[68:69], v225 offset:16384\n\t"
      "ds_read_b64_tr_b16 v[70:71], v225 offset:16896\n\t"
      "ds_read_b64_tr_b16 v[72:73], v226 offset:16384\n\t"
      "ds_read_b64_tr_b16 v[74:75], v226 offset:16896\n\t"
      "ds_read_b64_tr_b16 v[76:77], v227 offset:16384\n\t"
      "ds_read_b64_tr_b16 v[78:79], v227 offset:16896\n\t"
      "ds_read_b64_tr_b16 v[80:81], v224 offset:49152\n\t"
      "ds_read_b64_tr_b16 v[82:83], v224 offset:49664\n\t"
      "ds_read_b64_tr_b16 v[84:85], v225 offset:49152\n\t"
      "ds_read_b64_tr_b16 v[86:87], v225 offset:49664\n\t"
      "ds_read_b64_tr_b16 v[88:89], v226 offset:49152\n\t"
      "ds_read_b64_tr_b16 v[90:91], v226 offset:49664\n\t"
      "ds_read_b64_tr_b16 v[92:93], v227 offset:49152\n\t"
      "ds_read_b64_tr_b16 v[94:95], v227 offset:49664\n\t"
      "ds_read_b64_tr_b16 v[96:97], v224\n\t"
      "ds_read_b64_tr_b16 v[98:99], v224 offset:512\n\t"
      "ds_read_b64_tr_b16 v[100:101], v225\n\t"
      "ds_read_b64_tr_b16 v[102:103], v225 offset:512\n\t"
      "ds_read_b64_tr_b16 v[104:105], v226\n\t"
      "ds_read_b64_tr_b16 v[106:107], v226 offset:512\n\t"
      "ds_read_b64_tr_b16 v[108:109], v227\n\t"
      "ds_read_b64_tr_b16 v[110:111], v227 offset:512\n\t"
      "ds_read_b64_tr_b16 v[112:113], v224 offset:32768\n\t"
      "ds_read_b64_tr_b16 v[114:115], v224 offset:33280\n\t"
      "ds_read_b64_tr_b16 v[116:117], v225 offset:32768\n\t"
      "ds_read_b64_tr_b16 v[118:119], v225 offset:33280\n\t"
      "ds_read_b64_tr_b16 v[120:121], v226 offset:32768\n\t"
      "ds_read_b64_tr_b16 v[122:123], v226 offset:33280\n\t"
      "ds_read_b64_tr_b16 v[124:125], v227 offset:32768\n\t"
      "ds_read_b64_tr_b16 v[126:127], v227 offset:33280\n\t"
      "s_waitcnt vmcnt(2) lgkmcnt(0)\n\t"
      "GST_%=:\n\t"
      "s_add_u32 s66, s64, 65536\n\t"
      "s_addc_u32 s67, s65, 0\n\t"
      "s_add_u32 s68, s66, 65536\n\t"
      "s_addc_u32 s69, s67, 0\n\t"
      "s_lshl_b32 s34, s32, 18\n\t"
      "s_lshl_b32 s35, s24, 13\n\t"
      "s_add_u32 s34, s34, s35\n\t"
      "s_add_u32 s70, s48, s34\n\t"
      "s_addc_u32 s71, s49, 0\n\t"
      "s_add_u32 s72, s70, 65536\n\t"
      "s_addc_u32 s73, s71, 0\n\t"
      "s_add_u32 s74, s72, 65536\n\t"
      "s_addc_u32 s75, s73, 0\n\t"
      "s_add_u32 s76, s74, 65536\n\t"
      "s_addc_u32 s77, s75, 0\n\t"
      "s_mul_i32 s34, s30, 28\n\t"
      "s_add_u32 s34, s34, s29\n\t"
      "s_lshl_b32 s34, s34, 11\n\t"
      "s_add_u32 s34, s34, 10223616\n\t"
      "s_add_u32 s62, s20, s34\n\t"
      "s_addc_u32 s63, s21, 0\n\t"
      "v_lshlrev_b32_e32 v248, 16, v249\n\t"
      "v_and_b32_e32 v249, 0xffff0000, v249\n\t"
      "v_mov_b32_e32 v252, 0xbdd2d3e8\n\t"
      "v_xor_b32_e32 v253, 128, v245\n\t"
      "v_max3_u32 v251, v128, v129, v130\n\t"
      "v_max3_u32 v251, v251, v131, v132\n\t"
      "v_max3_u32 v251, v251, v133, v134\n\t"
      "v_max3_u32 v251, v251, v135, v136\n\t"
      "v_max3_u32 v251, v251, v137, v138\n\t"
      "v_max3_u32 v251, v251, v139, v140\n\t"
      "v_max3_u32 v251, v251, v141, v142\n\t"
      "v_max3_u32 v251, v251, v143, v144\n\t"
      "v_max3_u32 v251, v251, v145, v146\n\t"
      "v_max3_u32 v251, v251, v147, v148\n\t"
      "v_max3_u32 v251, v251, v149, v150\n\t"
      "v_max3_u32 v251, v251, v151, v152\n\t"
      "v_max3_u32 v251, v251, v153, v154\n\t"
      "v_max3_u32 v251, v251, v155, v156\n\t"
      "v_max3_u32 v251, v251, v157, v158\n\t"
      "v_max_u32_e32 v251, v251, v159\n\t"
      "v_cmp_eq_u32_e32 vcc, -1, v251\n\t"
      "s_cbranch_vccnz LA30R_%=\n\t"
      "LA30B_%=:\n\t"
      "v_mfma_f32_16x16x32_bf16 v[208:211], v[0:3], v[128:131], 0\n\t"
      "global_load_dwordx4 v[160:163], v250, s[66:67] offset:-4096 sc1\n\t"
      "v_mfma_f32_16x16x32_bf16 v[208:211], v[16:19], v[132:135], v[208:211]\n\t"
      "v_mfma_f32_16x16x32_bf16 v[208:211], v[32:35], v[136:139], v[208:211]\n\t"
      "global_load_dwordx4 v[164:167], v250, s[66:67] offset:-3072 sc1\n\t"
      "v_mfma_f32_16x16x32_bf16 v[208:211], v[48:51], v[140:143], v[208:211]\n\t"
      "v_mfma_f32_16x16x32_bf16 v[208:211], v[64:67], v[144:147], v[208:211]\n\t"
      "s_lshl_b32 s34, s2, 7\n\t"
      "s_add_u32 s34, s34, 65536\n\t"
      "v_mov_b32_e32 v230, s34\n\t"
      "s_cmp_lg_u32 s24, 3\n\t"
      "s_cbranch_scc1 SK17_%=\n\t"
      "global_load_dwordx2 v[228:229], v230, s[60:61] sc1\n\t"
      "SK17_%=:\n\t"
      "global_load_dwordx4 v[168:171], v250, s[66:67] offset:-2048 sc1\n\t"
      "v_mfma_f32_16x16x32_bf16 v[208:211], v[80:83], v[148:151], v[208:211]\n\t"
      "v_mfma_f32_16x16x32_bf16 v[208:211], v[96:99], v[152:155], v[208:211]\n\t"
      "global_load_dwordx4 v[172:175], v250, s[66:67] offset:-1024 sc1\n\t"
      "v_mfma_f32_16x16x32_bf16 v[208:211], v[112:115], v[156:159], v[208:211]\n\t"
      "v_mfma_f32_16x16x32_bf16 v[212:215], v[4:7], v[128:131], 0\n\t"
      "global_load_dwordx4 v[176:179], v250, s[66:67] sc1\n\t"
      "v_mfma_f32_16x16x32_bf16 v[212:215], v[20:23], v[132:135], v[212:215]\n\t"
      "v_mfma_f32_16x16x32_bf16 v[212:215], v[36:39], v[136:139], v[212:215]\n\t"
      "global_load_dwordx4 v[180:183], v250, s[66:67] offset:1024 sc1\n\t"
      "v_mfma_f32_16x16x32_bf16 v[212:215], v[52:55], v[140:143], v[212:215]\n\t"
      "v_mfma_f32_16x16x32_bf16 v[212:215], v[68:71], v[144:147], v[212:215]\n\t"
      "global_load_dwordx4 v[184:187], v250, s[66:67] offset:2048 sc1\n\t"
      "v_mfma_f32_16x16x32_bf16 v[212:215], v[84:87], v[148:151], v[212:215]\n\t"
      "v_mfma_f32_16x16x32_bf16 v[212:215], v[100:103], v[152:155], v[212:215]\n\t"
      "global_load_dwordx4 v[188:191], v250, s[66:67] offset:3072 sc1\n\t"
      "v_mfma_f32_16x16x32_bf16 v[212:215], v[116:119], v[156:159], v[212:215]\n\t"
      "ds_write_b128 v245, v[208:211]\n\t"
      "v_mfma_f32_16x16x32_bf16 v[216:219], v[8:11], v[128:131], 0\n\t"
      "global_load_dwordx4 v[192:195], v250, s[68:69] offset:-4096 sc1\n\t"
      "v_mfma_f32_16x16x32_bf16 v[216:219], v[24:27], v[132:135], v[216:219]\n\t"
      "v_mfma_f32_16x16x32_bf16 v[216:219], v[40:43], v[136:139], v[216:219]\n\t"
      "global_load_dwordx4 v[196:199], v250, s[68:69] offset:-3072 sc1\n\t"
      "v_mfma_f32_16x16x32_bf16 v[216:219], v[56:59], v[140:143], v[216:219]\n\t"
      "v_mfma_f32_16x16x32_bf16 v[216:219], v[72:75], v[144:147], v[216:219]\n\t"
      "global_load_dwordx4 v[200:203], v250, s[68:69] offset:-2048 sc1\n\t"
      "v_mfma_f32_16x16x32_bf16 v[216:219], v[88:91], v[148:151], v[216:219]\n\t"
      "v_mfma_f32_16x16x32_bf16 v[216:219], v[104:107], v[152:155], v[216:219]\n\t"
      "v_mfma_f32_16x16x32_bf16 v[216:219], v[120:123], v[156:159], v[216:219]\n\t"
      "ds_write_b128 v253, v[212:215] offset:1024\n\t"
      "global_load_dwordx4 v[204:207], v250, s[68:69] offset:-1024 sc1\n\t"
      "v_mfma_f32_16x16x32_bf16 v[220:223], v[12:15], v[128:131], 0\n\t"
      "v_mfma_f32_16x16x32_bf16 v[220:223], v[28:31], v[132:135], v[220:223]\n\t"
      "v_mfma_f32_16x16x32_bf16 v[220:223], v[44:47], v[136:139], v[220:223]\n\t"
      "v_mfma_f32_16x16x32_bf16 v[220:223], v[60:63], v[140:143], v[220:223]\n\t"
      "global_load_dwordx4 v[128:131], v250, s[68:69] sc1\n\t"
      "v_mfma_f32_16x16x32_bf16 v[220:223], v[76:79], v[144:147], v[220:223]\n\t"
      "v_mfma_f32_16x16x32_bf16 v[220:223], v[92:95], v[148:151], v[220:223]\n\t"
      "v_mfma_f32_16x16x32_bf16 v[220:223], v[108:111], v[152:155], v[220:223]\n\t"
      "v_mfma_f32_16x16x32_bf16 v[220:223], v[124:127], v[156:159], v[220:223]\n\t"
      "ds_write_b128 v245, v[216:219] offset:2048\n\t"
      "global_load_dwordx4 v[132:135], v250, s[68:69] offset:1024 sc1\n\t"
      "s_waitcnt vmcnt(10)\n\t"
      "v_mfma_f32_16x16x32_bf16 v[224:227], v[0:3], v[160:163], 0\n\t"
      "v_mfma_f32_16x16x32_bf16 v[224:227], v[16:19], v[164:167], v[224:227]\n\t"
      "s_nop 2\n\t"
      "ds_write_b128 v253, v[220:223] offset:3072\n\t"
      "v_mfma_f32_16x16x32_bf16 v[224:227], v[32:35], v[168:171], v[224:227]\n\t"
      "v_mfma_f32_16x16x32_bf16 v[224:227], v[48:51], v[172:175], v[224:227]\n\t"
      "global_load_dwordx4 v[136:139], v250, s[68:69] offset:2048 sc1\n\t"
      "s_cmp_lg_u32 s24, 3\n\t"
      "s_cbranch_scc1 LF6B_%=\n\t"
      "v_cmp_ne_u64_e32 vcc, s[22:23], v[228:229]\n\t"
      "s_cbranch_vccnz LF6R_%=\n\t"
      "LF6B_%=:\n\t"
      "s_waitcnt lgkmcnt(0)\n\t"
      "s_barrier\n\t"
      "s_cmp_lg_u32 s24, 3\n\t"
      "s_cbranch_scc1 HF3_%=\n\t"
      "s_lshl3_add_u32 s34, s25, s26\n\t"
      "s_lshl_b32 s34, s34, 7\n\t"
      "s_add_u32 s34, s34, 98304\n\t"
      "v_mov_b32_e32 v238, s34\n\t"
      "v_mov_b32_e32 v236, s22\n\t"
      "v_mov_b32_e32 v237, s23\n\t"
      "s_mov_b64 exec, 1\n\t"
      "global_store_dwordx2 v238, v[236:237], s[60:61] sc1\n\t"
      "s_mov_b64 exec, -1\n\t"
      "HF3_%=:\n\t"
      "ds_read_b64 v[228:229], v246\n\t"
      "s_setprio 1\n\t"
      "s_waitcnt vmcnt(7)\n\t"
      "v_mfma_f32_16x16x32_bf16 v[224:227], v[64:67], v[176:179], v[224:227]\n\t"
      "ds_read_b64 v[230:231], v246 offset:4096\n\t"
      "v_mfma_f32_16x16x32_bf16 v[224:227], v[80:83], v[180:183], v[224:227]\n\t"
      "ds_read_b64 v[232:233], v246 offset:8192\n\t"
      "v_mfma_f32_16x16x32_bf16 v[224:227], v[96:99], v[184:187], v[224:227]\n\t"
      "ds_read_b64 v[234:235], v246 offset:12288\n\t"
      "v_mfma_f32_16x16x32_bf16 v[224:227], v[112:115], v[188:191], v[224:227]\n\t"
      "global_load_dwordx4 v[140:143], v250, s[68:69] offset:3072 sc1\n\t"
      "ds_read_b64 v[236:237], v246 offset:16384\n\t"
      "v_mfma_f32_16x16x32_bf16 v[208:211], v[4:7], v[160:163], 0\n\t"
      "ds_read_b64 v[238:239], v246 offset:20480\n\t"
      "v_mfma_f32_16x16x32_bf16 v[208:211], v[20:23], v[164:167], v[208:211]\n\t"
      "ds_read_b64 v[240:241], v246 offset:24576\n\t"
      "v_mfma_f32_16x16x32_bf16 v[208:211], v[36:39], v[168:171], v[208:211]\n\t"
      "ds_read_b64 v[242:243], v246 offset:28672\n\t"
      "v_cmp_u_f32_e32 vcc, v224, v224\n\t"
      "s_cbranch_vccnz LA31R_%=\n\t"
      "LA31B_%=:\n\t"
      "v_mfma_f32_16x16x32_bf16 v[208:211], v[52:55], v[172:175], v[208:211]\n\t"
      "global_load_dwordx4 v[144:147], v250, s[70:71] offset:-4096 sc1\n\t"
      "v_mfma_f32_16x16x32_bf16 v[208:211], v[68:71], v[176:179], v[208:211]\n\t"
      "s_waitcnt lgkmcnt(4)\n\t"
      "v_add_f32_e32 v228, v228, v230\n\t"
      "v_add_f32_e32 v232, v232, v234\n\t"
      "v_mfma_f32_16x16x32_bf16 v[208:211], v[84:87], v[180:183], v[208:211]\n\t"
      "v_add_f32_e32 v229, v229, v231\n\t"
      "v_add_f32_e32 v233, v233, v235\n\t"
      "v_add_f32_e32 v228, v228, v232\n\t"
      "v_mfma_f32_16x16x32_bf16 v[208:211], v[100:103], v[184:187], v[208:211]\n\t"
      "v_add_f32_e32 v229, v229, v233\n\t"
      "s_waitcnt lgkmcnt(0)\n\t"
      "v_add_f32_e32 v236, v236, v238\n\t"
      "v_mfma_f32_16x16x32_bf16 v[208:211], v[116:119], v[188:191], v[208:211]\n\t"
      "v_add_f32_e32 v240, v240, v242\n\t"
      "v_add_f32_e32 v237, v237, v239\n\t"
      "v_add_f32_e32 v241, v241, v243\n\t"
      "ds_write_b128 v245, v[224:227] offset:32768\n\t"
      "global_load_dwordx4 v[148:151], v250, s[70:71] offset:-3072 sc1\n\t"
      "v_mfma_f32_16x16x32_bf16 v[212:215], v[8:11], v[160:163], 0\n\t"
      "v_add_f32_e32 v228, v228, v248\n\t"
      "v_add_f32_e32 v236, v236, v240\n\t"
      "v_mfma_f32_16x16x32_bf16 v[212:215], v[24:27], v[164:167], v[212:215]\n\t"
      "v_add_f32_e32 v229, v229, v249\n\t"
      "v_add_f32_e32 v237, v237, v241\n\t"
      "v_add_f32_e32 v228, v228, v236\n\t"
      "v_mfma_f32_16x16x32_bf16 v[212:215], v[40:43], v[168:171], v[212:215]\n\t"
      "v_add_f32_e32 v229, v229, v237\n\t"
      "v_mul_f32_e32 v230, v228, v228\n\t"
      "v_mul_f32_e32 v231, v229, v229\n\t"
      "v_mfma_f32_16x16x32_bf16 v[212:215], v[56:59], v[172:175], v[212:215]\n\t"
      "v_fmaak_f32 v230, v252, v230, 0xc0135761\n\t"
      "v_fmaak_f32 v231, v252, v231, 0xc0135761\n\t"
      "v_mul_f32_e32 v230, v228, v230\n\t"
      "global_load_dwordx4 v[152:155], v250, s[70:71] offset:-2048 sc1\n\t"
      "v_mfma_f32_16x16x32_bf16 v[212:215], v[72:75], v[176:179], v[212:215]\n\t"
      "v_mul_f32_e32 v231, v229, v231\n\t"
      "v_exp_f32_e32 v230, v230\n\t"
      "v_exp_f32_e32 v231, v231\n\t"
      "v_mfma_f32_16x16x32_bf16 v[212:215], v[88:91], v[180:183], v[212:215]\n\t"
      "v_add_f32_e32 v230, 1.0, v230\n\t"
      "v_add_f32_e32 v231, 1.0, v231\n\t"
      "v_mfma_f32_16x16x32_bf16 v[212:215], v[104:107], v[184:187], v[212:215]\n\t"
      "v_rcp_f32_e32 v230, v230\n\t"
      "v_rcp_f32_e32 v231, v231\n\t"
      "v_mul_f32_e32 v228, v228, v230\n\t"
      "v_mfma_f32_16x16x32_bf16 v[212:215], v[120:123], v[188:191], v[212:215]\n\t"
      "v_mul_f32_e32 v229, v229, v231\n\t"
      "v_cvt_pk_bf16_f32 v230, v228, v229\n\t"
      "v_min_u32_e32 v254, -2, v230\n\t"
      "ds_write_b128 v253, v[208:211] offset:33792\n\t"
      "global_load_dwordx4 v[156:159], v250, s[70:71] offset:-1024 sc1\n\t"
      "v_mfma_f32_16x16x32_bf16 v[216:219], v[12:15], v[160:163], 0\n\t"
      "global_store_dword v247, v254, s[62:63] sc1\n\t"
      "s_add_u32 s62, s62, 57344\n\t"
      "s_addc_u32 s63, s63, 0\n\t"
      "s_setprio 0\n\t"
      "v_mfma_f32_16x16x32_bf16 v[216:219], v[28:31], v[164:167], v[216:219]\n\t"
      "v_mfma_f32_16x16x32_bf16 v[216:219], v[44:47], v[168:171], v[216:219]\n\t"
      "v_mfma_f32_16x16x32_bf16 v[216:219], v[60:63], v[172:175], v[216:219]\n\t"
      "global_load_dwordx4 v[160:163], v250, s[70:71] sc1\n\t"
      "v_mfma_f32_16x16x32_bf16 v[216:219], v[76:79], v[176:179], v[216:219]\n\t"
      "v_mfma_f32_16x16x32_bf16 v[216:219], v[92:95], v[180:183], v[216:219]\n\t"
      "v_mfma_f32_16x16x32_bf16 v[216:219], v[108:111], v[184:187], v[216:219]\n\t"
      "v_mfma_f32_16x16x32_bf16 v[216:219], v[124:127], v[188:191], v[216:219]\n\t"
      "ds_write_b128 v245, v[212:215] offset:34816\n\t"
      "global_load_dwordx4 v[164:167], v250, s[70:71] offset:1024 sc1\n\t"
      "s_waitcnt vmcnt(11)\n\t"
      "v_mfma_f32_16x16x32_bf16 v[220:223], v[0:3], v[192:195], 0\n\t"
      "v_mfma_f32_16x16x32_bf16 v[220:223], v[16:19], v[196:199], v[220:223]\n\t"
      "s_nop 2\n\t"
      "ds_write_b128 v253, v[216:219] offset:35840\n\t"
      "v_mfma_f32_16x16x32_bf16 v[220:223], v[32:35], v[200:203], v[220:223]\n\t"
      "v_mfma_f32_16x16x32_bf16 v[220:223], v[48:51], v[204:207], v[220:223]\n\t"
      "global_load_dwordx4 v[168:171], v250, s[70:71] offset:2048 sc1\n\t"
      "s_waitcnt lgkmcnt(0)\n\t"
      "s_barrier\n\t"
      "ds_read_b64 v[228:229], v246 offset:32768\n\t"
      "s_setprio 1\n\t"
      "s_waitcnt vmcnt(8)\n\t"
      "v_mfma_f32_16x16x32_bf16 v[220:223], v[64:67], v[128:131], v[220:223]\n\t"
      "ds_read_b64 v[230:231], v246 offset:36864\n\t"
      "v_mfma_f32_16x16x32_bf16 v[220:223], v[80:83], v[132:135], v[220:223]\n\t"
      "ds_read_b64 v[232:233], v246 offset:40960\n\t"
      "v_mfma_f32_16x16x32_bf16 v[220:223], v[96:99], v[136:139], v[220:223]\n\t"
      "ds_read_b64 v[234:235], v246 offset:45056\n\t"
      "v_mfma_f32_16x16x32_bf16 v[220:223], v[112:115], v[140:143], v[220:223]\n\t"
      "global_load_dwordx4 v[172:175], v250, s[70:71] offset:3072 sc1\n\t"
      "ds_read_b64 v[236:237], v246 offset:49152\n\t"
      "v_mfma_f32_16x16x32_bf16 v[224:227], v[4:7], v[192:195], 0\n\t"
      "ds_read_b64 v[238:239], v246 offset:53248\n\t"
      "v_mfma_f32_16x16x32_bf16 v[224:227], v[20:23], v[196:199], v[224:227]\n\t"
      "ds_read_b64 v[240:241], v246 offset:57344\n\t"
      "v_mfma_f32_16x16x32_bf16 v[224:227], v[36:39], v[200:203], v[224:227]\n\t"
      "ds_read_b64 v[242:243], v246 offset:61440\n\t"
      "v_cmp_u_f32_e32 vcc, v220, v220\n\t"
      "s_cbranch_vccnz LA32R_%=\n\t"
      "LA32B_%=:\n\t"
      "v_mfma_f32_16x16x32_bf16 v[224:227], v[52:55], v[204:207], v[224:227]\n\t"
      "global_load_dwordx4 v[176:179], v250, s[72:73] offset:-4096 sc1\n\t"
      "v_mfma_f32_16x16x32_bf16 v[224:227], v[68:71], v[128:131], v[224:227]\n\t"
      "s_waitcnt lgkmcnt(4)\n\t"
      "v_add_f32_e32 v228, v228, v230\n\t"
      "v_add_f32_e32 v232, v232, v234\n\t"
      "v_mfma_f32_16x16x32_bf16 v[224:227], v[84:87], v[132:135], v[224:227]\n\t"
      "v_add_f32_e32 v229, v229, v231\n\t"
      "v_add_f32_e32 v233, v233, v235\n\t"
      "v_add_f32_e32 v228, v228, v232\n\t"
      "v_mfma_f32_16x16x32_bf16 v[224:227], v[100:103], v[136:139], v[224:227]\n\t"
      "v_add_f32_e32 v229, v229, v233\n\t"
      "s_waitcnt lgkmcnt(0)\n\t"
      "v_add_f32_e32 v236, v236, v238\n\t"
      "v_mfma_f32_16x16x32_bf16 v[224:227], v[116:119], v[140:143], v[224:227]\n\t"
      "v_add_f32_e32 v240, v240, v242\n\t"
      "v_add_f32_e32 v237, v237, v239\n\t"
      "v_add_f32_e32 v241, v241, v243\n\t"
      "ds_write_b128 v245, v[220:223]\n\t"
      "global_load_dwordx4 v[180:183], v250, s[72:73] offset:-3072 sc1\n\t"
      "v_mfma_f32_16x16x32_bf16 v[208:211], v[8:11], v[192:195], 0\n\t"
      "v_add_f32_e32 v228, v228, v248\n\t"
      "v_add_f32_e32 v236, v236, v240\n\t"
      "v_mfma_f32_16x16x32_bf16 v[208:211], v[24:27], v[196:199], v[208:211]\n\t"
      "v_add_f32_e32 v229, v229, v249\n\t"
      "v_add_f32_e32 v237, v237, v241\n\t"
      "v_add_f32_e32 v228, v228, v236\n\t"
      "v_mfma_f32_16x16x32_bf16 v[208:211], v[40:43], v[200:203], v[208:211]\n\t"
      "v_add_f32_e32 v229, v229, v237\n\t"
      "v_mul_f32_e32 v230, v228, v228\n\t"
      "v_mul_f32_e32 v231, v229, v229\n\t"
      "v_mfma_f32_16x16x32_bf16 v[208:211], v[56:59], v[204:207], v[208:211]\n\t"
      "v_fmaak_f32 v230, v252, v230, 0xc0135761\n\t"
      "v_fmaak_f32 v231, v252, v231, 0xc0135761\n\t"
      "v_mul_f32_e32 v230, v228, v230\n\t"
      "global_load_dwordx4 v[184:187], v250, s[72:73] offset:-2048 sc1\n\t"
      "v_mfma_f32_16x16x32_bf16 v[208:211], v[72:75], v[128:131], v[208:211]\n\t"
      "v_mul_f32_e32 v231, v229, v231\n\t"
      "v_exp_f32_e32 v230, v230\n\t"
      "v_exp_f32_e32 v231, v231\n\t"
      "v_mfma_f32_16x16x32_bf16 v[208:211], v[88:91], v[132:135], v[208:211]\n\t"
      "v_add_f32_e32 v230, 1.0, v230\n\t"
      "v_add_f32_e32 v231, 1.0, v231\n\t"
      "v_mfma_f32_16x16x32_bf16 v[208:211], v[104:107], v[136:139], v[208:211]\n\t"
      "v_rcp_f32_e32 v230, v230\n\t"
      "v_rcp_f32_e32 v231, v231\n\t"
      "v_mul_f32_e32 v228, v228, v230\n\t"
      "v_mfma_f32_16x16x32_bf16 v[208:211], v[120:123], v[140:143], v[208:211]\n\t"
      "v_mul_f32_e32 v229, v229, v231\n\t"
      "v_cvt_pk_bf16_f32 v230, v228, v229\n\t"
      "v_min_u32_e32 v254, -2, v230\n\t"
      "ds_write_b128 v253, v[224:227] offset:1024\n\t"
      "global_load_dwordx4 v[188:191], v250, s[72:73] offset:-1024 sc1\n\t"
      "v_mfma_f32_16x16x32_bf16 v[212:215], v[12:15], v[192:195], 0\n\t"
      "global_store_dword v247, v254, s[62:63] sc1\n\t"
      "s_add_u32 s62, s62, 57344\n\t"
      "s_addc_u32 s63, s63, 0\n\t"
      "s_setprio 0\n\t"
      "v_mfma_f32_16x16x32_bf16 v[212:215], v[28:31], v[196:199], v[212:215]\n\t"
      "v_mfma_f32_16x16x32_bf16 v[212:215], v[44:47], v[200:203], v[212:215]\n\t"
      "v_mfma_f32_16x16x32_bf16 v[212:215], v[60:63], v[204:207], v[212:215]\n\t"
      "global_load_dwordx4 v[192:195], v250, s[72:73] sc1\n\t"
      "v_mfma_f32_16x16x32_bf16 v[212:215], v[76:79], v[128:131], v[212:215]\n\t"
      "v_mfma_f32_16x16x32_bf16 v[212:215], v[92:95], v[132:135], v[212:215]\n\t"
      "v_mfma_f32_16x16x32_bf16 v[212:215], v[108:111], v[136:139], v[212:215]\n\t"
      "v_mfma_f32_16x16x32_bf16 v[212:215], v[124:127], v[140:143], v[212:215]\n\t"
      "ds_write_b128 v245, v[208:211] offset:2048\n\t"
      "global_load_dwordx4 v[196:199], v250, s[72:73] offset:1024 sc1\n\t"
      "s_waitcnt vmcnt(12)\n\t"
      "v_mfma_f32_16x16x32_bf16 v[216:219], v[0:3], v[144:147], 0\n\t"
      "v_mfma_f32_16x16x32_bf16 v[216:219], v[16:19], v[148:151], v[216:219]\n\t"
      "s_nop 2\n\t"
      "ds_write_b128 v253, v[212:215] offset:3072\n\t"
      "v_mfma_f32_16x16x32_bf16 v[216:219], v[32:35], v[152:155], v[216:219]\n\t"
      "v_mfma_f32_16x16x32_bf16 v[216:219], v[48:51], v[156:159], v[216:219]\n\t"
      "global_load_dwordx4 v[200:203], v250, s[72:73] offset:2048 sc1\n\t"
      "s_waitcnt lgkmcnt(0)\n\t"
      "s_barrier\n\t"
      "ds_read_b64 v[228:229], v246\n\t"
      "s_setprio 1\n\t"
      "s_waitcnt vmcnt(8)\n\t"
      "v_mfma_f32_16x16x32_bf16 v[216:219], v[64:67], v[160:163], v[216:219]\n\t"
      "ds_read_b64 v[230:231], v246 offset:4096\n\t"
      "v_mfma_f32_16x16x32_bf16 v[216:219], v[80:83], v[164:167], v[216:219]\n\t"
      "ds_read_b64 v[232:233], v246 offset:8192\n\t"
      "v_mfma_f32_16x16x32_bf16 v[216:219], v[96:99], v[168:171], v[216:219]\n\t"
      "ds_read_b64 v[234:235], v246 offset:12288\n\t"
      "v_mfma_f32_16x16x32_bf16 v[216:219], v[112:115], v[172:175], v[216:219]\n\t"
      "global_load_dwordx4 v[204:207], v250, s[72:73] offset:3072 sc1\n\t"
      "ds_read_b64 v[236:237], v246 offset:16384\n\t"
      "v_mfma_f32_16x16x32_bf16 v[220:223], v[4:7], v[144:147], 0\n\t"
      "ds_read_b64 v[238:239], v246 offset:20480\n\t"
      "v_mfma_f32_16x16x32_bf16 v[220:223], v[20:23], v[148:151], v[220:223]\n\t"
      "ds_read_b64 v[240:241], v246 offset:24576\n\t"
      "v_mfma_f32_16x16x32_bf16 v[220:223], v[36:39], v[152:155], v[220:223]\n\t"
      "ds_read_b64 v[242:243], v246 offset:28672\n\t"
      "v_cmp_u_f32_e32 vcc, v216, v216\n\t"
      "s_cbranch_vccnz LA33R_%=\n\t"
      "LA33B_%=:\n\t"
      "v_mfma_f32_16x16x32_bf16 v[220:223], v[52:55], v[156:159], v[220:223]\n\t"
      "global_load_dwordx4 v[128:131], v250, s[74:75] offset:-4096 sc1\n\t"
      "v_mfma_f32_16x16x32_bf16 v[220:223], v[68:71], v[160:163], v[220:223]\n\t"
      "s_waitcnt lgkmcnt(4)\n\t"
      "v_add_f32_e32 v228, v228, v230\n\t"
      "v_add_f32_e32 v232, v232, v234\n\t"
      "v_mfma_f32_16x16x32_bf16 v[220:223], v[84:87], v[164:167], v[220:223]\n\t"
      "v_add_f32_e32 v229, v229, v231\n\t"
      "v_add_f32_e32 v233, v233, v235\n\t"
      "v_add_f32_e32 v228, v228, v232\n\t"
      "v_mfma_f32_16x16x32_bf16 v[220:223], v[100:103], v[168:171], v[220:223]\n\t"
      "v_add_f32_e32 v229, v229, v233\n\t"
      "s_waitcnt lgkmcnt(0)\n\t"
      "v_add_f32_e32 v236, v236, v238\n\t"
      "v_mfma_f32_16x16x32_bf16 v[220:223], v[116:119], v[172:175], v[220:223]\n\t"
      "v_add_f32_e32 v240, v240, v242\n\t"
      "v_add_f32_e32 v237, v237, v239\n\t"
      "v_add_f32_e32 v241, v241, v243\n\t"
      "ds_write_b128 v245, v[216:219] offset:32768\n\t"
      "global_load_dwordx4 v[132:135], v250, s[74:75] offset:-3072 sc1\n\t"
      "v_mfma_f32_16x16x32_bf16 v[224:227], v[8:11], v[144:147], 0\n\t"
      "v_add_f32_e32 v228, v228, v248\n\t"
      "v_add_f32_e32 v236, v236, v240\n\t"
      "v_mfma_f32_16x16x32_bf16 v[224:227], v[24:27], v[148:151], v[224:227]\n\t"
      "v_add_f32_e32 v229, v229, v249\n\t"
      "v_add_f32_e32 v237, v237, v241\n\t"
      "v_add_f32_e32 v228, v228, v236\n\t"
      "v_mfma_f32_16x16x32_bf16 v[224:227], v[40:43], v[152:155], v[224:227]\n\t"
      "v_add_f32_e32 v229, v229, v237\n\t"
      "v_mul_f32_e32 v230, v228, v228\n\t"
      "v_mul_f32_e32 v231, v229, v229\n\t"
      "v_mfma_f32_16x16x32_bf16 v[224:227], v[56:59], v[156:159], v[224:227]\n\t"
      "v_fmaak_f32 v230, v252, v230, 0xc0135761\n\t"
      "v_fmaak_f32 v231, v252, v231, 0xc0135761\n\t"
      "v_mul_f32_e32 v230, v228, v230\n\t"
      "global_load_dwordx4 v[136:139], v250, s[74:75] offset:-2048 sc1\n\t"
      "v_mfma_f32_16x16x32_bf16 v[224:227], v[72:75], v[160:163], v[224:227]\n\t"
      "v_mul_f32_e32 v231, v229, v231\n\t"
      "v_exp_f32_e32 v230, v230\n\t"
      "v_exp_f32_e32 v231, v231\n\t"
      "v_mfma_f32_16x16x32_bf16 v[224:227], v[88:91], v[164:167], v[224:227]\n\t"
      "v_add_f32_e32 v230, 1.0, v230\n\t"
      "v_add_f32_e32 v231, 1.0, v231\n\t"
      "v_mfma_f32_16x16x32_bf16 v[224:227], v[104:107], v[168:171], v[224:227]\n\t"
      "v_rcp_f32_e32 v230, v230\n\t"
      "v_rcp_f32_e32 v231, v231\n\t"
      "v_mul_f32_e32 v228, v228, v230\n\t"
      "v_mfma_f32_16x16x32_bf16 v[224:227], v[120:123], v[172:175], v[224:227]\n\t"
      "v_mul_f32_e32 v229, v229, v231\n\t"
      "v_cvt_pk_bf16_f32 v230, v228, v229\n\t"
      "v_min_u32_e32 v254, -2, v230\n\t"
      "ds_write_b128 v253, v[220:223] offset:33792\n\t"
      "global_load_dwordx4 v[140:143], v250, s[74:75] offset:-1024 sc1\n\t"
      "v_mfma_f32_16x16x32_bf16 v[208:211], v[12:15], v[144:147], 0\n\t"
      "global_store_dword v247, v254, s[62:63] sc1\n\t"
      "s_add_u32 s62, s62, 57344\n\t"
      "s_addc_u32 s63, s63, 0\n\t"
      "s_setprio 0\n\t"
      "v_mfma_f32_16x16x32_bf16 v[208:211], v[28:31], v[148:151], v[208:211]\n\t"
      "v_mfma_f32_16x16x32_bf16 v[208:211], v[44:47], v[152:155], v[208:211]\n\t"
      "v_mfma_f32_16x16x32_bf16 v[208:211], v[60:63], v[156:159], v[208:211]\n\t"
      "global_load_dwordx4 v[144:147], v250, s[74:75] sc1\n\t"
      "v_mfma_f32_16x16x32_bf16 v[208:211], v[76:79], v[160:163], v[208:211]\n\t"
      "v_mfma_f32_16x16x32_bf16 v[208:211], v[92:95], v[164:167], v[208:211]\n\t"
      "v_mfma_f32_16x16x32_bf16 v[208:211], v[108:111], v[168:171], v[208:211]\n\t"
      "v_mfma_f32_16x16x32_bf16 v[208:211], v[124:127], v[172:175], v[208:211]\n\t"
      "ds_write_b128 v245, v[224:227] offset:34816\n\t"
      "global_load_dwordx4 v[148:151], v250, s[74:75] offset:1024 sc1\n\t"
      "s_waitcnt vmcnt(12)\n\t"
      "v_mfma_f32_16x16x32_bf16 v[212:215], v[0:3], v[176:179], 0\n\t"
      "v_mfma_f32_16x16x32_bf16 v[212:215], v[16:19], v[180:183], v[212:215]\n\t"
      "s_nop 2\n\t"
      "ds_write_b128 v253, v[208:211] offset:35840\n\t"
      "v_mfma_f32_16x16x32_bf16 v[212:215], v[32:35], v[184:187], v[212:215]\n\t"
      "v_mfma_f32_16x16x32_bf16 v[212:215], v[48:51], v[188:191], v[212:215]\n\t"
      "global_load_dwordx4 v[152:155], v250, s[74:75] offset:2048 sc1\n\t"
      "s_waitcnt lgkmcnt(0)\n\t"
      "s_barrier\n\t"
      "ds_read_b64 v[228:229], v246 offset:32768\n\t"
      "s_setprio 1\n\t"
      "s_waitcnt vmcnt(8)\n\t"
      "v_mfma_f32_16x16x32_bf16 v[212:215], v[64:67], v[192:195], v[212:215]\n\t"
      "ds_read_b64 v[230:231], v246 offset:36864\n\t"
      "v_mfma_f32_16x16x32_bf16 v[212:215], v[80:83], v[196:199], v[212:215]\n\t"
      "ds_read_b64 v[232:233], v246 offset:40960\n\t"
      "v_mfma_f32_16x16x32_bf16 v[212:215], v[96:99], v[200:203], v[212:215]\n\t"
      "ds_read_b64 v[234:235], v246 offset:45056\n\t"
      "v_mfma_f32_16x16x32_bf16 v[212:215], v[112:115], v[204:207], v[212:215]\n\t"
      "global_load_dwordx4 v[156:159], v250, s[74:75] offset:3072 sc1\n\t"
      "ds_read_b64 v[236:237], v246 offset:49152\n\t"
      "v_mfma_f32_16x16x32_bf16 v[216:219], v[4:7], v[176:179], 0\n\t"
      "ds_read_b64 v[238:239], v246 offset:53248\n\t"
      "v_mfma_f32_16x16x32_bf16 v[216:219], v[20:23], v[180:183], v[216:219]\n\t"
      "ds_read_b64 v[240:241], v246 offset:57344\n\t"
      "v_mfma_f32_16x16x32_bf16 v[216:219], v[36:39], v[184:187], v[216:219]\n\t"
      "ds_read_b64 v[242:243], v246 offset:61440\n\t"
      "v_cmp_u_f32_e32 vcc, v212, v212\n\t"
      "s_cbranch_vccnz LA34R_%=\n\t"
      "LA34B_%=:\n\t"
      "v_mfma_f32_16x16x32_bf16 v[216:219], v[52:55], v[188:191], v[216:219]\n\t"
      "global_load_dwordx4 v[160:163], v250, s[76:77] offset:-4096 sc1\n\t"
      "v_mfma_f32_16x16x32_bf16 v[216:219], v[68:71], v[192:195], v[216:219]\n\t"
      "s_waitcnt lgkmcnt(4)\n\t"
      "v_add_f32_e32 v228, v228, v230\n\t"
      "v_add_f32_e32 v232, v232, v234\n\t"
      "v_mfma_f32_16x16x32_bf16 v[216:219], v[84:87], v[196:199], v[216:219]\n\t"
      "v_add_f32_e32 v229, v229, v231\n\t"
      "v_add_f32_e32 v233, v233, v235\n\t"
      "v_add_f32_e32 v228, v228, v232\n\t"
      "v_mfma_f32_16x16x32_bf16 v[216:219], v[100:103], v[200:203], v[216:219]\n\t"
      "v_add_f32_e32 v229, v229, v233\n\t"
      "s_waitcnt lgkmcnt(0)\n\t"
      "v_add_f32_e32 v236, v236, v238\n\t"
      "v_mfma_f32_16x16x32_bf16 v[216:219], v[116:119], v[204:207], v[216:219]\n\t"
      "v_add_f32_e32 v240, v240, v242\n\t"
      "v_add_f32_e32 v237, v237, v239\n\t"
      "v_add_f32_e32 v241, v241, v243\n\t"
      "ds_write_b128 v245, v[212:215]\n\t"
      "global_load_dwordx4 v[164:167], v250, s[76:77] offset:-3072 sc1\n\t"
      "v_mfma_f32_16x16x32_bf16 v[220:223], v[8:11], v[176:179], 0\n\t"
      "v_add_f32_e32 v228, v228, v248\n\t"
      "v_add_f32_e32 v236, v236, v240\n\t"
      "v_mfma_f32_16x16x32_bf16 v[220:223], v[24:27], v[180:183], v[220:223]\n\t"
      "v_add_f32_e32 v229, v229, v249\n\t"
      "v_add_f32_e32 v237, v237, v241\n\t"
      "v_add_f32_e32 v228, v228, v236\n\t"
      "v_mfma_f32_16x16x32_bf16 v[220:223], v[40:43], v[184:187], v[220:223]\n\t"
      "v_add_f32_e32 v229, v229, v237\n\t"
      "v_mul_f32_e32 v230, v228, v228\n\t"
      "v_mul_f32_e32 v231, v229, v229\n\t"
      "v_mfma_f32_16x16x32_bf16 v[220:223], v[56:59], v[188:191], v[220:223]\n\t"
      "v_fmaak_f32 v230, v252, v230, 0xc0135761\n\t"
      "v_fmaak_f32 v231, v252, v231, 0xc0135761\n\t"
      "v_mul_f32_e32 v230, v228, v230\n\t"
      "global_load_dwordx4 v[168:171], v250, s[76:77] offset:-2048 sc1\n\t"
      "v_mfma_f32_16x16x32_bf16 v[220:223], v[72:75], v[192:195], v[220:223]\n\t"
      "v_mul_f32_e32 v231, v229, v231\n\t"
      "v_exp_f32_e32 v230, v230\n\t"
      "v_exp_f32_e32 v231, v231\n\t"
      "v_mfma_f32_16x16x32_bf16 v[220:223], v[88:91], v[196:199], v[220:223]\n\t"
      "v_add_f32_e32 v230, 1.0, v230\n\t"
      "v_add_f32_e32 v231, 1.0, v231\n\t"
      "v_mfma_f32_16x16x32_bf16 v[220:223], v[104:107], v[200:203], v[220:223]\n\t"
      "v_rcp_f32_e32 v230, v230\n\t"
      "v_rcp_f32_e32 v231, v231\n\t"
      "v_mul_f32_e32 v228, v228, v230\n\t"
      "v_mfma_f32_16x16x32_bf16 v[220:223], v[120:123], v[204:207], v[220:223]\n\t"
      "v_mul_f32_e32 v229, v229, v231\n\t"
      "v_cvt_pk_bf16_f32 v230, v228, v229\n\t"
      "v_min_u32_e32 v254, -2, v230\n\t"
      "ds_write_b128 v253, v[216:219] offset:1024\n\t"
      "global_load_dwordx4 v[172:175], v250, s[76:77] offset:-1024 sc1\n\t"
      "v_mfma_f32_16x16x32_bf16 v[224:227], v[12:15], v[176:179], 0\n\t"
      "global_store_dword v247, v254, s[62:63] sc1\n\t"
      "s_add_u32 s62, s62, 57344\n\t"
      "s_addc_u32 s63, s63, 0\n\t"
      "s_setprio 0\n\t"
      "v_mfma_f32_16x16x32_bf16 v[224:227], v[28:31], v[180:183], v[224:227]\n\t"
      "v_mfma_f32_16x16x32_bf16 v[224:227], v[44:47], v[184:187], v[224:227]\n\t"
      "v_mfma_f32_16x16x32_bf16 v[224:227], v[60:63], v[188:191], v[224:227]\n\t"
      "global_load_dwordx4 v[176:179], v250, s[76:77] sc1\n\t"
      "v_mfma_f32_16x16x32_bf16 v[224:227], v[76:79], v[192:195], v[224:227]\n\t"
      "v_mfma_f32_16x16x32_bf16 v[224:227], v[92:95], v[196:199], v[224:227]\n\t"
      "v_mfma_f32_16x16x32_bf16 v[224:227], v[108:111], v[200:203], v[224:227]\n\t"
      "v_mfma_f32_16x16x32_bf16 v[224:227], v[124:127], v[204:207], v[224:227]\n\t"
      "ds_write_b128 v245, v[220:223] offset:2048\n\t"
      "global_load_dwordx4 v[180:183], v250, s[76:77] offset:1024 sc1\n\t"
      "s_waitcnt vmcnt(12)\n\t"
      "v_mfma_f32_16x16x32_bf16 v[208:211], v[0:3], v[128:131], 0\n\t"
      "v_mfma_f32_16x16x32_bf16 v[208:211], v[16:19], v[132:135], v[208:211]\n\t"
      "s_nop 2\n\t"
      "ds_write_b128 v253, v[224:227] offset:3072\n\t"
      "v_mfma_f32_16x16x32_bf16 v[208:211], v[32:35], v[136:139], v[208:211]\n\t"
      "v_mfma_f32_16x16x32_bf16 v[208:211], v[48:51], v[140:143], v[208:211]\n\t"
      "global_load_dwordx4 v[184:187], v250, s[76:77] offset:2048 sc1\n\t"
      "s_waitcnt lgkmcnt(0)\n\t"
      "s_barrier\n\t"
      "ds_read_b64 v[228:229], v246\n\t"
      "s_setprio 1\n\t"
      "s_waitcnt vmcnt(8)\n\t"
      "v_mfma_f32_16x16x32_bf16 v[208:211], v[64:67], v[144:147], v[208:211]\n\t"
      "ds_read_b64 v[230:231], v246 offset:4096\n\t"
      "v_mfma_f32_16x16x32_bf16 v[208:211], v[80:83], v[148:151], v[208:211]\n\t"
      "ds_read_b64 v[232:233], v246 offset:8192\n\t"
      "v_mfma_f32_16x16x32_bf16 v[208:211], v[96:99], v[152:155], v[208:211]\n\t"
      "ds_read_b64 v[234:235], v246 offset:12288\n\t"
      "v_mfma_f32_16x16x32_bf16 v[208:211], v[112:115], v[156:159], v[208:211]\n\t"
      "global_load_dwordx4 v[188:191], v250, s[76:77] offset:3072 sc1\n\t"
      "ds_read_b64 v[236:237], v246 offset:16384\n\t"
      "v_mfma_f32_16x16x32_bf16 v[212:215], v[4:7], v[128:131], 0\n\t"
      "ds_read_b64 v[238:239], v246 offset:20480\n\t"
      "v_mfma_f32_16x16x32_bf16 v[212:215], v[20:23], v[132:135], v[212:215]\n\t"
      "ds_read_b64 v[240:241], v246 offset:24576\n\t"
      "v_mfma_f32_16x16x32_bf16 v[212:215], v[36:39], v[136:139], v[212:215]\n\t"
      "ds_read_b64 v[242:243], v246 offset:28672\n\t"
      "v_cmp_u_f32_e32 vcc, v208, v208\n\t"
      "s_cbranch_vccnz LA35R_%=\n\t"
      "LA35B_%=:\n\t"
      "v_mfma_f32_16x16x32_bf16 v[212:215], v[52:55], v[140:143], v[212:215]\n\t"
      "v_mfma_f32_16x16x32_bf16 v[212:215], v[68:71], v[144:147], v[212:215]\n\t"
      "s_waitcnt lgkmcnt(4)\n\t"
      "v_add_f32_e32 v228, v228, v230\n\t"
      "v_add_f32_e32 v232, v232, v234\n\t"
      "v_mfma_f32_16x16x32_bf16 v[212:215], v[84:87], v[148:151], v[212:215]\n\t"
      "v_add_f32_e32 v229, v229, v231\n\t"
      "v_add_f32_e32 v233, v233, v235\n\t"
      "v_add_f32_e32 v228, v228, v232\n\t"
      "v_mfma_f32_16x16x32_bf16 v[212:215], v[100:103], v[152:155], v[212:215]\n\t"
      "v_add_f32_e32 v229, v229, v233\n\t"
      "s_waitcnt lgkmcnt(0)\n\t"
      "v_add_f32_e32 v236, v236, v238\n\t"
      "v_mfma_f32_16x16x32_bf16 v[212:215], v[116:119], v[156:159], v[212:215]\n\t"
      "v_add_f32_e32 v240, v240, v242\n\t"
      "v_add_f32_e32 v237, v237, v239\n\t"
      "v_add_f32_e32 v241, v241, v243\n\t"
      "ds_write_b128 v245, v[208:211] offset:32768\n\t"
      "v_mfma_f32_16x16x32_bf16 v[216:219], v[8:11], v[128:131], 0\n\t"
      "v_add_f32_e32 v228, v228, v248\n\t"
      "v_add_f32_e32 v236, v236, v240\n\t"
      "v_mfma_f32_16x16x32_bf16 v[216:219], v[24:27], v[132:135], v[216:219]\n\t"
      "v_add_f32_e32 v229, v229, v249\n\t"
      "v_add_f32_e32 v237, v237, v241\n\t"
      "v_add_f32_e32 v228, v228, v236\n\t"
      "v_mfma_f32_16x16x32_bf16 v[216:219], v[40:43], v[136:139], v[216:219]\n\t"
      "v_add_f32_e32 v229, v229, v237\n\t"
      "v_mul_f32_e32 v230, v228, v228\n\t"
      "v_mul_f32_e32 v231, v229, v229\n\t"
      "v_mfma_f32_16x16x32_bf16 v[216:219], v[56:59], v[140:143], v[216:219]\n\t"
      "v_fmaak_f32 v230, v252, v230, 0xc0135761\n\t"
      "v_fmaak_f32 v231, v252, v231, 0xc0135761\n\t"
      "v_mul_f32_e32 v230, v228, v230\n\t"
      "v_mfma_f32_16x16x32_bf16 v[216:219], v[72:75], v[144:147], v[216:219]\n\t"
      "v_mul_f32_e32 v231, v229, v231\n\t"
      "v_exp_f32_e32 v230, v230\n\t"
      "v_exp_f32_e32 v231, v231\n\t"
      "v_mfma_f32_16x16x32_bf16 v[216:219], v[88:91], v[148:151], v[216:219]\n\t"
      "v_add_f32_e32 v230, 1.0, v230\n\t"
      "v_add_f32_e32 v231, 1.0, v231\n\t"
      "v_mfma_f32_16x16x32_bf16 v[216:219], v[104:107], v[152:155], v[216:219]\n\t"
      "v_rcp_f32_e32 v230, v230\n\t"
      "v_rcp_f32_e32 v231, v231\n\t"
      "v_mul_f32_e32 v228, v228, v230\n\t"
      "v_mfma_f32_16x16x32_bf16 v[216:219], v[120:123], v[156:159], v[216:219]\n\t"
      "v_mul_f32_e32 v229, v229, v231\n\t"
      "v_cvt_pk_bf16_f32 v230, v228, v229\n\t"
      "v_min_u32_e32 v254, -2, v230\n\t"
      "ds_write_b128 v253, v[212:215] offset:33792\n\t"
      "v_mfma_f32_16x16x32_bf16 v[220:223], v[12:15], v[128:131], 0\n\t"
      "global_store_dword v247, v254, s[62:63] sc1\n\t"
      "s_add_u32 s62, s62, 57344\n\t"
      "s_addc_u32 s63, s63, 0\n\t"
      "s_setprio 0\n\t"
      "v_mfma_f32_16x16x32_bf16 v[220:223], v[28:31], v[132:135], v[220:223]\n\t"
      "v_mfma_f32_16x16x32_bf16 v[220:223], v[44:47], v[136:139], v[220:223]\n\t"
      "v_mfma_f32_16x16x32_bf16 v[220:223], v[60:63], v[140:143], v[220:223]\n\t"
      "v_mfma_f32_16x16x32_bf16 v[220:223], v[76:79], v[144:147], v[220:223]\n\t"
      "v_mfma_f32_16x16x32_bf16 v[220:223], v[92:95], v[148:151], v[220:223]\n\t"
      "v_mfma_f32_16x16x32_bf16 v[220:223], v[108:111], v[152:155], v[220:223]\n\t"
      "v_mfma_f32_16x16x32_bf16 v[220:223], v[124:127], v[156:159], v[220:223]\n\t"
      "ds_write_b128 v245, v[216:219] offset:34816\n\t"
      "s_waitcnt vmcnt(6)\n\t"
      "v_mfma_f32_16x16x32_bf16 v[224:227], v[0:3], v[160:163], 0\n\t"
      "v_mfma_f32_16x16x32_bf16 v[224:227], v[16:19], v[164:167], v[224:227]\n\t"
      "s_nop 3\n\t"
      "ds_write_b128 v253, v[220:223] offset:35840\n\t"
      "v_mfma_f32_16x16x32_bf16 v[224:227], v[32:35], v[168:171], v[224:227]\n\t"
      "v_mfma_f32_16x16x32_bf16 v[224:227], v[48:51], v[172:175], v[224:227]\n\t"
      "s_waitcnt lgkmcnt(0)\n\t"
      "s_barrier\n\t"
      "ds_read_b64 v[228:229], v246 offset:32768\n\t"
      "s_setprio 1\n\t"
      "s_waitcnt vmcnt(1)\n\t"
      "v_mfma_f32_16x16x32_bf16 v[224:227], v[64:67], v[176:179], v[224:227]\n\t"
      "ds_read_b64 v[230:231], v246 offset:36864\n\t"
      "v_mfma_f32_16x16x32_bf16 v[224:227], v[80:83], v[180:183], v[224:227]\n\t"
      "ds_read_b64 v[232:233], v246 offset:40960\n\t"
      "v_mfma_f32_16x16x32_bf16 v[224:227], v[96:99], v[184:187], v[224:227]\n\t"
      "ds_read_b64 v[234:235], v246 offset:45056\n\t"
      "v_mfma_f32_16x16x32_bf16 v[224:227], v[112:115], v[188:191], v[224:227]\n\t"
      "ds_read_b64 v[236:237], v246 offset:49152\n\t"
      "v_mfma_f32_16x16x32_bf16 v[208:211], v[4:7], v[160:163], 0\n\t"
      "ds_read_b64 v[238:239], v246 offset:53248\n\t"
      "v_mfma_f32_16x16x32_bf16 v[208:211], v[20:23], v[164:167], v[208:211]\n\t"
      "ds_read_b64 v[240:241], v246 offset:57344\n\t"
      "v_mfma_f32_16x16x32_bf16 v[208:211], v[36:39], v[168:171], v[208:211]\n\t"
      "ds_read_b64 v[242:243], v246 offset:61440\n\t"
      "s_nop 0\n\t"
      "v_cmp_u_f32_e32 vcc, v224, v224\n\t"
      "s_cbranch_vccnz LA36R_%=\n\t"
      "LA36B_%=:\n\t"
      "v_mfma_f32_16x16x32_bf16 v[208:211], v[52:55], v[172:175], v[208:211]\n\t"
      "v_mfma_f32_16x16x32_bf16 v[208:211], v[68:71], v[176:179], v[208:211]\n\t"
      "s_waitcnt lgkmcnt(4)\n\t"
      "v_add_f32_e32 v228, v228, v230\n\t"
      "v_add_f32_e32 v232, v232, v234\n\t"
      "v_mfma_f32_16x16x32_bf16 v[208:211], v[84:87], v[180:183], v[208:211]\n\t"
      "v_add_f32_e32 v229, v229, v231\n\t"
      "v_add_f32_e32 v233, v233, v235\n\t"
      "v_add_f32_e32 v228, v228, v232\n\t"
      "v_mfma_f32_16x16x32_bf16 v[208:211], v[100:103], v[184:187], v[208:211]\n\t"
      "v_add_f32_e32 v229, v229, v233\n\t"
      "s_waitcnt lgkmcnt(0)\n\t"
      "v_add_f32_e32 v236, v236, v238\n\t"
      "v_mfma_f32_16x16x32_bf16 v[208:211], v[116:119], v[188:191], v[208:211]\n\t"
      "v_add_f32_e32 v240, v240, v242\n\t"
      "v_add_f32_e32 v237, v237, v239\n\t"
      "v_add_f32_e32 v241, v241, v243\n\t"
      "ds_write_b128 v245, v[224:227]\n\t"
      "v_mfma_f32_16x16x32_bf16 v[212:215], v[8:11], v[160:163], 0\n\t"
      "v_add_f32_e32 v228, v228, v248\n\t"
      "v_add_f32_e32 v236, v236, v240\n\t"
      "v_mfma_f32_16x16x32_bf16 v[212:215], v[24:27], v[164:167], v[212:215]\n\t"
      "v_add_f32_e32 v229, v229, v249\n\t"
      "v_add_f32_e32 v237, v237, v241\n\t"
      "v_add_f32_e32 v228, v228, v236\n\t"
      "v_mfma_f32_16x16x32_bf16 v[212:215], v[40:43], v[168:171], v[212:215]\n\t"
      "v_add_f32_e32 v229, v229, v237\n\t"
      "v_mul_f32_e32 v230, v228, v228\n\t"
      "v_mul_f32_e32 v231, v229, v229\n\t"
      "v_mfma_f32_16x16x32_bf16 v[212:215], v[56:59], v[172:175], v[212:215]\n\t"
      "v_fmaak_f32 v230, v252, v230, 0xc0135761\n\t"
      "v_fmaak_f32 v231, v252, v231, 0xc0135761\n\t"
      "v_mul_f32_e32 v230, v228, v230\n\t"
      "v_mfma_f32_16x16x32_bf16 v[212:215], v[72:75], v[176:179], v[212:215]\n\t"
      "v_mul_f32_e32 v231, v229, v231\n\t"
      "v_exp_f32_e32 v230, v230\n\t"
      "v_exp_f32_e32 v231, v231\n\t"
      "v_mfma_f32_16x16x32_bf16 v[212:215], v[88:91], v[180:183], v[212:215]\n\t"
      "v_add_f32_e32 v230, 1.0, v230\n\t"
      "v_add_f32_e32 v231, 1.0, v231\n\t"
      "v_mfma_f32_16x16x32_bf16 v[212:215], v[104:107], v[184:187], v[212:215]\n\t"
      "v_rcp_f32_e32 v230, v230\n\t"
      "v_rcp_f32_e32 v231, v231\n\t"
      "v_mul_f32_e32 v228, v228, v230\n\t"
      "v_mfma_f32_16x16x32_bf16 v[212:215], v[120:123], v[188:191], v[212:215]\n\t"
      "v_mul_f32_e32 v229, v229, v231\n\t"
      "v_cvt_pk_bf16_f32 v230, v228, v229\n\t"
      "v_min_u32_e32 v254, -2, v230\n\t"
      "ds_write_b128 v253, v[208:211] offset:1024\n\t"
      "v_mfma_f32_16x16x32_bf16 v[216:219], v[12:15], v[160:163], 0\n\t"
      "global_store_dword v247, v254, s[62:63] sc1\n\t"
      "s_add_u32 s62, s62, 57344\n\t"
      "s_addc_u32 s63, s63, 0\n\t"
      "s_setprio 0\n\t"
      "v_mfma_f32_16x16x32_bf16 v[216:219], v[28:31], v[164:167], v[216:219]\n\t"
      "v_mfma_f32_16x16x32_bf16 v[216:219], v[44:47], v[168:171], v[216:219]\n\t"
      "v_mfma_f32_16x16x32_bf16 v[216:219], v[60:63], v[172:175], v[216:219]\n\t"
      "v_mfma_f32_16x16x32_bf16 v[216:219], v[76:79], v[176:179], v[216:219]\n\t"
      "v_mfma_f32_16x16x32_bf16 v[216:219], v[92:95], v[180:183], v[216:219]\n\t"
      "v_mfma_f32_16x16x32_bf16 v[216:219], v[108:111], v[184:187], v[216:219]\n\t"
      "v_mfma_f32_16x16x32_bf16 v[216:219], v[124:127], v[188:191], v[216:219]\n\t"
      "ds_write_b128 v245, v[212:215] offset:2048\n\t"
      "s_nop 6\n\t"
      "ds_write_b128 v253, v[216:219] offset:3072\n\t"
      "s_waitcnt lgkmcnt(0)\n\t"
      "s_barrier\n\t"
      "ds_read_b64 v[228:229], v246\n\t"
      "ds_read_b64 v[230:231], v246 offset:4096\n\t"
      "ds_read_b64 v[232:233], v246 offset:8192\n\t"
      "ds_read_b64 v[234:235], v246 offset:12288\n\t"
      "ds_read_b64 v[236:237], v246 offset:16384\n\t"
      "ds_read_b64 v[238:239], v246 offset:20480\n\t"
      "ds_read_b64 v[240:241], v246 offset:24576\n\t"
      "ds_read_b64 v[242:243], v246 offset:28672\n\t"
      "s_waitcnt lgkmcnt(4)\n\t"
      "v_add_f32_e32 v228, v228, v230\n\t"
      "v_add_f32_e32 v232, v232, v234\n\t"
      "v_add_f32_e32 v229, v229, v231\n\t"
      "v_add_f32_e32 v233, v233, v235\n\t"
      "v_add_f32_e32 v228, v228, v232\n\t"
      "v_add_f32_e32 v229, v229, v233\n\t"
      "s_waitcnt lgkmcnt(0)\n\t"
      "v_add_f32_e32 v236, v236, v238\n\t"
      "v_add_f32_e32 v240, v240, v242\n\t"
      "v_add_f32_e32 v237, v237, v239\n\t"
      "v_add_f32_e32 v241, v241, v243\n\t"
      "v_add_f32_e32 v228, v228, v248\n\t"
      "v_add_f32_e32 v236, v236, v240\n\t"
      "v_add_f32_e32 v229, v229, v249\n\t"
      "v_add_f32_e32 v237, v237, v241\n\t"
      "v_add_f32_e32 v228, v228, v236\n\t"
      "v_add_f32_e32 v229, v229, v237\n\t"
      "v_mul_f32_e32 v230, v228, v228\n\t"
      "v_mul_f32_e32 v231, v229, v229\n\t"
      "v_fmaak_f32 v230, v252, v230, 0xc0135761\n\t"
      "v_fmaak_f32 v231, v252, v231, 0xc0135761\n\t"
      "v_mul_f32_e32 v230, v228, v230\n\t"
      "v_mul_f32_e32 v231, v229, v231\n\t"
      "v_exp_f32_e32 v230, v230\n\t"
      "v_exp_f32_e32 v231, v231\n\t"
      "v_add_f32_e32 v230, 1.0, v230\n\t"
      "v_add_f32_e32 v231, 1.0, v231\n\t"
      "v_rcp_f32_e32 v230, v230\n\t"
      "v_rcp_f32_e32 v231, v231\n\t"
      "v_mul_f32_e32 v228, v228, v230\n\t"
      "v_mul_f32_e32 v229, v229, v231\n\t"
      "v_cvt_pk_bf16_f32 v230, v228, v229\n\t"
      "v_min_u32_e32 v254, -2, v230\n\t"
      "global_store_dword v247, v254, s[62:63] sc1\n\t"
      "s_add_u32 s62, s62, 57344\n\t"
      "s_addc_u32 s63, s63, 0\n\t"
      "s_or_b32 s34, s2, s24\n\t"
      "s_cbranch_scc1 EPB_%=\n\t"
      "v_mov_b32_e32 v228, s22\n\t"
      "v_mov_b32_e32 v229, s23\n\t"
      "v_mov_b32_e32 v230, 0\n\t"
      "s_mov_b64 exec, 1\n\t"
      "global_store_dwordx2 v230, v[228:229], s[60:61] sc1\n\t"
      "s_mov_b64 exec, -1\n\t"
      "EPB_%=:\n\t"
      "s_endpgm\n\t"
      "LPATH_%=:\n\t"
      "s_add_u32 s22, s22, 1\n\t"
      "s_addc_u32 s23, s23, 0\n\t"
      "s_add_u32 s72, s26, 1\n\t"
      "s_and_b32 s72, s72, 6\n\t"
      "s_cselect_b32 s72, 0, 2\n\t"
      "s_or_b32 s27, s27, s72\n\t"
      "s_mul_i32 s72, s81, 172032\n\t"
      "s_add_u32 s72, s72, s37\n\t"
      "s_add_u32 s40, s6, s72\n\t"
      "s_addc_u32 s41, s7, 0\n\t"
      "s_add_u32 s81, s81, 1\n\t"
      "s_cmp_eq_u32 s81, 7\n\t"
      "s_cselect_b32 s81, 0, s81\n\t"
      "s_add_u32 m0, s39, 81920\n\t"
      "s_nop 0\n\t"
      "global_load_lds_dwordx4 v148, s[40:41]\n\t"
      "global_load_lds_dwordx4 v149, s[40:41] offset:1024\n\t"
      "global_load_lds_dwordx4 v150, s[40:41] offset:2048\n\t"
      "global_load_lds_dwordx4 v151, s[40:41] offset:3072\n\t"
      "s_mul_i32 s72, s81, 172032\n\t"
      "s_add_u32 s72, s72, s37\n\t"
      "s_add_u32 s40, s6, s72\n\t"
      "s_addc_u32 s41, s7, 0\n\t"
      "s_add_u32 s81, s81, 1\n\t"
      "s_cmp_eq_u32 s81, 7\n\t"
      "s_cselect_b32 s81, 0, s81\n\t"
      "s_add_u32 m0, s39, 98304\n\t"
      "s_nop 0\n\t"
      "global_load_lds_dwordx4 v148, s[40:41]\n\t"
      "global_load_lds_dwordx4 v149, s[40:41] offset:1024\n\t"
      "s_waitcnt vmcnt(14)\n\t"
      "s_barrier\n\t"
      "v_add_u32_e32 v172, 0x10000, v224\n\t"
      "v_add_u32_e32 v173, 0x10000, v225\n\t"
      "v_add_u32_e32 v174, 0x10000, v226\n\t"
      "v_add_u32_e32 v175, 0x10000, v227\n\t"
      "s_lshl_b32 s34, s25, 8\n\t"
      "s_add_u32 s34, s34, 4096\n\t"
      "v_and_b32_e32 v136, 1, v192\n\t"
      "v_lshl_add_u32 v136, v136, 7, s34\n\t"
      "s_lshl_b32 s34, s25, 7\n\t"
      "s_add_u32 s34, s34, 16384\n\t"
      "v_mov_b32_e32 v137, s34\n\t"
      "s_lshl_b32 s34, s2, 7\n\t"
      "s_add_u32 s34, s34, 32768\n\t"
      "v_mov_b32_e32 v143, s34\n\t"
      "s_bitcmp0_b32 s27, 1\n\t"
      "s_cbranch_scc1 SK6_%=\n\t"
      "s_lshr_b32 s73, s26, 2\n\t"
      "s_lshl1_add_u32 s73, s25, s73\n\t"
      "s_lshl_b32 s72, s73, 7\n\t"
      "s_add_u32 s72, s72, 4096\n\t"
      "v_mov_b32_e32 v130, s72\n\t"
      "s_lshl2_add_u32 s75, s73, s24\n\t"
      "v_mov_b32_e32 v128, s22\n\t"
      "v_mov_b32_e32 v129, s23\n\t"
      "v_lshrrev_b32_e32 v140, 1, v244\n\t"
      "s_lshl_b32 s72, s75, 9\n\t"
      "s_add_u32 s72, s72, 12582912\n\t"
      "v_mov_b32_e32 v132, -1\n\t"
      "v_mov_b32_e32 v133, -1\n\t"
      "v_mov_b32_e32 v134, -1\n\t"
      "v_mov_b32_e32 v135, -1\n\t"
      "s_add_u32 s42, s20, s72\n\t"
      "s_addc_u32 s43, s21, 0\n\t"
      "global_store_dwordx2 v140, v[132:133], s[42:43] sc1\n\t"
      "s_lshl_b32 s72, s75, 13\n\t"
      "s_add_u32 s72, s72, 4096\n\t"
      "s_add_u32 s42, s20, s72\n\t"
      "s_addc_u32 s43, s21, 0\n\t"
      "global_store_dwordx4 v244, v[132:135], s[42:43] offset:-4096 sc1\n\t"
      "global_store_dwordx4 v244, v[132:135], s[42:43] offset:-3072 sc1\n\t"
      "global_store_dwordx4 v244, v[132:135], s[42:43] offset:-2048 sc1\n\t"
      "SK6_%=:\n\t"
      "s_waitcnt vmcnt(10)\n\t"
      "s_barrier\n\t"
      "global_load_lds_dwordx4 v150, s[40:41] offset:2048\n\t"
      "global_load_lds_dwordx4 v151, s[40:41] offset:3072\n\t"
      "s_bitcmp0_b32 s27, 1\n\t"
      "s_cbranch_scc1 SK7_%=\n\t"
      "global_store_dwordx4 v244, v[132:135], s[42:43] offset:-1024 sc1\n\t"
      "global_store_dwordx4 v244, v[132:135], s[42:43] sc1\n\t"
      "global_store_dwordx4 v244, v[132:135], s[42:43] offset:1024 sc1\n\t"
      "SK7_%=:\n\t"
      "s_waitcnt vmcnt(8)\n\t"
      "s_barrier\n\t"
      "s_mul_i32 s72, s81, 172032\n\t"
      "s_add_u32 s72, s72, s37\n\t"
      "s_add_u32 s40, s6, s72\n\t"
      "s_addc_u32 s41, s7, 0\n\t"
      "s_add_u32 s81, s81, 1\n\t"
      "s_cmp_eq_u32 s81, 7\n\t"
      "s_cselect_b32 s81, 0, s81\n\t"
      "s_add_u32 m0, s39, 114688\n\t"
      "s_nop 0\n\t"
      "global_load_lds_dwordx4 v148, s[40:41]\n\t"
      "global_load_lds_dwordx4 v149, s[40:41] offset:1024\n\t"
      "global_load_lds_dwordx4 v150, s[40:41] offset:2048\n\t"
      "global_load_lds_dwordx4 v151, s[40:41] offset:3072\n\t"
      "s_bitcmp0_b32 s27, 1\n\t"
      "s_cbranch_scc1 SK8_%=\n\t"
      "global_store_dwordx4 v244, v[132:135], s[42:43] offset:2048 sc1\n\t"
      "global_store_dwordx4 v244, v[132:135], s[42:43] offset:3072 sc1\n\t"
      "SK8_%=:\n\t"
      "s_waitcnt vmcnt(8)\n\t"
      "s_barrier\n\t"
      "s_mul_i32 s72, s81, 172032\n\t"
      "s_add_u32 s72, s72, s37\n\t"
      "s_add_u32 s40, s6, s72\n\t"
      "s_addc_u32 s41, s7, 0\n\t"
      "s_add_u32 s81, s81, 1\n\t"
      "s_cmp_eq_u32 s81, 7\n\t"
      "s_cselect_b32 s81, 0, s81\n\t"
      "s_add_u32 m0, s39, 131072\n\t"
      "s_nop 0\n\t"
      "global_load_lds_dwordx4 v148, s[40:41]\n\t"
      "global_load_lds_dwordx4 v149, s[40:41] offset:1024\n\t"
      "global_load_lds_dwordx4 v150, s[40:41] offset:2048\n\t"
      "global_load_lds_dwordx4 v151, s[40:41] offset:3072\n\t"
      "s_waitcnt vmcnt(8)\n\t"
      "s_bitcmp0_b32 s27, 1\n\t"
      "s_cbranch_scc1 SK9_%=\n\t"
      "s_waitcnt vmcnt(4)\n\t"
      "SK9_%=:\n\t"
      "s_barrier\n\t"
      "s_mul_i32 s72, s81, 172032\n\t"
      "s_add_u32 s72, s72, s37\n\t"
      "s_add_u32 s40, s6, s72\n\t"
      "s_addc_u32 s41, s7, 0\n\t"
      "s_add_u32 s81, s81, 1\n\t"
      "s_cmp_eq_u32 s81, 7\n\t"
      "s_cselect_b32 s81, 0, s81\n\t"
      "s_add_u32 m0, s39, 147456\n\t"
      "s_nop 0\n\t"
      "global_load_lds_dwordx4 v148, s[40:41]\n\t"
      "global_load_lds_dwordx4 v149, s[40:41] offset:1024\n\t"
      "global_load_lds_dwordx4 v150, s[40:41] offset:2048\n\t"
      "global_load_lds_dwordx4 v151, s[40:41] offset:3072\n\t"
      "s_bitcmp0_b32 s27, 1\n\t"
      "s_cbranch_scc1 SK10_%=\n\t"
      "s_cmp_eq_u32 s24, 0\n\t"
      "s_cselect_b64 exec, 1, 0\n\t"
      "global_store_dwordx2 v130, v[128:129], s[60:61] sc1\n\t"
      "s_mov_b64 exec, -1\n\t"
      "s_mov_b32 m0, 64512\n\t"
      "s_mul_i32 s72, s75, 13\n\t"
      "v_min_u32_e32 v141, 12, v192\n\t"
      "v_add_u32_e32 v141, s72, v141\n\t"
      "v_min_u32_e32 v141, 3135, v141\n\t"
      "v_lshlrev_b32_e32 v141, 7, v141\n\t"
      "global_load_lds_dword v141, s[8:9]\n\t"
      "SK10_%=:\n\t"
      "s_waitcnt vmcnt(8)\n\t"
      "s_barrier\n\t"
      "s_mul_i32 s72, s82, 172032\n\t"
      "s_add_u32 s72, s72, s37\n\t"
      "s_add_u32 s72, s72, 1792\n\t"
      "s_add_u32 s40, s6, s72\n\t"
      "s_addc_u32 s41, s7, 0\n\t"
      "s_add_u32 s82, s82, 1\n\t"
      "s_cmp_eq_u32 s82, 7\n\t"
      "s_cselect_b32 s82, 0, s82\n\t"
      "s_add_u32 m0, s39, 65536\n\t"
      "s_nop 0\n\t"
      "global_load_lds_dwordx4 v148, s[40:41]\n\t"
      "global_load_lds_dwordx4 v152, s[40:41] offset:1024\n\t"
      "s_mul_i32 s72, s82, 172032\n\t"
      "s_add_u32 s72, s72, s37\n\t"
      "s_add_u32 s72, s72, 1792\n\t"
      "s_add_u32 s40, s6, s72\n\t"
      "s_addc_u32 s41, s7, 0\n\t"
      "s_add_u32 s82, s82, 1\n\t"
      "s_cmp_eq_u32 s82, 7\n\t"
      "s_cselect_b32 s82, 0, s82\n\t"
      "s_add_u32 m0, s39, 67584\n\t"
      "s_nop 0\n\t"
      "global_load_lds_dwordx4 v148, s[40:41]\n\t"
      "global_load_lds_dwordx4 v152, s[40:41] offset:1024\n\t"
      "s_waitcnt vmcnt(8)\n\t"
      "s_barrier\n\t"
      "s_mul_i32 s72, s82, 172032\n\t"
      "s_add_u32 s72, s72, s37\n\t"
      "s_add_u32 s72, s72, 1792\n\t"
      "s_add_u32 s40, s6, s72\n\t"
      "s_addc_u32 s41, s7, 0\n\t"
      "s_add_u32 s82, s82, 1\n\t"
      "s_cmp_eq_u32 s82, 7\n\t"
      "s_cselect_b32 s82, 0, s82\n\t"
      "s_add_u32 m0, s39, 81920\n\t"
      "s_nop 0\n\t"
      "global_load_lds_dwordx4 v148, s[40:41]\n\t"
      "global_load_lds_dwordx4 v152, s[40:41] offset:1024\n\t"
      "s_mul_i32 s72, s82, 172032\n\t"
      "s_add_u32 s72, s72, s37\n\t"
      "s_add_u32 s72, s72, 1792\n\t"
      "s_add_u32 s40, s6, s72\n\t"
      "s_addc_u32 s41, s7, 0\n\t"
      "s_add_u32 s82, s82, 1\n\t"
      "s_cmp_eq_u32 s82, 7\n\t"
      "s_cselect_b32 s82, 0, s82\n\t"
      "s_add_u32 m0, s39, 83968\n\t"
      "s_nop 0\n\t"
      "global_load_lds_dwordx4 v148, s[40:41]\n\t"
      "global_load_lds_dwordx4 v152, s[40:41] offset:1024\n\t"
      "s_waitcnt vmcnt(8)\n\t"
      "s_barrier\n\t"
      "s_cmp_lg_u32 s24, 3\n\t"
      "s_cbranch_scc1 SK11_%=\n\t"
      "global_load_dwordx2 v[138:139], v136, s[60:61] sc1\n\t"
      "SK11_%=:\n\t"
      "s_mul_i32 s72, s82, 172032\n\t"
      "s_add_u32 s72, s72, s37\n\t"
      "s_add_u32 s72, s72, 1792\n\t"
      "s_add_u32 s40, s6, s72\n\t"
      "s_addc_u32 s41, s7, 0\n\t"
      "s_add_u32 s82, s82, 1\n\t"
      "s_cmp_eq_u32 s82, 7\n\t"
      "s_cselect_b32 s82, 0, s82\n\t"
      "s_lshr_b32 s72, s39, 1\n\t"
      "s_add_u32 m0, s72, 98304\n\t"
      "s_nop 0\n\t"
      "global_load_lds_dwordx4 v148, s[40:41]\n\t"
      "global_load_lds_dwordx4 v152, s[40:41] offset:1024\n\t"
      "global_load_dword v249, v248, s[12:13]\n\t"
      "s_lshl2_add_u32 s72, s31, s24\n\t"
      "s_and_b32 s73, s72, 0x70\n\t"
      "s_add_u32 s72, s72, s73\n\t"
      "s_mov_b32 m0, 64512\n\t"
      "s_mul_i32 s72, s72, 28672\n\t"
      "s_and_b32 s73, s2, 7\n\t"
      "s_mul_i32 s73, s73, 448\n\t"
      "s_and_b32 s73, s73, 0xffffff80\n\t"
      "s_add_u32 s72, s72, s73\n\t"
      "v_bfe_u32 v141, v192, 2, 3\n\t"
      "s_cmp_ge_u32 s31, 28\n\t"
      "s_cselect_b32 s73, 32, 0\n\t"
      "v_and_b32_e32 v142, s73, v192\n\t"
      "v_lshl_or_b32 v141, v142, 2, v141\n\t"
      "v_mul_u32_u24_e32 v141, 3584, v141\n\t"
      "v_lshl_add_u32 v141, v196, 7, v141\n\t"
      "v_add_u32_e32 v141, s72, v141\n\t"
      "global_load_lds_dword v141, s[10:11]\n\t"
      "s_waitcnt vmcnt(8)\n\t"
      "s_barrier\n\t"
      "s_mul_i32 s72, s24, 14336\n\t"
      "s_add_u32 s40, s56, s72\n\t"
      "s_addc_u32 s41, s57, 0\n\t"
      "s_lshr_b32 s72, s39, 1\n\t"
      "s_add_u32 m0, s72, 106496\n\t"
      "s_nop 0\n\t"
      "global_load_lds_dwordx4 v154, s[40:41]\n\t"
      "global_load_lds_dwordx4 v155, s[40:41] offset:1024\n\t"
      "s_waitcnt vmcnt(6)\n\t"
      "s_barrier\n\t"
      "s_mul_i32 s72, s24, 28672\n\t"
      "s_add_u32 s72, s72, 57344\n\t"
      "s_add_u32 s40, s56, s72\n\t"
      "s_addc_u32 s41, s57, 0\n\t"
      "s_lshr_b32 s72, s39, 0\n\t"
      "s_add_u32 m0, s72, 114688\n\t"
      "s_nop 0\n\t"
      "global_load_lds_dwordx4 v154, s[40:41]\n\t"
      "global_load_lds_dwordx4 v155, s[40:41] offset:1024\n\t"
      "global_load_lds_dwordx4 v156, s[40:41] offset:2048\n\t"
      "global_load_lds_dwordx4 v157, s[40:41] offset:3072\n\t"
      "s_waitcnt vmcnt(8)\n\t"
      "s_cmp_lg_u32 s24, 3\n\t"
      "s_cbranch_scc1 LF1B_%=\n\t"
      "v_cmp_ne_u64_e32 vcc, s[22:23], v[138:139]\n\t"
      "s_cbranch_vccnz LF1R_%=\n\t"
      "LF1B_%=:\n\t"
      "s_barrier\n\t"
      "s_mul_i32 s72, s24, 28672\n\t"
      "s_add_u32 s72, s72, 172032\n\t"
      "s_add_u32 s40, s56, s72\n\t"
      "s_addc_u32 s41, s57, 0\n\t"
      "s_lshr_b32 s72, s39, 0\n\t"
      "s_add_u32 m0, s72, 131072\n\t"
      "s_nop 0\n\t"
      "global_load_lds_dwordx4 v154, s[40:41]\n\t"
      "global_load_lds_dwordx4 v155, s[40:41] offset:1024\n\t"
      "global_load_lds_dwordx4 v156, s[40:41] offset:2048\n\t"
      "global_load_lds_dwordx4 v157, s[40:41] offset:3072\n\t"
      "s_mul_i32 s72, s24, 28672\n\t"
      "s_add_u32 s72, s72, 286720\n\t"
      "s_add_u32 s40, s56, s72\n\t"
      "s_addc_u32 s41, s57, 0\n\t"
      "s_lshr_b32 s72, s39, 0\n\t"
      "s_add_u32 m0, s72, 147456\n\t"
      "s_nop 0\n\t"
      "global_load_lds_dwordx4 v154, s[40:41]\n\t"
      "global_load_lds_dwordx4 v155, s[40:41] offset:1024\n\t"
      "global_load_lds_dwordx4 v156, s[40:41] offset:2048\n\t"
      "global_load_lds_dwordx4 v157, s[40:41] offset:3072\n\t"
      "s_sub_u32 s72, s31, 14\n\t"
      "s_cmp_ge_u32 s72, 14\n\t"
      "s_cbranch_scc1 NTB_%=\n\t"
      "s_lshl2_add_u32 s72, s31, s24\n\t"
      "s_sub_u32 s72, s72, 56\n\t"
      "s_mov_b32 m0, 64512\n\t"
      "s_lshl1_add_u32 s72, s72, 0\n\t"
      "s_mul_i32 s72, s72, 65536\n\t"
      "s_and_b32 s73, s2, 7\n\t"
      "s_mul_i32 s73, s73, 512\n\t"
      "s_and_b32 s73, s73, 0xffffff80\n\t"
      "s_add_u32 s72, s72, s73\n\t"
      "v_bfe_u32 v141, v192, 2, 3\n\t"
      "v_mul_u32_u24_e32 v141, 4096, v141\n\t"
      "v_lshl_add_u32 v141, v196, 7, v141\n\t"
      "v_add_u32_e32 v141, s72, v141\n\t"
      "global_load_lds_dword v141, s[14:15]\n\t"
      "NTB_%=:\n\t"
      "s_barrier\n\t"
      "s_cmp_eq_u32 s26, 7\n\t"
      "s_cbranch_scc1 SPARE_%=\n\t"
      "s_lshl_b32 s72, s24, 10\n\t"
      "s_add_u32 s72, s72, 98304\n\t"
      "v_xor_b32_e32 v203, v197, v198\n\t"
      "v_lshl_add_u32 v203, v203, 4, s72\n\t"
      "v_lshl_add_u32 v203, v197, 7, v203\n\t"
      "ds_write_b128 v203, v[204:207]\n\t"
      "s_waitcnt lgkmcnt(0)\n\t"
      "s_bitcmp0_b32 s27, 0\n\t"
      "s_cbranch_scc1 L4_%=\n\t"
      "s_add_u32 s72, s76, 0\n\t"
      "s_add_u32 s40, s52, s72\n\t"
      "s_addc_u32 s41, s53, 0\n\t"
      "s_add_u32 m0, s39, 65536\n\t"
      "s_nop 0\n\t"
      "global_load_lds_dwordx4 v144, s[40:41]\n\t"
      "global_load_lds_dwordx4 v145, s[40:41] offset:1024\n\t"
      "global_load_lds_dwordx4 v146, s[40:41] offset:2048\n\t"
      "global_load_lds_dwordx4 v147, s[40:41] offset:3072\n\t"
      "s_add_u32 s72, s76, 229376\n\t"
      "s_add_u32 s40, s52, s72\n\t"
      "s_addc_u32 s41, s53, 0\n\t"
      "s_add_u32 m0, s39, 81920\n\t"
      "s_nop 0\n\t"
      "global_load_lds_dwordx4 v144, s[40:41]\n\t"
      "global_load_lds_dwordx4 v145, s[40:41] offset:1024\n\t"
      "global_load_lds_dwordx4 v146, s[40:41] offset:2048\n\t"
      "global_load_lds_dwordx4 v147, s[40:41] offset:3072\n\t"
      "s_waitcnt vmcnt(8)\n\t"
      "s_barrier\n\t"
      "s_barrier\n\t"
      "s_add_u32 s72, s76, 3670016\n\t"
      "s_add_u32 s40, s52, s72\n\t"
      "s_addc_u32 s41, s53, 0\n\t"
      "s_add_u32 m0, s39, 16384\n\t"
      "s_nop 0\n\t"
      "global_load_lds_dwordx4 v144, s[40:41]\n\t"
      "global_load_lds_dwordx4 v145, s[40:41] offset:1024\n\t"
      "global_load_lds_dwordx4 v146, s[40:41] offset:2048\n\t"
      "global_load_lds_dwordx4 v147, s[40:41] offset:3072\n\t"
      "s_add_u32 s72, s76, 3899392\n\t"
      "s_add_u32 s40, s52, s72\n\t"
      "s_addc_u32 s41, s53, 0\n\t"
      "s_add_u32 m0, s39, 49152\n\t"
      "s_nop 0\n\t"
      "global_load_lds_dwordx4 v144, s[40:41]\n\t"
      "global_load_lds_dwordx4 v145, s[40:41] offset:1024\n\t"
      "global_load_lds_dwordx4 v146, s[40:41] offset:2048\n\t"
      "global_load_lds_dwordx4 v147, s[40:41] offset:3072\n\t"
      "s_waitcnt vmcnt(12)\n\t"
      "ds_read_b64_tr_b16 v[0:1], v172\n\t"
      "ds_read_b64_tr_b16 v[2:3], v172 offset:512\n\t"
      "ds_read_b64_tr_b16 v[4:5], v173\n\t"
      "ds_read_b64_tr_b16 v[6:7], v173 offset:512\n\t"
      "ds_read_b64_tr_b16 v[8:9], v174\n\t"
      "ds_read_b64_tr_b16 v[10:11], v174 offset:512\n\t"
      "ds_read_b64_tr_b16 v[12:13], v175\n\t"
      "ds_read_b64_tr_b16 v[14:15], v175 offset:512\n\t"
      "s_waitcnt lgkmcnt(0)\n\t"
      "s_add_u32 s72, s77, 0\n\t"
      "s_add_u32 s40, s52, s72\n\t"
      "s_addc_u32 s41, s53, 0\n\t"
      "s_add_u32 m0, s39, 65536\n\t"
      "s_nop 0\n\t"
      "global_load_lds_dwordx4 v144, s[40:41]\n\t"
      "global_load_lds_dwordx4 v145, s[40:41] offset:1024\n\t"
      "global_load_lds_dwordx4 v146, s[40:41] offset:2048\n\t"
      "global_load_lds_dwordx4 v147, s[40:41] offset:3072\n\t"
      "s_waitcnt vmcnt(12)\n\t"
      "ds_read_b64_tr_b16 v[16:17], v172 offset:16384\n\t"
      "ds_read_b64_tr_b16 v[18:19], v172 offset:16896\n\t"
      "ds_read_b64_tr_b16 v[20:21], v173 offset:16384\n\t"
      "ds_read_b64_tr_b16 v[22:23], v173 offset:16896\n\t"
      "ds_read_b64_tr_b16 v[24:25], v174 offset:16384\n\t"
      "ds_read_b64_tr_b16 v[26:27], v174 offset:16896\n\t"
      "ds_read_b64_tr_b16 v[28:29], v175 offset:16384\n\t"
      "ds_read_b64_tr_b16 v[30:31], v175 offset:16896\n\t"
      "s_waitcnt lgkmcnt(0)\n\t"
      "s_add_u32 s72, s77, 229376\n\t"
      "s_add_u32 s40, s52, s72\n\t"
      "s_addc_u32 s41, s53, 0\n\t"
      "s_add_u32 m0, s39, 81920\n\t"
      "s_nop 0\n\t"
      "global_load_lds_dwordx4 v144, s[40:41]\n\t"
      "global_load_lds_dwordx4 v145, s[40:41] offset:1024\n\t"
      "global_load_lds_dwordx4 v146, s[40:41] offset:2048\n\t"
      "global_load_lds_dwordx4 v147, s[40:41] offset:3072\n\t"
      "s_add_u32 s72, s77, 3670016\n\t"
      "s_add_u32 s40, s52, s72\n\t"
      "s_addc_u32 s41, s53, 0\n\t"
      "s_add_u32 m0, s39, 0\n\t"
      "s_nop 0\n\t"
      "global_load_lds_dwordx4 v144, s[40:41]\n\t"
      "global_load_lds_dwordx4 v145, s[40:41] offset:1024\n\t"
      "global_load_lds_dwordx4 v146, s[40:41] offset:2048\n\t"
      "global_load_lds_dwordx4 v147, s[40:41] offset:3072\n\t"
      "s_add_u32 s72, s77, 3899392\n\t"
      "s_add_u32 s40, s52, s72\n\t"
      "s_addc_u32 s41, s53, 0\n\t"
      "s_add_u32 m0, s39, 32768\n\t"
      "s_nop 0\n\t"
      "global_load_lds_dwordx4 v144, s[40:41]\n\t"
      "global_load_lds_dwordx4 v145, s[40:41] offset:1024\n\t"
      "global_load_lds_dwordx4 v146, s[40:41] offset:2048\n\t"
      "global_load_lds_dwordx4 v147, s[40:41] offset:3072\n\t"
      "s_waitcnt vmcnt(12)\n\t"
      "ds_read_b64_tr_b16 v[32:33], v172\n\t"
      "ds_read_b64_tr_b16 v[34:35], v172 offset:512\n\t"
      "ds_read_b64_tr_b16 v[36:37], v173\n\t"
      "ds_read_b64_tr_b16 v[38:39], v173 offset:512\n\t"
      "ds_read_b64_tr_b16 v[40:41], v174\n\t"
      "ds_read_b64_tr_b16 v[42:43], v174 offset:512\n\t"
      "ds_read_b64_tr_b16 v[44:45], v175\n\t"
      "ds_read_b64_tr_b16 v[46:47], v175 offset:512\n\t"
      "s_waitcnt lgkmcnt(0)\n\t"
      "s_add_u32 s72, s78, 0\n\t"
      "s_add_u32 s40, s52, s72\n\t"
      "s_addc_u32 s41, s53, 0\n\t"
      "s_add_u32 m0, s39, 65536\n\t"
      "s_nop 0\n\t"
      "global_load_lds_dwordx4 v144, s[40:41]\n\t"
      "global_load_lds_dwordx4 v145, s[40:41] offset:1024\n\t"
      "global_load_lds_dwordx4 v146, s[40:41] offset:2048\n\t"
      "global_load_lds_dwordx4 v147, s[40:41] offset:3072\n\t"
      "s_waitcnt vmcnt(12)\n\t"
      "ds_read_b64_tr_b16 v[48:49], v172 offset:16384\n\t"
      "ds_read_b64_tr_b16 v[50:51], v172 offset:16896\n\t"
      "ds_read_b64_tr_b16 v[52:53], v173 offset:16384\n\t"
      "ds_read_b64_tr_b16 v[54:55], v173 offset:16896\n\t"
      "ds_read_b64_tr_b16 v[56:57], v174 offset:16384\n\t"
      "ds_read_b64_tr_b16 v[58:59], v174 offset:16896\n\t"
      "ds_read_b64_tr_b16 v[60:61], v175 offset:16384\n\t"
      "ds_read_b64_tr_b16 v[62:63], v175 offset:16896\n\t"
      "s_waitcnt lgkmcnt(0)\n\t"
      "s_add_u32 s72, s78, 229376\n\t"
      "s_add_u32 s40, s52, s72\n\t"
      "s_addc_u32 s41, s53, 0\n\t"
      "s_add_u32 m0, s39, 81920\n\t"
      "s_nop 0\n\t"
      "global_load_lds_dwordx4 v144, s[40:41]\n\t"
      "global_load_lds_dwordx4 v145, s[40:41] offset:1024\n\t"
      "global_load_lds_dwordx4 v146, s[40:41] offset:2048\n\t"
      "global_load_lds_dwordx4 v147, s[40:41] offset:3072\n\t"
      "s_waitcnt vmcnt(8)\n\t"
      "s_barrier\n\t"
      "s_cmp_lg_u32 s24, 3\n\t"
      "s_cbranch_scc1 SK13_%=\n\t"
      "global_load_dwordx2 v[138:139], v137, s[60:61] sc1\n\t"
      "SK13_%=:\n\t"
      "s_add_u32 s72, s79, 0\n\t"
      "s_add_u32 s40, s52, s72\n\t"
      "s_addc_u32 s41, s53, 0\n\t"
      "s_add_u32 m0, s39, 98304\n\t"
      "s_nop 0\n\t"
      "global_load_lds_dwordx4 v144, s[40:41]\n\t"
      "global_load_lds_dwordx4 v145, s[40:41] offset:1024\n\t"
      "global_load_lds_dwordx4 v146, s[40:41] offset:2048\n\t"
      "global_load_lds_dwordx4 v147, s[40:41] offset:3072\n\t"
      "s_add_u32 s72, s79, 229376\n\t"
      "s_add_u32 s40, s52, s72\n\t"
      "s_addc_u32 s41, s53, 0\n\t"
      "s_add_u32 m0, s39, 114688\n\t"
      "s_nop 0\n\t"
      "global_load_lds_dwordx4 v144, s[40:41]\n\t"
      "global_load_lds_dwordx4 v145, s[40:41] offset:1024\n\t"
      "global_load_lds_dwordx4 v146, s[40:41] offset:2048\n\t"
      "global_load_lds_dwordx4 v147, s[40:41] offset:3072\n\t"
      "s_cmp_lg_u32 s24, 3\n\t"
      "s_cbranch_scc1 LF2B_%=\n\t"
      "s_waitcnt vmcnt(8)\n\t"
      "v_cmp_ne_u64_e32 vcc, s[22:23], v[138:139]\n\t"
      "s_cbranch_vccnz LF2R_%=\n\t"
      "LF2B_%=:\n\t"
      "s_barrier\n\t"
      "s_add_u32 s72, s78, 3670016\n\t"
      "s_add_u32 s40, s52, s72\n\t"
      "s_addc_u32 s41, s53, 0\n\t"
      "s_add_u32 m0, s39, 16384\n\t"
      "s_nop 0\n\t"
      "global_load_lds_dwordx4 v144, s[40:41]\n\t"
      "global_load_lds_dwordx4 v145, s[40:41] offset:1024\n\t"
      "global_load_lds_dwordx4 v146, s[40:41] offset:2048\n\t"
      "global_load_lds_dwordx4 v147, s[40:41] offset:3072\n\t"
      "s_add_u32 s72, s78, 3899392\n\t"
      "s_add_u32 s40, s52, s72\n\t"
      "s_addc_u32 s41, s53, 0\n\t"
      "s_add_u32 m0, s39, 49152\n\t"
      "s_nop 0\n\t"
      "global_load_lds_dwordx4 v144, s[40:41]\n\t"
      "global_load_lds_dwordx4 v145, s[40:41] offset:1024\n\t"
      "global_load_lds_dwordx4 v146, s[40:41] offset:2048\n\t"
      "global_load_lds_dwordx4 v147, s[40:41] offset:3072\n\t"
      "s_add_u32 s72, s79, 3670016\n\t"
      "s_add_u32 s40, s52, s72\n\t"
      "s_addc_u32 s41, s53, 0\n\t"
      "s_add_u32 m0, s39, 0\n\t"
      "s_nop 0\n\t"
      "global_load_lds_dwordx4 v144, s[40:41]\n\t"
      "global_load_lds_dwordx4 v145, s[40:41] offset:1024\n\t"
      "global_load_lds_dwordx4 v146, s[40:41] offset:2048\n\t"
      "global_load_lds_dwordx4 v147, s[40:41] offset:3072\n\t"
      "s_add_u32 s72, s79, 3899392\n\t"
      "s_add_u32 s40, s52, s72\n\t"
      "s_addc_u32 s41, s53, 0\n\t"
      "s_add_u32 m0, s39, 32768\n\t"
      "s_nop 0\n\t"
      "global_load_lds_dwordx4 v144, s[40:41]\n\t"
      "global_load_lds_dwordx4 v145, s[40:41] offset:1024\n\t"
      "global_load_lds_dwordx4 v146, s[40:41] offset:2048\n\t"
      "global_load_lds_dwordx4 v147, s[40:41] offset:3072\n\t"
      "s_cmp_lg_u32 s24, 3\n\t"
      "s_cbranch_scc1 SK14_%=\n\t"
      "global_load_dwordx2 v[138:139], v143, s[60:61] sc1\n\t"
      "SK14_%=:\n\t"
      "s_waitcnt vmcnt(28)\n\t"
      "ds_read_b64_tr_b16 v[64:65], v172\n\t"
      "ds_read_b64_tr_b16 v[66:67], v172 offset:512\n\t"
      "ds_read_b64_tr_b16 v[68:69], v173\n\t"
      "ds_read_b64_tr_b16 v[70:71], v173 offset:512\n\t"
      "ds_read_b64_tr_b16 v[72:73], v174\n\t"
      "ds_read_b64_tr_b16 v[74:75], v174 offset:512\n\t"
      "ds_read_b64_tr_b16 v[76:77], v175\n\t"
      "ds_read_b64_tr_b16 v[78:79], v175 offset:512\n\t"
      "s_waitcnt vmcnt(24)\n\t"
      "ds_read_b64_tr_b16 v[80:81], v172 offset:16384\n\t"
      "ds_read_b64_tr_b16 v[82:83], v172 offset:16896\n\t"
      "ds_read_b64_tr_b16 v[84:85], v173 offset:16384\n\t"
      "ds_read_b64_tr_b16 v[86:87], v173 offset:16896\n\t"
      "ds_read_b64_tr_b16 v[88:89], v174 offset:16384\n\t"
      "ds_read_b64_tr_b16 v[90:91], v174 offset:16896\n\t"
      "ds_read_b64_tr_b16 v[92:93], v175 offset:16384\n\t"
      "ds_read_b64_tr_b16 v[94:95], v175 offset:16896\n\t"
      "s_waitcnt vmcnt(20)\n\t"
      "ds_read_b64_tr_b16 v[96:97], v172 offset:32768\n\t"
      "ds_read_b64_tr_b16 v[98:99], v172 offset:33280\n\t"
      "ds_read_b64_tr_b16 v[100:101], v173 offset:32768\n\t"
      "ds_read_b64_tr_b16 v[102:103], v173 offset:33280\n\t"
      "ds_read_b64_tr_b16 v[104:105], v174 offset:32768\n\t"
      "ds_read_b64_tr_b16 v[106:107], v174 offset:33280\n\t"
      "ds_read_b64_tr_b16 v[108:109], v175 offset:32768\n\t"
      "ds_read_b64_tr_b16 v[110:111], v175 offset:33280\n\t"
      "s_waitcnt vmcnt(16)\n\t"
      "ds_read_b64_tr_b16 v[112:113], v172 offset:49152\n\t"
      "ds_read_b64_tr_b16 v[114:115], v172 offset:49664\n\t"
      "ds_read_b64_tr_b16 v[116:117], v173 offset:49152\n\t"
      "ds_read_b64_tr_b16 v[118:119], v173 offset:49664\n\t"
      "ds_read_b64_tr_b16 v[120:121], v174 offset:49152\n\t"
      "ds_read_b64_tr_b16 v[122:123], v174 offset:49664\n\t"
      "ds_read_b64_tr_b16 v[124:125], v175 offset:49152\n\t"
      "ds_read_b64_tr_b16 v[126:127], v175 offset:49664\n\t"
      "s_waitcnt vmcnt(0)\n\t"
      "s_cmp_lg_u32 s24, 3\n\t"
      "s_cbranch_scc1 LF3B_%=\n\t"
      "s_waitcnt vmcnt(0)\n\t"
      "v_cmp_ne_u64_e32 vcc, s[22:23], v[138:139]\n\t"
      "s_cbranch_vccnz LF3R_%=\n\t"
      "LF3B_%=:\n\t"
      "s_waitcnt lgkmcnt(0)\n\t"
      "s_barrier\n\t"
      "s_mul_i32 s34, s32, 196608\n\t"
      "s_mul_i32 s35, s24, 8192\n\t"
      "s_add_u32 s34, s34, s35\n\t"
      "s_add_u32 s34, s34, 1048576\n\t"
      "s_add_u32 s64, s48, s34\n\t"
      "s_addc_u32 s65, s49, 0\n\t"
      "v_add_u32_e32 v250, 4096, v244\n\t"
      "global_load_dwordx4 v[128:131], v250, s[64:65] offset:-4096 sc1\n\t"
      "global_load_dwordx4 v[132:135], v250, s[64:65] offset:-3072 sc1\n\t"
      "global_load_dwordx4 v[136:139], v250, s[64:65] offset:-2048 sc1\n\t"
      "global_load_dwordx4 v[140:143], v250, s[64:65] offset:-1024 sc1\n\t"
      "global_load_dwordx4 v[144:147], v250, s[64:65] sc1\n\t"
      "global_load_dwordx4 v[148:151], v250, s[64:65] offset:1024 sc1\n\t"
      "global_load_dwordx4 v[152:155], v250, s[64:65] offset:2048 sc1\n\t"
      "global_load_dwordx4 v[156:159], v250, s[64:65] offset:3072 sc1\n\t"
      "s_waitcnt vmcnt(0) lgkmcnt(0)\n\t"
      "s_branch GST_%=\n\t"
      "G4C_%=:\n\t"
      "global_store_dwordx4 v241, v[208:211], s[20:21] offset:-2048 sc1\n\t"
      "global_store_dwordx4 v241, v[212:215], s[20:21] offset:2048 sc1\n\t"
      "s_setprio 0\n\t"
      "G4_%=:\n\t"
      "s_mul_i32 s34, s32, 401408\n\t"
      "s_mul_i32 s35, s24, 7168\n\t"
      "s_add_u32 s34, s34, s35\n\t"
      "s_add_u32 s34, s34, 1835008\n\t"
      "s_add_u32 s64, s48, s34\n\t"
      "s_addc_u32 s65, s49, 0\n\t"
      "s_and_b32 s34, s30, 3\n\t"
      "s_lshl_b32 s34, s34, 11\n\t"
      "s_add_u32 s40, s34, 0\n\t"
      "s_sub_u32 s35, s40, 7168\n\t"
      "s_min_u32 s40, s40, s35\n\t"
      "s_add_u32 s41, s34, 1024\n\t"
      "s_sub_u32 s35, s41, 7168\n\t"
      "s_min_u32 s41, s41, s35\n\t"
      "s_add_u32 s42, s34, 2048\n\t"
      "s_sub_u32 s35, s42, 7168\n\t"
      "s_min_u32 s42, s42, s35\n\t"
      "s_add_u32 s43, s34, 3072\n\t"
      "s_sub_u32 s35, s43, 7168\n\t"
      "s_min_u32 s43, s43, s35\n\t"
      "s_add_u32 s44, s34, 4096\n\t"
      "s_sub_u32 s35, s44, 7168\n\t"
      "s_min_u32 s44, s44, s35\n\t"
      "s_add_u32 s45, s34, 5120\n\t"
      "s_sub_u32 s35, s45, 7168\n\t"
      "s_min_u32 s45, s45, s35\n\t"
      "s_add_u32 s46, s34, 6144\n\t"
      "s_sub_u32 s35, s46, 7168\n\t"
      "s_min_u32 s46, s46, s35\n\t"
      "s_add_u32 s66, s64, 57344\n\t"
      "s_addc_u32 s67, s65, 0\n\t"
      "s_add_u32 s68, s66, 57344\n\t"
      "s_addc_u32 s69, s67, 0\n\t"
      "s_add_u32 s70, s68, 57344\n\t"
      "s_addc_u32 s71, s69, 0\n\t"
      "s_add_u32 s72, s70, 57344\n\t"
      "s_addc_u32 s73, s71, 0\n\t"
      "s_add_u32 s74, s72, 57344\n\t"
      "s_addc_u32 s75, s73, 0\n\t"
      "s_add_u32 s76, s74, 57344\n\t"
      "s_addc_u32 s77, s75, 0\n\t"
      "v_lshrrev_b32_e32 v240, 7, v244\n\t"
      "v_and_b32_e32 v241, 0x70, v244\n\t"
      "v_and_b32_e32 v242, 6, v240\n\t"
      "v_lshlrev_b32_e32 v242, 4, v242\n\t"
      "v_xor_b32_e32 v242, v242, v241\n\t"
      "v_lshl_or_b32 v228, v240, 12, v242\n\t"
      "s_mul_i32 s34, s24, 917504\n\t"
      "s_lshl_b32 s35, s29, 7\n\t"
      "s_add_u32 s34, s34, s35\n\t"
      "s_lshl_b32 s39, s24, 12\n\t"
      "s_add_u32 s54, s14, s34\n\t"
      "s_addc_u32 s55, s15, 0\n\t"
      "v_add_u32_e32 v229, 31744, v228\n\t"
      "v_add_u32_e32 v230, 63488, v228\n\t"
      "v_add_u32_e32 v231, 95232, v228\n\t"
      "s_lshl_b32 s37, s40, 7\n\t"
      "s_add_u32 s58, s54, s37\n\t"
      "s_addc_u32 s59, s55, 0\n\t"
      "s_add_u32 m0, s39, 0\n\t"
      "s_nop 0\n\t"
      "global_load_lds_dwordx4 v228, s[58:59]\n\t"
      "global_load_lds_dwordx4 v229, s[58:59] offset:1024\n\t"
      "global_load_lds_dwordx4 v230, s[58:59] offset:2048\n\t"
      "global_load_lds_dwordx4 v231, s[58:59] offset:3072\n\t"
      "s_lshl_b32 s37, s41, 7\n\t"
      "s_add_u32 s58, s54, s37\n\t"
      "s_addc_u32 s59, s55, 0\n\t"
      "s_add_u32 m0, s39, 32768\n\t"
      "s_nop 0\n\t"
      "global_load_lds_dwordx4 v228, s[58:59]\n\t"
      "global_load_lds_dwordx4 v229, s[58:59] offset:1024\n\t"
      "global_load_lds_dwordx4 v230, s[58:59] offset:2048\n\t"
      "global_load_lds_dwordx4 v231, s[58:59] offset:3072\n\t"
      "s_lshr_b32 s34, s24, 1\n\t"
      "s_lshl2_add_u32 s35, s29, s34\n\t"
      "s_and_b32 s36, s24, 1\n\t"
      "s_lshl4_add_u32 s35, s35, s36\n\t"
      "v_lshl_add_u32 v242, v240, 1, s35\n\t"
      "v_lshlrev_b32_e32 v248, 1, v242\n\t"
      "global_load_ushort v249, v248, s[16:17]\n\t"
      "s_movk_i32 s35, 896\n\t"
      "v_and_b32_e32 v247, 64, v244\n\t"
      "v_lshrrev_b32_e32 v247, 2, v247\n\t"
      "v_mad_u32_u24 v247, v242, s35, v247\n\t"
      "v_and_b32_e32 v242, 0x30, v244\n\t"
      "v_lshl_add_u32 v247, v242, 1, v247\n\t"
      "s_mov_b32 s82, 0x77777777\n\t"
      "s_mov_b32 s83, 0x77777777\n\t"
      "s_lshl_b32 s35, s32, 7\n\t"
      "s_add_u32 s62, s18, s35\n\t"
      "s_addc_u32 s63, s19, 0\n\t"
      "s_mul_i32 s35, s32, 96\n\t"
      "s_add_u32 s35, s35, 512\n\t"
      "s_add_u32 s18, s18, s35\n\t"
      "s_addc_u32 s19, s19, 0\n\t"
      "s_mul_i32 s35, s29, 28\n\t"
      "s_lshl2_add_u32 s35, s32, s35\n\t"
      "s_lshl1_add_u32 s35, s35, s36\n\t"
      "s_lshl2_add_u32 s35, s35, s34\n\t"
      "s_lshl_b32 s35, s35, 8\n\t"
      "s_add_u32 s35, s35, 6295552\n\t"
      "s_add_u32 s50, s20, s35\n\t"
      "s_addc_u32 s51, s21, 0\n\t"
      "v_lshrrev_b32_e32 v248, 2, v244\n\t"
      "v_bfe_u32 v242, v244, 8, 1\n\t"
      "v_lshlrev_b32_e32 v242, 4, v242\n\t"
      "v_xor_b32_e32 v245, v244, v242\n\t"
      "v_bfe_u32 v242, v244, 9, 1\n\t"
      "v_lshlrev_b32_e32 v242, 7, v242\n\t"
      "v_xor_b32_e32 v245, v245, v242\n\t"
      "s_add_u32 s35, s39, 98304\n\t"
      "v_add_u32_e32 v245, s35, v245\n\t"
      "v_lshl_or_b32 v246, v240, 1, s36\n\t"
      "v_bfe_u32 v242, v244, 5, 1\n\t"
      "v_xor_b32_e32 v246, v246, v242\n\t"
      "v_bfe_u32 v242, v244, 6, 1\n\t"
      "v_lshlrev_b32_e32 v242, 3, v242\n\t"
      "v_xor_b32_e32 v246, v246, v242\n\t"
      "v_lshlrev_b32_e32 v246, 4, v246\n\t"
      "v_and_b32_e32 v242, 0x60, v244\n\t"
      "v_lshl_or_b32 v246, v242, 3, v246\n\t"
      "v_bfe_u32 v242, v244, 4, 1\n\t"
      "v_lshl_or_b32 v246, v242, 3, v246\n\t"
      "s_lshl_b32 s34, s34, 10\n\t"
      "s_add_u32 s34, s34, 98304\n\t"
      "v_add_u32_e32 v246, s34, v246\n\t"
      "v_and_b32_e32 v240, 0x3c0, v244\n\t"
      "v_lshlrev_b32_e32 v240, 1, v240\n\t"
      "v_and_b32_e32 v241, 0x180, v244\n\t"
      "v_lshrrev_b32_e32 v241, 2, v241\n\t"
      "v_and_b32_e32 v242, 0x30, v244\n\t"
      "v_lshrrev_b32_e32 v242, 1, v242\n\t"
      "v_or3_b32 v232, v240, v241, v242\n\t"
      "v_add_u32_e32 v232, s39, v232\n\t"
      "v_min_u32_e32 v252, 432, v244\n\t"
      "v_lshlrev_b32_e32 v252, 4, v252\n\t"
      "v_xor_b32_e32 v233, 32, v232\n\t"
      "v_xor_b32_e32 v234, 64, v232\n\t"
      "v_xor_b32_e32 v235, 96, v232\n\t"
      "v_xor_b32_e32 v236, 65536, v232\n\t"
      "v_xor_b32_e32 v237, 65568, v232\n\t"
      "v_xor_b32_e32 v238, 65600, v232\n\t"
      "v_xor_b32_e32 v239, 65632, v232\n\t"
      "v_min_u32_e32 v240, 48, v244\n\t"
      "v_mul_u32_u24_e32 v240, 7, v240\n\t"
      "s_lshl_b32 s34, s29, 4\n\t"
      "v_add_u32_e32 v240, s34, v240\n\t"
      "v_subrev_u32_e32 v241, 448, v240\n\t"
      "v_min_u32_e32 v240, v240, v241\n\t"
      "v_subrev_u32_e32 v241, 448, v240\n\t"
      "v_min_u32_e32 v240, v240, v241\n\t"
      "s_and_b32 s34, s29, 7\n\t"
      "s_lshl_b32 s34, s34, 8\n\t"
      "v_lshl_add_u32 v253, v240, 7, s34\n\t"
      "s_sub_u32 s52, s64, 0\n\t"
      "s_subb_u32 s53, s65, 0\n\t"
      "v_add_u32_e32 v143, 32768, v143\n\t"
      "s_barrier\n\t"
      "s_lshl_b32 s37, s42, 7\n\t"
      "s_add_u32 s58, s54, s37\n\t"
      "s_addc_u32 s59, s55, 0\n\t"
      "s_add_u32 m0, s39, 65536\n\t"
      "s_nop 0\n\t"
      "global_load_lds_dwordx4 v228, s[58:59]\n\t"
      "global_load_lds_dwordx4 v229, s[58:59] offset:1024\n\t"
      "global_load_lds_dwordx4 v230, s[58:59] offset:2048\n\t"
      "global_load_lds_dwordx4 v231, s[58:59] offset:3072\n\t"
      "s_lshl_b32 s37, s43, 7\n\t"
      "s_add_u32 s58, s54, s37\n\t"
      "s_addc_u32 s59, s55, 0\n\t"
      "s_add_u32 m0, s39, 98304\n\t"
      "s_nop 0\n\t"
      "global_load_lds_dwordx4 v228, s[58:59]\n\t"
      "global_load_lds_dwordx4 v229, s[58:59] offset:1024\n\t"
      "global_load_lds_dwordx4 v230, s[58:59] offset:2048\n\t"
      "global_load_lds_dwordx4 v231, s[58:59] offset:3072\n\t"
      "s_waitcnt vmcnt(13)\n\t"
      "ds_read_b64_tr_b16 v[0:1], v232\n\t"
      "ds_read_b64_tr_b16 v[2:3], v232 offset:2048\n\t"
      "ds_read_b64_tr_b16 v[4:5], v233\n\t"
      "ds_read_b64_tr_b16 v[6:7], v233 offset:2048\n\t"
      "ds_read_b64_tr_b16 v[8:9], v234\n\t"
      "ds_read_b64_tr_b16 v[10:11], v234 offset:2048\n\t"
      "ds_read_b64_tr_b16 v[12:13], v235\n\t"
      "ds_read_b64_tr_b16 v[14:15], v235 offset:2048\n\t"
      "s_waitcnt lgkmcnt(0)\n\t"
      "s_lshl_b32 s37, s44, 7\n\t"
      "s_add_u32 s58, s54, s37\n\t"
      "s_addc_u32 s59, s55, 0\n\t"
      "s_add_u32 m0, s39, 0\n\t"
      "s_nop 0\n\t"
      "global_load_lds_dwordx4 v228, s[58:59]\n\t"
      "global_load_lds_dwordx4 v229, s[58:59] offset:1024\n\t"
      "global_load_lds_dwordx4 v230, s[58:59] offset:2048\n\t"
      "global_load_lds_dwordx4 v231, s[58:59] offset:3072\n\t"
      "s_waitcnt vmcnt(13)\n\t"
      "ds_read_b64_tr_b16 v[16:17], v232 offset:32768\n\t"
      "ds_read_b64_tr_b16 v[18:19], v232 offset:34816\n\t"
      "ds_read_b64_tr_b16 v[20:21], v233 offset:32768\n\t"
      "ds_read_b64_tr_b16 v[22:23], v233 offset:34816\n\t"
      "ds_read_b64_tr_b16 v[24:25], v234 offset:32768\n\t"
      "ds_read_b64_tr_b16 v[26:27], v234 offset:34816\n\t"
      "ds_read_b64_tr_b16 v[28:29], v235 offset:32768\n\t"
      "ds_read_b64_tr_b16 v[30:31], v235 offset:34816\n\t"
      "s_cmp_lg_u32 s24, 3\n\t"
      "s_cbranch_scc1 SK18_%=\n\t"
      "global_load_dwordx2 v[138:139], v143, s[60:61] sc1\n\t"
      "SK18_%=:\n\t"
      "s_waitcnt lgkmcnt(0)\n\t"
      "s_lshl_b32 s37, s45, 7\n\t"
      "s_add_u32 s58, s54, s37\n\t"
      "s_addc_u32 s59, s55, 0\n\t"
      "s_add_u32 m0, s39, 32768\n\t"
      "s_nop 0\n\t"
      "global_load_lds_dwordx4 v228, s[58:59]\n\t"
      "global_load_lds_dwordx4 v229, s[58:59] offset:1024\n\t"
      "global_load_lds_dwordx4 v230, s[58:59] offset:2048\n\t"
      "global_load_lds_dwordx4 v231, s[58:59] offset:3072\n\t"
      "s_waitcnt vmcnt(12)\n\t"
      "ds_read_b64_tr_b16 v[32:33], v236\n\t"
      "ds_read_b64_tr_b16 v[34:35], v236 offset:2048\n\t"
      "ds_read_b64_tr_b16 v[36:37], v237\n\t"
      "ds_read_b64_tr_b16 v[38:39], v237 offset:2048\n\t"
      "ds_read_b64_tr_b16 v[40:41], v238\n\t"
      "ds_read_b64_tr_b16 v[42:43], v238 offset:2048\n\t"
      "ds_read_b64_tr_b16 v[44:45], v239\n\t"
      "ds_read_b64_tr_b16 v[46:47], v239 offset:2048\n\t"
      "s_waitcnt lgkmcnt(0)\n\t"
      "s_lshl_b32 s37, s46, 7\n\t"
      "s_add_u32 s58, s54, s37\n\t"
      "s_addc_u32 s59, s55, 0\n\t"
      "s_add_u32 m0, s39, 65536\n\t"
      "s_nop 0\n\t"
      "global_load_lds_dwordx4 v228, s[58:59]\n\t"
      "global_load_lds_dwordx4 v229, s[58:59] offset:1024\n\t"
      "global_load_lds_dwordx4 v230, s[58:59] offset:2048\n\t"
      "global_load_lds_dwordx4 v231, s[58:59] offset:3072\n\t"
      "s_waitcnt vmcnt(12)\n\t"
      "ds_read_b64_tr_b16 v[48:49], v236 offset:32768\n\t"
      "ds_read_b64_tr_b16 v[50:51], v236 offset:34816\n\t"
      "ds_read_b64_tr_b16 v[52:53], v237 offset:32768\n\t"
      "ds_read_b64_tr_b16 v[54:55], v237 offset:34816\n\t"
      "ds_read_b64_tr_b16 v[56:57], v238 offset:32768\n\t"
      "ds_read_b64_tr_b16 v[58:59], v238 offset:34816\n\t"
      "ds_read_b64_tr_b16 v[60:61], v239 offset:32768\n\t"
      "ds_read_b64_tr_b16 v[62:63], v239 offset:34816\n\t"
      "s_waitcnt vmcnt(8)\n\t"
      "ds_read_b64_tr_b16 v[64:65], v232\n\t"
      "ds_read_b64_tr_b16 v[66:67], v232 offset:2048\n\t"
      "ds_read_b64_tr_b16 v[68:69], v233\n\t"
      "ds_read_b64_tr_b16 v[70:71], v233 offset:2048\n\t"
      "ds_read_b64_tr_b16 v[72:73], v234\n\t"
      "ds_read_b64_tr_b16 v[74:75], v234 offset:2048\n\t"
      "ds_read_b64_tr_b16 v[76:77], v235\n\t"
      "ds_read_b64_tr_b16 v[78:79], v235 offset:2048\n\t"
      "s_cmp_lg_u32 s24, 3\n\t"
      "s_cbranch_scc1 LF5B_%=\n\t"
      "s_waitcnt vmcnt(8)\n\t"
      "v_cmp_ne_u64_e32 vcc, s[22:23], v[138:139]\n\t"
      "s_cbranch_vccnz LF5R_%=\n\t"
      "LF5B_%=:\n\t"
      "s_barrier\n\t"
      "s_cmp_lg_u32 s24, 3\n\t"
      "s_cbranch_scc1 HF4_%=\n\t"
      "s_lshl3_add_u32 s34, s25, s26\n\t"
      "s_lshl_b32 s34, s34, 7\n\t"
      "s_add_u32 s34, s34, 98304\n\t"
      "v_mov_b32_e32 v146, s34\n\t"
      "v_mov_b32_e32 v144, s22\n\t"
      "v_mov_b32_e32 v145, s23\n\t"
      "s_mov_b64 exec, 1\n\t"
      "global_store_dwordx2 v146, v[144:145], s[60:61] sc1\n\t"
      "s_mov_b64 exec, -1\n\t"
      "HF4_%=:\n\t"
      "s_cmp_lg_u32 s24, 0\n\t"
      "s_cbranch_scc1 PG0_%=\n\t"
      "global_load_dword v251, v253, s[52:53] offset:252 sc1\n\t"
      "PG0_%=:\n\t"
      "s_waitcnt vmcnt(4)\n\t"
      "ds_read_b64_tr_b16 v[80:81], v232 offset:32768\n\t"
      "ds_read_b64_tr_b16 v[82:83], v232 offset:34816\n\t"
      "ds_read_b64_tr_b16 v[84:85], v233 offset:32768\n\t"
      "ds_read_b64_tr_b16 v[86:87], v233 offset:34816\n\t"
      "ds_read_b64_tr_b16 v[88:89], v234 offset:32768\n\t"
      "ds_read_b64_tr_b16 v[90:91], v234 offset:34816\n\t"
      "ds_read_b64_tr_b16 v[92:93], v235 offset:32768\n\t"
      "ds_read_b64_tr_b16 v[94:95], v235 offset:34816\n\t"
      "s_waitcnt vmcnt(0)\n\t"
      "ds_read_b64_tr_b16 v[96:97], v236\n\t"
      "ds_read_b64_tr_b16 v[98:99], v236 offset:2048\n\t"
      "ds_read_b64_tr_b16 v[100:101], v237\n\t"
      "ds_read_b64_tr_b16 v[102:103], v237 offset:2048\n\t"
      "ds_read_b64_tr_b16 v[104:105], v238\n\t"
      "ds_read_b64_tr_b16 v[106:107], v238 offset:2048\n\t"
      "ds_read_b64_tr_b16 v[108:109], v239\n\t"
      "ds_read_b64_tr_b16 v[110:111], v239 offset:2048\n\t"
      "v_lshlrev_b32_e32 v249, 16, v249\n\t"
      "s_and_b32 s34, s29, 30\n\t"
      "s_lshl_b32 s34, s34, 10\n\t"
      "s_add_u32 s34, s34, 98304\n\t"
      "v_bfe_u32 v143, v244, 5, 2\n\t"
      "v_add_u32_e32 v143, 3, v143\n\t"
      "v_writelane_b32 v143, s32, 0\n\t"
      "v_writelane_b32 v143, s32, 1\n\t"
      "v_lshl_add_u32 v143, v143, 7, s34\n\t"
      "v_and_b32_e32 v142, 16, v244\n\t"
      "v_lshl_add_u32 v143, v142, 6, v143\n\t"
      "s_waitcnt vmcnt(0) lgkmcnt(0)\n\t"
      "s_cmp_lg_u32 s24, 0\n\t"
      "s_cbranch_scc1 PGB_%=\n\t"
      "v_cmp_eq_u32_e32 vcc, -1, v251\n\t"
      "s_cbranch_vccnz PGR_%=\n\t"
      "PGB_%=:\n\t"
      "s_barrier\n\t"
      "s_cmp_lg_u32 s24, 3\n\t"
      "s_cbranch_scc1 SK19_%=\n\t"
      "global_load_dwordx2 v[240:241], v143, s[60:61] sc1\n\t"
      "SK19_%=:\n\t"
      "v_mov_b32_e32 v242, v143\n\t"
      "s_add_u32 s58, s64, s40\n\t"
      "s_addc_u32 s59, s65, 0\n\t"
      "global_load_dwordx4 v[128:131], v244, s[58:59] sc1\n\t"
      "s_add_u32 s58, s64, s41\n\t"
      "s_addc_u32 s59, s65, 0\n\t"
      "global_load_dwordx4 v[132:135], v244, s[58:59] sc1\n\t"
      "s_add_u32 s58, s64, s42\n\t"
      "s_addc_u32 s59, s65, 0\n\t"
      "global_load_dwordx4 v[136:139], v244, s[58:59] sc1\n\t"
      "s_add_u32 s58, s64, s43\n\t"
      "s_addc_u32 s59, s65, 0\n\t"
      "global_load_dwordx4 v[140:143], v244, s[58:59] sc1\n\t"
      "s_add_u32 s58, s64, s44\n\t"
      "s_addc_u32 s59, s65, 0\n\t"
      "global_load_dwordx4 v[144:147], v244, s[58:59] sc1\n\t"
      "s_add_u32 s58, s64, s45\n\t"
      "s_addc_u32 s59, s65, 0\n\t"
      "global_load_dwordx4 v[148:151], v244, s[58:59] sc1\n\t"
      "s_add_u32 s58, s64, s46\n\t"
      "s_addc_u32 s59, s65, 0\n\t"
      "global_load_dwordx4 v[152:155], v244, s[58:59] sc1\n\t"
      "v_mov_b32_e32 v254, v249\n\t"
      "s_waitcnt vmcnt(6)\n\t"
      "v_mfma_f32_16x16x32_bf16 v[196:199], v[128:131], v[0:3], 0\n\t"
      "v_mfma_f32_16x16x32_bf16 v[200:203], v[128:131], v[4:7], 0\n\t"
      "v_mfma_f32_16x16x32_bf16 v[204:207], v[128:131], v[8:11], 0\n\t"
      "v_mfma_f32_16x16x32_bf16 v[208:211], v[128:131], v[12:15], 0\n\t"
      "s_waitcnt vmcnt(5)\n\t"
      "v_mfma_f32_16x16x32_bf16 v[196:199], v[132:135], v[16:19], v[196:199]\n\t"
      "v_mfma_f32_16x16x32_bf16 v[200:203], v[132:135], v[20:23], v[200:203]\n\t"
      "v_mfma_f32_16x16x32_bf16 v[204:207], v[132:135], v[24:27], v[204:207]\n\t"
      "s_cmp_lg_u32 s24, 3\n\t"
      "s_cbranch_scc1 LF8B_%=\n\t"
      "v_cmp_ne_u64_e32 vcc, s[22:23], v[240:241]\n\t"
      "s_cbranch_vccnz LF8R_%=\n\t"
      "LF8B_%=:\n\t"
      "s_barrier\n\t"
      "v_mfma_f32_16x16x32_bf16 v[208:211], v[132:135], v[28:31], v[208:211]\n\t"
      "global_load_dword v115, v248, s[50:51] offset:-4096 sc1\n\t"
      "global_load_dword v116, v248, s[50:51] offset:-2048 sc1\n\t"
      "s_waitcnt vmcnt(6)\n\t"
      "v_mfma_f32_16x16x32_bf16 v[196:199], v[136:139], v[32:35], v[196:199]\n\t"
      "global_load_dword v117, v248, s[50:51] sc1\n\t"
      "global_load_dword v118, v248, s[50:51] offset:2048 sc1\n\t"
      "v_mfma_f32_16x16x32_bf16 v[200:203], v[136:139], v[36:39], v[200:203]\n\t"
      "s_sub_u32 s34, 16, s32\n\t"
      "s_lshl_b32 s34, s34, 11\n\t"
      "s_add_u32 s50, s50, s34\n\t"
      "s_addc_u32 s51, s51, 0\n\t"
      "global_load_dword v112, v248, s[50:51] offset:-4096 sc1\n\t"
      "global_load_dword v113, v248, s[50:51] offset:-2048 sc1\n\t"
      "v_mfma_f32_16x16x32_bf16 v[204:207], v[136:139], v[40:43], v[204:207]\n\t"
      "global_load_dword v114, v248, s[50:51] sc1\n\t"
      "s_add_u32 s58, s66, s40\n\t"
      "s_addc_u32 s59, s67, 0\n\t"
      "global_load_dwordx4 v[156:159], v244, s[58:59] sc1\n\t"
      "v_mfma_f32_16x16x32_bf16 v[208:211], v[136:139], v[44:47], v[208:211]\n\t"
      "s_waitcnt vmcnt(11)\n\t"
      "v_mfma_f32_16x16x32_bf16 v[196:199], v[140:143], v[48:51], v[196:199]\n\t"
      "v_mfma_f32_16x16x32_bf16 v[200:203], v[140:143], v[52:55], v[200:203]\n\t"
      "v_mfma_f32_16x16x32_bf16 v[204:207], v[140:143], v[56:59], v[204:207]\n\t"
      "s_add_u32 s58, s66, s41\n\t"
      "s_addc_u32 s59, s67, 0\n\t"
      "global_load_dwordx4 v[160:163], v244, s[58:59] sc1\n\t"
      "v_mfma_f32_16x16x32_bf16 v[208:211], v[140:143], v[60:63], v[208:211]\n\t"
      "s_waitcnt vmcnt(11)\n\t"
      "v_mfma_f32_16x16x32_bf16 v[196:199], v[144:147], v[64:67], v[196:199]\n\t"
      "v_mfma_f32_16x16x32_bf16 v[200:203], v[144:147], v[68:71], v[200:203]\n\t"
      "v_mfma_f32_16x16x32_bf16 v[204:207], v[144:147], v[72:75], v[204:207]\n\t"
      "s_add_u32 s58, s66, s42\n\t"
      "s_addc_u32 s59, s67, 0\n\t"
      "global_load_dwordx4 v[164:167], v244, s[58:59] sc1\n\t"
      "v_mfma_f32_16x16x32_bf16 v[208:211], v[144:147], v[76:79], v[208:211]\n\t"
      "s_waitcnt vmcnt(11)\n\t"
      "v_mfma_f32_16x16x32_bf16 v[196:199], v[148:151], v[80:83], v[196:199]\n\t"
      "s_waitcnt vmcnt(10)\n\t"
      "v_mfma_f32_16x16x32_bf16 v[196:199], v[152:155], v[96:99], v[196:199]\n\t"
      "v_mfma_f32_16x16x32_bf16 v[200:203], v[148:151], v[84:87], v[200:203]\n\t"
      "s_add_u32 s58, s66, s43\n\t"
      "s_addc_u32 s59, s67, 0\n\t"
      "global_load_dwordx4 v[168:171], v244, s[58:59] sc1\n\t"
      "v_mfma_f32_16x16x32_bf16 v[200:203], v[152:155], v[100:103], v[200:203]\n\t"
      "v_mfma_f32_16x16x32_bf16 v[204:207], v[148:151], v[88:91], v[204:207]\n\t"
      "s_nop 1\n\t"
      "v_add_f32_e32 v251, v196, v197\n\t"
      "v_add_f32_e32 v251, v251, v198\n\t"
      "v_cmp_u_f32_e32 vcc, v251, v199\n\t"
      "s_cbranch_vccnz LA40R_%=\n\t"
      "LA40B_%=:\n\t"
      "v_mfma_f32_16x16x32_bf16 v[204:207], v[152:155], v[104:107], v[204:207]\n\t"
      "ds_write_b128 v245, v[196:199]\n\t"
      "v_mfma_f32_16x16x32_bf16 v[208:211], v[148:151], v[92:95], v[208:211]\n\t"
      "s_add_u32 s58, s66, s44\n\t"
      "s_addc_u32 s59, s67, 0\n\t"
      "global_load_dwordx4 v[172:175], v244, s[58:59] sc1\n\t"
      "v_mfma_f32_16x16x32_bf16 v[208:211], v[152:155], v[108:111], v[208:211]\n\t"
      "ds_write_b128 v245, v[200:203] offset:1024\n\t"
      "s_waitcnt vmcnt(4)\n\t"
      "v_mfma_f32_16x16x32_bf16 v[212:215], v[156:159], v[0:3], 0\n\t"
      "v_mfma_f32_16x16x32_bf16 v[216:219], v[156:159], v[4:7], 0\n\t"
      "ds_write_b128 v245, v[204:207] offset:2048\n\t"
      "v_mfma_f32_16x16x32_bf16 v[220:223], v[156:159], v[8:11], 0\n\t"
      "s_add_u32 s58, s66, s45\n\t"
      "s_addc_u32 s59, s67, 0\n\t"
      "global_load_dwordx4 v[176:179], v244, s[58:59] sc1\n\t"
      "v_mfma_f32_16x16x32_bf16 v[224:227], v[156:159], v[12:15], 0\n\t"
      "ds_write_b128 v245, v[208:211] offset:3072\n\t"
      "s_add_u32 s58, s66, s46\n\t"
      "s_addc_u32 s59, s67, 0\n\t"
      "global_load_dwordx4 v[180:183], v244, s[58:59] sc1\n\t"
      "s_waitcnt vmcnt(5)\n\t"
      "v_mfma_f32_16x16x32_bf16 v[212:215], v[160:163], v[16:19], v[212:215]\n\t"
      "v_mfma_f32_16x16x32_bf16 v[216:219], v[160:163], v[20:23], v[216:219]\n\t"
      "v_mfma_f32_16x16x32_bf16 v[220:223], v[160:163], v[24:27], v[220:223]\n\t"
      "v_mfma_f32_16x16x32_bf16 v[224:227], v[160:163], v[28:31], v[224:227]\n\t"
      "s_waitcnt lgkmcnt(0)\n\t"
      "s_barrier\n\t"
      "ds_read_b64 v[228:229], v246\n\t"
      "ds_read_b64 v[230:231], v246 offset:4096\n\t"
      "s_waitcnt vmcnt(4)\n\t"
      "v_mfma_f32_16x16x32_bf16 v[212:215], v[164:167], v[32:35], v[212:215]\n\t"
      "ds_read_b64 v[232:233], v246 offset:8192\n\t"
      "ds_read_b64 v[234:235], v246 offset:12288\n\t"
      "v_mfma_f32_16x16x32_bf16 v[216:219], v[164:167], v[36:39], v[216:219]\n\t"
      "ds_read_b64 v[236:237], v246 offset:16384\n\t"
      "ds_read_b64 v[238:239], v246 offset:20480\n\t"
      "s_add_u32 s58, s68, s40\n\t"
      "s_addc_u32 s59, s69, 0\n\t"
      "global_load_dwordx4 v[184:187], v244, s[58:59] sc1\n\t"
      "v_mfma_f32_16x16x32_bf16 v[220:223], v[164:167], v[40:43], v[220:223]\n\t"
      "ds_read_b64 v[240:241], v246 offset:24576\n\t"
      "ds_read_b64 v[242:243], v246 offset:28672\n\t"
      "v_mfma_f32_16x16x32_bf16 v[224:227], v[164:167], v[44:47], v[224:227]\n\t"
      "v_lshlrev_b32_e32 v248, 16, v112\n\t"
      "s_waitcnt vmcnt(4)\n\t"
      "v_mfma_f32_16x16x32_bf16 v[212:215], v[168:171], v[48:51], v[212:215]\n\t"
      "v_and_b32_e32 v249, 0xffff0000, v112\n\t"
      "v_mfma_f32_16x16x32_bf16 v[216:219], v[168:171], v[52:55], v[216:219]\n\t"
      "v_add_f32_e32 v248, v254, v248\n\t"
      "v_add_f32_e32 v249, v254, v249\n\t"
      "s_add_u32 s58, s68, s41\n\t"
      "s_addc_u32 s59, s69, 0\n\t"
      "global_load_dwordx4 v[188:191], v244, s[58:59] sc1\n\t"
      "v_mfma_f32_16x16x32_bf16 v[220:223], v[168:171], v[56:59], v[220:223]\n\t"
      "s_waitcnt lgkmcnt(4)\n\t"
      "v_mfma_f32_16x16x32_bf16 v[224:227], v[168:171], v[60:63], v[224:227]\n\t"
      "v_add_f32_e32 v228, v228, v230\n\t"
      "v_add_f32_e32 v232, v232, v234\n\t"
      "s_waitcnt vmcnt(4)\n\t"
      "v_mfma_f32_16x16x32_bf16 v[212:215], v[172:175], v[64:67], v[212:215]\n\t"
      "v_add_f32_e32 v229, v229, v231\n\t"
      "v_mfma_f32_16x16x32_bf16 v[216:219], v[172:175], v[68:71], v[216:219]\n\t"
      "v_add_f32_e32 v233, v233, v235\n\t"
      "v_add_f32_e32 v228, v228, v232\n\t"
      "s_add_u32 s58, s68, s42\n\t"
      "s_addc_u32 s59, s69, 0\n\t"
      "global_load_dwordx4 v[192:195], v244, s[58:59] sc1\n\t"
      "v_mfma_f32_16x16x32_bf16 v[220:223], v[172:175], v[72:75], v[220:223]\n\t"
      "v_add_f32_e32 v229, v229, v233\n\t"
      "v_mfma_f32_16x16x32_bf16 v[224:227], v[172:175], v[76:79], v[224:227]\n\t"
      "s_waitcnt lgkmcnt(0)\n\t"
      "v_add_f32_e32 v236, v236, v238\n\t"
      "s_waitcnt vmcnt(4)\n\t"
      "v_mfma_f32_16x16x32_bf16 v[212:215], v[176:179], v[80:83], v[212:215]\n\t"
      "v_add_f32_e32 v240, v240, v242\n\t"
      "s_waitcnt vmcnt(3)\n\t"
      "v_mfma_f32_16x16x32_bf16 v[212:215], v[180:183], v[96:99], v[212:215]\n\t"
      "v_add_f32_e32 v237, v237, v239\n\t"
      "v_add_f32_e32 v241, v241, v243\n\t"
      "s_add_u32 s58, s68, s43\n\t"
      "s_addc_u32 s59, s69, 0\n\t"
      "global_load_dwordx4 v[128:131], v244, s[58:59] sc1\n\t"
      "v_mfma_f32_16x16x32_bf16 v[216:219], v[176:179], v[84:87], v[216:219]\n\t"
      "v_add_f32_e32 v228, v228, v248\n\t"
      "v_mfma_f32_16x16x32_bf16 v[216:219], v[180:183], v[100:103], v[216:219]\n\t"
      "v_add_f32_e32 v236, v236, v240\n\t"
      "v_add_f32_e32 v229, v229, v249\n\t"
      "v_mfma_f32_16x16x32_bf16 v[220:223], v[176:179], v[88:91], v[220:223]\n\t"
      "v_add_f32_e32 v237, v237, v241\n\t"
      "v_add_f32_e32 v251, v212, v213\n\t"
      "v_add_f32_e32 v251, v251, v214\n\t"
      "v_cmp_u_f32_e32 vcc, v251, v215\n\t"
      "s_cbranch_vccnz LA41R_%=\n\t"
      "LA41B_%=:\n\t"
      "v_mfma_f32_16x16x32_bf16 v[220:223], v[180:183], v[104:107], v[220:223]\n\t"
      "v_add_f32_e32 v228, v228, v236\n\t"
      "v_add_f32_e32 v229, v229, v237\n\t"
      "ds_write_b128 v245, v[212:215] offset:32768\n\t"
      "s_add_u32 s58, s68, s44\n\t"
      "s_addc_u32 s59, s69, 0\n\t"
      "global_load_dwordx4 v[132:135], v244, s[58:59] sc1\n\t"
      "v_mfma_f32_16x16x32_bf16 v[224:227], v[176:179], v[92:95], v[224:227]\n\t"
      "v_cvt_pk_bf16_f32 v232, v228, v229\n\t"
      "v_mfma_f32_16x16x32_bf16 v[224:227], v[180:183], v[108:111], v[224:227]\n\t"
      "s_mov_b32 vcc_lo, 0xeeeeeeee\n\t"
      "s_mov_b32 vcc_hi, 0xeeeeeeee\n\t"
      "v_cndmask_b32_dpp v120, v232, v120, vcc quad_perm:[0,0,0,0] row_mask:0xf bank_mask:0xf\n\t"
      "v_cndmask_b32_dpp v121, v232, v121, vcc quad_perm:[1,1,1,1] row_mask:0xf bank_mask:0xf\n\t"
      "v_cndmask_b32_dpp v122, v232, v122, vcc quad_perm:[2,2,2,2] row_mask:0xf bank_mask:0xf\n\t"
      "v_cndmask_b32_dpp v123, v232, v123, vcc quad_perm:[3,3,3,3] row_mask:0xf bank_mask:0xf\n\t"
      "ds_write_b128 v245, v[216:219] offset:33792\n\t"
      "s_waitcnt vmcnt(4)\n\t"
      "v_mfma_f32_16x16x32_bf16 v[196:199], v[184:187], v[0:3], 0\n\t"
      "v_mfma_f32_16x16x32_bf16 v[200:203], v[184:187], v[4:7], 0\n\t"
      "ds_write_b128 v245, v[220:223] offset:34816\n\t"
      "s_add_u32 s58, s68, s45\n\t"
      "s_addc_u32 s59, s69, 0\n\t"
      "global_load_dwordx4 v[136:139], v244, s[58:59] sc1\n\t"
      "v_mfma_f32_16x16x32_bf16 v[204:207], v[184:187], v[8:11], 0\n\t"
      "s_add_u32 s58, s68, s46\n\t"
      "s_addc_u32 s59, s69, 0\n\t"
      "global_load_dwordx4 v[140:143], v244, s[58:59] sc1\n\t"
      "v_mfma_f32_16x16x32_bf16 v[208:211], v[184:187], v[12:15], 0\n\t"
      "ds_write_b128 v245, v[224:227] offset:35840\n\t"
      "s_waitcnt vmcnt(5)\n\t"
      "v_mfma_f32_16x16x32_bf16 v[196:199], v[188:191], v[16:19], v[196:199]\n\t"
      "v_mfma_f32_16x16x32_bf16 v[200:203], v[188:191], v[20:23], v[200:203]\n\t"
      "v_mfma_f32_16x16x32_bf16 v[204:207], v[188:191], v[24:27], v[204:207]\n\t"
      "v_mfma_f32_16x16x32_bf16 v[208:211], v[188:191], v[28:31], v[208:211]\n\t"
      "s_waitcnt lgkmcnt(0)\n\t"
      "s_barrier\n\t"
      "ds_read_b64 v[228:229], v246 offset:32768\n\t"
      "ds_read_b64 v[230:231], v246 offset:36864\n\t"
      "s_waitcnt vmcnt(4)\n\t"
      "v_mfma_f32_16x16x32_bf16 v[196:199], v[192:195], v[32:35], v[196:199]\n\t"
      "ds_read_b64 v[232:233], v246 offset:40960\n\t"
      "ds_read_b64 v[234:235], v246 offset:45056\n\t"
      "v_mfma_f32_16x16x32_bf16 v[200:203], v[192:195], v[36:39], v[200:203]\n\t"
      "ds_read_b64 v[236:237], v246 offset:49152\n\t"
      "ds_read_b64 v[238:239], v246 offset:53248\n\t"
      "s_add_u32 s58, s70, s40\n\t"
      "s_addc_u32 s59, s71, 0\n\t"
      "global_load_dwordx4 v[144:147], v244, s[58:59] sc1\n\t"
      "v_mfma_f32_16x16x32_bf16 v[204:207], v[192:195], v[40:43], v[204:207]\n\t"
      "ds_read_b64 v[240:241], v246 offset:57344\n\t"
      "ds_read_b64 v[242:243], v246 offset:61440\n\t"
      "v_mfma_f32_16x16x32_bf16 v[208:211], v[192:195], v[44:47], v[208:211]\n\t"
      "v_lshlrev_b32_e32 v248, 16, v113\n\t"
      "s_waitcnt vmcnt(4)\n\t"
      "v_mfma_f32_16x16x32_bf16 v[196:199], v[128:131], v[48:51], v[196:199]\n\t"
      "v_and_b32_e32 v249, 0xffff0000, v113\n\t"
      "v_mfma_f32_16x16x32_bf16 v[200:203], v[128:131], v[52:55], v[200:203]\n\t"
      "v_add_f32_e32 v248, v254, v248\n\t"
      "v_add_f32_e32 v249, v254, v249\n\t"
      "s_add_u32 s58, s70, s41\n\t"
      "s_addc_u32 s59, s71, 0\n\t"
      "global_load_dwordx4 v[148:151], v244, s[58:59] sc1\n\t"
      "v_mfma_f32_16x16x32_bf16 v[204:207], v[128:131], v[56:59], v[204:207]\n\t"
      "s_waitcnt lgkmcnt(4)\n\t"
      "v_mfma_f32_16x16x32_bf16 v[208:211], v[128:131], v[60:63], v[208:211]\n\t"
      "v_add_f32_e32 v228, v228, v230\n\t"
      "v_add_f32_e32 v232, v232, v234\n\t"
      "s_waitcnt vmcnt(4)\n\t"
      "v_mfma_f32_16x16x32_bf16 v[196:199], v[132:135], v[64:67], v[196:199]\n\t"
      "v_add_f32_e32 v229, v229, v231\n\t"
      "v_mfma_f32_16x16x32_bf16 v[200:203], v[132:135], v[68:71], v[200:203]\n\t"
      "v_add_f32_e32 v233, v233, v235\n\t"
      "v_add_f32_e32 v228, v228, v232\n\t"
      "s_add_u32 s58, s70, s42\n\t"
      "s_addc_u32 s59, s71, 0\n\t"
      "global_load_dwordx4 v[152:155], v244, s[58:59] sc1\n\t"
      "v_mfma_f32_16x16x32_bf16 v[204:207], v[132:135], v[72:75], v[204:207]\n\t"
      "v_add_f32_e32 v229, v229, v233\n\t"
      "v_mfma_f32_16x16x32_bf16 v[208:211], v[132:135], v[76:79], v[208:211]\n\t"
      "s_waitcnt lgkmcnt(0)\n\t"
      "v_add_f32_e32 v236, v236, v238\n\t"
      "s_waitcnt vmcnt(4)\n\t"
      "v_mfma_f32_16x16x32_bf16 v[196:199], v[136:139], v[80:83], v[196:199]\n\t"
      "v_add_f32_e32 v240, v240, v242\n\t"
      "s_waitcnt vmcnt(3)\n\t"
      "v_mfma_f32_16x16x32_bf16 v[196:199], v[140:143], v[96:99], v[196:199]\n\t"
      "v_add_f32_e32 v237, v237, v239\n\t"
      "v_add_f32_e32 v241, v241, v243\n\t"
      "s_add_u32 s58, s70, s43\n\t"
      "s_addc_u32 s59, s71, 0\n\t"
      "global_load_dwordx4 v[156:159], v244, s[58:59] sc1\n\t"
      "v_mfma_f32_16x16x32_bf16 v[200:203], v[136:139], v[84:87], v[200:203]\n\t"
      "v_add_f32_e32 v228, v228, v248\n\t"
      "v_mfma_f32_16x16x32_bf16 v[200:203], v[140:143], v[100:103], v[200:203]\n\t"
      "v_add_f32_e32 v236, v236, v240\n\t"
      "v_add_f32_e32 v229, v229, v249\n\t"
      "v_mfma_f32_16x16x32_bf16 v[204:207], v[136:139], v[88:91], v[204:207]\n\t"
      "v_add_f32_e32 v237, v237, v241\n\t"
      "v_add_f32_e32 v251, v196, v197\n\t"
      "v_add_f32_e32 v251, v251, v198\n\t"
      "v_cmp_u_f32_e32 vcc, v251, v199\n\t"
      "s_cbranch_vccnz LA42R_%=\n\t"
      "LA42B_%=:\n\t"
      "v_mfma_f32_16x16x32_bf16 v[204:207], v[140:143], v[104:107], v[204:207]\n\t"
      "v_add_f32_e32 v228, v228, v236\n\t"
      "v_add_f32_e32 v229, v229, v237\n\t"
      "ds_write_b128 v245, v[196:199]\n\t"
      "s_add_u32 s58, s70, s44\n\t"
      "s_addc_u32 s59, s71, 0\n\t"
      "global_load_dwordx4 v[160:163], v244, s[58:59] sc1\n\t"
      "v_mfma_f32_16x16x32_bf16 v[208:211], v[136:139], v[92:95], v[208:211]\n\t"
      "v_cvt_pk_bf16_f32 v232, v228, v229\n\t"
      "v_mfma_f32_16x16x32_bf16 v[208:211], v[140:143], v[108:111], v[208:211]\n\t"
      "s_mov_b32 vcc_lo, 0xdddddddd\n\t"
      "s_mov_b32 vcc_hi, 0xdddddddd\n\t"
      "v_cndmask_b32_dpp v120, v232, v120, vcc quad_perm:[0,0,0,0] row_mask:0xf bank_mask:0xf\n\t"
      "v_cndmask_b32_dpp v121, v232, v121, vcc quad_perm:[1,1,1,1] row_mask:0xf bank_mask:0xf\n\t"
      "v_cndmask_b32_dpp v122, v232, v122, vcc quad_perm:[2,2,2,2] row_mask:0xf bank_mask:0xf\n\t"
      "v_cndmask_b32_dpp v123, v232, v123, vcc quad_perm:[3,3,3,3] row_mask:0xf bank_mask:0xf\n\t"
      "ds_write_b128 v245, v[200:203] offset:1024\n\t"
      "s_waitcnt vmcnt(4)\n\t"
      "v_mfma_f32_16x16x32_bf16 v[212:215], v[144:147], v[0:3], 0\n\t"
      "v_mfma_f32_16x16x32_bf16 v[216:219], v[144:147], v[4:7], 0\n\t"
      "ds_write_b128 v245, v[204:207] offset:2048\n\t"
      "s_add_u32 s58, s70, s45\n\t"
      "s_addc_u32 s59, s71, 0\n\t"
      "global_load_dwordx4 v[164:167], v244, s[58:59] sc1\n\t"
      "v_mfma_f32_16x16x32_bf16 v[220:223], v[144:147], v[8:11], 0\n\t"
      "s_add_u32 s58, s70, s46\n\t"
      "s_addc_u32 s59, s71, 0\n\t"
      "global_load_dwordx4 v[168:171], v244, s[58:59] sc1\n\t"
      "v_mfma_f32_16x16x32_bf16 v[224:227], v[144:147], v[12:15], 0\n\t"
      "ds_write_b128 v245, v[208:211] offset:3072\n\t"
      "s_waitcnt vmcnt(5)\n\t"
      "v_mfma_f32_16x16x32_bf16 v[212:215], v[148:151], v[16:19], v[212:215]\n\t"
      "v_mfma_f32_16x16x32_bf16 v[216:219], v[148:151], v[20:23], v[216:219]\n\t"
      "v_mfma_f32_16x16x32_bf16 v[220:223], v[148:151], v[24:27], v[220:223]\n\t"
      "v_mfma_f32_16x16x32_bf16 v[224:227], v[148:151], v[28:31], v[224:227]\n\t"
      "s_waitcnt lgkmcnt(0)\n\t"
      "s_barrier\n\t"
      "ds_read_b64 v[228:229], v246\n\t"
      "ds_read_b64 v[230:231], v246 offset:4096\n\t"
      "s_waitcnt vmcnt(4)\n\t"
      "v_mfma_f32_16x16x32_bf16 v[212:215], v[152:155], v[32:35], v[212:215]\n\t"
      "ds_read_b64 v[232:233], v246 offset:8192\n\t"
      "ds_read_b64 v[234:235], v246 offset:12288\n\t"
      "v_mfma_f32_16x16x32_bf16 v[216:219], v[152:155], v[36:39], v[216:219]\n\t"
      "ds_read_b64 v[236:237], v246 offset:16384\n\t"
      "ds_read_b64 v[238:239], v246 offset:20480\n\t"
      "s_add_u32 s58, s72, s40\n\t"
      "s_addc_u32 s59, s73, 0\n\t"
      "global_load_dwordx4 v[172:175], v244, s[58:59] sc1\n\t"
      "v_mfma_f32_16x16x32_bf16 v[220:223], v[152:155], v[40:43], v[220:223]\n\t"
      "ds_read_b64 v[240:241], v246 offset:24576\n\t"
      "ds_read_b64 v[242:243], v246 offset:28672\n\t"
      "v_mfma_f32_16x16x32_bf16 v[224:227], v[152:155], v[44:47], v[224:227]\n\t"
      "v_lshlrev_b32_e32 v248, 16, v114\n\t"
      "s_waitcnt vmcnt(4)\n\t"
      "v_mfma_f32_16x16x32_bf16 v[212:215], v[156:159], v[48:51], v[212:215]\n\t"
      "v_and_b32_e32 v249, 0xffff0000, v114\n\t"
      "v_mfma_f32_16x16x32_bf16 v[216:219], v[156:159], v[52:55], v[216:219]\n\t"
      "v_add_f32_e32 v248, v254, v248\n\t"
      "v_add_f32_e32 v249, v254, v249\n\t"
      "s_add_u32 s58, s72, s41\n\t"
      "s_addc_u32 s59, s73, 0\n\t"
      "global_load_dwordx4 v[176:179], v244, s[58:59] sc1\n\t"
      "v_mfma_f32_16x16x32_bf16 v[220:223], v[156:159], v[56:59], v[220:223]\n\t"
      "s_waitcnt lgkmcnt(4)\n\t"
      "v_mfma_f32_16x16x32_bf16 v[224:227], v[156:159], v[60:63], v[224:227]\n\t"
      "v_add_f32_e32 v228, v228, v230\n\t"
      "v_add_f32_e32 v232, v232, v234\n\t"
      "s_waitcnt vmcnt(4)\n\t"
      "v_mfma_f32_16x16x32_bf16 v[212:215], v[160:163], v[64:67], v[212:215]\n\t"
      "v_add_f32_e32 v229, v229, v231\n\t"
      "v_mfma_f32_16x16x32_bf16 v[216:219], v[160:163], v[68:71], v[216:219]\n\t"
      "v_add_f32_e32 v233, v233, v235\n\t"
      "v_add_f32_e32 v228, v228, v232\n\t"
      "s_add_u32 s58, s72, s42\n\t"
      "s_addc_u32 s59, s73, 0\n\t"
      "global_load_dwordx4 v[180:183], v244, s[58:59] sc1\n\t"
      "v_mfma_f32_16x16x32_bf16 v[220:223], v[160:163], v[72:75], v[220:223]\n\t"
      "v_add_f32_e32 v229, v229, v233\n\t"
      "v_mfma_f32_16x16x32_bf16 v[224:227], v[160:163], v[76:79], v[224:227]\n\t"
      "s_waitcnt lgkmcnt(0)\n\t"
      "v_add_f32_e32 v236, v236, v238\n\t"
      "s_waitcnt vmcnt(4)\n\t"
      "v_mfma_f32_16x16x32_bf16 v[212:215], v[164:167], v[80:83], v[212:215]\n\t"
      "v_add_f32_e32 v240, v240, v242\n\t"
      "s_waitcnt vmcnt(3)\n\t"
      "v_mfma_f32_16x16x32_bf16 v[212:215], v[168:171], v[96:99], v[212:215]\n\t"
      "v_add_f32_e32 v237, v237, v239\n\t"
      "v_add_f32_e32 v241, v241, v243\n\t"
      "s_add_u32 s58, s72, s43\n\t"
      "s_addc_u32 s59, s73, 0\n\t"
      "global_load_dwordx4 v[184:187], v244, s[58:59] sc1\n\t"
      "v_mfma_f32_16x16x32_bf16 v[216:219], v[164:167], v[84:87], v[216:219]\n\t"
      "v_add_f32_e32 v228, v228, v248\n\t"
      "v_mfma_f32_16x16x32_bf16 v[216:219], v[168:171], v[100:103], v[216:219]\n\t"
      "v_add_f32_e32 v236, v236, v240\n\t"
      "v_add_f32_e32 v229, v229, v249\n\t"
      "v_mfma_f32_16x16x32_bf16 v[220:223], v[164:167], v[88:91], v[220:223]\n\t"
      "v_add_f32_e32 v237, v237, v241\n\t"
      "v_add_f32_e32 v251, v212, v213\n\t"
      "v_add_f32_e32 v251, v251, v214\n\t"
      "v_cmp_u_f32_e32 vcc, v251, v215\n\t"
      "s_cbranch_vccnz LA43R_%=\n\t"
      "LA43B_%=:\n\t"
      "v_mfma_f32_16x16x32_bf16 v[220:223], v[168:171], v[104:107], v[220:223]\n\t"
      "v_add_f32_e32 v228, v228, v236\n\t"
      "v_add_f32_e32 v229, v229, v237\n\t"
      "ds_write_b128 v245, v[212:215] offset:32768\n\t"
      "s_add_u32 s58, s72, s44\n\t"
      "s_addc_u32 s59, s73, 0\n\t"
      "global_load_dwordx4 v[188:191], v244, s[58:59] sc1\n\t"
      "v_mfma_f32_16x16x32_bf16 v[224:227], v[164:167], v[92:95], v[224:227]\n\t"
      "v_cvt_pk_bf16_f32 v232, v228, v229\n\t"
      "v_mfma_f32_16x16x32_bf16 v[224:227], v[168:171], v[108:111], v[224:227]\n\t"
      "s_mov_b32 vcc_lo, 0xbbbbbbbb\n\t"
      "s_mov_b32 vcc_hi, 0xbbbbbbbb\n\t"
      "v_cndmask_b32_dpp v120, v232, v120, vcc quad_perm:[0,0,0,0] row_mask:0xf bank_mask:0xf\n\t"
      "v_cndmask_b32_dpp v121, v232, v121, vcc quad_perm:[1,1,1,1] row_mask:0xf bank_mask:0xf\n\t"
      "v_cndmask_b32_dpp v122, v232, v122, vcc quad_perm:[2,2,2,2] row_mask:0xf bank_mask:0xf\n\t"
      "v_cndmask_b32_dpp v123, v232, v123, vcc quad_perm:[3,3,3,3] row_mask:0xf bank_mask:0xf\n\t"
      "ds_write_b128 v245, v[216:219] offset:33792\n\t"
      "s_waitcnt vmcnt(4)\n\t"
      "v_mfma_f32_16x16x32_bf16 v[196:199], v[172:175], v[0:3], 0\n\t"
      "v_mfma_f32_16x16x32_bf16 v[200:203], v[172:175], v[4:7], 0\n\t"
      "ds_write_b128 v245, v[220:223] offset:34816\n\t"
      "s_add_u32 s58, s72, s45\n\t"
      "s_addc_u32 s59, s73, 0\n\t"
      "global_load_dwordx4 v[192:195], v244, s[58:59] sc1\n\t"
      "v_mfma_f32_16x16x32_bf16 v[204:207], v[172:175], v[8:11], 0\n\t"
      "s_add_u32 s58, s72, s46\n\t"
      "s_addc_u32 s59, s73, 0\n\t"
      "global_load_dwordx4 v[128:131], v244, s[58:59] sc1\n\t"
      "v_mfma_f32_16x16x32_bf16 v[208:211], v[172:175], v[12:15], 0\n\t"
      "ds_write_b128 v245, v[224:227] offset:35840\n\t"
      "s_waitcnt vmcnt(5)\n\t"
      "v_mfma_f32_16x16x32_bf16 v[196:199], v[176:179], v[16:19], v[196:199]\n\t"
      "v_mfma_f32_16x16x32_bf16 v[200:203], v[176:179], v[20:23], v[200:203]\n\t"
      "v_mfma_f32_16x16x32_bf16 v[204:207], v[176:179], v[24:27], v[204:207]\n\t"
      "v_mfma_f32_16x16x32_bf16 v[208:211], v[176:179], v[28:31], v[208:211]\n\t"
      "s_waitcnt lgkmcnt(0)\n\t"
      "s_barrier\n\t"
      "ds_read_b64 v[228:229], v246 offset:32768\n\t"
      "ds_read_b64 v[230:231], v246 offset:36864\n\t"
      "s_waitcnt vmcnt(4)\n\t"
      "v_mfma_f32_16x16x32_bf16 v[196:199], v[180:183], v[32:35], v[196:199]\n\t"
      "ds_read_b64 v[232:233], v246 offset:40960\n\t"
      "ds_read_b64 v[234:235], v246 offset:45056\n\t"
      "v_mfma_f32_16x16x32_bf16 v[200:203], v[180:183], v[36:39], v[200:203]\n\t"
      "ds_read_b64 v[236:237], v246 offset:49152\n\t"
      "ds_read_b64 v[238:239], v246 offset:53248\n\t"
      "s_add_u32 s58, s74, s40\n\t"
      "s_addc_u32 s59, s75, 0\n\t"
      "global_load_dwordx4 v[132:135], v244, s[58:59] sc1\n\t"
      "v_mfma_f32_16x16x32_bf16 v[204:207], v[180:183], v[40:43], v[204:207]\n\t"
      "ds_read_b64 v[240:241], v246 offset:57344\n\t"
      "ds_read_b64 v[242:243], v246 offset:61440\n\t"
      "v_mfma_f32_16x16x32_bf16 v[208:211], v[180:183], v[44:47], v[208:211]\n\t"
      "v_lshlrev_b32_e32 v248, 16, v115\n\t"
      "s_waitcnt vmcnt(4)\n\t"
      "v_mfma_f32_16x16x32_bf16 v[196:199], v[184:187], v[48:51], v[196:199]\n\t"
      "v_and_b32_e32 v249, 0xffff0000, v115\n\t"
      "v_mfma_f32_16x16x32_bf16 v[200:203], v[184:187], v[52:55], v[200:203]\n\t"
      "v_add_f32_e32 v248, v254, v248\n\t"
      "v_add_f32_e32 v249, v254, v249\n\t"
      "s_add_u32 s58, s74, s41\n\t"
      "s_addc_u32 s59, s75, 0\n\t"
      "global_load_dwordx4 v[136:139], v244, s[58:59] sc1\n\t"
      "v_mfma_f32_16x16x32_bf16 v[204:207], v[184:187], v[56:59], v[204:207]\n\t"
      "s_waitcnt lgkmcnt(4)\n\t"
      "v_mfma_f32_16x16x32_bf16 v[208:211], v[184:187], v[60:63], v[208:211]\n\t"
      "v_add_f32_e32 v228, v228, v230\n\t"
      "v_add_f32_e32 v232, v232, v234\n\t"
      "s_waitcnt vmcnt(4)\n\t"
      "v_mfma_f32_16x16x32_bf16 v[196:199], v[188:191], v[64:67], v[196:199]\n\t"
      "v_add_f32_e32 v229, v229, v231\n\t"
      "v_mfma_f32_16x16x32_bf16 v[200:203], v[188:191], v[68:71], v[200:203]\n\t"
      "v_add_f32_e32 v233, v233, v235\n\t"
      "v_add_f32_e32 v228, v228, v232\n\t"
      "s_add_u32 s58, s74, s42\n\t"
      "s_addc_u32 s59, s75, 0\n\t"
      "global_load_dwordx4 v[140:143], v244, s[58:59] sc1\n\t"
      "v_mfma_f32_16x16x32_bf16 v[204:207], v[188:191], v[72:75], v[204:207]\n\t"
      "v_add_f32_e32 v229, v229, v233\n\t"
      "v_mfma_f32_16x16x32_bf16 v[208:211], v[188:191], v[76:79], v[208:211]\n\t"
      "s_waitcnt lgkmcnt(0)\n\t"
      "v_add_f32_e32 v236, v236, v238\n\t"
      "s_waitcnt vmcnt(4)\n\t"
      "v_mfma_f32_16x16x32_bf16 v[196:199], v[192:195], v[80:83], v[196:199]\n\t"
      "v_add_f32_e32 v240, v240, v242\n\t"
      "s_waitcnt vmcnt(3)\n\t"
      "v_mfma_f32_16x16x32_bf16 v[196:199], v[128:131], v[96:99], v[196:199]\n\t"
      "v_add_f32_e32 v237, v237, v239\n\t"
      "v_add_f32_e32 v241, v241, v243\n\t"
      "s_add_u32 s58, s74, s43\n\t"
      "s_addc_u32 s59, s75, 0\n\t"
      "global_load_dwordx4 v[144:147], v244, s[58:59] sc1\n\t"
      "v_mfma_f32_16x16x32_bf16 v[200:203], v[192:195], v[84:87], v[200:203]\n\t"
      "v_add_f32_e32 v228, v228, v248\n\t"
      "v_mfma_f32_16x16x32_bf16 v[200:203], v[128:131], v[100:103], v[200:203]\n\t"
      "v_add_f32_e32 v236, v236, v240\n\t"
      "v_add_f32_e32 v229, v229, v249\n\t"
      "v_mfma_f32_16x16x32_bf16 v[204:207], v[192:195], v[88:91], v[204:207]\n\t"
      "v_add_f32_e32 v237, v237, v241\n\t"
      "v_add_f32_e32 v251, v196, v197\n\t"
      "v_add_f32_e32 v251, v251, v198\n\t"
      "v_cmp_u_f32_e32 vcc, v251, v199\n\t"
      "s_cbranch_vccnz LA44R_%=\n\t"
      "LA44B_%=:\n\t"
      "v_mfma_f32_16x16x32_bf16 v[204:207], v[128:131], v[104:107], v[204:207]\n\t"
      "v_add_f32_e32 v228, v228, v236\n\t"
      "v_add_f32_e32 v229, v229, v237\n\t"
      "ds_write_b128 v245, v[196:199]\n\t"
      "s_add_u32 s58, s74, s44\n\t"
      "s_addc_u32 s59, s75, 0\n\t"
      "global_load_dwordx4 v[148:151], v244, s[58:59] sc1\n\t"
      "v_mfma_f32_16x16x32_bf16 v[208:211], v[192:195], v[92:95], v[208:211]\n\t"
      "v_cvt_pk_bf16_f32 v232, v228, v229\n\t"
      "v_mfma_f32_16x16x32_bf16 v[208:211], v[128:131], v[108:111], v[208:211]\n\t"
      "s_mov_b32 vcc_lo, 0xeeeeeeee\n\t"
      "s_mov_b32 vcc_hi, 0xeeeeeeee\n\t"
      "v_cndmask_b32_dpp v124, v232, v124, vcc quad_perm:[0,0,0,0] row_mask:0xf bank_mask:0xf\n\t"
      "v_cndmask_b32_dpp v125, v232, v125, vcc quad_perm:[1,1,1,1] row_mask:0xf bank_mask:0xf\n\t"
      "v_cndmask_b32_dpp v126, v232, v126, vcc quad_perm:[2,2,2,2] row_mask:0xf bank_mask:0xf\n\t"
      "v_cndmask_b32_dpp v127, v232, v127, vcc quad_perm:[3,3,3,3] row_mask:0xf bank_mask:0xf\n\t"
      "ds_write_b128 v245, v[200:203] offset:1024\n\t"
      "s_waitcnt vmcnt(4)\n\t"
      "v_mfma_f32_16x16x32_bf16 v[212:215], v[132:135], v[0:3], 0\n\t"
      "v_mfma_f32_16x16x32_bf16 v[216:219], v[132:135], v[4:7], 0\n\t"
      "ds_write_b128 v245, v[204:207] offset:2048\n\t"
      "s_add_u32 s58, s74, s45\n\t"
      "s_addc_u32 s59, s75, 0\n\t"
      "global_load_dwordx4 v[152:155], v244, s[58:59] sc1\n\t"
      "v_mfma_f32_16x16x32_bf16 v[220:223], v[132:135], v[8:11], 0\n\t"
      "s_add_u32 s58, s74, s46\n\t"
      "s_addc_u32 s59, s75, 0\n\t"
      "global_load_dwordx4 v[156:159], v244, s[58:59] sc1\n\t"
      "v_mfma_f32_16x16x32_bf16 v[224:227], v[132:135], v[12:15], 0\n\t"
      "ds_write_b128 v245, v[208:211] offset:3072\n\t"
      "s_waitcnt vmcnt(5)\n\t"
      "v_mfma_f32_16x16x32_bf16 v[212:215], v[136:139], v[16:19], v[212:215]\n\t"
      "v_mfma_f32_16x16x32_bf16 v[216:219], v[136:139], v[20:23], v[216:219]\n\t"
      "v_mfma_f32_16x16x32_bf16 v[220:223], v[136:139], v[24:27], v[220:223]\n\t"
      "v_mfma_f32_16x16x32_bf16 v[224:227], v[136:139], v[28:31], v[224:227]\n\t"
      "s_waitcnt lgkmcnt(0)\n\t"
      "s_barrier\n\t"
      "ds_read_b64 v[228:229], v246\n\t"
      "ds_read_b64 v[230:231], v246 offset:4096\n\t"
      "s_waitcnt vmcnt(4)\n\t"
      "v_mfma_f32_16x16x32_bf16 v[212:215], v[140:143], v[32:35], v[212:215]\n\t"
      "ds_read_b64 v[232:233], v246 offset:8192\n\t"
      "ds_read_b64 v[234:235], v246 offset:12288\n\t"
      "s_add_u32 s58, s76, s40\n\t"
      "s_addc_u32 s59, s77, 0\n\t"
      "global_load_dwordx4 v[160:163], v244, s[58:59] sc1\n\t"
      "v_mfma_f32_16x16x32_bf16 v[216:219], v[140:143], v[36:39], v[216:219]\n\t"
      "ds_read_b64 v[236:237], v246 offset:16384\n\t"
      "ds_read_b64 v[238:239], v246 offset:20480\n\t"
      "v_mfma_f32_16x16x32_bf16 v[220:223], v[140:143], v[40:43], v[220:223]\n\t"
      "ds_read_b64 v[240:241], v246 offset:24576\n\t"
      "ds_read_b64 v[242:243], v246 offset:28672\n\t"
      "v_mfma_f32_16x16x32_bf16 v[224:227], v[140:143], v[44:47], v[224:227]\n\t"
      "v_lshlrev_b32_e32 v248, 16, v116\n\t"
      "s_waitcnt vmcnt(4)\n\t"
      "v_mfma_f32_16x16x32_bf16 v[212:215], v[144:147], v[48:51], v[212:215]\n\t"
      "v_and_b32_e32 v249, 0xffff0000, v116\n\t"
      "s_add_u32 s58, s76, s41\n\t"
      "s_addc_u32 s59, s77, 0\n\t"
      "global_load_dwordx4 v[164:167], v244, s[58:59] sc1\n\t"
      "v_mfma_f32_16x16x32_bf16 v[216:219], v[144:147], v[52:55], v[216:219]\n\t"
      "v_add_f32_e32 v248, v254, v248\n\t"
      "v_add_f32_e32 v249, v254, v249\n\t"
      "v_mfma_f32_16x16x32_bf16 v[220:223], v[144:147], v[56:59], v[220:223]\n\t"
      "s_waitcnt lgkmcnt(4)\n\t"
      "v_mfma_f32_16x16x32_bf16 v[224:227], v[144:147], v[60:63], v[224:227]\n\t"
      "v_add_f32_e32 v228, v228, v230\n\t"
      "v_add_f32_e32 v232, v232, v234\n\t"
      "s_waitcnt vmcnt(4)\n\t"
      "v_mfma_f32_16x16x32_bf16 v[212:215], v[148:151], v[64:67], v[212:215]\n\t"
      "v_add_f32_e32 v229, v229, v231\n\t"
      "s_add_u32 s58, s76, s42\n\t"
      "s_addc_u32 s59, s77, 0\n\t"
      "global_load_dwordx4 v[168:171], v244, s[58:59] sc1\n\t"
      "v_mfma_f32_16x16x32_bf16 v[216:219], v[148:151], v[68:71], v[216:219]\n\t"
      "v_add_f32_e32 v233, v233, v235\n\t"
      "v_add_f32_e32 v228, v228, v232\n\t"
      "v_mfma_f32_16x16x32_bf16 v[220:223], v[148:151], v[72:75], v[220:223]\n\t"
      "v_add_f32_e32 v229, v229, v233\n\t"
      "v_mfma_f32_16x16x32_bf16 v[224:227], v[148:151], v[76:79], v[224:227]\n\t"
      "s_waitcnt lgkmcnt(0)\n\t"
      "v_add_f32_e32 v236, v236, v238\n\t"
      "s_waitcnt vmcnt(4)\n\t"
      "v_mfma_f32_16x16x32_bf16 v[212:215], v[152:155], v[80:83], v[212:215]\n\t"
      "v_add_f32_e32 v240, v240, v242\n\t"
      "s_add_u32 s58, s76, s43\n\t"
      "s_addc_u32 s59, s77, 0\n\t"
      "global_load_dwordx4 v[172:175], v244, s[58:59] sc1\n\t"
      "s_waitcnt vmcnt(4)\n\t"
      "v_mfma_f32_16x16x32_bf16 v[212:215], v[156:159], v[96:99], v[212:215]\n\t"
      "v_add_f32_e32 v237, v237, v239\n\t"
      "v_add_f32_e32 v241, v241, v243\n\t"
      "v_mfma_f32_16x16x32_bf16 v[216:219], v[152:155], v[84:87], v[216:219]\n\t"
      "v_add_f32_e32 v228, v228, v248\n\t"
      "v_mfma_f32_16x16x32_bf16 v[216:219], v[156:159], v[100:103], v[216:219]\n\t"
      "v_add_f32_e32 v236, v236, v240\n\t"
      "v_add_f32_e32 v229, v229, v249\n\t"
      "v_mfma_f32_16x16x32_bf16 v[220:223], v[152:155], v[88:91], v[220:223]\n\t"
      "v_add_f32_e32 v237, v237, v241\n\t"
      "v_add_f32_e32 v251, v212, v213\n\t"
      "v_add_f32_e32 v251, v251, v214\n\t"
      "v_cmp_u_f32_e32 vcc, v251, v215\n\t"
      "s_cbranch_vccnz LA45R_%=\n\t"
      "LA45B_%=:\n\t"
      "s_add_u32 s58, s76, s44\n\t"
      "s_addc_u32 s59, s77, 0\n\t"
      "global_load_dwordx4 v[176:179], v244, s[58:59] sc1\n\t"
      "v_mfma_f32_16x16x32_bf16 v[220:223], v[156:159], v[104:107], v[220:223]\n\t"
      "v_add_f32_e32 v228, v228, v236\n\t"
      "v_add_f32_e32 v229, v229, v237\n\t"
      "ds_write_b128 v245, v[212:215] offset:32768\n\t"
      "v_mfma_f32_16x16x32_bf16 v[224:227], v[152:155], v[92:95], v[224:227]\n\t"
      "v_cvt_pk_bf16_f32 v232, v228, v229\n\t"
      "v_mfma_f32_16x16x32_bf16 v[224:227], v[156:159], v[108:111], v[224:227]\n\t"
      "s_mov_b32 vcc_lo, 0xdddddddd\n\t"
      "s_mov_b32 vcc_hi, 0xdddddddd\n\t"
      "v_cndmask_b32_dpp v124, v232, v124, vcc quad_perm:[0,0,0,0] row_mask:0xf bank_mask:0xf\n\t"
      "v_cndmask_b32_dpp v125, v232, v125, vcc quad_perm:[1,1,1,1] row_mask:0xf bank_mask:0xf\n\t"
      "v_cndmask_b32_dpp v126, v232, v126, vcc quad_perm:[2,2,2,2] row_mask:0xf bank_mask:0xf\n\t"
      "v_cndmask_b32_dpp v127, v232, v127, vcc quad_perm:[3,3,3,3] row_mask:0xf bank_mask:0xf\n\t"
      "ds_write_b128 v245, v[216:219] offset:33792\n\t"
      "s_waitcnt vmcnt(4)\n\t"
      "v_mfma_f32_16x16x32_bf16 v[196:199], v[160:163], v[0:3], 0\n\t"
      "s_add_u32 s58, s76, s45\n\t"
      "s_addc_u32 s59, s77, 0\n\t"
      "global_load_dwordx4 v[180:183], v244, s[58:59] sc1\n\t"
      "v_mfma_f32_16x16x32_bf16 v[200:203], v[160:163], v[4:7], 0\n\t"
      "ds_write_b128 v245, v[220:223] offset:34816\n\t"
      "s_add_u32 s58, s76, s46\n\t"
      "s_addc_u32 s59, s77, 0\n\t"
      "global_load_dwordx4 v[184:187], v244, s[58:59] sc1\n\t"
      "v_mfma_f32_16x16x32_bf16 v[204:207], v[160:163], v[8:11], 0\n\t"
      "v_mfma_f32_16x16x32_bf16 v[208:211], v[160:163], v[12:15], 0\n\t"
      "ds_write_b128 v245, v[224:227] offset:35840\n\t"
      "s_waitcnt vmcnt(5)\n\t"
      "v_mfma_f32_16x16x32_bf16 v[196:199], v[164:167], v[16:19], v[196:199]\n\t"
      "v_mfma_f32_16x16x32_bf16 v[200:203], v[164:167], v[20:23], v[200:203]\n\t"
      "v_mfma_f32_16x16x32_bf16 v[204:207], v[164:167], v[24:27], v[204:207]\n\t"
      "v_mfma_f32_16x16x32_bf16 v[208:211], v[164:167], v[28:31], v[208:211]\n\t"
      "s_waitcnt lgkmcnt(0)\n\t"
      "s_barrier\n\t"
      "ds_read_b64 v[228:229], v246 offset:32768\n\t"
      "ds_read_b64 v[230:231], v246 offset:36864\n\t"
      "s_waitcnt vmcnt(4)\n\t"
      "v_mfma_f32_16x16x32_bf16 v[196:199], v[168:171], v[32:35], v[196:199]\n\t"
      "ds_read_b64 v[232:233], v246 offset:40960\n\t"
      "ds_read_b64 v[234:235], v246 offset:45056\n\t"
      "v_mfma_f32_16x16x32_bf16 v[200:203], v[168:171], v[36:39], v[200:203]\n\t"
      "ds_read_b64 v[236:237], v246 offset:49152\n\t"
      "ds_read_b64 v[238:239], v246 offset:53248\n\t"
      "v_mfma_f32_16x16x32_bf16 v[204:207], v[168:171], v[40:43], v[204:207]\n\t"
      "ds_read_b64 v[240:241], v246 offset:57344\n\t"
      "ds_read_b64 v[242:243], v246 offset:61440\n\t"
      "v_mfma_f32_16x16x32_bf16 v[208:211], v[168:171], v[44:47], v[208:211]\n\t"
      "v_lshlrev_b32_e32 v248, 16, v117\n\t"
      "s_waitcnt vmcnt(3)\n\t"
      "v_mfma_f32_16x16x32_bf16 v[196:199], v[172:175], v[48:51], v[196:199]\n\t"
      "v_and_b32_e32 v249, 0xffff0000, v117\n\t"
      "v_add_f32_e32 v248, v254, v248\n\t"
      "v_mfma_f32_16x16x32_bf16 v[200:203], v[172:175], v[52:55], v[200:203]\n\t"
      "v_add_f32_e32 v249, v254, v249\n\t"
      "v_mfma_f32_16x16x32_bf16 v[204:207], v[172:175], v[56:59], v[204:207]\n\t"
      "s_waitcnt lgkmcnt(4)\n\t"
      "v_add_f32_e32 v228, v228, v230\n\t"
      "v_mfma_f32_16x16x32_bf16 v[208:211], v[172:175], v[60:63], v[208:211]\n\t"
      "v_add_f32_e32 v232, v232, v234\n\t"
      "s_waitcnt vmcnt(2)\n\t"
      "v_mfma_f32_16x16x32_bf16 v[196:199], v[176:179], v[64:67], v[196:199]\n\t"
      "v_add_f32_e32 v229, v229, v231\n\t"
      "v_add_f32_e32 v233, v233, v235\n\t"
      "v_mfma_f32_16x16x32_bf16 v[200:203], v[176:179], v[68:71], v[200:203]\n\t"
      "v_add_f32_e32 v228, v228, v232\n\t"
      "v_add_f32_e32 v229, v229, v233\n\t"
      "v_mfma_f32_16x16x32_bf16 v[204:207], v[176:179], v[72:75], v[204:207]\n\t"
      "s_waitcnt lgkmcnt(0)\n\t"
      "v_mfma_f32_16x16x32_bf16 v[208:211], v[176:179], v[76:79], v[208:211]\n\t"
      "v_add_f32_e32 v236, v236, v238\n\t"
      "v_add_f32_e32 v240, v240, v242\n\t"
      "s_waitcnt vmcnt(1)\n\t"
      "v_mfma_f32_16x16x32_bf16 v[196:199], v[180:183], v[80:83], v[196:199]\n\t"
      "v_add_f32_e32 v237, v237, v239\n\t"
      "s_waitcnt vmcnt(0)\n\t"
      "v_mfma_f32_16x16x32_bf16 v[196:199], v[184:187], v[96:99], v[196:199]\n\t"
      "v_add_f32_e32 v241, v241, v243\n\t"
      "v_add_f32_e32 v228, v228, v248\n\t"
      "v_mfma_f32_16x16x32_bf16 v[200:203], v[180:183], v[84:87], v[200:203]\n\t"
      "v_add_f32_e32 v236, v236, v240\n\t"
      "v_add_f32_e32 v229, v229, v249\n\t"
      "v_mfma_f32_16x16x32_bf16 v[200:203], v[184:187], v[100:103], v[200:203]\n\t"
      "v_add_f32_e32 v237, v237, v241\n\t"
      "v_mfma_f32_16x16x32_bf16 v[204:207], v[180:183], v[88:91], v[204:207]\n\t"
      "v_add_f32_e32 v228, v228, v236\n\t"
      "v_add_f32_e32 v229, v229, v237\n\t"
      "v_add_f32_e32 v251, v196, v197\n\t"
      "v_add_f32_e32 v251, v251, v198\n\t"
      "v_cmp_u_f32_e32 vcc, v251, v199\n\t"
      "s_cbranch_vccnz LA46R_%=\n\t"
      "LA46B_%=:\n\t"
      "v_mfma_f32_16x16x32_bf16 v[204:207], v[184:187], v[104:107], v[204:207]\n\t"
      "v_cvt_pk_bf16_f32 v232, v228, v229\n\t"
      "ds_write_b128 v245, v[196:199]\n\t"
      "v_mfma_f32_16x16x32_bf16 v[208:211], v[180:183], v[92:95], v[208:211]\n\t"
      "s_mov_b32 vcc_lo, 0xbbbbbbbb\n\t"
      "s_mov_b32 vcc_hi, 0xbbbbbbbb\n\t"
      "v_cndmask_b32_dpp v124, v232, v124, vcc quad_perm:[0,0,0,0] row_mask:0xf bank_mask:0xf\n\t"
      "v_cndmask_b32_dpp v125, v232, v125, vcc quad_perm:[1,1,1,1] row_mask:0xf bank_mask:0xf\n\t"
      "v_cndmask_b32_dpp v126, v232, v126, vcc quad_perm:[2,2,2,2] row_mask:0xf bank_mask:0xf\n\t"
      "v_cndmask_b32_dpp v127, v232, v127, vcc quad_perm:[3,3,3,3] row_mask:0xf bank_mask:0xf\n\t"
      "v_mfma_f32_16x16x32_bf16 v[208:211], v[184:187], v[108:111], v[208:211]\n\t"
      "ds_write_b128 v245, v[200:203] offset:1024\n\t"
      "s_mov_b64 exec, s[82:83]\n\t"
      "global_store_dwordx4 v247, v[120:123], s[18:19] sc1\n\t"
      "s_mov_b64 exec, -1\n\t"
      "ds_write_b128 v245, v[204:207] offset:2048\n\t"
      "s_nop 2\n\t"
      "ds_write_b128 v245, v[208:211] offset:3072\n\t"
      "s_waitcnt lgkmcnt(0)\n\t"
      "s_barrier\n\t"
      "ds_read_b64 v[228:229], v246\n\t"
      "ds_read_b64 v[230:231], v246 offset:4096\n\t"
      "ds_read_b64 v[232:233], v246 offset:8192\n\t"
      "ds_read_b64 v[234:235], v246 offset:12288\n\t"
      "ds_read_b64 v[236:237], v246 offset:16384\n\t"
      "ds_read_b64 v[238:239], v246 offset:20480\n\t"
      "ds_read_b64 v[240:241], v246 offset:24576\n\t"
      "ds_read_b64 v[242:243], v246 offset:28672\n\t"
      "v_lshlrev_b32_e32 v248, 16, v118\n\t"
      "v_and_b32_e32 v249, 0xffff0000, v118\n\t"
      "v_add_f32_e32 v248, v254, v248\n\t"
      "v_add_f32_e32 v249, v254, v249\n\t"
      "s_waitcnt lgkmcnt(4)\n\t"
      "v_add_f32_e32 v228, v228, v230\n\t"
      "v_add_f32_e32 v232, v232, v234\n\t"
      "v_add_f32_e32 v229, v229, v231\n\t"
      "v_add_f32_e32 v233, v233, v235\n\t"
      "v_add_f32_e32 v228, v228, v232\n\t"
      "v_add_f32_e32 v229, v229, v233\n\t"
      "s_waitcnt lgkmcnt(0)\n\t"
      "v_add_f32_e32 v236, v236, v238\n\t"
      "v_add_f32_e32 v240, v240, v242\n\t"
      "v_add_f32_e32 v237, v237, v239\n\t"
      "v_add_f32_e32 v241, v241, v243\n\t"
      "v_add_f32_e32 v228, v228, v248\n\t"
      "v_add_f32_e32 v236, v236, v240\n\t"
      "v_add_f32_e32 v229, v229, v249\n\t"
      "v_add_f32_e32 v237, v237, v241\n\t"
      "v_add_f32_e32 v228, v228, v236\n\t"
      "v_add_f32_e32 v229, v229, v237\n\t"
      "v_cvt_pk_bf16_f32 v232, v228, v229\n\t"
      "s_mov_b32 vcc_lo, 0x77777777\n\t"
      "s_mov_b32 vcc_hi, 0x77777777\n\t"
      "v_cndmask_b32_dpp v124, v232, v124, vcc quad_perm:[0,0,0,0] row_mask:0xf bank_mask:0xf\n\t"
      "v_cndmask_b32_dpp v125, v232, v125, vcc quad_perm:[1,1,1,1] row_mask:0xf bank_mask:0xf\n\t"
      "v_cndmask_b32_dpp v126, v232, v126, vcc quad_perm:[2,2,2,2] row_mask:0xf bank_mask:0xf\n\t"
      "v_cndmask_b32_dpp v127, v232, v127, vcc quad_perm:[3,3,3,3] row_mask:0xf bank_mask:0xf\n\t"
      "global_store_dwordx4 v247, v[124:127], s[62:63] sc1\n\t"
      "s_endpgm\n\t"
      "LO0R_%=:\n\t"
      "s_mov_b32 s80, 0\n\t"
      "LO0L_%=:\n\t"
      "s_add_u32 s80, s80, 1\n\t"
      "s_cmp_lt_u32 s80, 262144\n\t"
      "s_cbranch_scc0 LO0X_%=\n\t"
      "s_waitcnt vmcnt(0)\n\t"
      "s_sleep 1\n\t"
      "global_load_dwordx4 v[128:131], v244, s[42:43] offset:-2048 sc1\n\t"
      "global_load_dwordx4 v[132:135], v244, s[42:43] offset:-1024 sc1\n\t"
      "global_load_dwordx4 v[136:139], v244, s[42:43] sc1\n\t"
      "global_load_dwordx4 v[140:143], v244, s[42:43] offset:1024 sc1\n\t"
      "global_load_dwordx4 v[144:147], v244, s[42:43] offset:2048 sc1\n\t"
      "global_load_dwordx4 v[148:151], v244, s[42:43] offset:3072 sc1\n\t"
      "global_load_dwordx4 v[152:155], v244, s[44:45] offset:-4096 sc1\n\t"
      "global_load_dwordx4 v[156:159], v244, s[44:45] offset:-3072 sc1\n\t"
      "global_load_dwordx4 v[160:163], v244, s[44:45] offset:-2048 sc1\n\t"
      "global_load_dwordx4 v[164:167], v244, s[44:45] offset:-1024 sc1\n\t"
      "global_load_dwordx4 v[168:171], v244, s[44:45] sc1\n\t"
      "global_load_dwordx4 v[172:175], v244, s[44:45] offset:1024 sc1\n\t"
      "global_load_dwordx4 v[176:179], v244, s[44:45] offset:2048 sc1\n\t"
      "global_load_dwordx4 v[180:183], v244, s[44:45] offset:3072 sc1\n\t"
      "global_load_dwordx4 v[184:187], v244, s[42:43] offset:-4096 sc1\n\t"
      "global_load_dwordx4 v[188:191], v244, s[42:43] offset:-3072 sc1\n\t"
      "s_waitcnt vmcnt(0)\n\t"
      "v_max3_u32 v232, v128, v129, v130\n\t"
      "v_max3_u32 v232, v232, v131, v132\n\t"
      "v_max3_u32 v232, v232, v133, v134\n\t"
      "v_max_u32_e32 v232, v232, v135\n\t"
      "v_cmp_eq_u32_e32 vcc, -1, v232\n\t"
      "s_cbranch_vccnz LO0L_%=\n\t"
      "LO0X_%=:\n\t"
      "s_branch LO0B_%=\n\t"
      "LO2R_%=:\n\t"
      "s_mov_b32 s80, 0\n\t"
      "LO2L_%=:\n\t"
      "s_add_u32 s80, s80, 1\n\t"
      "s_cmp_lt_u32 s80, 262144\n\t"
      "s_cbranch_scc0 LO2X_%=\n\t"
      "s_waitcnt vmcnt(0)\n\t"
      "s_sleep 1\n\t"
      "global_load_dwordx4 v[136:139], v244, s[42:43] sc1\n\t"
      "global_load_dwordx4 v[140:143], v244, s[42:43] offset:1024 sc1\n\t"
      "global_load_dwordx4 v[144:147], v244, s[42:43] offset:2048 sc1\n\t"
      "global_load_dwordx4 v[148:151], v244, s[42:43] offset:3072 sc1\n\t"
      "global_load_dwordx4 v[152:155], v244, s[44:45] offset:-4096 sc1\n\t"
      "global_load_dwordx4 v[156:159], v244, s[44:45] offset:-3072 sc1\n\t"
      "global_load_dwordx4 v[160:163], v244, s[44:45] offset:-2048 sc1\n\t"
      "global_load_dwordx4 v[164:167], v244, s[44:45] offset:-1024 sc1\n\t"
      "global_load_dwordx4 v[168:171], v244, s[44:45] sc1\n\t"
      "global_load_dwordx4 v[172:175], v244, s[44:45] offset:1024 sc1\n\t"
      "global_load_dwordx4 v[176:179], v244, s[44:45] offset:2048 sc1\n\t"
      "global_load_dwordx4 v[180:183], v244, s[44:45] offset:3072 sc1\n\t"
      "global_load_dwordx4 v[184:187], v244, s[42:43] offset:-4096 sc1\n\t"
      "global_load_dwordx4 v[188:191], v244, s[42:43] offset:-3072 sc1\n\t"
      "s_waitcnt vmcnt(0)\n\t"
      "v_max3_u32 v232, v136, v137, v138\n\t"
      "v_max3_u32 v232, v232, v139, v140\n\t"
      "v_max3_u32 v232, v232, v141, v142\n\t"
      "v_max_u32_e32 v232, v232, v143\n\t"
      "v_cmp_eq_u32_e32 vcc, -1, v232\n\t"
      "s_cbranch_vccnz LO2L_%=\n\t"
      "LO2X_%=:\n\t"
      "s_branch LO2B_%=\n\t"
      "LO4R_%=:\n\t"
      "s_mov_b32 s80, 0\n\t"
      "LO4L_%=:\n\t"
      "s_add_u32 s80, s80, 1\n\t"
      "s_cmp_lt_u32 s80, 262144\n\t"
      "s_cbranch_scc0 LO4X_%=\n\t"
      "s_waitcnt vmcnt(0)\n\t"
      "s_sleep 1\n\t"
      "global_load_dwordx4 v[144:147], v244, s[42:43] offset:2048 sc1\n\t"
      "global_load_dwordx4 v[148:151], v244, s[42:43] offset:3072 sc1\n\t"
      "global_load_dwordx4 v[152:155], v244, s[44:45] offset:-4096 sc1\n\t"
      "global_load_dwordx4 v[156:159], v244, s[44:45] offset:-3072 sc1\n\t"
      "global_load_dwordx4 v[160:163], v244, s[44:45] offset:-2048 sc1\n\t"
      "global_load_dwordx4 v[164:167], v244, s[44:45] offset:-1024 sc1\n\t"
      "global_load_dwordx4 v[168:171], v244, s[44:45] sc1\n\t"
      "global_load_dwordx4 v[172:175], v244, s[44:45] offset:1024 sc1\n\t"
      "global_load_dwordx4 v[176:179], v244, s[44:45] offset:2048 sc1\n\t"
      "global_load_dwordx4 v[180:183], v244, s[44:45] offset:3072 sc1\n\t"
      "global_load_dwordx4 v[184:187], v244, s[42:43] offset:-4096 sc1\n\t"
      "global_load_dwordx4 v[188:191], v244, s[42:43] offset:-3072 sc1\n\t"
      "s_waitcnt vmcnt(0)\n\t"
      "v_max3_u32 v232, v144, v145, v146\n\t"
      "v_max3_u32 v232, v232, v147, v148\n\t"
      "v_max3_u32 v232, v232, v149, v150\n\t"
      "v_max_u32_e32 v232, v232, v151\n\t"
      "v_cmp_eq_u32_e32 vcc, -1, v232\n\t"
      "s_cbranch_vccnz LO4L_%=\n\t"
      "LO4X_%=:\n\t"
      "s_branch LO4B_%=\n\t"
      "LO6R_%=:\n\t"
      "s_mov_b32 s80, 0\n\t"
      "LO6L_%=:\n\t"
      "s_add_u32 s80, s80, 1\n\t"
      "s_cmp_lt_u32 s80, 262144\n\t"
      "s_cbranch_scc0 LO6X_%=\n\t"
      "s_waitcnt vmcnt(0)\n\t"
      "s_sleep 1\n\t"
      "global_load_dwordx4 v[152:155], v244, s[44:45] offset:-4096 sc1\n\t"
      "global_load_dwordx4 v[156:159], v244, s[44:45] offset:-3072 sc1\n\t"
      "global_load_dwordx4 v[160:163], v244, s[44:45] offset:-2048 sc1\n\t"
      "global_load_dwordx4 v[164:167], v244, s[44:45] offset:-1024 sc1\n\t"
      "global_load_dwordx4 v[168:171], v244, s[44:45] sc1\n\t"
      "global_load_dwordx4 v[172:175], v244, s[44:45] offset:1024 sc1\n\t"
      "global_load_dwordx4 v[176:179], v244, s[44:45] offset:2048 sc1\n\t"
      "global_load_dwordx4 v[180:183], v244, s[44:45] offset:3072 sc1\n\t"
      "global_load_dwordx4 v[184:187], v244, s[42:43] offset:-4096 sc1\n\t"
      "global_load_dwordx4 v[188:191], v244, s[42:43] offset:-3072 sc1\n\t"
      "s_waitcnt vmcnt(0)\n\t"
      "v_max3_u32 v232, v152, v153, v154\n\t"
      "v_max3_u32 v232, v232, v155, v156\n\t"
      "v_max3_u32 v232, v232, v157, v158\n\t"
      "v_max_u32_e32 v232, v232, v159\n\t"
      "v_cmp_eq_u32_e32 vcc, -1, v232\n\t"
      "s_cbranch_vccnz LO6L_%=\n\t"
      "LO6X_%=:\n\t"
      "s_branch LO6B_%=\n\t"
      "LO8R_%=:\n\t"
      "s_mov_b32 s80, 0\n\t"
      "LO8L_%=:\n\t"
      "s_add_u32 s80, s80, 1\n\t"
      "s_cmp_lt_u32 s80, 262144\n\t"
      "s_cbranch_scc0 LO8X_%=\n\t"
      "s_waitcnt vmcnt(0)\n\t"
      "s_sleep 1\n\t"
      "global_load_dwordx4 v[160:163], v244, s[44:45] offset:-2048 sc1\n\t"
      "global_load_dwordx4 v[164:167], v244, s[44:45] offset:-1024 sc1\n\t"
      "global_load_dwordx4 v[168:171], v244, s[44:45] sc1\n\t"
      "global_load_dwordx4 v[172:175], v244, s[44:45] offset:1024 sc1\n\t"
      "global_load_dwordx4 v[176:179], v244, s[44:45] offset:2048 sc1\n\t"
      "global_load_dwordx4 v[180:183], v244, s[44:45] offset:3072 sc1\n\t"
      "global_load_dwordx4 v[184:187], v244, s[42:43] offset:-4096 sc1\n\t"
      "global_load_dwordx4 v[188:191], v244, s[42:43] offset:-3072 sc1\n\t"
      "s_waitcnt vmcnt(0)\n\t"
      "v_max3_u32 v232, v160, v161, v162\n\t"
      "v_max3_u32 v232, v232, v163, v164\n\t"
      "v_max3_u32 v232, v232, v165, v166\n\t"
      "v_max_u32_e32 v232, v232, v167\n\t"
      "v_cmp_eq_u32_e32 vcc, -1, v232\n\t"
      "s_cbranch_vccnz LO8L_%=\n\t"
      "LO8X_%=:\n\t"
      "s_branch LO8B_%=\n\t"
      "LO10R_%=:\n\t"
      "s_mov_b32 s80, 0\n\t"
      "LO10L_%=:\n\t"
      "s_add_u32 s80, s80, 1\n\t"
      "s_cmp_lt_u32 s80, 262144\n\t"
      "s_cbranch_scc0 LO10X_%=\n\t"
      "s_waitcnt vmcnt(0)\n\t"
      "s_sleep 1\n\t"
      "global_load_dwordx4 v[168:171], v244, s[44:45] sc1\n\t"
      "global_load_dwordx4 v[172:175], v244, s[44:45] offset:1024 sc1\n\t"
      "global_load_dwordx4 v[176:179], v244, s[44:45] offset:2048 sc1\n\t"
      "global_load_dwordx4 v[180:183], v244, s[44:45] offset:3072 sc1\n\t"
      "global_load_dwordx4 v[184:187], v244, s[42:43] offset:-4096 sc1\n\t"
      "global_load_dwordx4 v[188:191], v244, s[42:43] offset:-3072 sc1\n\t"
      "s_waitcnt vmcnt(0)\n\t"
      "v_max3_u32 v232, v168, v169, v170\n\t"
      "v_max3_u32 v232, v232, v171, v172\n\t"
      "v_max3_u32 v232, v232, v173, v174\n\t"
      "v_max_u32_e32 v232, v232, v175\n\t"
      "v_cmp_eq_u32_e32 vcc, -1, v232\n\t"
      "s_cbranch_vccnz LO10L_%=\n\t"
      "LO10X_%=:\n\t"
      "s_branch LO10B_%=\n\t"
      "LO12R_%=:\n\t"
      "s_mov_b32 s80, 0\n\t"
      "LO12L_%=:\n\t"
      "s_add_u32 s80, s80, 1\n\t"
      "s_cmp_lt_u32 s80, 262144\n\t"
      "s_cbranch_scc0 LO12X_%=\n\t"
      "s_waitcnt vmcnt(0)\n\t"
      "s_sleep 1\n\t"
      "global_load_dwordx4 v[176:179], v244, s[44:45] offset:2048 sc1\n\t"
      "global_load_dwordx4 v[180:183], v244, s[44:45] offset:3072 sc1\n\t"
      "global_load_dwordx4 v[184:187], v244, s[42:43] offset:-4096 sc1\n\t"
      "global_load_dwordx4 v[188:191], v244, s[42:43] offset:-3072 sc1\n\t"
      "s_waitcnt vmcnt(0)\n\t"
      "v_max3_u32 v232, v176, v177, v178\n\t"
      "v_max3_u32 v232, v232, v179, v180\n\t"
      "v_max3_u32 v232, v232, v181, v182\n\t"
      "v_max_u32_e32 v232, v232, v183\n\t"
      "v_cmp_eq_u32_e32 vcc, -1, v232\n\t"
      "s_cbranch_vccnz LO12L_%=\n\t"
      "LO12X_%=:\n\t"
      "s_branch LO12B_%=\n\t"
      "LO14R_%=:\n\t"
      "s_mov_b32 s80, 0\n\t"
      "LO14L_%=:\n\t"
      "s_add_u32 s80, s80, 1\n\t"
      "s_cmp_lt_u32 s80, 262144\n\t"
      "s_cbranch_scc0 LO14X_%=\n\t"
      "s_waitcnt vmcnt(0)\n\t"
      "s_sleep 1\n\t"
      "global_load_dwordx4 v[184:187], v244, s[42:43] offset:-4096 sc1\n\t"
      "global_load_dwordx4 v[188:191], v244, s[42:43] offset:-3072 sc1\n\t"
      "s_waitcnt vmcnt(0)\n\t"
      "v_max3_u32 v232, v184, v185, v186\n\t"
      "v_max3_u32 v232, v232, v187, v188\n\t"
      "v_max3_u32 v232, v232, v189, v190\n\t"
      "v_max_u32_e32 v232, v232, v191\n\t"
      "v_cmp_eq_u32_e32 vcc, -1, v232\n\t"
      "s_cbranch_vccnz LO14L_%=\n\t"
      "LO14X_%=:\n\t"
      "s_branch LO14B_%=\n\t"
      "LGX_%=:\n\t"
      "v_max3_u32 v232, v128, v129, v130\n\t"
      "v_max3_u32 v232, v232, v131, v132\n\t"
      "v_max3_u32 v232, v232, v133, v134\n\t"
      "v_max3_u32 v232, v232, v135, v136\n\t"
      "v_max3_u32 v232, v232, v137, v138\n\t"
      "v_max3_u32 v232, v232, v139, v140\n\t"
      "v_max3_u32 v232, v232, v141, v142\n\t"
      "v_max3_u32 v232, v232, v143, v144\n\t"
      "v_max3_u32 v232, v232, v145, v146\n\t"
      "v_max3_u32 v232, v232, v147, v148\n\t"
      "v_max3_u32 v232, v232, v149, v150\n\t"
      "v_max3_u32 v232, v232, v151, v152\n\t"
      "v_max3_u32 v232, v232, v153, v154\n\t"
      "v_max3_u32 v232, v232, v155, v156\n\t"
      "v_max3_u32 v232, v232, v157, v158\n\t"
      "v_max3_u32 v232, v232, v159, v160\n\t"
      "v_max3_u32 v232, v232, v161, v162\n\t"
      "v_max3_u32 v232, v232, v163, v164\n\t"
      "v_max3_u32 v232, v232, v165, v166\n\t"
      "v_max3_u32 v232, v232, v167, v168\n\t"
      "v_max3_u32 v232, v232, v169, v170\n\t"
      "v_max3_u32 v232, v232, v171, v172\n\t"
      "v_max3_u32 v232, v232, v173, v174\n\t"
      "v_max3_u32 v232, v232, v175, v176\n\t"
      "v_max3_u32 v232, v232, v177, v178\n\t"
      "v_max3_u32 v232, v232, v179, v180\n\t"
      "v_max3_u32 v232, v232, v181, v182\n\t"
      "v_max3_u32 v232, v232, v183, v184\n\t"
      "v_max3_u32 v232, v232, v185, v186\n\t"
      "v_max3_u32 v232, v232, v187, v188\n\t"
      "v_max3_u32 v232, v232, v189, v190\n\t"
      "v_max_u32_e32 v232, v232, v191\n\t"
      "v_cmp_eq_u32_e32 vcc, -1, v232\n\t"
      "s_cbranch_vccz LGB_%=\n\t"
      "LGYR_%=:\n\t"
      "s_mov_b32 s80, 0\n\t"
      "LGYL_%=:\n\t"
      "s_add_u32 s80, s80, 1\n\t"
      "s_cmp_lt_u32 s80, 262144\n\t"
      "s_cbranch_scc0 LGYX_%=\n\t"
      "s_sleep 1\n\t"
      "global_load_dwordx4 v[128:131], v244, s[42:43] offset:-2048 sc1\n\t"
      "global_load_dwordx4 v[132:135], v244, s[42:43] offset:-1024 sc1\n\t"
      "global_load_dwordx4 v[136:139], v244, s[42:43] sc1\n\t"
      "global_load_dwordx4 v[140:143], v244, s[42:43] offset:1024 sc1\n\t"
      "global_load_dwordx4 v[144:147], v244, s[42:43] offset:2048 sc1\n\t"
      "global_load_dwordx4 v[148:151], v244, s[42:43] offset:3072 sc1\n\t"
      "global_load_dwordx4 v[152:155], v244, s[44:45] offset:-4096 sc1\n\t"
      "global_load_dwordx4 v[156:159], v244, s[44:45] offset:-3072 sc1\n\t"
      "global_load_dwordx4 v[160:163], v244, s[44:45] offset:-2048 sc1\n\t"
      "global_load_dwordx4 v[164:167], v244, s[44:45] offset:-1024 sc1\n\t"
      "global_load_dwordx4 v[168:171], v244, s[44:45] sc1\n\t"
      "global_load_dwordx4 v[172:175], v244, s[44:45] offset:1024 sc1\n\t"
      "global_load_dwordx4 v[176:179], v244, s[44:45] offset:2048 sc1\n\t"
      "global_load_dwordx4 v[180:183], v244, s[44:45] offset:3072 sc1\n\t"
      "global_load_dwordx4 v[184:187], v244, s[42:43] offset:-4096 sc1\n\t"
      "global_load_dwordx4 v[188:191], v244, s[42:43] offset:-3072 sc1\n\t"
      "s_waitcnt vmcnt(0)\n\t"
      "v_max3_u32 v232, v128, v129, v130\n\t"
      "v_max3_u32 v232, v232, v131, v132\n\t"
      "v_max3_u32 v232, v232, v133, v134\n\t"
      "v_max3_u32 v232, v232, v135, v136\n\t"
      "v_max3_u32 v232, v232, v137, v138\n\t"
      "v_max3_u32 v232, v232, v139, v140\n\t"
      "v_max3_u32 v232, v232, v141, v142\n\t"
      "v_max3_u32 v232, v232, v143, v144\n\t"
      "v_max3_u32 v232, v232, v145, v146\n\t"
      "v_max3_u32 v232, v232, v147, v148\n\t"
      "v_max3_u32 v232, v232, v149, v150\n\t"
      "v_max3_u32 v232, v232, v151, v152\n\t"
      "v_max3_u32 v232, v232, v153, v154\n\t"
      "v_max3_u32 v232, v232, v155, v156\n\t"
      "v_max3_u32 v232, v232, v157, v158\n\t"
      "v_max3_u32 v232, v232, v159, v160\n\t"
      "v_max3_u32 v232, v232, v161, v162\n\t"
      "v_max3_u32 v232, v232, v163, v164\n\t"
      "v_max3_u32 v232, v232, v165, v166\n\t"
      "v_max3_u32 v232, v232, v167, v168\n\t"
      "v_max3_u32 v232, v232, v169, v170\n\t"
      "v_max3_u32 v232, v232, v171, v172\n\t"
      "v_max3_u32 v232, v232, v173, v174\n\t"
      "v_max3_u32 v232, v232, v175, v176\n\t"
      "v_max3_u32 v232, v232, v177, v178\n\t"
      "v_max3_u32 v232, v232, v179, v180\n\t"
      "v_max3_u32 v232, v232, v181, v182\n\t"
      "v_max3_u32 v232, v232, v183, v184\n\t"
      "v_max3_u32 v232, v232, v185, v186\n\t"
      "v_max3_u32 v232, v232, v187, v188\n\t"
      "v_max3_u32 v232, v232, v189, v190\n\t"
      "v_max_u32_e32 v232, v232, v191\n\t"
      "v_cmp_eq_u32_e32 vcc, -1, v232\n\t"
      "s_cbranch_vccnz LGYL_%=\n\t"
      "LGYX_%=:\n\t"
      "s_cbranch_vccnz LGB_%=\n\t"
      "s_waitcnt lgkmcnt(0)\n\t"
      "ds_read_b64_tr_b16 v[0:1], v216 offset:7168\n\t"
      "ds_read_b64_tr_b16 v[2:3], v220 offset:7168\n\t"
      "ds_read_b64_tr_b16 v[4:5], v217 offset:7168\n\t"
      "ds_read_b64_tr_b16 v[6:7], v221 offset:7168\n\t"
      "ds_read_b64_tr_b16 v[8:9], v218 offset:7168\n\t"
      "ds_read_b64_tr_b16 v[10:11], v222 offset:7168\n\t"
      "ds_read_b64_tr_b16 v[12:13], v219 offset:7168\n\t"
      "ds_read_b64_tr_b16 v[14:15], v223 offset:7168\n\t"
      "ds_read_b64_tr_b16 v[16:17], v218 offset:10752\n\t"
      "ds_read_b64_tr_b16 v[18:19], v222 offset:10752\n\t"
      "ds_read_b64_tr_b16 v[20:21], v219 offset:10752\n\t"
      "ds_read_b64_tr_b16 v[22:23], v223 offset:10752\n\t"
      "ds_read_b64_tr_b16 v[24:25], v216 offset:10752\n\t"
      "ds_read_b64_tr_b16 v[26:27], v220 offset:10752\n\t"
      "ds_read_b64_tr_b16 v[28:29], v217 offset:10752\n\t"
      "ds_read_b64_tr_b16 v[30:31], v221 offset:10752\n\t"
      "ds_read_b64_tr_b16 v[32:33], v216 offset:14336\n\t"
      "ds_read_b64_tr_b16 v[34:35], v220 offset:14336\n\t"
      "ds_read_b64_tr_b16 v[36:37], v217 offset:14336\n\t"
      "ds_read_b64_tr_b16 v[38:39], v221 offset:14336\n\t"
      "ds_read_b64_tr_b16 v[40:41], v218 offset:14336\n\t"
      "ds_read_b64_tr_b16 v[42:43], v222 offset:14336\n\t"
      "ds_read_b64_tr_b16 v[44:45], v219 offset:14336\n\t"
      "ds_read_b64_tr_b16 v[46:47], v223 offset:14336\n\t"
      "ds_read_b64_tr_b16 v[48:49], v218 offset:17920\n\t"
      "ds_read_b64_tr_b16 v[50:51], v222 offset:17920\n\t"
      "ds_read_b64_tr_b16 v[52:53], v219 offset:17920\n\t"
      "ds_read_b64_tr_b16 v[54:55], v223 offset:17920\n\t"
      "ds_read_b64_tr_b16 v[56:57], v216 offset:17920\n\t"
      "ds_read_b64_tr_b16 v[58:59], v220 offset:17920\n\t"
      "ds_read_b64_tr_b16 v[60:61], v217 offset:17920\n\t"
      "ds_read_b64_tr_b16 v[62:63], v221 offset:17920\n\t"
      "ds_read_b64_tr_b16 v[64:65], v216 offset:21504\n\t"
      "ds_read_b64_tr_b16 v[66:67], v220 offset:21504\n\t"
      "ds_read_b64_tr_b16 v[68:69], v217 offset:21504\n\t"
      "ds_read_b64_tr_b16 v[70:71], v221 offset:21504\n\t"
      "ds_read_b64_tr_b16 v[72:73], v218 offset:21504\n\t"
      "ds_read_b64_tr_b16 v[74:75], v222 offset:21504\n\t"
      "ds_read_b64_tr_b16 v[76:77], v219 offset:21504\n\t"
      "ds_read_b64_tr_b16 v[78:79], v223 offset:21504\n\t"
      "ds_read_b64_tr_b16 v[80:81], v218 offset:25088\n\t"
      "ds_read_b64_tr_b16 v[82:83], v222 offset:25088\n\t"
      "ds_read_b64_tr_b16 v[84:85], v219 offset:25088\n\t"
      "ds_read_b64_tr_b16 v[86:87], v223 offset:25088\n\t"
      "ds_read_b64_tr_b16 v[88:89], v216 offset:25088\n\t"
      "ds_read_b64_tr_b16 v[90:91], v220 offset:25088\n\t"
      "ds_read_b64_tr_b16 v[92:93], v217 offset:25088\n\t"
      "ds_read_b64_tr_b16 v[94:95], v221 offset:25088\n\t"
      "ds_read_b64_tr_b16 v[96:97], v216 offset:28672\n\t"
      "ds_read_b64_tr_b16 v[98:99], v220 offset:28672\n\t"
      "ds_read_b64_tr_b16 v[100:101], v217 offset:28672\n\t"
      "ds_read_b64_tr_b16 v[102:103], v221 offset:28672\n\t"
      "ds_read_b64_tr_b16 v[104:105], v218 offset:28672\n\t"
      "ds_read_b64_tr_b16 v[106:107], v222 offset:28672\n\t"
      "ds_read_b64_tr_b16 v[108:109], v219 offset:28672\n\t"
      "ds_read_b64_tr_b16 v[110:111], v223 offset:28672\n\t"
      "ds_read_b64_tr_b16 v[112:113], v218 offset:32256\n\t"
      "ds_read_b64_tr_b16 v[114:115], v222 offset:32256\n\t"
      "ds_read_b64_tr_b16 v[116:117], v219 offset:32256\n\t"
      "ds_read_b64_tr_b16 v[118:119], v223 offset:32256\n\t"
      "ds_read_b64_tr_b16 v[120:121], v216 offset:32256\n\t"
      "ds_read_b64_tr_b16 v[122:123], v220 offset:32256\n\t"
      "ds_read_b64_tr_b16 v[124:125], v217 offset:32256\n\t"
      "ds_read_b64_tr_b16 v[126:127], v221 offset:32256\n\t"
      "v_lshlrev_b32_e32 v192, 16, v208\n\t"
      "v_and_b32_e32 v193, 0xffff0000, v208\n\t"
      "v_lshlrev_b32_e32 v194, 16, v209\n\t"
      "v_and_b32_e32 v195, 0xffff0000, v209\n\t"
      "v_lshlrev_b32_e32 v196, 16, v210\n\t"
      "v_and_b32_e32 v197, 0xffff0000, v210\n\t"
      "v_lshlrev_b32_e32 v198, 16, v211\n\t"
      "v_and_b32_e32 v199, 0xffff0000, v211\n\t"
      "v_lshlrev_b32_e32 v200, 16, v212\n\t"
      "v_and_b32_e32 v201, 0xffff0000, v212\n\t"
      "v_lshlrev_b32_e32 v202, 16, v213\n\t"
      "v_and_b32_e32 v203, 0xffff0000, v213\n\t"
      "v_lshlrev_b32_e32 v204, 16, v214\n\t"
      "v_and_b32_e32 v205, 0xffff0000, v214\n\t"
      "v_lshlrev_b32_e32 v206, 16, v215\n\t"
      "v_and_b32_e32 v207, 0xffff0000, v215\n\t"
      "s_waitcnt lgkmcnt(0)\n\t"
      "s_branch LGM_%=\n\t"
      "LSR_%=:\n\t"
      "s_mov_b32 s80, 0\n\t"
      "LSL_%=:\n\t"
      "s_add_u32 s80, s80, 1\n\t"
      "s_cmp_lt_u32 s80, 262144\n\t"
      "s_cbranch_scc0 LSX_%=\n\t"
      "s_sleep 1\n\t"
      "global_load_dwordx2 v[148:149], v231, s[46:47] sc1\n\t"
      "global_load_dwordx2 v[150:151], v231, s[46:47] offset:384 sc1\n\t"
      "s_waitcnt vmcnt(0)\n\t"
      "v_max3_u32 v232, v148, v149, v150\n\t"
      "v_max_u32_e32 v232, v232, v151\n\t"
      "v_cmp_eq_u32_e32 vcc, -1, v232\n\t"
      "s_cbranch_vccnz LSL_%=\n\t"
      "LSX_%=:\n\t"
      "s_branch LSB_%=\n\t"
      "LF1R_%=:\n\t"
      "s_mov_b32 s80, 0\n\t"
      "LF1L_%=:\n\t"
      "s_add_u32 s80, s80, 1\n\t"
      "s_cmp_lt_u32 s80, 262144\n\t"
      "s_cbranch_scc0 LF1X_%=\n\t"
      "s_sleep 2\n\t"
      "global_load_dwordx2 v[138:139], v136, s[60:61] sc1\n\t"
      "s_waitcnt vmcnt(0)\n\t"
      "v_cmp_ne_u64_e32 vcc, s[22:23], v[138:139]\n\t"
      "s_cbranch_vccnz LF1L_%=\n\t"
      "LF1X_%=:\n\t"
      "s_branch LF1B_%=\n\t"
      "LF4R_%=:\n\t"
      "s_mov_b32 s80, 0\n\t"
      "LF4L_%=:\n\t"
      "s_add_u32 s80, s80, 1\n\t"
      "s_cmp_lt_u32 s80, 262144\n\t"
      "s_cbranch_scc0 LF4X_%=\n\t"
      "s_sleep 2\n\t"
      "global_load_dwordx2 v[138:139], v137, s[60:61] sc1\n\t"
      "s_waitcnt vmcnt(0)\n\t"
      "v_cmp_ne_u64_e32 vcc, s[22:23], v[138:139]\n\t"
      "s_cbranch_vccnz LF4L_%=\n\t"
      "LF4X_%=:\n\t"
      "s_branch LF4B_%=\n\t"
      "L4_%=:\n\t"
      "s_waitcnt vmcnt(1)\n\t"
      "s_barrier\n\t"
      "s_barrier\n\t"
      "s_lshl2_add_u32 s72, s31, s24\n\t"
      "s_sub_u32 s72, s72, 56\n\t"
      "s_mov_b32 m0, 64512\n\t"
      "s_cmp_ge_u32 s72, 64\n\t"
      "s_cselect_b32 s73, 8, 0\n\t"
      "s_sub_u32 s73, s72, s73\n\t"
      "s_and_b32 s73, s73, 0x38\n\t"
      "s_add_u32 s72, s72, s73\n\t"
      "s_add_u32 s72, s72, 8\n\t"
      "s_mul_i32 s72, s72, 57344\n\t"
      "s_and_b32 s73, s2, 7\n\t"
      "s_mul_i32 s73, s73, 448\n\t"
      "s_and_b32 s73, s73, 0xffffff80\n\t"
      "s_add_u32 s72, s72, s73\n\t"
      "v_bfe_u32 v141, v192, 2, 4\n\t"
      "v_mul_u32_u24_e32 v141, 3584, v141\n\t"
      "v_lshl_add_u32 v141, v196, 7, v141\n\t"
      "v_add_u32_e32 v141, s72, v141\n\t"
      "global_load_lds_dword v141, s[10:11]\n\t"
      "s_barrier\n\t"
      "s_cmp_lg_u32 s24, 3\n\t"
      "s_cbranch_scc1 SK12_%=\n\t"
      "global_load_dwordx2 v[138:139], v137, s[60:61] sc1\n\t"
      "SK12_%=:\n\t"
      "s_cmp_lg_u32 s24, 3\n\t"
      "s_cbranch_scc1 LF4B_%=\n\t"
      "s_waitcnt vmcnt(0)\n\t"
      "v_cmp_ne_u64_e32 vcc, s[22:23], v[138:139]\n\t"
      "s_cbranch_vccnz LF4R_%=\n\t"
      "LF4B_%=:\n\t"
      "s_barrier\n\t"
      "s_waitcnt vmcnt(0)\n\t"
      "s_barrier\n\t"
      "s_branch G4_%=\n\t"
      "LF2R_%=:\n\t"
      "s_mov_b32 s80, 0\n\t"
      "LF2L_%=:\n\t"
      "s_add_u32 s80, s80, 1\n\t"
      "s_cmp_lt_u32 s80, 262144\n\t"
      "s_cbranch_scc0 LF2X_%=\n\t"
      "s_sleep 2\n\t"
      "global_load_dwordx2 v[138:139], v137, s[60:61] sc1\n\t"
      "s_waitcnt vmcnt(0)\n\t"
      "v_cmp_ne_u64_e32 vcc, s[22:23], v[138:139]\n\t"
      "s_cbranch_vccnz LF2L_%=\n\t"
      "LF2X_%=:\n\t"
      "s_branch LF2B_%=\n\t"
      "LF3R_%=:\n\t"
      "s_mov_b32 s80, 0\n\t"
      "LF3L_%=:\n\t"
      "s_add_u32 s80, s80, 1\n\t"
      "s_cmp_lt_u32 s80, 262144\n\t"
      "s_cbranch_scc0 LF3X_%=\n\t"
      "s_sleep 2\n\t"
      "global_load_dwordx2 v[138:139], v143, s[60:61] sc1\n\t"
      "s_waitcnt vmcnt(0)\n\t"
      "v_cmp_ne_u64_e32 vcc, s[22:23], v[138:139]\n\t"
      "s_cbranch_vccnz LF3L_%=\n\t"
      "LF3X_%=:\n\t"
      "s_branch LF3B_%=\n\t"
      "LF70R_%=:\n\t"
      "s_mov_b32 s80, 0\n\t"
      "LF70L_%=:\n\t"
      "s_add_u32 s80, s80, 1\n\t"
      "s_cmp_lt_u32 s80, 262144\n\t"
      "s_cbranch_scc0 LF70X_%=\n\t"
      "s_sleep 4\n\t"
      "global_load_dwordx2 v[138:139], v136, s[60:61] sc1\n\t"
      "s_waitcnt vmcnt(0)\n\t"
      "v_cmp_ne_u64_e32 vcc, s[22:23], v[138:139]\n\t"
      "s_cbranch_vccnz LF70L_%=\n\t"
      "LF70X_%=:\n\t"
      "s_branch LF70B_%=\n\t"
      "LF71R_%=:\n\t"
      "s_mov_b32 s80, 0\n\t"
      "LF71L_%=:\n\t"
      "s_add_u32 s80, s80, 1\n\t"
      "s_cmp_lt_u32 s80, 262144\n\t"
      "s_cbranch_scc0 LF71X_%=\n\t"
      "s_sleep 4\n\t"
      "global_load_dwordx2 v[138:139], v136, s[60:61] sc1\n\t"
      "s_waitcnt vmcnt(0)\n\t"
      "v_cmp_ne_u64_e32 vcc, s[22:23], v[138:139]\n\t"
      "s_cbranch_vccnz LF71L_%=\n\t"
      "LF71X_%=:\n\t"
      "s_branch LF71B_%=\n\t"
      "SPARE_%=:\n\t"
      "v_mov_b32_e32 v132, -1\n\t"
      "v_mov_b32_e32 v133, -1\n\t"
      "v_mov_b32_e32 v134, -1\n\t"
      "v_mov_b32_e32 v135, -1\n\t"
      "s_and_b32 s34, s25, 3\n\t"
      "s_lshl_b32 s34, s34, 15\n\t"
      "s_mul_i32 s35, s24, 917504\n\t"
      "s_add_u32 s34, s34, s35\n\t"
      "s_and_b32 s35, s2, 7\n\t"
      "s_lshl_b32 s35, s35, 9\n\t"
      "s_add_u32 s34, s34, s35\n\t"
      "v_and_b32_e32 v141, 0x1c0, v244\n\t"
      "v_lshlrev_b32_e32 v141, 6, v141\n\t"
      "v_and_b32_e32 v142, 0x30, v244\n\t"
      "v_lshl_add_u32 v141, v142, 3, v141\n\t"
      "v_add_u32_e32 v141, s34, v141\n\t"
      "s_mov_b32 m0, 64512\n\t"
      "s_lshr_b32 s34, s24, 1\n\t"
      "s_lshl_b32 s34, s34, 5\n\t"
      "s_add_u32 s34, s34, s25\n\t"
      "s_and_b32 s35, s24, 1\n\t"
      "s_lshl1_add_u32 s34, s34, s35\n\t"
      "s_lshl_b32 s34, s34, 10\n\t"
      "s_add_u32 s34, s34, 8388608\n\t"
      "s_add_u32 s40, s20, s34\n\t"
      "s_addc_u32 s41, s21, 0\n\t"
      "global_store_dwordx4 v244, v[132:135], s[40:41] sc1\n\t"
      "s_add_u32 s40, s40, 262144\n\t"
      "s_addc_u32 s41, s41, 0\n\t"
      "global_store_dwordx4 v244, v[132:135], s[40:41] sc1\n\t"
      "s_add_u32 s40, s40, 262144\n\t"
      "s_addc_u32 s41, s41, 0\n\t"
      "global_store_dwordx4 v244, v[132:135], s[40:41] sc1\n\t"
      "s_add_u32 s40, s40, 262144\n\t"
      "s_addc_u32 s41, s41, 0\n\t"
      "global_store_dwordx4 v244, v[132:135], s[40:41] sc1\n\t"
      "s_add_u32 s40, s40, 262144\n\t"
      "s_addc_u32 s41, s41, 0\n\t"
      "global_store_dwordx4 v244, v[132:135], s[40:41] sc1\n\t"
      "s_add_u32 s40, s40, 262144\n\t"
      "s_addc_u32 s41, s41, 0\n\t"
      "global_store_dwordx4 v244, v[132:135], s[40:41] sc1\n\t"
      "s_add_u32 s40, s40, 262144\n\t"
      "s_addc_u32 s41, s41, 0\n\t"
      "global_store_dwordx4 v244, v[132:135], s[40:41] sc1\n\t"
      "s_add_u32 s40, s40, 262144\n\t"
      "s_addc_u32 s41, s41, 0\n\t"
      "s_lshl_b32 s34, s25, 7\n\t"
      "s_add_u32 s34, s34, 16384\n\t"
      "v_mov_b32_e32 v131, s34\n\t"
      "s_waitcnt vmcnt(0)\n\t"
      "s_barrier\n\t"
      "s_cmp_lg_u32 s24, 0\n\t"
      "s_cbranch_scc1 SP0_%=\n\t"
      "s_mov_b64 exec, 1\n\t"
      "global_store_dwordx2 v131, v[128:129], s[60:61] sc1\n\t"
      "s_mov_b64 exec, -1\n\t"
      "SP0_%=:\n\t"
      "s_and_b32 s34, s25, 3\n\t"
      "s_lshl3_add_u32 s34, s34, s24\n\t"
      "s_cmp_lg_u32 s34, 19\n\t"
      "s_cbranch_scc1 SQ0_%=\n\t"
      "v_and_b32_e32 v136, 0x1f0, v244\n\t"
      "v_lshlrev_b32_e32 v131, 6, v136\n\t"
      "v_lshlrev_b32_e32 v136, 3, v136\n\t"
      "v_add_u32_e32 v136, 16384, v136\n\t"
      "s_and_b32 s34, s2, 7\n\t"
      "s_lshl_b32 s34, s34, 7\n\t"
      "s_add_u32 s34, s34, 32768\n\t"
      "v_add_u32_e32 v131, s34, v131\n\t"
      "s_cmp_lg_u32 s24, 3\n\t"
      "s_cbranch_scc1 SK15_%=\n\t"
      "global_load_dwordx2 v[138:139], v136, s[60:61] sc1\n\t"
      "SK15_%=:\n\t"
      "s_cmp_lg_u32 s24, 3\n\t"
      "s_cbranch_scc1 LF70B_%=\n\t"
      "s_waitcnt vmcnt(0)\n\t"
      "v_cmp_ne_u64_e32 vcc, s[22:23], v[138:139]\n\t"
      "s_cbranch_vccnz LF70R_%=\n\t"
      "LF70B_%=:\n\t"
      "global_store_dwordx2 v131, v[128:129], s[60:61] sc1\n\t"
      "SQ0_%=:\n\t"
      "s_barrier\n\t"
      "s_lshl3_add_u32 s34, s25, s24\n\t"
      "s_mul_i32 s34, s34, 7168\n\t"
      "s_add_u32 s34, s34, 10227712\n\t"
      "s_add_u32 s40, s20, s34\n\t"
      "s_addc_u32 s41, s21, 0\n\t"
      "global_store_dwordx4 v244, v[132:135], s[40:41] offset:-4096 sc1\n\t"
      "global_store_dwordx4 v244, v[132:135], s[40:41] offset:-3072 sc1\n\t"
      "global_store_dwordx4 v244, v[132:135], s[40:41] offset:-2048 sc1\n\t"
      "global_store_dwordx4 v244, v[132:135], s[40:41] offset:-1024 sc1\n\t"
      "global_store_dwordx4 v244, v[132:135], s[40:41] sc1\n\t"
      "global_store_dwordx4 v244, v[132:135], s[40:41] offset:1024 sc1\n\t"
      "global_store_dwordx4 v244, v[132:135], s[40:41] offset:2048 sc1\n\t"
      "s_lshl_b32 s34, s25, 7\n\t"
      "s_add_u32 s34, s34, 24576\n\t"
      "v_mov_b32_e32 v131, s34\n\t"
      "s_waitcnt vmcnt(0)\n\t"
      "s_barrier\n\t"
      "s_cmp_lg_u32 s24, 0\n\t"
      "s_cbranch_scc1 SP1_%=\n\t"
      "s_mov_b64 exec, 1\n\t"
      "global_store_dwordx2 v131, v[128:129], s[60:61] sc1\n\t"
      "s_mov_b64 exec, -1\n\t"
      "SP1_%=:\n\t"
      "s_and_b32 s34, s25, 3\n\t"
      "s_lshl3_add_u32 s34, s34, s24\n\t"
      "s_cmp_lg_u32 s34, 27\n\t"
      "s_cbranch_scc1 SQ1_%=\n\t"
      "v_and_b32_e32 v136, 0x1f0, v244\n\t"
      "v_lshlrev_b32_e32 v131, 6, v136\n\t"
      "v_lshlrev_b32_e32 v136, 3, v136\n\t"
      "v_add_u32_e32 v136, 24576, v136\n\t"
      "s_and_b32 s34, s2, 7\n\t"
      "s_lshl_b32 s34, s34, 7\n\t"
      "s_add_u32 s34, s34, 65536\n\t"
      "v_add_u32_e32 v131, s34, v131\n\t"
      "s_cmp_lg_u32 s24, 3\n\t"
      "s_cbranch_scc1 SK16_%=\n\t"
      "global_load_dwordx2 v[138:139], v136, s[60:61] sc1\n\t"
      "SK16_%=:\n\t"
      "s_cmp_lg_u32 s24, 3\n\t"
      "s_cbranch_scc1 LF71B_%=\n\t"
      "s_waitcnt vmcnt(0)\n\t"
      "v_cmp_ne_u64_e32 vcc, s[22:23], v[138:139]\n\t"
      "s_cbranch_vccnz LF71R_%=\n\t"
      "LF71B_%=:\n\t"
      "global_store_dwordx2 v131, v[128:129], s[60:61] sc1\n\t"
      "SQ1_%=:\n\t"
      "s_barrier\n\t"
      "s_and_b32 s35, s25, 3\n\t"
      "s_lshl2_add_u32 s31, s26, s35\n\t"
      "s_and_b32 s35, s2, 7\n\t"
      "s_mul_i32 s34, s35, 14\n\t"
      "s_lshl4_add_u32 s35, s35, -14\n\t"
      "s_cmp_lt_u32 s31, 14\n\t"
      "s_cselect_b32 s34, s34, s35\n\t"
      "s_cselect_b32 s36, 1, 0\n\t"
      "s_or_b32 s27, s27, s36\n\t"
      "s_add_u32 s34, s34, s31\n\t"
      "s_lshr_b32 s29, s34, 2\n\t"
      "s_and_b32 s32, s34, 3\n\t"
      "s_mul_i32 s30, s32, 7\n\t"
      "v_and_b32_e32 v199, 60, v192\n\t"
      "v_and_b32_e32 v200, 24, v192\n\t"
      "v_lshlrev_b32_e32 v202, 3, v196\n\t"
      "v_lshl_or_b32 v202, v200, 2, v202\n\t"
      "v_lshl_or_b32 v202, v195, 7, v202\n\t"
      "v_lshl_or_b32 v202, v193, 10, v202\n\t"
      "v_add_u32_e32 v224, s39, v202\n\t"
      "v_xor_b32_e32 v225, 32, v224\n\t"
      "v_xor_b32_e32 v226, 64, v224\n\t"
      "v_xor_b32_e32 v227, 96, v224\n\t"
      "s_lshl_b32 s34, s24, 12\n\t"
      "s_add_u32 s34, s34, 98304\n\t"
      "v_add_u32_e32 v245, s34, v244\n\t"
      "v_lshlrev_b32_e32 v199, 2, v199\n\t"
      "s_lshl_b32 s34, s24, 8\n\t"
      "v_lshl_add_u32 v247, v192, 2, s34\n\t"
      "v_and_b32_e32 v200, 2, v192\n\t"
      "v_mul_u32_u24_e32 v202, 576, v200\n\t"
      "v_xor_b32_e32 v199, v202, v199\n\t"
      "v_and_b32_e32 v202, 1, v192\n\t"
      "v_lshl_or_b32 v199, v202, 3, v199\n\t"
      "s_and_b32 s35, s34, 0x400\n\t"
      "s_add_u32 s34, s34, s35\n\t"
      "s_add_u32 s34, s34, 98304\n\t"
      "v_add_u32_e32 v246, s34, v199\n\t"
      "v_lshlrev_b32_e32 v199, 2, v202\n\t"
      "v_lshl_or_b32 v199, v200, 4, v199\n\t"
      "s_min_u32 s34, s29, 27\n\t"
      "s_lshl_b32 s34, s34, 7\n\t"
      "s_lshr_b32 s35, s24, 2\n\t"
      "s_lshl_b32 s35, s35, 6\n\t"
      "s_add_u32 s34, s34, s35\n\t"
      "s_lshr_b32 s35, s39, 9\n\t"
      "s_add_u32 s34, s34, s35\n\t"
      "v_add_u32_e32 v248, s34, v199\n\t"
      "s_add_u32 s48, s20, 8388608\n\t"
      "s_addc_u32 s49, s21, 0\n\t"
      "s_cmp_ge_u32 s31, 30\n\t"
      "s_cbranch_scc1 BYE_%=\n\t"
      "s_setprio 0\n\t"
      "s_waitcnt vmcnt(0)\n\t"
      "s_barrier\n\t"
      "s_branch G4_%=\n\t"
      "BYE_%=:\n\t"
      "s_endpgm\n\t"
      "LA30R_%=:\n\t"
      "s_mov_b32 s80, 0\n\t"
      "LA30L_%=:\n\t"
      "s_add_u32 s80, s80, 1\n\t"
      "s_cmp_lt_u32 s80, 262144\n\t"
      "s_cbranch_scc0 LA30X_%=\n\t"
      "s_waitcnt vmcnt(0)\n\t"
      "s_sleep 1\n\t"
      "global_load_dwordx4 v[128:131], v250, s[64:65] offset:-4096 sc1\n\t"
      "global_load_dwordx4 v[132:135], v250, s[64:65] offset:-3072 sc1\n\t"
      "global_load_dwordx4 v[136:139], v250, s[64:65] offset:-2048 sc1\n\t"
      "global_load_dwordx4 v[140:143], v250, s[64:65] offset:-1024 sc1\n\t"
      "global_load_dwordx4 v[144:147], v250, s[64:65] sc1\n\t"
      "global_load_dwordx4 v[148:151], v250, s[64:65] offset:1024 sc1\n\t"
      "global_load_dwordx4 v[152:155], v250, s[64:65] offset:2048 sc1\n\t"
      "global_load_dwordx4 v[156:159], v250, s[64:65] offset:3072 sc1\n\t"
      "s_waitcnt vmcnt(0)\n\t"
      "v_max3_u32 v251, v128, v129, v130\n\t"
      "v_max3_u32 v251, v251, v131, v132\n\t"
      "v_max3_u32 v251, v251, v133, v134\n\t"
      "v_max3_u32 v251, v251, v135, v136\n\t"
      "v_max3_u32 v251, v251, v137, v138\n\t"
      "v_max3_u32 v251, v251, v139, v140\n\t"
      "v_max3_u32 v251, v251, v141, v142\n\t"
      "v_max3_u32 v251, v251, v143, v144\n\t"
      "v_max3_u32 v251, v251, v145, v146\n\t"
      "v_max3_u32 v251, v251, v147, v148\n\t"
      "v_max3_u32 v251, v251, v149, v150\n\t"
      "v_max3_u32 v251, v251, v151, v152\n\t"
      "v_max3_u32 v251, v251, v153, v154\n\t"
      "v_max3_u32 v251, v251, v155, v156\n\t"
      "v_max3_u32 v251, v251, v157, v158\n\t"
      "v_max_u32_e32 v251, v251, v159\n\t"
      "v_cmp_eq_u32_e32 vcc, -1, v251\n\t"
      "s_cbranch_vccnz LA30L_%=\n\t"
      "LA30X_%=:\n\t"
      "s_branch LA30B_%=\n\t"
      "LF6R_%=:\n\t"
      "s_mov_b32 s80, 0\n\t"
      "LF6L_%=:\n\t"
      "s_add_u32 s80, s80, 1\n\t"
      "s_cmp_lt_u32 s80, 262144\n\t"
      "s_cbranch_scc0 LF6X_%=\n\t"
      "s_sleep 2\n\t"
      "global_load_dwordx2 v[228:229], v230, s[60:61] sc1\n\t"
      "s_waitcnt vmcnt(0)\n\t"
      "v_cmp_ne_u64_e32 vcc, s[22:23], v[228:229]\n\t"
      "s_cbranch_vccnz LF6L_%=\n\t"
      "LF6X_%=:\n\t"
      "s_branch LF6B_%=\n\t"
      "LA31R_%=:\n\t"
      "s_mov_b32 s80, 0\n\t"
      "LA31L_%=:\n\t"
      "s_add_u32 s80, s80, 1\n\t"
      "s_cmp_lt_u32 s80, 262144\n\t"
      "s_cbranch_scc0 LA31X_%=\n\t"
      "s_waitcnt vmcnt(0)\n\t"
      "s_sleep 1\n\t"
      "global_load_dwordx4 v[160:163], v250, s[66:67] offset:-4096 sc1\n\t"
      "global_load_dwordx4 v[164:167], v250, s[66:67] offset:-3072 sc1\n\t"
      "global_load_dwordx4 v[168:171], v250, s[66:67] offset:-2048 sc1\n\t"
      "global_load_dwordx4 v[172:175], v250, s[66:67] offset:-1024 sc1\n\t"
      "global_load_dwordx4 v[176:179], v250, s[66:67] sc1\n\t"
      "global_load_dwordx4 v[180:183], v250, s[66:67] offset:1024 sc1\n\t"
      "global_load_dwordx4 v[184:187], v250, s[66:67] offset:2048 sc1\n\t"
      "global_load_dwordx4 v[188:191], v250, s[66:67] offset:3072 sc1\n\t"
      "s_waitcnt vmcnt(0)\n\t"
      "v_max3_u32 v251, v160, v161, v162\n\t"
      "v_max3_u32 v251, v251, v163, v164\n\t"
      "v_max3_u32 v251, v251, v165, v166\n\t"
      "v_max3_u32 v251, v251, v167, v168\n\t"
      "v_max3_u32 v251, v251, v169, v170\n\t"
      "v_max3_u32 v251, v251, v171, v172\n\t"
      "v_max3_u32 v251, v251, v173, v174\n\t"
      "v_max3_u32 v251, v251, v175, v176\n\t"
      "v_max3_u32 v251, v251, v177, v178\n\t"
      "v_max3_u32 v251, v251, v179, v180\n\t"
      "v_max3_u32 v251, v251, v181, v182\n\t"
      "v_max3_u32 v251, v251, v183, v184\n\t"
      "v_max3_u32 v251, v251, v185, v186\n\t"
      "v_max3_u32 v251, v251, v187, v188\n\t"
      "v_max3_u32 v251, v251, v189, v190\n\t"
      "v_max_u32_e32 v251, v251, v191\n\t"
      "v_cmp_eq_u32_e32 vcc, -1, v251\n\t"
      "s_cbranch_vccnz LA31L_%=\n\t"
      "LA31X_%=:\n\t"
      "v_mfma_f32_16x16x32_bf16 v[224:227], v[0:3], v[160:163], 0\n\t"
      "v_mfma_f32_16x16x32_bf16 v[224:227], v[16:19], v[164:167], v[224:227]\n\t"
      "v_mfma_f32_16x16x32_bf16 v[224:227], v[32:35], v[168:171], v[224:227]\n\t"
      "v_mfma_f32_16x16x32_bf16 v[224:227], v[48:51], v[172:175], v[224:227]\n\t"
      "v_mfma_f32_16x16x32_bf16 v[224:227], v[64:67], v[176:179], v[224:227]\n\t"
      "v_mfma_f32_16x16x32_bf16 v[224:227], v[80:83], v[180:183], v[224:227]\n\t"
      "v_mfma_f32_16x16x32_bf16 v[224:227], v[96:99], v[184:187], v[224:227]\n\t"
      "v_mfma_f32_16x16x32_bf16 v[224:227], v[112:115], v[188:191], v[224:227]\n\t"
      "v_mfma_f32_16x16x32_bf16 v[208:211], v[4:7], v[160:163], 0\n\t"
      "v_mfma_f32_16x16x32_bf16 v[208:211], v[20:23], v[164:167], v[208:211]\n\t"
      "v_mfma_f32_16x16x32_bf16 v[208:211], v[36:39], v[168:171], v[208:211]\n\t"
      "s_nop 7\n\t"
      "s_branch LA31B_%=\n\t"
      "LA32R_%=:\n\t"
      "s_mov_b32 s80, 0\n\t"
      "LA32L_%=:\n\t"
      "s_add_u32 s80, s80, 1\n\t"
      "s_cmp_lt_u32 s80, 262144\n\t"
      "s_cbranch_scc0 LA32X_%=\n\t"
      "s_waitcnt vmcnt(0)\n\t"
      "s_sleep 1\n\t"
      "global_load_dwordx4 v[192:195], v250, s[68:69] offset:-4096 sc1\n\t"
      "global_load_dwordx4 v[196:199], v250, s[68:69] offset:-3072 sc1\n\t"
      "global_load_dwordx4 v[200:203], v250, s[68:69] offset:-2048 sc1\n\t"
      "global_load_dwordx4 v[204:207], v250, s[68:69] offset:-1024 sc1\n\t"
      "global_load_dwordx4 v[128:131], v250, s[68:69] sc1\n\t"
      "global_load_dwordx4 v[132:135], v250, s[68:69] offset:1024 sc1\n\t"
      "global_load_dwordx4 v[136:139], v250, s[68:69] offset:2048 sc1\n\t"
      "global_load_dwordx4 v[140:143], v250, s[68:69] offset:3072 sc1\n\t"
      "s_waitcnt vmcnt(0)\n\t"
      "v_max3_u32 v251, v192, v193, v194\n\t"
      "v_max3_u32 v251, v251, v195, v196\n\t"
      "v_max3_u32 v251, v251, v197, v198\n\t"
      "v_max3_u32 v251, v251, v199, v200\n\t"
      "v_max3_u32 v251, v251, v201, v202\n\t"
      "v_max3_u32 v251, v251, v203, v204\n\t"
      "v_max3_u32 v251, v251, v205, v206\n\t"
      "v_max3_u32 v251, v251, v207, v128\n\t"
      "v_max3_u32 v251, v251, v129, v130\n\t"
      "v_max3_u32 v251, v251, v131, v132\n\t"
      "v_max3_u32 v251, v251, v133, v134\n\t"
      "v_max3_u32 v251, v251, v135, v136\n\t"
      "v_max3_u32 v251, v251, v137, v138\n\t"
      "v_max3_u32 v251, v251, v139, v140\n\t"
      "v_max3_u32 v251, v251, v141, v142\n\t"
      "v_max_u32_e32 v251, v251, v143\n\t"
      "v_cmp_eq_u32_e32 vcc, -1, v251\n\t"
      "s_cbranch_vccnz LA32L_%=\n\t"
      "LA32X_%=:\n\t"
      "v_mfma_f32_16x16x32_bf16 v[220:223], v[0:3], v[192:195], 0\n\t"
      "v_mfma_f32_16x16x32_bf16 v[220:223], v[16:19], v[196:199], v[220:223]\n\t"
      "v_mfma_f32_16x16x32_bf16 v[220:223], v[32:35], v[200:203], v[220:223]\n\t"
      "v_mfma_f32_16x16x32_bf16 v[220:223], v[48:51], v[204:207], v[220:223]\n\t"
      "v_mfma_f32_16x16x32_bf16 v[220:223], v[64:67], v[128:131], v[220:223]\n\t"
      "v_mfma_f32_16x16x32_bf16 v[220:223], v[80:83], v[132:135], v[220:223]\n\t"
      "v_mfma_f32_16x16x32_bf16 v[220:223], v[96:99], v[136:139], v[220:223]\n\t"
      "v_mfma_f32_16x16x32_bf16 v[220:223], v[112:115], v[140:143], v[220:223]\n\t"
      "v_mfma_f32_16x16x32_bf16 v[224:227], v[4:7], v[192:195], 0\n\t"
      "v_mfma_f32_16x16x32_bf16 v[224:227], v[20:23], v[196:199], v[224:227]\n\t"
      "v_mfma_f32_16x16x32_bf16 v[224:227], v[36:39], v[200:203], v[224:227]\n\t"
      "s_nop 7\n\t"
      "s_branch LA32B_%=\n\t"
      "LA33R_%=:\n\t"
      "s_mov_b32 s80, 0\n\t"
      "LA33L_%=:\n\t"
      "s_add_u32 s80, s80, 1\n\t"
      "s_cmp_lt_u32 s80, 262144\n\t"
      "s_cbranch_scc0 LA33X_%=\n\t"
      "s_waitcnt vmcnt(0)\n\t"
      "s_sleep 1\n\t"
      "global_load_dwordx4 v[144:147], v250, s[70:71] offset:-4096 sc1\n\t"
      "global_load_dwordx4 v[148:151], v250, s[70:71] offset:-3072 sc1\n\t"
      "global_load_dwordx4 v[152:155], v250, s[70:71] offset:-2048 sc1\n\t"
      "global_load_dwordx4 v[156:159], v250, s[70:71] offset:-1024 sc1\n\t"
      "global_load_dwordx4 v[160:163], v250, s[70:71] sc1\n\t"
      "global_load_dwordx4 v[164:167], v250, s[70:71] offset:1024 sc1\n\t"
      "global_load_dwordx4 v[168:171], v250, s[70:71] offset:2048 sc1\n\t"
      "global_load_dwordx4 v[172:175], v250, s[70:71] offset:3072 sc1\n\t"
      "s_waitcnt vmcnt(0)\n\t"
      "v_max3_u32 v251, v144, v145, v146\n\t"
      "v_max3_u32 v251, v251, v147, v148\n\t"
      "v_max3_u32 v251, v251, v149, v150\n\t"
      "v_max3_u32 v251, v251, v151, v152\n\t"
      "v_max3_u32 v251, v251, v153, v154\n\t"
      "v_max3_u32 v251, v251, v155, v156\n\t"
      "v_max3_u32 v251, v251, v157, v158\n\t"
      "v_max3_u32 v251, v251, v159, v160\n\t"
      "v_max3_u32 v251, v251, v161, v162\n\t"
      "v_max3_u32 v251, v251, v163, v164\n\t"
      "v_max3_u32 v251, v251, v165, v166\n\t"
      "v_max3_u32 v251, v251, v167, v168\n\t"
      "v_max3_u32 v251, v251, v169, v170\n\t"
      "v_max3_u32 v251, v251, v171, v172\n\t"
      "v_max3_u32 v251, v251, v173, v174\n\t"
      "v_max_u32_e32 v251, v251, v175\n\t"
      "v_cmp_eq_u32_e32 vcc, -1, v251\n\t"
      "s_cbranch_vccnz LA33L_%=\n\t"
      "LA33X_%=:\n\t"
      "v_mfma_f32_16x16x32_bf16 v[216:219], v[0:3], v[144:147], 0\n\t"
      "v_mfma_f32_16x16x32_bf16 v[216:219], v[16:19], v[148:151], v[216:219]\n\t"
      "v_mfma_f32_16x16x32_bf16 v[216:219], v[32:35], v[152:155], v[216:219]\n\t"
      "v_mfma_f32_16x16x32_bf16 v[216:219], v[48:51], v[156:159], v[216:219]\n\t"
      "v_mfma_f32_16x16x32_bf16 v[216:219], v[64:67], v[160:163], v[216:219]\n\t"
      "v_mfma_f32_16x16x32_bf16 v[216:219], v[80:83], v[164:167], v[216:219]\n\t"
      "v_mfma_f32_16x16x32_bf16 v[216:219], v[96:99], v[168:171], v[216:219]\n\t"
      "v_mfma_f32_16x16x32_bf16 v[216:219], v[112:115], v[172:175], v[216:219]\n\t"
      "v_mfma_f32_16x16x32_bf16 v[220:223], v[4:7], v[144:147], 0\n\t"
      "v_mfma_f32_16x16x32_bf16 v[220:223], v[20:23], v[148:151], v[220:223]\n\t"
      "v_mfma_f32_16x16x32_bf16 v[220:223], v[36:39], v[152:155], v[220:223]\n\t"
      "s_nop 7\n\t"
      "s_branch LA33B_%=\n\t"
      "LA34R_%=:\n\t"
      "s_mov_b32 s80, 0\n\t"
      "LA34L_%=:\n\t"
      "s_add_u32 s80, s80, 1\n\t"
      "s_cmp_lt_u32 s80, 262144\n\t"
      "s_cbranch_scc0 LA34X_%=\n\t"
      "s_waitcnt vmcnt(0)\n\t"
      "s_sleep 1\n\t"
      "global_load_dwordx4 v[176:179], v250, s[72:73] offset:-4096 sc1\n\t"
      "global_load_dwordx4 v[180:183], v250, s[72:73] offset:-3072 sc1\n\t"
      "global_load_dwordx4 v[184:187], v250, s[72:73] offset:-2048 sc1\n\t"
      "global_load_dwordx4 v[188:191], v250, s[72:73] offset:-1024 sc1\n\t"
      "global_load_dwordx4 v[192:195], v250, s[72:73] sc1\n\t"
      "global_load_dwordx4 v[196:199], v250, s[72:73] offset:1024 sc1\n\t"
      "global_load_dwordx4 v[200:203], v250, s[72:73] offset:2048 sc1\n\t"
      "global_load_dwordx4 v[204:207], v250, s[72:73] offset:3072 sc1\n\t"
      "s_waitcnt vmcnt(0)\n\t"
      "v_max3_u32 v251, v176, v177, v178\n\t"
      "v_max3_u32 v251, v251, v179, v180\n\t"
      "v_max3_u32 v251, v251, v181, v182\n\t"
      "v_max3_u32 v251, v251, v183, v184\n\t"
      "v_max3_u32 v251, v251, v185, v186\n\t"
      "v_max3_u32 v251, v251, v187, v188\n\t"
      "v_max3_u32 v251, v251, v189, v190\n\t"
      "v_max3_u32 v251, v251, v191, v192\n\t"
      "v_max3_u32 v251, v251, v193, v194\n\t"
      "v_max3_u32 v251, v251, v195, v196\n\t"
      "v_max3_u32 v251, v251, v197, v198\n\t"
      "v_max3_u32 v251, v251, v199, v200\n\t"
      "v_max3_u32 v251, v251, v201, v202\n\t"
      "v_max3_u32 v251, v251, v203, v204\n\t"
      "v_max3_u32 v251, v251, v205, v206\n\t"
      "v_max_u32_e32 v251, v251, v207\n\t"
      "v_cmp_eq_u32_e32 vcc, -1, v251\n\t"
      "s_cbranch_vccnz LA34L_%=\n\t"
      "LA34X_%=:\n\t"
      "v_mfma_f32_16x16x32_bf16 v[212:215], v[0:3], v[176:179], 0\n\t"
      "v_mfma_f32_16x16x32_bf16 v[212:215], v[16:19], v[180:183], v[212:215]\n\t"
      "v_mfma_f32_16x16x32_bf16 v[212:215], v[32:35], v[184:187], v[212:215]\n\t"
      "v_mfma_f32_16x16x32_bf16 v[212:215], v[48:51], v[188:191], v[212:215]\n\t"
      "v_mfma_f32_16x16x32_bf16 v[212:215], v[64:67], v[192:195], v[212:215]\n\t"
      "v_mfma_f32_16x16x32_bf16 v[212:215], v[80:83], v[196:199], v[212:215]\n\t"
      "v_mfma_f32_16x16x32_bf16 v[212:215], v[96:99], v[200:203], v[212:215]\n\t"
      "v_mfma_f32_16x16x32_bf16 v[212:215], v[112:115], v[204:207], v[212:215]\n\t"
      "v_mfma_f32_16x16x32_bf16 v[216:219], v[4:7], v[176:179], 0\n\t"
      "v_mfma_f32_16x16x32_bf16 v[216:219], v[20:23], v[180:183], v[216:219]\n\t"
      "v_mfma_f32_16x16x32_bf16 v[216:219], v[36:39], v[184:187], v[216:219]\n\t"
      "s_nop 7\n\t"
      "s_branch LA34B_%=\n\t"
      "LA35R_%=:\n\t"
      "s_mov_b32 s80, 0\n\t"
      "LA35L_%=:\n\t"
      "s_add_u32 s80, s80, 1\n\t"
      "s_cmp_lt_u32 s80, 262144\n\t"
      "s_cbranch_scc0 LA35X_%=\n\t"
      "s_waitcnt vmcnt(0)\n\t"
      "s_sleep 1\n\t"
      "global_load_dwordx4 v[128:131], v250, s[74:75] offset:-4096 sc1\n\t"
      "global_load_dwordx4 v[132:135], v250, s[74:75] offset:-3072 sc1\n\t"
      "global_load_dwordx4 v[136:139], v250, s[74:75] offset:-2048 sc1\n\t"
      "global_load_dwordx4 v[140:143], v250, s[74:75] offset:-1024 sc1\n\t"
      "global_load_dwordx4 v[144:147], v250, s[74:75] sc1\n\t"
      "global_load_dwordx4 v[148:151], v250, s[74:75] offset:1024 sc1\n\t"
      "global_load_dwordx4 v[152:155], v250, s[74:75] offset:2048 sc1\n\t"
      "global_load_dwordx4 v[156:159], v250, s[74:75] offset:3072 sc1\n\t"
      "s_waitcnt vmcnt(0)\n\t"
      "v_max3_u32 v251, v128, v129, v130\n\t"
      "v_max3_u32 v251, v251, v131, v132\n\t"
      "v_max3_u32 v251, v251, v133, v134\n\t"
      "v_max3_u32 v251, v251, v135, v136\n\t"
      "v_max3_u32 v251, v251, v137, v138\n\t"
      "v_max3_u32 v251, v251, v139, v140\n\t"
      "v_max3_u32 v251, v251, v141, v142\n\t"
      "v_max3_u32 v251, v251, v143, v144\n\t"
      "v_max3_u32 v251, v251, v145, v146\n\t"
      "v_max3_u32 v251, v251, v147, v148\n\t"
      "v_max3_u32 v251, v251, v149, v150\n\t"
      "v_max3_u32 v251, v251, v151, v152\n\t"
      "v_max3_u32 v251, v251, v153, v154\n\t"
      "v_max3_u32 v251, v251, v155, v156\n\t"
      "v_max3_u32 v251, v251, v157, v158\n\t"
      "v_max_u32_e32 v251, v251, v159\n\t"
      "v_cmp_eq_u32_e32 vcc, -1, v251\n\t"
      "s_cbranch_vccnz LA35L_%=\n\t"
      "LA35X_%=:\n\t"
      "v_mfma_f32_16x16x32_bf16 v[208:211], v[0:3], v[128:131], 0\n\t"
      "v_mfma_f32_16x16x32_bf16 v[208:211], v[16:19], v[132:135], v[208:211]\n\t"
      "v_mfma_f32_16x16x32_bf16 v[208:211], v[32:35], v[136:139], v[208:211]\n\t"
      "v_mfma_f32_16x16x32_bf16 v[208:211], v[48:51], v[140:143], v[208:211]\n\t"
      "v_mfma_f32_16x16x32_bf16 v[208:211], v[64:67], v[144:147], v[208:211]\n\t"
      "v_mfma_f32_16x16x32_bf16 v[208:211], v[80:83], v[148:151], v[208:211]\n\t"
      "v_mfma_f32_16x16x32_bf16 v[208:211], v[96:99], v[152:155], v[208:211]\n\t"
      "v_mfma_f32_16x16x32_bf16 v[208:211], v[112:115], v[156:159], v[208:211]\n\t"
      "v_mfma_f32_16x16x32_bf16 v[212:215], v[4:7], v[128:131], 0\n\t"
      "v_mfma_f32_16x16x32_bf16 v[212:215], v[20:23], v[132:135], v[212:215]\n\t"
      "v_mfma_f32_16x16x32_bf16 v[212:215], v[36:39], v[136:139], v[212:215]\n\t"
      "s_nop 7\n\t"
      "s_branch LA35B_%=\n\t"
      "LA36R_%=:\n\t"
      "s_mov_b32 s80, 0\n\t"
      "LA36L_%=:\n\t"
      "s_add_u32 s80, s80, 1\n\t"
      "s_cmp_lt_u32 s80, 262144\n\t"
      "s_cbranch_scc0 LA36X_%=\n\t"
      "s_waitcnt vmcnt(0)\n\t"
      "s_sleep 1\n\t"
      "global_load_dwordx4 v[160:163], v250, s[76:77] offset:-4096 sc1\n\t"
      "global_load_dwordx4 v[164:167], v250, s[76:77] offset:-3072 sc1\n\t"
      "global_load_dwordx4 v[168:171], v250, s[76:77] offset:-2048 sc1\n\t"
      "global_load_dwordx4 v[172:175], v250, s[76:77] offset:-1024 sc1\n\t"
      "global_load_dwordx4 v[176:179], v250, s[76:77] sc1\n\t"
      "global_load_dwordx4 v[180:183], v250, s[76:77] offset:1024 sc1\n\t"
      "global_load_dwordx4 v[184:187], v250, s[76:77] offset:2048 sc1\n\t"
      "global_load_dwordx4 v[188:191], v250, s[76:77] offset:3072 sc1\n\t"
      "s_waitcnt vmcnt(0)\n\t"
      "v_max3_u32 v251, v160, v161, v162\n\t"
      "v_max3_u32 v251, v251, v163, v164\n\t"
      "v_max3_u32 v251, v251, v165, v166\n\t"
      "v_max3_u32 v251, v251, v167, v168\n\t"
      "v_max3_u32 v251, v251, v169, v170\n\t"
      "v_max3_u32 v251, v251, v171, v172\n\t"
      "v_max3_u32 v251, v251, v173, v174\n\t"
      "v_max3_u32 v251, v251, v175, v176\n\t"
      "v_max3_u32 v251, v251, v177, v178\n\t"
      "v_max3_u32 v251, v251, v179, v180\n\t"
      "v_max3_u32 v251, v251, v181, v182\n\t"
      "v_max3_u32 v251, v251, v183, v184\n\t"
      "v_max3_u32 v251, v251, v185, v186\n\t"
      "v_max3_u32 v251, v251, v187, v188\n\t"
      "v_max3_u32 v251, v251, v189, v190\n\t"
      "v_max_u32_e32 v251, v251, v191\n\t"
      "v_cmp_eq_u32_e32 vcc, -1, v251\n\t"
      "s_cbranch_vccnz LA36L_%=\n\t"
      "LA36X_%=:\n\t"
      "v_mfma_f32_16x16x32_bf16 v[224:227], v[0:3], v[160:163], 0\n\t"
      "v_mfma_f32_16x16x32_bf16 v[224:227], v[16:19], v[164:167], v[224:227]\n\t"
      "v_mfma_f32_16x16x32_bf16 v[224:227], v[32:35], v[168:171], v[224:227]\n\t"
      "v_mfma_f32_16x16x32_bf16 v[224:227], v[48:51], v[172:175], v[224:227]\n\t"
      "v_mfma_f32_16x16x32_bf16 v[224:227], v[64:67], v[176:179], v[224:227]\n\t"
      "v_mfma_f32_16x16x32_bf16 v[224:227], v[80:83], v[180:183], v[224:227]\n\t"
      "v_mfma_f32_16x16x32_bf16 v[224:227], v[96:99], v[184:187], v[224:227]\n\t"
      "v_mfma_f32_16x16x32_bf16 v[224:227], v[112:115], v[188:191], v[224:227]\n\t"
      "v_mfma_f32_16x16x32_bf16 v[208:211], v[4:7], v[160:163], 0\n\t"
      "v_mfma_f32_16x16x32_bf16 v[208:211], v[20:23], v[164:167], v[208:211]\n\t"
      "v_mfma_f32_16x16x32_bf16 v[208:211], v[36:39], v[168:171], v[208:211]\n\t"
      "s_nop 7\n\t"
      "s_branch LA36B_%=\n\t"
      "LF5R_%=:\n\t"
      "s_mov_b32 s80, 0\n\t"
      "LF5L_%=:\n\t"
      "s_add_u32 s80, s80, 1\n\t"
      "s_cmp_lt_u32 s80, 262144\n\t"
      "s_cbranch_scc0 LF5X_%=\n\t"
      "s_sleep 2\n\t"
      "global_load_dwordx2 v[138:139], v143, s[60:61] sc1\n\t"
      "s_waitcnt vmcnt(0)\n\t"
      "v_cmp_ne_u64_e32 vcc, s[22:23], v[138:139]\n\t"
      "s_cbranch_vccnz LF5L_%=\n\t"
      "LF5X_%=:\n\t"
      "s_branch LF5B_%=\n\t"
      "PGR_%=:\n\t"
      "s_mov_b32 s80, 0\n\t"
      "PGL_%=:\n\t"
      "s_add_u32 s80, s80, 1\n\t"
      "s_cmp_lt_u32 s80, 262144\n\t"
      "s_cbranch_scc0 PGX_%=\n\t"
      "s_sleep 2\n\t"
      "global_load_dword v251, v253, s[52:53] offset:252 sc1\n\t"
      "s_waitcnt vmcnt(0)\n\t"
      "v_cmp_eq_u32_e32 vcc, -1, v251\n\t"
      "s_cbranch_vccnz PGL_%=\n\t"
      "PGX_%=:\n\t"
      "s_sleep 5\n\t"
      "s_branch PGB_%=\n\t"
      "LF8R_%=:\n\t"
      "s_mov_b32 s80, 0\n\t"
      "LF8L_%=:\n\t"
      "s_add_u32 s80, s80, 1\n\t"
      "s_cmp_lt_u32 s80, 262144\n\t"
      "s_cbranch_scc0 LF8X_%=\n\t"
      "s_sleep 2\n\t"
      "global_load_dwordx2 v[240:241], v242, s[60:61] sc1\n\t"
      "s_waitcnt vmcnt(0)\n\t"
      "v_cmp_ne_u64_e32 vcc, s[22:23], v[240:241]\n\t"
      "s_cbranch_vccnz LF8L_%=\n\t"
      "LF8X_%=:\n\t"
      "s_branch LF8B_%=\n\t"
      "LA40R_%=:\n\t"
      "s_mov_b32 s80, 0\n\t"
      "LA40L_%=:\n\t"
      "s_add_u32 s80, s80, 1\n\t"
      "s_cmp_lt_u32 s80, 262144\n\t"
      "s_cbranch_scc0 LA40X_%=\n\t"
      "s_waitcnt vmcnt(0)\n\t"
      "PQ0R_%=:\n\t"
      "PQ0L_%=:\n\t"
      "s_add_u32 s80, s80, 1\n\t"
      "s_cmp_lt_u32 s80, 262144\n\t"
      "s_cbranch_scc0 PQ0X_%=\n\t"
      "s_sleep 1\n\t"
      "global_load_dword v251, v252, s[64:65] offset:252 sc1\n\t"
      "s_waitcnt vmcnt(0)\n\t"
      "v_cmp_eq_u32_e32 vcc, -1, v251\n\t"
      "s_cbranch_vccnz PQ0L_%=\n\t"
      "PQ0X_%=:\n\t"
      "s_add_u32 s58, s64, s40\n\t"
      "s_addc_u32 s59, s65, 0\n\t"
      "global_load_dwordx4 v[128:131], v244, s[58:59] sc1\n\t"
      "s_add_u32 s58, s64, s41\n\t"
      "s_addc_u32 s59, s65, 0\n\t"
      "global_load_dwordx4 v[132:135], v244, s[58:59] sc1\n\t"
      "s_add_u32 s58, s64, s42\n\t"
      "s_addc_u32 s59, s65, 0\n\t"
      "global_load_dwordx4 v[136:139], v244, s[58:59] sc1\n\t"
      "s_add_u32 s58, s64, s43\n\t"
      "s_addc_u32 s59, s65, 0\n\t"
      "global_load_dwordx4 v[140:143], v244, s[58:59] sc1\n\t"
      "s_add_u32 s58, s64, s44\n\t"
      "s_addc_u32 s59, s65, 0\n\t"
      "global_load_dwordx4 v[144:147], v244, s[58:59] sc1\n\t"
      "s_add_u32 s58, s64, s45\n\t"
      "s_addc_u32 s59, s65, 0\n\t"
      "global_load_dwordx4 v[148:151], v244, s[58:59] sc1\n\t"
      "s_add_u32 s58, s64, s46\n\t"
      "s_addc_u32 s59, s65, 0\n\t"
      "global_load_dwordx4 v[152:155], v244, s[58:59] sc1\n\t"
      "s_waitcnt vmcnt(0)\n\t"
      "v_max3_u32 v251, v128, v129, v130\n\t"
      "v_max3_u32 v251, v251, v131, v132\n\t"
      "v_max3_u32 v251, v251, v133, v134\n\t"
      "v_max3_u32 v251, v251, v135, v136\n\t"
      "v_max3_u32 v251, v251, v137, v138\n\t"
      "v_max3_u32 v251, v251, v139, v140\n\t"
      "v_max3_u32 v251, v251, v141, v142\n\t"
      "v_max3_u32 v251, v251, v143, v144\n\t"
      "v_max3_u32 v251, v251, v145, v146\n\t"
      "v_max3_u32 v251, v251, v147, v148\n\t"
      "v_max3_u32 v251, v251, v149, v150\n\t"
      "v_max3_u32 v251, v251, v151, v152\n\t"
      "v_max3_u32 v251, v251, v153, v154\n\t"
      "v_max_u32_e32 v251, v251, v155\n\t"
      "v_cmp_eq_u32_e32 vcc, -1, v251\n\t"
      "s_cbranch_vccnz LA40L_%=\n\t"
      "LA40X_%=:\n\t"
      "v_mfma_f32_16x16x32_bf16 v[196:199], v[128:131], v[0:3], 0\n\t"
      "v_mfma_f32_16x16x32_bf16 v[200:203], v[128:131], v[4:7], 0\n\t"
      "v_mfma_f32_16x16x32_bf16 v[204:207], v[128:131], v[8:11], 0\n\t"
      "v_mfma_f32_16x16x32_bf16 v[208:211], v[128:131], v[12:15], 0\n\t"
      "v_mfma_f32_16x16x32_bf16 v[196:199], v[132:135], v[16:19], v[196:199]\n\t"
      "v_mfma_f32_16x16x32_bf16 v[200:203], v[132:135], v[20:23], v[200:203]\n\t"
      "v_mfma_f32_16x16x32_bf16 v[204:207], v[132:135], v[24:27], v[204:207]\n\t"
      "v_mfma_f32_16x16x32_bf16 v[208:211], v[132:135], v[28:31], v[208:211]\n\t"
      "v_mfma_f32_16x16x32_bf16 v[196:199], v[136:139], v[32:35], v[196:199]\n\t"
      "v_mfma_f32_16x16x32_bf16 v[200:203], v[136:139], v[36:39], v[200:203]\n\t"
      "v_mfma_f32_16x16x32_bf16 v[204:207], v[136:139], v[40:43], v[204:207]\n\t"
      "v_mfma_f32_16x16x32_bf16 v[208:211], v[136:139], v[44:47], v[208:211]\n\t"
      "v_mfma_f32_16x16x32_bf16 v[196:199], v[140:143], v[48:51], v[196:199]\n\t"
      "v_mfma_f32_16x16x32_bf16 v[200:203], v[140:143], v[52:55], v[200:203]\n\t"
      "v_mfma_f32_16x16x32_bf16 v[204:207], v[140:143], v[56:59], v[204:207]\n\t"
      "v_mfma_f32_16x16x32_bf16 v[208:211], v[140:143], v[60:63], v[208:211]\n\t"
      "v_mfma_f32_16x16x32_bf16 v[196:199], v[144:147], v[64:67], v[196:199]\n\t"
      "v_mfma_f32_16x16x32_bf16 v[200:203], v[144:147], v[68:71], v[200:203]\n\t"
      "v_mfma_f32_16x16x32_bf16 v[204:207], v[144:147], v[72:75], v[204:207]\n\t"
      "v_mfma_f32_16x16x32_bf16 v[208:211], v[144:147], v[76:79], v[208:211]\n\t"
      "v_mfma_f32_16x16x32_bf16 v[196:199], v[148:151], v[80:83], v[196:199]\n\t"
      "v_mfma_f32_16x16x32_bf16 v[196:199], v[152:155], v[96:99], v[196:199]\n\t"
      "v_mfma_f32_16x16x32_bf16 v[200:203], v[148:151], v[84:87], v[200:203]\n\t"
      "v_mfma_f32_16x16x32_bf16 v[200:203], v[152:155], v[100:103], v[200:203]\n\t"
      "v_mfma_f32_16x16x32_bf16 v[204:207], v[148:151], v[88:91], v[204:207]\n\t"
      "s_nop 7\n\t"
      "s_branch LA40B_%=\n\t"
      "LA41R_%=:\n\t"
      "s_mov_b32 s80, 0\n\t"
      "LA41L_%=:\n\t"
      "s_add_u32 s80, s80, 1\n\t"
      "s_cmp_lt_u32 s80, 262144\n\t"
      "s_cbranch_scc0 LA41X_%=\n\t"
      "s_waitcnt vmcnt(0)\n\t"
      "PQ1R_%=:\n\t"
      "PQ1L_%=:\n\t"
      "s_add_u32 s80, s80, 1\n\t"
      "s_cmp_lt_u32 s80, 262144\n\t"
      "s_cbranch_scc0 PQ1X_%=\n\t"
      "s_sleep 1\n\t"
      "global_load_dword v251, v252, s[66:67] offset:252 sc1\n\t"
      "s_waitcnt vmcnt(0)\n\t"
      "v_cmp_eq_u32_e32 vcc, -1, v251\n\t"
      "s_cbranch_vccnz PQ1L_%=\n\t"
      "PQ1X_%=:\n\t"
      "s_add_u32 s58, s66, s40\n\t"
      "s_addc_u32 s59, s67, 0\n\t"
      "global_load_dwordx4 v[156:159], v244, s[58:59] sc1\n\t"
      "s_add_u32 s58, s66, s41\n\t"
      "s_addc_u32 s59, s67, 0\n\t"
      "global_load_dwordx4 v[160:163], v244, s[58:59] sc1\n\t"
      "s_add_u32 s58, s66, s42\n\t"
      "s_addc_u32 s59, s67, 0\n\t"
      "global_load_dwordx4 v[164:167], v244, s[58:59] sc1\n\t"
      "s_add_u32 s58, s66, s43\n\t"
      "s_addc_u32 s59, s67, 0\n\t"
      "global_load_dwordx4 v[168:171], v244, s[58:59] sc1\n\t"
      "s_add_u32 s58, s66, s44\n\t"
      "s_addc_u32 s59, s67, 0\n\t"
      "global_load_dwordx4 v[172:175], v244, s[58:59] sc1\n\t"
      "s_add_u32 s58, s66, s45\n\t"
      "s_addc_u32 s59, s67, 0\n\t"
      "global_load_dwordx4 v[176:179], v244, s[58:59] sc1\n\t"
      "s_add_u32 s58, s66, s46\n\t"
      "s_addc_u32 s59, s67, 0\n\t"
      "global_load_dwordx4 v[180:183], v244, s[58:59] sc1\n\t"
      "s_waitcnt vmcnt(0)\n\t"
      "v_max3_u32 v251, v156, v157, v158\n\t"
      "v_max3_u32 v251, v251, v159, v160\n\t"
      "v_max3_u32 v251, v251, v161, v162\n\t"
      "v_max3_u32 v251, v251, v163, v164\n\t"
      "v_max3_u32 v251, v251, v165, v166\n\t"
      "v_max3_u32 v251, v251, v167, v168\n\t"
      "v_max3_u32 v251, v251, v169, v170\n\t"
      "v_max3_u32 v251, v251, v171, v172\n\t"
      "v_max3_u32 v251, v251, v173, v174\n\t"
      "v_max3_u32 v251, v251, v175, v176\n\t"
      "v_max3_u32 v251, v251, v177, v178\n\t"
      "v_max3_u32 v251, v251, v179, v180\n\t"
      "v_max3_u32 v251, v251, v181, v182\n\t"
      "v_max_u32_e32 v251, v251, v183\n\t"
      "v_cmp_eq_u32_e32 vcc, -1, v251\n\t"
      "s_cbranch_vccnz LA41L_%=\n\t"
      "LA41X_%=:\n\t"
      "v_mfma_f32_16x16x32_bf16 v[212:215], v[156:159], v[0:3], 0\n\t"
      "v_mfma_f32_16x16x32_bf16 v[216:219], v[156:159], v[4:7], 0\n\t"
      "v_mfma_f32_16x16x32_bf16 v[220:223], v[156:159], v[8:11], 0\n\t"
      "v_mfma_f32_16x16x32_bf16 v[224:227], v[156:159], v[12:15], 0\n\t"
      "v_mfma_f32_16x16x32_bf16 v[212:215], v[160:163], v[16:19], v[212:215]\n\t"
      "v_mfma_f32_16x16x32_bf16 v[216:219], v[160:163], v[20:23], v[216:219]\n\t"
      "v_mfma_f32_16x16x32_bf16 v[220:223], v[160:163], v[24:27], v[220:223]\n\t"
      "v_mfma_f32_16x16x32_bf16 v[224:227], v[160:163], v[28:31], v[224:227]\n\t"
      "v_mfma_f32_16x16x32_bf16 v[212:215], v[164:167], v[32:35], v[212:215]\n\t"
      "v_mfma_f32_16x16x32_bf16 v[216:219], v[164:167], v[36:39], v[216:219]\n\t"
      "v_mfma_f32_16x16x32_bf16 v[220:223], v[164:167], v[40:43], v[220:223]\n\t"
      "v_mfma_f32_16x16x32_bf16 v[224:227], v[164:167], v[44:47], v[224:227]\n\t"
      "v_mfma_f32_16x16x32_bf16 v[212:215], v[168:171], v[48:51], v[212:215]\n\t"
      "v_mfma_f32_16x16x32_bf16 v[216:219], v[168:171], v[52:55], v[216:219]\n\t"
      "v_mfma_f32_16x16x32_bf16 v[220:223], v[168:171], v[56:59], v[220:223]\n\t"
      "v_mfma_f32_16x16x32_bf16 v[224:227], v[168:171], v[60:63], v[224:227]\n\t"
      "v_mfma_f32_16x16x32_bf16 v[212:215], v[172:175], v[64:67], v[212:215]\n\t"
      "v_mfma_f32_16x16x32_bf16 v[216:219], v[172:175], v[68:71], v[216:219]\n\t"
      "v_mfma_f32_16x16x32_bf16 v[220:223], v[172:175], v[72:75], v[220:223]\n\t"
      "v_mfma_f32_16x16x32_bf16 v[224:227], v[172:175], v[76:79], v[224:227]\n\t"
      "v_mfma_f32_16x16x32_bf16 v[212:215], v[176:179], v[80:83], v[212:215]\n\t"
      "v_mfma_f32_16x16x32_bf16 v[212:215], v[180:183], v[96:99], v[212:215]\n\t"
      "v_mfma_f32_16x16x32_bf16 v[216:219], v[176:179], v[84:87], v[216:219]\n\t"
      "v_mfma_f32_16x16x32_bf16 v[216:219], v[180:183], v[100:103], v[216:219]\n\t"
      "v_mfma_f32_16x16x32_bf16 v[220:223], v[176:179], v[88:91], v[220:223]\n\t"
      "s_nop 7\n\t"
      "s_branch LA41B_%=\n\t"
      "LA42R_%=:\n\t"
      "s_mov_b32 s80, 0\n\t"
      "LA42L_%=:\n\t"
      "s_add_u32 s80, s80, 1\n\t"
      "s_cmp_lt_u32 s80, 262144\n\t"
      "s_cbranch_scc0 LA42X_%=\n\t"
      "s_waitcnt vmcnt(0)\n\t"
      "PQ2R_%=:\n\t"
      "PQ2L_%=:\n\t"
      "s_add_u32 s80, s80, 1\n\t"
      "s_cmp_lt_u32 s80, 262144\n\t"
      "s_cbranch_scc0 PQ2X_%=\n\t"
      "s_sleep 1\n\t"
      "global_load_dword v251, v252, s[68:69] offset:252 sc1\n\t"
      "s_waitcnt vmcnt(0)\n\t"
      "v_cmp_eq_u32_e32 vcc, -1, v251\n\t"
      "s_cbranch_vccnz PQ2L_%=\n\t"
      "PQ2X_%=:\n\t"
      "s_add_u32 s58, s68, s40\n\t"
      "s_addc_u32 s59, s69, 0\n\t"
      "global_load_dwordx4 v[184:187], v244, s[58:59] sc1\n\t"
      "s_add_u32 s58, s68, s41\n\t"
      "s_addc_u32 s59, s69, 0\n\t"
      "global_load_dwordx4 v[188:191], v244, s[58:59] sc1\n\t"
      "s_add_u32 s58, s68, s42\n\t"
      "s_addc_u32 s59, s69, 0\n\t"
      "global_load_dwordx4 v[192:195], v244, s[58:59] sc1\n\t"
      "s_add_u32 s58, s68, s43\n\t"
      "s_addc_u32 s59, s69, 0\n\t"
      "global_load_dwordx4 v[128:131], v244, s[58:59] sc1\n\t"
      "s_add_u32 s58, s68, s44\n\t"
      "s_addc_u32 s59, s69, 0\n\t"
      "global_load_dwordx4 v[132:135], v244, s[58:59] sc1\n\t"
      "s_add_u32 s58, s68, s45\n\t"
      "s_addc_u32 s59, s69, 0\n\t"
      "global_load_dwordx4 v[136:139], v244, s[58:59] sc1\n\t"
      "s_add_u32 s58, s68, s46\n\t"
      "s_addc_u32 s59, s69, 0\n\t"
      "global_load_dwordx4 v[140:143], v244, s[58:59] sc1\n\t"
      "s_waitcnt vmcnt(0)\n\t"
      "v_max3_u32 v251, v184, v185, v186\n\t"
      "v_max3_u32 v251, v251, v187, v188\n\t"
      "v_max3_u32 v251, v251, v189, v190\n\t"
      "v_max3_u32 v251, v251, v191, v192\n\t"
      "v_max3_u32 v251, v251, v193, v194\n\t"
      "v_max3_u32 v251, v251, v195, v128\n\t"
      "v_max3_u32 v251, v251, v129, v130\n\t"
      "v_max3_u32 v251, v251, v131, v132\n\t"
      "v_max3_u32 v251, v251, v133, v134\n\t"
      "v_max3_u32 v251, v251, v135, v136\n\t"
      "v_max3_u32 v251, v251, v137, v138\n\t"
      "v_max3_u32 v251, v251, v139, v140\n\t"
      "v_max3_u32 v251, v251, v141, v142\n\t"
      "v_max_u32_e32 v251, v251, v143\n\t"
      "v_cmp_eq_u32_e32 vcc, -1, v251\n\t"
      "s_cbranch_vccnz LA42L_%=\n\t"
      "LA42X_%=:\n\t"
      "v_mfma_f32_16x16x32_bf16 v[196:199], v[184:187], v[0:3], 0\n\t"
      "v_mfma_f32_16x16x32_bf16 v[200:203], v[184:187], v[4:7], 0\n\t"
      "v_mfma_f32_16x16x32_bf16 v[204:207], v[184:187], v[8:11], 0\n\t"
      "v_mfma_f32_16x16x32_bf16 v[208:211], v[184:187], v[12:15], 0\n\t"
      "v_mfma_f32_16x16x32_bf16 v[196:199], v[188:191], v[16:19], v[196:199]\n\t"
      "v_mfma_f32_16x16x32_bf16 v[200:203], v[188:191], v[20:23], v[200:203]\n\t"
      "v_mfma_f32_16x16x32_bf16 v[204:207], v[188:191], v[24:27], v[204:207]\n\t"
      "v_mfma_f32_16x16x32_bf16 v[208:211], v[188:191], v[28:31], v[208:211]\n\t"
      "v_mfma_f32_16x16x32_bf16 v[196:199], v[192:195], v[32:35], v[196:199]\n\t"
      "v_mfma_f32_16x16x32_bf16 v[200:203], v[192:195], v[36:39], v[200:203]\n\t"
      "v_mfma_f32_16x16x32_bf16 v[204:207], v[192:195], v[40:43], v[204:207]\n\t"
      "v_mfma_f32_16x16x32_bf16 v[208:211], v[192:195], v[44:47], v[208:211]\n\t"
      "v_mfma_f32_16x16x32_bf16 v[196:199], v[128:131], v[48:51], v[196:199]\n\t"
      "v_mfma_f32_16x16x32_bf16 v[200:203], v[128:131], v[52:55], v[200:203]\n\t"
      "v_mfma_f32_16x16x32_bf16 v[204:207], v[128:131], v[56:59], v[204:207]\n\t"
      "v_mfma_f32_16x16x32_bf16 v[208:211], v[128:131], v[60:63], v[208:211]\n\t"
      "v_mfma_f32_16x16x32_bf16 v[196:199], v[132:135], v[64:67], v[196:199]\n\t"
      "v_mfma_f32_16x16x32_bf16 v[200:203], v[132:135], v[68:71], v[200:203]\n\t"
      "v_mfma_f32_16x16x32_bf16 v[204:207], v[132:135], v[72:75], v[204:207]\n\t"
      "v_mfma_f32_16x16x32_bf16 v[208:211], v[132:135], v[76:79], v[208:211]\n\t"
      "v_mfma_f32_16x16x32_bf16 v[196:199], v[136:139], v[80:83], v[196:199]\n\t"
      "v_mfma_f32_16x16x32_bf16 v[196:199], v[140:143], v[96:99], v[196:199]\n\t"
      "v_mfma_f32_16x16x32_bf16 v[200:203], v[136:139], v[84:87], v[200:203]\n\t"
      "v_mfma_f32_16x16x32_bf16 v[200:203], v[140:143], v[100:103], v[200:203]\n\t"
      "v_mfma_f32_16x16x32_bf16 v[204:207], v[136:139], v[88:91], v[204:207]\n\t"
      "s_nop 7\n\t"
      "s_branch LA42B_%=\n\t"
      "LA43R_%=:\n\t"
      "s_mov_b32 s80, 0\n\t"
      "LA43L_%=:\n\t"
      "s_add_u32 s80, s80, 1\n\t"
      "s_cmp_lt_u32 s80, 262144\n\t"
      "s_cbranch_scc0 LA43X_%=\n\t"
      "s_waitcnt vmcnt(0)\n\t"
      "PQ3R_%=:\n\t"
      "PQ3L_%=:\n\t"
      "s_add_u32 s80, s80, 1\n\t"
      "s_cmp_lt_u32 s80, 262144\n\t"
      "s_cbranch_scc0 PQ3X_%=\n\t"
      "s_sleep 1\n\t"
      "global_load_dword v251, v252, s[70:71] offset:252 sc1\n\t"
      "s_waitcnt vmcnt(0)\n\t"
      "v_cmp_eq_u32_e32 vcc, -1, v251\n\t"
      "s_cbranch_vccnz PQ3L_%=\n\t"
      "PQ3X_%=:\n\t"
      "s_add_u32 s58, s70, s40\n\t"
      "s_addc_u32 s59, s71, 0\n\t"
      "global_load_dwordx4 v[144:147], v244, s[58:59] sc1\n\t"
      "s_add_u32 s58, s70, s41\n\t"
      "s_addc_u32 s59, s71, 0\n\t"
      "global_load_dwordx4 v[148:151], v244, s[58:59] sc1\n\t"
      "s_add_u32 s58, s70, s42\n\t"
      "s_addc_u32 s59, s71, 0\n\t"
      "global_load_dwordx4 v[152:155], v244, s[58:59] sc1\n\t"
      "s_add_u32 s58, s70, s43\n\t"
      "s_addc_u32 s59, s71, 0\n\t"
      "global_load_dwordx4 v[156:159], v244, s[58:59] sc1\n\t"
      "s_add_u32 s58, s70, s44\n\t"
      "s_addc_u32 s59, s71, 0\n\t"
      "global_load_dwordx4 v[160:163], v244, s[58:59] sc1\n\t"
      "s_add_u32 s58, s70, s45\n\t"
      "s_addc_u32 s59, s71, 0\n\t"
      "global_load_dwordx4 v[164:167], v244, s[58:59] sc1\n\t"
      "s_add_u32 s58, s70, s46\n\t"
      "s_addc_u32 s59, s71, 0\n\t"
      "global_load_dwordx4 v[168:171], v244, s[58:59] sc1\n\t"
      "s_waitcnt vmcnt(0)\n\t"
      "v_max3_u32 v251, v144, v145, v146\n\t"
      "v_max3_u32 v251, v251, v147, v148\n\t"
      "v_max3_u32 v251, v251, v149, v150\n\t"
      "v_max3_u32 v251, v251, v151, v152\n\t"
      "v_max3_u32 v251, v251, v153, v154\n\t"
      "v_max3_u32 v251, v251, v155, v156\n\t"
      "v_max3_u32 v251, v251, v157, v158\n\t"
      "v_max3_u32 v251, v251, v159, v160\n\t"
      "v_max3_u32 v251, v251, v161, v162\n\t"
      "v_max3_u32 v251, v251, v163, v164\n\t"
      "v_max3_u32 v251, v251, v165, v166\n\t"
      "v_max3_u32 v251, v251, v167, v168\n\t"
      "v_max3_u32 v251, v251, v169, v170\n\t"
      "v_max_u32_e32 v251, v251, v171\n\t"
      "v_cmp_eq_u32_e32 vcc, -1, v251\n\t"
      "s_cbranch_vccnz LA43L_%=\n\t"
      "LA43X_%=:\n\t"
      "v_mfma_f32_16x16x32_bf16 v[212:215], v[144:147], v[0:3], 0\n\t"
      "v_mfma_f32_16x16x32_bf16 v[216:219], v[144:147], v[4:7], 0\n\t"
      "v_mfma_f32_16x16x32_bf16 v[220:223], v[144:147], v[8:11], 0\n\t"
      "v_mfma_f32_16x16x32_bf16 v[224:227], v[144:147], v[12:15], 0\n\t"
      "v_mfma_f32_16x16x32_bf16 v[212:215], v[148:151], v[16:19], v[212:215]\n\t"
      "v_mfma_f32_16x16x32_bf16 v[216:219], v[148:151], v[20:23], v[216:219]\n\t"
      "v_mfma_f32_16x16x32_bf16 v[220:223], v[148:151], v[24:27], v[220:223]\n\t"
      "v_mfma_f32_16x16x32_bf16 v[224:227], v[148:151], v[28:31], v[224:227]\n\t"
      "v_mfma_f32_16x16x32_bf16 v[212:215], v[152:155], v[32:35], v[212:215]\n\t"
      "v_mfma_f32_16x16x32_bf16 v[216:219], v[152:155], v[36:39], v[216:219]\n\t"
      "v_mfma_f32_16x16x32_bf16 v[220:223], v[152:155], v[40:43], v[220:223]\n\t"
      "v_mfma_f32_16x16x32_bf16 v[224:227], v[152:155], v[44:47], v[224:227]\n\t"
      "v_mfma_f32_16x16x32_bf16 v[212:215], v[156:159], v[48:51], v[212:215]\n\t"
      "v_mfma_f32_16x16x32_bf16 v[216:219], v[156:159], v[52:55], v[216:219]\n\t"
      "v_mfma_f32_16x16x32_bf16 v[220:223], v[156:159], v[56:59], v[220:223]\n\t"
      "v_mfma_f32_16x16x32_bf16 v[224:227], v[156:159], v[60:63], v[224:227]\n\t"
      "v_mfma_f32_16x16x32_bf16 v[212:215], v[160:163], v[64:67], v[212:215]\n\t"
      "v_mfma_f32_16x16x32_bf16 v[216:219], v[160:163], v[68:71], v[216:219]\n\t"
      "v_mfma_f32_16x16x32_bf16 v[220:223], v[160:163], v[72:75], v[220:223]\n\t"
      "v_mfma_f32_16x16x32_bf16 v[224:227], v[160:163], v[76:79], v[224:227]\n\t"
      "v_mfma_f32_16x16x32_bf16 v[212:215], v[164:167], v[80:83], v[212:215]\n\t"
      "v_mfma_f32_16x16x32_bf16 v[212:215], v[168:171], v[96:99], v[212:215]\n\t"
      "v_mfma_f32_16x16x32_bf16 v[216:219], v[164:167], v[84:87], v[216:219]\n\t"
      "v_mfma_f32_16x16x32_bf16 v[216:219], v[168:171], v[100:103], v[216:219]\n\t"
      "v_mfma_f32_16x16x32_bf16 v[220:223], v[164:167], v[88:91], v[220:223]\n\t"
      "s_nop 7\n\t"
      "s_branch LA43B_%=\n\t"
      "LA44R_%=:\n\t"
      "s_mov_b32 s80, 0\n\t"
      "LA44L_%=:\n\t"
      "s_add_u32 s80, s80, 1\n\t"
      "s_cmp_lt_u32 s80, 262144\n\t"
      "s_cbranch_scc0 LA44X_%=\n\t"
      "s_waitcnt vmcnt(0)\n\t"
      "PQ4R_%=:\n\t"
      "PQ4L_%=:\n\t"
      "s_add_u32 s80, s80, 1\n\t"
      "s_cmp_lt_u32 s80, 262144\n\t"
      "s_cbranch_scc0 PQ4X_%=\n\t"
      "s_sleep 1\n\t"
      "global_load_dword v251, v252, s[72:73] offset:252 sc1\n\t"
      "s_waitcnt vmcnt(0)\n\t"
      "v_cmp_eq_u32_e32 vcc, -1, v251\n\t"
      "s_cbranch_vccnz PQ4L_%=\n\t"
      "PQ4X_%=:\n\t"
      "s_add_u32 s58, s72, s40\n\t"
      "s_addc_u32 s59, s73, 0\n\t"
      "global_load_dwordx4 v[172:175], v244, s[58:59] sc1\n\t"
      "s_add_u32 s58, s72, s41\n\t"
      "s_addc_u32 s59, s73, 0\n\t"
      "global_load_dwordx4 v[176:179], v244, s[58:59] sc1\n\t"
      "s_add_u32 s58, s72, s42\n\t"
      "s_addc_u32 s59, s73, 0\n\t"
      "global_load_dwordx4 v[180:183], v244, s[58:59] sc1\n\t"
      "s_add_u32 s58, s72, s43\n\t"
      "s_addc_u32 s59, s73, 0\n\t"
      "global_load_dwordx4 v[184:187], v244, s[58:59] sc1\n\t"
      "s_add_u32 s58, s72, s44\n\t"
      "s_addc_u32 s59, s73, 0\n\t"
      "global_load_dwordx4 v[188:191], v244, s[58:59] sc1\n\t"
      "s_add_u32 s58, s72, s45\n\t"
      "s_addc_u32 s59, s73, 0\n\t"
      "global_load_dwordx4 v[192:195], v244, s[58:59] sc1\n\t"
      "s_add_u32 s58, s72, s46\n\t"
      "s_addc_u32 s59, s73, 0\n\t"
      "global_load_dwordx4 v[128:131], v244, s[58:59] sc1\n\t"
      "s_waitcnt vmcnt(0)\n\t"
      "v_max3_u32 v251, v172, v173, v174\n\t"
      "v_max3_u32 v251, v251, v175, v176\n\t"
      "v_max3_u32 v251, v251, v177, v178\n\t"
      "v_max3_u32 v251, v251, v179, v180\n\t"
      "v_max3_u32 v251, v251, v181, v182\n\t"
      "v_max3_u32 v251, v251, v183, v184\n\t"
      "v_max3_u32 v251, v251, v185, v186\n\t"
      "v_max3_u32 v251, v251, v187, v188\n\t"
      "v_max3_u32 v251, v251, v189, v190\n\t"
      "v_max3_u32 v251, v251, v191, v192\n\t"
      "v_max3_u32 v251, v251, v193, v194\n\t"
      "v_max3_u32 v251, v251, v195, v128\n\t"
      "v_max3_u32 v251, v251, v129, v130\n\t"
      "v_max_u32_e32 v251, v251, v131\n\t"
      "v_cmp_eq_u32_e32 vcc, -1, v251\n\t"
      "s_cbranch_vccnz LA44L_%=\n\t"
      "LA44X_%=:\n\t"
      "v_mfma_f32_16x16x32_bf16 v[196:199], v[172:175], v[0:3], 0\n\t"
      "v_mfma_f32_16x16x32_bf16 v[200:203], v[172:175], v[4:7], 0\n\t"
      "v_mfma_f32_16x16x32_bf16 v[204:207], v[172:175], v[8:11], 0\n\t"
      "v_mfma_f32_16x16x32_bf16 v[208:211], v[172:175], v[12:15], 0\n\t"
      "v_mfma_f32_16x16x32_bf16 v[196:199], v[176:179], v[16:19], v[196:199]\n\t"
      "v_mfma_f32_16x16x32_bf16 v[200:203], v[176:179], v[20:23], v[200:203]\n\t"
      "v_mfma_f32_16x16x32_bf16 v[204:207], v[176:179], v[24:27], v[204:207]\n\t"
      "v_mfma_f32_16x16x32_bf16 v[208:211], v[176:179], v[28:31], v[208:211]\n\t"
      "v_mfma_f32_16x16x32_bf16 v[196:199], v[180:183], v[32:35], v[196:199]\n\t"
      "v_mfma_f32_16x16x32_bf16 v[200:203], v[180:183], v[36:39], v[200:203]\n\t"
      "v_mfma_f32_16x16x32_bf16 v[204:207], v[180:183], v[40:43], v[204:207]\n\t"
      "v_mfma_f32_16x16x32_bf16 v[208:211], v[180:183], v[44:47], v[208:211]\n\t"
      "v_mfma_f32_16x16x32_bf16 v[196:199], v[184:187], v[48:51], v[196:199]\n\t"
      "v_mfma_f32_16x16x32_bf16 v[200:203], v[184:187], v[52:55], v[200:203]\n\t"
      "v_mfma_f32_16x16x32_bf16 v[204:207], v[184:187], v[56:59], v[204:207]\n\t"
      "v_mfma_f32_16x16x32_bf16 v[208:211], v[184:187], v[60:63], v[208:211]\n\t"
      "v_mfma_f32_16x16x32_bf16 v[196:199], v[188:191], v[64:67], v[196:199]\n\t"
      "v_mfma_f32_16x16x32_bf16 v[200:203], v[188:191], v[68:71], v[200:203]\n\t"
      "v_mfma_f32_16x16x32_bf16 v[204:207], v[188:191], v[72:75], v[204:207]\n\t"
      "v_mfma_f32_16x16x32_bf16 v[208:211], v[188:191], v[76:79], v[208:211]\n\t"
      "v_mfma_f32_16x16x32_bf16 v[196:199], v[192:195], v[80:83], v[196:199]\n\t"
      "v_mfma_f32_16x16x32_bf16 v[196:199], v[128:131], v[96:99], v[196:199]\n\t"
      "v_mfma_f32_16x16x32_bf16 v[200:203], v[192:195], v[84:87], v[200:203]\n\t"
      "v_mfma_f32_16x16x32_bf16 v[200:203], v[128:131], v[100:103], v[200:203]\n\t"
      "v_mfma_f32_16x16x32_bf16 v[204:207], v[192:195], v[88:91], v[204:207]\n\t"
      "s_nop 7\n\t"
      "s_branch LA44B_%=\n\t"
      "LA45R_%=:\n\t"
      "s_mov_b32 s80, 0\n\t"
      "LA45L_%=:\n\t"
      "s_add_u32 s80, s80, 1\n\t"
      "s_cmp_lt_u32 s80, 262144\n\t"
      "s_cbranch_scc0 LA45X_%=\n\t"
      "s_waitcnt vmcnt(0)\n\t"
      "PQ5R_%=:\n\t"
      "PQ5L_%=:\n\t"
      "s_add_u32 s80, s80, 1\n\t"
      "s_cmp_lt_u32 s80, 262144\n\t"
      "s_cbranch_scc0 PQ5X_%=\n\t"
      "s_sleep 1\n\t"
      "global_load_dword v251, v252, s[74:75] offset:252 sc1\n\t"
      "s_waitcnt vmcnt(0)\n\t"
      "v_cmp_eq_u32_e32 vcc, -1, v251\n\t"
      "s_cbranch_vccnz PQ5L_%=\n\t"
      "PQ5X_%=:\n\t"
      "s_add_u32 s58, s74, s40\n\t"
      "s_addc_u32 s59, s75, 0\n\t"
      "global_load_dwordx4 v[132:135], v244, s[58:59] sc1\n\t"
      "s_add_u32 s58, s74, s41\n\t"
      "s_addc_u32 s59, s75, 0\n\t"
      "global_load_dwordx4 v[136:139], v244, s[58:59] sc1\n\t"
      "s_add_u32 s58, s74, s42\n\t"
      "s_addc_u32 s59, s75, 0\n\t"
      "global_load_dwordx4 v[140:143], v244, s[58:59] sc1\n\t"
      "s_add_u32 s58, s74, s43\n\t"
      "s_addc_u32 s59, s75, 0\n\t"
      "global_load_dwordx4 v[144:147], v244, s[58:59] sc1\n\t"
      "s_add_u32 s58, s74, s44\n\t"
      "s_addc_u32 s59, s75, 0\n\t"
      "global_load_dwordx4 v[148:151], v244, s[58:59] sc1\n\t"
      "s_add_u32 s58, s74, s45\n\t"
      "s_addc_u32 s59, s75, 0\n\t"
      "global_load_dwordx4 v[152:155], v244, s[58:59] sc1\n\t"
      "s_add_u32 s58, s74, s46\n\t"
      "s_addc_u32 s59, s75, 0\n\t"
      "global_load_dwordx4 v[156:159], v244, s[58:59] sc1\n\t"
      "s_waitcnt vmcnt(0)\n\t"
      "v_max3_u32 v251, v132, v133, v134\n\t"
      "v_max3_u32 v251, v251, v135, v136\n\t"
      "v_max3_u32 v251, v251, v137, v138\n\t"
      "v_max3_u32 v251, v251, v139, v140\n\t"
      "v_max3_u32 v251, v251, v141, v142\n\t"
      "v_max3_u32 v251, v251, v143, v144\n\t"
      "v_max3_u32 v251, v251, v145, v146\n\t"
      "v_max3_u32 v251, v251, v147, v148\n\t"
      "v_max3_u32 v251, v251, v149, v150\n\t"
      "v_max3_u32 v251, v251, v151, v152\n\t"
      "v_max3_u32 v251, v251, v153, v154\n\t"
      "v_max3_u32 v251, v251, v155, v156\n\t"
      "v_max3_u32 v251, v251, v157, v158\n\t"
      "v_max_u32_e32 v251, v251, v159\n\t"
      "v_cmp_eq_u32_e32 vcc, -1, v251\n\t"
      "s_cbranch_vccnz LA45L_%=\n\t"
      "LA45X_%=:\n\t"
      "v_mfma_f32_16x16x32_bf16 v[212:215], v[132:135], v[0:3], 0\n\t"
      "v_mfma_f32_16x16x32_bf16 v[216:219], v[132:135], v[4:7], 0\n\t"
      "v_mfma_f32_16x16x32_bf16 v[220:223], v[132:135], v[8:11], 0\n\t"
      "v_mfma_f32_16x16x32_bf16 v[224:227], v[132:135], v[12:15], 0\n\t"
      "v_mfma_f32_16x16x32_bf16 v[212:215], v[136:139], v[16:19], v[212:215]\n\t"
      "v_mfma_f32_16x16x32_bf16 v[216:219], v[136:139], v[20:23], v[216:219]\n\t"
      "v_mfma_f32_16x16x32_bf16 v[220:223], v[136:139], v[24:27], v[220:223]\n\t"
      "v_mfma_f32_16x16x32_bf16 v[224:227], v[136:139], v[28:31], v[224:227]\n\t"
      "v_mfma_f32_16x16x32_bf16 v[212:215], v[140:143], v[32:35], v[212:215]\n\t"
      "v_mfma_f32_16x16x32_bf16 v[216:219], v[140:143], v[36:39], v[216:219]\n\t"
      "v_mfma_f32_16x16x32_bf16 v[220:223], v[140:143], v[40:43], v[220:223]\n\t"
      "v_mfma_f32_16x16x32_bf16 v[224:227], v[140:143], v[44:47], v[224:227]\n\t"
      "v_mfma_f32_16x16x32_bf16 v[212:215], v[144:147], v[48:51], v[212:215]\n\t"
      "v_mfma_f32_16x16x32_bf16 v[216:219], v[144:147], v[52:55], v[216:219]\n\t"
      "v_mfma_f32_16x16x32_bf16 v[220:223], v[144:147], v[56:59], v[220:223]\n\t"
      "v_mfma_f32_16x16x32_bf16 v[224:227], v[144:147], v[60:63], v[224:227]\n\t"
      "v_mfma_f32_16x16x32_bf16 v[212:215], v[148:151], v[64:67], v[212:215]\n\t"
      "v_mfma_f32_16x16x32_bf16 v[216:219], v[148:151], v[68:71], v[216:219]\n\t"
      "v_mfma_f32_16x16x32_bf16 v[220:223], v[148:151], v[72:75], v[220:223]\n\t"
      "v_mfma_f32_16x16x32_bf16 v[224:227], v[148:151], v[76:79], v[224:227]\n\t"
      "v_mfma_f32_16x16x32_bf16 v[212:215], v[152:155], v[80:83], v[212:215]\n\t"
      "v_mfma_f32_16x16x32_bf16 v[212:215], v[156:159], v[96:99], v[212:215]\n\t"
      "v_mfma_f32_16x16x32_bf16 v[216:219], v[152:155], v[84:87], v[216:219]\n\t"
      "v_mfma_f32_16x16x32_bf16 v[216:219], v[156:159], v[100:103], v[216:219]\n\t"
      "v_mfma_f32_16x16x32_bf16 v[220:223], v[152:155], v[88:91], v[220:223]\n\t"
      "s_nop 7\n\t"
      "s_branch LA45B_%=\n\t"
      "LA46R_%=:\n\t"
      "s_mov_b32 s80, 0\n\t"
      "LA46L_%=:\n\t"
      "s_add_u32 s80, s80, 1\n\t"
      "s_cmp_lt_u32 s80, 262144\n\t"
      "s_cbranch_scc0 LA46X_%=\n\t"
      "s_waitcnt vmcnt(0)\n\t"
      "PQ6R_%=:\n\t"
      "PQ6L_%=:\n\t"
      "s_add_u32 s80, s80, 1\n\t"
      "s_cmp_lt_u32 s80, 262144\n\t"
      "s_cbranch_scc0 PQ6X_%=\n\t"
      "s_sleep 1\n\t"
      "global_load_dword v251, v252, s[76:77] offset:252 sc1\n\t"
      "s_waitcnt vmcnt(0)\n\t"
      "v_cmp_eq_u32_e32 vcc, -1, v251\n\t"
      "s_cbranch_vccnz PQ6L_%=\n\t"
      "PQ6X_%=:\n\t"
      "s_add_u32 s58, s76, s40\n\t"
      "s_addc_u32 s59, s77, 0\n\t"
      "global_load_dwordx4 v[160:163], v244, s[58:59] sc1\n\t"
      "s_add_u32 s58, s76, s41\n\t"
      "s_addc_u32 s59, s77, 0\n\t"
      "global_load_dwordx4 v[164:167], v244, s[58:59] sc1\n\t"
      "s_add_u32 s58, s76, s42\n\t"
      "s_addc_u32 s59, s77, 0\n\t"
      "global_load_dwordx4 v[168:171], v244, s[58:59] sc1\n\t"
      "s_add_u32 s58, s76, s43\n\t"
      "s_addc_u32 s59, s77, 0\n\t"
      "global_load_dwordx4 v[172:175], v244, s[58:59] sc1\n\t"
      "s_add_u32 s58, s76, s44\n\t"
      "s_addc_u32 s59, s77, 0\n\t"
      "global_load_dwordx4 v[176:179], v244, s[58:59] sc1\n\t"
      "s_add_u32 s58, s76, s45\n\t"
      "s_addc_u32 s59, s77, 0\n\t"
      "global_load_dwordx4 v[180:183], v244, s[58:59] sc1\n\t"
      "s_add_u32 s58, s76, s46\n\t"
      "s_addc_u32 s59, s77, 0\n\t"
      "global_load_dwordx4 v[184:187], v244, s[58:59] sc1\n\t"
      "s_waitcnt vmcnt(0)\n\t"
      "v_max3_u32 v251, v160, v161, v162\n\t"
      "v_max3_u32 v251, v251, v163, v164\n\t"
      "v_max3_u32 v251, v251, v165, v166\n\t"
      "v_max3_u32 v251, v251, v167, v168\n\t"
      "v_max3_u32 v251, v251, v169, v170\n\t"
      "v_max3_u32 v251, v251, v171, v172\n\t"
      "v_max3_u32 v251, v251, v173, v174\n\t"
      "v_max3_u32 v251, v251, v175, v176\n\t"
      "v_max3_u32 v251, v251, v177, v178\n\t"
      "v_max3_u32 v251, v251, v179, v180\n\t"
      "v_max3_u32 v251, v251, v181, v182\n\t"
      "v_max3_u32 v251, v251, v183, v184\n\t"
      "v_max3_u32 v251, v251, v185, v186\n\t"
      "v_max_u32_e32 v251, v251, v187\n\t"
      "v_cmp_eq_u32_e32 vcc, -1, v251\n\t"
      "s_cbranch_vccnz LA46L_%=\n\t"
      "LA46X_%=:\n\t"
      "v_mfma_f32_16x16x32_bf16 v[196:199], v[160:163], v[0:3], 0\n\t"
      "v_mfma_f32_16x16x32_bf16 v[200:203], v[160:163], v[4:7], 0\n\t"
      "v_mfma_f32_16x16x32_bf16 v[204:207], v[160:163], v[8:11], 0\n\t"
      "v_mfma_f32_16x16x32_bf16 v[208:211], v[160:163], v[12:15], 0\n\t"
      "v_mfma_f32_16x16x32_bf16 v[196:199], v[164:167], v[16:19], v[196:199]\n\t"
      "v_mfma_f32_16x16x32_bf16 v[200:203], v[164:167], v[20:23], v[200:203]\n\t"
      "v_mfma_f32_16x16x32_bf16 v[204:207], v[164:167], v[24:27], v[204:207]\n\t"
      "v_mfma_f32_16x16x32_bf16 v[208:211], v[164:167], v[28:31], v[208:211]\n\t"
      "v_mfma_f32_16x16x32_bf16 v[196:199], v[168:171], v[32:35], v[196:199]\n\t"
      "v_mfma_f32_16x16x32_bf16 v[200:203], v[168:171], v[36:39], v[200:203]\n\t"
      "v_mfma_f32_16x16x32_bf16 v[204:207], v[168:171], v[40:43], v[204:207]\n\t"
      "v_mfma_f32_16x16x32_bf16 v[208:211], v[168:171], v[44:47], v[208:211]\n\t"
      "v_mfma_f32_16x16x32_bf16 v[196:199], v[172:175], v[48:51], v[196:199]\n\t"
      "v_mfma_f32_16x16x32_bf16 v[200:203], v[172:175], v[52:55], v[200:203]\n\t"
      "v_mfma_f32_16x16x32_bf16 v[204:207], v[172:175], v[56:59], v[204:207]\n\t"
      "v_mfma_f32_16x16x32_bf16 v[208:211], v[172:175], v[60:63], v[208:211]\n\t"
      "v_mfma_f32_16x16x32_bf16 v[196:199], v[176:179], v[64:67], v[196:199]\n\t"
      "v_mfma_f32_16x16x32_bf16 v[200:203], v[176:179], v[68:71], v[200:203]\n\t"
      "v_mfma_f32_16x16x32_bf16 v[204:207], v[176:179], v[72:75], v[204:207]\n\t"
      "v_mfma_f32_16x16x32_bf16 v[208:211], v[176:179], v[76:79], v[208:211]\n\t"
      "v_mfma_f32_16x16x32_bf16 v[196:199], v[180:183], v[80:83], v[196:199]\n\t"
      "v_mfma_f32_16x16x32_bf16 v[196:199], v[184:187], v[96:99], v[196:199]\n\t"
      "v_mfma_f32_16x16x32_bf16 v[200:203], v[180:183], v[84:87], v[200:203]\n\t"
      "v_mfma_f32_16x16x32_bf16 v[200:203], v[184:187], v[100:103], v[200:203]\n\t"
      "v_mfma_f32_16x16x32_bf16 v[204:207], v[180:183], v[88:91], v[204:207]\n\t"
      "s_nop 7\n\t"
      "s_branch LA46B_%=\n\t"
      : "+{s[0:1]}"(ka), "+{v0}"(tid)
      : "{s2}"(blockIdx.x)
      : "memory", "vcc", "scc", "m0",
        "v1", "v2", "v3", "v4", "v5", "v6", "v7", "v8", "v9", "v10", "v11", "v12", "v13", "v14", "v15", "v16", "v17",
        "v18", "v19", "v20", "v21", "v22", "v23", "v24", "v25", "v26", "v27", "v28", "v29", "v30", "v31", "v32", "v33",
        "v34", "v35", "v36", "v37", "v38", "v39", "v40", "v41", "v42", "v43", "v44", "v45", "v46", "v47", "v48", "v49",
        "v50", "v51", "v52", "v53", "v54", "v55", "v56", "v57", "v58", "v59", "v60", "v61", "v62", "v63", "v64", "v65",
        "v66", "v67", "v68", "v69", "v70", "v71", "v72", "v73", "v74", "v75", "v76", "v77", "v78", "v79", "v80", "v81",
        "v82", "v83", "v84", "v85", "v86", "v87", "v88", "v89", "v90", "v91", "v92", "v93", "v94", "v95", "v96", "v97",
        "v98", "v99", "v100", "v101", "v102", "v103", "v104", "v105", "v106", "v107", "v108", "v109", "v110", "v111",
        "v112", "v113", "v114", "v115", "v116", "v117", "v118", "v119", "v120", "v121", "v122", "v123", "v124", "v125",
        "v126", "v127", "v128", "v129", "v130", "v131", "v132", "v133", "v134", "v135", "v136", "v137", "v138", "v139",
        "v140", "v141", "v142", "v143", "v144", "v145", "v146", "v147", "v148", "v149", "v150", "v151", "v152", "v153",
        "v154", "v155", "v156", "v157", "v158", "v159", "v160", "v161", "v162", "v163", "v164", "v165", "v166", "v167",
        "v168", "v169", "v170", "v171", "v172", "v173", "v174", "v175", "v176", "v177", "v178", "v179", "v180", "v181",
        "v182", "v183", "v184", "v185", "v186", "v187", "v188", "v189", "v190", "v191", "v192", "v193", "v194", "v195",
        "v196", "v197", "v198", "v199", "v200", "v201", "v202", "v203", "v204", "v205", "v206", "v207", "v208", "v209",
        "v210", "v211", "v212", "v213", "v214", "v215", "v216", "v217", "v218", "v219", "v220", "v221", "v222", "v223",
        "v224", "v225", "v226", "v227", "v228", "v229", "v230", "v231", "v232", "v233", "v234", "v235", "v236", "v237",
        "v238", "v239", "v240", "v241", "v242", "v243", "v244", "v245", "v246", "v247", "v248", "v249", "v250", "v251",
        "v252", "v253", "v254", "v255", "s3", "s4", "s5", "s6", "s7", "s8", "s9", "s10", "s11", "s12", "s13", "s14",
        "s15", "s16", "s17", "s18", "s19", "s20", "s21", "s22", "s23", "s24", "s25", "s26", "s27", "s28", "s29", "s30",
        "s31", "s32", "s33", "s34", "s35", "s36", "s37", "s38", "s39", "s40", "s41", "s42", "s43", "s44", "s45", "s46",
        "s47", "s48", "s49", "s50", "s51", "s52", "s53", "s54", "s55", "s56", "s57", "s58", "s59", "s60", "s61", "s62",
        "s63", "s64", "s65", "s66", "s67", "s68", "s69", "s70", "s71", "s72", "s73", "s74", "s75", "s76", "s77", "s78",
        "s79", "s80", "s81", "s82", "s83"
  );
#endif
}

extern "C" void kernel_launch(void* const* d_in, const int* in_sizes, int n_in, void* d_out, int out_size,
                              void* d_ws, size_t ws_size, hipStream_t stream) {
  (void)hipFuncSetAttribute((const void*)fused_block, hipFuncAttributeMaxDynamicSharedMemorySize, 163840);
  fused_block<<<256, 512, 163840, stream>>>((const u16*)d_in[0], (const u16*)d_in[1], (const u16*)d_in[2],
                                            (const u16*)d_in[4], (const u16*)d_in[5], (const u16*)d_in[6],
                                            (const u16*)d_in[7], (u16*)d_out, (char*)d_ws);
}
```
